```python
import torch

def _softmax(x, axis=-1):
    return torch.softmax(x, dim=axis)
def _gelu(x):
    return torch.nn.functional.gelu(x, approximate="tanh")
def _silu(x):
    return torch.nn.functional.silu(x)
def _sigmoid(x):
    return torch.sigmoid(x)
def _layernorm(x, axis=-1, eps=1e-5):
    m = x.mean(dim=axis, keepdim=True)
    v = x.var(dim=axis, keepdim=True, unbiased=False)
    return (x - m) / torch.sqrt(v + eps)
def _scatter_add(tgt, idx, src, axis=0):
    return tgt.clone().index_add_(axis, idx, src)
def _segment_sum(src, seg, n):
    out = torch.zeros((n,) + src.shape[1:], dtype=src.dtype)
    return out.index_add_(0, seg, src)
def _pad(x, pw):
    flat = tuple(v for pair in reversed(pw) for v in pair)
    return torch.nn.functional.pad(x, flat)

kernel_name = 'sequential_preLN__sliding_local_dilated__mixer_token_mlp__prompt'

def setup_inputs():

    _w = (lambda a: (lambda t: t.to(torch.bfloat16) if t.is_floating_point() else t)(torch.as_tensor(a)))
    def _A_setup():
        B, S, D = 1, 2048, 448
        return {
            "x": _w((lambda *s: torch.randn(*s, dtype=torch.bfloat16) * 0.05)(B, S, D)),
            "wqkv": _w((lambda *s: torch.randn(*s, dtype=torch.bfloat16) * 0.05)(D, 3 * D)),
            "wo": _w((lambda *s: torch.randn(*s, dtype=torch.bfloat16) * 0.05)(D, D)),
        }
    _A_in = _A_setup()

    def _B_setup():
        B, S, D, F = 1, 2048, 448, 1792
        return {
            "x": _w((lambda *s: torch.randn(*s, dtype=torch.bfloat16) * 0.05)(B, S, D)),
            "w1": _w((lambda *s: torch.randn(*s, dtype=torch.bfloat16) * 1.6)(S, F) * (S ** -0.5)),
            "b1": _w((lambda *s: torch.randn(*s, dtype=torch.bfloat16) * 12.8)(F) * 0.02),
            "w2": _w((lambda *s: torch.randn(*s, dtype=torch.bfloat16) * 0.4)(F, S) * (F ** -0.5)),
            "b2": _w((lambda *s: torch.randn(*s, dtype=torch.bfloat16) * 6.4)(S) * 0.02),
        }
    _B_in = _B_setup()

    return {
        "A_x": _A_in["x"],
        "A_wqkv": _A_in["wqkv"],
        "A_wo": _A_in["wo"],
        "B_x": _B_in["x"],
        "B_w1": _B_in["w1"],
        "B_b1": _B_in["b1"],
        "B_w2": _B_in["w2"],
        "B_b2": _B_in["b2"],
    }

def reference(A_x, A_wqkv, A_wo, B_x, B_w1, B_b1, B_w2, B_b2):
    def _A_ref(x, wqkv, wo):
        B, S, D = x.shape
        H = 16; Dh = D // H
        W, dil = 8, 2
        qkv = (lambda x, *d: x.permute(*d))((x @ wqkv).reshape(B, S, 3, H, Dh), 2, 0, 3, 1, 4)
        q, k, v = qkv[0], qkv[1], qkv[2]
        scale = Dh ** -0.5
        pos = torch.arange(S)
        off = torch.arange(W) * dil
        idx = pos[:, None] - off[None, :]
        mask = idx >= 0
        idx_c = torch.where(mask, idx, 0).reshape(-1)
        kg = k[:, :, idx_c, :].reshape(B, H, S, W, Dh)
        vg = v[:, :, idx_c, :].reshape(B, H, S, W, Dh)
        logits = torch.einsum("bhsd,bhswd->bhsw", q, kg) * scale
        logits = torch.where(mask[None, None, :, :], logits, -1e9)
        attn = _softmax(logits, axis=-1)
        o = torch.einsum("bhsw,bhswd->bhsd", attn, vg)
        return (lambda x, *d: x.permute(*d))(o, 0, 2, 1, 3).reshape(B, S, D) @ wo

    def _B_ref(x, w1, b1, w2, b2):
        B, S, D = x.shape
        h = _layernorm(x, axis=-1)
        u = torch.einsum("bsd,sf->bdf", h, w1) + b1
        u = _gelu(u)
        y = torch.einsum("bdf,fs->bds", u, w2) + b2
        return (lambda x, *d: x.permute(*d))(y, 0, 2, 1)

    x = A_x
    h = x + _A_ref(_layernorm(x, axis=-1), A_wqkv, A_wo)
    h2 = h + _B_ref(_layernorm(h, axis=-1), B_w1, B_b1, B_w2, B_b2)
    return h2
```

```cpp
#include <hip/hip_runtime.h>
#include <stdint.h>

#define WS_O 0
#define WS_HN 2097152
#define WS_H 4194304
#define WS_G 6291456
#define WS_ST 8388608
#define ST_FL 65536
#define ST_H3 262144
#define ST_H4 393216
#define ST_H2 32768
#define WS_EP 8519680
#define A_UNIT 5120
#define A_HALO 40960
#define A_SLOT 43008
#define A_CS 131072
#define A_ST 132096
#define A_EX 142336
#define A_FL 154624
#define B_UNIT 7168
#define B_SLOT 57344
#define B_X 114688
#define B_OP 928
#define B_PS 131072
#define G_WV 16384
#define G_RED 0
#define G_RED2 32768
#define LDS_KEY 163312
#define LDS_SINK 163328
#define LDS_BYTES 163840
#define XO 3

typedef unsigned short u16;
typedef unsigned long long u64;
typedef __attribute__((ext_vector_type(4))) unsigned int u32x4;
typedef __attribute__((ext_vector_type(8))) unsigned int u32x8;
typedef __attribute__((ext_vector_type(16))) unsigned int u32x16;
#define UNI(x) (unsigned)__builtin_amdgcn_readfirstlane((int)(x))
#define PIN() __builtin_amdgcn_sched_barrier(0)

__device__ __forceinline__ int swz_tr(int row) { return (((row >> 1) & 1) | (((row >> 3) & 1) << 1)) << 1; }
__device__ __forceinline__ int swz_row(int row) { return (row >> 1) & 7; }
template <class T>
__device__ __forceinline__ void put(T& s, int i, u64 p) { s[i] = (unsigned)p; s[i + 1] = (unsigned)(p >> 32); }
template <class T>
__device__ __forceinline__ void put2(T& s, int i, unsigned lo, unsigned hi) { s[i] = UNI(lo); s[i + 1] = UNI(hi); }

__global__ __launch_bounds__(512, 2) void fused_block(const char* __restrict__ x, const char* __restrict__ wqkv,
                                                   const char* __restrict__ wo, const char* __restrict__ w1,
                                                   const char* __restrict__ b1, const char* __restrict__ w2,
                                                   const char* __restrict__ b2, char* __restrict__ out,
                                                   char* __restrict__ ws) {
  u32x8 f;
  asm volatile(
      "s_load_dwordx4 s[4:7], s[0:1], 0x0\n\t"
      "v_readfirstlane_b32 s3, v0\n\t"
      "s_getpc_b64 s[40:41]\n\t"
      "s_or_b32 s12, s3, s2\n\t"
      "s_cmp_gt_u32 s12, 7\n\t"
      "s_cbranch_scc1 ET_%=\n\t"
      "s_mov_b32 m0, 163328\n\t"
      "v_lshlrev_b32_e32 v3, 7, v0\n\t"
      "global_load_lds_dword v3, s[40:41]\n\t"
      "ET_%=:\n\t"
      "v_lshrrev_b32_e32 v1, 3, v0\n\t"
      "v_and_b32_e32 v2, 7, v0\n\t"
      "s_and_b32 s17, s2, 7\n\t"
      "s_lshr_b32 s18, s2, 3\n\t"
      "s_lshr_b32 s3, s3, 6\n\t"
      "s_cmpk_gt_u32 s2, 0x7f\n\t"
      "s_cbranch_scc1 EB_%=\n\t"
      "v_and_b32_e32 v3, 2, v1\n\t"
      "v_lshrrev_b32_e32 v4, 1, v1\n\t"
      "s_lshr_b32 s19, s18, 3\n\t"
      "s_lshl1_add_u32 s19, s17, s19\n\t"
      "v_and_or_b32 v3, v4, 4, v3\n\t"
      "v_xor_b32_e32 v3, v3, v2\n\t"
      "v_min_u32_e32 v3, 6, v3\n\t"
      "v_mul_u32_u24_e32 v5, 2688, v1\n\t"
      "v_lshl_add_u32 v3, v3, 4, v5\n\t"
      "v_add_u32_e32 v195, 2048, v3\n\t"
      "v_add_u32_e32 v196, 1920, v3\n\t"
      "v_add_u32_e32 v197, 5888, v3\n\t"
      "s_mul_i32 s12, s19, 114688\n\t"
      "v_and_b32_e32 v5, 0x38, v1\n\t"
      "v_add_u32_e32 v5, v5, v1\n\t"
      "v_mul_u32_u24_e32 v5, 896, v5\n\t"
      "s_and_b32 s13, s18, 7\n\t"
      "s_mul_i32 s13, s13, 112\n\t"
      "v_and_b32_e32 v6, 3, v4\n\t"
      "v_xor_b32_e32 v6, v6, v2\n\t"
      "v_lshl_add_u32 v5, v6, 4, v5\n\t"
      "s_sub_u32 s12, s12, 15360\n\t"
      "s_ashr_i32 s14, s12, 31\n\t"
      "s_sub_u32 s13, s13, 4096\n\t"
      "v_add_u32_e32 v192, 15360, v5\n\t"
      "v_xor_b32_e32 v5, 64, v5\n\t"
      "v_add_u32_e32 v193, 21504, v5\n\t"
      "s_mul_i32 s9, s3, 5120\n\t"
      "s_waitcnt lgkmcnt(0)\n\t"
      "s_add_u32 s4, s4, s12\n\t"
      "s_addc_u32 s5, s5, s14\n\t"
      "s_add_u32 s6, s6, s13\n\t"
      "s_addc_u32 s7, s7, -1\n\t"
      "s_add_u32 m0, s9, 0\n\t"
      "s_nop 0\n\t"
      "global_load_lds_dwordx4 v195, s[6:7] offset:2048\n\t"
      "global_load_lds_dwordx4 v192, s[4:5]\n\t"
      "global_load_lds_dwordx4 v193, s[4:5] offset:1024\n\t"
      "global_load_lds_dwordx4 v196, s[6:7] offset:3072\n\t"
      "s_add_u32 m0, s9, 4096\n\t"
      "s_nop 0\n\t"
      "global_load_lds_dwordx4 v197, s[6:7]\n\t"
      "s_sub_u32 s3, s3, 4\n\t"
      "s_cbranch_scc1 EX_%=\n\t"
      "s_cmp_eq_u32 s19, 0\n\t"
      "s_cselect_b32 s13, 16, 0\n\t"
      "s_lshl2_add_u32 s13, s3, s13\n\t"
      "v_and_or_b32 v3, v1, 3, s13\n\t"
      "v_bfe_u32 v4, v3, 1, 3\n\t"
      "v_mul_u32_u24_e32 v3, 896, v3\n\t"
      "s_cmp_eq_u32 s3, 0\n\t"
      "s_cselect_b32 s10, 0xffffff00, -1\n\t"
      "s_mov_b32 s11, 0\n\t"
      "v_xor_b32_e32 v4, v4, v2\n\t"
      "v_lshl_add_u32 v3, v4, 4, v3\n\t"
      "v_add_u32_e32 v194, 1024, v3\n\t"
      "s_lshl_b32 s12, s3, 9\n\t"
      "s_add_u32 s12, s12, 40960\n\t"
      "s_add_u32 m0, s12, 0\n\t"
      "s_mov_b64 exec, s[10:11]\n\t"
      "global_load_lds_dwordx4 v194, s[4:5]\n\t"
      "s_mov_b64 exec, -1\n\t"
      "s_branch EX_%=\n\t"
      "EB_%=:\n\t"
      "s_load_dwordx2 s[10:11], s[0:1], 0x40\n\t"
      "v_and_b32_e32 v3, 63, v0\n\t"
      "v_lshlrev_b32_e32 v3, 4, v3\n\t"
      "s_lshl4_add_u32 s19, s17, s18\n\t"
      "s_sub_u32 s19, s19, 16\n\t"
      "s_lshl3_add_u32 s12, s19, s3\n\t"
      "s_lshl_b32 s12, s12, 11\n\t"
      "s_mul_i32 s19, s19, 14336\n\t"
      "s_min_u32 s16, s3, 6\n\t"
      "s_lshl_b32 s16, s16, 11\n\t"
      "s_add_u32 s19, s19, s16\n\t"
      "s_add_u32 s8, s16, 114688\n\t"
      "v_mov_b32_e32 v196, -1\n\t"
      "v_mov_b32_e32 v197, -1\n\t"
      "v_mov_b32_e32 v198, -1\n\t"
      "v_mov_b32_e32 v199, -1\n\t"
      "s_lshl3_add_u32 s16, s18, s3\n\t"
      "s_sub_u32 s16, s16, 128\n\t"
      "s_lshl_b32 s15, s16, 2\n\t"
      "v_and_or_b32 v6, v0, 3, s15\n\t"
      "v_min_u32_e32 v6, 489, v6\n\t"
      "v_lshlrev_b32_e32 v6, 7, v6\n\t"
      "s_mul_i32 s13, s16, 8064\n\t"
      "s_add_u32 s13, s13, 172032\n\t"
      "s_mul_i32 s14, s16, 1408\n\t"
      "s_add_u32 s14, s14, 0\n\t"
      "v_min_u32_e32 v4, 992, v3\n\t"
      "v_lshl_add_u32 v4, v4, 3, s13\n\t"
      "v_min_u32_e32 v4, 1204096, v4\n\t"
      "v_min_u32_e32 v5, 160, v3\n\t"
      "v_lshl_add_u32 v5, v5, 3, s14\n\t"
      "s_waitcnt lgkmcnt(0)\n\t"
      "s_add_u32 s4, s4, s19\n\t"
      "s_addc_u32 s5, s5, 0\n\t"
      "s_add_u32 s10, s10, s12\n\t"
      "s_addc_u32 s11, s11, 0\n\t"
      "s_mov_b32 m0, s8\n\t"
      "s_nop 0\n\t"
      "global_load_lds_dwordx4 v3, s[4:5]\n\t"
      "global_load_lds_dwordx4 v3, s[4:5] offset:1024\n\t"
      "s_mov_b32 m0, 163328\n\t"
      "s_nop 0\n\t"
      "global_load_lds_dword v6, s[40:41]\n\t"
      "s_mov_b32 m0, 163328\n\t"
      "s_nop 0\n\t"
      "global_load_lds_dword v4, s[6:7]\n\t"
      "global_store_dwordx4 v3, v[196:199], s[10:11] sc1\n\t"
      "global_store_dwordx4 v3, v[196:199], s[10:11] offset:1024 sc1\n\t"
      "EX_%=:\n\t"
      : "={v[192:199]}"(f)
      : "{s[0:1]}"(__builtin_amdgcn_kernarg_segment_ptr()), "{s2}"(blockIdx.x), "{v0}"(threadIdx.x)
      : "memory", "scc", "s3", "s4", "s5", "s6", "s7", "s8", "s9", "s10", "s11", "s12", "s13", "s14", "s15", "s16", "s17", "s18", "s19", "v1", "v2",
        "v3", "v4", "v5", "v6", "s40", "s41", "s42", "s43", "s48", "s49", "s50"
      );
  PIN();
  const int wave = __builtin_amdgcn_readfirstlane(threadIdx.x >> 6);
  const int xcd = blockIdx.x & 7, idx = blockIdx.x >> 3;
#define LANE_BITS(l) \
  const int lane = (l), g = lane >> 4, c = lane & 15, q4 = c >> 2, p4 = lane & 3, lr = lane >> 3, lc = lane & 7

  if (__builtin_expect(idx < 16, 1)) {
    const int rt = 2 * xcd + (idx >> 3), hp = idx & 7, hh = (wave >> 1) & 1, half = wave & 1;
    const unsigned sta = A_ST + half * 1024;
    if (wave < 4) {
      LANE_BITS(threadIdx.x & 63);
      u32x16 v;
      u32x8 s;
      v[0] = 4 * half * A_UNIT + c * 128 + ((g ^ swz_row(c)) << 4);
      v[1] = A_HALO + c * 128 + ((g ^ swz_row(c)) << 4);
#pragma unroll
      for (int j = 0; j < 2; ++j) {
        const int by = 56 * hh + 32 * j + 8 * p4;
        v[2 + j] = g * A_UNIT + 2048 + q4 * 128 + (((by >> 4) ^ swz_tr(8 * g + q4)) << 4) + (by & 8);
      }
      const unsigned csa = A_CS + hh * 384, exa = A_EX + hh * 3 * 2048;
      v[4] = csa + g * 16;
      v[5] = csa + c * 4;
      v[6] = v[4] + 64 * half;
      v[7] = v[5] + 64 * half;
      v[8] = A_FL + (wave ^ 1) * 8;
      v[9] = sta + g * 16;
      v[10] = sta + c * 4;
      const int key = 4 * g + (c & 1);
      v[11] = sta + 384 + (key <= c ? 128 : 64) + key * 4;
      unsigned vk1 = sta + 384 + (key + 2 <= c ? 128 : 64) + key * 4 + 8;
      v[12] = exa + half * 2048 + lane * 16;
      v[13] = exa + half * 2048 + lane * 8;
      v[14] = exa + lane * 16 + 8 * half;
      v[15] = lane * 16;
      put(s, 0, (u64)(ws + WS_O + ((8 * rt + 4 * half) * 16 + 2 * hp + hh) * 1024));
      s[2] = half;
      s[3] = UNI(rt == 0 && half == 0 ? 0xff800000u : 0u);
      s[4] = exa + 1024 + 512 * half;
      s[5] = A_FL + 32 + 4 * half;
      put(s, 6, (u64)(ws + WS_ST));
      unsigned hint = ST_H2 + (((rt * 8 + hp) * 2 + half) * 2 + hh) * 64;
    asm volatile(
      "s_waitcnt lgkmcnt(0)\n\ts_nop 4\n\t"
      "s_load_dwordx2 s[52:53], s[10:11], 0x20000\n\t"
      "v_xor_b32_e32 v248, 64, v224\n\t"
      "v_xor_b32_e32 v249, 64, v225\n\t"
      "s_mov_b32 s36, 0xaaaaaaaa\n\t"
      "s_mov_b32 s37, 0xaaaaaaaa\n\t"
      "s_mov_b32 s38, 0xfff0ffff\n\t"
      "s_mov_b32 s39, 0xf000ff00\n\t"
      "s_mov_b32 s40, 0xffc0fffc\n\t"
      "s_mov_b32 s41, 0xc000fc00\n\t"
      "s_mov_b32 s42, 0x0\n\t"
      "s_mov_b32 s43, 0xffff0000\n\t"
      "s_mov_b32 s44, 0x10001\n\t"
      "s_mov_b32 s45, 0x10001\n\t"
      "v_mov_b32_e32 v216, 0xffff\n\t"
      "v_mov_b32_e32 v217, 0xffff0000\n\t"
      "v_cndmask_b32_e64 v216, v216, v217, s[36:37]\n\t"
      "v_cndmask_b32_e64 v240, v216, 0, s[38:39]\n\t"
      "v_cndmask_b32_e64 v241, v216, 0, s[40:41]\n\t"
      "v_cndmask_b32_e64 v242, 0, v216, s[38:39]\n\t"
      "v_cndmask_b32_e64 v243, 0, v216, s[40:41]\n\t"
      "v_mov_b32_e32 v244, s7\n\t"
      "v_mov_b32_e32 v245, s7\n\t"
      "v_mov_b32_e32 v246, s7\n\t"
      "v_mov_b32_e32 v247, s7\n\t"
      "v_mov_b64_e32 v[0:1], 0\n\t"
      "v_mov_b64_e32 v[2:3], 0\n\t"
      "v_mov_b64_e32 v[4:5], 0\n\t"
      "v_mov_b64_e32 v[6:7], 0\n\t"
      "v_mov_b64_e32 v[8:9], 0\n\t"
      "v_mov_b64_e32 v[10:11], 0\n\t"
      "v_mov_b64_e32 v[12:13], 0\n\t"
      "v_mov_b64_e32 v[14:15], 0\n\t"
      "v_mov_b64_e32 v[16:17], 0\n\t"
      "v_mov_b64_e32 v[18:19], 0\n\t"
      "v_mov_b64_e32 v[20:21], 0\n\t"
      "v_mov_b64_e32 v[22:23], 0\n\t"
      "v_mov_b64_e32 v[24:25], 0\n\t"
      "v_mov_b64_e32 v[26:27], 0\n\t"
      "v_mov_b64_e32 v[28:29], 0\n\t"
      "v_mov_b64_e32 v[30:31], 0\n\t"
      "v_mov_b64_e32 v[32:33], 0\n\t"
      "v_mov_b64_e32 v[34:35], 0\n\t"
      "v_mov_b64_e32 v[36:37], 0\n\t"
      "v_mov_b64_e32 v[38:39], 0\n\t"
      "v_mov_b64_e32 v[40:41], 0\n\t"
      "v_mov_b64_e32 v[42:43], 0\n\t"
      "v_mov_b64_e32 v[44:45], 0\n\t"
      "v_mov_b64_e32 v[46:47], 0\n\t"
      "v_mov_b64_e32 v[48:49], 0\n\t"
      "v_mov_b64_e32 v[50:51], 0\n\t"
      "v_mov_b64_e32 v[52:53], 0\n\t"
      "v_mov_b64_e32 v[54:55], 0\n\t"
      "v_mov_b64_e32 v[56:57], 0\n\t"
      "v_mov_b64_e32 v[58:59], 0\n\t"
      "v_mov_b64_e32 v[60:61], 0\n\t"
      "v_mov_b64_e32 v[62:63], 0\n\t"
      "v_mov_b64_e32 v[64:65], 0\n\t"
      "v_mov_b64_e32 v[66:67], 0\n\t"
      "v_mov_b64_e32 v[68:69], 0\n\t"
      "v_mov_b64_e32 v[70:71], 0\n\t"
      "v_mov_b64_e32 v[72:73], 0\n\t"
      "v_mov_b64_e32 v[74:75], 0\n\t"
      "v_mov_b64_e32 v[76:77], 0\n\t"
      "v_mov_b64_e32 v[78:79], 0\n\t"
      "v_mov_b64_e32 v[80:81], 0\n\t"
      "v_mov_b64_e32 v[82:83], 0\n\t"
      "v_mov_b64_e32 v[84:85], 0\n\t"
      "v_mov_b64_e32 v[86:87], 0\n\t"
      "v_mov_b64_e32 v[88:89], 0\n\t"
      "v_mov_b64_e32 v[90:91], 0\n\t"
      "v_mov_b64_e32 v[92:93], 0\n\t"
      "v_mov_b64_e32 v[94:95], 0\n\t"
      "v_mov_b64_e32 v[96:97], 0\n\t"
      "v_mov_b64_e32 v[98:99], 0\n\t"
      "v_mov_b64_e32 v[100:101], 0\n\t"
      "v_mov_b64_e32 v[102:103], 0\n\t"
      "v_mov_b64_e32 v[104:105], 0\n\t"
      "v_mov_b64_e32 v[106:107], 0\n\t"
      "s_mov_b32 s47, 6\n\t"
      "s_waitcnt lgkmcnt(0)\n\t"
      "s_add_u32 s52, s52, 1\n\t"
      "s_addc_u32 s53, s53, 0\n\t"
      "s_mul_i32 s48, s52, 0x9e3779b9\n\t"
      "s_mul_hi_u32 s55, s52, 0x7f4a7c15\n\t"
      "s_add_u32 s55, s55, s48\n\t"
      "s_mul_i32 s48, s53, 0x7f4a7c15\n\t"
      "s_add_u32 s55, s55, s48\n\t"
      "s_mul_i32 s54, s52, 0x7f4a7c15\n\t"
      "s_add_u32 s54, s54, 0xd192ed03\n\t"
      "s_addc_u32 s55, s55, 0xd1b54a32\n\t"
      "s_cmp_eq_u32 s6, 0\n\t"
      "s_cbranch_scc0 LOOP1_%=\n\t"
      "s_waitcnt vmcnt(0)\n\t"
      "s_barrier\n\t"
      "ds_read_b64_tr_b16 v[108:109], v226\n\t"
      "ds_read_b64_tr_b16 v[110:111], v226 offset:512\n\t"
      "ds_read_b128 v[132:135], v224\n\t"
      "ds_read_b128 v[136:139], v224 offset:5120\n\t"
      "ds_read_b128 v[140:143], v224 offset:10240\n\t"
      "ds_read_b128 v[144:147], v224 offset:15360\n\t"
      "ds_read_b64_tr_b16 v[112:113], v227\n\t"
      "ds_read_b64_tr_b16 v[114:115], v227 offset:512\n\t"
      "ds_read_b128 v[148:151], v225\n\t"
      "ds_read_b64_tr_b16 v[116:117], v226 offset:1024\n\t"
      "ds_read_b64_tr_b16 v[118:119], v226 offset:1536\n\t"
      "ds_read_b64_tr_b16 v[120:121], v227 offset:1024\n\t"
      "ds_read_b64_tr_b16 v[122:123], v227 offset:1536\n\t"
      "ds_read_b64_tr_b16 v[124:125], v226 offset:2048\n\t"
      "ds_read_b64_tr_b16 v[126:127], v226 offset:2560\n\t"
      "ds_read_b64_tr_b16 v[128:129], v227 offset:2048\n\t"
      "ds_read_b64_tr_b16 v[130:131], v227 offset:2560\n\t"
      "s_branch EVEN0_%=\n\t"
      "s_waitcnt lgkmcnt(0)\n\t"
      ".p2align 6\n\t"
      "ODD0_%=:\n\t"
      "s_waitcnt lgkmcnt(0)\n\t"
      "v_mfma_f32_16x16x32_bf16 v[0:3], v[152:155], v[176:179], v[0:3]\n\t"
      "v_add_u32_e32 v224, s46, v224\n\t"
      "v_add_u32_e32 v226, s46, v226\n\t"
      "v_mfma_f32_16x16x32_bf16 v[24:27], v[152:155], v[180:183], v[24:27]\n\t"
      "v_add_u32_e32 v227, s46, v227\n\t"
      "v_add_u32_e32 v225, s46, v225\n\t"
      "v_mfma_f32_16x16x32_bf16 v[48:51], v[152:155], v[184:187], v[48:51]\n\t"
      "s_barrier\n\t"
      "ds_read_b64_tr_b16 v[108:109], v226\n\t"
      "v_mfma_f32_16x16x32_bf16 v[72:75], v[152:155], v[188:191], v[72:75]\n\t"
      "ds_read_b64_tr_b16 v[110:111], v226 offset:512\n\t"
      "v_mfma_f32_16x16x32_bf16 v[4:7], v[156:159], v[176:179], v[4:7]\n\t"
      "ds_read_b128 v[132:135], v224\n\t"
      "v_mfma_f32_16x16x32_bf16 v[28:31], v[156:159], v[180:183], v[28:31]\n\t"
      "ds_read_b128 v[136:139], v224 offset:5120\n\t"
      "v_mfma_f32_16x16x32_bf16 v[52:55], v[156:159], v[184:187], v[52:55]\n\t"
      "ds_read_b128 v[140:143], v224 offset:10240\n\t"
      "v_mfma_f32_16x16x32_bf16 v[76:79], v[156:159], v[188:191], v[76:79]\n\t"
      "ds_read_b128 v[144:147], v224 offset:15360\n\t"
      "v_mfma_f32_16x16x32_bf16 v[96:99], v[152:155], v[192:195], v[96:99]\n\t"
      "v_mfma_f32_16x16x32_bf16 v[8:11], v[160:163], v[176:179], v[8:11]\n\t"
      "ds_read_b64_tr_b16 v[112:113], v227\n\t"
      "v_mfma_f32_16x16x32_bf16 v[32:35], v[160:163], v[180:183], v[32:35]\n\t"
      "ds_read_b64_tr_b16 v[114:115], v227 offset:512\n\t"
      "v_mfma_f32_16x16x32_bf16 v[56:59], v[160:163], v[184:187], v[56:59]\n\t"
      "ds_read_b128 v[148:151], v225\n\t"
      "v_mfma_f32_16x16x32_bf16 v[80:83], v[160:163], v[188:191], v[80:83]\n\t"
      "ds_read_b64_tr_b16 v[116:117], v226 offset:1024\n\t"
      "v_mfma_f32_16x16x32_bf16 v[12:15], v[164:167], v[176:179], v[12:15]\n\t"
      "ds_read_b64_tr_b16 v[118:119], v226 offset:1536\n\t"
      "v_mfma_f32_16x16x32_bf16 v[36:39], v[164:167], v[180:183], v[36:39]\n\t"
      "ds_read_b64_tr_b16 v[120:121], v227 offset:1024\n\t"
      "v_mfma_f32_16x16x32_bf16 v[60:63], v[164:167], v[184:187], v[60:63]\n\t"
      "v_mfma_f32_16x16x32_bf16 v[84:87], v[164:167], v[188:191], v[84:87]\n\t"
      "ds_read_b64_tr_b16 v[122:123], v227 offset:1536\n\t"
      "v_mfma_f32_16x16x32_bf16 v[100:103], v[160:163], v[192:195], v[100:103]\n\t"
      "ds_read_b64_tr_b16 v[124:125], v226 offset:2048\n\t"
      "v_mfma_f32_16x16x32_bf16 v[16:19], v[176:179], v[168:171], v[16:19]\n\t"
      "ds_read_b64_tr_b16 v[126:127], v226 offset:2560\n\t"
      "v_mfma_f32_16x16x32_bf16 v[40:43], v[180:183], v[168:171], v[40:43]\n\t"
      "ds_read_b64_tr_b16 v[128:129], v227 offset:2048\n\t"
      "v_mfma_f32_16x16x32_bf16 v[64:67], v[184:187], v[168:171], v[64:67]\n\t"
      "ds_read_b64_tr_b16 v[130:131], v227 offset:2560\n\t"
      "v_mfma_f32_16x16x32_bf16 v[88:91], v[188:191], v[168:171], v[88:91]\n\t"
      "v_mfma_f32_16x16x32_bf16 v[20:23], v[176:179], v[172:175], v[20:23]\n\t"
      "v_mfma_f32_16x16x32_bf16 v[44:47], v[180:183], v[172:175], v[44:47]\n\t"
      "v_add_u32_e32 v248, s46, v248\n\t"
      "v_add_u32_e32 v249, s46, v249\n\t"
      "v_mfma_f32_16x16x32_bf16 v[68:71], v[184:187], v[172:175], v[68:71]\n\t"
      "v_mfma_f32_16x16x32_bf16 v[92:95], v[188:191], v[172:175], v[92:95]\n\t"
      "v_mfma_f32_16x16x32_bf16 v[104:107], v[192:195], v[168:171], v[104:107]\n\t"
      "EVEN0_%=:\n\t"
      "s_waitcnt lgkmcnt(0)\n\t"
      "v_mfma_f32_16x16x32_bf16 v[0:3], v[108:111], v[132:135], v[0:3]\n\t"
      "ds_read_b64_tr_b16 v[152:153], v226 offset:20480\n\t"
      "v_mfma_f32_16x16x32_bf16 v[24:27], v[108:111], v[136:139], v[24:27]\n\t"
      "ds_read_b64_tr_b16 v[154:155], v226 offset:20992\n\t"
      "v_mfma_f32_16x16x32_bf16 v[48:51], v[108:111], v[140:143], v[48:51]\n\t"
      "ds_read_b128 v[176:179], v248\n\t"
      "v_mfma_f32_16x16x32_bf16 v[72:75], v[108:111], v[144:147], v[72:75]\n\t"
      "ds_read_b128 v[180:183], v248 offset:5120\n\t"
      "v_mfma_f32_16x16x32_bf16 v[4:7], v[112:115], v[132:135], v[4:7]\n\t"
      "v_mfma_f32_16x16x32_bf16 v[28:31], v[112:115], v[136:139], v[28:31]\n\t"
      "ds_read_b128 v[184:187], v248 offset:10240\n\t"
      "v_mfma_f32_16x16x32_bf16 v[52:55], v[112:115], v[140:143], v[52:55]\n\t"
      "ds_read_b128 v[188:191], v248 offset:15360\n\t"
      "v_mfma_f32_16x16x32_bf16 v[76:79], v[112:115], v[144:147], v[76:79]\n\t"
      "ds_read_b64_tr_b16 v[156:157], v227 offset:20480\n\t"
      "v_mfma_f32_16x16x32_bf16 v[96:99], v[108:111], v[148:151], v[96:99]\n\t"
      "v_mfma_f32_16x16x32_bf16 v[8:11], v[116:119], v[132:135], v[8:11]\n\t"
      "ds_read_b64_tr_b16 v[158:159], v227 offset:20992\n\t"
      "v_mfma_f32_16x16x32_bf16 v[32:35], v[116:119], v[136:139], v[32:35]\n\t"
      "ds_read_b128 v[192:195], v249\n\t"
      "v_mfma_f32_16x16x32_bf16 v[56:59], v[116:119], v[140:143], v[56:59]\n\t"
      "ds_read_b64_tr_b16 v[160:161], v226 offset:21504\n\t"
      "v_mfma_f32_16x16x32_bf16 v[80:83], v[116:119], v[144:147], v[80:83]\n\t"
      "ds_read_b64_tr_b16 v[162:163], v226 offset:22016\n\t"
      "v_mfma_f32_16x16x32_bf16 v[12:15], v[120:123], v[132:135], v[12:15]\n\t"
      "v_mfma_f32_16x16x32_bf16 v[36:39], v[120:123], v[136:139], v[36:39]\n\t"
      "ds_read_b64_tr_b16 v[164:165], v227 offset:21504\n\t"
      "v_mfma_f32_16x16x32_bf16 v[60:63], v[120:123], v[140:143], v[60:63]\n\t"
      "ds_read_b64_tr_b16 v[166:167], v227 offset:22016\n\t"
      "v_mfma_f32_16x16x32_bf16 v[84:87], v[120:123], v[144:147], v[84:87]\n\t"
      "ds_read_b64_tr_b16 v[168:169], v226 offset:22528\n\t"
      "v_mfma_f32_16x16x32_bf16 v[100:103], v[116:119], v[148:151], v[100:103]\n\t"
      "v_mfma_f32_16x16x32_bf16 v[16:19], v[132:135], v[124:127], v[16:19]\n\t"
      "ds_read_b64_tr_b16 v[170:171], v226 offset:23040\n\t"
      "v_mfma_f32_16x16x32_bf16 v[40:43], v[136:139], v[124:127], v[40:43]\n\t"
      "ds_read_b64_tr_b16 v[172:173], v227 offset:22528\n\t"
      "v_mfma_f32_16x16x32_bf16 v[64:67], v[140:143], v[124:127], v[64:67]\n\t"
      "ds_read_b64_tr_b16 v[174:175], v227 offset:23040\n\t"
      "s_cmp_eq_u32 s47, 4\n\t"
      "s_cselect_b32 s46, -2, 1\n\t"
      "v_mfma_f32_16x16x32_bf16 v[88:91], v[144:147], v[124:127], v[88:91]\n\t"
      "s_cmp_eq_u32 s47, 1\n\t"
      "s_cselect_b32 s46, -2, s46\n\t"
      "v_mfma_f32_16x16x32_bf16 v[20:23], v[132:135], v[128:131], v[20:23]\n\t"
      "s_mul_i32 s46, s46, 43008\n\t"
      "v_mfma_f32_16x16x32_bf16 v[44:47], v[136:139], v[128:131], v[44:47]\n\t"
      "v_mfma_f32_16x16x32_bf16 v[68:71], v[140:143], v[128:131], v[68:71]\n\t"
      "v_mfma_f32_16x16x32_bf16 v[92:95], v[144:147], v[128:131], v[92:95]\n\t"
      "s_sub_u32 s47, s47, 1\n\t"
      "v_mfma_f32_16x16x32_bf16 v[104:107], v[148:151], v[124:127], v[104:107]\n\t"
      "s_cbranch_scc0 ODD0_%=\n\t"
      "s_waitcnt lgkmcnt(0)\n\t"
      "v_mfma_f32_16x16x32_bf16 v[96:99], v[152:155], v[192:195], v[96:99]\n\t"
      "v_mov_b32_e32 v212, s9\n\t"
      "ds_read_b32 v212, v212\n\t"
      "ds_read2_b32 v[128:129], v234 offset0:32 offset1:32\n\t"
      "v_mfma_f32_16x16x32_bf16 v[100:103], v[160:163], v[192:195], v[100:103]\n\t"
      "ds_read2_b32 v[130:131], v234 offset0:48 offset1:48\n\t"
      "v_mfma_f32_16x16x32_bf16 v[104:107], v[192:195], v[168:171], v[104:107]\n\t"
      "ds_read2_b32 v[132:133], v234 offset0:64 offset1:64\n\t"
      "ds_read2_b32 v[134:135], v234 offset0:80 offset1:80\n\t"
      "v_mfma_f32_16x16x32_bf16 v[80:83], v[160:163], v[188:191], v[80:83]\n\t"
      "ds_read2_b32 v[248:249], v234 offset0:0 offset1:0\n\t"
      "v_mfma_f32_16x16x32_bf16 v[84:87], v[164:167], v[188:191], v[84:87]\n\t"
      "ds_read_b128 v[196:199], v233\n\t"
      "ds_read_b128 v[136:139], v233 offset:128\n\t"
      "v_mfma_f32_16x16x32_bf16 v[88:91], v[188:191], v[168:171], v[88:91]\n\t"
      "ds_read_b128 v[140:143], v233 offset:192\n\t"
      "v_mfma_f32_16x16x32_bf16 v[92:95], v[188:191], v[172:175], v[92:95]\n\t"
      "ds_read_b128 v[144:147], v233 offset:256\n\t"
      "ds_read_b128 v[148:151], v233 offset:320\n\t"
      "v_mfma_f32_16x16x32_bf16 v[56:59], v[160:163], v[184:187], v[56:59]\n\t"
      "s_mov_b64 exec, s[44:45]\n\t"
      "ds_write_b128 v230, v[96:99]\n\t"
      "ds_write_b128 v230, v[100:103] offset:128\n\t"
      "s_mov_b64 exec, 0xffff\n\t"
      "ds_write_b32 v231, v104 offset:256\n\t"
      "v_xor_b32_e32 v227, 8, v232\n\t"
      "ds_write_b32 v227, v227\n\t"
      "s_mov_b64 exec, -1\n\t"
      "v_mfma_f32_16x16x32_bf16 v[60:63], v[164:167], v[184:187], v[60:63]\n\t"
      "v_mfma_f32_16x16x32_bf16 v[64:67], v[184:187], v[168:171], v[64:67]\n\t"
      "v_mfma_f32_16x16x32_bf16 v[68:71], v[184:187], v[172:175], v[68:71]\n\t"
      "s_waitcnt lgkmcnt(4)\n\t"
      "v_mfma_f32_16x16x32_bf16 v[32:35], v[160:163], v[180:183], v[32:35]\n\t"
      "ds_read_b32 v226, v232\n\t"
      "ds_read_b128 v[116:119], v228 offset:128\n\t"
      "v_mfma_f32_16x16x32_bf16 v[36:39], v[164:167], v[180:183], v[36:39]\n\t"
      "ds_read_b128 v[120:123], v228 offset:192\n\t"
      "ds_read2_b32 v[250:251], v229 offset0:64 offset1:64\n\t"
      "v_mfma_f32_16x16x32_bf16 v[40:43], v[180:183], v[168:171], v[40:43]\n\t"
      "ds_read2_b32 v[252:253], v229 offset0:80 offset1:80\n\t"
      "ds_read_b128 v[124:127], v230 offset:128\n\t"
      "v_mfma_f32_16x16x32_bf16 v[44:47], v[180:183], v[172:175], v[44:47]\n\t"
      "ds_read2_b32 v[224:225], v231 offset0:64 offset1:64\n\t"
      "ds_read_b128 v[108:111], v228\n\t"
      "v_mfma_f32_16x16x32_bf16 v[8:11], v[160:163], v[176:179], v[8:11]\n\t"
      "ds_read_b128 v[112:115], v228 offset:64\n\t"
      "ds_read2_b32 v[204:205], v235 offset0:0 offset1:16\n\t"
      "v_mfma_f32_16x16x32_bf16 v[12:15], v[164:167], v[176:179], v[12:15]\n\t"
      "ds_read2_b32 v[206:207], v235 offset0:32 offset1:48\n\t"
      "ds_read2_b32 v[208:209], v254 offset0:0 offset1:16\n\t"
      "v_mfma_f32_16x16x32_bf16 v[16:19], v[176:179], v[168:171], v[16:19]\n\t"
      "ds_read2_b32 v[210:211], v254 offset0:32 offset1:48\n\t"
      "ds_read2_b32 v[200:201], v234 offset0:192 offset1:208\n\t"
      "v_mfma_f32_16x16x32_bf16 v[20:23], v[176:179], v[172:175], v[20:23]\n\t"
      "ds_read2_b32 v[202:203], v234 offset0:224 offset1:240\n\t"
      "LOOPX_%=:\n\t"
      "s_waitcnt lgkmcnt(14)\n\t"
      "v_min_u32_e32 v226, v226, v212\n\t"
      "v_cmp_eq_u32_e32 vcc, 0, v226\n\t"
      "s_cbranch_vccnz XAR_%=\n\t"
      "XAB_%=:\n\t"
      "s_waitcnt lgkmcnt(12)\n\t"
      "v_pk_fma_f32 v[80:81], v[134:135], v[116:117], v[80:81]\n\t"
      "v_pk_fma_f32 v[82:83], v[134:135], v[118:119], v[82:83]\n\t"
      "v_pk_fma_f32 v[84:85], v[134:135], v[120:121], v[84:85]\n\t"
      "v_pk_fma_f32 v[86:87], v[134:135], v[122:123], v[86:87]\n\t"
      "v_mfma_f32_16x16x32_bf16 v[72:75], v[152:155], v[188:191], v[72:75]\n\t"
      "v_cvt_pk_bf16_f32 v80, v80, v81\n\t"
      "v_cvt_pk_bf16_f32 v81, v82, v83\n\t"
      "v_cvt_pk_bf16_f32 v82, v84, v85\n\t"
      "v_cvt_pk_bf16_f32 v83, v86, v87\n\t"
      "v_mfma_f32_16x16x32_bf16 v[76:79], v[156:159], v[188:191], v[76:79]\n\t"
      "ds_write_b128 v236, v[80:83] offset:2048\n\t"
      "s_waitcnt lgkmcnt(11)\n\t"
      "v_pk_fma_f32 v[88:89], v[148:149], v[250:251], v[88:89]\n\t"
      "v_pk_fma_f32 v[90:91], v[150:151], v[250:251], v[90:91]\n\t"
      "v_pk_fma_f32 v[92:93], v[148:149], v[252:253], v[92:93]\n\t"
      "v_pk_fma_f32 v[94:95], v[150:151], v[252:253], v[94:95]\n\t"
      "v_mfma_f32_16x16x32_bf16 v[48:51], v[152:155], v[184:187], v[48:51]\n\t"
      "v_cvt_pk_bf16_f32 v216, v88, v89\n\t"
      "v_cvt_pk_bf16_f32 v217, v90, v91\n\t"
      "v_cvt_pk_bf16_f32 v218, v92, v93\n\t"
      "v_cvt_pk_bf16_f32 v219, v94, v95\n\t"
      "v_mfma_f32_16x16x32_bf16 v[52:55], v[156:159], v[184:187], v[52:55]\n\t"
      "ds_write_b64 v237, v[216:217] offset:3072\n\t"
      "ds_write_b64 v237, v[218:219] offset:3584\n\t"
      "s_waitcnt lgkmcnt(11)\n\t"
      "v_pk_fma_f32 v[100:101], v[248:249], v[124:125], v[100:101]\n\t"
      "v_pk_fma_f32 v[104:105], v[196:197], v[224:225], v[104:105]\n\t"
      "v_pk_fma_f32 v[102:103], v[248:249], v[126:127], v[102:103]\n\t"
      "v_pk_fma_f32 v[106:107], v[198:199], v[224:225], v[106:107]\n\t"
      "v_mfma_f32_16x16x32_bf16 v[24:27], v[152:155], v[180:183], v[24:27]\n\t"
      "v_cvt_pk_bf16_f32 v220, v100, v101\n\t"
      "v_cvt_pk_bf16_f32 v221, v102, v103\n\t"
      "v_cvt_pk_bf16_f32 v222, v104, v105\n\t"
      "v_cvt_pk_bf16_f32 v223, v106, v107\n\t"
      "v_mfma_f32_16x16x32_bf16 v[28:31], v[156:159], v[180:183], v[28:31]\n\t"
      "ds_write_b64 v238, v[220:221]\n\t"
      "v_lshrrev_b32_e32 v96, 1, v239\n\t"
      "v_add_u32_e32 v96, s8, v96\n\t"
      "ds_write_b64 v96, v[222:223]\n\t"
      "v_xor_b32_e32 v227, 8, v232\n\t"
      "ds_write_b32 v227, v227 offset:4\n\t"
      "v_pk_fma_f32 v[56:57], v[132:133], v[116:117], v[56:57]\n\t"
      "v_pk_fma_f32 v[58:59], v[132:133], v[118:119], v[58:59]\n\t"
      "v_pk_fma_f32 v[60:61], v[132:133], v[120:121], v[60:61]\n\t"
      "v_pk_fma_f32 v[62:63], v[132:133], v[122:123], v[62:63]\n\t"
      "v_mfma_f32_16x16x32_bf16 v[0:3], v[152:155], v[176:179], v[0:3]\n\t"
      "v_cvt_pk_bf16_f32 v56, v56, v57\n\t"
      "v_cvt_pk_bf16_f32 v57, v58, v59\n\t"
      "v_cvt_pk_bf16_f32 v58, v60, v61\n\t"
      "v_cvt_pk_bf16_f32 v59, v62, v63\n\t"
      "v_mfma_f32_16x16x32_bf16 v[4:7], v[156:159], v[176:179], v[4:7]\n\t"
      "v_mov_b32_e32 v164, v216\n\t"
      "v_mov_b32_e32 v165, v217\n\t"
      "v_mov_b32_e32 v174, v218\n\t"
      "v_mov_b32_e32 v175, v219\n\t"
      "s_waitcnt lgkmcnt(12)\n\t"
      "v_pk_fma_f32 v[72:73], v[134:135], v[108:109], v[72:73]\n\t"
      "v_pk_fma_f32 v[74:75], v[134:135], v[110:111], v[74:75]\n\t"
      "v_pk_fma_f32 v[76:77], v[134:135], v[112:113], v[76:77]\n\t"
      "v_pk_fma_f32 v[78:79], v[134:135], v[114:115], v[78:79]\n\t"
      "v_cvt_pk_bf16_f32 v72, v72, v73\n\t"
      "v_cvt_pk_bf16_f32 v73, v74, v75\n\t"
      "v_cvt_pk_bf16_f32 v74, v76, v77\n\t"
      "v_cvt_pk_bf16_f32 v75, v78, v79\n\t"
      "v_cndmask_b32_e64 v74, v74, 0, s[42:43]\n\t"
      "v_cndmask_b32_e64 v75, v75, 0, s[42:43]\n\t"
      "s_nop 1\n\t"
      "v_mfma_f32_16x16x32_bf16 v[76:79], v[56:59], v[72:75], 0\n\t"
      "v_mfma_f32_16x16x32_bf16 v[84:87], v[80:83], v[72:75], 0\n\t"
      "v_pk_fma_f32 v[32:33], v[130:131], v[116:117], v[32:33]\n\t"
      "v_pk_fma_f32 v[34:35], v[130:131], v[118:119], v[34:35]\n\t"
      "v_pk_fma_f32 v[36:37], v[130:131], v[120:121], v[36:37]\n\t"
      "v_pk_fma_f32 v[38:39], v[130:131], v[122:123], v[38:39]\n\t"
      "v_cvt_pk_bf16_f32 v32, v32, v33\n\t"
      "v_cvt_pk_bf16_f32 v33, v34, v35\n\t"
      "v_cvt_pk_bf16_f32 v34, v36, v37\n\t"
      "v_cvt_pk_bf16_f32 v35, v38, v39\n\t"
      "v_pk_fma_f32 v[48:49], v[132:133], v[108:109], v[48:49]\n\t"
      "v_pk_fma_f32 v[50:51], v[132:133], v[110:111], v[50:51]\n\t"
      "v_pk_fma_f32 v[52:53], v[132:133], v[112:113], v[52:53]\n\t"
      "v_pk_fma_f32 v[54:55], v[132:133], v[114:115], v[54:55]\n\t"
      "v_cvt_pk_bf16_f32 v48, v48, v49\n\t"
      "v_cvt_pk_bf16_f32 v49, v50, v51\n\t"
      "v_cvt_pk_bf16_f32 v50, v52, v53\n\t"
      "v_cvt_pk_bf16_f32 v51, v54, v55\n\t"
      "v_cndmask_b32_e64 v50, v50, 0, s[42:43]\n\t"
      "v_cndmask_b32_e64 v51, v51, 0, s[42:43]\n\t"
      "v_pk_fma_f32 v[64:65], v[144:145], v[250:251], v[64:65]\n\t"
      "v_pk_fma_f32 v[66:67], v[146:147], v[250:251], v[66:67]\n\t"
      "v_pk_fma_f32 v[68:69], v[144:145], v[252:253], v[68:69]\n\t"
      "v_pk_fma_f32 v[70:71], v[146:147], v[252:253], v[70:71]\n\t"
      "v_cvt_pk_bf16_f32 v162, v64, v65\n\t"
      "v_cvt_pk_bf16_f32 v163, v66, v67\n\t"
      "v_cvt_pk_bf16_f32 v172, v68, v69\n\t"
      "v_cvt_pk_bf16_f32 v173, v70, v71\n\t"
      "ds_read_b32 v226, v232 offset:4\n\t"
      "ds_read_b128 v[152:155], v236\n\t"
      "ds_read_b64 v[156:157], v237 offset:1024\n\t"
      "ds_read_b64 v[166:167], v237 offset:1536\n\t"
      "v_mfma_f32_16x16x32_bf16 v[52:55], v[32:35], v[48:51], 0\n\t"
      "v_mfma_f32_16x16x32_bf16 v[60:63], v[56:59], v[48:51], 0\n\t"
      "v_pk_fma_f32 v[8:9], v[128:129], v[116:117], v[8:9]\n\t"
      "v_pk_fma_f32 v[10:11], v[128:129], v[118:119], v[10:11]\n\t"
      "v_pk_fma_f32 v[12:13], v[128:129], v[120:121], v[12:13]\n\t"
      "v_pk_fma_f32 v[14:15], v[128:129], v[122:123], v[14:15]\n\t"
      "v_cvt_pk_bf16_f32 v8, v8, v9\n\t"
      "v_cvt_pk_bf16_f32 v9, v10, v11\n\t"
      "v_cvt_pk_bf16_f32 v10, v12, v13\n\t"
      "v_cvt_pk_bf16_f32 v11, v14, v15\n\t"
      "v_pk_fma_f32 v[24:25], v[130:131], v[108:109], v[24:25]\n\t"
      "v_pk_fma_f32 v[26:27], v[130:131], v[110:111], v[26:27]\n\t"
      "v_pk_fma_f32 v[28:29], v[130:131], v[112:113], v[28:29]\n\t"
      "v_pk_fma_f32 v[30:31], v[130:131], v[114:115], v[30:31]\n\t"
      "v_cvt_pk_bf16_f32 v24, v24, v25\n\t"
      "v_cvt_pk_bf16_f32 v25, v26, v27\n\t"
      "v_cvt_pk_bf16_f32 v26, v28, v29\n\t"
      "v_cvt_pk_bf16_f32 v27, v30, v31\n\t"
      "v_cndmask_b32_e64 v26, v26, 0, s[42:43]\n\t"
      "v_cndmask_b32_e64 v27, v27, 0, s[42:43]\n\t"
      "v_pk_fma_f32 v[40:41], v[140:141], v[250:251], v[40:41]\n\t"
      "v_pk_fma_f32 v[42:43], v[142:143], v[250:251], v[42:43]\n\t"
      "v_pk_fma_f32 v[44:45], v[140:141], v[252:253], v[44:45]\n\t"
      "v_pk_fma_f32 v[46:47], v[142:143], v[252:253], v[46:47]\n\t"
      "v_cvt_pk_bf16_f32 v160, v40, v41\n\t"
      "v_cvt_pk_bf16_f32 v161, v42, v43\n\t"
      "v_cvt_pk_bf16_f32 v170, v44, v45\n\t"
      "v_cvt_pk_bf16_f32 v171, v46, v47\n\t"
      "v_mfma_f32_16x16x32_bf16 v[28:31], v[8:11], v[24:27], 0\n\t"
      "v_mfma_f32_16x16x32_bf16 v[36:39], v[32:35], v[24:27], 0\n\t"
      "v_pk_fma_f32 v[0:1], v[128:129], v[108:109], v[0:1]\n\t"
      "v_pk_fma_f32 v[2:3], v[128:129], v[110:111], v[2:3]\n\t"
      "v_pk_fma_f32 v[4:5], v[128:129], v[112:113], v[4:5]\n\t"
      "v_pk_fma_f32 v[6:7], v[128:129], v[114:115], v[6:7]\n\t"
      "v_cvt_pk_bf16_f32 v0, v0, v1\n\t"
      "v_cvt_pk_bf16_f32 v1, v2, v3\n\t"
      "v_cvt_pk_bf16_f32 v2, v4, v5\n\t"
      "v_cvt_pk_bf16_f32 v3, v6, v7\n\t"
      "v_cndmask_b32_e64 v2, v2, 0, s[42:43]\n\t"
      "v_cndmask_b32_e64 v3, v3, 0, s[42:43]\n\t"
      "v_pk_fma_f32 v[16:17], v[136:137], v[250:251], v[16:17]\n\t"
      "v_pk_fma_f32 v[18:19], v[138:139], v[250:251], v[18:19]\n\t"
      "v_pk_fma_f32 v[20:21], v[136:137], v[252:253], v[20:21]\n\t"
      "v_pk_fma_f32 v[22:23], v[138:139], v[252:253], v[22:23]\n\t"
      "v_cvt_pk_bf16_f32 v158, v16, v17\n\t"
      "v_cvt_pk_bf16_f32 v159, v18, v19\n\t"
      "v_cvt_pk_bf16_f32 v168, v20, v21\n\t"
      "v_cvt_pk_bf16_f32 v169, v22, v23\n\t"
      "s_waitcnt lgkmcnt(3)\n\t"
      "v_cmp_eq_u32_e32 vcc, 0, v226\n\t"
      "s_cbranch_vccnz XBR_%=\n\t"
      "XBB_%=:\n\t"
      "s_waitcnt lgkmcnt(2)\n\t"
      "v_mfma_f32_16x16x32_bf16 v[4:7], v[152:155], v[0:3], v[244:247]\n\t"
      "v_mfma_f32_16x16x32_bf16 v[12:15], v[8:11], v[0:3], 0\n\t"
      "v_cndmask_b32_e64 v76, v76, v77, s[36:37]\n\t"
      "v_cndmask_b32_e64 v84, v84, v85, s[36:37]\n\t"
      "v_cndmask_b32_e64 v78, v78, v79, s[36:37]\n\t"
      "v_cndmask_b32_e64 v86, v86, v87, s[36:37]\n\t"
      "v_cndmask_b32_e64 v179, v76, v84, s[38:39]\n\t"
      "v_cndmask_b32_e64 v183, v78, v86, s[40:41]\n\t"
      "v_cndmask_b32_e64 v52, v52, v53, s[36:37]\n\t"
      "v_cndmask_b32_e64 v60, v60, v61, s[36:37]\n\t"
      "v_cndmask_b32_e64 v54, v54, v55, s[36:37]\n\t"
      "v_cndmask_b32_e64 v62, v62, v63, s[36:37]\n\t"
      "v_cndmask_b32_e64 v178, v52, v60, s[38:39]\n\t"
      "v_cndmask_b32_e64 v182, v54, v62, s[40:41]\n\t"
      "v_pk_mul_f32 v[178:179], v[178:179], v[206:207]\n\t"
      "v_pk_mul_f32 v[182:183], v[182:183], v[210:211]\n\t"
      "v_max_f32_e32 v219, v179, v183\n\t"
      "v_max_f32_e32 v218, v178, v182\n\t"
      "v_cndmask_b32_e64 v28, v28, v29, s[36:37]\n\t"
      "v_cndmask_b32_e64 v36, v36, v37, s[36:37]\n\t"
      "v_cndmask_b32_e64 v30, v30, v31, s[36:37]\n\t"
      "v_cndmask_b32_e64 v38, v38, v39, s[36:37]\n\t"
      "v_cndmask_b32_e64 v177, v28, v36, s[38:39]\n\t"
      "v_cndmask_b32_e64 v181, v30, v38, s[40:41]\n\t"
      "v_cndmask_b32_e64 v4, v4, v5, s[36:37]\n\t"
      "v_cndmask_b32_e64 v12, v12, v13, s[36:37]\n\t"
      "v_cndmask_b32_e64 v6, v6, v7, s[36:37]\n\t"
      "v_cndmask_b32_e64 v14, v14, v15, s[36:37]\n\t"
      "v_cndmask_b32_e64 v176, v4, v12, s[38:39]\n\t"
      "v_cndmask_b32_e64 v180, v6, v14, s[40:41]\n\t"
      "v_pk_mul_f32 v[176:177], v[176:177], v[204:205]\n\t"
      "v_pk_mul_f32 v[180:181], v[180:181], v[208:209]\n\t"
      "v_max_f32_e32 v217, v177, v181\n\t"
      "v_max_f32_e32 v216, v176, v180\n\t"
      "v_permlane16_swap_b32_e32 v218, v219\n\t"
      "s_nop 0\n\t"
      "v_permlane16_swap_b32_e32 v216, v217\n\t"
      "v_max_f32_e32 v218, v218, v219\n\t"
      "v_max_f32_e32 v216, v216, v217\n\t"
      "v_mov_b32_e32 v219, v218\n\t"
      "v_mov_b32_e32 v217, v216\n\t"
      "s_nop 0\n\t"
      "v_permlane32_swap_b32_e32 v218, v219\n\t"
      "v_permlane32_swap_b32_e32 v216, v217\n\t"
      "v_max_f32_e32 v218, v218, v219\n\t"
      "v_max_f32_e32 v216, v216, v217\n\t"
      "v_mov_b32_e32 v219, v218\n\t"
      "v_mov_b32_e32 v217, v216\n\t"
      "s_nop 0\n\t"
      "v_permlane16_swap_b32_e32 v218, v219\n\t"
      "v_permlane16_swap_b32_e32 v216, v217\n\t"
      "v_pk_add_f32 v[178:179], v[178:179], v[218:219] neg_lo:[0,1] neg_hi:[0,1]\n\t"
      "v_pk_add_f32 v[182:183], v[182:183], v[218:219] neg_lo:[0,1] neg_hi:[0,1]\n\t"
      "v_pk_add_f32 v[176:177], v[176:177], v[216:217] neg_lo:[0,1] neg_hi:[0,1]\n\t"
      "v_pk_add_f32 v[180:181], v[180:181], v[216:217] neg_lo:[0,1] neg_hi:[0,1]\n\t"
      "v_pk_mul_f32 v[178:179], v[178:179], v[202:203]\n\t"
      "v_pk_mul_f32 v[182:183], v[182:183], v[202:203]\n\t"
      "v_pk_mul_f32 v[176:177], v[176:177], v[200:201]\n\t"
      "v_pk_mul_f32 v[180:181], v[180:181], v[200:201]\n\t"
      "v_exp_f32_e32 v179, v179\n\t"
      "v_exp_f32_e32 v183, v183\n\t"
      "v_exp_f32_e32 v178, v178\n\t"
      "v_exp_f32_e32 v182, v182\n\t"
      "v_exp_f32_e32 v177, v177\n\t"
      "v_exp_f32_e32 v181, v181\n\t"
      "v_exp_f32_e32 v176, v176\n\t"
      "v_exp_f32_e32 v180, v180\n\t"
      "v_mov_b32_e32 v250, s54\n\t"
      "v_mov_b32_e32 v251, s55\n\t"
      "v_mov_b32_e32 v252, s12\n\t"
      "s_mov_b64 exec, 1\n\t"
      "global_store_dwordx2 v252, v[250:251], s[10:11] sc1\n\t"
      "s_mov_b64 exec, -1\n\t"
      "s_waitcnt lgkmcnt(0)\n\t"
      "v_pk_add_f32 v[218:219], v[178:179], v[182:183]\n\t"
      "v_pk_mul_f32 v[178:179], v[178:179], v[206:207]\n\t"
      "v_pk_mul_f32 v[182:183], v[182:183], v[210:211]\n\t"
      "v_cvt_pk_bf16_f32 v179, v179, v179\n\t"
      "v_cvt_pk_bf16_f32 v183, v183, v183\n\t"
      "v_and_b32_e32 v72, v240, v179\n\t"
      "v_and_b32_e32 v73, v241, v183\n\t"
      "v_and_b32_e32 v74, v242, v179\n\t"
      "v_and_b32_e32 v75, v243, v183\n\t"
      "v_cvt_pk_bf16_f32 v178, v178, v178\n\t"
      "v_cvt_pk_bf16_f32 v182, v182, v182\n\t"
      "v_and_b32_e32 v48, v240, v178\n\t"
      "v_and_b32_e32 v49, v241, v182\n\t"
      "v_and_b32_e32 v50, v242, v178\n\t"
      "v_and_b32_e32 v51, v243, v182\n\t"
      "v_mfma_f32_16x16x32_bf16 v[88:91], v[162:165], v[72:75], 0\n\t"
      "v_mfma_f32_16x16x32_bf16 v[92:95], v[172:175], v[72:75], 0\n\t"
      "v_pk_add_f32 v[216:217], v[176:177], v[180:181]\n\t"
      "v_pk_mul_f32 v[176:177], v[176:177], v[204:205]\n\t"
      "v_pk_mul_f32 v[180:181], v[180:181], v[208:209]\n\t"
      "v_cvt_pk_bf16_f32 v177, v177, v177\n\t"
      "v_cvt_pk_bf16_f32 v181, v181, v181\n\t"
      "v_and_b32_e32 v24, v240, v177\n\t"
      "v_and_b32_e32 v25, v241, v181\n\t"
      "v_and_b32_e32 v26, v242, v177\n\t"
      "v_and_b32_e32 v27, v243, v181\n\t"
      "v_mfma_f32_16x16x32_bf16 v[64:67], v[160:163], v[48:51], 0\n\t"
      "v_mfma_f32_16x16x32_bf16 v[68:71], v[170:173], v[48:51], 0\n\t"
      "v_cvt_pk_bf16_f32 v176, v176, v176\n\t"
      "v_cvt_pk_bf16_f32 v180, v180, v180\n\t"
      "v_and_b32_e32 v0, v240, v176\n\t"
      "v_and_b32_e32 v1, v241, v180\n\t"
      "v_and_b32_e32 v2, v242, v176\n\t"
      "v_and_b32_e32 v3, v243, v180\n\t"
      "v_mfma_f32_16x16x32_bf16 v[40:43], v[158:161], v[24:27], 0\n\t"
      "v_mfma_f32_16x16x32_bf16 v[44:47], v[168:171], v[24:27], 0\n\t"
      "s_add_u32 s48, s4, 49152\n\t"
      "s_addc_u32 s49, s5, 0\n\t"
      "v_mfma_f32_16x16x32_bf16 v[16:19], v[156:159], v[0:3], 0\n\t"
      "v_mfma_f32_16x16x32_bf16 v[20:23], v[166:169], v[0:3], 0\n\t"
      "v_permlane16_swap_b32_e32 v218, v219\n\t"
      "v_permlane16_swap_b32_e32 v216, v217\n\t"
      "v_add_f32_e32 v218, v218, v219\n\t"
      "v_add_f32_e32 v216, v216, v217\n\t"
      "v_mov_b32_e32 v219, v218\n\t"
      "v_mov_b32_e32 v217, v216\n\t"
      "s_nop 0\n\t"
      "v_permlane32_swap_b32_e32 v218, v219\n\t"
      "v_permlane32_swap_b32_e32 v216, v217\n\t"
      "v_add_f32_e32 v218, v218, v219\n\t"
      "v_add_f32_e32 v216, v216, v217\n\t"
      "v_mov_b32_e32 v219, v218\n\t"
      "v_mov_b32_e32 v217, v216\n\t"
      "s_nop 0\n\t"
      "v_permlane16_swap_b32_e32 v218, v219\n\t"
      "v_permlane16_swap_b32_e32 v216, v217\n\t"
      "v_rcp_f32_e32 v190, v219\n\t"
      "v_rcp_f32_e32 v188, v218\n\t"
      "v_rcp_f32_e32 v186, v217\n\t"
      "v_rcp_f32_e32 v184, v216\n\t"
      "v_mov_b32_e32 v191, v190\n\t"
      "v_mov_b32_e32 v189, v188\n\t"
      "v_mov_b32_e32 v187, v186\n\t"
      "v_mov_b32_e32 v185, v184\n\t"
      "v_pk_mul_f32 v[88:89], v[190:191], v[88:89]\n\t"
      "v_pk_mul_f32 v[90:91], v[190:191], v[90:91]\n\t"
      "v_pk_mul_f32 v[92:93], v[190:191], v[92:93]\n\t"
      "v_pk_mul_f32 v[94:95], v[190:191], v[94:95]\n\t"
      "v_cvt_pk_bf16_f32 v80, v88, v89\n\t"
      "v_cvt_pk_bf16_f32 v81, v90, v91\n\t"
      "v_cvt_pk_bf16_f32 v82, v92, v93\n\t"
      "v_cvt_pk_bf16_f32 v83, v94, v95\n\t"
      "v_min_u32_e32 v80, -2, v80\n\t"
      "v_min_u32_e32 v81, -2, v81\n\t"
      "v_min_u32_e32 v82, -2, v82\n\t"
      "v_min_u32_e32 v83, -2, v83\n\t"
      "global_store_dwordx4 v239, v[80:83], s[48:49] sc1\n\t"
      "s_sub_u32 s48, s48, 16384\n\t"
      "s_subb_u32 s49, s49, 0\n\t"
      "v_pk_mul_f32 v[64:65], v[188:189], v[64:65]\n\t"
      "v_pk_mul_f32 v[66:67], v[188:189], v[66:67]\n\t"
      "v_pk_mul_f32 v[68:69], v[188:189], v[68:69]\n\t"
      "v_pk_mul_f32 v[70:71], v[188:189], v[70:71]\n\t"
      "v_cvt_pk_bf16_f32 v56, v64, v65\n\t"
      "v_cvt_pk_bf16_f32 v57, v66, v67\n\t"
      "v_cvt_pk_bf16_f32 v58, v68, v69\n\t"
      "v_cvt_pk_bf16_f32 v59, v70, v71\n\t"
      "v_min_u32_e32 v56, -2, v56\n\t"
      "v_min_u32_e32 v57, -2, v57\n\t"
      "v_min_u32_e32 v58, -2, v58\n\t"
      "v_min_u32_e32 v59, -2, v59\n\t"
      "global_store_dwordx4 v239, v[56:59], s[48:49] sc1\n\t"
      "s_sub_u32 s48, s48, 16384\n\t"
      "s_subb_u32 s49, s49, 0\n\t"
      "v_pk_mul_f32 v[40:41], v[186:187], v[40:41]\n\t"
      "v_pk_mul_f32 v[42:43], v[186:187], v[42:43]\n\t"
      "v_pk_mul_f32 v[44:45], v[186:187], v[44:45]\n\t"
      "v_pk_mul_f32 v[46:47], v[186:187], v[46:47]\n\t"
      "v_cvt_pk_bf16_f32 v32, v40, v41\n\t"
      "v_cvt_pk_bf16_f32 v33, v42, v43\n\t"
      "v_cvt_pk_bf16_f32 v34, v44, v45\n\t"
      "v_cvt_pk_bf16_f32 v35, v46, v47\n\t"
      "v_min_u32_e32 v32, -2, v32\n\t"
      "v_min_u32_e32 v33, -2, v33\n\t"
      "v_min_u32_e32 v34, -2, v34\n\t"
      "v_min_u32_e32 v35, -2, v35\n\t"
      "global_store_dwordx4 v239, v[32:35], s[48:49] sc1\n\t"
      "s_sub_u32 s48, s48, 16384\n\t"
      "s_subb_u32 s49, s49, 0\n\t"
      "v_pk_mul_f32 v[16:17], v[184:185], v[16:17]\n\t"
      "v_pk_mul_f32 v[18:19], v[184:185], v[18:19]\n\t"
      "v_pk_mul_f32 v[20:21], v[184:185], v[20:21]\n\t"
      "v_pk_mul_f32 v[22:23], v[184:185], v[22:23]\n\t"
      "v_cvt_pk_bf16_f32 v8, v16, v17\n\t"
      "v_cvt_pk_bf16_f32 v9, v18, v19\n\t"
      "v_cvt_pk_bf16_f32 v10, v20, v21\n\t"
      "v_cvt_pk_bf16_f32 v11, v22, v23\n\t"
      "v_min_u32_e32 v8, -2, v8\n\t"
      "v_min_u32_e32 v9, -2, v9\n\t"
      "v_min_u32_e32 v10, -2, v10\n\t"
      "v_min_u32_e32 v11, -2, v11\n\t"
      "global_store_dwordx4 v239, v[8:11], s[48:49] sc1\n\t"
      "s_barrier\n\t"
      "s_nop 1\n\t"
      "s_branch CEND_%=\n\t"
      "LOOP1_%=:\n\t"
      "s_waitcnt vmcnt(0)\n\t"
      "s_barrier\n\t"
      "ds_read_b64_tr_b16 v[108:109], v226\n\t"
      "ds_read_b64_tr_b16 v[110:111], v226 offset:512\n\t"
      "ds_read_b128 v[132:135], v224\n\t"
      "ds_read_b128 v[136:139], v224 offset:5120\n\t"
      "ds_read_b128 v[140:143], v224 offset:10240\n\t"
      "ds_read_b128 v[144:147], v224 offset:15360\n\t"
      "ds_read_b64_tr_b16 v[112:113], v227\n\t"
      "ds_read_b64_tr_b16 v[114:115], v227 offset:512\n\t"
      "ds_read_b128 v[148:151], v225\n\t"
      "ds_read_b64_tr_b16 v[116:117], v226 offset:1024\n\t"
      "ds_read_b64_tr_b16 v[118:119], v226 offset:1536\n\t"
      "ds_read_b64_tr_b16 v[120:121], v227 offset:1024\n\t"
      "ds_read_b64_tr_b16 v[122:123], v227 offset:1536\n\t"
      "ds_read_b64_tr_b16 v[124:125], v226 offset:2048\n\t"
      "ds_read_b64_tr_b16 v[126:127], v226 offset:2560\n\t"
      "ds_read_b64_tr_b16 v[128:129], v227 offset:2048\n\t"
      "ds_read_b64_tr_b16 v[130:131], v227 offset:2560\n\t"
      "s_branch EVEN1_%=\n\t"
      "s_waitcnt lgkmcnt(0)\n\t"
      ".p2align 6\n\t"
      "ODD1_%=:\n\t"
      "s_waitcnt lgkmcnt(0)\n\t"
      "v_mfma_f32_16x16x32_bf16 v[0:3], v[152:155], v[176:179], v[0:3]\n\t"
      "v_add_u32_e32 v224, s46, v224\n\t"
      "v_add_u32_e32 v226, s46, v226\n\t"
      "v_mfma_f32_16x16x32_bf16 v[24:27], v[152:155], v[180:183], v[24:27]\n\t"
      "v_add_u32_e32 v227, s46, v227\n\t"
      "v_add_u32_e32 v225, s46, v225\n\t"
      "v_mfma_f32_16x16x32_bf16 v[48:51], v[152:155], v[184:187], v[48:51]\n\t"
      "s_barrier\n\t"
      "ds_read_b64_tr_b16 v[108:109], v226\n\t"
      "v_mfma_f32_16x16x32_bf16 v[72:75], v[152:155], v[188:191], v[72:75]\n\t"
      "ds_read_b64_tr_b16 v[110:111], v226 offset:512\n\t"
      "v_mfma_f32_16x16x32_bf16 v[4:7], v[156:159], v[176:179], v[4:7]\n\t"
      "ds_read_b128 v[132:135], v224\n\t"
      "v_mfma_f32_16x16x32_bf16 v[28:31], v[156:159], v[180:183], v[28:31]\n\t"
      "ds_read_b128 v[136:139], v224 offset:5120\n\t"
      "v_mfma_f32_16x16x32_bf16 v[52:55], v[156:159], v[184:187], v[52:55]\n\t"
      "ds_read_b128 v[140:143], v224 offset:10240\n\t"
      "v_mfma_f32_16x16x32_bf16 v[76:79], v[156:159], v[188:191], v[76:79]\n\t"
      "ds_read_b128 v[144:147], v224 offset:15360\n\t"
      "v_mfma_f32_16x16x32_bf16 v[96:99], v[156:159], v[192:195], v[96:99]\n\t"
      "v_mfma_f32_16x16x32_bf16 v[8:11], v[160:163], v[176:179], v[8:11]\n\t"
      "ds_read_b64_tr_b16 v[112:113], v227\n\t"
      "v_mfma_f32_16x16x32_bf16 v[32:35], v[160:163], v[180:183], v[32:35]\n\t"
      "ds_read_b64_tr_b16 v[114:115], v227 offset:512\n\t"
      "v_mfma_f32_16x16x32_bf16 v[56:59], v[160:163], v[184:187], v[56:59]\n\t"
      "ds_read_b128 v[148:151], v225\n\t"
      "v_mfma_f32_16x16x32_bf16 v[80:83], v[160:163], v[188:191], v[80:83]\n\t"
      "ds_read_b64_tr_b16 v[116:117], v226 offset:1024\n\t"
      "v_mfma_f32_16x16x32_bf16 v[12:15], v[164:167], v[176:179], v[12:15]\n\t"
      "ds_read_b64_tr_b16 v[118:119], v226 offset:1536\n\t"
      "v_mfma_f32_16x16x32_bf16 v[36:39], v[164:167], v[180:183], v[36:39]\n\t"
      "ds_read_b64_tr_b16 v[120:121], v227 offset:1024\n\t"
      "v_mfma_f32_16x16x32_bf16 v[60:63], v[164:167], v[184:187], v[60:63]\n\t"
      "v_mfma_f32_16x16x32_bf16 v[84:87], v[164:167], v[188:191], v[84:87]\n\t"
      "ds_read_b64_tr_b16 v[122:123], v227 offset:1536\n\t"
      "v_mfma_f32_16x16x32_bf16 v[100:103], v[164:167], v[192:195], v[100:103]\n\t"
      "ds_read_b64_tr_b16 v[124:125], v226 offset:2048\n\t"
      "v_mfma_f32_16x16x32_bf16 v[16:19], v[176:179], v[168:171], v[16:19]\n\t"
      "ds_read_b64_tr_b16 v[126:127], v226 offset:2560\n\t"
      "v_mfma_f32_16x16x32_bf16 v[40:43], v[180:183], v[168:171], v[40:43]\n\t"
      "ds_read_b64_tr_b16 v[128:129], v227 offset:2048\n\t"
      "v_mfma_f32_16x16x32_bf16 v[64:67], v[184:187], v[168:171], v[64:67]\n\t"
      "ds_read_b64_tr_b16 v[130:131], v227 offset:2560\n\t"
      "v_mfma_f32_16x16x32_bf16 v[88:91], v[188:191], v[168:171], v[88:91]\n\t"
      "v_mfma_f32_16x16x32_bf16 v[20:23], v[176:179], v[172:175], v[20:23]\n\t"
      "v_mfma_f32_16x16x32_bf16 v[44:47], v[180:183], v[172:175], v[44:47]\n\t"
      "v_add_u32_e32 v248, s46, v248\n\t"
      "v_add_u32_e32 v249, s46, v249\n\t"
      "v_mfma_f32_16x16x32_bf16 v[68:71], v[184:187], v[172:175], v[68:71]\n\t"
      "v_mfma_f32_16x16x32_bf16 v[92:95], v[188:191], v[172:175], v[92:95]\n\t"
      "v_mfma_f32_16x16x32_bf16 v[104:107], v[192:195], v[172:175], v[104:107]\n\t"
      "EVEN1_%=:\n\t"
      "s_waitcnt lgkmcnt(0)\n\t"
      "v_mfma_f32_16x16x32_bf16 v[0:3], v[108:111], v[132:135], v[0:3]\n\t"
      "ds_read_b64_tr_b16 v[152:153], v226 offset:20480\n\t"
      "v_mfma_f32_16x16x32_bf16 v[24:27], v[108:111], v[136:139], v[24:27]\n\t"
      "ds_read_b64_tr_b16 v[154:155], v226 offset:20992\n\t"
      "v_mfma_f32_16x16x32_bf16 v[48:51], v[108:111], v[140:143], v[48:51]\n\t"
      "ds_read_b128 v[176:179], v248\n\t"
      "v_mfma_f32_16x16x32_bf16 v[72:75], v[108:111], v[144:147], v[72:75]\n\t"
      "ds_read_b128 v[180:183], v248 offset:5120\n\t"
      "v_mfma_f32_16x16x32_bf16 v[4:7], v[112:115], v[132:135], v[4:7]\n\t"
      "v_mfma_f32_16x16x32_bf16 v[28:31], v[112:115], v[136:139], v[28:31]\n\t"
      "ds_read_b128 v[184:187], v248 offset:10240\n\t"
      "v_mfma_f32_16x16x32_bf16 v[52:55], v[112:115], v[140:143], v[52:55]\n\t"
      "ds_read_b128 v[188:191], v248 offset:15360\n\t"
      "v_mfma_f32_16x16x32_bf16 v[76:79], v[112:115], v[144:147], v[76:79]\n\t"
      "ds_read_b64_tr_b16 v[156:157], v227 offset:20480\n\t"
      "v_mfma_f32_16x16x32_bf16 v[96:99], v[112:115], v[148:151], v[96:99]\n\t"
      "v_mfma_f32_16x16x32_bf16 v[8:11], v[116:119], v[132:135], v[8:11]\n\t"
      "ds_read_b64_tr_b16 v[158:159], v227 offset:20992\n\t"
      "v_mfma_f32_16x16x32_bf16 v[32:35], v[116:119], v[136:139], v[32:35]\n\t"
      "ds_read_b128 v[192:195], v249\n\t"
      "v_mfma_f32_16x16x32_bf16 v[56:59], v[116:119], v[140:143], v[56:59]\n\t"
      "ds_read_b64_tr_b16 v[160:161], v226 offset:21504\n\t"
      "v_mfma_f32_16x16x32_bf16 v[80:83], v[116:119], v[144:147], v[80:83]\n\t"
      "ds_read_b64_tr_b16 v[162:163], v226 offset:22016\n\t"
      "v_mfma_f32_16x16x32_bf16 v[12:15], v[120:123], v[132:135], v[12:15]\n\t"
      "v_mfma_f32_16x16x32_bf16 v[36:39], v[120:123], v[136:139], v[36:39]\n\t"
      "ds_read_b64_tr_b16 v[164:165], v227 offset:21504\n\t"
      "v_mfma_f32_16x16x32_bf16 v[60:63], v[120:123], v[140:143], v[60:63]\n\t"
      "ds_read_b64_tr_b16 v[166:167], v227 offset:22016\n\t"
      "v_mfma_f32_16x16x32_bf16 v[84:87], v[120:123], v[144:147], v[84:87]\n\t"
      "ds_read_b64_tr_b16 v[168:169], v226 offset:22528\n\t"
      "v_mfma_f32_16x16x32_bf16 v[100:103], v[120:123], v[148:151], v[100:103]\n\t"
      "v_mfma_f32_16x16x32_bf16 v[16:19], v[132:135], v[124:127], v[16:19]\n\t"
      "ds_read_b64_tr_b16 v[170:171], v226 offset:23040\n\t"
      "v_mfma_f32_16x16x32_bf16 v[40:43], v[136:139], v[124:127], v[40:43]\n\t"
      "ds_read_b64_tr_b16 v[172:173], v227 offset:22528\n\t"
      "v_mfma_f32_16x16x32_bf16 v[64:67], v[140:143], v[124:127], v[64:67]\n\t"
      "ds_read_b64_tr_b16 v[174:175], v227 offset:23040\n\t"
      "s_cmp_eq_u32 s47, 4\n\t"
      "s_cselect_b32 s46, -2, 1\n\t"
      "v_mfma_f32_16x16x32_bf16 v[88:91], v[144:147], v[124:127], v[88:91]\n\t"
      "s_cmp_eq_u32 s47, 1\n\t"
      "s_cselect_b32 s46, -2, s46\n\t"
      "v_mfma_f32_16x16x32_bf16 v[20:23], v[132:135], v[128:131], v[20:23]\n\t"
      "s_mul_i32 s46, s46, 43008\n\t"
      "v_mfma_f32_16x16x32_bf16 v[44:47], v[136:139], v[128:131], v[44:47]\n\t"
      "v_mfma_f32_16x16x32_bf16 v[68:71], v[140:143], v[128:131], v[68:71]\n\t"
      "v_mfma_f32_16x16x32_bf16 v[92:95], v[144:147], v[128:131], v[92:95]\n\t"
      "s_sub_u32 s47, s47, 1\n\t"
      "v_mfma_f32_16x16x32_bf16 v[104:107], v[148:151], v[128:131], v[104:107]\n\t"
      "s_cbranch_scc0 ODD1_%=\n\t"
      "s_waitcnt lgkmcnt(0)\n\t"
      "v_mfma_f32_16x16x32_bf16 v[96:99], v[156:159], v[192:195], v[96:99]\n\t"
      "v_mov_b32_e32 v212, s9\n\t"
      "ds_read_b32 v212, v212\n\t"
      "ds_read2_b32 v[128:129], v234 offset0:32 offset1:32\n\t"
      "v_mfma_f32_16x16x32_bf16 v[100:103], v[164:167], v[192:195], v[100:103]\n\t"
      "ds_read2_b32 v[130:131], v234 offset0:48 offset1:48\n\t"
      "v_mfma_f32_16x16x32_bf16 v[104:107], v[192:195], v[172:175], v[104:107]\n\t"
      "ds_read2_b32 v[132:133], v234 offset0:64 offset1:64\n\t"
      "ds_read2_b32 v[134:135], v234 offset0:80 offset1:80\n\t"
      "v_mfma_f32_16x16x32_bf16 v[80:83], v[160:163], v[188:191], v[80:83]\n\t"
      "ds_read2_b32 v[248:249], v234 offset0:0 offset1:0\n\t"
      "v_mfma_f32_16x16x32_bf16 v[84:87], v[164:167], v[188:191], v[84:87]\n\t"
      "ds_read_b128 v[196:199], v233\n\t"
      "ds_read_b128 v[136:139], v233 offset:128\n\t"
      "v_mfma_f32_16x16x32_bf16 v[88:91], v[188:191], v[168:171], v[88:91]\n\t"
      "ds_read_b128 v[140:143], v233 offset:192\n\t"
      "v_mfma_f32_16x16x32_bf16 v[92:95], v[188:191], v[172:175], v[92:95]\n\t"
      "ds_read_b128 v[144:147], v233 offset:256\n\t"
      "ds_read_b128 v[148:151], v233 offset:320\n\t"
      "v_mfma_f32_16x16x32_bf16 v[56:59], v[160:163], v[184:187], v[56:59]\n\t"
      "s_mov_b64 exec, s[44:45]\n\t"
      "ds_write_b128 v230, v[96:99]\n\t"
      "ds_write_b128 v230, v[100:103] offset:128\n\t"
      "s_mov_b64 exec, 0xffff\n\t"
      "ds_write_b32 v231, v104 offset:256\n\t"
      "v_xor_b32_e32 v227, 8, v232\n\t"
      "ds_write_b32 v227, v227\n\t"
      "s_mov_b64 exec, -1\n\t"
      "v_mfma_f32_16x16x32_bf16 v[60:63], v[164:167], v[184:187], v[60:63]\n\t"
      "v_mfma_f32_16x16x32_bf16 v[64:67], v[184:187], v[168:171], v[64:67]\n\t"
      "v_mfma_f32_16x16x32_bf16 v[68:71], v[184:187], v[172:175], v[68:71]\n\t"
      "s_waitcnt lgkmcnt(4)\n\t"
      "v_mfma_f32_16x16x32_bf16 v[32:35], v[160:163], v[180:183], v[32:35]\n\t"
      "ds_read_b32 v226, v232\n\t"
      "ds_read_b128 v[116:119], v228 offset:128\n\t"
      "v_mfma_f32_16x16x32_bf16 v[36:39], v[164:167], v[180:183], v[36:39]\n\t"
      "ds_read_b128 v[120:123], v228 offset:192\n\t"
      "ds_read2_b32 v[250:251], v229 offset0:64 offset1:64\n\t"
      "v_mfma_f32_16x16x32_bf16 v[40:43], v[180:183], v[168:171], v[40:43]\n\t"
      "ds_read2_b32 v[252:253], v229 offset0:80 offset1:80\n\t"
      "ds_read_b128 v[124:127], v230 offset:128\n\t"
      "v_mfma_f32_16x16x32_bf16 v[44:47], v[180:183], v[172:175], v[44:47]\n\t"
      "ds_read2_b32 v[224:225], v231 offset0:64 offset1:64\n\t"
      "ds_read_b128 v[108:111], v228\n\t"
      "v_mfma_f32_16x16x32_bf16 v[8:11], v[160:163], v[176:179], v[8:11]\n\t"
      "ds_read_b128 v[112:115], v228 offset:64\n\t"
      "ds_read2_b32 v[204:205], v235 offset0:0 offset1:16\n\t"
      "v_mfma_f32_16x16x32_bf16 v[12:15], v[164:167], v[176:179], v[12:15]\n\t"
      "ds_read2_b32 v[206:207], v235 offset0:32 offset1:48\n\t"
      "ds_read2_b32 v[208:209], v254 offset0:0 offset1:16\n\t"
      "v_mfma_f32_16x16x32_bf16 v[16:19], v[176:179], v[168:171], v[16:19]\n\t"
      "ds_read2_b32 v[210:211], v254 offset0:32 offset1:48\n\t"
      "ds_read2_b32 v[200:201], v234 offset0:192 offset1:208\n\t"
      "v_mfma_f32_16x16x32_bf16 v[20:23], v[176:179], v[172:175], v[20:23]\n\t"
      "ds_read2_b32 v[202:203], v234 offset0:224 offset1:240\n\t"
      "s_branch LOOPX_%=\n\t"
      "XAR_%=:\n\t"
      "s_mov_b32 s51, 0\n\t"
      "XAL_%=:\n\t"
      "s_add_u32 s51, s51, 1\n\t"
      "s_cmp_lt_u32 s51, 262144\n\t"
      "s_cbranch_scc0 XAX_%=\n\t"
      "s_waitcnt lgkmcnt(0)\n\t"
      "v_mov_b32_e32 v212, s9\n\t"
      "ds_read_b32 v212, v212\n\t"
      "ds_read2_b32 v[128:129], v234 offset0:32 offset1:32\n\t"
      "ds_read2_b32 v[130:131], v234 offset0:48 offset1:48\n\t"
      "ds_read2_b32 v[132:133], v234 offset0:64 offset1:64\n\t"
      "ds_read2_b32 v[134:135], v234 offset0:80 offset1:80\n\t"
      "ds_read2_b32 v[248:249], v234 offset0:0 offset1:0\n\t"
      "ds_read_b128 v[196:199], v233\n\t"
      "ds_read_b128 v[136:139], v233 offset:128\n\t"
      "ds_read_b128 v[140:143], v233 offset:192\n\t"
      "ds_read_b128 v[144:147], v233 offset:256\n\t"
      "ds_read_b128 v[148:151], v233 offset:320\n\t"
      "s_waitcnt lgkmcnt(0)\n\t"
      "ds_read_b32 v226, v232\n\t"
      "ds_read_b128 v[116:119], v228 offset:128\n\t"
      "ds_read_b128 v[120:123], v228 offset:192\n\t"
      "ds_read2_b32 v[250:251], v229 offset0:64 offset1:64\n\t"
      "ds_read2_b32 v[252:253], v229 offset0:80 offset1:80\n\t"
      "ds_read_b128 v[124:127], v230 offset:128\n\t"
      "ds_read2_b32 v[224:225], v231 offset0:64 offset1:64\n\t"
      "ds_read_b128 v[108:111], v228\n\t"
      "ds_read_b128 v[112:115], v228 offset:64\n\t"
      "ds_read2_b32 v[204:205], v235 offset0:0 offset1:16\n\t"
      "ds_read2_b32 v[206:207], v235 offset0:32 offset1:48\n\t"
      "ds_read2_b32 v[208:209], v254 offset0:0 offset1:16\n\t"
      "ds_read2_b32 v[210:211], v254 offset0:32 offset1:48\n\t"
      "ds_read2_b32 v[200:201], v234 offset0:192 offset1:208\n\t"
      "ds_read2_b32 v[202:203], v234 offset0:224 offset1:240\n\t"
      "s_waitcnt lgkmcnt(0)\n\t"
      "v_min_u32_e32 v226, v226, v212\n\t"
      "v_cmp_eq_u32_e32 vcc, 0, v226\n\t"
      "s_cbranch_vccnz XAL_%=\n\t"
      "XAX_%=:\n\t"
      "s_branch XAB_%=\n\t"
      "XBR_%=:\n\t"
      "s_mov_b32 s51, 0\n\t"
      "XBL_%=:\n\t"
      "s_add_u32 s51, s51, 1\n\t"
      "s_cmp_lt_u32 s51, 262144\n\t"
      "s_cbranch_scc0 XBX_%=\n\t"
      "s_waitcnt lgkmcnt(0)\n\t"
      "ds_read_b32 v226, v232 offset:4\n\t"
      "ds_read_b128 v[152:155], v236\n\t"
      "ds_read_b64 v[156:157], v237 offset:1024\n\t"
      "ds_read_b64 v[166:167], v237 offset:1536\n\t"
      "s_waitcnt lgkmcnt(0)\n\t"
      "v_cmp_eq_u32_e32 vcc, 0, v226\n\t"
      "s_cbranch_vccnz XBL_%=\n\t"
      "XBX_%=:\n\t"
      "s_branch XBB_%=\n\t"
      "CEND_%=:\n\t"
      : "+{s[4:11]}"(s), "+{s12}"(hint), "+{v[224:239]}"(v), "+{v254}"(vk1)
      :
      : "memory", "vcc", "scc",
        "v0", "v1", "v2", "v3", "v4", "v5", "v6", "v7", "v8", "v9", "v10", "v11", "v12", "v13", "v14", "v15", "v16",
        "v17", "v18", "v19", "v20", "v21", "v22", "v23", "v24", "v25", "v26", "v27", "v28", "v29", "v30", "v31", "v32",
        "v33", "v34", "v35", "v36", "v37", "v38", "v39", "v40", "v41", "v42", "v43", "v44", "v45", "v46", "v47", "v48",
        "v49", "v50", "v51", "v52", "v53", "v54", "v55", "v56", "v57", "v58", "v59", "v60", "v61", "v62", "v63", "v64",
        "v65", "v66", "v67", "v68", "v69", "v70", "v71", "v72", "v73", "v74", "v75", "v76", "v77", "v78", "v79", "v80",
        "v81", "v82", "v83", "v84", "v85", "v86", "v87", "v88", "v89", "v90", "v91", "v92", "v93", "v94", "v95", "v96",
        "v97", "v98", "v99", "v100", "v101", "v102", "v103", "v104", "v105", "v106", "v107", "v108", "v109", "v110",
        "v111", "v112", "v113", "v114", "v115", "v116", "v117", "v118", "v119", "v120", "v121", "v122", "v123", "v124",
        "v125", "v126", "v127", "v128", "v129", "v130", "v131", "v132", "v133", "v134", "v135", "v136", "v137", "v138",
        "v139", "v140", "v141", "v142", "v143", "v144", "v145", "v146", "v147", "v148", "v149", "v150", "v151", "v152",
        "v153", "v154", "v155", "v156", "v157", "v158", "v159", "v160", "v161", "v162", "v163", "v164", "v165", "v166",
        "v167", "v168", "v169", "v170", "v171", "v172", "v173", "v174", "v175", "v176", "v177", "v178", "v179", "v180",
        "v181", "v182", "v183", "v184", "v185", "v186", "v187", "v188", "v189", "v190", "v191", "v192", "v193", "v194",
        "v195", "v196", "v197", "v198", "v199", "v200", "v201", "v202", "v203", "v204", "v205", "v206", "v207", "v208",
        "v209", "v210", "v211", "v212", "v213", "v214", "v215", "v216", "v217", "v218", "v219", "v220", "v221", "v222",
        "v223", "v240", "v241", "v242", "v243", "v244", "v245", "v246", "v247", "v248", "v249", "v250", "v251", "v252",
        "v253", "s36", "s37", "s38", "s39", "s40", "s41", "s42", "s43", "s44", "s45", "s46", "s47", "s48", "s49", "s50",
        "s51", "s52", "s53", "s54", "s55"
    );
    } else {
      LANE_BITS(threadIdx.x & 63);
      u32x8 v;
      u32x16 s;
      v[0] = ((8 * rt + 4 * half + g) * 16 + c) * 16;
      v[1] = (max(8 * rt + ((g & 1) ? 0 : 4 * half) - 1, 0) * 16 + c) * 16;
      v[2] = sta + 128 + lane * 4;
      v[3] = sta + ((g & 1) ? 0 : 64) + c * 4;
      v[4] = lr * A_SLOT + A_HALO + lc * 16;
      v[5] = (lane >> 2) * 3584 + (p4 + ((7 * xcd) >> 1)) * 128;
      u64 tw = (u64)(w1 + (idx * 4 + wave - 4) * 16 * 3584);
      put(s, 0, (u64)(x + (rt * 128 * 896 + 128) - (16 * 896 + 1024)));
      put(s, 2, (u64)(wqkv + (hp * 112 + 64 * 2688) - 4096));
      s[5] = wave * A_UNIT;
      put2(s, 6, wave > 4 ? 0xffffffffu : 0xffffff00u, 0u);
      s[8] = A_HALO + (wave - 4) * 512;
      s[9] = A_FL + 32 + 4 * half;
      put(s, 10, (u64)(ws + WS_ST));
    asm volatile(
      "s_waitcnt lgkmcnt(0)\n\ts_nop 4\n\t"
      "s_load_dwordx2 s[18:19], s[14:15], 0x20000\n\t"
      "s_sub_u32 s40, s4, 57344\n\t"
      "s_subb_u32 s41, s5, 0\n\t"
      "s_sub_u32 s42, s6, 86016\n\t"
      "s_subb_u32 s43, s7, 0\n\t"
      "s_setprio 2\n\t"
      "v_mov_b32_e32 v0, 0x3f803f80\n\t"
      "v_mov_b32_e32 v1, 0x3f803f80\n\t"
      "v_mov_b32_e32 v2, 0x3f803f80\n\t"
      "v_mov_b32_e32 v3, 0x3f803f80\n\t"
      "s_mov_b64 exec, 0xffffff\n\t"
      "ds_write_b128 v204, v[0:3]\n\t"
      "s_mov_b64 exec, -1\n\t"
      "v_mov_b32_e32 v32, 154624\n\t"
      "v_mov_b32_e32 v33, s13\n\t"
      "v_mov_b32_e32 v36, 0\n\t"
      "v_mov_b32_e32 v37, 0\n\t"
      "v_mov_b32_e32 v38, 0\n\t"
      "v_mov_b32_e32 v39, 0\n\t"
      "ds_write_b128 v32, v[36:39]\n\t"
      "ds_write_b128 v32, v[36:39] offset:16\n\t"
      "ds_write_b128 v32, v[36:39] offset:32\n\t"
      "s_add_u32 s44, s9, 86016\n\t"
      "s_add_u32 s45, s12, 86016\n\t"
      "s_mov_b32 s46, 4\n\t"
      "s_add_u32 m0, s9, 22528\n\t"
      "s_nop 0\n\t"
      "global_load_lds_dwordx4 v195, s[42:43] offset:2048\n\t"
      "global_load_lds_dwordx4 v192, s[40:41]\n\t"
      "global_load_lds_dwordx4 v193, s[40:41] offset:1024\n\t"
      "global_load_lds_dwordx4 v196, s[42:43] offset:3072\n\t"
      "s_add_u32 m0, s9, 26624\n\t"
      "s_nop 0\n\t"
      "global_load_lds_dwordx4 v197, s[42:43]\n\t"
      "s_waitcnt lgkmcnt(0)\n\t"
      "s_add_u32 s18, s18, 1\n\t"
      "s_addc_u32 s19, s19, 0\n\t"
      "s_mul_i32 s48, s18, 0x9e3779b9\n\t"
      "s_mul_hi_u32 s17, s18, 0x7f4a7c15\n\t"
      "s_add_u32 s17, s17, s48\n\t"
      "s_mul_i32 s48, s19, 0x7f4a7c15\n\t"
      "s_add_u32 s17, s17, s48\n\t"
      "s_mul_i32 s16, s18, 0x7f4a7c15\n\t"
      "s_add_u32 s16, s16, 0xd192ed03\n\t"
      "s_addc_u32 s17, s17, 0xd1b54a32\n\t"
      "v_mov_b32_e32 v12, s16\n\t"
      "v_mov_b32_e32 v13, s17\n\t"
      "v_mov_b32_e32 v14, s18\n\t"
      "v_mov_b32_e32 v15, s19\n\t"
      "v_mov_b32_e32 v16, 163312\n\t"
      "s_mov_b64 exec, 1\n\t"
      "ds_write_b128 v16, v[12:15]\n\t"
      "s_mov_b64 exec, -1\n\t"
      "s_waitcnt vmcnt(5) lgkmcnt(0)\n\t"
      "s_barrier\n\t"
      "s_add_u32 m0, s9, 43008\n\t"
      "s_nop 0\n\t"
      "global_load_lds_dwordx4 v195, s[6:7] offset:2048\n\t"
      "global_load_lds_dwordx4 v192, s[4:5]\n\t"
      "global_load_lds_dwordx4 v193, s[4:5] offset:1024\n\t"
      "global_load_lds_dwordx4 v196, s[6:7] offset:3072\n\t"
      "s_add_u32 m0, s9, 47104\n\t"
      "s_nop 0\n\t"
      "global_load_lds_dwordx4 v197, s[6:7]\n\t"
      "s_add_u32 m0, s12, 43008\n\t"
      "s_mov_b64 exec, s[10:11]\n\t"
      "global_load_lds_dwordx4 v194, s[4:5]\n\t"
      "s_mov_b64 exec, -1\n\t"
      "s_branch HM_%=\n\t"
      ".p2align 6\n\t"
      "HL_%=:\n\t"
      "s_waitcnt vmcnt(11)\n\t"
      "s_barrier\n\t"
      "HM_%=:\n\t"
      "s_sub_u32 s46, s46, 1\n\t"
      "s_cbranch_scc0 HG_%=\n\t"
      "global_load_dwordx4 v[4:7], v200, s[14:15] sc1\n\t"
      "global_load_dwordx4 v[8:11], v201, s[14:15] sc1\n\t"
      "HG_%=:\n\t"
      "s_add_u32 s4, s4, 128\n\t"
      "s_addc_u32 s5, s5, 0\n\t"
      "s_add_u32 s6, s6, 172032\n\t"
      "s_addc_u32 s7, s7, 0\n\t"
      "s_add_u32 s40, s40, 128\n\t"
      "s_addc_u32 s41, s41, 0\n\t"
      "s_add_u32 s42, s42, 172032\n\t"
      "s_addc_u32 s43, s43, 0\n\t"
      "s_add_u32 m0, s44, 4294946816\n\t"
      "s_nop 0\n\t"
      "global_load_lds_dwordx4 v195, s[42:43] offset:2048\n\t"
      "global_load_lds_dwordx4 v192, s[40:41]\n\t"
      "global_load_lds_dwordx4 v193, s[40:41] offset:1024\n\t"
      "global_load_lds_dwordx4 v196, s[42:43] offset:3072\n\t"
      "s_add_u32 m0, s44, 4294950912\n\t"
      "s_nop 0\n\t"
      "global_load_lds_dwordx4 v197, s[42:43]\n\t"
      "s_add_u32 m0, s44, 0\n\t"
      "s_nop 0\n\t"
      "global_load_lds_dwordx4 v195, s[6:7] offset:2048\n\t"
      "global_load_lds_dwordx4 v192, s[4:5]\n\t"
      "global_load_lds_dwordx4 v193, s[4:5] offset:1024\n\t"
      "global_load_lds_dwordx4 v196, s[6:7] offset:3072\n\t"
      "s_add_u32 m0, s44, 4096\n\t"
      "s_nop 0\n\t"
      "global_load_lds_dwordx4 v197, s[6:7]\n\t"
      "s_mov_b32 m0, s45\n\t"
      "s_mov_b64 exec, s[10:11]\n\t"
      "global_load_lds_dwordx4 v194, s[4:5]\n\t"
      "s_mov_b64 exec, -1\n\t"
      "s_cmp_ge_u32 s44, 86016\n\t"
      "s_cselect_b32 s48, -2, 1\n\t"
      "s_mul_i32 s48, s48, 43008\n\t"
      "s_add_u32 s44, s44, s48\n\t"
      "s_add_u32 s45, s45, s48\n\t"
      "s_cmp_lt_i32 s46, 0\n\t"
      "s_cbranch_scc0 HL_%=\n\t"
      "s_waitcnt vmcnt(13)\n\t"
      "s_barrier\n\t"
      "s_mov_b32 m0, 163328\n\t"
      "s_nop 0\n\t"
      "global_load_lds_dword v205, s[20:21]\n\t"
      "s_add_u32 s20, s20, 3670016\n\t"
      "s_addc_u32 s21, s21, 0\n\t"
      "s_waitcnt vmcnt(12)\n\t"
      "v_xor_b32_e32 v6, v6, v4\n\t"
      "v_xor_b32_e32 v7, v7, v5\n\t"
      "v_xor_b32_e32 v10, v10, v8\n\t"
      "v_xor_b32_e32 v11, v11, v9\n\t"
      "v_cmp_ne_u64_e64 s[48:49], s[16:17], v[6:7]\n\t"
      "v_cmp_ne_u64_e32 vcc, s[16:17], v[10:11]\n\t"
      "s_nop 0\n\t"
      "s_or_b64 vcc, vcc, s[48:49]\n\t"
      "s_cbranch_scc1 GRR_%=\n\t"
      "GRB_%=:\n\t"
      "v_mul_f32_e32 v6, 0x3e8b97f6, v5\n\t"
      "ds_write2_b32 v202, v4, v5 offset0:0 offset1:96\n\t"
      "ds_write2_b32 v203, v8, v9 offset0:0 offset1:96\n\t"
      "ds_write_b32 v202, v6 offset:640\n\t"
      "s_mov_b64 exec, 1\n\t"
      "ds_write_b32 v33, v33\n\t"
      "s_mov_b64 exec, -1\n\t"
      "s_waitcnt vmcnt(1)\n\t"
      "s_barrier\n\t"
      "s_setprio 0\n\t"
      "s_waitcnt vmcnt(0)\n\t"
      "s_mov_b32 m0, 163328\n\t"
      "s_nop 0\n\t"
      "global_load_lds_dword v205, s[20:21]\n\t"
      "s_add_u32 s20, s20, 3670016\n\t"
      "s_addc_u32 s21, s21, 0\n\t"
      "s_waitcnt lgkmcnt(0)\n\t"
      "s_barrier\n\t"
      "s_branch HEND_%=\n\t"
      "GRR_%=:\n\t"
      "s_mov_b32 s51, 0\n\t"
      "GRL_%=:\n\t"
      "s_add_u32 s51, s51, 1\n\t"
      "s_cmp_lt_u32 s51, 262144\n\t"
      "s_cbranch_scc0 GRX_%=\n\t"
      "s_sleep 2\n\t"
      "global_load_dwordx4 v[4:7], v200, s[14:15] sc1\n\t"
      "global_load_dwordx4 v[8:11], v201, s[14:15] sc1\n\t"
      "s_waitcnt vmcnt(0)\n\t"
      "v_xor_b32_e32 v6, v6, v4\n\t"
      "v_xor_b32_e32 v7, v7, v5\n\t"
      "v_xor_b32_e32 v10, v10, v8\n\t"
      "v_xor_b32_e32 v11, v11, v9\n\t"
      "v_cmp_ne_u64_e64 s[48:49], s[16:17], v[6:7]\n\t"
      "v_cmp_ne_u64_e32 vcc, s[16:17], v[10:11]\n\t"
      "s_nop 0\n\t"
      "s_or_b64 vcc, vcc, s[48:49]\n\t"
      "s_cbranch_scc1 GRL_%=\n\t"
      "GRX_%=:\n\t"
      "s_branch GRB_%=\n\t"
      "HEND_%=:\n\t"
      : "+{s[4:19]}"(s), "+{s[20:21]}"(tw), "+{v[192:199]}"(f), "+{v[200:207]}"(v)
      :
      : "memory", "vcc", "scc",
        "v0", "v1", "v2", "v3", "v4", "v5", "v6", "v7", "v8", "v9", "v10", "v11", "v12", "v13", "v14", "v15", "v16",
        "v17", "v18", "v19", "v20", "v21", "v22", "v23", "v24", "v25", "v26", "v27", "v28", "v29", "v30", "v31", "v32",
        "v33", "v34", "v35", "v36", "v37", "v38", "v39", "s40", "s41", "s42", "s43", "s44", "s45", "s46", "s47", "s48",
        "s49", "s50", "s51"
    );
    }
    PIN();
    LANE_BITS(__builtin_amdgcn_mbcnt_hi(~0u, __builtin_amdgcn_mbcnt_lo(~0u, 0u)));
    const bool has3 = idx < 14;
    const int ft = has3 ? (14 * xcd + idx) >> 2 : 0, blk0 = 7 * ((14 * xcd + idx) & 3);
    u32x8 v;
    u32x16 s;
    v[0] = lane * 16;
    const int rot = (2 * (14 * xcd + idx) + 2 * ft) & 7;
    u64 kr = (u64)(7 - rot) | ((u64)rot << 32);
    v[1] = has3 ? lr * 3584 + ((lc ^ swz_tr(lr)) << 4) : (lane >> 2) * 4096 + p4 * 128 + xcd * 512;
    v[2] = wave * G_WV + g * 1024 + q4 * 128 + (((2 * (wave & 3) + (p4 >> 1)) ^ swz_tr(8 * g + q4)) << 4) + (p4 & 1) * 8;
    v[3] = wave * 4096 + (wave & 3) * 1024 + ((lane * 16) ^ ((g & 1) << 7));
    const int gg = 2 * (lane >> 5) + (lane & 1);
    v[4] = (wave & 3) * 1024 + (((gg * 16 + ((lane >> 1) & 15)) * 16) ^ ((gg & 1) << 7));
    v[5] = (wave & 3) * 512 + lane * 8;
    v[6] = g * 8;
    v[7] = ST_H3 + (wave * 16 + c) * 128 + xcd * 16384;
    put(s, 0, has3 ? (u64)(w1 + ft * 128 + (wave * 256 + rot * 32) * 3584) : (u64)(w2 + (wave * 224 + (idx & 1) * 16) * 4096));
    put(s, 2, (u64)(ws + WS_HN + (blk0 * 64 + 8 * wave) * 1024));
    put(s, 4, (u64)(ws + WS_G + (blk0 * 56 + 2 * ft) * 1024));
    put(s, 6, (u64)(b1 + (64 * ft + 16 * (wave & 3)) * 2));
    put(s, 8, (u64)(ws + WS_ST));
    unsigned hint = ST_H4 + (blk0 * 4 + ft) * 128;
    s[12] = UNI((has3 ? 4 : 0) | (wave == ((xcd + XO) & 7) ? 16 : 0));
    s[13] = wave * G_WV;
    asm volatile(
      "s_waitcnt lgkmcnt(0)\n\ts_nop 4\n\t"
      "s_bitcmp0_b32 s16, 2\n\t"
      "s_cbranch_scc1 ASP_%=\n\t"
      "v_mov_b32_e32 v216, 163312\n\t"
      "ds_read_b128 v[216:219], v216\n\t"
      "v_xor_b32_e32 v248, 64, v241\n\t"
      "v_add_u32_e32 v249, 55296, v241\n\t"
      "v_add_u32_e32 v250, 82944, v248\n\t"
      "v_add_u32_e32 v248, 27648, v248\n\t"
      "v_xor_b32_e32 v251, 32, v242\n\t"
      "v_xor_b32_e32 v252, 64, v242\n\t"
      "v_xor_b32_e32 v253, 96, v242\n\t"
      "s_add_u32 m0, s17, 0\n\t"
      "s_nop 0\n\t"
      "global_load_lds_dwordx4 v241, s[4:5]\n\t"
      "global_load_lds_dwordx4 v248, s[4:5] offset:1024\n\t"
      "global_load_lds_dwordx4 v249, s[4:5] offset:2048\n\t"
      "global_load_lds_dwordx4 v250, s[4:5] offset:3072\n\t"
      "s_sub_u32 s20, s20, 1\n\t"
      "s_cselect_b32 s48, -7, 1\n\t"
      "s_cselect_b32 s49, -1, 0\n\t"
      "s_mul_i32 s48, s48, 114688\n\t"
      "s_add_u32 s4, s4, s48\n\t"
      "s_addc_u32 s5, s5, s49\n\t"
      "global_load_dwordx2 v[238:239], v246, s[10:11]\n\t"
      "s_waitcnt lgkmcnt(0)\n\t"
      "v_readfirstlane_b32 s14, v216\n\t"
      "v_readfirstlane_b32 s15, v217\n\t"
      "v_readfirstlane_b32 s46, v218\n\t"
      "v_readfirstlane_b32 s47, v219\n\t"
      "s_add_u32 s52, s6, 0\n\t"
      "s_addc_u32 s53, s7, 0\n\t"
      "s_add_u32 s54, s6, 65536\n\t"
      "s_addc_u32 s55, s7, 0\n\t"
      "s_add_u32 s56, s6, 131072\n\t"
      "s_addc_u32 s57, s7, 0\n\t"
      "s_add_u32 s58, s6, 196608\n\t"
      "s_addc_u32 s59, s7, 0\n\t"
      "s_add_u32 s60, s6, 262144\n\t"
      "s_addc_u32 s61, s7, 0\n\t"
      "s_add_u32 s62, s6, 327680\n\t"
      "s_addc_u32 s63, s7, 0\n\t"
      "s_add_u32 s64, s6, 393216\n\t"
      "s_addc_u32 s65, s7, 0\n\t"
      "s_add_u32 s48, s21, 0\n\t"
      "s_cmp_ge_u32 s48, 8\n\t"
      "s_cselect_b32 s49, 8, 0\n\t"
      "s_sub_u32 s48, s48, s49\n\t"
      "v_lshl_add_u32 v176, s48, 10, v240\n\t"
      "s_add_u32 s48, s21, 1\n\t"
      "s_cmp_ge_u32 s48, 8\n\t"
      "s_cselect_b32 s49, 8, 0\n\t"
      "s_sub_u32 s48, s48, s49\n\t"
      "v_lshl_add_u32 v177, s48, 10, v240\n\t"
      "s_add_u32 s48, s21, 2\n\t"
      "s_cmp_ge_u32 s48, 8\n\t"
      "s_cselect_b32 s49, 8, 0\n\t"
      "s_sub_u32 s48, s48, s49\n\t"
      "v_lshl_add_u32 v178, s48, 10, v240\n\t"
      "s_add_u32 s48, s21, 3\n\t"
      "s_cmp_ge_u32 s48, 8\n\t"
      "s_cselect_b32 s49, 8, 0\n\t"
      "s_sub_u32 s48, s48, s49\n\t"
      "v_lshl_add_u32 v179, s48, 10, v240\n\t"
      "s_add_u32 s48, s21, 4\n\t"
      "s_cmp_ge_u32 s48, 8\n\t"
      "s_cselect_b32 s49, 8, 0\n\t"
      "s_sub_u32 s48, s48, s49\n\t"
      "v_lshl_add_u32 v180, s48, 10, v240\n\t"
      "s_add_u32 s48, s21, 5\n\t"
      "s_cmp_ge_u32 s48, 8\n\t"
      "s_cselect_b32 s49, 8, 0\n\t"
      "s_sub_u32 s48, s48, s49\n\t"
      "v_lshl_add_u32 v181, s48, 10, v240\n\t"
      "s_add_u32 s48, s21, 6\n\t"
      "s_cmp_ge_u32 s48, 8\n\t"
      "s_cselect_b32 s49, 8, 0\n\t"
      "s_sub_u32 s48, s48, s49\n\t"
      "v_lshl_add_u32 v182, s48, 10, v240\n\t"
      "s_add_u32 s48, s21, 7\n\t"
      "s_cmp_ge_u32 s48, 8\n\t"
      "s_cselect_b32 s49, 8, 0\n\t"
      "s_sub_u32 s48, s48, s49\n\t"
      "v_lshl_add_u32 v183, s48, 10, v240\n\t"
      "s_waitcnt vmcnt(1)\n\t"
      "s_add_u32 m0, s17, 4096\n\t"
      "s_nop 0\n\t"
      "global_load_lds_dwordx4 v241, s[4:5]\n\t"
      "global_load_lds_dwordx4 v248, s[4:5] offset:1024\n\t"
      "global_load_lds_dwordx4 v249, s[4:5] offset:2048\n\t"
      "global_load_lds_dwordx4 v250, s[4:5] offset:3072\n\t"
      "s_sub_u32 s20, s20, 1\n\t"
      "s_cselect_b32 s48, -7, 1\n\t"
      "s_cselect_b32 s49, -1, 0\n\t"
      "s_mul_i32 s48, s48, 114688\n\t"
      "s_add_u32 s4, s4, s48\n\t"
      "s_addc_u32 s5, s5, s49\n\t"
      "ds_read_b64_tr_b16 v[0:1], v242\n\t"
      "ds_read_b64_tr_b16 v[2:3], v242 offset:512\n\t"
      "ds_read_b64_tr_b16 v[4:5], v251\n\t"
      "ds_read_b64_tr_b16 v[6:7], v251 offset:512\n\t"
      "ds_read_b64_tr_b16 v[8:9], v252\n\t"
      "ds_read_b64_tr_b16 v[10:11], v252 offset:512\n\t"
      "ds_read_b64_tr_b16 v[12:13], v253\n\t"
      "ds_read_b64_tr_b16 v[14:15], v253 offset:512\n\t"
      "s_waitcnt vmcnt(0)\n\t"
      "s_add_u32 m0, s17, 8192\n\t"
      "s_nop 0\n\t"
      "global_load_lds_dwordx4 v241, s[4:5]\n\t"
      "global_load_lds_dwordx4 v248, s[4:5] offset:1024\n\t"
      "global_load_lds_dwordx4 v249, s[4:5] offset:2048\n\t"
      "global_load_lds_dwordx4 v250, s[4:5] offset:3072\n\t"
      "s_sub_u32 s20, s20, 1\n\t"
      "s_cselect_b32 s48, -7, 1\n\t"
      "s_cselect_b32 s49, -1, 0\n\t"
      "s_mul_i32 s48, s48, 114688\n\t"
      "s_add_u32 s4, s4, s48\n\t"
      "s_addc_u32 s5, s5, s49\n\t"
      "ds_read_b64_tr_b16 v[16:17], v242 offset:4096\n\t"
      "ds_read_b64_tr_b16 v[18:19], v242 offset:4608\n\t"
      "ds_read_b64_tr_b16 v[20:21], v251 offset:4096\n\t"
      "ds_read_b64_tr_b16 v[22:23], v251 offset:4608\n\t"
      "ds_read_b64_tr_b16 v[24:25], v252 offset:4096\n\t"
      "ds_read_b64_tr_b16 v[26:27], v252 offset:4608\n\t"
      "ds_read_b64_tr_b16 v[28:29], v253 offset:4096\n\t"
      "ds_read_b64_tr_b16 v[30:31], v253 offset:4608\n\t"
      "v_xor_b32_e32 v164, 1024, v243\n\t"
      "v_xor_b32_e32 v165, 2048, v243\n\t"
      "v_xor_b32_e32 v166, 3072, v243\n\t"
      "v_lshlrev_b32_e32 v236, 16, v238\n\t"
      "v_and_b32_e32 v237, 0xffff0000, v238\n\t"
      "v_lshlrev_b32_e32 v238, 16, v239\n\t"
      "v_and_b32_e32 v239, 0xffff0000, v239\n\t"
      "v_mov_b32_e32 v235, 0xbdd2d3e8\n\t"
      "s_waitcnt vmcnt(0)\n\t"
      "s_add_u32 m0, s17, 12288\n\t"
      "s_nop 0\n\t"
      "global_load_lds_dwordx4 v241, s[4:5]\n\t"
      "global_load_lds_dwordx4 v248, s[4:5] offset:1024\n\t"
      "global_load_lds_dwordx4 v249, s[4:5] offset:2048\n\t"
      "global_load_lds_dwordx4 v250, s[4:5] offset:3072\n\t"
      "s_sub_u32 s20, s20, 1\n\t"
      "s_cselect_b32 s48, -7, 1\n\t"
      "s_cselect_b32 s49, -1, 0\n\t"
      "s_mul_i32 s48, s48, 114688\n\t"
      "s_add_u32 s4, s4, s48\n\t"
      "s_addc_u32 s5, s5, s49\n\t"
      "ds_read_b64_tr_b16 v[32:33], v242 offset:8192\n\t"
      "ds_read_b64_tr_b16 v[34:35], v242 offset:8704\n\t"
      "ds_read_b64_tr_b16 v[36:37], v251 offset:8192\n\t"
      "ds_read_b64_tr_b16 v[38:39], v251 offset:8704\n\t"
      "ds_read_b64_tr_b16 v[40:41], v252 offset:8192\n\t"
      "ds_read_b64_tr_b16 v[42:43], v252 offset:8704\n\t"
      "ds_read_b64_tr_b16 v[44:45], v253 offset:8192\n\t"
      "ds_read_b64_tr_b16 v[46:47], v253 offset:8704\n\t"
      "s_waitcnt vmcnt(0)\n\t"
      "s_waitcnt lgkmcnt(14)\n\t"
      "s_add_u32 m0, s17, 0\n\t"
      "s_nop 0\n\t"
      "global_load_lds_dwordx4 v241, s[4:5]\n\t"
      "global_load_lds_dwordx4 v248, s[4:5] offset:1024\n\t"
      "global_load_lds_dwordx4 v249, s[4:5] offset:2048\n\t"
      "global_load_lds_dwordx4 v250, s[4:5] offset:3072\n\t"
      "s_sub_u32 s20, s20, 1\n\t"
      "s_cselect_b32 s48, -7, 1\n\t"
      "s_cselect_b32 s49, -1, 0\n\t"
      "s_mul_i32 s48, s48, 114688\n\t"
      "s_add_u32 s4, s4, s48\n\t"
      "s_addc_u32 s5, s5, s49\n\t"
      "ds_read_b64_tr_b16 v[48:49], v242 offset:12288\n\t"
      "ds_read_b64_tr_b16 v[50:51], v242 offset:12800\n\t"
      "ds_read_b64_tr_b16 v[52:53], v251 offset:12288\n\t"
      "ds_read_b64_tr_b16 v[54:55], v251 offset:12800\n\t"
      "ds_read_b64_tr_b16 v[56:57], v252 offset:12288\n\t"
      "ds_read_b64_tr_b16 v[58:59], v252 offset:12800\n\t"
      "ds_read_b64_tr_b16 v[60:61], v253 offset:12288\n\t"
      "ds_read_b64_tr_b16 v[62:63], v253 offset:12800\n\t"
      "s_waitcnt vmcnt(0)\n\t"
      "s_waitcnt lgkmcnt(14)\n\t"
      "s_add_u32 m0, s17, 4096\n\t"
      "s_nop 0\n\t"
      "global_load_lds_dwordx4 v241, s[4:5]\n\t"
      "global_load_lds_dwordx4 v248, s[4:5] offset:1024\n\t"
      "global_load_lds_dwordx4 v249, s[4:5] offset:2048\n\t"
      "global_load_lds_dwordx4 v250, s[4:5] offset:3072\n\t"
      "s_sub_u32 s20, s20, 1\n\t"
      "s_cselect_b32 s48, -7, 1\n\t"
      "s_cselect_b32 s49, -1, 0\n\t"
      "s_mul_i32 s48, s48, 114688\n\t"
      "s_add_u32 s4, s4, s48\n\t"
      "s_addc_u32 s5, s5, s49\n\t"
      "ds_read_b64_tr_b16 v[64:65], v242\n\t"
      "ds_read_b64_tr_b16 v[66:67], v242 offset:512\n\t"
      "ds_read_b64_tr_b16 v[68:69], v251\n\t"
      "ds_read_b64_tr_b16 v[70:71], v251 offset:512\n\t"
      "ds_read_b64_tr_b16 v[72:73], v252\n\t"
      "ds_read_b64_tr_b16 v[74:75], v252 offset:512\n\t"
      "ds_read_b64_tr_b16 v[76:77], v253\n\t"
      "ds_read_b64_tr_b16 v[78:79], v253 offset:512\n\t"
      "s_waitcnt vmcnt(0)\n\t"
      "s_waitcnt lgkmcnt(14)\n\t"
      "s_add_u32 m0, s17, 8192\n\t"
      "s_nop 0\n\t"
      "global_load_lds_dwordx4 v241, s[4:5]\n\t"
      "global_load_lds_dwordx4 v248, s[4:5] offset:1024\n\t"
      "global_load_lds_dwordx4 v249, s[4:5] offset:2048\n\t"
      "global_load_lds_dwordx4 v250, s[4:5] offset:3072\n\t"
      "s_sub_u32 s20, s20, 1\n\t"
      "s_cselect_b32 s48, -7, 1\n\t"
      "s_cselect_b32 s49, -1, 0\n\t"
      "s_mul_i32 s48, s48, 114688\n\t"
      "s_add_u32 s4, s4, s48\n\t"
      "s_addc_u32 s5, s5, s49\n\t"
      "ds_read_b64_tr_b16 v[80:81], v242 offset:4096\n\t"
      "ds_read_b64_tr_b16 v[82:83], v242 offset:4608\n\t"
      "ds_read_b64_tr_b16 v[84:85], v251 offset:4096\n\t"
      "ds_read_b64_tr_b16 v[86:87], v251 offset:4608\n\t"
      "ds_read_b64_tr_b16 v[88:89], v252 offset:4096\n\t"
      "ds_read_b64_tr_b16 v[90:91], v252 offset:4608\n\t"
      "ds_read_b64_tr_b16 v[92:93], v253 offset:4096\n\t"
      "ds_read_b64_tr_b16 v[94:95], v253 offset:4608\n\t"
      "s_waitcnt vmcnt(0)\n\t"
      "s_waitcnt lgkmcnt(14)\n\t"
      "s_add_u32 m0, s17, 12288\n\t"
      "s_nop 0\n\t"
      "global_load_lds_dwordx4 v241, s[4:5]\n\t"
      "global_load_lds_dwordx4 v248, s[4:5] offset:1024\n\t"
      "global_load_lds_dwordx4 v249, s[4:5] offset:2048\n\t"
      "global_load_lds_dwordx4 v250, s[4:5] offset:3072\n\t"
      "ds_read_b64_tr_b16 v[96:97], v242 offset:8192\n\t"
      "ds_read_b64_tr_b16 v[98:99], v242 offset:8704\n\t"
      "ds_read_b64_tr_b16 v[100:101], v251 offset:8192\n\t"
      "ds_read_b64_tr_b16 v[102:103], v251 offset:8704\n\t"
      "ds_read_b64_tr_b16 v[104:105], v252 offset:8192\n\t"
      "ds_read_b64_tr_b16 v[106:107], v252 offset:8704\n\t"
      "ds_read_b64_tr_b16 v[108:109], v253 offset:8192\n\t"
      "ds_read_b64_tr_b16 v[110:111], v253 offset:8704\n\t"
      "s_waitcnt vmcnt(0)\n\t"
      "ds_read_b64_tr_b16 v[112:113], v242 offset:12288\n\t"
      "ds_read_b64_tr_b16 v[114:115], v242 offset:12800\n\t"
      "ds_read_b64_tr_b16 v[116:117], v251 offset:12288\n\t"
      "ds_read_b64_tr_b16 v[118:119], v251 offset:12800\n\t"
      "ds_read_b64_tr_b16 v[120:121], v252 offset:12288\n\t"
      "ds_read_b64_tr_b16 v[122:123], v252 offset:12800\n\t"
      "ds_read_b64_tr_b16 v[124:125], v253 offset:12288\n\t"
      "ds_read_b64_tr_b16 v[126:127], v253 offset:12800\n\t"
      "s_waitcnt lgkmcnt(0)\n\t"
      "s_barrier\n\t"
      "s_mov_b32 s51, 0\n\t"
      "s_branch PRE_%=\n\t"
      "PRL_%=:\n\t"
      "s_sleep 4\n\t"
      "s_add_u32 s51, s51, 1\n\t"
      "s_cmp_lt_u32 s51, 262144\n\t"
      "s_cbranch_scc0 PRX_%=\n\t"
      "PRE_%=:\n\t"
      "s_mov_b64 exec, 65535\n\t"
      "s_bitcmp1_b32 s16, 4\n\t"
      "s_cbranch_scc1 PM_%=\n\t"
      "global_load_dwordx2 v[232:233], v247, s[12:13] sc1\n\t"
      "s_waitcnt vmcnt(0)\n\t"
      "PM_%=:\n\t"
      "s_cbranch_scc0 PN_%=\n\t"
      "v_mov_b64_e32 v[232:233], 0\n\t"
      "global_atomic_or_x2 v[232:233], v247, v[232:233], s[12:13] sc0 sc1\n\t"
      "s_waitcnt vmcnt(0)\n\t"
      "PN_%=:\n\t"
      "v_subrev_u32_e32 v232, s14, v232\n\t"
      "v_cmp_ne_u32_e64 s[48:49], s15, v233\n\t"
      "v_subrev_u32_e32 v232, 0, v232\n\t"
      "v_cmp_lt_u32_e32 vcc, 7, v232\n\t"
      "s_mov_b64 exec, -1\n\t"
      "s_or_b64 vcc, vcc, s[48:49]\n\t"
      "s_cbranch_vccnz PRL_%=\n\t"
      "PRX_%=:\n\t"
      "PRY_%=:\n\t"
      "global_load_dwordx4 v[128:131], v176, s[52:53] sc1\n\t"
      "global_load_dwordx4 v[132:135], v177, s[52:53] sc1\n\t"
      "global_load_dwordx4 v[136:139], v178, s[52:53] sc1\n\t"
      "global_load_dwordx4 v[140:143], v179, s[52:53] sc1\n\t"
      "global_load_dwordx4 v[144:147], v180, s[52:53] sc1\n\t"
      "global_load_dwordx4 v[148:151], v181, s[52:53] sc1\n\t"
      "global_load_dwordx4 v[152:155], v182, s[52:53] sc1\n\t"
      "global_load_dwordx4 v[156:159], v183, s[52:53] sc1\n\t"
      "s_cmp_ge_u32 s17, 65536\n\t"
      "s_cbranch_scc1 GQ_%=\n\t"
      ".p2align 6\n\t"
      "s_setprio 1\n\t"
      "v_mov_b32_e32 v232, s14\n\t"
      "v_mov_b32_e32 v233, s15\n\t"
      "v_lshl_add_u32 v247, v240, 10, s22\n\t"
      "s_waitcnt vmcnt(7)\n\t"
      "v_max3_u32 v234, v128, v129, v130\n\t"
      "v_max_u32_e32 v234, v234, v131\n\t"
      "v_cmp_eq_u32_e32 vcc, -1, v234\n\t"
      "s_cbranch_vccnz FP0R_%=\n\t"
      "FP0B_%=:\n\t"
      "v_mfma_f32_16x16x32_bf16 v[184:187], v[0:3], v[128:131], 0\n\t"
      "v_mfma_f32_16x16x32_bf16 v[188:191], v[4:7], v[128:131], 0\n\t"
      "v_mfma_f32_16x16x32_bf16 v[192:195], v[8:11], v[128:131], 0\n\t"
      "v_mfma_f32_16x16x32_bf16 v[196:199], v[12:15], v[128:131], 0\n\t"
      "global_load_dwordx4 v[128:131], v176, s[54:55] sc1\n\t"
      "s_waitcnt vmcnt(7)\n\t"
      "v_max3_u32 v234, v132, v133, v134\n\t"
      "v_max_u32_e32 v234, v234, v135\n\t"
      "v_cmp_eq_u32_e32 vcc, -1, v234\n\t"
      "s_cbranch_vccnz FP1R_%=\n\t"
      "FP1B_%=:\n\t"
      "v_mfma_f32_16x16x32_bf16 v[184:187], v[16:19], v[132:135], v[184:187]\n\t"
      "v_mfma_f32_16x16x32_bf16 v[188:191], v[20:23], v[132:135], v[188:191]\n\t"
      "v_mfma_f32_16x16x32_bf16 v[192:195], v[24:27], v[132:135], v[192:195]\n\t"
      "v_mfma_f32_16x16x32_bf16 v[196:199], v[28:31], v[132:135], v[196:199]\n\t"
      "global_load_dwordx4 v[132:135], v177, s[54:55] sc1\n\t"
      "s_waitcnt vmcnt(7)\n\t"
      "v_max3_u32 v234, v136, v137, v138\n\t"
      "v_max_u32_e32 v234, v234, v139\n\t"
      "v_cmp_eq_u32_e32 vcc, -1, v234\n\t"
      "s_cbranch_vccnz FP2R_%=\n\t"
      "FP2B_%=:\n\t"
      "v_mfma_f32_16x16x32_bf16 v[184:187], v[32:35], v[136:139], v[184:187]\n\t"
      "v_mfma_f32_16x16x32_bf16 v[188:191], v[36:39], v[136:139], v[188:191]\n\t"
      "v_mfma_f32_16x16x32_bf16 v[192:195], v[40:43], v[136:139], v[192:195]\n\t"
      "v_mfma_f32_16x16x32_bf16 v[196:199], v[44:47], v[136:139], v[196:199]\n\t"
      "global_load_dwordx4 v[136:139], v178, s[54:55] sc1\n\t"
      "s_waitcnt vmcnt(7)\n\t"
      "v_max3_u32 v234, v140, v141, v142\n\t"
      "v_max_u32_e32 v234, v234, v143\n\t"
      "v_cmp_eq_u32_e32 vcc, -1, v234\n\t"
      "s_cbranch_vccnz FP3R_%=\n\t"
      "FP3B_%=:\n\t"
      "v_mfma_f32_16x16x32_bf16 v[184:187], v[48:51], v[140:143], v[184:187]\n\t"
      "v_mfma_f32_16x16x32_bf16 v[188:191], v[52:55], v[140:143], v[188:191]\n\t"
      "v_mfma_f32_16x16x32_bf16 v[192:195], v[56:59], v[140:143], v[192:195]\n\t"
      "v_mfma_f32_16x16x32_bf16 v[196:199], v[60:63], v[140:143], v[196:199]\n\t"
      "global_load_dwordx4 v[140:143], v179, s[54:55] sc1\n\t"
      "s_waitcnt vmcnt(7)\n\t"
      "v_max3_u32 v234, v144, v145, v146\n\t"
      "v_max_u32_e32 v234, v234, v147\n\t"
      "v_cmp_eq_u32_e32 vcc, -1, v234\n\t"
      "s_cbranch_vccnz FP4R_%=\n\t"
      "FP4B_%=:\n\t"
      "v_mfma_f32_16x16x32_bf16 v[184:187], v[64:67], v[144:147], v[184:187]\n\t"
      "v_mfma_f32_16x16x32_bf16 v[188:191], v[68:71], v[144:147], v[188:191]\n\t"
      "v_mfma_f32_16x16x32_bf16 v[192:195], v[72:75], v[144:147], v[192:195]\n\t"
      "v_mfma_f32_16x16x32_bf16 v[196:199], v[76:79], v[144:147], v[196:199]\n\t"
      "global_load_dwordx4 v[144:147], v180, s[54:55] sc1\n\t"
      "s_waitcnt vmcnt(7)\n\t"
      "v_max3_u32 v234, v148, v149, v150\n\t"
      "v_max_u32_e32 v234, v234, v151\n\t"
      "v_cmp_eq_u32_e32 vcc, -1, v234\n\t"
      "s_cbranch_vccnz FP5R_%=\n\t"
      "FP5B_%=:\n\t"
      "v_mfma_f32_16x16x32_bf16 v[184:187], v[80:83], v[148:151], v[184:187]\n\t"
      "v_mfma_f32_16x16x32_bf16 v[188:191], v[84:87], v[148:151], v[188:191]\n\t"
      "v_mfma_f32_16x16x32_bf16 v[192:195], v[88:91], v[148:151], v[192:195]\n\t"
      "v_mfma_f32_16x16x32_bf16 v[196:199], v[92:95], v[148:151], v[196:199]\n\t"
      "global_load_dwordx4 v[148:151], v181, s[54:55] sc1\n\t"
      "s_waitcnt vmcnt(7)\n\t"
      "v_max3_u32 v234, v152, v153, v154\n\t"
      "v_max_u32_e32 v234, v234, v155\n\t"
      "v_cmp_eq_u32_e32 vcc, -1, v234\n\t"
      "s_cbranch_vccnz FP6R_%=\n\t"
      "FP6B_%=:\n\t"
      "v_mfma_f32_16x16x32_bf16 v[184:187], v[96:99], v[152:155], v[184:187]\n\t"
      "v_mfma_f32_16x16x32_bf16 v[188:191], v[100:103], v[152:155], v[188:191]\n\t"
      "v_mfma_f32_16x16x32_bf16 v[192:195], v[104:107], v[152:155], v[192:195]\n\t"
      "v_mfma_f32_16x16x32_bf16 v[196:199], v[108:111], v[152:155], v[196:199]\n\t"
      "global_load_dwordx4 v[152:155], v182, s[54:55] sc1\n\t"
      "s_waitcnt vmcnt(7)\n\t"
      "v_max3_u32 v234, v156, v157, v158\n\t"
      "v_max_u32_e32 v234, v234, v159\n\t"
      "v_cmp_eq_u32_e32 vcc, -1, v234\n\t"
      "s_cbranch_vccnz FP7R_%=\n\t"
      "FP7B_%=:\n\t"
      "v_mfma_f32_16x16x32_bf16 v[184:187], v[112:115], v[156:159], v[184:187]\n\t"
      "v_mfma_f32_16x16x32_bf16 v[188:191], v[116:119], v[156:159], v[188:191]\n\t"
      "v_mfma_f32_16x16x32_bf16 v[192:195], v[120:123], v[156:159], v[192:195]\n\t"
      "v_mfma_f32_16x16x32_bf16 v[196:199], v[124:127], v[156:159], v[196:199]\n\t"
      "global_load_dwordx4 v[156:159], v183, s[54:55] sc1\n\t"
      "s_waitcnt vmcnt(7)\n\t"
      "v_mfma_f32_16x16x32_bf16 v[200:203], v[0:3], v[128:131], 0\n\t"
      "s_nop 1\n\t"
      "ds_write_b128 v243, v[184:187]\n\t"
      "v_mfma_f32_16x16x32_bf16 v[204:207], v[4:7], v[128:131], 0\n\t"
      "v_mfma_f32_16x16x32_bf16 v[208:211], v[8:11], v[128:131], 0\n\t"
      "v_mfma_f32_16x16x32_bf16 v[212:215], v[12:15], v[128:131], 0\n\t"
      "global_load_dwordx4 v[128:131], v176, s[56:57] sc1\n\t"
      "ds_write_b128 v164, v[188:191]\n\t"
      "s_waitcnt vmcnt(7)\n\t"
      "v_mfma_f32_16x16x32_bf16 v[200:203], v[16:19], v[132:135], v[200:203]\n\t"
      "v_mfma_f32_16x16x32_bf16 v[204:207], v[20:23], v[132:135], v[204:207]\n\t"
      "v_mfma_f32_16x16x32_bf16 v[208:211], v[24:27], v[132:135], v[208:211]\n\t"
      "ds_write_b128 v165, v[192:195]\n\t"
      "v_mfma_f32_16x16x32_bf16 v[212:215], v[28:31], v[132:135], v[212:215]\n\t"
      "global_load_dwordx4 v[132:135], v177, s[56:57] sc1\n\t"
      "s_waitcnt vmcnt(7)\n\t"
      "v_mfma_f32_16x16x32_bf16 v[200:203], v[32:35], v[136:139], v[200:203]\n\t"
      "v_mfma_f32_16x16x32_bf16 v[204:207], v[36:39], v[136:139], v[204:207]\n\t"
      "ds_write_b128 v166, v[196:199]\n\t"
      "v_mfma_f32_16x16x32_bf16 v[208:211], v[40:43], v[136:139], v[208:211]\n\t"
      "v_mfma_f32_16x16x32_bf16 v[212:215], v[44:47], v[136:139], v[212:215]\n\t"
      "global_load_dwordx4 v[136:139], v178, s[56:57] sc1\n\t"
      "s_waitcnt vmcnt(7)\n\t"
      "v_mfma_f32_16x16x32_bf16 v[200:203], v[48:51], v[140:143], v[200:203]\n\t"
      "s_waitcnt lgkmcnt(0)\n\t"
      "s_barrier\n\t"
      "ds_read_b128 v[216:219], v244\n\t"
      "v_mfma_f32_16x16x32_bf16 v[204:207], v[52:55], v[140:143], v[204:207]\n\t"
      "ds_read_b128 v[220:223], v244 offset:4096\n\t"
      "v_mfma_f32_16x16x32_bf16 v[208:211], v[56:59], v[140:143], v[208:211]\n\t"
      "ds_read_b128 v[224:227], v244 offset:8192\n\t"
      "v_mfma_f32_16x16x32_bf16 v[212:215], v[60:63], v[140:143], v[212:215]\n\t"
      "global_load_dwordx4 v[140:143], v179, s[56:57] sc1\n\t"
      "ds_read_b128 v[228:231], v244 offset:12288\n\t"
      "s_waitcnt vmcnt(7)\n\t"
      "v_mfma_f32_16x16x32_bf16 v[200:203], v[64:67], v[144:147], v[200:203]\n\t"
      "ds_read_b128 v[184:187], v244 offset:16384\n\t"
      "v_mfma_f32_16x16x32_bf16 v[204:207], v[68:71], v[144:147], v[204:207]\n\t"
      "ds_read_b128 v[188:191], v244 offset:20480\n\t"
      "v_mfma_f32_16x16x32_bf16 v[208:211], v[72:75], v[144:147], v[208:211]\n\t"
      "ds_read_b128 v[192:195], v244 offset:24576\n\t"
      "v_mfma_f32_16x16x32_bf16 v[212:215], v[76:79], v[144:147], v[212:215]\n\t"
      "global_load_dwordx4 v[144:147], v180, s[56:57] sc1\n\t"
      "ds_read_b128 v[196:199], v244 offset:28672\n\t"
      "s_waitcnt vmcnt(7)\n\t"
      "v_mfma_f32_16x16x32_bf16 v[200:203], v[80:83], v[148:151], v[200:203]\n\t"
      "v_mfma_f32_16x16x32_bf16 v[204:207], v[84:87], v[148:151], v[204:207]\n\t"
      "v_mfma_f32_16x16x32_bf16 v[208:211], v[88:91], v[148:151], v[208:211]\n\t"
      "s_waitcnt lgkmcnt(6)\n\t"
      "v_add_f32_e32 v216, v216, v220\n\t"
      "v_add_f32_e32 v217, v217, v221\n\t"
      "v_add_f32_e32 v218, v218, v222\n\t"
      "v_add_f32_e32 v219, v219, v223\n\t"
      "s_waitcnt lgkmcnt(4)\n\t"
      "v_add_f32_e32 v224, v224, v228\n\t"
      "v_add_f32_e32 v225, v225, v229\n\t"
      "v_add_f32_e32 v226, v226, v230\n\t"
      "v_add_f32_e32 v227, v227, v231\n\t"
      "s_waitcnt lgkmcnt(2)\n\t"
      "v_add_f32_e32 v184, v184, v188\n\t"
      "v_add_f32_e32 v185, v185, v189\n\t"
      "v_add_f32_e32 v186, v186, v190\n\t"
      "v_add_f32_e32 v187, v187, v191\n\t"
      "v_add_f32_e32 v216, v216, v224\n\t"
      "v_add_f32_e32 v217, v217, v225\n\t"
      "v_add_f32_e32 v218, v218, v226\n\t"
      "v_add_f32_e32 v219, v219, v227\n\t"
      "s_waitcnt lgkmcnt(0)\n\t"
      "v_add_f32_e32 v192, v192, v196\n\t"
      "v_add_f32_e32 v193, v193, v197\n\t"
      "v_add_f32_e32 v194, v194, v198\n\t"
      "v_add_f32_e32 v195, v195, v199\n\t"
      "v_add_f32_e32 v184, v184, v192\n\t"
      "v_add_f32_e32 v185, v185, v193\n\t"
      "v_add_f32_e32 v186, v186, v194\n\t"
      "v_add_f32_e32 v187, v187, v195\n\t"
      "v_add_f32_e32 v216, v216, v184\n\t"
      "v_add_f32_e32 v217, v217, v185\n\t"
      "v_add_f32_e32 v218, v218, v186\n\t"
      "v_add_f32_e32 v219, v219, v187\n\t"
      "v_mul_f32_e32 v220, v216, v216\n\t"
      "v_mul_f32_e32 v221, v217, v217\n\t"
      "v_mul_f32_e32 v222, v218, v218\n\t"
      "v_mul_f32_e32 v223, v219, v219\n\t"
      "v_fmaak_f32 v220, v235, v220, 0xc0135761\n\t"
      "v_fmaak_f32 v221, v235, v221, 0xc0135761\n\t"
      "v_fmaak_f32 v222, v235, v222, 0xc0135761\n\t"
      "v_fmaak_f32 v223, v235, v223, 0xc0135761\n\t"
      "v_mul_f32_e32 v220, v216, v220\n\t"
      "v_mul_f32_e32 v221, v217, v221\n\t"
      "v_mul_f32_e32 v222, v218, v222\n\t"
      "v_mul_f32_e32 v223, v219, v223\n\t"
      "v_exp_f32_e32 v220, v220\n\t"
      "v_exp_f32_e32 v221, v221\n\t"
      "v_exp_f32_e32 v222, v222\n\t"
      "v_exp_f32_e32 v223, v223\n\t"
      "v_add_f32_e32 v220, 1.0, v220\n\t"
      "v_add_f32_e32 v221, 1.0, v221\n\t"
      "v_add_f32_e32 v222, 1.0, v222\n\t"
      "v_add_f32_e32 v223, 1.0, v223\n\t"
      "v_rcp_f32_e32 v220, v220\n\t"
      "v_rcp_f32_e32 v221, v221\n\t"
      "v_rcp_f32_e32 v222, v222\n\t"
      "v_rcp_f32_e32 v223, v223\n\t"
      "v_mul_f32_e32 v216, v216, v220\n\t"
      "v_mul_f32_e32 v217, v217, v221\n\t"
      "v_mul_f32_e32 v218, v218, v222\n\t"
      "v_mul_f32_e32 v219, v219, v223\n\t"
      "v_cvt_pk_bf16_f32 v220, v216, v217\n\t"
      "v_cvt_pk_bf16_f32 v221, v218, v219\n\t"
      "v_min_u32_e32 v220, -2, v220\n\t"
      "v_min_u32_e32 v221, -2, v221\n\t"
      "s_cmp_eq_u32 s17, 0\n\t"
      "s_cbranch_scc0 HI0_%=\n\t"
      "s_mov_b64 exec, 0xff\n\t"
      "global_store_dwordx2 v247, v[232:233], s[12:13] sc1\n\t"
      "s_mov_b64 exec, -1\n\t"
      "s_nop 0\n\t"
      "v_add_u32_e32 v232, 6, v232\n\t"
      "HI0_%=:\n\t"
      "global_store_dwordx2 v245, v[220:221], s[8:9] sc1\n\t"
      "s_add_u32 s8, s8, 57344\n\t"
      "s_addc_u32 s9, s9, 0\n\t"
      "v_mfma_f32_16x16x32_bf16 v[212:215], v[92:95], v[148:151], v[212:215]\n\t"
      "global_load_dwordx4 v[148:151], v181, s[56:57] sc1\n\t"
      "s_waitcnt vmcnt(8)\n\t"
      "v_mfma_f32_16x16x32_bf16 v[200:203], v[96:99], v[152:155], v[200:203]\n\t"
      "v_mfma_f32_16x16x32_bf16 v[204:207], v[100:103], v[152:155], v[204:207]\n\t"
      "v_mfma_f32_16x16x32_bf16 v[208:211], v[104:107], v[152:155], v[208:211]\n\t"
      "v_mfma_f32_16x16x32_bf16 v[212:215], v[108:111], v[152:155], v[212:215]\n\t"
      "global_load_dwordx4 v[152:155], v182, s[56:57] sc1\n\t"
      "s_waitcnt vmcnt(8)\n\t"
      "v_mfma_f32_16x16x32_bf16 v[200:203], v[112:115], v[156:159], v[200:203]\n\t"
      "v_mfma_f32_16x16x32_bf16 v[204:207], v[116:119], v[156:159], v[204:207]\n\t"
      "v_mfma_f32_16x16x32_bf16 v[208:211], v[120:123], v[156:159], v[208:211]\n\t"
      "v_mfma_f32_16x16x32_bf16 v[212:215], v[124:127], v[156:159], v[212:215]\n\t"
      "global_load_dwordx4 v[156:159], v183, s[56:57] sc1\n\t"
      "s_nop 3\n\t"
      "v_cmp_u_f32_e32 vcc, v200, v200\n\t"
      "s_cbranch_vccnz NP1R_%=\n\t"
      "NP1B_%=:\n\t"
      "s_waitcnt vmcnt(8)\n\t"
      "v_mfma_f32_16x16x32_bf16 v[184:187], v[0:3], v[128:131], 0\n\t"
      "ds_write_b128 v243, v[200:203] offset:32768\n\t"
      "v_mfma_f32_16x16x32_bf16 v[188:191], v[4:7], v[128:131], 0\n\t"
      "v_mfma_f32_16x16x32_bf16 v[192:195], v[8:11], v[128:131], 0\n\t"
      "v_mfma_f32_16x16x32_bf16 v[196:199], v[12:15], v[128:131], 0\n\t"
      "global_load_dwordx4 v[128:131], v176, s[58:59] sc1\n\t"
      "ds_write_b128 v164, v[204:207] offset:32768\n\t"
      "s_waitcnt vmcnt(8)\n\t"
      "v_mfma_f32_16x16x32_bf16 v[184:187], v[16:19], v[132:135], v[184:187]\n\t"
      "v_mfma_f32_16x16x32_bf16 v[188:191], v[20:23], v[132:135], v[188:191]\n\t"
      "v_mfma_f32_16x16x32_bf16 v[192:195], v[24:27], v[132:135], v[192:195]\n\t"
      "ds_write_b128 v165, v[208:211] offset:32768\n\t"
      "v_mfma_f32_16x16x32_bf16 v[196:199], v[28:31], v[132:135], v[196:199]\n\t"
      "global_load_dwordx4 v[132:135], v177, s[58:59] sc1\n\t"
      "s_waitcnt vmcnt(8)\n\t"
      "v_mfma_f32_16x16x32_bf16 v[184:187], v[32:35], v[136:139], v[184:187]\n\t"
      "v_mfma_f32_16x16x32_bf16 v[188:191], v[36:39], v[136:139], v[188:191]\n\t"
      "ds_write_b128 v166, v[212:215] offset:32768\n\t"
      "v_mfma_f32_16x16x32_bf16 v[192:195], v[40:43], v[136:139], v[192:195]\n\t"
      "v_mfma_f32_16x16x32_bf16 v[196:199], v[44:47], v[136:139], v[196:199]\n\t"
      "global_load_dwordx4 v[136:139], v178, s[58:59] sc1\n\t"
      "s_waitcnt vmcnt(8)\n\t"
      "v_mfma_f32_16x16x32_bf16 v[184:187], v[48:51], v[140:143], v[184:187]\n\t"
      "s_waitcnt lgkmcnt(0)\n\t"
      "s_barrier\n\t"
      "ds_read_b128 v[216:219], v244 offset:32768\n\t"
      "v_mfma_f32_16x16x32_bf16 v[188:191], v[52:55], v[140:143], v[188:191]\n\t"
      "ds_read_b128 v[220:223], v244 offset:36864\n\t"
      "v_mfma_f32_16x16x32_bf16 v[192:195], v[56:59], v[140:143], v[192:195]\n\t"
      "ds_read_b128 v[224:227], v244 offset:40960\n\t"
      "v_mfma_f32_16x16x32_bf16 v[196:199], v[60:63], v[140:143], v[196:199]\n\t"
      "global_load_dwordx4 v[140:143], v179, s[58:59] sc1\n\t"
      "ds_read_b128 v[228:231], v244 offset:45056\n\t"
      "s_waitcnt vmcnt(8)\n\t"
      "v_mfma_f32_16x16x32_bf16 v[184:187], v[64:67], v[144:147], v[184:187]\n\t"
      "ds_read_b128 v[200:203], v244 offset:49152\n\t"
      "v_mfma_f32_16x16x32_bf16 v[188:191], v[68:71], v[144:147], v[188:191]\n\t"
      "ds_read_b128 v[204:207], v244 offset:53248\n\t"
      "v_mfma_f32_16x16x32_bf16 v[192:195], v[72:75], v[144:147], v[192:195]\n\t"
      "ds_read_b128 v[208:211], v244 offset:57344\n\t"
      "v_mfma_f32_16x16x32_bf16 v[196:199], v[76:79], v[144:147], v[196:199]\n\t"
      "global_load_dwordx4 v[144:147], v180, s[58:59] sc1\n\t"
      "ds_read_b128 v[212:215], v244 offset:61440\n\t"
      "s_waitcnt vmcnt(7)\n\t"
      "v_mfma_f32_16x16x32_bf16 v[184:187], v[80:83], v[148:151], v[184:187]\n\t"
      "v_mfma_f32_16x16x32_bf16 v[188:191], v[84:87], v[148:151], v[188:191]\n\t"
      "v_mfma_f32_16x16x32_bf16 v[192:195], v[88:91], v[148:151], v[192:195]\n\t"
      "s_waitcnt lgkmcnt(6)\n\t"
      "v_add_f32_e32 v216, v216, v220\n\t"
      "v_add_f32_e32 v217, v217, v221\n\t"
      "v_add_f32_e32 v218, v218, v222\n\t"
      "v_add_f32_e32 v219, v219, v223\n\t"
      "s_waitcnt lgkmcnt(4)\n\t"
      "v_add_f32_e32 v224, v224, v228\n\t"
      "v_add_f32_e32 v225, v225, v229\n\t"
      "v_add_f32_e32 v226, v226, v230\n\t"
      "v_add_f32_e32 v227, v227, v231\n\t"
      "s_waitcnt lgkmcnt(2)\n\t"
      "v_add_f32_e32 v200, v200, v204\n\t"
      "v_add_f32_e32 v201, v201, v205\n\t"
      "v_add_f32_e32 v202, v202, v206\n\t"
      "v_add_f32_e32 v203, v203, v207\n\t"
      "v_add_f32_e32 v216, v216, v224\n\t"
      "v_add_f32_e32 v217, v217, v225\n\t"
      "v_add_f32_e32 v218, v218, v226\n\t"
      "v_add_f32_e32 v219, v219, v227\n\t"
      "s_waitcnt lgkmcnt(0)\n\t"
      "v_add_f32_e32 v208, v208, v212\n\t"
      "v_add_f32_e32 v209, v209, v213\n\t"
      "v_add_f32_e32 v210, v210, v214\n\t"
      "v_add_f32_e32 v211, v211, v215\n\t"
      "v_add_f32_e32 v200, v200, v208\n\t"
      "v_add_f32_e32 v201, v201, v209\n\t"
      "v_add_f32_e32 v202, v202, v210\n\t"
      "v_add_f32_e32 v203, v203, v211\n\t"
      "v_add_f32_e32 v216, v216, v200\n\t"
      "v_add_f32_e32 v217, v217, v201\n\t"
      "v_add_f32_e32 v218, v218, v202\n\t"
      "v_add_f32_e32 v219, v219, v203\n\t"
      "v_mul_f32_e32 v220, v216, v216\n\t"
      "v_mul_f32_e32 v221, v217, v217\n\t"
      "v_mul_f32_e32 v222, v218, v218\n\t"
      "v_mul_f32_e32 v223, v219, v219\n\t"
      "v_fmaak_f32 v220, v235, v220, 0xc0135761\n\t"
      "v_fmaak_f32 v221, v235, v221, 0xc0135761\n\t"
      "v_fmaak_f32 v222, v235, v222, 0xc0135761\n\t"
      "v_fmaak_f32 v223, v235, v223, 0xc0135761\n\t"
      "v_mul_f32_e32 v220, v216, v220\n\t"
      "v_mul_f32_e32 v221, v217, v221\n\t"
      "v_mul_f32_e32 v222, v218, v222\n\t"
      "v_mul_f32_e32 v223, v219, v223\n\t"
      "v_exp_f32_e32 v220, v220\n\t"
      "v_exp_f32_e32 v221, v221\n\t"
      "v_exp_f32_e32 v222, v222\n\t"
      "v_exp_f32_e32 v223, v223\n\t"
      "v_add_f32_e32 v220, 1.0, v220\n\t"
      "v_add_f32_e32 v221, 1.0, v221\n\t"
      "v_add_f32_e32 v222, 1.0, v222\n\t"
      "v_add_f32_e32 v223, 1.0, v223\n\t"
      "v_rcp_f32_e32 v220, v220\n\t"
      "v_rcp_f32_e32 v221, v221\n\t"
      "v_rcp_f32_e32 v222, v222\n\t"
      "v_rcp_f32_e32 v223, v223\n\t"
      "v_mul_f32_e32 v216, v216, v220\n\t"
      "v_mul_f32_e32 v217, v217, v221\n\t"
      "v_mul_f32_e32 v218, v218, v222\n\t"
      "v_mul_f32_e32 v219, v219, v223\n\t"
      "v_cvt_pk_bf16_f32 v220, v216, v217\n\t"
      "v_cvt_pk_bf16_f32 v221, v218, v219\n\t"
      "v_min_u32_e32 v220, -2, v220\n\t"
      "v_min_u32_e32 v221, -2, v221\n\t"
      "global_store_dwordx2 v245, v[220:221], s[8:9] sc1\n\t"
      "s_add_u32 s8, s8, 57344\n\t"
      "s_addc_u32 s9, s9, 0\n\t"
      "v_mfma_f32_16x16x32_bf16 v[196:199], v[92:95], v[148:151], v[196:199]\n\t"
      "global_load_dwordx4 v[148:151], v181, s[58:59] sc1\n\t"
      "s_waitcnt vmcnt(8)\n\t"
      "v_mfma_f32_16x16x32_bf16 v[184:187], v[96:99], v[152:155], v[184:187]\n\t"
      "v_mfma_f32_16x16x32_bf16 v[188:191], v[100:103], v[152:155], v[188:191]\n\t"
      "v_mfma_f32_16x16x32_bf16 v[192:195], v[104:107], v[152:155], v[192:195]\n\t"
      "v_mfma_f32_16x16x32_bf16 v[196:199], v[108:111], v[152:155], v[196:199]\n\t"
      "global_load_dwordx4 v[152:155], v182, s[58:59] sc1\n\t"
      "s_waitcnt vmcnt(8)\n\t"
      "v_mfma_f32_16x16x32_bf16 v[184:187], v[112:115], v[156:159], v[184:187]\n\t"
      "v_mfma_f32_16x16x32_bf16 v[188:191], v[116:119], v[156:159], v[188:191]\n\t"
      "v_mfma_f32_16x16x32_bf16 v[192:195], v[120:123], v[156:159], v[192:195]\n\t"
      "v_mfma_f32_16x16x32_bf16 v[196:199], v[124:127], v[156:159], v[196:199]\n\t"
      "global_load_dwordx4 v[156:159], v183, s[58:59] sc1\n\t"
      "s_nop 3\n\t"
      "v_cmp_u_f32_e32 vcc, v184, v184\n\t"
      "s_cbranch_vccnz NP2R_%=\n\t"
      "NP2B_%=:\n\t"
      "s_waitcnt vmcnt(8)\n\t"
      "v_mfma_f32_16x16x32_bf16 v[200:203], v[0:3], v[128:131], 0\n\t"
      "ds_write_b128 v243, v[184:187]\n\t"
      "v_mfma_f32_16x16x32_bf16 v[204:207], v[4:7], v[128:131], 0\n\t"
      "v_mfma_f32_16x16x32_bf16 v[208:211], v[8:11], v[128:131], 0\n\t"
      "v_mfma_f32_16x16x32_bf16 v[212:215], v[12:15], v[128:131], 0\n\t"
      "global_load_dwordx4 v[128:131], v176, s[60:61] sc1\n\t"
      "ds_write_b128 v164, v[188:191]\n\t"
      "s_waitcnt vmcnt(8)\n\t"
      "v_mfma_f32_16x16x32_bf16 v[200:203], v[16:19], v[132:135], v[200:203]\n\t"
      "v_mfma_f32_16x16x32_bf16 v[204:207], v[20:23], v[132:135], v[204:207]\n\t"
      "v_mfma_f32_16x16x32_bf16 v[208:211], v[24:27], v[132:135], v[208:211]\n\t"
      "ds_write_b128 v165, v[192:195]\n\t"
      "v_mfma_f32_16x16x32_bf16 v[212:215], v[28:31], v[132:135], v[212:215]\n\t"
      "global_load_dwordx4 v[132:135], v177, s[60:61] sc1\n\t"
      "s_waitcnt vmcnt(8)\n\t"
      "v_mfma_f32_16x16x32_bf16 v[200:203], v[32:35], v[136:139], v[200:203]\n\t"
      "v_mfma_f32_16x16x32_bf16 v[204:207], v[36:39], v[136:139], v[204:207]\n\t"
      "ds_write_b128 v166, v[196:199]\n\t"
      "v_mfma_f32_16x16x32_bf16 v[208:211], v[40:43], v[136:139], v[208:211]\n\t"
      "v_mfma_f32_16x16x32_bf16 v[212:215], v[44:47], v[136:139], v[212:215]\n\t"
      "global_load_dwordx4 v[136:139], v178, s[60:61] sc1\n\t"
      "s_waitcnt vmcnt(8)\n\t"
      "v_mfma_f32_16x16x32_bf16 v[200:203], v[48:51], v[140:143], v[200:203]\n\t"
      "s_waitcnt lgkmcnt(0)\n\t"
      "s_barrier\n\t"
      "ds_read_b128 v[216:219], v244\n\t"
      "v_mfma_f32_16x16x32_bf16 v[204:207], v[52:55], v[140:143], v[204:207]\n\t"
      "ds_read_b128 v[220:223], v244 offset:4096\n\t"
      "v_mfma_f32_16x16x32_bf16 v[208:211], v[56:59], v[140:143], v[208:211]\n\t"
      "ds_read_b128 v[224:227], v244 offset:8192\n\t"
      "v_mfma_f32_16x16x32_bf16 v[212:215], v[60:63], v[140:143], v[212:215]\n\t"
      "global_load_dwordx4 v[140:143], v179, s[60:61] sc1\n\t"
      "ds_read_b128 v[228:231], v244 offset:12288\n\t"
      "s_waitcnt vmcnt(8)\n\t"
      "v_mfma_f32_16x16x32_bf16 v[200:203], v[64:67], v[144:147], v[200:203]\n\t"
      "ds_read_b128 v[184:187], v244 offset:16384\n\t"
      "v_mfma_f32_16x16x32_bf16 v[204:207], v[68:71], v[144:147], v[204:207]\n\t"
      "ds_read_b128 v[188:191], v244 offset:20480\n\t"
      "v_mfma_f32_16x16x32_bf16 v[208:211], v[72:75], v[144:147], v[208:211]\n\t"
      "ds_read_b128 v[192:195], v244 offset:24576\n\t"
      "v_mfma_f32_16x16x32_bf16 v[212:215], v[76:79], v[144:147], v[212:215]\n\t"
      "global_load_dwordx4 v[144:147], v180, s[60:61] sc1\n\t"
      "ds_read_b128 v[196:199], v244 offset:28672\n\t"
      "s_waitcnt vmcnt(7)\n\t"
      "v_mfma_f32_16x16x32_bf16 v[200:203], v[80:83], v[148:151], v[200:203]\n\t"
      "v_mfma_f32_16x16x32_bf16 v[204:207], v[84:87], v[148:151], v[204:207]\n\t"
      "v_mfma_f32_16x16x32_bf16 v[208:211], v[88:91], v[148:151], v[208:211]\n\t"
      "s_waitcnt lgkmcnt(6)\n\t"
      "v_add_f32_e32 v216, v216, v220\n\t"
      "v_add_f32_e32 v217, v217, v221\n\t"
      "v_add_f32_e32 v218, v218, v222\n\t"
      "v_add_f32_e32 v219, v219, v223\n\t"
      "s_waitcnt lgkmcnt(4)\n\t"
      "v_add_f32_e32 v224, v224, v228\n\t"
      "v_add_f32_e32 v225, v225, v229\n\t"
      "v_add_f32_e32 v226, v226, v230\n\t"
      "v_add_f32_e32 v227, v227, v231\n\t"
      "s_waitcnt lgkmcnt(2)\n\t"
      "v_add_f32_e32 v184, v184, v188\n\t"
      "v_add_f32_e32 v185, v185, v189\n\t"
      "v_add_f32_e32 v186, v186, v190\n\t"
      "v_add_f32_e32 v187, v187, v191\n\t"
      "v_add_f32_e32 v216, v216, v224\n\t"
      "v_add_f32_e32 v217, v217, v225\n\t"
      "v_add_f32_e32 v218, v218, v226\n\t"
      "v_add_f32_e32 v219, v219, v227\n\t"
      "s_waitcnt lgkmcnt(0)\n\t"
      "v_add_f32_e32 v192, v192, v196\n\t"
      "v_add_f32_e32 v193, v193, v197\n\t"
      "v_add_f32_e32 v194, v194, v198\n\t"
      "v_add_f32_e32 v195, v195, v199\n\t"
      "v_add_f32_e32 v184, v184, v192\n\t"
      "v_add_f32_e32 v185, v185, v193\n\t"
      "v_add_f32_e32 v186, v186, v194\n\t"
      "v_add_f32_e32 v187, v187, v195\n\t"
      "v_add_f32_e32 v216, v216, v184\n\t"
      "v_add_f32_e32 v217, v217, v185\n\t"
      "v_add_f32_e32 v218, v218, v186\n\t"
      "v_add_f32_e32 v219, v219, v187\n\t"
      "v_mul_f32_e32 v220, v216, v216\n\t"
      "v_mul_f32_e32 v221, v217, v217\n\t"
      "v_mul_f32_e32 v222, v218, v218\n\t"
      "v_mul_f32_e32 v223, v219, v219\n\t"
      "v_fmaak_f32 v220, v235, v220, 0xc0135761\n\t"
      "v_fmaak_f32 v221, v235, v221, 0xc0135761\n\t"
      "v_fmaak_f32 v222, v235, v222, 0xc0135761\n\t"
      "v_fmaak_f32 v223, v235, v223, 0xc0135761\n\t"
      "v_mul_f32_e32 v220, v216, v220\n\t"
      "v_mul_f32_e32 v221, v217, v221\n\t"
      "v_mul_f32_e32 v222, v218, v222\n\t"
      "v_mul_f32_e32 v223, v219, v223\n\t"
      "v_exp_f32_e32 v220, v220\n\t"
      "v_exp_f32_e32 v221, v221\n\t"
      "v_exp_f32_e32 v222, v222\n\t"
      "v_exp_f32_e32 v223, v223\n\t"
      "v_add_f32_e32 v220, 1.0, v220\n\t"
      "v_add_f32_e32 v221, 1.0, v221\n\t"
      "v_add_f32_e32 v222, 1.0, v222\n\t"
      "v_add_f32_e32 v223, 1.0, v223\n\t"
      "v_rcp_f32_e32 v220, v220\n\t"
      "v_rcp_f32_e32 v221, v221\n\t"
      "v_rcp_f32_e32 v222, v222\n\t"
      "v_rcp_f32_e32 v223, v223\n\t"
      "v_mul_f32_e32 v216, v216, v220\n\t"
      "v_mul_f32_e32 v217, v217, v221\n\t"
      "v_mul_f32_e32 v218, v218, v222\n\t"
      "v_mul_f32_e32 v219, v219, v223\n\t"
      "v_cvt_pk_bf16_f32 v220, v216, v217\n\t"
      "v_cvt_pk_bf16_f32 v221, v218, v219\n\t"
      "v_min_u32_e32 v220, -2, v220\n\t"
      "v_min_u32_e32 v221, -2, v221\n\t"
      "global_store_dwordx2 v245, v[220:221], s[8:9] sc1\n\t"
      "s_add_u32 s8, s8, 57344\n\t"
      "s_addc_u32 s9, s9, 0\n\t"
      "v_mfma_f32_16x16x32_bf16 v[212:215], v[92:95], v[148:151], v[212:215]\n\t"
      "global_load_dwordx4 v[148:151], v181, s[60:61] sc1\n\t"
      "s_waitcnt vmcnt(8)\n\t"
      "v_mfma_f32_16x16x32_bf16 v[200:203], v[96:99], v[152:155], v[200:203]\n\t"
      "v_mfma_f32_16x16x32_bf16 v[204:207], v[100:103], v[152:155], v[204:207]\n\t"
      "v_mfma_f32_16x16x32_bf16 v[208:211], v[104:107], v[152:155], v[208:211]\n\t"
      "v_mfma_f32_16x16x32_bf16 v[212:215], v[108:111], v[152:155], v[212:215]\n\t"
      "global_load_dwordx4 v[152:155], v182, s[60:61] sc1\n\t"
      "s_waitcnt vmcnt(8)\n\t"
      "v_mfma_f32_16x16x32_bf16 v[200:203], v[112:115], v[156:159], v[200:203]\n\t"
      "v_mfma_f32_16x16x32_bf16 v[204:207], v[116:119], v[156:159], v[204:207]\n\t"
      "v_mfma_f32_16x16x32_bf16 v[208:211], v[120:123], v[156:159], v[208:211]\n\t"
      "v_mfma_f32_16x16x32_bf16 v[212:215], v[124:127], v[156:159], v[212:215]\n\t"
      "global_load_dwordx4 v[156:159], v183, s[60:61] sc1\n\t"
      "s_nop 3\n\t"
      "v_cmp_u_f32_e32 vcc, v200, v200\n\t"
      "s_cbranch_vccnz NP3R_%=\n\t"
      "NP3B_%=:\n\t"
      "s_waitcnt vmcnt(8)\n\t"
      "v_mfma_f32_16x16x32_bf16 v[184:187], v[0:3], v[128:131], 0\n\t"
      "ds_write_b128 v243, v[200:203] offset:32768\n\t"
      "v_mfma_f32_16x16x32_bf16 v[188:191], v[4:7], v[128:131], 0\n\t"
      "v_mfma_f32_16x16x32_bf16 v[192:195], v[8:11], v[128:131], 0\n\t"
      "v_mfma_f32_16x16x32_bf16 v[196:199], v[12:15], v[128:131], 0\n\t"
      "global_load_dwordx4 v[128:131], v176, s[62:63] sc1\n\t"
      "ds_write_b128 v164, v[204:207] offset:32768\n\t"
      "s_waitcnt vmcnt(8)\n\t"
      "v_mfma_f32_16x16x32_bf16 v[184:187], v[16:19], v[132:135], v[184:187]\n\t"
      "v_mfma_f32_16x16x32_bf16 v[188:191], v[20:23], v[132:135], v[188:191]\n\t"
      "v_mfma_f32_16x16x32_bf16 v[192:195], v[24:27], v[132:135], v[192:195]\n\t"
      "ds_write_b128 v165, v[208:211] offset:32768\n\t"
      "v_mfma_f32_16x16x32_bf16 v[196:199], v[28:31], v[132:135], v[196:199]\n\t"
      "global_load_dwordx4 v[132:135], v177, s[62:63] sc1\n\t"
      "s_waitcnt vmcnt(8)\n\t"
      "v_mfma_f32_16x16x32_bf16 v[184:187], v[32:35], v[136:139], v[184:187]\n\t"
      "v_mfma_f32_16x16x32_bf16 v[188:191], v[36:39], v[136:139], v[188:191]\n\t"
      "ds_write_b128 v166, v[212:215] offset:32768\n\t"
      "v_mfma_f32_16x16x32_bf16 v[192:195], v[40:43], v[136:139], v[192:195]\n\t"
      "v_mfma_f32_16x16x32_bf16 v[196:199], v[44:47], v[136:139], v[196:199]\n\t"
      "global_load_dwordx4 v[136:139], v178, s[62:63] sc1\n\t"
      "s_waitcnt vmcnt(8)\n\t"
      "v_mfma_f32_16x16x32_bf16 v[184:187], v[48:51], v[140:143], v[184:187]\n\t"
      "s_waitcnt lgkmcnt(0)\n\t"
      "s_barrier\n\t"
      "ds_read_b128 v[216:219], v244 offset:32768\n\t"
      "v_mfma_f32_16x16x32_bf16 v[188:191], v[52:55], v[140:143], v[188:191]\n\t"
      "ds_read_b128 v[220:223], v244 offset:36864\n\t"
      "v_mfma_f32_16x16x32_bf16 v[192:195], v[56:59], v[140:143], v[192:195]\n\t"
      "ds_read_b128 v[224:227], v244 offset:40960\n\t"
      "v_mfma_f32_16x16x32_bf16 v[196:199], v[60:63], v[140:143], v[196:199]\n\t"
      "global_load_dwordx4 v[140:143], v179, s[62:63] sc1\n\t"
      "ds_read_b128 v[228:231], v244 offset:45056\n\t"
      "s_waitcnt vmcnt(8)\n\t"
      "v_mfma_f32_16x16x32_bf16 v[184:187], v[64:67], v[144:147], v[184:187]\n\t"
      "ds_read_b128 v[200:203], v244 offset:49152\n\t"
      "v_mfma_f32_16x16x32_bf16 v[188:191], v[68:71], v[144:147], v[188:191]\n\t"
      "ds_read_b128 v[204:207], v244 offset:53248\n\t"
      "v_mfma_f32_16x16x32_bf16 v[192:195], v[72:75], v[144:147], v[192:195]\n\t"
      "ds_read_b128 v[208:211], v244 offset:57344\n\t"
      "v_mfma_f32_16x16x32_bf16 v[196:199], v[76:79], v[144:147], v[196:199]\n\t"
      "global_load_dwordx4 v[144:147], v180, s[62:63] sc1\n\t"
      "ds_read_b128 v[212:215], v244 offset:61440\n\t"
      "s_waitcnt vmcnt(7)\n\t"
      "v_mfma_f32_16x16x32_bf16 v[184:187], v[80:83], v[148:151], v[184:187]\n\t"
      "v_mfma_f32_16x16x32_bf16 v[188:191], v[84:87], v[148:151], v[188:191]\n\t"
      "v_mfma_f32_16x16x32_bf16 v[192:195], v[88:91], v[148:151], v[192:195]\n\t"
      "s_waitcnt lgkmcnt(6)\n\t"
      "v_add_f32_e32 v216, v216, v220\n\t"
      "v_add_f32_e32 v217, v217, v221\n\t"
      "v_add_f32_e32 v218, v218, v222\n\t"
      "v_add_f32_e32 v219, v219, v223\n\t"
      "s_waitcnt lgkmcnt(4)\n\t"
      "v_add_f32_e32 v224, v224, v228\n\t"
      "v_add_f32_e32 v225, v225, v229\n\t"
      "v_add_f32_e32 v226, v226, v230\n\t"
      "v_add_f32_e32 v227, v227, v231\n\t"
      "s_waitcnt lgkmcnt(2)\n\t"
      "v_add_f32_e32 v200, v200, v204\n\t"
      "v_add_f32_e32 v201, v201, v205\n\t"
      "v_add_f32_e32 v202, v202, v206\n\t"
      "v_add_f32_e32 v203, v203, v207\n\t"
      "v_add_f32_e32 v216, v216, v224\n\t"
      "v_add_f32_e32 v217, v217, v225\n\t"
      "v_add_f32_e32 v218, v218, v226\n\t"
      "v_add_f32_e32 v219, v219, v227\n\t"
      "s_waitcnt lgkmcnt(0)\n\t"
      "v_add_f32_e32 v208, v208, v212\n\t"
      "v_add_f32_e32 v209, v209, v213\n\t"
      "v_add_f32_e32 v210, v210, v214\n\t"
      "v_add_f32_e32 v211, v211, v215\n\t"
      "v_add_f32_e32 v200, v200, v208\n\t"
      "v_add_f32_e32 v201, v201, v209\n\t"
      "v_add_f32_e32 v202, v202, v210\n\t"
      "v_add_f32_e32 v203, v203, v211\n\t"
      "v_add_f32_e32 v216, v216, v200\n\t"
      "v_add_f32_e32 v217, v217, v201\n\t"
      "v_add_f32_e32 v218, v218, v202\n\t"
      "v_add_f32_e32 v219, v219, v203\n\t"
      "v_mul_f32_e32 v220, v216, v216\n\t"
      "v_mul_f32_e32 v221, v217, v217\n\t"
      "v_mul_f32_e32 v222, v218, v218\n\t"
      "v_mul_f32_e32 v223, v219, v219\n\t"
      "v_fmaak_f32 v220, v235, v220, 0xc0135761\n\t"
      "v_fmaak_f32 v221, v235, v221, 0xc0135761\n\t"
      "v_fmaak_f32 v222, v235, v222, 0xc0135761\n\t"
      "v_fmaak_f32 v223, v235, v223, 0xc0135761\n\t"
      "v_mul_f32_e32 v220, v216, v220\n\t"
      "v_mul_f32_e32 v221, v217, v221\n\t"
      "v_mul_f32_e32 v222, v218, v222\n\t"
      "v_mul_f32_e32 v223, v219, v223\n\t"
      "v_exp_f32_e32 v220, v220\n\t"
      "v_exp_f32_e32 v221, v221\n\t"
      "v_exp_f32_e32 v222, v222\n\t"
      "v_exp_f32_e32 v223, v223\n\t"
      "v_add_f32_e32 v220, 1.0, v220\n\t"
      "v_add_f32_e32 v221, 1.0, v221\n\t"
      "v_add_f32_e32 v222, 1.0, v222\n\t"
      "v_add_f32_e32 v223, 1.0, v223\n\t"
      "v_rcp_f32_e32 v220, v220\n\t"
      "v_rcp_f32_e32 v221, v221\n\t"
      "v_rcp_f32_e32 v222, v222\n\t"
      "v_rcp_f32_e32 v223, v223\n\t"
      "v_mul_f32_e32 v216, v216, v220\n\t"
      "v_mul_f32_e32 v217, v217, v221\n\t"
      "v_mul_f32_e32 v218, v218, v222\n\t"
      "v_mul_f32_e32 v219, v219, v223\n\t"
      "v_cvt_pk_bf16_f32 v220, v216, v217\n\t"
      "v_cvt_pk_bf16_f32 v221, v218, v219\n\t"
      "v_min_u32_e32 v220, -2, v220\n\t"
      "v_min_u32_e32 v221, -2, v221\n\t"
      "global_store_dwordx2 v245, v[220:221], s[8:9] sc1\n\t"
      "s_add_u32 s8, s8, 57344\n\t"
      "s_addc_u32 s9, s9, 0\n\t"
      "v_mfma_f32_16x16x32_bf16 v[196:199], v[92:95], v[148:151], v[196:199]\n\t"
      "global_load_dwordx4 v[148:151], v181, s[62:63] sc1\n\t"
      "s_waitcnt vmcnt(8)\n\t"
      "v_mfma_f32_16x16x32_bf16 v[184:187], v[96:99], v[152:155], v[184:187]\n\t"
      "v_mfma_f32_16x16x32_bf16 v[188:191], v[100:103], v[152:155], v[188:191]\n\t"
      "v_mfma_f32_16x16x32_bf16 v[192:195], v[104:107], v[152:155], v[192:195]\n\t"
      "v_mfma_f32_16x16x32_bf16 v[196:199], v[108:111], v[152:155], v[196:199]\n\t"
      "global_load_dwordx4 v[152:155], v182, s[62:63] sc1\n\t"
      "s_waitcnt vmcnt(8)\n\t"
      "v_mfma_f32_16x16x32_bf16 v[184:187], v[112:115], v[156:159], v[184:187]\n\t"
      "v_mfma_f32_16x16x32_bf16 v[188:191], v[116:119], v[156:159], v[188:191]\n\t"
      "v_mfma_f32_16x16x32_bf16 v[192:195], v[120:123], v[156:159], v[192:195]\n\t"
      "v_mfma_f32_16x16x32_bf16 v[196:199], v[124:127], v[156:159], v[196:199]\n\t"
      "global_load_dwordx4 v[156:159], v183, s[62:63] sc1\n\t"
      "s_nop 3\n\t"
      "v_cmp_u_f32_e32 vcc, v184, v184\n\t"
      "s_cbranch_vccnz NP4R_%=\n\t"
      "NP4B_%=:\n\t"
      "s_waitcnt vmcnt(8)\n\t"
      "v_mfma_f32_16x16x32_bf16 v[200:203], v[0:3], v[128:131], 0\n\t"
      "ds_write_b128 v243, v[184:187]\n\t"
      "v_mfma_f32_16x16x32_bf16 v[204:207], v[4:7], v[128:131], 0\n\t"
      "v_mfma_f32_16x16x32_bf16 v[208:211], v[8:11], v[128:131], 0\n\t"
      "v_mfma_f32_16x16x32_bf16 v[212:215], v[12:15], v[128:131], 0\n\t"
      "global_load_dwordx4 v[128:131], v176, s[64:65] sc1\n\t"
      "ds_write_b128 v164, v[188:191]\n\t"
      "s_waitcnt vmcnt(8)\n\t"
      "v_mfma_f32_16x16x32_bf16 v[200:203], v[16:19], v[132:135], v[200:203]\n\t"
      "v_mfma_f32_16x16x32_bf16 v[204:207], v[20:23], v[132:135], v[204:207]\n\t"
      "v_mfma_f32_16x16x32_bf16 v[208:211], v[24:27], v[132:135], v[208:211]\n\t"
      "ds_write_b128 v165, v[192:195]\n\t"
      "v_mfma_f32_16x16x32_bf16 v[212:215], v[28:31], v[132:135], v[212:215]\n\t"
      "global_load_dwordx4 v[132:135], v177, s[64:65] sc1\n\t"
      "s_waitcnt vmcnt(8)\n\t"
      "v_mfma_f32_16x16x32_bf16 v[200:203], v[32:35], v[136:139], v[200:203]\n\t"
      "v_mfma_f32_16x16x32_bf16 v[204:207], v[36:39], v[136:139], v[204:207]\n\t"
      "ds_write_b128 v166, v[196:199]\n\t"
      "v_mfma_f32_16x16x32_bf16 v[208:211], v[40:43], v[136:139], v[208:211]\n\t"
      "v_mfma_f32_16x16x32_bf16 v[212:215], v[44:47], v[136:139], v[212:215]\n\t"
      "global_load_dwordx4 v[136:139], v178, s[64:65] sc1\n\t"
      "s_waitcnt vmcnt(8)\n\t"
      "v_mfma_f32_16x16x32_bf16 v[200:203], v[48:51], v[140:143], v[200:203]\n\t"
      "s_waitcnt lgkmcnt(0)\n\t"
      "s_barrier\n\t"
      "ds_read_b128 v[216:219], v244\n\t"
      "v_mfma_f32_16x16x32_bf16 v[204:207], v[52:55], v[140:143], v[204:207]\n\t"
      "ds_read_b128 v[220:223], v244 offset:4096\n\t"
      "v_mfma_f32_16x16x32_bf16 v[208:211], v[56:59], v[140:143], v[208:211]\n\t"
      "ds_read_b128 v[224:227], v244 offset:8192\n\t"
      "v_mfma_f32_16x16x32_bf16 v[212:215], v[60:63], v[140:143], v[212:215]\n\t"
      "global_load_dwordx4 v[140:143], v179, s[64:65] sc1\n\t"
      "ds_read_b128 v[228:231], v244 offset:12288\n\t"
      "s_waitcnt vmcnt(8)\n\t"
      "v_mfma_f32_16x16x32_bf16 v[200:203], v[64:67], v[144:147], v[200:203]\n\t"
      "ds_read_b128 v[184:187], v244 offset:16384\n\t"
      "v_mfma_f32_16x16x32_bf16 v[204:207], v[68:71], v[144:147], v[204:207]\n\t"
      "ds_read_b128 v[188:191], v244 offset:20480\n\t"
      "v_mfma_f32_16x16x32_bf16 v[208:211], v[72:75], v[144:147], v[208:211]\n\t"
      "ds_read_b128 v[192:195], v244 offset:24576\n\t"
      "v_mfma_f32_16x16x32_bf16 v[212:215], v[76:79], v[144:147], v[212:215]\n\t"
      "global_load_dwordx4 v[144:147], v180, s[64:65] sc1\n\t"
      "ds_read_b128 v[196:199], v244 offset:28672\n\t"
      "s_waitcnt vmcnt(7)\n\t"
      "v_mfma_f32_16x16x32_bf16 v[200:203], v[80:83], v[148:151], v[200:203]\n\t"
      "v_mfma_f32_16x16x32_bf16 v[204:207], v[84:87], v[148:151], v[204:207]\n\t"
      "v_mfma_f32_16x16x32_bf16 v[208:211], v[88:91], v[148:151], v[208:211]\n\t"
      "s_waitcnt lgkmcnt(6)\n\t"
      "v_add_f32_e32 v216, v216, v220\n\t"
      "v_add_f32_e32 v217, v217, v221\n\t"
      "v_add_f32_e32 v218, v218, v222\n\t"
      "v_add_f32_e32 v219, v219, v223\n\t"
      "s_waitcnt lgkmcnt(4)\n\t"
      "v_add_f32_e32 v224, v224, v228\n\t"
      "v_add_f32_e32 v225, v225, v229\n\t"
      "v_add_f32_e32 v226, v226, v230\n\t"
      "v_add_f32_e32 v227, v227, v231\n\t"
      "s_waitcnt lgkmcnt(2)\n\t"
      "v_add_f32_e32 v184, v184, v188\n\t"
      "v_add_f32_e32 v185, v185, v189\n\t"
      "v_add_f32_e32 v186, v186, v190\n\t"
      "v_add_f32_e32 v187, v187, v191\n\t"
      "v_add_f32_e32 v216, v216, v224\n\t"
      "v_add_f32_e32 v217, v217, v225\n\t"
      "v_add_f32_e32 v218, v218, v226\n\t"
      "v_add_f32_e32 v219, v219, v227\n\t"
      "s_waitcnt lgkmcnt(0)\n\t"
      "v_add_f32_e32 v192, v192, v196\n\t"
      "v_add_f32_e32 v193, v193, v197\n\t"
      "v_add_f32_e32 v194, v194, v198\n\t"
      "v_add_f32_e32 v195, v195, v199\n\t"
      "v_add_f32_e32 v184, v184, v192\n\t"
      "v_add_f32_e32 v185, v185, v193\n\t"
      "v_add_f32_e32 v186, v186, v194\n\t"
      "v_add_f32_e32 v187, v187, v195\n\t"
      "v_add_f32_e32 v216, v216, v184\n\t"
      "v_add_f32_e32 v217, v217, v185\n\t"
      "v_add_f32_e32 v218, v218, v186\n\t"
      "v_add_f32_e32 v219, v219, v187\n\t"
      "v_mul_f32_e32 v220, v216, v216\n\t"
      "v_mul_f32_e32 v221, v217, v217\n\t"
      "v_mul_f32_e32 v222, v218, v218\n\t"
      "v_mul_f32_e32 v223, v219, v219\n\t"
      "v_fmaak_f32 v220, v235, v220, 0xc0135761\n\t"
      "v_fmaak_f32 v221, v235, v221, 0xc0135761\n\t"
      "v_fmaak_f32 v222, v235, v222, 0xc0135761\n\t"
      "v_fmaak_f32 v223, v235, v223, 0xc0135761\n\t"
      "v_mul_f32_e32 v220, v216, v220\n\t"
      "v_mul_f32_e32 v221, v217, v221\n\t"
      "v_mul_f32_e32 v222, v218, v222\n\t"
      "v_mul_f32_e32 v223, v219, v223\n\t"
      "v_exp_f32_e32 v220, v220\n\t"
      "v_exp_f32_e32 v221, v221\n\t"
      "v_exp_f32_e32 v222, v222\n\t"
      "v_exp_f32_e32 v223, v223\n\t"
      "v_add_f32_e32 v220, 1.0, v220\n\t"
      "v_add_f32_e32 v221, 1.0, v221\n\t"
      "v_add_f32_e32 v222, 1.0, v222\n\t"
      "v_add_f32_e32 v223, 1.0, v223\n\t"
      "v_rcp_f32_e32 v220, v220\n\t"
      "v_rcp_f32_e32 v221, v221\n\t"
      "v_rcp_f32_e32 v222, v222\n\t"
      "v_rcp_f32_e32 v223, v223\n\t"
      "v_mul_f32_e32 v216, v216, v220\n\t"
      "v_mul_f32_e32 v217, v217, v221\n\t"
      "v_mul_f32_e32 v218, v218, v222\n\t"
      "v_mul_f32_e32 v219, v219, v223\n\t"
      "v_cvt_pk_bf16_f32 v220, v216, v217\n\t"
      "v_cvt_pk_bf16_f32 v221, v218, v219\n\t"
      "v_min_u32_e32 v220, -2, v220\n\t"
      "v_min_u32_e32 v221, -2, v221\n\t"
      "global_store_dwordx2 v245, v[220:221], s[8:9] sc1\n\t"
      "s_add_u32 s8, s8, 57344\n\t"
      "s_addc_u32 s9, s9, 0\n\t"
      "v_mfma_f32_16x16x32_bf16 v[212:215], v[92:95], v[148:151], v[212:215]\n\t"
      "global_load_dwordx4 v[148:151], v181, s[64:65] sc1\n\t"
      "s_waitcnt vmcnt(8)\n\t"
      "v_mfma_f32_16x16x32_bf16 v[200:203], v[96:99], v[152:155], v[200:203]\n\t"
      "v_mfma_f32_16x16x32_bf16 v[204:207], v[100:103], v[152:155], v[204:207]\n\t"
      "v_mfma_f32_16x16x32_bf16 v[208:211], v[104:107], v[152:155], v[208:211]\n\t"
      "v_mfma_f32_16x16x32_bf16 v[212:215], v[108:111], v[152:155], v[212:215]\n\t"
      "global_load_dwordx4 v[152:155], v182, s[64:65] sc1\n\t"
      "s_waitcnt vmcnt(8)\n\t"
      "v_mfma_f32_16x16x32_bf16 v[200:203], v[112:115], v[156:159], v[200:203]\n\t"
      "v_mfma_f32_16x16x32_bf16 v[204:207], v[116:119], v[156:159], v[204:207]\n\t"
      "v_mfma_f32_16x16x32_bf16 v[208:211], v[120:123], v[156:159], v[208:211]\n\t"
      "v_mfma_f32_16x16x32_bf16 v[212:215], v[124:127], v[156:159], v[212:215]\n\t"
      "global_load_dwordx4 v[156:159], v183, s[64:65] sc1\n\t"
      "s_nop 3\n\t"
      "v_cmp_u_f32_e32 vcc, v200, v200\n\t"
      "s_cbranch_vccnz NP5R_%=\n\t"
      "NP5B_%=:\n\t"
      "s_waitcnt vmcnt(8)\n\t"
      "v_mfma_f32_16x16x32_bf16 v[184:187], v[0:3], v[128:131], 0\n\t"
      "ds_write_b128 v243, v[200:203] offset:32768\n\t"
      "v_mfma_f32_16x16x32_bf16 v[188:191], v[4:7], v[128:131], 0\n\t"
      "v_mfma_f32_16x16x32_bf16 v[192:195], v[8:11], v[128:131], 0\n\t"
      "v_mfma_f32_16x16x32_bf16 v[196:199], v[12:15], v[128:131], 0\n\t"
      "ds_write_b128 v164, v[204:207] offset:32768\n\t"
      "s_waitcnt vmcnt(7)\n\t"
      "v_mfma_f32_16x16x32_bf16 v[184:187], v[16:19], v[132:135], v[184:187]\n\t"
      "v_mfma_f32_16x16x32_bf16 v[188:191], v[20:23], v[132:135], v[188:191]\n\t"
      "v_mfma_f32_16x16x32_bf16 v[192:195], v[24:27], v[132:135], v[192:195]\n\t"
      "ds_write_b128 v165, v[208:211] offset:32768\n\t"
      "v_mfma_f32_16x16x32_bf16 v[196:199], v[28:31], v[132:135], v[196:199]\n\t"
      "s_waitcnt vmcnt(6)\n\t"
      "v_mfma_f32_16x16x32_bf16 v[184:187], v[32:35], v[136:139], v[184:187]\n\t"
      "v_mfma_f32_16x16x32_bf16 v[188:191], v[36:39], v[136:139], v[188:191]\n\t"
      "ds_write_b128 v166, v[212:215] offset:32768\n\t"
      "v_mfma_f32_16x16x32_bf16 v[192:195], v[40:43], v[136:139], v[192:195]\n\t"
      "v_mfma_f32_16x16x32_bf16 v[196:199], v[44:47], v[136:139], v[196:199]\n\t"
      "s_waitcnt vmcnt(5)\n\t"
      "v_mfma_f32_16x16x32_bf16 v[184:187], v[48:51], v[140:143], v[184:187]\n\t"
      "s_waitcnt lgkmcnt(0)\n\t"
      "s_barrier\n\t"
      "ds_read_b128 v[216:219], v244 offset:32768\n\t"
      "v_mfma_f32_16x16x32_bf16 v[188:191], v[52:55], v[140:143], v[188:191]\n\t"
      "ds_read_b128 v[220:223], v244 offset:36864\n\t"
      "v_mfma_f32_16x16x32_bf16 v[192:195], v[56:59], v[140:143], v[192:195]\n\t"
      "ds_read_b128 v[224:227], v244 offset:40960\n\t"
      "v_mfma_f32_16x16x32_bf16 v[196:199], v[60:63], v[140:143], v[196:199]\n\t"
      "ds_read_b128 v[228:231], v244 offset:45056\n\t"
      "s_waitcnt vmcnt(4)\n\t"
      "v_mfma_f32_16x16x32_bf16 v[184:187], v[64:67], v[144:147], v[184:187]\n\t"
      "ds_read_b128 v[200:203], v244 offset:49152\n\t"
      "v_mfma_f32_16x16x32_bf16 v[188:191], v[68:71], v[144:147], v[188:191]\n\t"
      "ds_read_b128 v[204:207], v244 offset:53248\n\t"
      "v_mfma_f32_16x16x32_bf16 v[192:195], v[72:75], v[144:147], v[192:195]\n\t"
      "ds_read_b128 v[208:211], v244 offset:57344\n\t"
      "v_mfma_f32_16x16x32_bf16 v[196:199], v[76:79], v[144:147], v[196:199]\n\t"
      "ds_read_b128 v[212:215], v244 offset:61440\n\t"
      "s_waitcnt vmcnt(2)\n\t"
      "v_mfma_f32_16x16x32_bf16 v[184:187], v[80:83], v[148:151], v[184:187]\n\t"
      "v_mfma_f32_16x16x32_bf16 v[188:191], v[84:87], v[148:151], v[188:191]\n\t"
      "v_mfma_f32_16x16x32_bf16 v[192:195], v[88:91], v[148:151], v[192:195]\n\t"
      "s_waitcnt lgkmcnt(6)\n\t"
      "v_add_f32_e32 v216, v216, v220\n\t"
      "v_add_f32_e32 v217, v217, v221\n\t"
      "v_add_f32_e32 v218, v218, v222\n\t"
      "v_add_f32_e32 v219, v219, v223\n\t"
      "s_waitcnt lgkmcnt(4)\n\t"
      "v_add_f32_e32 v224, v224, v228\n\t"
      "v_add_f32_e32 v225, v225, v229\n\t"
      "v_add_f32_e32 v226, v226, v230\n\t"
      "v_add_f32_e32 v227, v227, v231\n\t"
      "s_waitcnt lgkmcnt(2)\n\t"
      "v_add_f32_e32 v200, v200, v204\n\t"
      "v_add_f32_e32 v201, v201, v205\n\t"
      "v_add_f32_e32 v202, v202, v206\n\t"
      "v_add_f32_e32 v203, v203, v207\n\t"
      "v_add_f32_e32 v216, v216, v224\n\t"
      "v_add_f32_e32 v217, v217, v225\n\t"
      "v_add_f32_e32 v218, v218, v226\n\t"
      "v_add_f32_e32 v219, v219, v227\n\t"
      "s_waitcnt lgkmcnt(0)\n\t"
      "v_add_f32_e32 v208, v208, v212\n\t"
      "v_add_f32_e32 v209, v209, v213\n\t"
      "v_add_f32_e32 v210, v210, v214\n\t"
      "v_add_f32_e32 v211, v211, v215\n\t"
      "v_add_f32_e32 v200, v200, v208\n\t"
      "v_add_f32_e32 v201, v201, v209\n\t"
      "v_add_f32_e32 v202, v202, v210\n\t"
      "v_add_f32_e32 v203, v203, v211\n\t"
      "v_add_f32_e32 v216, v216, v200\n\t"
      "v_add_f32_e32 v217, v217, v201\n\t"
      "v_add_f32_e32 v218, v218, v202\n\t"
      "v_add_f32_e32 v219, v219, v203\n\t"
      "v_mul_f32_e32 v220, v216, v216\n\t"
      "v_mul_f32_e32 v221, v217, v217\n\t"
      "v_mul_f32_e32 v222, v218, v218\n\t"
      "v_mul_f32_e32 v223, v219, v219\n\t"
      "v_fmaak_f32 v220, v235, v220, 0xc0135761\n\t"
      "v_fmaak_f32 v221, v235, v221, 0xc0135761\n\t"
      "v_fmaak_f32 v222, v235, v222, 0xc0135761\n\t"
      "v_fmaak_f32 v223, v235, v223, 0xc0135761\n\t"
      "v_mul_f32_e32 v220, v216, v220\n\t"
      "v_mul_f32_e32 v221, v217, v221\n\t"
      "v_mul_f32_e32 v222, v218, v222\n\t"
      "v_mul_f32_e32 v223, v219, v223\n\t"
      "v_exp_f32_e32 v220, v220\n\t"
      "v_exp_f32_e32 v221, v221\n\t"
      "v_exp_f32_e32 v222, v222\n\t"
      "v_exp_f32_e32 v223, v223\n\t"
      "v_add_f32_e32 v220, 1.0, v220\n\t"
      "v_add_f32_e32 v221, 1.0, v221\n\t"
      "v_add_f32_e32 v222, 1.0, v222\n\t"
      "v_add_f32_e32 v223, 1.0, v223\n\t"
      "v_rcp_f32_e32 v220, v220\n\t"
      "v_rcp_f32_e32 v221, v221\n\t"
      "v_rcp_f32_e32 v222, v222\n\t"
      "v_rcp_f32_e32 v223, v223\n\t"
      "v_mul_f32_e32 v216, v216, v220\n\t"
      "v_mul_f32_e32 v217, v217, v221\n\t"
      "v_mul_f32_e32 v218, v218, v222\n\t"
      "v_mul_f32_e32 v219, v219, v223\n\t"
      "v_cvt_pk_bf16_f32 v220, v216, v217\n\t"
      "v_cvt_pk_bf16_f32 v221, v218, v219\n\t"
      "v_min_u32_e32 v220, -2, v220\n\t"
      "v_min_u32_e32 v221, -2, v221\n\t"
      "global_store_dwordx2 v245, v[220:221], s[8:9] sc1\n\t"
      "s_add_u32 s8, s8, 57344\n\t"
      "s_addc_u32 s9, s9, 0\n\t"
      "v_mfma_f32_16x16x32_bf16 v[196:199], v[92:95], v[148:151], v[196:199]\n\t"
      "s_waitcnt vmcnt(2)\n\t"
      "v_mfma_f32_16x16x32_bf16 v[184:187], v[96:99], v[152:155], v[184:187]\n\t"
      "v_mfma_f32_16x16x32_bf16 v[188:191], v[100:103], v[152:155], v[188:191]\n\t"
      "v_mfma_f32_16x16x32_bf16 v[192:195], v[104:107], v[152:155], v[192:195]\n\t"
      "v_mfma_f32_16x16x32_bf16 v[196:199], v[108:111], v[152:155], v[196:199]\n\t"
      "s_waitcnt vmcnt(1)\n\t"
      "v_mfma_f32_16x16x32_bf16 v[184:187], v[112:115], v[156:159], v[184:187]\n\t"
      "v_mfma_f32_16x16x32_bf16 v[188:191], v[116:119], v[156:159], v[188:191]\n\t"
      "v_mfma_f32_16x16x32_bf16 v[192:195], v[120:123], v[156:159], v[192:195]\n\t"
      "v_mfma_f32_16x16x32_bf16 v[196:199], v[124:127], v[156:159], v[196:199]\n\t"
      "s_nop 4\n\t"
      "v_cmp_u_f32_e32 vcc, v184, v184\n\t"
      "s_cbranch_vccnz NP6R_%=\n\t"
      "NP6B_%=:\n\t"
      "ds_write_b128 v243, v[184:187]\n\t"
      "ds_write_b128 v164, v[188:191]\n\t"
      "ds_write_b128 v165, v[192:195]\n\t"
      "ds_write_b128 v166, v[196:199]\n\t"
      "s_waitcnt lgkmcnt(0)\n\t"
      "s_barrier\n\t"
      "ds_read_b128 v[216:219], v244\n\t"
      "ds_read_b128 v[220:223], v244 offset:4096\n\t"
      "ds_read_b128 v[224:227], v244 offset:8192\n\t"
      "ds_read_b128 v[228:231], v244 offset:12288\n\t"
      "ds_read_b128 v[184:187], v244 offset:16384\n\t"
      "ds_read_b128 v[188:191], v244 offset:20480\n\t"
      "ds_read_b128 v[192:195], v244 offset:24576\n\t"
      "ds_read_b128 v[196:199], v244 offset:28672\n\t"
      "s_waitcnt lgkmcnt(6)\n\t"
      "v_add_f32_e32 v216, v216, v220\n\t"
      "v_add_f32_e32 v217, v217, v221\n\t"
      "v_add_f32_e32 v218, v218, v222\n\t"
      "v_add_f32_e32 v219, v219, v223\n\t"
      "s_waitcnt lgkmcnt(4)\n\t"
      "v_add_f32_e32 v224, v224, v228\n\t"
      "v_add_f32_e32 v225, v225, v229\n\t"
      "v_add_f32_e32 v226, v226, v230\n\t"
      "v_add_f32_e32 v227, v227, v231\n\t"
      "s_waitcnt lgkmcnt(2)\n\t"
      "v_add_f32_e32 v184, v184, v188\n\t"
      "v_add_f32_e32 v185, v185, v189\n\t"
      "v_add_f32_e32 v186, v186, v190\n\t"
      "v_add_f32_e32 v187, v187, v191\n\t"
      "v_add_f32_e32 v216, v216, v224\n\t"
      "v_add_f32_e32 v217, v217, v225\n\t"
      "v_add_f32_e32 v218, v218, v226\n\t"
      "v_add_f32_e32 v219, v219, v227\n\t"
      "s_waitcnt lgkmcnt(0)\n\t"
      "v_add_f32_e32 v192, v192, v196\n\t"
      "v_add_f32_e32 v193, v193, v197\n\t"
      "v_add_f32_e32 v194, v194, v198\n\t"
      "v_add_f32_e32 v195, v195, v199\n\t"
      "v_add_f32_e32 v184, v184, v192\n\t"
      "v_add_f32_e32 v185, v185, v193\n\t"
      "v_add_f32_e32 v186, v186, v194\n\t"
      "v_add_f32_e32 v187, v187, v195\n\t"
      "v_add_f32_e32 v216, v216, v184\n\t"
      "v_add_f32_e32 v217, v217, v185\n\t"
      "v_add_f32_e32 v218, v218, v186\n\t"
      "v_add_f32_e32 v219, v219, v187\n\t"
      "v_mul_f32_e32 v220, v216, v216\n\t"
      "v_mul_f32_e32 v221, v217, v217\n\t"
      "v_mul_f32_e32 v222, v218, v218\n\t"
      "v_mul_f32_e32 v223, v219, v219\n\t"
      "v_fmaak_f32 v220, v235, v220, 0xc0135761\n\t"
      "v_fmaak_f32 v221, v235, v221, 0xc0135761\n\t"
      "v_fmaak_f32 v222, v235, v222, 0xc0135761\n\t"
      "v_fmaak_f32 v223, v235, v223, 0xc0135761\n\t"
      "v_mul_f32_e32 v220, v216, v220\n\t"
      "v_mul_f32_e32 v221, v217, v221\n\t"
      "v_mul_f32_e32 v222, v218, v222\n\t"
      "v_mul_f32_e32 v223, v219, v223\n\t"
      "v_exp_f32_e32 v220, v220\n\t"
      "v_exp_f32_e32 v221, v221\n\t"
      "v_exp_f32_e32 v222, v222\n\t"
      "v_exp_f32_e32 v223, v223\n\t"
      "v_add_f32_e32 v220, 1.0, v220\n\t"
      "v_add_f32_e32 v221, 1.0, v221\n\t"
      "v_add_f32_e32 v222, 1.0, v222\n\t"
      "v_add_f32_e32 v223, 1.0, v223\n\t"
      "v_rcp_f32_e32 v220, v220\n\t"
      "v_rcp_f32_e32 v221, v221\n\t"
      "v_rcp_f32_e32 v222, v222\n\t"
      "v_rcp_f32_e32 v223, v223\n\t"
      "v_mul_f32_e32 v216, v216, v220\n\t"
      "v_mul_f32_e32 v217, v217, v221\n\t"
      "v_mul_f32_e32 v218, v218, v222\n\t"
      "v_mul_f32_e32 v219, v219, v223\n\t"
      "v_cvt_pk_bf16_f32 v220, v216, v217\n\t"
      "v_cvt_pk_bf16_f32 v221, v218, v219\n\t"
      "v_min_u32_e32 v220, -2, v220\n\t"
      "v_min_u32_e32 v221, -2, v221\n\t"
      "s_cmp_eq_u32 s17, 0\n\t"
      "s_cbranch_scc0 HI6_%=\n\t"
      "s_mov_b64 exec, 0xff\n\t"
      "global_store_dwordx2 v247, v[232:233], s[12:13] sc1\n\t"
      "s_mov_b64 exec, -1\n\t"
      "HI6_%=:\n\t"
      "global_store_dwordx2 v245, v[220:221], s[8:9] sc1\n\t"
      "GX_%=:\n\t"
      "AEND_%=:\n\t"
      "s_endpgm\n\t"
      ".p2align 6\n\t"
      "GQ_%=:\n\t"
      "s_waitcnt vmcnt(7)\n\t"
      "v_max3_u32 v234, v128, v129, v130\n\t"
      "v_max_u32_e32 v234, v234, v131\n\t"
      "v_cmp_eq_u32_e32 vcc, -1, v234\n\t"
      "s_cbranch_vccnz FQ0R_%=\n\t"
      "FQ0B_%=:\n\t"
      "v_mfma_f32_16x16x32_bf16 v[184:187], v[0:3], v[128:131], v[236:239]\n\t"
      "v_mfma_f32_16x16x32_bf16 v[188:191], v[4:7], v[128:131], 0\n\t"
      "v_mfma_f32_16x16x32_bf16 v[192:195], v[8:11], v[128:131], 0\n\t"
      "v_mfma_f32_16x16x32_bf16 v[196:199], v[12:15], v[128:131], 0\n\t"
      "global_load_dwordx4 v[128:131], v176, s[54:55] sc1\n\t"
      "s_waitcnt vmcnt(7)\n\t"
      "v_max3_u32 v234, v132, v133, v134\n\t"
      "v_max_u32_e32 v234, v234, v135\n\t"
      "v_cmp_eq_u32_e32 vcc, -1, v234\n\t"
      "s_cbranch_vccnz FQ1R_%=\n\t"
      "FQ1B_%=:\n\t"
      "v_mfma_f32_16x16x32_bf16 v[184:187], v[16:19], v[132:135], v[184:187]\n\t"
      "v_mfma_f32_16x16x32_bf16 v[188:191], v[20:23], v[132:135], v[188:191]\n\t"
      "v_mfma_f32_16x16x32_bf16 v[192:195], v[24:27], v[132:135], v[192:195]\n\t"
      "v_mfma_f32_16x16x32_bf16 v[196:199], v[28:31], v[132:135], v[196:199]\n\t"
      "global_load_dwordx4 v[132:135], v177, s[54:55] sc1\n\t"
      "s_waitcnt vmcnt(7)\n\t"
      "v_max3_u32 v234, v136, v137, v138\n\t"
      "v_max_u32_e32 v234, v234, v139\n\t"
      "v_cmp_eq_u32_e32 vcc, -1, v234\n\t"
      "s_cbranch_vccnz FQ2R_%=\n\t"
      "FQ2B_%=:\n\t"
      "v_mfma_f32_16x16x32_bf16 v[184:187], v[32:35], v[136:139], v[184:187]\n\t"
      "v_mfma_f32_16x16x32_bf16 v[188:191], v[36:39], v[136:139], v[188:191]\n\t"
      "v_mfma_f32_16x16x32_bf16 v[192:195], v[40:43], v[136:139], v[192:195]\n\t"
      "v_mfma_f32_16x16x32_bf16 v[196:199], v[44:47], v[136:139], v[196:199]\n\t"
      "global_load_dwordx4 v[136:139], v178, s[54:55] sc1\n\t"
      "s_waitcnt vmcnt(7)\n\t"
      "v_max3_u32 v234, v140, v141, v142\n\t"
      "v_max_u32_e32 v234, v234, v143\n\t"
      "v_cmp_eq_u32_e32 vcc, -1, v234\n\t"
      "s_cbranch_vccnz FQ3R_%=\n\t"
      "FQ3B_%=:\n\t"
      "v_mfma_f32_16x16x32_bf16 v[184:187], v[48:51], v[140:143], v[184:187]\n\t"
      "v_mfma_f32_16x16x32_bf16 v[188:191], v[52:55], v[140:143], v[188:191]\n\t"
      "v_mfma_f32_16x16x32_bf16 v[192:195], v[56:59], v[140:143], v[192:195]\n\t"
      "v_mfma_f32_16x16x32_bf16 v[196:199], v[60:63], v[140:143], v[196:199]\n\t"
      "global_load_dwordx4 v[140:143], v179, s[54:55] sc1\n\t"
      "s_waitcnt vmcnt(7)\n\t"
      "v_max3_u32 v234, v144, v145, v146\n\t"
      "v_max_u32_e32 v234, v234, v147\n\t"
      "v_cmp_eq_u32_e32 vcc, -1, v234\n\t"
      "s_cbranch_vccnz FQ4R_%=\n\t"
      "FQ4B_%=:\n\t"
      "v_mfma_f32_16x16x32_bf16 v[184:187], v[64:67], v[144:147], v[184:187]\n\t"
      "v_mfma_f32_16x16x32_bf16 v[188:191], v[68:71], v[144:147], v[188:191]\n\t"
      "v_mfma_f32_16x16x32_bf16 v[192:195], v[72:75], v[144:147], v[192:195]\n\t"
      "v_mfma_f32_16x16x32_bf16 v[196:199], v[76:79], v[144:147], v[196:199]\n\t"
      "global_load_dwordx4 v[144:147], v180, s[54:55] sc1\n\t"
      "s_waitcnt vmcnt(7)\n\t"
      "v_max3_u32 v234, v148, v149, v150\n\t"
      "v_max_u32_e32 v234, v234, v151\n\t"
      "v_cmp_eq_u32_e32 vcc, -1, v234\n\t"
      "s_cbranch_vccnz FQ5R_%=\n\t"
      "FQ5B_%=:\n\t"
      "v_mfma_f32_16x16x32_bf16 v[184:187], v[80:83], v[148:151], v[184:187]\n\t"
      "v_mfma_f32_16x16x32_bf16 v[188:191], v[84:87], v[148:151], v[188:191]\n\t"
      "v_mfma_f32_16x16x32_bf16 v[192:195], v[88:91], v[148:151], v[192:195]\n\t"
      "v_mfma_f32_16x16x32_bf16 v[196:199], v[92:95], v[148:151], v[196:199]\n\t"
      "global_load_dwordx4 v[148:151], v181, s[54:55] sc1\n\t"
      "s_waitcnt vmcnt(7)\n\t"
      "v_max3_u32 v234, v152, v153, v154\n\t"
      "v_max_u32_e32 v234, v234, v155\n\t"
      "v_cmp_eq_u32_e32 vcc, -1, v234\n\t"
      "s_cbranch_vccnz FQ6R_%=\n\t"
      "FQ6B_%=:\n\t"
      "v_mfma_f32_16x16x32_bf16 v[184:187], v[96:99], v[152:155], v[184:187]\n\t"
      "v_mfma_f32_16x16x32_bf16 v[188:191], v[100:103], v[152:155], v[188:191]\n\t"
      "v_mfma_f32_16x16x32_bf16 v[192:195], v[104:107], v[152:155], v[192:195]\n\t"
      "v_mfma_f32_16x16x32_bf16 v[196:199], v[108:111], v[152:155], v[196:199]\n\t"
      "global_load_dwordx4 v[152:155], v182, s[54:55] sc1\n\t"
      "s_waitcnt vmcnt(7)\n\t"
      "v_max3_u32 v234, v156, v157, v158\n\t"
      "v_max_u32_e32 v234, v234, v159\n\t"
      "v_cmp_eq_u32_e32 vcc, -1, v234\n\t"
      "s_cbranch_vccnz FQ7R_%=\n\t"
      "FQ7B_%=:\n\t"
      "v_mfma_f32_16x16x32_bf16 v[184:187], v[112:115], v[156:159], v[184:187]\n\t"
      "v_mfma_f32_16x16x32_bf16 v[188:191], v[116:119], v[156:159], v[188:191]\n\t"
      "v_mfma_f32_16x16x32_bf16 v[192:195], v[120:123], v[156:159], v[192:195]\n\t"
      "v_mfma_f32_16x16x32_bf16 v[196:199], v[124:127], v[156:159], v[196:199]\n\t"
      "global_load_dwordx4 v[156:159], v183, s[54:55] sc1\n\t"
      "s_waitcnt vmcnt(7)\n\t"
      "v_mfma_f32_16x16x32_bf16 v[200:203], v[0:3], v[128:131], v[236:239]\n\t"
      "v_mfma_f32_16x16x32_bf16 v[204:207], v[4:7], v[128:131], 0\n\t"
      "v_mfma_f32_16x16x32_bf16 v[208:211], v[8:11], v[128:131], 0\n\t"
      "v_mfma_f32_16x16x32_bf16 v[212:215], v[12:15], v[128:131], 0\n\t"
      "global_load_dwordx4 v[128:131], v176, s[56:57] sc1\n\t"
      "s_waitcnt vmcnt(7)\n\t"
      "v_mfma_f32_16x16x32_bf16 v[200:203], v[16:19], v[132:135], v[200:203]\n\t"
      "ds_write_b128 v243, v[184:187]\n\t"
      "v_mfma_f32_16x16x32_bf16 v[204:207], v[20:23], v[132:135], v[204:207]\n\t"
      "v_mfma_f32_16x16x32_bf16 v[208:211], v[24:27], v[132:135], v[208:211]\n\t"
      "v_mfma_f32_16x16x32_bf16 v[212:215], v[28:31], v[132:135], v[212:215]\n\t"
      "global_load_dwordx4 v[132:135], v177, s[56:57] sc1\n\t"
      "ds_write_b128 v164, v[188:191]\n\t"
      "s_waitcnt vmcnt(7)\n\t"
      "v_mfma_f32_16x16x32_bf16 v[200:203], v[32:35], v[136:139], v[200:203]\n\t"
      "v_mfma_f32_16x16x32_bf16 v[204:207], v[36:39], v[136:139], v[204:207]\n\t"
      "v_mfma_f32_16x16x32_bf16 v[208:211], v[40:43], v[136:139], v[208:211]\n\t"
      "ds_write_b128 v165, v[192:195]\n\t"
      "v_mfma_f32_16x16x32_bf16 v[212:215], v[44:47], v[136:139], v[212:215]\n\t"
      "global_load_dwordx4 v[136:139], v178, s[56:57] sc1\n\t"
      "s_waitcnt vmcnt(7)\n\t"
      "v_mfma_f32_16x16x32_bf16 v[200:203], v[48:51], v[140:143], v[200:203]\n\t"
      "v_mfma_f32_16x16x32_bf16 v[204:207], v[52:55], v[140:143], v[204:207]\n\t"
      "ds_write_b128 v166, v[196:199]\n\t"
      "v_mfma_f32_16x16x32_bf16 v[208:211], v[56:59], v[140:143], v[208:211]\n\t"
      "v_mfma_f32_16x16x32_bf16 v[212:215], v[60:63], v[140:143], v[212:215]\n\t"
      "global_load_dwordx4 v[140:143], v179, s[56:57] sc1\n\t"
      "s_waitcnt vmcnt(7)\n\t"
      "v_mfma_f32_16x16x32_bf16 v[200:203], v[64:67], v[144:147], v[200:203]\n\t"
      "s_waitcnt lgkmcnt(0)\n\t"
      "s_barrier\n\t"
      "v_mfma_f32_16x16x32_bf16 v[204:207], v[68:71], v[144:147], v[204:207]\n\t"
      "v_mfma_f32_16x16x32_bf16 v[208:211], v[72:75], v[144:147], v[208:211]\n\t"
      "v_mfma_f32_16x16x32_bf16 v[212:215], v[76:79], v[144:147], v[212:215]\n\t"
      "global_load_dwordx4 v[144:147], v180, s[56:57] sc1\n\t"
      "s_waitcnt vmcnt(7)\n\t"
      "v_mfma_f32_16x16x32_bf16 v[200:203], v[80:83], v[148:151], v[200:203]\n\t"
      "v_mfma_f32_16x16x32_bf16 v[204:207], v[84:87], v[148:151], v[204:207]\n\t"
      "v_mfma_f32_16x16x32_bf16 v[208:211], v[88:91], v[148:151], v[208:211]\n\t"
      "v_mfma_f32_16x16x32_bf16 v[212:215], v[92:95], v[148:151], v[212:215]\n\t"
      "global_load_dwordx4 v[148:151], v181, s[56:57] sc1\n\t"
      "s_waitcnt vmcnt(7)\n\t"
      "v_mfma_f32_16x16x32_bf16 v[200:203], v[96:99], v[152:155], v[200:203]\n\t"
      "v_mfma_f32_16x16x32_bf16 v[204:207], v[100:103], v[152:155], v[204:207]\n\t"
      "v_mfma_f32_16x16x32_bf16 v[208:211], v[104:107], v[152:155], v[208:211]\n\t"
      "v_mfma_f32_16x16x32_bf16 v[212:215], v[108:111], v[152:155], v[212:215]\n\t"
      "global_load_dwordx4 v[152:155], v182, s[56:57] sc1\n\t"
      "s_waitcnt vmcnt(7)\n\t"
      "v_mfma_f32_16x16x32_bf16 v[200:203], v[112:115], v[156:159], v[200:203]\n\t"
      "v_mfma_f32_16x16x32_bf16 v[204:207], v[116:119], v[156:159], v[204:207]\n\t"
      "v_mfma_f32_16x16x32_bf16 v[208:211], v[120:123], v[156:159], v[208:211]\n\t"
      "v_mfma_f32_16x16x32_bf16 v[212:215], v[124:127], v[156:159], v[212:215]\n\t"
      "global_load_dwordx4 v[156:159], v183, s[56:57] sc1\n\t"
      "s_waitcnt vmcnt(7)\n\t"
      "v_mfma_f32_16x16x32_bf16 v[184:187], v[0:3], v[128:131], v[236:239]\n\t"
      "v_mfma_f32_16x16x32_bf16 v[188:191], v[4:7], v[128:131], 0\n\t"
      "v_mfma_f32_16x16x32_bf16 v[192:195], v[8:11], v[128:131], 0\n\t"
      "v_mfma_f32_16x16x32_bf16 v[196:199], v[12:15], v[128:131], 0\n\t"
      "global_load_dwordx4 v[128:131], v176, s[58:59] sc1\n\t"
      "v_cmp_u_f32_e32 vcc, v200, v200\n\t"
      "s_cbranch_vccnz NQ1R_%=\n\t"
      "NQ1B_%=:\n\t"
      "s_waitcnt vmcnt(7)\n\t"
      "v_mfma_f32_16x16x32_bf16 v[184:187], v[16:19], v[132:135], v[184:187]\n\t"
      "ds_write_b128 v243, v[200:203] offset:32768\n\t"
      "v_mfma_f32_16x16x32_bf16 v[188:191], v[20:23], v[132:135], v[188:191]\n\t"
      "v_mfma_f32_16x16x32_bf16 v[192:195], v[24:27], v[132:135], v[192:195]\n\t"
      "v_mfma_f32_16x16x32_bf16 v[196:199], v[28:31], v[132:135], v[196:199]\n\t"
      "global_load_dwordx4 v[132:135], v177, s[58:59] sc1\n\t"
      "ds_write_b128 v164, v[204:207] offset:32768\n\t"
      "s_waitcnt vmcnt(7)\n\t"
      "v_mfma_f32_16x16x32_bf16 v[184:187], v[32:35], v[136:139], v[184:187]\n\t"
      "v_mfma_f32_16x16x32_bf16 v[188:191], v[36:39], v[136:139], v[188:191]\n\t"
      "v_mfma_f32_16x16x32_bf16 v[192:195], v[40:43], v[136:139], v[192:195]\n\t"
      "ds_write_b128 v165, v[208:211] offset:32768\n\t"
      "v_mfma_f32_16x16x32_bf16 v[196:199], v[44:47], v[136:139], v[196:199]\n\t"
      "global_load_dwordx4 v[136:139], v178, s[58:59] sc1\n\t"
      "s_waitcnt vmcnt(7)\n\t"
      "v_mfma_f32_16x16x32_bf16 v[184:187], v[48:51], v[140:143], v[184:187]\n\t"
      "v_mfma_f32_16x16x32_bf16 v[188:191], v[52:55], v[140:143], v[188:191]\n\t"
      "ds_write_b128 v166, v[212:215] offset:32768\n\t"
      "v_mfma_f32_16x16x32_bf16 v[192:195], v[56:59], v[140:143], v[192:195]\n\t"
      "v_mfma_f32_16x16x32_bf16 v[196:199], v[60:63], v[140:143], v[196:199]\n\t"
      "global_load_dwordx4 v[140:143], v179, s[58:59] sc1\n\t"
      "s_waitcnt vmcnt(7)\n\t"
      "v_mfma_f32_16x16x32_bf16 v[184:187], v[64:67], v[144:147], v[184:187]\n\t"
      "s_waitcnt lgkmcnt(0)\n\t"
      "s_barrier\n\t"
      "v_mfma_f32_16x16x32_bf16 v[188:191], v[68:71], v[144:147], v[188:191]\n\t"
      "v_mfma_f32_16x16x32_bf16 v[192:195], v[72:75], v[144:147], v[192:195]\n\t"
      "v_mfma_f32_16x16x32_bf16 v[196:199], v[76:79], v[144:147], v[196:199]\n\t"
      "global_load_dwordx4 v[144:147], v180, s[58:59] sc1\n\t"
      "s_waitcnt vmcnt(7)\n\t"
      "v_mfma_f32_16x16x32_bf16 v[184:187], v[80:83], v[148:151], v[184:187]\n\t"
      "v_mfma_f32_16x16x32_bf16 v[188:191], v[84:87], v[148:151], v[188:191]\n\t"
      "v_mfma_f32_16x16x32_bf16 v[192:195], v[88:91], v[148:151], v[192:195]\n\t"
      "v_mfma_f32_16x16x32_bf16 v[196:199], v[92:95], v[148:151], v[196:199]\n\t"
      "global_load_dwordx4 v[148:151], v181, s[58:59] sc1\n\t"
      "s_waitcnt vmcnt(7)\n\t"
      "v_mfma_f32_16x16x32_bf16 v[184:187], v[96:99], v[152:155], v[184:187]\n\t"
      "v_mfma_f32_16x16x32_bf16 v[188:191], v[100:103], v[152:155], v[188:191]\n\t"
      "v_mfma_f32_16x16x32_bf16 v[192:195], v[104:107], v[152:155], v[192:195]\n\t"
      "v_mfma_f32_16x16x32_bf16 v[196:199], v[108:111], v[152:155], v[196:199]\n\t"
      "global_load_dwordx4 v[152:155], v182, s[58:59] sc1\n\t"
      "s_waitcnt vmcnt(7)\n\t"
      "v_mfma_f32_16x16x32_bf16 v[184:187], v[112:115], v[156:159], v[184:187]\n\t"
      "v_mfma_f32_16x16x32_bf16 v[188:191], v[116:119], v[156:159], v[188:191]\n\t"
      "v_mfma_f32_16x16x32_bf16 v[192:195], v[120:123], v[156:159], v[192:195]\n\t"
      "v_mfma_f32_16x16x32_bf16 v[196:199], v[124:127], v[156:159], v[196:199]\n\t"
      "global_load_dwordx4 v[156:159], v183, s[58:59] sc1\n\t"
      "s_waitcnt vmcnt(7)\n\t"
      "v_mfma_f32_16x16x32_bf16 v[200:203], v[0:3], v[128:131], v[236:239]\n\t"
      "v_mfma_f32_16x16x32_bf16 v[204:207], v[4:7], v[128:131], 0\n\t"
      "v_mfma_f32_16x16x32_bf16 v[208:211], v[8:11], v[128:131], 0\n\t"
      "v_mfma_f32_16x16x32_bf16 v[212:215], v[12:15], v[128:131], 0\n\t"
      "global_load_dwordx4 v[128:131], v176, s[60:61] sc1\n\t"
      "v_cmp_u_f32_e32 vcc, v184, v184\n\t"
      "s_cbranch_vccnz NQ2R_%=\n\t"
      "NQ2B_%=:\n\t"
      "s_waitcnt vmcnt(7)\n\t"
      "v_mfma_f32_16x16x32_bf16 v[200:203], v[16:19], v[132:135], v[200:203]\n\t"
      "ds_write_b128 v243, v[184:187]\n\t"
      "v_mfma_f32_16x16x32_bf16 v[204:207], v[20:23], v[132:135], v[204:207]\n\t"
      "v_mfma_f32_16x16x32_bf16 v[208:211], v[24:27], v[132:135], v[208:211]\n\t"
      "v_mfma_f32_16x16x32_bf16 v[212:215], v[28:31], v[132:135], v[212:215]\n\t"
      "global_load_dwordx4 v[132:135], v177, s[60:61] sc1\n\t"
      "ds_write_b128 v164, v[188:191]\n\t"
      "s_waitcnt vmcnt(7)\n\t"
      "v_mfma_f32_16x16x32_bf16 v[200:203], v[32:35], v[136:139], v[200:203]\n\t"
      "v_mfma_f32_16x16x32_bf16 v[204:207], v[36:39], v[136:139], v[204:207]\n\t"
      "v_mfma_f32_16x16x32_bf16 v[208:211], v[40:43], v[136:139], v[208:211]\n\t"
      "ds_write_b128 v165, v[192:195]\n\t"
      "v_mfma_f32_16x16x32_bf16 v[212:215], v[44:47], v[136:139], v[212:215]\n\t"
      "global_load_dwordx4 v[136:139], v178, s[60:61] sc1\n\t"
      "s_waitcnt vmcnt(7)\n\t"
      "v_mfma_f32_16x16x32_bf16 v[200:203], v[48:51], v[140:143], v[200:203]\n\t"
      "v_mfma_f32_16x16x32_bf16 v[204:207], v[52:55], v[140:143], v[204:207]\n\t"
      "ds_write_b128 v166, v[196:199]\n\t"
      "v_mfma_f32_16x16x32_bf16 v[208:211], v[56:59], v[140:143], v[208:211]\n\t"
      "v_mfma_f32_16x16x32_bf16 v[212:215], v[60:63], v[140:143], v[212:215]\n\t"
      "global_load_dwordx4 v[140:143], v179, s[60:61] sc1\n\t"
      "s_waitcnt vmcnt(7)\n\t"
      "v_mfma_f32_16x16x32_bf16 v[200:203], v[64:67], v[144:147], v[200:203]\n\t"
      "s_waitcnt lgkmcnt(0)\n\t"
      "s_barrier\n\t"
      "v_mfma_f32_16x16x32_bf16 v[204:207], v[68:71], v[144:147], v[204:207]\n\t"
      "v_mfma_f32_16x16x32_bf16 v[208:211], v[72:75], v[144:147], v[208:211]\n\t"
      "v_mfma_f32_16x16x32_bf16 v[212:215], v[76:79], v[144:147], v[212:215]\n\t"
      "global_load_dwordx4 v[144:147], v180, s[60:61] sc1\n\t"
      "s_waitcnt vmcnt(7)\n\t"
      "v_mfma_f32_16x16x32_bf16 v[200:203], v[80:83], v[148:151], v[200:203]\n\t"
      "v_mfma_f32_16x16x32_bf16 v[204:207], v[84:87], v[148:151], v[204:207]\n\t"
      "v_mfma_f32_16x16x32_bf16 v[208:211], v[88:91], v[148:151], v[208:211]\n\t"
      "v_mfma_f32_16x16x32_bf16 v[212:215], v[92:95], v[148:151], v[212:215]\n\t"
      "global_load_dwordx4 v[148:151], v181, s[60:61] sc1\n\t"
      "s_waitcnt vmcnt(7)\n\t"
      "v_mfma_f32_16x16x32_bf16 v[200:203], v[96:99], v[152:155], v[200:203]\n\t"
      "v_mfma_f32_16x16x32_bf16 v[204:207], v[100:103], v[152:155], v[204:207]\n\t"
      "v_mfma_f32_16x16x32_bf16 v[208:211], v[104:107], v[152:155], v[208:211]\n\t"
      "v_mfma_f32_16x16x32_bf16 v[212:215], v[108:111], v[152:155], v[212:215]\n\t"
      "global_load_dwordx4 v[152:155], v182, s[60:61] sc1\n\t"
      "s_waitcnt vmcnt(7)\n\t"
      "v_mfma_f32_16x16x32_bf16 v[200:203], v[112:115], v[156:159], v[200:203]\n\t"
      "v_mfma_f32_16x16x32_bf16 v[204:207], v[116:119], v[156:159], v[204:207]\n\t"
      "v_mfma_f32_16x16x32_bf16 v[208:211], v[120:123], v[156:159], v[208:211]\n\t"
      "v_mfma_f32_16x16x32_bf16 v[212:215], v[124:127], v[156:159], v[212:215]\n\t"
      "global_load_dwordx4 v[156:159], v183, s[60:61] sc1\n\t"
      "s_waitcnt vmcnt(7)\n\t"
      "v_mfma_f32_16x16x32_bf16 v[184:187], v[0:3], v[128:131], v[236:239]\n\t"
      "v_mfma_f32_16x16x32_bf16 v[188:191], v[4:7], v[128:131], 0\n\t"
      "v_mfma_f32_16x16x32_bf16 v[192:195], v[8:11], v[128:131], 0\n\t"
      "v_mfma_f32_16x16x32_bf16 v[196:199], v[12:15], v[128:131], 0\n\t"
      "global_load_dwordx4 v[128:131], v176, s[62:63] sc1\n\t"
      "v_cmp_u_f32_e32 vcc, v200, v200\n\t"
      "s_cbranch_vccnz NQ3R_%=\n\t"
      "NQ3B_%=:\n\t"
      "s_waitcnt vmcnt(7)\n\t"
      "v_mfma_f32_16x16x32_bf16 v[184:187], v[16:19], v[132:135], v[184:187]\n\t"
      "ds_write_b128 v243, v[200:203] offset:32768\n\t"
      "v_mfma_f32_16x16x32_bf16 v[188:191], v[20:23], v[132:135], v[188:191]\n\t"
      "v_mfma_f32_16x16x32_bf16 v[192:195], v[24:27], v[132:135], v[192:195]\n\t"
      "v_mfma_f32_16x16x32_bf16 v[196:199], v[28:31], v[132:135], v[196:199]\n\t"
      "global_load_dwordx4 v[132:135], v177, s[62:63] sc1\n\t"
      "ds_write_b128 v164, v[204:207] offset:32768\n\t"
      "s_waitcnt vmcnt(7)\n\t"
      "v_mfma_f32_16x16x32_bf16 v[184:187], v[32:35], v[136:139], v[184:187]\n\t"
      "v_mfma_f32_16x16x32_bf16 v[188:191], v[36:39], v[136:139], v[188:191]\n\t"
      "v_mfma_f32_16x16x32_bf16 v[192:195], v[40:43], v[136:139], v[192:195]\n\t"
      "ds_write_b128 v165, v[208:211] offset:32768\n\t"
      "v_mfma_f32_16x16x32_bf16 v[196:199], v[44:47], v[136:139], v[196:199]\n\t"
      "global_load_dwordx4 v[136:139], v178, s[62:63] sc1\n\t"
      "s_waitcnt vmcnt(7)\n\t"
      "v_mfma_f32_16x16x32_bf16 v[184:187], v[48:51], v[140:143], v[184:187]\n\t"
      "v_mfma_f32_16x16x32_bf16 v[188:191], v[52:55], v[140:143], v[188:191]\n\t"
      "ds_write_b128 v166, v[212:215] offset:32768\n\t"
      "v_mfma_f32_16x16x32_bf16 v[192:195], v[56:59], v[140:143], v[192:195]\n\t"
      "v_mfma_f32_16x16x32_bf16 v[196:199], v[60:63], v[140:143], v[196:199]\n\t"
      "global_load_dwordx4 v[140:143], v179, s[62:63] sc1\n\t"
      "s_waitcnt vmcnt(7)\n\t"
      "v_mfma_f32_16x16x32_bf16 v[184:187], v[64:67], v[144:147], v[184:187]\n\t"
      "s_waitcnt lgkmcnt(0)\n\t"
      "s_barrier\n\t"
      "v_mfma_f32_16x16x32_bf16 v[188:191], v[68:71], v[144:147], v[188:191]\n\t"
      "v_mfma_f32_16x16x32_bf16 v[192:195], v[72:75], v[144:147], v[192:195]\n\t"
      "v_mfma_f32_16x16x32_bf16 v[196:199], v[76:79], v[144:147], v[196:199]\n\t"
      "global_load_dwordx4 v[144:147], v180, s[62:63] sc1\n\t"
      "s_waitcnt vmcnt(7)\n\t"
      "v_mfma_f32_16x16x32_bf16 v[184:187], v[80:83], v[148:151], v[184:187]\n\t"
      "v_mfma_f32_16x16x32_bf16 v[188:191], v[84:87], v[148:151], v[188:191]\n\t"
      "v_mfma_f32_16x16x32_bf16 v[192:195], v[88:91], v[148:151], v[192:195]\n\t"
      "v_mfma_f32_16x16x32_bf16 v[196:199], v[92:95], v[148:151], v[196:199]\n\t"
      "global_load_dwordx4 v[148:151], v181, s[62:63] sc1\n\t"
      "s_waitcnt vmcnt(7)\n\t"
      "v_mfma_f32_16x16x32_bf16 v[184:187], v[96:99], v[152:155], v[184:187]\n\t"
      "v_mfma_f32_16x16x32_bf16 v[188:191], v[100:103], v[152:155], v[188:191]\n\t"
      "v_mfma_f32_16x16x32_bf16 v[192:195], v[104:107], v[152:155], v[192:195]\n\t"
      "v_mfma_f32_16x16x32_bf16 v[196:199], v[108:111], v[152:155], v[196:199]\n\t"
      "global_load_dwordx4 v[152:155], v182, s[62:63] sc1\n\t"
      "s_waitcnt vmcnt(7)\n\t"
      "v_mfma_f32_16x16x32_bf16 v[184:187], v[112:115], v[156:159], v[184:187]\n\t"
      "v_mfma_f32_16x16x32_bf16 v[188:191], v[116:119], v[156:159], v[188:191]\n\t"
      "v_mfma_f32_16x16x32_bf16 v[192:195], v[120:123], v[156:159], v[192:195]\n\t"
      "v_mfma_f32_16x16x32_bf16 v[196:199], v[124:127], v[156:159], v[196:199]\n\t"
      "global_load_dwordx4 v[156:159], v183, s[62:63] sc1\n\t"
      "s_waitcnt vmcnt(7)\n\t"
      "v_mfma_f32_16x16x32_bf16 v[200:203], v[0:3], v[128:131], v[236:239]\n\t"
      "v_mfma_f32_16x16x32_bf16 v[204:207], v[4:7], v[128:131], 0\n\t"
      "v_mfma_f32_16x16x32_bf16 v[208:211], v[8:11], v[128:131], 0\n\t"
      "v_mfma_f32_16x16x32_bf16 v[212:215], v[12:15], v[128:131], 0\n\t"
      "global_load_dwordx4 v[128:131], v176, s[64:65] sc1\n\t"
      "v_cmp_u_f32_e32 vcc, v184, v184\n\t"
      "s_cbranch_vccnz NQ4R_%=\n\t"
      "NQ4B_%=:\n\t"
      "s_waitcnt vmcnt(7)\n\t"
      "v_mfma_f32_16x16x32_bf16 v[200:203], v[16:19], v[132:135], v[200:203]\n\t"
      "ds_write_b128 v243, v[184:187]\n\t"
      "v_mfma_f32_16x16x32_bf16 v[204:207], v[20:23], v[132:135], v[204:207]\n\t"
      "v_mfma_f32_16x16x32_bf16 v[208:211], v[24:27], v[132:135], v[208:211]\n\t"
      "v_mfma_f32_16x16x32_bf16 v[212:215], v[28:31], v[132:135], v[212:215]\n\t"
      "global_load_dwordx4 v[132:135], v177, s[64:65] sc1\n\t"
      "ds_write_b128 v164, v[188:191]\n\t"
      "s_waitcnt vmcnt(7)\n\t"
      "v_mfma_f32_16x16x32_bf16 v[200:203], v[32:35], v[136:139], v[200:203]\n\t"
      "v_mfma_f32_16x16x32_bf16 v[204:207], v[36:39], v[136:139], v[204:207]\n\t"
      "v_mfma_f32_16x16x32_bf16 v[208:211], v[40:43], v[136:139], v[208:211]\n\t"
      "ds_write_b128 v165, v[192:195]\n\t"
      "v_mfma_f32_16x16x32_bf16 v[212:215], v[44:47], v[136:139], v[212:215]\n\t"
      "global_load_dwordx4 v[136:139], v178, s[64:65] sc1\n\t"
      "s_waitcnt vmcnt(7)\n\t"
      "v_mfma_f32_16x16x32_bf16 v[200:203], v[48:51], v[140:143], v[200:203]\n\t"
      "v_mfma_f32_16x16x32_bf16 v[204:207], v[52:55], v[140:143], v[204:207]\n\t"
      "ds_write_b128 v166, v[196:199]\n\t"
      "v_mfma_f32_16x16x32_bf16 v[208:211], v[56:59], v[140:143], v[208:211]\n\t"
      "v_mfma_f32_16x16x32_bf16 v[212:215], v[60:63], v[140:143], v[212:215]\n\t"
      "global_load_dwordx4 v[140:143], v179, s[64:65] sc1\n\t"
      "s_waitcnt vmcnt(7)\n\t"
      "v_mfma_f32_16x16x32_bf16 v[200:203], v[64:67], v[144:147], v[200:203]\n\t"
      "s_waitcnt lgkmcnt(0)\n\t"
      "s_barrier\n\t"
      "v_mfma_f32_16x16x32_bf16 v[204:207], v[68:71], v[144:147], v[204:207]\n\t"
      "v_mfma_f32_16x16x32_bf16 v[208:211], v[72:75], v[144:147], v[208:211]\n\t"
      "v_mfma_f32_16x16x32_bf16 v[212:215], v[76:79], v[144:147], v[212:215]\n\t"
      "global_load_dwordx4 v[144:147], v180, s[64:65] sc1\n\t"
      "s_waitcnt vmcnt(7)\n\t"
      "v_mfma_f32_16x16x32_bf16 v[200:203], v[80:83], v[148:151], v[200:203]\n\t"
      "v_mfma_f32_16x16x32_bf16 v[204:207], v[84:87], v[148:151], v[204:207]\n\t"
      "v_mfma_f32_16x16x32_bf16 v[208:211], v[88:91], v[148:151], v[208:211]\n\t"
      "v_mfma_f32_16x16x32_bf16 v[212:215], v[92:95], v[148:151], v[212:215]\n\t"
      "global_load_dwordx4 v[148:151], v181, s[64:65] sc1\n\t"
      "s_waitcnt vmcnt(7)\n\t"
      "v_mfma_f32_16x16x32_bf16 v[200:203], v[96:99], v[152:155], v[200:203]\n\t"
      "v_mfma_f32_16x16x32_bf16 v[204:207], v[100:103], v[152:155], v[204:207]\n\t"
      "v_mfma_f32_16x16x32_bf16 v[208:211], v[104:107], v[152:155], v[208:211]\n\t"
      "v_mfma_f32_16x16x32_bf16 v[212:215], v[108:111], v[152:155], v[212:215]\n\t"
      "global_load_dwordx4 v[152:155], v182, s[64:65] sc1\n\t"
      "s_waitcnt vmcnt(7)\n\t"
      "v_mfma_f32_16x16x32_bf16 v[200:203], v[112:115], v[156:159], v[200:203]\n\t"
      "v_mfma_f32_16x16x32_bf16 v[204:207], v[116:119], v[156:159], v[204:207]\n\t"
      "v_mfma_f32_16x16x32_bf16 v[208:211], v[120:123], v[156:159], v[208:211]\n\t"
      "v_mfma_f32_16x16x32_bf16 v[212:215], v[124:127], v[156:159], v[212:215]\n\t"
      "global_load_dwordx4 v[156:159], v183, s[64:65] sc1\n\t"
      "s_waitcnt vmcnt(7)\n\t"
      "v_mfma_f32_16x16x32_bf16 v[184:187], v[0:3], v[128:131], v[236:239]\n\t"
      "v_mfma_f32_16x16x32_bf16 v[188:191], v[4:7], v[128:131], 0\n\t"
      "v_mfma_f32_16x16x32_bf16 v[192:195], v[8:11], v[128:131], 0\n\t"
      "v_mfma_f32_16x16x32_bf16 v[196:199], v[12:15], v[128:131], 0\n\t"
      "v_cmp_u_f32_e32 vcc, v200, v200\n\t"
      "s_cbranch_vccnz NQ5R_%=\n\t"
      "NQ5B_%=:\n\t"
      "s_waitcnt vmcnt(6)\n\t"
      "v_mfma_f32_16x16x32_bf16 v[184:187], v[16:19], v[132:135], v[184:187]\n\t"
      "ds_write_b128 v243, v[200:203] offset:32768\n\t"
      "v_mfma_f32_16x16x32_bf16 v[188:191], v[20:23], v[132:135], v[188:191]\n\t"
      "v_mfma_f32_16x16x32_bf16 v[192:195], v[24:27], v[132:135], v[192:195]\n\t"
      "v_mfma_f32_16x16x32_bf16 v[196:199], v[28:31], v[132:135], v[196:199]\n\t"
      "ds_write_b128 v164, v[204:207] offset:32768\n\t"
      "s_waitcnt vmcnt(5)\n\t"
      "v_mfma_f32_16x16x32_bf16 v[184:187], v[32:35], v[136:139], v[184:187]\n\t"
      "v_mfma_f32_16x16x32_bf16 v[188:191], v[36:39], v[136:139], v[188:191]\n\t"
      "v_mfma_f32_16x16x32_bf16 v[192:195], v[40:43], v[136:139], v[192:195]\n\t"
      "ds_write_b128 v165, v[208:211] offset:32768\n\t"
      "v_mfma_f32_16x16x32_bf16 v[196:199], v[44:47], v[136:139], v[196:199]\n\t"
      "s_waitcnt vmcnt(4)\n\t"
      "v_mfma_f32_16x16x32_bf16 v[184:187], v[48:51], v[140:143], v[184:187]\n\t"
      "v_mfma_f32_16x16x32_bf16 v[188:191], v[52:55], v[140:143], v[188:191]\n\t"
      "ds_write_b128 v166, v[212:215] offset:32768\n\t"
      "v_mfma_f32_16x16x32_bf16 v[192:195], v[56:59], v[140:143], v[192:195]\n\t"
      "v_mfma_f32_16x16x32_bf16 v[196:199], v[60:63], v[140:143], v[196:199]\n\t"
      "s_waitcnt vmcnt(3)\n\t"
      "v_mfma_f32_16x16x32_bf16 v[184:187], v[64:67], v[144:147], v[184:187]\n\t"
      "s_waitcnt lgkmcnt(0)\n\t"
      "s_barrier\n\t"
      "v_mfma_f32_16x16x32_bf16 v[188:191], v[68:71], v[144:147], v[188:191]\n\t"
      "v_mfma_f32_16x16x32_bf16 v[192:195], v[72:75], v[144:147], v[192:195]\n\t"
      "v_mfma_f32_16x16x32_bf16 v[196:199], v[76:79], v[144:147], v[196:199]\n\t"
      "s_waitcnt vmcnt(2)\n\t"
      "v_mfma_f32_16x16x32_bf16 v[184:187], v[80:83], v[148:151], v[184:187]\n\t"
      "v_mfma_f32_16x16x32_bf16 v[188:191], v[84:87], v[148:151], v[188:191]\n\t"
      "v_mfma_f32_16x16x32_bf16 v[192:195], v[88:91], v[148:151], v[192:195]\n\t"
      "v_mfma_f32_16x16x32_bf16 v[196:199], v[92:95], v[148:151], v[196:199]\n\t"
      "s_waitcnt vmcnt(1)\n\t"
      "v_mfma_f32_16x16x32_bf16 v[184:187], v[96:99], v[152:155], v[184:187]\n\t"
      "v_mfma_f32_16x16x32_bf16 v[188:191], v[100:103], v[152:155], v[188:191]\n\t"
      "v_mfma_f32_16x16x32_bf16 v[192:195], v[104:107], v[152:155], v[192:195]\n\t"
      "v_mfma_f32_16x16x32_bf16 v[196:199], v[108:111], v[152:155], v[196:199]\n\t"
      "s_waitcnt vmcnt(0)\n\t"
      "v_mfma_f32_16x16x32_bf16 v[184:187], v[112:115], v[156:159], v[184:187]\n\t"
      "v_mfma_f32_16x16x32_bf16 v[188:191], v[116:119], v[156:159], v[188:191]\n\t"
      "v_mfma_f32_16x16x32_bf16 v[192:195], v[120:123], v[156:159], v[192:195]\n\t"
      "v_mfma_f32_16x16x32_bf16 v[196:199], v[124:127], v[156:159], v[196:199]\n\t"
      "s_nop 4\n\t"
      "v_cmp_u_f32_e32 vcc, v184, v184\n\t"
      "s_cbranch_vccnz NQ6R_%=\n\t"
      "NQ6B_%=:\n\t"
      "ds_write_b128 v243, v[184:187]\n\t"
      "ds_write_b128 v164, v[188:191]\n\t"
      "ds_write_b128 v165, v[192:195]\n\t"
      "ds_write_b128 v166, v[196:199]\n\t"
      "s_waitcnt lgkmcnt(0)\n\t"
      "s_barrier\n\t"
      "s_branch GX_%=\n\t"
      "ASP_%=:\n\t"
      "s_mov_b32 m0, 163328\n\t"
      "s_nop 0\n\t"
      "global_load_lds_dword v241, s[4:5]\n\t"
      "s_add_u32 s4, s4, 131072\n\t"
      "s_addc_u32 s5, s5, 0\n\t"
      "global_load_lds_dword v241, s[4:5]\n\t"
      "s_add_u32 s4, s4, 131072\n\t"
      "s_addc_u32 s5, s5, 0\n\t"
      "global_load_lds_dword v241, s[4:5]\n\t"
      "s_add_u32 s4, s4, 131072\n\t"
      "s_addc_u32 s5, s5, 0\n\t"
      "global_load_lds_dword v241, s[4:5]\n\t"
      "s_add_u32 s4, s4, 131072\n\t"
      "s_addc_u32 s5, s5, 0\n\t"
      "global_load_lds_dword v241, s[4:5]\n\t"
      "s_add_u32 s4, s4, 131072\n\t"
      "s_addc_u32 s5, s5, 0\n\t"
      "global_load_lds_dword v241, s[4:5]\n\t"
      "s_add_u32 s4, s4, 131072\n\t"
      "s_addc_u32 s5, s5, 0\n\t"
      "global_load_lds_dword v241, s[4:5]\n\t"
      "s_add_u32 s4, s4, 131072\n\t"
      "s_addc_u32 s5, s5, 0\n\t"
      "s_branch AEND_%=\n\t"
      "FQ0R_%=:\n\t"
      "s_mov_b32 s51, 0\n\t"
      "FQ0L_%=:\n\t"
      "s_add_u32 s51, s51, 1\n\t"
      "s_cmp_lt_u32 s51, 262144\n\t"
      "s_cbranch_scc0 FQ0X_%=\n\t"
      "s_waitcnt vmcnt(0)\n\t"
      "s_sleep 1\n\t"
      "global_load_dwordx4 v[128:131], v176, s[52:53] sc1\n\t"
      "global_load_dwordx4 v[132:135], v177, s[52:53] sc1\n\t"
      "global_load_dwordx4 v[136:139], v178, s[52:53] sc1\n\t"
      "global_load_dwordx4 v[140:143], v179, s[52:53] sc1\n\t"
      "global_load_dwordx4 v[144:147], v180, s[52:53] sc1\n\t"
      "global_load_dwordx4 v[148:151], v181, s[52:53] sc1\n\t"
      "global_load_dwordx4 v[152:155], v182, s[52:53] sc1\n\t"
      "global_load_dwordx4 v[156:159], v183, s[52:53] sc1\n\t"
      "s_waitcnt vmcnt(7)\n\t"
      "v_max3_u32 v234, v128, v129, v130\n\t"
      "v_max_u32_e32 v234, v234, v131\n\t"
      "v_cmp_eq_u32_e32 vcc, -1, v234\n\t"
      "s_cbranch_vccnz FQ0L_%=\n\t"
      "FQ0X_%=:\n\t"
      "s_branch FQ0B_%=\n\t"
      "FQ1R_%=:\n\t"
      "s_mov_b32 s51, 0\n\t"
      "FQ1L_%=:\n\t"
      "s_add_u32 s51, s51, 1\n\t"
      "s_cmp_lt_u32 s51, 262144\n\t"
      "s_cbranch_scc0 FQ1X_%=\n\t"
      "s_waitcnt vmcnt(0)\n\t"
      "s_sleep 1\n\t"
      "global_load_dwordx4 v[132:135], v177, s[52:53] sc1\n\t"
      "global_load_dwordx4 v[136:139], v178, s[52:53] sc1\n\t"
      "global_load_dwordx4 v[140:143], v179, s[52:53] sc1\n\t"
      "global_load_dwordx4 v[144:147], v180, s[52:53] sc1\n\t"
      "global_load_dwordx4 v[148:151], v181, s[52:53] sc1\n\t"
      "global_load_dwordx4 v[152:155], v182, s[52:53] sc1\n\t"
      "global_load_dwordx4 v[156:159], v183, s[52:53] sc1\n\t"
      "global_load_dwordx4 v[128:131], v176, s[54:55] sc1\n\t"
      "s_waitcnt vmcnt(7)\n\t"
      "v_max3_u32 v234, v132, v133, v134\n\t"
      "v_max_u32_e32 v234, v234, v135\n\t"
      "v_cmp_eq_u32_e32 vcc, -1, v234\n\t"
      "s_cbranch_vccnz FQ1L_%=\n\t"
      "FQ1X_%=:\n\t"
      "s_branch FQ1B_%=\n\t"
      "FQ2R_%=:\n\t"
      "s_mov_b32 s51, 0\n\t"
      "FQ2L_%=:\n\t"
      "s_add_u32 s51, s51, 1\n\t"
      "s_cmp_lt_u32 s51, 262144\n\t"
      "s_cbranch_scc0 FQ2X_%=\n\t"
      "s_waitcnt vmcnt(0)\n\t"
      "s_sleep 1\n\t"
      "global_load_dwordx4 v[136:139], v178, s[52:53] sc1\n\t"
      "global_load_dwordx4 v[140:143], v179, s[52:53] sc1\n\t"
      "global_load_dwordx4 v[144:147], v180, s[52:53] sc1\n\t"
      "global_load_dwordx4 v[148:151], v181, s[52:53] sc1\n\t"
      "global_load_dwordx4 v[152:155], v182, s[52:53] sc1\n\t"
      "global_load_dwordx4 v[156:159], v183, s[52:53] sc1\n\t"
      "global_load_dwordx4 v[128:131], v176, s[54:55] sc1\n\t"
      "global_load_dwordx4 v[132:135], v177, s[54:55] sc1\n\t"
      "s_waitcnt vmcnt(7)\n\t"
      "v_max3_u32 v234, v136, v137, v138\n\t"
      "v_max_u32_e32 v234, v234, v139\n\t"
      "v_cmp_eq_u32_e32 vcc, -1, v234\n\t"
      "s_cbranch_vccnz FQ2L_%=\n\t"
      "FQ2X_%=:\n\t"
      "s_branch FQ2B_%=\n\t"
      "FQ3R_%=:\n\t"
      "s_mov_b32 s51, 0\n\t"
      "FQ3L_%=:\n\t"
      "s_add_u32 s51, s51, 1\n\t"
      "s_cmp_lt_u32 s51, 262144\n\t"
      "s_cbranch_scc0 FQ3X_%=\n\t"
      "s_waitcnt vmcnt(0)\n\t"
      "s_sleep 1\n\t"
      "global_load_dwordx4 v[140:143], v179, s[52:53] sc1\n\t"
      "global_load_dwordx4 v[144:147], v180, s[52:53] sc1\n\t"
      "global_load_dwordx4 v[148:151], v181, s[52:53] sc1\n\t"
      "global_load_dwordx4 v[152:155], v182, s[52:53] sc1\n\t"
      "global_load_dwordx4 v[156:159], v183, s[52:53] sc1\n\t"
      "global_load_dwordx4 v[128:131], v176, s[54:55] sc1\n\t"
      "global_load_dwordx4 v[132:135], v177, s[54:55] sc1\n\t"
      "global_load_dwordx4 v[136:139], v178, s[54:55] sc1\n\t"
      "s_waitcnt vmcnt(7)\n\t"
      "v_max3_u32 v234, v140, v141, v142\n\t"
      "v_max_u32_e32 v234, v234, v143\n\t"
      "v_cmp_eq_u32_e32 vcc, -1, v234\n\t"
      "s_cbranch_vccnz FQ3L_%=\n\t"
      "FQ3X_%=:\n\t"
      "s_branch FQ3B_%=\n\t"
      "FQ4R_%=:\n\t"
      "s_mov_b32 s51, 0\n\t"
      "FQ4L_%=:\n\t"
      "s_add_u32 s51, s51, 1\n\t"
      "s_cmp_lt_u32 s51, 262144\n\t"
      "s_cbranch_scc0 FQ4X_%=\n\t"
      "s_waitcnt vmcnt(0)\n\t"
      "s_sleep 1\n\t"
      "global_load_dwordx4 v[144:147], v180, s[52:53] sc1\n\t"
      "global_load_dwordx4 v[148:151], v181, s[52:53] sc1\n\t"
      "global_load_dwordx4 v[152:155], v182, s[52:53] sc1\n\t"
      "global_load_dwordx4 v[156:159], v183, s[52:53] sc1\n\t"
      "global_load_dwordx4 v[128:131], v176, s[54:55] sc1\n\t"
      "global_load_dwordx4 v[132:135], v177, s[54:55] sc1\n\t"
      "global_load_dwordx4 v[136:139], v178, s[54:55] sc1\n\t"
      "global_load_dwordx4 v[140:143], v179, s[54:55] sc1\n\t"
      "s_waitcnt vmcnt(7)\n\t"
      "v_max3_u32 v234, v144, v145, v146\n\t"
      "v_max_u32_e32 v234, v234, v147\n\t"
      "v_cmp_eq_u32_e32 vcc, -1, v234\n\t"
      "s_cbranch_vccnz FQ4L_%=\n\t"
      "FQ4X_%=:\n\t"
      "s_branch FQ4B_%=\n\t"
      "FQ5R_%=:\n\t"
      "s_mov_b32 s51, 0\n\t"
      "FQ5L_%=:\n\t"
      "s_add_u32 s51, s51, 1\n\t"
      "s_cmp_lt_u32 s51, 262144\n\t"
      "s_cbranch_scc0 FQ5X_%=\n\t"
      "s_waitcnt vmcnt(0)\n\t"
      "s_sleep 1\n\t"
      "global_load_dwordx4 v[148:151], v181, s[52:53] sc1\n\t"
      "global_load_dwordx4 v[152:155], v182, s[52:53] sc1\n\t"
      "global_load_dwordx4 v[156:159], v183, s[52:53] sc1\n\t"
      "global_load_dwordx4 v[128:131], v176, s[54:55] sc1\n\t"
      "global_load_dwordx4 v[132:135], v177, s[54:55] sc1\n\t"
      "global_load_dwordx4 v[136:139], v178, s[54:55] sc1\n\t"
      "global_load_dwordx4 v[140:143], v179, s[54:55] sc1\n\t"
      "global_load_dwordx4 v[144:147], v180, s[54:55] sc1\n\t"
      "s_waitcnt vmcnt(7)\n\t"
      "v_max3_u32 v234, v148, v149, v150\n\t"
      "v_max_u32_e32 v234, v234, v151\n\t"
      "v_cmp_eq_u32_e32 vcc, -1, v234\n\t"
      "s_cbranch_vccnz FQ5L_%=\n\t"
      "FQ5X_%=:\n\t"
      "s_branch FQ5B_%=\n\t"
      "FQ6R_%=:\n\t"
      "s_mov_b32 s51, 0\n\t"
      "FQ6L_%=:\n\t"
      "s_add_u32 s51, s51, 1\n\t"
      "s_cmp_lt_u32 s51, 262144\n\t"
      "s_cbranch_scc0 FQ6X_%=\n\t"
      "s_waitcnt vmcnt(0)\n\t"
      "s_sleep 1\n\t"
      "global_load_dwordx4 v[152:155], v182, s[52:53] sc1\n\t"
      "global_load_dwordx4 v[156:159], v183, s[52:53] sc1\n\t"
      "global_load_dwordx4 v[128:131], v176, s[54:55] sc1\n\t"
      "global_load_dwordx4 v[132:135], v177, s[54:55] sc1\n\t"
      "global_load_dwordx4 v[136:139], v178, s[54:55] sc1\n\t"
      "global_load_dwordx4 v[140:143], v179, s[54:55] sc1\n\t"
      "global_load_dwordx4 v[144:147], v180, s[54:55] sc1\n\t"
      "global_load_dwordx4 v[148:151], v181, s[54:55] sc1\n\t"
      "s_waitcnt vmcnt(7)\n\t"
      "v_max3_u32 v234, v152, v153, v154\n\t"
      "v_max_u32_e32 v234, v234, v155\n\t"
      "v_cmp_eq_u32_e32 vcc, -1, v234\n\t"
      "s_cbranch_vccnz FQ6L_%=\n\t"
      "FQ6X_%=:\n\t"
      "s_branch FQ6B_%=\n\t"
      "FQ7R_%=:\n\t"
      "s_mov_b32 s51, 0\n\t"
      "FQ7L_%=:\n\t"
      "s_add_u32 s51, s51, 1\n\t"
      "s_cmp_lt_u32 s51, 262144\n\t"
      "s_cbranch_scc0 FQ7X_%=\n\t"
      "s_waitcnt vmcnt(0)\n\t"
      "s_sleep 1\n\t"
      "global_load_dwordx4 v[156:159], v183, s[52:53] sc1\n\t"
      "global_load_dwordx4 v[128:131], v176, s[54:55] sc1\n\t"
      "global_load_dwordx4 v[132:135], v177, s[54:55] sc1\n\t"
      "global_load_dwordx4 v[136:139], v178, s[54:55] sc1\n\t"
      "global_load_dwordx4 v[140:143], v179, s[54:55] sc1\n\t"
      "global_load_dwordx4 v[144:147], v180, s[54:55] sc1\n\t"
      "global_load_dwordx4 v[148:151], v181, s[54:55] sc1\n\t"
      "global_load_dwordx4 v[152:155], v182, s[54:55] sc1\n\t"
      "s_waitcnt vmcnt(7)\n\t"
      "v_max3_u32 v234, v156, v157, v158\n\t"
      "v_max_u32_e32 v234, v234, v159\n\t"
      "v_cmp_eq_u32_e32 vcc, -1, v234\n\t"
      "s_cbranch_vccnz FQ7L_%=\n\t"
      "FQ7X_%=:\n\t"
      "s_branch FQ7B_%=\n\t"
      "NQ1R_%=:\n\t"
      "s_mov_b32 s51, 0\n\t"
      "NQ1L_%=:\n\t"
      "s_add_u32 s51, s51, 1\n\t"
      "s_cmp_lt_u32 s51, 262144\n\t"
      "s_cbranch_scc0 NQ1X_%=\n\t"
      "s_waitcnt vmcnt(0)\n\t"
      "s_sleep 1\n\t"
      "global_load_dwordx4 v[128:131], v176, s[54:55] sc1\n\t"
      "global_load_dwordx4 v[132:135], v177, s[54:55] sc1\n\t"
      "global_load_dwordx4 v[136:139], v178, s[54:55] sc1\n\t"
      "global_load_dwordx4 v[140:143], v179, s[54:55] sc1\n\t"
      "global_load_dwordx4 v[144:147], v180, s[54:55] sc1\n\t"
      "global_load_dwordx4 v[148:151], v181, s[54:55] sc1\n\t"
      "global_load_dwordx4 v[152:155], v182, s[54:55] sc1\n\t"
      "global_load_dwordx4 v[156:159], v183, s[54:55] sc1\n\t"
      "s_waitcnt vmcnt(0)\n\t"
      "v_max3_u32 v234, v128, v129, v130\n\t"
      "v_max3_u32 v234, v234, v131, v132\n\t"
      "v_max3_u32 v234, v234, v133, v134\n\t"
      "v_max3_u32 v234, v234, v135, v136\n\t"
      "v_max3_u32 v234, v234, v137, v138\n\t"
      "v_max3_u32 v234, v234, v139, v140\n\t"
      "v_max3_u32 v234, v234, v141, v142\n\t"
      "v_max3_u32 v234, v234, v143, v144\n\t"
      "v_max3_u32 v234, v234, v145, v146\n\t"
      "v_max3_u32 v234, v234, v147, v148\n\t"
      "v_max3_u32 v234, v234, v149, v150\n\t"
      "v_max3_u32 v234, v234, v151, v152\n\t"
      "v_max3_u32 v234, v234, v153, v154\n\t"
      "v_max3_u32 v234, v234, v155, v156\n\t"
      "v_max3_u32 v234, v234, v157, v158\n\t"
      "v_max_u32_e32 v234, v234, v159\n\t"
      "v_cmp_eq_u32_e32 vcc, -1, v234\n\t"
      "s_cbranch_vccnz NQ1L_%=\n\t"
      "NQ1X_%=:\n\t"
      "v_mfma_f32_16x16x32_bf16 v[200:203], v[0:3], v[128:131], v[236:239]\n\t"
      "v_mfma_f32_16x16x32_bf16 v[204:207], v[4:7], v[128:131], 0\n\t"
      "v_mfma_f32_16x16x32_bf16 v[208:211], v[8:11], v[128:131], 0\n\t"
      "v_mfma_f32_16x16x32_bf16 v[212:215], v[12:15], v[128:131], 0\n\t"
      "v_mfma_f32_16x16x32_bf16 v[200:203], v[16:19], v[132:135], v[200:203]\n\t"
      "v_mfma_f32_16x16x32_bf16 v[204:207], v[20:23], v[132:135], v[204:207]\n\t"
      "v_mfma_f32_16x16x32_bf16 v[208:211], v[24:27], v[132:135], v[208:211]\n\t"
      "v_mfma_f32_16x16x32_bf16 v[212:215], v[28:31], v[132:135], v[212:215]\n\t"
      "v_mfma_f32_16x16x32_bf16 v[200:203], v[32:35], v[136:139], v[200:203]\n\t"
      "v_mfma_f32_16x16x32_bf16 v[204:207], v[36:39], v[136:139], v[204:207]\n\t"
      "v_mfma_f32_16x16x32_bf16 v[208:211], v[40:43], v[136:139], v[208:211]\n\t"
      "v_mfma_f32_16x16x32_bf16 v[212:215], v[44:47], v[136:139], v[212:215]\n\t"
      "v_mfma_f32_16x16x32_bf16 v[200:203], v[48:51], v[140:143], v[200:203]\n\t"
      "v_mfma_f32_16x16x32_bf16 v[204:207], v[52:55], v[140:143], v[204:207]\n\t"
      "v_mfma_f32_16x16x32_bf16 v[208:211], v[56:59], v[140:143], v[208:211]\n\t"
      "v_mfma_f32_16x16x32_bf16 v[212:215], v[60:63], v[140:143], v[212:215]\n\t"
      "v_mfma_f32_16x16x32_bf16 v[200:203], v[64:67], v[144:147], v[200:203]\n\t"
      "v_mfma_f32_16x16x32_bf16 v[204:207], v[68:71], v[144:147], v[204:207]\n\t"
      "v_mfma_f32_16x16x32_bf16 v[208:211], v[72:75], v[144:147], v[208:211]\n\t"
      "v_mfma_f32_16x16x32_bf16 v[212:215], v[76:79], v[144:147], v[212:215]\n\t"
      "v_mfma_f32_16x16x32_bf16 v[200:203], v[80:83], v[148:151], v[200:203]\n\t"
      "v_mfma_f32_16x16x32_bf16 v[204:207], v[84:87], v[148:151], v[204:207]\n\t"
      "v_mfma_f32_16x16x32_bf16 v[208:211], v[88:91], v[148:151], v[208:211]\n\t"
      "v_mfma_f32_16x16x32_bf16 v[212:215], v[92:95], v[148:151], v[212:215]\n\t"
      "v_mfma_f32_16x16x32_bf16 v[200:203], v[96:99], v[152:155], v[200:203]\n\t"
      "v_mfma_f32_16x16x32_bf16 v[204:207], v[100:103], v[152:155], v[204:207]\n\t"
      "v_mfma_f32_16x16x32_bf16 v[208:211], v[104:107], v[152:155], v[208:211]\n\t"
      "v_mfma_f32_16x16x32_bf16 v[212:215], v[108:111], v[152:155], v[212:215]\n\t"
      "v_mfma_f32_16x16x32_bf16 v[200:203], v[112:115], v[156:159], v[200:203]\n\t"
      "v_mfma_f32_16x16x32_bf16 v[204:207], v[116:119], v[156:159], v[204:207]\n\t"
      "v_mfma_f32_16x16x32_bf16 v[208:211], v[120:123], v[156:159], v[208:211]\n\t"
      "v_mfma_f32_16x16x32_bf16 v[212:215], v[124:127], v[156:159], v[212:215]\n\t"
      "global_load_dwordx4 v[132:135], v177, s[56:57] sc1\n\t"
      "global_load_dwordx4 v[136:139], v178, s[56:57] sc1\n\t"
      "global_load_dwordx4 v[140:143], v179, s[56:57] sc1\n\t"
      "global_load_dwordx4 v[144:147], v180, s[56:57] sc1\n\t"
      "global_load_dwordx4 v[148:151], v181, s[56:57] sc1\n\t"
      "global_load_dwordx4 v[152:155], v182, s[56:57] sc1\n\t"
      "global_load_dwordx4 v[156:159], v183, s[56:57] sc1\n\t"
      "global_load_dwordx4 v[128:131], v176, s[58:59] sc1\n\t"
      "s_waitcnt vmcnt(0)\n\t"
      "s_nop 7\n\t"
      "s_branch NQ1B_%=\n\t"
      "NQ2R_%=:\n\t"
      "s_mov_b32 s51, 0\n\t"
      "NQ2L_%=:\n\t"
      "s_add_u32 s51, s51, 1\n\t"
      "s_cmp_lt_u32 s51, 262144\n\t"
      "s_cbranch_scc0 NQ2X_%=\n\t"
      "s_waitcnt vmcnt(0)\n\t"
      "s_sleep 1\n\t"
      "global_load_dwordx4 v[128:131], v176, s[56:57] sc1\n\t"
      "global_load_dwordx4 v[132:135], v177, s[56:57] sc1\n\t"
      "global_load_dwordx4 v[136:139], v178, s[56:57] sc1\n\t"
      "global_load_dwordx4 v[140:143], v179, s[56:57] sc1\n\t"
      "global_load_dwordx4 v[144:147], v180, s[56:57] sc1\n\t"
      "global_load_dwordx4 v[148:151], v181, s[56:57] sc1\n\t"
      "global_load_dwordx4 v[152:155], v182, s[56:57] sc1\n\t"
      "global_load_dwordx4 v[156:159], v183, s[56:57] sc1\n\t"
      "s_waitcnt vmcnt(0)\n\t"
      "v_max3_u32 v234, v128, v129, v130\n\t"
      "v_max3_u32 v234, v234, v131, v132\n\t"
      "v_max3_u32 v234, v234, v133, v134\n\t"
      "v_max3_u32 v234, v234, v135, v136\n\t"
      "v_max3_u32 v234, v234, v137, v138\n\t"
      "v_max3_u32 v234, v234, v139, v140\n\t"
      "v_max3_u32 v234, v234, v141, v142\n\t"
      "v_max3_u32 v234, v234, v143, v144\n\t"
      "v_max3_u32 v234, v234, v145, v146\n\t"
      "v_max3_u32 v234, v234, v147, v148\n\t"
      "v_max3_u32 v234, v234, v149, v150\n\t"
      "v_max3_u32 v234, v234, v151, v152\n\t"
      "v_max3_u32 v234, v234, v153, v154\n\t"
      "v_max3_u32 v234, v234, v155, v156\n\t"
      "v_max3_u32 v234, v234, v157, v158\n\t"
      "v_max_u32_e32 v234, v234, v159\n\t"
      "v_cmp_eq_u32_e32 vcc, -1, v234\n\t"
      "s_cbranch_vccnz NQ2L_%=\n\t"
      "NQ2X_%=:\n\t"
      "v_mfma_f32_16x16x32_bf16 v[184:187], v[0:3], v[128:131], v[236:239]\n\t"
      "v_mfma_f32_16x16x32_bf16 v[188:191], v[4:7], v[128:131], 0\n\t"
      "v_mfma_f32_16x16x32_bf16 v[192:195], v[8:11], v[128:131], 0\n\t"
      "v_mfma_f32_16x16x32_bf16 v[196:199], v[12:15], v[128:131], 0\n\t"
      "v_mfma_f32_16x16x32_bf16 v[184:187], v[16:19], v[132:135], v[184:187]\n\t"
      "v_mfma_f32_16x16x32_bf16 v[188:191], v[20:23], v[132:135], v[188:191]\n\t"
      "v_mfma_f32_16x16x32_bf16 v[192:195], v[24:27], v[132:135], v[192:195]\n\t"
      "v_mfma_f32_16x16x32_bf16 v[196:199], v[28:31], v[132:135], v[196:199]\n\t"
      "v_mfma_f32_16x16x32_bf16 v[184:187], v[32:35], v[136:139], v[184:187]\n\t"
      "v_mfma_f32_16x16x32_bf16 v[188:191], v[36:39], v[136:139], v[188:191]\n\t"
      "v_mfma_f32_16x16x32_bf16 v[192:195], v[40:43], v[136:139], v[192:195]\n\t"
      "v_mfma_f32_16x16x32_bf16 v[196:199], v[44:47], v[136:139], v[196:199]\n\t"
      "v_mfma_f32_16x16x32_bf16 v[184:187], v[48:51], v[140:143], v[184:187]\n\t"
      "v_mfma_f32_16x16x32_bf16 v[188:191], v[52:55], v[140:143], v[188:191]\n\t"
      "v_mfma_f32_16x16x32_bf16 v[192:195], v[56:59], v[140:143], v[192:195]\n\t"
      "v_mfma_f32_16x16x32_bf16 v[196:199], v[60:63], v[140:143], v[196:199]\n\t"
      "v_mfma_f32_16x16x32_bf16 v[184:187], v[64:67], v[144:147], v[184:187]\n\t"
      "v_mfma_f32_16x16x32_bf16 v[188:191], v[68:71], v[144:147], v[188:191]\n\t"
      "v_mfma_f32_16x16x32_bf16 v[192:195], v[72:75], v[144:147], v[192:195]\n\t"
      "v_mfma_f32_16x16x32_bf16 v[196:199], v[76:79], v[144:147], v[196:199]\n\t"
      "v_mfma_f32_16x16x32_bf16 v[184:187], v[80:83], v[148:151], v[184:187]\n\t"
      "v_mfma_f32_16x16x32_bf16 v[188:191], v[84:87], v[148:151], v[188:191]\n\t"
      "v_mfma_f32_16x16x32_bf16 v[192:195], v[88:91], v[148:151], v[192:195]\n\t"
      "v_mfma_f32_16x16x32_bf16 v[196:199], v[92:95], v[148:151], v[196:199]\n\t"
      "v_mfma_f32_16x16x32_bf16 v[184:187], v[96:99], v[152:155], v[184:187]\n\t"
      "v_mfma_f32_16x16x32_bf16 v[188:191], v[100:103], v[152:155], v[188:191]\n\t"
      "v_mfma_f32_16x16x32_bf16 v[192:195], v[104:107], v[152:155], v[192:195]\n\t"
      "v_mfma_f32_16x16x32_bf16 v[196:199], v[108:111], v[152:155], v[196:199]\n\t"
      "v_mfma_f32_16x16x32_bf16 v[184:187], v[112:115], v[156:159], v[184:187]\n\t"
      "v_mfma_f32_16x16x32_bf16 v[188:191], v[116:119], v[156:159], v[188:191]\n\t"
      "v_mfma_f32_16x16x32_bf16 v[192:195], v[120:123], v[156:159], v[192:195]\n\t"
      "v_mfma_f32_16x16x32_bf16 v[196:199], v[124:127], v[156:159], v[196:199]\n\t"
      "global_load_dwordx4 v[132:135], v177, s[58:59] sc1\n\t"
      "global_load_dwordx4 v[136:139], v178, s[58:59] sc1\n\t"
      "global_load_dwordx4 v[140:143], v179, s[58:59] sc1\n\t"
      "global_load_dwordx4 v[144:147], v180, s[58:59] sc1\n\t"
      "global_load_dwordx4 v[148:151], v181, s[58:59] sc1\n\t"
      "global_load_dwordx4 v[152:155], v182, s[58:59] sc1\n\t"
      "global_load_dwordx4 v[156:159], v183, s[58:59] sc1\n\t"
      "global_load_dwordx4 v[128:131], v176, s[60:61] sc1\n\t"
      "s_waitcnt vmcnt(0)\n\t"
      "s_nop 7\n\t"
      "s_branch NQ2B_%=\n\t"
      "NQ3R_%=:\n\t"
      "s_mov_b32 s51, 0\n\t"
      "NQ3L_%=:\n\t"
      "s_add_u32 s51, s51, 1\n\t"
      "s_cmp_lt_u32 s51, 262144\n\t"
      "s_cbranch_scc0 NQ3X_%=\n\t"
      "s_waitcnt vmcnt(0)\n\t"
      "s_sleep 1\n\t"
      "global_load_dwordx4 v[128:131], v176, s[58:59] sc1\n\t"
      "global_load_dwordx4 v[132:135], v177, s[58:59] sc1\n\t"
      "global_load_dwordx4 v[136:139], v178, s[58:59] sc1\n\t"
      "global_load_dwordx4 v[140:143], v179, s[58:59] sc1\n\t"
      "global_load_dwordx4 v[144:147], v180, s[58:59] sc1\n\t"
      "global_load_dwordx4 v[148:151], v181, s[58:59] sc1\n\t"
      "global_load_dwordx4 v[152:155], v182, s[58:59] sc1\n\t"
      "global_load_dwordx4 v[156:159], v183, s[58:59] sc1\n\t"
      "s_waitcnt vmcnt(0)\n\t"
      "v_max3_u32 v234, v128, v129, v130\n\t"
      "v_max3_u32 v234, v234, v131, v132\n\t"
      "v_max3_u32 v234, v234, v133, v134\n\t"
      "v_max3_u32 v234, v234, v135, v136\n\t"
      "v_max3_u32 v234, v234, v137, v138\n\t"
      "v_max3_u32 v234, v234, v139, v140\n\t"
      "v_max3_u32 v234, v234, v141, v142\n\t"
      "v_max3_u32 v234, v234, v143, v144\n\t"
      "v_max3_u32 v234, v234, v145, v146\n\t"
      "v_max3_u32 v234, v234, v147, v148\n\t"
      "v_max3_u32 v234, v234, v149, v150\n\t"
      "v_max3_u32 v234, v234, v151, v152\n\t"
      "v_max3_u32 v234, v234, v153, v154\n\t"
      "v_max3_u32 v234, v234, v155, v156\n\t"
      "v_max3_u32 v234, v234, v157, v158\n\t"
      "v_max_u32_e32 v234, v234, v159\n\t"
      "v_cmp_eq_u32_e32 vcc, -1, v234\n\t"
      "s_cbranch_vccnz NQ3L_%=\n\t"
      "NQ3X_%=:\n\t"
      "v_mfma_f32_16x16x32_bf16 v[200:203], v[0:3], v[128:131], v[236:239]\n\t"
      "v_mfma_f32_16x16x32_bf16 v[204:207], v[4:7], v[128:131], 0\n\t"
      "v_mfma_f32_16x16x32_bf16 v[208:211], v[8:11], v[128:131], 0\n\t"
      "v_mfma_f32_16x16x32_bf16 v[212:215], v[12:15], v[128:131], 0\n\t"
      "v_mfma_f32_16x16x32_bf16 v[200:203], v[16:19], v[132:135], v[200:203]\n\t"
      "v_mfma_f32_16x16x32_bf16 v[204:207], v[20:23], v[132:135], v[204:207]\n\t"
      "v_mfma_f32_16x16x32_bf16 v[208:211], v[24:27], v[132:135], v[208:211]\n\t"
      "v_mfma_f32_16x16x32_bf16 v[212:215], v[28:31], v[132:135], v[212:215]\n\t"
      "v_mfma_f32_16x16x32_bf16 v[200:203], v[32:35], v[136:139], v[200:203]\n\t"
      "v_mfma_f32_16x16x32_bf16 v[204:207], v[36:39], v[136:139], v[204:207]\n\t"
      "v_mfma_f32_16x16x32_bf16 v[208:211], v[40:43], v[136:139], v[208:211]\n\t"
      "v_mfma_f32_16x16x32_bf16 v[212:215], v[44:47], v[136:139], v[212:215]\n\t"
      "v_mfma_f32_16x16x32_bf16 v[200:203], v[48:51], v[140:143], v[200:203]\n\t"
      "v_mfma_f32_16x16x32_bf16 v[204:207], v[52:55], v[140:143], v[204:207]\n\t"
      "v_mfma_f32_16x16x32_bf16 v[208:211], v[56:59], v[140:143], v[208:211]\n\t"
      "v_mfma_f32_16x16x32_bf16 v[212:215], v[60:63], v[140:143], v[212:215]\n\t"
      "v_mfma_f32_16x16x32_bf16 v[200:203], v[64:67], v[144:147], v[200:203]\n\t"
      "v_mfma_f32_16x16x32_bf16 v[204:207], v[68:71], v[144:147], v[204:207]\n\t"
      "v_mfma_f32_16x16x32_bf16 v[208:211], v[72:75], v[144:147], v[208:211]\n\t"
      "v_mfma_f32_16x16x32_bf16 v[212:215], v[76:79], v[144:147], v[212:215]\n\t"
      "v_mfma_f32_16x16x32_bf16 v[200:203], v[80:83], v[148:151], v[200:203]\n\t"
      "v_mfma_f32_16x16x32_bf16 v[204:207], v[84:87], v[148:151], v[204:207]\n\t"
      "v_mfma_f32_16x16x32_bf16 v[208:211], v[88:91], v[148:151], v[208:211]\n\t"
      "v_mfma_f32_16x16x32_bf16 v[212:215], v[92:95], v[148:151], v[212:215]\n\t"
      "v_mfma_f32_16x16x32_bf16 v[200:203], v[96:99], v[152:155], v[200:203]\n\t"
      "v_mfma_f32_16x16x32_bf16 v[204:207], v[100:103], v[152:155], v[204:207]\n\t"
      "v_mfma_f32_16x16x32_bf16 v[208:211], v[104:107], v[152:155], v[208:211]\n\t"
      "v_mfma_f32_16x16x32_bf16 v[212:215], v[108:111], v[152:155], v[212:215]\n\t"
      "v_mfma_f32_16x16x32_bf16 v[200:203], v[112:115], v[156:159], v[200:203]\n\t"
      "v_mfma_f32_16x16x32_bf16 v[204:207], v[116:119], v[156:159], v[204:207]\n\t"
      "v_mfma_f32_16x16x32_bf16 v[208:211], v[120:123], v[156:159], v[208:211]\n\t"
      "v_mfma_f32_16x16x32_bf16 v[212:215], v[124:127], v[156:159], v[212:215]\n\t"
      "global_load_dwordx4 v[132:135], v177, s[60:61] sc1\n\t"
      "global_load_dwordx4 v[136:139], v178, s[60:61] sc1\n\t"
      "global_load_dwordx4 v[140:143], v179, s[60:61] sc1\n\t"
      "global_load_dwordx4 v[144:147], v180, s[60:61] sc1\n\t"
      "global_load_dwordx4 v[148:151], v181, s[60:61] sc1\n\t"
      "global_load_dwordx4 v[152:155], v182, s[60:61] sc1\n\t"
      "global_load_dwordx4 v[156:159], v183, s[60:61] sc1\n\t"
      "global_load_dwordx4 v[128:131], v176, s[62:63] sc1\n\t"
      "s_waitcnt vmcnt(0)\n\t"
      "s_nop 7\n\t"
      "s_branch NQ3B_%=\n\t"
      "NQ4R_%=:\n\t"
      "s_mov_b32 s51, 0\n\t"
      "NQ4L_%=:\n\t"
      "s_add_u32 s51, s51, 1\n\t"
      "s_cmp_lt_u32 s51, 262144\n\t"
      "s_cbranch_scc0 NQ4X_%=\n\t"
      "s_waitcnt vmcnt(0)\n\t"
      "s_sleep 1\n\t"
      "global_load_dwordx4 v[128:131], v176, s[60:61] sc1\n\t"
      "global_load_dwordx4 v[132:135], v177, s[60:61] sc1\n\t"
      "global_load_dwordx4 v[136:139], v178, s[60:61] sc1\n\t"
      "global_load_dwordx4 v[140:143], v179, s[60:61] sc1\n\t"
      "global_load_dwordx4 v[144:147], v180, s[60:61] sc1\n\t"
      "global_load_dwordx4 v[148:151], v181, s[60:61] sc1\n\t"
      "global_load_dwordx4 v[152:155], v182, s[60:61] sc1\n\t"
      "global_load_dwordx4 v[156:159], v183, s[60:61] sc1\n\t"
      "s_waitcnt vmcnt(0)\n\t"
      "v_max3_u32 v234, v128, v129, v130\n\t"
      "v_max3_u32 v234, v234, v131, v132\n\t"
      "v_max3_u32 v234, v234, v133, v134\n\t"
      "v_max3_u32 v234, v234, v135, v136\n\t"
      "v_max3_u32 v234, v234, v137, v138\n\t"
      "v_max3_u32 v234, v234, v139, v140\n\t"
      "v_max3_u32 v234, v234, v141, v142\n\t"
      "v_max3_u32 v234, v234, v143, v144\n\t"
      "v_max3_u32 v234, v234, v145, v146\n\t"
      "v_max3_u32 v234, v234, v147, v148\n\t"
      "v_max3_u32 v234, v234, v149, v150\n\t"
      "v_max3_u32 v234, v234, v151, v152\n\t"
      "v_max3_u32 v234, v234, v153, v154\n\t"
      "v_max3_u32 v234, v234, v155, v156\n\t"
      "v_max3_u32 v234, v234, v157, v158\n\t"
      "v_max_u32_e32 v234, v234, v159\n\t"
      "v_cmp_eq_u32_e32 vcc, -1, v234\n\t"
      "s_cbranch_vccnz NQ4L_%=\n\t"
      "NQ4X_%=:\n\t"
      "v_mfma_f32_16x16x32_bf16 v[184:187], v[0:3], v[128:131], v[236:239]\n\t"
      "v_mfma_f32_16x16x32_bf16 v[188:191], v[4:7], v[128:131], 0\n\t"
      "v_mfma_f32_16x16x32_bf16 v[192:195], v[8:11], v[128:131], 0\n\t"
      "v_mfma_f32_16x16x32_bf16 v[196:199], v[12:15], v[128:131], 0\n\t"
      "v_mfma_f32_16x16x32_bf16 v[184:187], v[16:19], v[132:135], v[184:187]\n\t"
      "v_mfma_f32_16x16x32_bf16 v[188:191], v[20:23], v[132:135], v[188:191]\n\t"
      "v_mfma_f32_16x16x32_bf16 v[192:195], v[24:27], v[132:135], v[192:195]\n\t"
      "v_mfma_f32_16x16x32_bf16 v[196:199], v[28:31], v[132:135], v[196:199]\n\t"
      "v_mfma_f32_16x16x32_bf16 v[184:187], v[32:35], v[136:139], v[184:187]\n\t"
      "v_mfma_f32_16x16x32_bf16 v[188:191], v[36:39], v[136:139], v[188:191]\n\t"
      "v_mfma_f32_16x16x32_bf16 v[192:195], v[40:43], v[136:139], v[192:195]\n\t"
      "v_mfma_f32_16x16x32_bf16 v[196:199], v[44:47], v[136:139], v[196:199]\n\t"
      "v_mfma_f32_16x16x32_bf16 v[184:187], v[48:51], v[140:143], v[184:187]\n\t"
      "v_mfma_f32_16x16x32_bf16 v[188:191], v[52:55], v[140:143], v[188:191]\n\t"
      "v_mfma_f32_16x16x32_bf16 v[192:195], v[56:59], v[140:143], v[192:195]\n\t"
      "v_mfma_f32_16x16x32_bf16 v[196:199], v[60:63], v[140:143], v[196:199]\n\t"
      "v_mfma_f32_16x16x32_bf16 v[184:187], v[64:67], v[144:147], v[184:187]\n\t"
      "v_mfma_f32_16x16x32_bf16 v[188:191], v[68:71], v[144:147], v[188:191]\n\t"
      "v_mfma_f32_16x16x32_bf16 v[192:195], v[72:75], v[144:147], v[192:195]\n\t"
      "v_mfma_f32_16x16x32_bf16 v[196:199], v[76:79], v[144:147], v[196:199]\n\t"
      "v_mfma_f32_16x16x32_bf16 v[184:187], v[80:83], v[148:151], v[184:187]\n\t"
      "v_mfma_f32_16x16x32_bf16 v[188:191], v[84:87], v[148:151], v[188:191]\n\t"
      "v_mfma_f32_16x16x32_bf16 v[192:195], v[88:91], v[148:151], v[192:195]\n\t"
      "v_mfma_f32_16x16x32_bf16 v[196:199], v[92:95], v[148:151], v[196:199]\n\t"
      "v_mfma_f32_16x16x32_bf16 v[184:187], v[96:99], v[152:155], v[184:187]\n\t"
      "v_mfma_f32_16x16x32_bf16 v[188:191], v[100:103], v[152:155], v[188:191]\n\t"
      "v_mfma_f32_16x16x32_bf16 v[192:195], v[104:107], v[152:155], v[192:195]\n\t"
      "v_mfma_f32_16x16x32_bf16 v[196:199], v[108:111], v[152:155], v[196:199]\n\t"
      "v_mfma_f32_16x16x32_bf16 v[184:187], v[112:115], v[156:159], v[184:187]\n\t"
      "v_mfma_f32_16x16x32_bf16 v[188:191], v[116:119], v[156:159], v[188:191]\n\t"
      "v_mfma_f32_16x16x32_bf16 v[192:195], v[120:123], v[156:159], v[192:195]\n\t"
      "v_mfma_f32_16x16x32_bf16 v[196:199], v[124:127], v[156:159], v[196:199]\n\t"
      "global_load_dwordx4 v[132:135], v177, s[62:63] sc1\n\t"
      "global_load_dwordx4 v[136:139], v178, s[62:63] sc1\n\t"
      "global_load_dwordx4 v[140:143], v179, s[62:63] sc1\n\t"
      "global_load_dwordx4 v[144:147], v180, s[62:63] sc1\n\t"
      "global_load_dwordx4 v[148:151], v181, s[62:63] sc1\n\t"
      "global_load_dwordx4 v[152:155], v182, s[62:63] sc1\n\t"
      "global_load_dwordx4 v[156:159], v183, s[62:63] sc1\n\t"
      "global_load_dwordx4 v[128:131], v176, s[64:65] sc1\n\t"
      "s_waitcnt vmcnt(0)\n\t"
      "s_nop 7\n\t"
      "s_branch NQ4B_%=\n\t"
      "NQ5R_%=:\n\t"
      "s_mov_b32 s51, 0\n\t"
      "NQ5L_%=:\n\t"
      "s_add_u32 s51, s51, 1\n\t"
      "s_cmp_lt_u32 s51, 262144\n\t"
      "s_cbranch_scc0 NQ5X_%=\n\t"
      "s_waitcnt vmcnt(0)\n\t"
      "s_sleep 1\n\t"
      "global_load_dwordx4 v[128:131], v176, s[62:63] sc1\n\t"
      "global_load_dwordx4 v[132:135], v177, s[62:63] sc1\n\t"
      "global_load_dwordx4 v[136:139], v178, s[62:63] sc1\n\t"
      "global_load_dwordx4 v[140:143], v179, s[62:63] sc1\n\t"
      "global_load_dwordx4 v[144:147], v180, s[62:63] sc1\n\t"
      "global_load_dwordx4 v[148:151], v181, s[62:63] sc1\n\t"
      "global_load_dwordx4 v[152:155], v182, s[62:63] sc1\n\t"
      "global_load_dwordx4 v[156:159], v183, s[62:63] sc1\n\t"
      "s_waitcnt vmcnt(0)\n\t"
      "v_max3_u32 v234, v128, v129, v130\n\t"
      "v_max3_u32 v234, v234, v131, v132\n\t"
      "v_max3_u32 v234, v234, v133, v134\n\t"
      "v_max3_u32 v234, v234, v135, v136\n\t"
      "v_max3_u32 v234, v234, v137, v138\n\t"
      "v_max3_u32 v234, v234, v139, v140\n\t"
      "v_max3_u32 v234, v234, v141, v142\n\t"
      "v_max3_u32 v234, v234, v143, v144\n\t"
      "v_max3_u32 v234, v234, v145, v146\n\t"
      "v_max3_u32 v234, v234, v147, v148\n\t"
      "v_max3_u32 v234, v234, v149, v150\n\t"
      "v_max3_u32 v234, v234, v151, v152\n\t"
      "v_max3_u32 v234, v234, v153, v154\n\t"
      "v_max3_u32 v234, v234, v155, v156\n\t"
      "v_max3_u32 v234, v234, v157, v158\n\t"
      "v_max_u32_e32 v234, v234, v159\n\t"
      "v_cmp_eq_u32_e32 vcc, -1, v234\n\t"
      "s_cbranch_vccnz NQ5L_%=\n\t"
      "NQ5X_%=:\n\t"
      "v_mfma_f32_16x16x32_bf16 v[200:203], v[0:3], v[128:131], v[236:239]\n\t"
      "v_mfma_f32_16x16x32_bf16 v[204:207], v[4:7], v[128:131], 0\n\t"
      "v_mfma_f32_16x16x32_bf16 v[208:211], v[8:11], v[128:131], 0\n\t"
      "v_mfma_f32_16x16x32_bf16 v[212:215], v[12:15], v[128:131], 0\n\t"
      "v_mfma_f32_16x16x32_bf16 v[200:203], v[16:19], v[132:135], v[200:203]\n\t"
      "v_mfma_f32_16x16x32_bf16 v[204:207], v[20:23], v[132:135], v[204:207]\n\t"
      "v_mfma_f32_16x16x32_bf16 v[208:211], v[24:27], v[132:135], v[208:211]\n\t"
      "v_mfma_f32_16x16x32_bf16 v[212:215], v[28:31], v[132:135], v[212:215]\n\t"
      "v_mfma_f32_16x16x32_bf16 v[200:203], v[32:35], v[136:139], v[200:203]\n\t"
      "v_mfma_f32_16x16x32_bf16 v[204:207], v[36:39], v[136:139], v[204:207]\n\t"
      "v_mfma_f32_16x16x32_bf16 v[208:211], v[40:43], v[136:139], v[208:211]\n\t"
      "v_mfma_f32_16x16x32_bf16 v[212:215], v[44:47], v[136:139], v[212:215]\n\t"
      "v_mfma_f32_16x16x32_bf16 v[200:203], v[48:51], v[140:143], v[200:203]\n\t"
      "v_mfma_f32_16x16x32_bf16 v[204:207], v[52:55], v[140:143], v[204:207]\n\t"
      "v_mfma_f32_16x16x32_bf16 v[208:211], v[56:59], v[140:143], v[208:211]\n\t"
      "v_mfma_f32_16x16x32_bf16 v[212:215], v[60:63], v[140:143], v[212:215]\n\t"
      "v_mfma_f32_16x16x32_bf16 v[200:203], v[64:67], v[144:147], v[200:203]\n\t"
      "v_mfma_f32_16x16x32_bf16 v[204:207], v[68:71], v[144:147], v[204:207]\n\t"
      "v_mfma_f32_16x16x32_bf16 v[208:211], v[72:75], v[144:147], v[208:211]\n\t"
      "v_mfma_f32_16x16x32_bf16 v[212:215], v[76:79], v[144:147], v[212:215]\n\t"
      "v_mfma_f32_16x16x32_bf16 v[200:203], v[80:83], v[148:151], v[200:203]\n\t"
      "v_mfma_f32_16x16x32_bf16 v[204:207], v[84:87], v[148:151], v[204:207]\n\t"
      "v_mfma_f32_16x16x32_bf16 v[208:211], v[88:91], v[148:151], v[208:211]\n\t"
      "v_mfma_f32_16x16x32_bf16 v[212:215], v[92:95], v[148:151], v[212:215]\n\t"
      "v_mfma_f32_16x16x32_bf16 v[200:203], v[96:99], v[152:155], v[200:203]\n\t"
      "v_mfma_f32_16x16x32_bf16 v[204:207], v[100:103], v[152:155], v[204:207]\n\t"
      "v_mfma_f32_16x16x32_bf16 v[208:211], v[104:107], v[152:155], v[208:211]\n\t"
      "v_mfma_f32_16x16x32_bf16 v[212:215], v[108:111], v[152:155], v[212:215]\n\t"
      "v_mfma_f32_16x16x32_bf16 v[200:203], v[112:115], v[156:159], v[200:203]\n\t"
      "v_mfma_f32_16x16x32_bf16 v[204:207], v[116:119], v[156:159], v[204:207]\n\t"
      "v_mfma_f32_16x16x32_bf16 v[208:211], v[120:123], v[156:159], v[208:211]\n\t"
      "v_mfma_f32_16x16x32_bf16 v[212:215], v[124:127], v[156:159], v[212:215]\n\t"
      "global_load_dwordx4 v[132:135], v177, s[64:65] sc1\n\t"
      "global_load_dwordx4 v[136:139], v178, s[64:65] sc1\n\t"
      "global_load_dwordx4 v[140:143], v179, s[64:65] sc1\n\t"
      "global_load_dwordx4 v[144:147], v180, s[64:65] sc1\n\t"
      "global_load_dwordx4 v[148:151], v181, s[64:65] sc1\n\t"
      "global_load_dwordx4 v[152:155], v182, s[64:65] sc1\n\t"
      "global_load_dwordx4 v[156:159], v183, s[64:65] sc1\n\t"
      "s_waitcnt vmcnt(0)\n\t"
      "s_nop 7\n\t"
      "s_branch NQ5B_%=\n\t"
      "NQ6R_%=:\n\t"
      "s_mov_b32 s51, 0\n\t"
      "NQ6L_%=:\n\t"
      "s_add_u32 s51, s51, 1\n\t"
      "s_cmp_lt_u32 s51, 262144\n\t"
      "s_cbranch_scc0 NQ6X_%=\n\t"
      "s_waitcnt vmcnt(0)\n\t"
      "s_sleep 1\n\t"
      "global_load_dwordx4 v[128:131], v176, s[64:65] sc1\n\t"
      "global_load_dwordx4 v[132:135], v177, s[64:65] sc1\n\t"
      "global_load_dwordx4 v[136:139], v178, s[64:65] sc1\n\t"
      "global_load_dwordx4 v[140:143], v179, s[64:65] sc1\n\t"
      "global_load_dwordx4 v[144:147], v180, s[64:65] sc1\n\t"
      "global_load_dwordx4 v[148:151], v181, s[64:65] sc1\n\t"
      "global_load_dwordx4 v[152:155], v182, s[64:65] sc1\n\t"
      "global_load_dwordx4 v[156:159], v183, s[64:65] sc1\n\t"
      "s_waitcnt vmcnt(0)\n\t"
      "v_max3_u32 v234, v128, v129, v130\n\t"
      "v_max3_u32 v234, v234, v131, v132\n\t"
      "v_max3_u32 v234, v234, v133, v134\n\t"
      "v_max3_u32 v234, v234, v135, v136\n\t"
      "v_max3_u32 v234, v234, v137, v138\n\t"
      "v_max3_u32 v234, v234, v139, v140\n\t"
      "v_max3_u32 v234, v234, v141, v142\n\t"
      "v_max3_u32 v234, v234, v143, v144\n\t"
      "v_max3_u32 v234, v234, v145, v146\n\t"
      "v_max3_u32 v234, v234, v147, v148\n\t"
      "v_max3_u32 v234, v234, v149, v150\n\t"
      "v_max3_u32 v234, v234, v151, v152\n\t"
      "v_max3_u32 v234, v234, v153, v154\n\t"
      "v_max3_u32 v234, v234, v155, v156\n\t"
      "v_max3_u32 v234, v234, v157, v158\n\t"
      "v_max_u32_e32 v234, v234, v159\n\t"
      "v_cmp_eq_u32_e32 vcc, -1, v234\n\t"
      "s_cbranch_vccnz NQ6L_%=\n\t"
      "NQ6X_%=:\n\t"
      "v_mfma_f32_16x16x32_bf16 v[184:187], v[0:3], v[128:131], v[236:239]\n\t"
      "v_mfma_f32_16x16x32_bf16 v[188:191], v[4:7], v[128:131], 0\n\t"
      "v_mfma_f32_16x16x32_bf16 v[192:195], v[8:11], v[128:131], 0\n\t"
      "v_mfma_f32_16x16x32_bf16 v[196:199], v[12:15], v[128:131], 0\n\t"
      "v_mfma_f32_16x16x32_bf16 v[184:187], v[16:19], v[132:135], v[184:187]\n\t"
      "v_mfma_f32_16x16x32_bf16 v[188:191], v[20:23], v[132:135], v[188:191]\n\t"
      "v_mfma_f32_16x16x32_bf16 v[192:195], v[24:27], v[132:135], v[192:195]\n\t"
      "v_mfma_f32_16x16x32_bf16 v[196:199], v[28:31], v[132:135], v[196:199]\n\t"
      "v_mfma_f32_16x16x32_bf16 v[184:187], v[32:35], v[136:139], v[184:187]\n\t"
      "v_mfma_f32_16x16x32_bf16 v[188:191], v[36:39], v[136:139], v[188:191]\n\t"
      "v_mfma_f32_16x16x32_bf16 v[192:195], v[40:43], v[136:139], v[192:195]\n\t"
      "v_mfma_f32_16x16x32_bf16 v[196:199], v[44:47], v[136:139], v[196:199]\n\t"
      "v_mfma_f32_16x16x32_bf16 v[184:187], v[48:51], v[140:143], v[184:187]\n\t"
      "v_mfma_f32_16x16x32_bf16 v[188:191], v[52:55], v[140:143], v[188:191]\n\t"
      "v_mfma_f32_16x16x32_bf16 v[192:195], v[56:59], v[140:143], v[192:195]\n\t"
      "v_mfma_f32_16x16x32_bf16 v[196:199], v[60:63], v[140:143], v[196:199]\n\t"
      "v_mfma_f32_16x16x32_bf16 v[184:187], v[64:67], v[144:147], v[184:187]\n\t"
      "v_mfma_f32_16x16x32_bf16 v[188:191], v[68:71], v[144:147], v[188:191]\n\t"
      "v_mfma_f32_16x16x32_bf16 v[192:195], v[72:75], v[144:147], v[192:195]\n\t"
      "v_mfma_f32_16x16x32_bf16 v[196:199], v[76:79], v[144:147], v[196:199]\n\t"
      "v_mfma_f32_16x16x32_bf16 v[184:187], v[80:83], v[148:151], v[184:187]\n\t"
      "v_mfma_f32_16x16x32_bf16 v[188:191], v[84:87], v[148:151], v[188:191]\n\t"
      "v_mfma_f32_16x16x32_bf16 v[192:195], v[88:91], v[148:151], v[192:195]\n\t"
      "v_mfma_f32_16x16x32_bf16 v[196:199], v[92:95], v[148:151], v[196:199]\n\t"
      "v_mfma_f32_16x16x32_bf16 v[184:187], v[96:99], v[152:155], v[184:187]\n\t"
      "v_mfma_f32_16x16x32_bf16 v[188:191], v[100:103], v[152:155], v[188:191]\n\t"
      "v_mfma_f32_16x16x32_bf16 v[192:195], v[104:107], v[152:155], v[192:195]\n\t"
      "v_mfma_f32_16x16x32_bf16 v[196:199], v[108:111], v[152:155], v[196:199]\n\t"
      "v_mfma_f32_16x16x32_bf16 v[184:187], v[112:115], v[156:159], v[184:187]\n\t"
      "v_mfma_f32_16x16x32_bf16 v[188:191], v[116:119], v[156:159], v[188:191]\n\t"
      "v_mfma_f32_16x16x32_bf16 v[192:195], v[120:123], v[156:159], v[192:195]\n\t"
      "v_mfma_f32_16x16x32_bf16 v[196:199], v[124:127], v[156:159], v[196:199]\n\t"
      "s_waitcnt vmcnt(0)\n\t"
      "s_nop 7\n\t"
      "s_branch NQ6B_%=\n\t"
      "FP0R_%=:\n\t"
      "s_mov_b32 s51, 0\n\t"
      "FP0L_%=:\n\t"
      "s_add_u32 s51, s51, 1\n\t"
      "s_cmp_lt_u32 s51, 262144\n\t"
      "s_cbranch_scc0 FP0X_%=\n\t"
      "s_waitcnt vmcnt(0)\n\t"
      "s_sleep 1\n\t"
      "global_load_dwordx4 v[128:131], v176, s[52:53] sc1\n\t"
      "global_load_dwordx4 v[132:135], v177, s[52:53] sc1\n\t"
      "global_load_dwordx4 v[136:139], v178, s[52:53] sc1\n\t"
      "global_load_dwordx4 v[140:143], v179, s[52:53] sc1\n\t"
      "global_load_dwordx4 v[144:147], v180, s[52:53] sc1\n\t"
      "global_load_dwordx4 v[148:151], v181, s[52:53] sc1\n\t"
      "global_load_dwordx4 v[152:155], v182, s[52:53] sc1\n\t"
      "global_load_dwordx4 v[156:159], v183, s[52:53] sc1\n\t"
      "s_waitcnt vmcnt(7)\n\t"
      "v_max3_u32 v234, v128, v129, v130\n\t"
      "v_max_u32_e32 v234, v234, v131\n\t"
      "v_cmp_eq_u32_e32 vcc, -1, v234\n\t"
      "s_cbranch_vccnz FP0L_%=\n\t"
      "FP0X_%=:\n\t"
      "s_branch FP0B_%=\n\t"
      "FP1R_%=:\n\t"
      "s_mov_b32 s51, 0\n\t"
      "FP1L_%=:\n\t"
      "s_add_u32 s51, s51, 1\n\t"
      "s_cmp_lt_u32 s51, 262144\n\t"
      "s_cbranch_scc0 FP1X_%=\n\t"
      "s_waitcnt vmcnt(0)\n\t"
      "s_sleep 1\n\t"
      "global_load_dwordx4 v[132:135], v177, s[52:53] sc1\n\t"
      "global_load_dwordx4 v[136:139], v178, s[52:53] sc1\n\t"
      "global_load_dwordx4 v[140:143], v179, s[52:53] sc1\n\t"
      "global_load_dwordx4 v[144:147], v180, s[52:53] sc1\n\t"
      "global_load_dwordx4 v[148:151], v181, s[52:53] sc1\n\t"
      "global_load_dwordx4 v[152:155], v182, s[52:53] sc1\n\t"
      "global_load_dwordx4 v[156:159], v183, s[52:53] sc1\n\t"
      "global_load_dwordx4 v[128:131], v176, s[54:55] sc1\n\t"
      "s_waitcnt vmcnt(7)\n\t"
      "v_max3_u32 v234, v132, v133, v134\n\t"
      "v_max_u32_e32 v234, v234, v135\n\t"
      "v_cmp_eq_u32_e32 vcc, -1, v234\n\t"
      "s_cbranch_vccnz FP1L_%=\n\t"
      "FP1X_%=:\n\t"
      "s_branch FP1B_%=\n\t"
      "FP2R_%=:\n\t"
      "s_mov_b32 s51, 0\n\t"
      "FP2L_%=:\n\t"
      "s_add_u32 s51, s51, 1\n\t"
      "s_cmp_lt_u32 s51, 262144\n\t"
      "s_cbranch_scc0 FP2X_%=\n\t"
      "s_waitcnt vmcnt(0)\n\t"
      "s_sleep 1\n\t"
      "global_load_dwordx4 v[136:139], v178, s[52:53] sc1\n\t"
      "global_load_dwordx4 v[140:143], v179, s[52:53] sc1\n\t"
      "global_load_dwordx4 v[144:147], v180, s[52:53] sc1\n\t"
      "global_load_dwordx4 v[148:151], v181, s[52:53] sc1\n\t"
      "global_load_dwordx4 v[152:155], v182, s[52:53] sc1\n\t"
      "global_load_dwordx4 v[156:159], v183, s[52:53] sc1\n\t"
      "global_load_dwordx4 v[128:131], v176, s[54:55] sc1\n\t"
      "global_load_dwordx4 v[132:135], v177, s[54:55] sc1\n\t"
      "s_waitcnt vmcnt(7)\n\t"
      "v_max3_u32 v234, v136, v137, v138\n\t"
      "v_max_u32_e32 v234, v234, v139\n\t"
      "v_cmp_eq_u32_e32 vcc, -1, v234\n\t"
      "s_cbranch_vccnz FP2L_%=\n\t"
      "FP2X_%=:\n\t"
      "s_branch FP2B_%=\n\t"
      "FP3R_%=:\n\t"
      "s_mov_b32 s51, 0\n\t"
      "FP3L_%=:\n\t"
      "s_add_u32 s51, s51, 1\n\t"
      "s_cmp_lt_u32 s51, 262144\n\t"
      "s_cbranch_scc0 FP3X_%=\n\t"
      "s_waitcnt vmcnt(0)\n\t"
      "s_sleep 1\n\t"
      "global_load_dwordx4 v[140:143], v179, s[52:53] sc1\n\t"
      "global_load_dwordx4 v[144:147], v180, s[52:53] sc1\n\t"
      "global_load_dwordx4 v[148:151], v181, s[52:53] sc1\n\t"
      "global_load_dwordx4 v[152:155], v182, s[52:53] sc1\n\t"
      "global_load_dwordx4 v[156:159], v183, s[52:53] sc1\n\t"
      "global_load_dwordx4 v[128:131], v176, s[54:55] sc1\n\t"
      "global_load_dwordx4 v[132:135], v177, s[54:55] sc1\n\t"
      "global_load_dwordx4 v[136:139], v178, s[54:55] sc1\n\t"
      "s_waitcnt vmcnt(7)\n\t"
      "v_max3_u32 v234, v140, v141, v142\n\t"
      "v_max_u32_e32 v234, v234, v143\n\t"
      "v_cmp_eq_u32_e32 vcc, -1, v234\n\t"
      "s_cbranch_vccnz FP3L_%=\n\t"
      "FP3X_%=:\n\t"
      "s_branch FP3B_%=\n\t"
      "FP4R_%=:\n\t"
      "s_mov_b32 s51, 0\n\t"
      "FP4L_%=:\n\t"
      "s_add_u32 s51, s51, 1\n\t"
      "s_cmp_lt_u32 s51, 262144\n\t"
      "s_cbranch_scc0 FP4X_%=\n\t"
      "s_waitcnt vmcnt(0)\n\t"
      "s_sleep 1\n\t"
      "global_load_dwordx4 v[144:147], v180, s[52:53] sc1\n\t"
      "global_load_dwordx4 v[148:151], v181, s[52:53] sc1\n\t"
      "global_load_dwordx4 v[152:155], v182, s[52:53] sc1\n\t"
      "global_load_dwordx4 v[156:159], v183, s[52:53] sc1\n\t"
      "global_load_dwordx4 v[128:131], v176, s[54:55] sc1\n\t"
      "global_load_dwordx4 v[132:135], v177, s[54:55] sc1\n\t"
      "global_load_dwordx4 v[136:139], v178, s[54:55] sc1\n\t"
      "global_load_dwordx4 v[140:143], v179, s[54:55] sc1\n\t"
      "s_waitcnt vmcnt(7)\n\t"
      "v_max3_u32 v234, v144, v145, v146\n\t"
      "v_max_u32_e32 v234, v234, v147\n\t"
      "v_cmp_eq_u32_e32 vcc, -1, v234\n\t"
      "s_cbranch_vccnz FP4L_%=\n\t"
      "FP4X_%=:\n\t"
      "s_branch FP4B_%=\n\t"
      "FP5R_%=:\n\t"
      "s_mov_b32 s51, 0\n\t"
      "FP5L_%=:\n\t"
      "s_add_u32 s51, s51, 1\n\t"
      "s_cmp_lt_u32 s51, 262144\n\t"
      "s_cbranch_scc0 FP5X_%=\n\t"
      "s_waitcnt vmcnt(0)\n\t"
      "s_sleep 1\n\t"
      "global_load_dwordx4 v[148:151], v181, s[52:53] sc1\n\t"
      "global_load_dwordx4 v[152:155], v182, s[52:53] sc1\n\t"
      "global_load_dwordx4 v[156:159], v183, s[52:53] sc1\n\t"
      "global_load_dwordx4 v[128:131], v176, s[54:55] sc1\n\t"
      "global_load_dwordx4 v[132:135], v177, s[54:55] sc1\n\t"
      "global_load_dwordx4 v[136:139], v178, s[54:55] sc1\n\t"
      "global_load_dwordx4 v[140:143], v179, s[54:55] sc1\n\t"
      "global_load_dwordx4 v[144:147], v180, s[54:55] sc1\n\t"
      "s_waitcnt vmcnt(7)\n\t"
      "v_max3_u32 v234, v148, v149, v150\n\t"
      "v_max_u32_e32 v234, v234, v151\n\t"
      "v_cmp_eq_u32_e32 vcc, -1, v234\n\t"
      "s_cbranch_vccnz FP5L_%=\n\t"
      "FP5X_%=:\n\t"
      "s_branch FP5B_%=\n\t"
      "FP6R_%=:\n\t"
      "s_mov_b32 s51, 0\n\t"
      "FP6L_%=:\n\t"
      "s_add_u32 s51, s51, 1\n\t"
      "s_cmp_lt_u32 s51, 262144\n\t"
      "s_cbranch_scc0 FP6X_%=\n\t"
      "s_waitcnt vmcnt(0)\n\t"
      "s_sleep 1\n\t"
      "global_load_dwordx4 v[152:155], v182, s[52:53] sc1\n\t"
      "global_load_dwordx4 v[156:159], v183, s[52:53] sc1\n\t"
      "global_load_dwordx4 v[128:131], v176, s[54:55] sc1\n\t"
      "global_load_dwordx4 v[132:135], v177, s[54:55] sc1\n\t"
      "global_load_dwordx4 v[136:139], v178, s[54:55] sc1\n\t"
      "global_load_dwordx4 v[140:143], v179, s[54:55] sc1\n\t"
      "global_load_dwordx4 v[144:147], v180, s[54:55] sc1\n\t"
      "global_load_dwordx4 v[148:151], v181, s[54:55] sc1\n\t"
      "s_waitcnt vmcnt(7)\n\t"
      "v_max3_u32 v234, v152, v153, v154\n\t"
      "v_max_u32_e32 v234, v234, v155\n\t"
      "v_cmp_eq_u32_e32 vcc, -1, v234\n\t"
      "s_cbranch_vccnz FP6L_%=\n\t"
      "FP6X_%=:\n\t"
      "s_branch FP6B_%=\n\t"
      "FP7R_%=:\n\t"
      "s_mov_b32 s51, 0\n\t"
      "FP7L_%=:\n\t"
      "s_add_u32 s51, s51, 1\n\t"
      "s_cmp_lt_u32 s51, 262144\n\t"
      "s_cbranch_scc0 FP7X_%=\n\t"
      "s_waitcnt vmcnt(0)\n\t"
      "s_sleep 1\n\t"
      "global_load_dwordx4 v[156:159], v183, s[52:53] sc1\n\t"
      "global_load_dwordx4 v[128:131], v176, s[54:55] sc1\n\t"
      "global_load_dwordx4 v[132:135], v177, s[54:55] sc1\n\t"
      "global_load_dwordx4 v[136:139], v178, s[54:55] sc1\n\t"
      "global_load_dwordx4 v[140:143], v179, s[54:55] sc1\n\t"
      "global_load_dwordx4 v[144:147], v180, s[54:55] sc1\n\t"
      "global_load_dwordx4 v[148:151], v181, s[54:55] sc1\n\t"
      "global_load_dwordx4 v[152:155], v182, s[54:55] sc1\n\t"
      "s_waitcnt vmcnt(7)\n\t"
      "v_max3_u32 v234, v156, v157, v158\n\t"
      "v_max_u32_e32 v234, v234, v159\n\t"
      "v_cmp_eq_u32_e32 vcc, -1, v234\n\t"
      "s_cbranch_vccnz FP7L_%=\n\t"
      "FP7X_%=:\n\t"
      "s_branch FP7B_%=\n\t"
      "NP1R_%=:\n\t"
      "s_mov_b32 s51, 0\n\t"
      "NP1L_%=:\n\t"
      "s_add_u32 s51, s51, 1\n\t"
      "s_cmp_lt_u32 s51, 262144\n\t"
      "s_cbranch_scc0 NP1X_%=\n\t"
      "s_waitcnt vmcnt(0)\n\t"
      "s_sleep 1\n\t"
      "global_load_dwordx4 v[128:131], v176, s[54:55] sc1\n\t"
      "global_load_dwordx4 v[132:135], v177, s[54:55] sc1\n\t"
      "global_load_dwordx4 v[136:139], v178, s[54:55] sc1\n\t"
      "global_load_dwordx4 v[140:143], v179, s[54:55] sc1\n\t"
      "global_load_dwordx4 v[144:147], v180, s[54:55] sc1\n\t"
      "global_load_dwordx4 v[148:151], v181, s[54:55] sc1\n\t"
      "global_load_dwordx4 v[152:155], v182, s[54:55] sc1\n\t"
      "global_load_dwordx4 v[156:159], v183, s[54:55] sc1\n\t"
      "s_waitcnt vmcnt(0)\n\t"
      "v_max3_u32 v234, v128, v129, v130\n\t"
      "v_max3_u32 v234, v234, v131, v132\n\t"
      "v_max3_u32 v234, v234, v133, v134\n\t"
      "v_max3_u32 v234, v234, v135, v136\n\t"
      "v_max3_u32 v234, v234, v137, v138\n\t"
      "v_max3_u32 v234, v234, v139, v140\n\t"
      "v_max3_u32 v234, v234, v141, v142\n\t"
      "v_max3_u32 v234, v234, v143, v144\n\t"
      "v_max3_u32 v234, v234, v145, v146\n\t"
      "v_max3_u32 v234, v234, v147, v148\n\t"
      "v_max3_u32 v234, v234, v149, v150\n\t"
      "v_max3_u32 v234, v234, v151, v152\n\t"
      "v_max3_u32 v234, v234, v153, v154\n\t"
      "v_max3_u32 v234, v234, v155, v156\n\t"
      "v_max3_u32 v234, v234, v157, v158\n\t"
      "v_max_u32_e32 v234, v234, v159\n\t"
      "v_cmp_eq_u32_e32 vcc, -1, v234\n\t"
      "s_cbranch_vccnz NP1L_%=\n\t"
      "NP1X_%=:\n\t"
      "v_mfma_f32_16x16x32_bf16 v[200:203], v[0:3], v[128:131], 0\n\t"
      "v_mfma_f32_16x16x32_bf16 v[204:207], v[4:7], v[128:131], 0\n\t"
      "v_mfma_f32_16x16x32_bf16 v[208:211], v[8:11], v[128:131], 0\n\t"
      "v_mfma_f32_16x16x32_bf16 v[212:215], v[12:15], v[128:131], 0\n\t"
      "v_mfma_f32_16x16x32_bf16 v[200:203], v[16:19], v[132:135], v[200:203]\n\t"
      "v_mfma_f32_16x16x32_bf16 v[204:207], v[20:23], v[132:135], v[204:207]\n\t"
      "v_mfma_f32_16x16x32_bf16 v[208:211], v[24:27], v[132:135], v[208:211]\n\t"
      "v_mfma_f32_16x16x32_bf16 v[212:215], v[28:31], v[132:135], v[212:215]\n\t"
      "v_mfma_f32_16x16x32_bf16 v[200:203], v[32:35], v[136:139], v[200:203]\n\t"
      "v_mfma_f32_16x16x32_bf16 v[204:207], v[36:39], v[136:139], v[204:207]\n\t"
      "v_mfma_f32_16x16x32_bf16 v[208:211], v[40:43], v[136:139], v[208:211]\n\t"
      "v_mfma_f32_16x16x32_bf16 v[212:215], v[44:47], v[136:139], v[212:215]\n\t"
      "v_mfma_f32_16x16x32_bf16 v[200:203], v[48:51], v[140:143], v[200:203]\n\t"
      "v_mfma_f32_16x16x32_bf16 v[204:207], v[52:55], v[140:143], v[204:207]\n\t"
      "v_mfma_f32_16x16x32_bf16 v[208:211], v[56:59], v[140:143], v[208:211]\n\t"
      "v_mfma_f32_16x16x32_bf16 v[212:215], v[60:63], v[140:143], v[212:215]\n\t"
      "v_mfma_f32_16x16x32_bf16 v[200:203], v[64:67], v[144:147], v[200:203]\n\t"
      "v_mfma_f32_16x16x32_bf16 v[204:207], v[68:71], v[144:147], v[204:207]\n\t"
      "v_mfma_f32_16x16x32_bf16 v[208:211], v[72:75], v[144:147], v[208:211]\n\t"
      "v_mfma_f32_16x16x32_bf16 v[212:215], v[76:79], v[144:147], v[212:215]\n\t"
      "v_mfma_f32_16x16x32_bf16 v[200:203], v[80:83], v[148:151], v[200:203]\n\t"
      "v_mfma_f32_16x16x32_bf16 v[204:207], v[84:87], v[148:151], v[204:207]\n\t"
      "v_mfma_f32_16x16x32_bf16 v[208:211], v[88:91], v[148:151], v[208:211]\n\t"
      "v_mfma_f32_16x16x32_bf16 v[212:215], v[92:95], v[148:151], v[212:215]\n\t"
      "v_mfma_f32_16x16x32_bf16 v[200:203], v[96:99], v[152:155], v[200:203]\n\t"
      "v_mfma_f32_16x16x32_bf16 v[204:207], v[100:103], v[152:155], v[204:207]\n\t"
      "v_mfma_f32_16x16x32_bf16 v[208:211], v[104:107], v[152:155], v[208:211]\n\t"
      "v_mfma_f32_16x16x32_bf16 v[212:215], v[108:111], v[152:155], v[212:215]\n\t"
      "v_mfma_f32_16x16x32_bf16 v[200:203], v[112:115], v[156:159], v[200:203]\n\t"
      "v_mfma_f32_16x16x32_bf16 v[204:207], v[116:119], v[156:159], v[204:207]\n\t"
      "v_mfma_f32_16x16x32_bf16 v[208:211], v[120:123], v[156:159], v[208:211]\n\t"
      "v_mfma_f32_16x16x32_bf16 v[212:215], v[124:127], v[156:159], v[212:215]\n\t"
      "global_load_dwordx4 v[128:131], v176, s[56:57] sc1\n\t"
      "global_load_dwordx4 v[132:135], v177, s[56:57] sc1\n\t"
      "global_load_dwordx4 v[136:139], v178, s[56:57] sc1\n\t"
      "global_load_dwordx4 v[140:143], v179, s[56:57] sc1\n\t"
      "global_load_dwordx4 v[144:147], v180, s[56:57] sc1\n\t"
      "global_load_dwordx4 v[148:151], v181, s[56:57] sc1\n\t"
      "global_load_dwordx4 v[152:155], v182, s[56:57] sc1\n\t"
      "global_load_dwordx4 v[156:159], v183, s[56:57] sc1\n\t"
      "s_waitcnt vmcnt(0)\n\t"
      "s_nop 7\n\t"
      "s_branch NP1B_%=\n\t"
      "NP2R_%=:\n\t"
      "s_mov_b32 s51, 0\n\t"
      "NP2L_%=:\n\t"
      "s_add_u32 s51, s51, 1\n\t"
      "s_cmp_lt_u32 s51, 262144\n\t"
      "s_cbranch_scc0 NP2X_%=\n\t"
      "s_waitcnt vmcnt(0)\n\t"
      "s_sleep 1\n\t"
      "global_load_dwordx4 v[128:131], v176, s[56:57] sc1\n\t"
      "global_load_dwordx4 v[132:135], v177, s[56:57] sc1\n\t"
      "global_load_dwordx4 v[136:139], v178, s[56:57] sc1\n\t"
      "global_load_dwordx4 v[140:143], v179, s[56:57] sc1\n\t"
      "global_load_dwordx4 v[144:147], v180, s[56:57] sc1\n\t"
      "global_load_dwordx4 v[148:151], v181, s[56:57] sc1\n\t"
      "global_load_dwordx4 v[152:155], v182, s[56:57] sc1\n\t"
      "global_load_dwordx4 v[156:159], v183, s[56:57] sc1\n\t"
      "s_waitcnt vmcnt(0)\n\t"
      "v_max3_u32 v234, v128, v129, v130\n\t"
      "v_max3_u32 v234, v234, v131, v132\n\t"
      "v_max3_u32 v234, v234, v133, v134\n\t"
      "v_max3_u32 v234, v234, v135, v136\n\t"
      "v_max3_u32 v234, v234, v137, v138\n\t"
      "v_max3_u32 v234, v234, v139, v140\n\t"
      "v_max3_u32 v234, v234, v141, v142\n\t"
      "v_max3_u32 v234, v234, v143, v144\n\t"
      "v_max3_u32 v234, v234, v145, v146\n\t"
      "v_max3_u32 v234, v234, v147, v148\n\t"
      "v_max3_u32 v234, v234, v149, v150\n\t"
      "v_max3_u32 v234, v234, v151, v152\n\t"
      "v_max3_u32 v234, v234, v153, v154\n\t"
      "v_max3_u32 v234, v234, v155, v156\n\t"
      "v_max3_u32 v234, v234, v157, v158\n\t"
      "v_max_u32_e32 v234, v234, v159\n\t"
      "v_cmp_eq_u32_e32 vcc, -1, v234\n\t"
      "s_cbranch_vccnz NP2L_%=\n\t"
      "NP2X_%=:\n\t"
      "v_mfma_f32_16x16x32_bf16 v[184:187], v[0:3], v[128:131], 0\n\t"
      "v_mfma_f32_16x16x32_bf16 v[188:191], v[4:7], v[128:131], 0\n\t"
      "v_mfma_f32_16x16x32_bf16 v[192:195], v[8:11], v[128:131], 0\n\t"
      "v_mfma_f32_16x16x32_bf16 v[196:199], v[12:15], v[128:131], 0\n\t"
      "v_mfma_f32_16x16x32_bf16 v[184:187], v[16:19], v[132:135], v[184:187]\n\t"
      "v_mfma_f32_16x16x32_bf16 v[188:191], v[20:23], v[132:135], v[188:191]\n\t"
      "v_mfma_f32_16x16x32_bf16 v[192:195], v[24:27], v[132:135], v[192:195]\n\t"
      "v_mfma_f32_16x16x32_bf16 v[196:199], v[28:31], v[132:135], v[196:199]\n\t"
      "v_mfma_f32_16x16x32_bf16 v[184:187], v[32:35], v[136:139], v[184:187]\n\t"
      "v_mfma_f32_16x16x32_bf16 v[188:191], v[36:39], v[136:139], v[188:191]\n\t"
      "v_mfma_f32_16x16x32_bf16 v[192:195], v[40:43], v[136:139], v[192:195]\n\t"
      "v_mfma_f32_16x16x32_bf16 v[196:199], v[44:47], v[136:139], v[196:199]\n\t"
      "v_mfma_f32_16x16x32_bf16 v[184:187], v[48:51], v[140:143], v[184:187]\n\t"
      "v_mfma_f32_16x16x32_bf16 v[188:191], v[52:55], v[140:143], v[188:191]\n\t"
      "v_mfma_f32_16x16x32_bf16 v[192:195], v[56:59], v[140:143], v[192:195]\n\t"
      "v_mfma_f32_16x16x32_bf16 v[196:199], v[60:63], v[140:143], v[196:199]\n\t"
      "v_mfma_f32_16x16x32_bf16 v[184:187], v[64:67], v[144:147], v[184:187]\n\t"
      "v_mfma_f32_16x16x32_bf16 v[188:191], v[68:71], v[144:147], v[188:191]\n\t"
      "v_mfma_f32_16x16x32_bf16 v[192:195], v[72:75], v[144:147], v[192:195]\n\t"
      "v_mfma_f32_16x16x32_bf16 v[196:199], v[76:79], v[144:147], v[196:199]\n\t"
      "v_mfma_f32_16x16x32_bf16 v[184:187], v[80:83], v[148:151], v[184:187]\n\t"
      "v_mfma_f32_16x16x32_bf16 v[188:191], v[84:87], v[148:151], v[188:191]\n\t"
      "v_mfma_f32_16x16x32_bf16 v[192:195], v[88:91], v[148:151], v[192:195]\n\t"
      "v_mfma_f32_16x16x32_bf16 v[196:199], v[92:95], v[148:151], v[196:199]\n\t"
      "v_mfma_f32_16x16x32_bf16 v[184:187], v[96:99], v[152:155], v[184:187]\n\t"
      "v_mfma_f32_16x16x32_bf16 v[188:191], v[100:103], v[152:155], v[188:191]\n\t"
      "v_mfma_f32_16x16x32_bf16 v[192:195], v[104:107], v[152:155], v[192:195]\n\t"
      "v_mfma_f32_16x16x32_bf16 v[196:199], v[108:111], v[152:155], v[196:199]\n\t"
      "v_mfma_f32_16x16x32_bf16 v[184:187], v[112:115], v[156:159], v[184:187]\n\t"
      "v_mfma_f32_16x16x32_bf16 v[188:191], v[116:119], v[156:159], v[188:191]\n\t"
      "v_mfma_f32_16x16x32_bf16 v[192:195], v[120:123], v[156:159], v[192:195]\n\t"
      "v_mfma_f32_16x16x32_bf16 v[196:199], v[124:127], v[156:159], v[196:199]\n\t"
      "global_load_dwordx4 v[128:131], v176, s[58:59] sc1\n\t"
      "global_load_dwordx4 v[132:135], v177, s[58:59] sc1\n\t"
      "global_load_dwordx4 v[136:139], v178, s[58:59] sc1\n\t"
      "global_load_dwordx4 v[140:143], v179, s[58:59] sc1\n\t"
      "global_load_dwordx4 v[144:147], v180, s[58:59] sc1\n\t"
      "global_load_dwordx4 v[148:151], v181, s[58:59] sc1\n\t"
      "global_load_dwordx4 v[152:155], v182, s[58:59] sc1\n\t"
      "global_load_dwordx4 v[156:159], v183, s[58:59] sc1\n\t"
      "s_waitcnt vmcnt(0)\n\t"
      "s_nop 7\n\t"
      "s_branch NP2B_%=\n\t"
      "NP3R_%=:\n\t"
      "s_mov_b32 s51, 0\n\t"
      "NP3L_%=:\n\t"
      "s_add_u32 s51, s51, 1\n\t"
      "s_cmp_lt_u32 s51, 262144\n\t"
      "s_cbranch_scc0 NP3X_%=\n\t"
      "s_waitcnt vmcnt(0)\n\t"
      "s_sleep 1\n\t"
      "global_load_dwordx4 v[128:131], v176, s[58:59] sc1\n\t"
      "global_load_dwordx4 v[132:135], v177, s[58:59] sc1\n\t"
      "global_load_dwordx4 v[136:139], v178, s[58:59] sc1\n\t"
      "global_load_dwordx4 v[140:143], v179, s[58:59] sc1\n\t"
      "global_load_dwordx4 v[144:147], v180, s[58:59] sc1\n\t"
      "global_load_dwordx4 v[148:151], v181, s[58:59] sc1\n\t"
      "global_load_dwordx4 v[152:155], v182, s[58:59] sc1\n\t"
      "global_load_dwordx4 v[156:159], v183, s[58:59] sc1\n\t"
      "s_waitcnt vmcnt(0)\n\t"
      "v_max3_u32 v234, v128, v129, v130\n\t"
      "v_max3_u32 v234, v234, v131, v132\n\t"
      "v_max3_u32 v234, v234, v133, v134\n\t"
      "v_max3_u32 v234, v234, v135, v136\n\t"
      "v_max3_u32 v234, v234, v137, v138\n\t"
      "v_max3_u32 v234, v234, v139, v140\n\t"
      "v_max3_u32 v234, v234, v141, v142\n\t"
      "v_max3_u32 v234, v234, v143, v144\n\t"
      "v_max3_u32 v234, v234, v145, v146\n\t"
      "v_max3_u32 v234, v234, v147, v148\n\t"
      "v_max3_u32 v234, v234, v149, v150\n\t"
      "v_max3_u32 v234, v234, v151, v152\n\t"
      "v_max3_u32 v234, v234, v153, v154\n\t"
      "v_max3_u32 v234, v234, v155, v156\n\t"
      "v_max3_u32 v234, v234, v157, v158\n\t"
      "v_max_u32_e32 v234, v234, v159\n\t"
      "v_cmp_eq_u32_e32 vcc, -1, v234\n\t"
      "s_cbranch_vccnz NP3L_%=\n\t"
      "NP3X_%=:\n\t"
      "v_mfma_f32_16x16x32_bf16 v[200:203], v[0:3], v[128:131], 0\n\t"
      "v_mfma_f32_16x16x32_bf16 v[204:207], v[4:7], v[128:131], 0\n\t"
      "v_mfma_f32_16x16x32_bf16 v[208:211], v[8:11], v[128:131], 0\n\t"
      "v_mfma_f32_16x16x32_bf16 v[212:215], v[12:15], v[128:131], 0\n\t"
      "v_mfma_f32_16x16x32_bf16 v[200:203], v[16:19], v[132:135], v[200:203]\n\t"
      "v_mfma_f32_16x16x32_bf16 v[204:207], v[20:23], v[132:135], v[204:207]\n\t"
      "v_mfma_f32_16x16x32_bf16 v[208:211], v[24:27], v[132:135], v[208:211]\n\t"
      "v_mfma_f32_16x16x32_bf16 v[212:215], v[28:31], v[132:135], v[212:215]\n\t"
      "v_mfma_f32_16x16x32_bf16 v[200:203], v[32:35], v[136:139], v[200:203]\n\t"
      "v_mfma_f32_16x16x32_bf16 v[204:207], v[36:39], v[136:139], v[204:207]\n\t"
      "v_mfma_f32_16x16x32_bf16 v[208:211], v[40:43], v[136:139], v[208:211]\n\t"
      "v_mfma_f32_16x16x32_bf16 v[212:215], v[44:47], v[136:139], v[212:215]\n\t"
      "v_mfma_f32_16x16x32_bf16 v[200:203], v[48:51], v[140:143], v[200:203]\n\t"
      "v_mfma_f32_16x16x32_bf16 v[204:207], v[52:55], v[140:143], v[204:207]\n\t"
      "v_mfma_f32_16x16x32_bf16 v[208:211], v[56:59], v[140:143], v[208:211]\n\t"
      "v_mfma_f32_16x16x32_bf16 v[212:215], v[60:63], v[140:143], v[212:215]\n\t"
      "v_mfma_f32_16x16x32_bf16 v[200:203], v[64:67], v[144:147], v[200:203]\n\t"
      "v_mfma_f32_16x16x32_bf16 v[204:207], v[68:71], v[144:147], v[204:207]\n\t"
      "v_mfma_f32_16x16x32_bf16 v[208:211], v[72:75], v[144:147], v[208:211]\n\t"
      "v_mfma_f32_16x16x32_bf16 v[212:215], v[76:79], v[144:147], v[212:215]\n\t"
      "v_mfma_f32_16x16x32_bf16 v[200:203], v[80:83], v[148:151], v[200:203]\n\t"
      "v_mfma_f32_16x16x32_bf16 v[204:207], v[84:87], v[148:151], v[204:207]\n\t"
      "v_mfma_f32_16x16x32_bf16 v[208:211], v[88:91], v[148:151], v[208:211]\n\t"
      "v_mfma_f32_16x16x32_bf16 v[212:215], v[92:95], v[148:151], v[212:215]\n\t"
      "v_mfma_f32_16x16x32_bf16 v[200:203], v[96:99], v[152:155], v[200:203]\n\t"
      "v_mfma_f32_16x16x32_bf16 v[204:207], v[100:103], v[152:155], v[204:207]\n\t"
      "v_mfma_f32_16x16x32_bf16 v[208:211], v[104:107], v[152:155], v[208:211]\n\t"
      "v_mfma_f32_16x16x32_bf16 v[212:215], v[108:111], v[152:155], v[212:215]\n\t"
      "v_mfma_f32_16x16x32_bf16 v[200:203], v[112:115], v[156:159], v[200:203]\n\t"
      "v_mfma_f32_16x16x32_bf16 v[204:207], v[116:119], v[156:159], v[204:207]\n\t"
      "v_mfma_f32_16x16x32_bf16 v[208:211], v[120:123], v[156:159], v[208:211]\n\t"
      "v_mfma_f32_16x16x32_bf16 v[212:215], v[124:127], v[156:159], v[212:215]\n\t"
      "global_load_dwordx4 v[128:131], v176, s[60:61] sc1\n\t"
      "global_load_dwordx4 v[132:135], v177, s[60:61] sc1\n\t"
      "global_load_dwordx4 v[136:139], v178, s[60:61] sc1\n\t"
      "global_load_dwordx4 v[140:143], v179, s[60:61] sc1\n\t"
      "global_load_dwordx4 v[144:147], v180, s[60:61] sc1\n\t"
      "global_load_dwordx4 v[148:151], v181, s[60:61] sc1\n\t"
      "global_load_dwordx4 v[152:155], v182, s[60:61] sc1\n\t"
      "global_load_dwordx4 v[156:159], v183, s[60:61] sc1\n\t"
      "s_waitcnt vmcnt(0)\n\t"
      "s_nop 7\n\t"
      "s_branch NP3B_%=\n\t"
      "NP4R_%=:\n\t"
      "s_mov_b32 s51, 0\n\t"
      "NP4L_%=:\n\t"
      "s_add_u32 s51, s51, 1\n\t"
      "s_cmp_lt_u32 s51, 262144\n\t"
      "s_cbranch_scc0 NP4X_%=\n\t"
      "s_waitcnt vmcnt(0)\n\t"
      "s_sleep 1\n\t"
      "global_load_dwordx4 v[128:131], v176, s[60:61] sc1\n\t"
      "global_load_dwordx4 v[132:135], v177, s[60:61] sc1\n\t"
      "global_load_dwordx4 v[136:139], v178, s[60:61] sc1\n\t"
      "global_load_dwordx4 v[140:143], v179, s[60:61] sc1\n\t"
      "global_load_dwordx4 v[144:147], v180, s[60:61] sc1\n\t"
      "global_load_dwordx4 v[148:151], v181, s[60:61] sc1\n\t"
      "global_load_dwordx4 v[152:155], v182, s[60:61] sc1\n\t"
      "global_load_dwordx4 v[156:159], v183, s[60:61] sc1\n\t"
      "s_waitcnt vmcnt(0)\n\t"
      "v_max3_u32 v234, v128, v129, v130\n\t"
      "v_max3_u32 v234, v234, v131, v132\n\t"
      "v_max3_u32 v234, v234, v133, v134\n\t"
      "v_max3_u32 v234, v234, v135, v136\n\t"
      "v_max3_u32 v234, v234, v137, v138\n\t"
      "v_max3_u32 v234, v234, v139, v140\n\t"
      "v_max3_u32 v234, v234, v141, v142\n\t"
      "v_max3_u32 v234, v234, v143, v144\n\t"
      "v_max3_u32 v234, v234, v145, v146\n\t"
      "v_max3_u32 v234, v234, v147, v148\n\t"
      "v_max3_u32 v234, v234, v149, v150\n\t"
      "v_max3_u32 v234, v234, v151, v152\n\t"
      "v_max3_u32 v234, v234, v153, v154\n\t"
      "v_max3_u32 v234, v234, v155, v156\n\t"
      "v_max3_u32 v234, v234, v157, v158\n\t"
      "v_max_u32_e32 v234, v234, v159\n\t"
      "v_cmp_eq_u32_e32 vcc, -1, v234\n\t"
      "s_cbranch_vccnz NP4L_%=\n\t"
      "NP4X_%=:\n\t"
      "v_mfma_f32_16x16x32_bf16 v[184:187], v[0:3], v[128:131], 0\n\t"
      "v_mfma_f32_16x16x32_bf16 v[188:191], v[4:7], v[128:131], 0\n\t"
      "v_mfma_f32_16x16x32_bf16 v[192:195], v[8:11], v[128:131], 0\n\t"
      "v_mfma_f32_16x16x32_bf16 v[196:199], v[12:15], v[128:131], 0\n\t"
      "v_mfma_f32_16x16x32_bf16 v[184:187], v[16:19], v[132:135], v[184:187]\n\t"
      "v_mfma_f32_16x16x32_bf16 v[188:191], v[20:23], v[132:135], v[188:191]\n\t"
      "v_mfma_f32_16x16x32_bf16 v[192:195], v[24:27], v[132:135], v[192:195]\n\t"
      "v_mfma_f32_16x16x32_bf16 v[196:199], v[28:31], v[132:135], v[196:199]\n\t"
      "v_mfma_f32_16x16x32_bf16 v[184:187], v[32:35], v[136:139], v[184:187]\n\t"
      "v_mfma_f32_16x16x32_bf16 v[188:191], v[36:39], v[136:139], v[188:191]\n\t"
      "v_mfma_f32_16x16x32_bf16 v[192:195], v[40:43], v[136:139], v[192:195]\n\t"
      "v_mfma_f32_16x16x32_bf16 v[196:199], v[44:47], v[136:139], v[196:199]\n\t"
      "v_mfma_f32_16x16x32_bf16 v[184:187], v[48:51], v[140:143], v[184:187]\n\t"
      "v_mfma_f32_16x16x32_bf16 v[188:191], v[52:55], v[140:143], v[188:191]\n\t"
      "v_mfma_f32_16x16x32_bf16 v[192:195], v[56:59], v[140:143], v[192:195]\n\t"
      "v_mfma_f32_16x16x32_bf16 v[196:199], v[60:63], v[140:143], v[196:199]\n\t"
      "v_mfma_f32_16x16x32_bf16 v[184:187], v[64:67], v[144:147], v[184:187]\n\t"
      "v_mfma_f32_16x16x32_bf16 v[188:191], v[68:71], v[144:147], v[188:191]\n\t"
      "v_mfma_f32_16x16x32_bf16 v[192:195], v[72:75], v[144:147], v[192:195]\n\t"
      "v_mfma_f32_16x16x32_bf16 v[196:199], v[76:79], v[144:147], v[196:199]\n\t"
      "v_mfma_f32_16x16x32_bf16 v[184:187], v[80:83], v[148:151], v[184:187]\n\t"
      "v_mfma_f32_16x16x32_bf16 v[188:191], v[84:87], v[148:151], v[188:191]\n\t"
      "v_mfma_f32_16x16x32_bf16 v[192:195], v[88:91], v[148:151], v[192:195]\n\t"
      "v_mfma_f32_16x16x32_bf16 v[196:199], v[92:95], v[148:151], v[196:199]\n\t"
      "v_mfma_f32_16x16x32_bf16 v[184:187], v[96:99], v[152:155], v[184:187]\n\t"
      "v_mfma_f32_16x16x32_bf16 v[188:191], v[100:103], v[152:155], v[188:191]\n\t"
      "v_mfma_f32_16x16x32_bf16 v[192:195], v[104:107], v[152:155], v[192:195]\n\t"
      "v_mfma_f32_16x16x32_bf16 v[196:199], v[108:111], v[152:155], v[196:199]\n\t"
      "v_mfma_f32_16x16x32_bf16 v[184:187], v[112:115], v[156:159], v[184:187]\n\t"
      "v_mfma_f32_16x16x32_bf16 v[188:191], v[116:119], v[156:159], v[188:191]\n\t"
      "v_mfma_f32_16x16x32_bf16 v[192:195], v[120:123], v[156:159], v[192:195]\n\t"
      "v_mfma_f32_16x16x32_bf16 v[196:199], v[124:127], v[156:159], v[196:199]\n\t"
      "global_load_dwordx4 v[128:131], v176, s[62:63] sc1\n\t"
      "global_load_dwordx4 v[132:135], v177, s[62:63] sc1\n\t"
      "global_load_dwordx4 v[136:139], v178, s[62:63] sc1\n\t"
      "global_load_dwordx4 v[140:143], v179, s[62:63] sc1\n\t"
      "global_load_dwordx4 v[144:147], v180, s[62:63] sc1\n\t"
      "global_load_dwordx4 v[148:151], v181, s[62:63] sc1\n\t"
      "global_load_dwordx4 v[152:155], v182, s[62:63] sc1\n\t"
      "global_load_dwordx4 v[156:159], v183, s[62:63] sc1\n\t"
      "s_waitcnt vmcnt(0)\n\t"
      "s_nop 7\n\t"
      "s_branch NP4B_%=\n\t"
      "NP5R_%=:\n\t"
      "s_mov_b32 s51, 0\n\t"
      "NP5L_%=:\n\t"
      "s_add_u32 s51, s51, 1\n\t"
      "s_cmp_lt_u32 s51, 262144\n\t"
      "s_cbranch_scc0 NP5X_%=\n\t"
      "s_waitcnt vmcnt(0)\n\t"
      "s_sleep 1\n\t"
      "global_load_dwordx4 v[128:131], v176, s[62:63] sc1\n\t"
      "global_load_dwordx4 v[132:135], v177, s[62:63] sc1\n\t"
      "global_load_dwordx4 v[136:139], v178, s[62:63] sc1\n\t"
      "global_load_dwordx4 v[140:143], v179, s[62:63] sc1\n\t"
      "global_load_dwordx4 v[144:147], v180, s[62:63] sc1\n\t"
      "global_load_dwordx4 v[148:151], v181, s[62:63] sc1\n\t"
      "global_load_dwordx4 v[152:155], v182, s[62:63] sc1\n\t"
      "global_load_dwordx4 v[156:159], v183, s[62:63] sc1\n\t"
      "s_waitcnt vmcnt(0)\n\t"
      "v_max3_u32 v234, v128, v129, v130\n\t"
      "v_max3_u32 v234, v234, v131, v132\n\t"
      "v_max3_u32 v234, v234, v133, v134\n\t"
      "v_max3_u32 v234, v234, v135, v136\n\t"
      "v_max3_u32 v234, v234, v137, v138\n\t"
      "v_max3_u32 v234, v234, v139, v140\n\t"
      "v_max3_u32 v234, v234, v141, v142\n\t"
      "v_max3_u32 v234, v234, v143, v144\n\t"
      "v_max3_u32 v234, v234, v145, v146\n\t"
      "v_max3_u32 v234, v234, v147, v148\n\t"
      "v_max3_u32 v234, v234, v149, v150\n\t"
      "v_max3_u32 v234, v234, v151, v152\n\t"
      "v_max3_u32 v234, v234, v153, v154\n\t"
      "v_max3_u32 v234, v234, v155, v156\n\t"
      "v_max3_u32 v234, v234, v157, v158\n\t"
      "v_max_u32_e32 v234, v234, v159\n\t"
      "v_cmp_eq_u32_e32 vcc, -1, v234\n\t"
      "s_cbranch_vccnz NP5L_%=\n\t"
      "NP5X_%=:\n\t"
      "v_mfma_f32_16x16x32_bf16 v[200:203], v[0:3], v[128:131], 0\n\t"
      "v_mfma_f32_16x16x32_bf16 v[204:207], v[4:7], v[128:131], 0\n\t"
      "v_mfma_f32_16x16x32_bf16 v[208:211], v[8:11], v[128:131], 0\n\t"
      "v_mfma_f32_16x16x32_bf16 v[212:215], v[12:15], v[128:131], 0\n\t"
      "v_mfma_f32_16x16x32_bf16 v[200:203], v[16:19], v[132:135], v[200:203]\n\t"
      "v_mfma_f32_16x16x32_bf16 v[204:207], v[20:23], v[132:135], v[204:207]\n\t"
      "v_mfma_f32_16x16x32_bf16 v[208:211], v[24:27], v[132:135], v[208:211]\n\t"
      "v_mfma_f32_16x16x32_bf16 v[212:215], v[28:31], v[132:135], v[212:215]\n\t"
      "v_mfma_f32_16x16x32_bf16 v[200:203], v[32:35], v[136:139], v[200:203]\n\t"
      "v_mfma_f32_16x16x32_bf16 v[204:207], v[36:39], v[136:139], v[204:207]\n\t"
      "v_mfma_f32_16x16x32_bf16 v[208:211], v[40:43], v[136:139], v[208:211]\n\t"
      "v_mfma_f32_16x16x32_bf16 v[212:215], v[44:47], v[136:139], v[212:215]\n\t"
      "v_mfma_f32_16x16x32_bf16 v[200:203], v[48:51], v[140:143], v[200:203]\n\t"
      "v_mfma_f32_16x16x32_bf16 v[204:207], v[52:55], v[140:143], v[204:207]\n\t"
      "v_mfma_f32_16x16x32_bf16 v[208:211], v[56:59], v[140:143], v[208:211]\n\t"
      "v_mfma_f32_16x16x32_bf16 v[212:215], v[60:63], v[140:143], v[212:215]\n\t"
      "v_mfma_f32_16x16x32_bf16 v[200:203], v[64:67], v[144:147], v[200:203]\n\t"
      "v_mfma_f32_16x16x32_bf16 v[204:207], v[68:71], v[144:147], v[204:207]\n\t"
      "v_mfma_f32_16x16x32_bf16 v[208:211], v[72:75], v[144:147], v[208:211]\n\t"
      "v_mfma_f32_16x16x32_bf16 v[212:215], v[76:79], v[144:147], v[212:215]\n\t"
      "v_mfma_f32_16x16x32_bf16 v[200:203], v[80:83], v[148:151], v[200:203]\n\t"
      "v_mfma_f32_16x16x32_bf16 v[204:207], v[84:87], v[148:151], v[204:207]\n\t"
      "v_mfma_f32_16x16x32_bf16 v[208:211], v[88:91], v[148:151], v[208:211]\n\t"
      "v_mfma_f32_16x16x32_bf16 v[212:215], v[92:95], v[148:151], v[212:215]\n\t"
      "v_mfma_f32_16x16x32_bf16 v[200:203], v[96:99], v[152:155], v[200:203]\n\t"
      "v_mfma_f32_16x16x32_bf16 v[204:207], v[100:103], v[152:155], v[204:207]\n\t"
      "v_mfma_f32_16x16x32_bf16 v[208:211], v[104:107], v[152:155], v[208:211]\n\t"
      "v_mfma_f32_16x16x32_bf16 v[212:215], v[108:111], v[152:155], v[212:215]\n\t"
      "v_mfma_f32_16x16x32_bf16 v[200:203], v[112:115], v[156:159], v[200:203]\n\t"
      "v_mfma_f32_16x16x32_bf16 v[204:207], v[116:119], v[156:159], v[204:207]\n\t"
      "v_mfma_f32_16x16x32_bf16 v[208:211], v[120:123], v[156:159], v[208:211]\n\t"
      "v_mfma_f32_16x16x32_bf16 v[212:215], v[124:127], v[156:159], v[212:215]\n\t"
      "global_load_dwordx4 v[128:131], v176, s[64:65] sc1\n\t"
      "global_load_dwordx4 v[132:135], v177, s[64:65] sc1\n\t"
      "global_load_dwordx4 v[136:139], v178, s[64:65] sc1\n\t"
      "global_load_dwordx4 v[140:143], v179, s[64:65] sc1\n\t"
      "global_load_dwordx4 v[144:147], v180, s[64:65] sc1\n\t"
      "global_load_dwordx4 v[148:151], v181, s[64:65] sc1\n\t"
      "global_load_dwordx4 v[152:155], v182, s[64:65] sc1\n\t"
      "global_load_dwordx4 v[156:159], v183, s[64:65] sc1\n\t"
      "s_waitcnt vmcnt(0)\n\t"
      "s_nop 7\n\t"
      "s_branch NP5B_%=\n\t"
      "NP6R_%=:\n\t"
      "s_mov_b32 s51, 0\n\t"
      "NP6L_%=:\n\t"
      "s_add_u32 s51, s51, 1\n\t"
      "s_cmp_lt_u32 s51, 262144\n\t"
      "s_cbranch_scc0 NP6X_%=\n\t"
      "s_waitcnt vmcnt(0)\n\t"
      "s_sleep 1\n\t"
      "global_load_dwordx4 v[128:131], v176, s[64:65] sc1\n\t"
      "global_load_dwordx4 v[132:135], v177, s[64:65] sc1\n\t"
      "global_load_dwordx4 v[136:139], v178, s[64:65] sc1\n\t"
      "global_load_dwordx4 v[140:143], v179, s[64:65] sc1\n\t"
      "global_load_dwordx4 v[144:147], v180, s[64:65] sc1\n\t"
      "global_load_dwordx4 v[148:151], v181, s[64:65] sc1\n\t"
      "global_load_dwordx4 v[152:155], v182, s[64:65] sc1\n\t"
      "global_load_dwordx4 v[156:159], v183, s[64:65] sc1\n\t"
      "s_waitcnt vmcnt(0)\n\t"
      "v_max3_u32 v234, v128, v129, v130\n\t"
      "v_max3_u32 v234, v234, v131, v132\n\t"
      "v_max3_u32 v234, v234, v133, v134\n\t"
      "v_max3_u32 v234, v234, v135, v136\n\t"
      "v_max3_u32 v234, v234, v137, v138\n\t"
      "v_max3_u32 v234, v234, v139, v140\n\t"
      "v_max3_u32 v234, v234, v141, v142\n\t"
      "v_max3_u32 v234, v234, v143, v144\n\t"
      "v_max3_u32 v234, v234, v145, v146\n\t"
      "v_max3_u32 v234, v234, v147, v148\n\t"
      "v_max3_u32 v234, v234, v149, v150\n\t"
      "v_max3_u32 v234, v234, v151, v152\n\t"
      "v_max3_u32 v234, v234, v153, v154\n\t"
      "v_max3_u32 v234, v234, v155, v156\n\t"
      "v_max3_u32 v234, v234, v157, v158\n\t"
      "v_max_u32_e32 v234, v234, v159\n\t"
      "v_cmp_eq_u32_e32 vcc, -1, v234\n\t"
      "s_cbranch_vccnz NP6L_%=\n\t"
      "NP6X_%=:\n\t"
      "v_mfma_f32_16x16x32_bf16 v[184:187], v[0:3], v[128:131], 0\n\t"
      "v_mfma_f32_16x16x32_bf16 v[188:191], v[4:7], v[128:131], 0\n\t"
      "v_mfma_f32_16x16x32_bf16 v[192:195], v[8:11], v[128:131], 0\n\t"
      "v_mfma_f32_16x16x32_bf16 v[196:199], v[12:15], v[128:131], 0\n\t"
      "v_mfma_f32_16x16x32_bf16 v[184:187], v[16:19], v[132:135], v[184:187]\n\t"
      "v_mfma_f32_16x16x32_bf16 v[188:191], v[20:23], v[132:135], v[188:191]\n\t"
      "v_mfma_f32_16x16x32_bf16 v[192:195], v[24:27], v[132:135], v[192:195]\n\t"
      "v_mfma_f32_16x16x32_bf16 v[196:199], v[28:31], v[132:135], v[196:199]\n\t"
      "v_mfma_f32_16x16x32_bf16 v[184:187], v[32:35], v[136:139], v[184:187]\n\t"
      "v_mfma_f32_16x16x32_bf16 v[188:191], v[36:39], v[136:139], v[188:191]\n\t"
      "v_mfma_f32_16x16x32_bf16 v[192:195], v[40:43], v[136:139], v[192:195]\n\t"
      "v_mfma_f32_16x16x32_bf16 v[196:199], v[44:47], v[136:139], v[196:199]\n\t"
      "v_mfma_f32_16x16x32_bf16 v[184:187], v[48:51], v[140:143], v[184:187]\n\t"
      "v_mfma_f32_16x16x32_bf16 v[188:191], v[52:55], v[140:143], v[188:191]\n\t"
      "v_mfma_f32_16x16x32_bf16 v[192:195], v[56:59], v[140:143], v[192:195]\n\t"
      "v_mfma_f32_16x16x32_bf16 v[196:199], v[60:63], v[140:143], v[196:199]\n\t"
      "v_mfma_f32_16x16x32_bf16 v[184:187], v[64:67], v[144:147], v[184:187]\n\t"
      "v_mfma_f32_16x16x32_bf16 v[188:191], v[68:71], v[144:147], v[188:191]\n\t"
      "v_mfma_f32_16x16x32_bf16 v[192:195], v[72:75], v[144:147], v[192:195]\n\t"
      "v_mfma_f32_16x16x32_bf16 v[196:199], v[76:79], v[144:147], v[196:199]\n\t"
      "v_mfma_f32_16x16x32_bf16 v[184:187], v[80:83], v[148:151], v[184:187]\n\t"
      "v_mfma_f32_16x16x32_bf16 v[188:191], v[84:87], v[148:151], v[188:191]\n\t"
      "v_mfma_f32_16x16x32_bf16 v[192:195], v[88:91], v[148:151], v[192:195]\n\t"
      "v_mfma_f32_16x16x32_bf16 v[196:199], v[92:95], v[148:151], v[196:199]\n\t"
      "v_mfma_f32_16x16x32_bf16 v[184:187], v[96:99], v[152:155], v[184:187]\n\t"
      "v_mfma_f32_16x16x32_bf16 v[188:191], v[100:103], v[152:155], v[188:191]\n\t"
      "v_mfma_f32_16x16x32_bf16 v[192:195], v[104:107], v[152:155], v[192:195]\n\t"
      "v_mfma_f32_16x16x32_bf16 v[196:199], v[108:111], v[152:155], v[196:199]\n\t"
      "v_mfma_f32_16x16x32_bf16 v[184:187], v[112:115], v[156:159], v[184:187]\n\t"
      "v_mfma_f32_16x16x32_bf16 v[188:191], v[116:119], v[156:159], v[188:191]\n\t"
      "v_mfma_f32_16x16x32_bf16 v[192:195], v[120:123], v[156:159], v[192:195]\n\t"
      "v_mfma_f32_16x16x32_bf16 v[196:199], v[124:127], v[156:159], v[196:199]\n\t"
      "s_waitcnt vmcnt(0)\n\t"
      "s_nop 7\n\t"
      "s_branch NP6B_%=\n\t"
      : "+{s[4:19]}"(s), "+{s[20:21]}"(kr), "+{s22}"(hint), "+{v[240:247]}"(v)
      :
      : "memory", "vcc", "scc",
        "v0", "v1", "v2", "v3", "v4", "v5", "v6", "v7", "v8", "v9", "v10", "v11", "v12", "v13", "v14", "v15", "v16",
        "v17", "v18", "v19", "v20", "v21", "v22", "v23", "v24", "v25", "v26", "v27", "v28", "v29", "v30", "v31", "v32",
        "v33", "v34", "v35", "v36", "v37", "v38", "v39", "v40", "v41", "v42", "v43", "v44", "v45", "v46", "v47", "v48",
        "v49", "v50", "v51", "v52", "v53", "v54", "v55", "v56", "v57", "v58", "v59", "v60", "v61", "v62", "v63", "v64",
        "v65", "v66", "v67", "v68", "v69", "v70", "v71", "v72", "v73", "v74", "v75", "v76", "v77", "v78", "v79", "v80",
        "v81", "v82", "v83", "v84", "v85", "v86", "v87", "v88", "v89", "v90", "v91", "v92", "v93", "v94", "v95", "v96",
        "v97", "v98", "v99", "v100", "v101", "v102", "v103", "v104", "v105", "v106", "v107", "v108", "v109", "v110",
        "v111", "v112", "v113", "v114", "v115", "v116", "v117", "v118", "v119", "v120", "v121", "v122", "v123", "v124",
        "v125", "v126", "v127", "v128", "v129", "v130", "v131", "v132", "v133", "v134", "v135", "v136", "v137", "v138",
        "v139", "v140", "v141", "v142", "v143", "v144", "v145", "v146", "v147", "v148", "v149", "v150", "v151", "v152",
        "v153", "v154", "v155", "v156", "v157", "v158", "v159", "v160", "v161", "v162", "v163", "v164", "v165", "v166",
        "v167", "v168", "v169", "v170", "v171", "v172", "v173", "v174", "v175", "v176", "v177", "v178", "v179", "v180",
        "v181", "v182", "v183", "v184", "v185", "v186", "v187", "v188", "v189", "v190", "v191", "v192", "v193", "v194",
        "v195", "v196", "v197", "v198", "v199", "v200", "v201", "v202", "v203", "v204", "v205", "v206", "v207", "v208",
        "v209", "v210", "v211", "v212", "v213", "v214", "v215", "v216", "v217", "v218", "v219", "v220", "v221", "v222",
        "v223", "v224", "v225", "v226", "v227", "v228", "v229", "v230", "v231", "v232", "v233", "v234", "v235", "v236",
        "v237", "v238", "v239", "v248", "v249", "v250", "v251", "v252", "v253", "s44", "s45", "s46", "s47", "s48",
        "s49", "s50", "s51", "s52", "s53", "s54", "s55", "s56", "s57", "s58", "s59", "s60", "s61", "s62", "s63", "s64",
        "s65", "s66", "s67", "s68", "s69", "s70"
    );
    __builtin_amdgcn_endpgm();
  } else {
    const int ib = idx - 16, ts = 16 * xcd + ib, t = 16 * ((xcd + XO) & 7) + ib;
    const int st = 4 * xcd + (ib >> 2), blk0 = 7 * (ib & 3);
    const int rot = (2 * (ib & 3) + 2 * st) % 7;
    const unsigned bits = UNI((wave >= 4 ? 1 : 0) | (wave >= 4 ? 2 : 0) | (wave == 0 ? 16 : 0) |
                              (t == 0 && wave == 0 ? 32 : 0));
    {
      LANE_BITS(threadIdx.x & 63);
      const int cb0 = wave < 4 ? 4 * wave : 4 + 3 * wave;
      const int k0 = (ib + 2 * xcd) % 7;
      u32x16 v, s;
      u32x8 s2;
      u32x4 vo;
      v[0] = (8 * (wave & 3) + lr) * 896 + (wave >> 2) * 128 + ((lc ^ swz_tr(8 * wave + lr)) << 4) + 4096;
#pragma unroll
      for (int b = 0; b < 4; ++b) {
        const int cb = min(cb0 + b, 27);
        v[1 + b] = g * B_UNIT + (cb >> 2) * 1024 + q4 * 128 + (((2 * (cb & 3) + (p4 >> 1)) ^ swz_tr(8 * g + q4)) << 4) + (p4 & 1) * 8;
        const int by = 112 * wave + 56 * (b >> 1) + 32 * (b & 1) + 8 * g - 128 * k0;
        vo[b] = B_X + c * B_OP + (by < 0 ? by + 896 : by);
      }
      v[5] = B_X + (4 * g + q4) * 896 + cb0 * 32 + p4 * 8;
      v[6] = B_X + c * B_OP + 16 * g;
      v[7] = ((cb0 + (g & 1)) * 64 + (t >> 1)) * 1024 + (2 * (t & 1) + (g >> 1)) * 256 + c * 16;
      v[8] = B_PS + (4 * g + q4) * 64 + wave * 8;
      v[9] = B_PS + (4 * g + p4) * 64 + q4 * 16;
      v[10] = (ts * 16 + 4 * g + p4) * 16;
      v[11] = (lane >> 2) * 3584 + (p4 + ((7 * xcd) >> 1)) * 128;
      v[12] = lane * 16;
      v[14] = ib * 112 * 4096;
      v[15] = ST_H3 + t * 128 + lc * 16384;
      put(s, 0, (u64)w2);
      put(s, 2, (u64)(wo + k0 * 64 * 896 - 4096));
      put(s, 4, (u64)(ws + WS_ST));
      put(s, 6, (u64)(ws + WS_O + (t * 16 + 2 * wave) * 1024));
      put(s, 10, (u64)(ws + WS_HN));
      put(s, 12, (u64)(ws + WS_G + (t * 8 + wave) * 1568));
      put(s, 14, (u64)(w1 + (ib * 8 + wave) * 16 * 3584));
      u64 px = (u64)(x + t * 14336 + min(wave, 6) * 2048);
      unsigned lx = B_X + min(wave, 6) * 2048;
      u64 pb1 = (u64)(b1 + min(ib, 6) * 512), pb2 = (u64)(b2 + (ib & 7) * 512), pob = (u64)(out + ib * 114688);
      unsigned vq = ST_H2 + ((((t >> 3) * 8 + wave) * 2 + ((t >> 2) & 1)) * 2 + (lane & 1)) * 64;
      s2[0] = bits;
      s2[1] = (wave & 3) * B_UNIT + (wave >> 2) * 1024;
      s2[2] = UNI(wave < 4 ? ~0u : 0u);
      s2[3] = 13 - 2 * k0;
      put2(s2, 4, wave < 4 ? ~0u : 0xffffu, wave < 4 ? ~0u : 0xffffu);
    asm volatile(
      "s_waitcnt lgkmcnt(0)\n\ts_nop 4\n\t"
      "s_load_dwordx2 s[26:27], s[8:9], 0x20000\n\t"
      "v_mov_b32_e32 v248, -1\n\t"
      "v_mov_b32_e32 v249, -1\n\t"
      "v_mov_b32_e32 v250, -1\n\t"
      "v_mov_b32_e32 v251, -1\n\t"
      "v_add_u32_e32 v221, 4294965504, v224\n\t"
      "v_add_u32_e32 v222, 512, v224\n\t"
      "v_add_u32_e32 v223, 4294966016, v224\n\t"
      "s_bitcmp1_b32 s20, 0\n\t"
      "s_cselect_b64 s[46:47], -1, 0\n\t"
      "s_mov_b64 vcc, s[46:47]\n\t"
      "v_add_u32_e32 v237, 131072, v231\n\t"
      "s_mov_b32 s44, 0xffffffff\n\t"
      "s_mov_b32 s45, 0xffff\n\t"
      "s_add_u32 s40, s14, 2097152\n\t"
      "s_addc_u32 s41, s15, 0\n\t"
      "s_waitcnt vmcnt(4)\n\t"
      "s_barrier\n\t"
      "ds_read_b64_tr_b16 v[204:205], v229\n\t"
      "ds_read_b64_tr_b16 v[206:207], v229 offset:32\n\t"
      "ds_read_b64_tr_b16 v[208:209], v229 offset:64\n\t"
      "ds_read_b64_tr_b16 v[210:211], v229 offset:96\n\t"
      "s_waitcnt lgkmcnt(0)\n\t"
      "s_add_u32 s26, s26, 1\n\t"
      "s_addc_u32 s27, s27, 0\n\t"
      "s_mul_i32 s48, s26, 0x9e3779b9\n\t"
      "s_mul_hi_u32 s13, s26, 0x7f4a7c15\n\t"
      "s_add_u32 s13, s13, s48\n\t"
      "s_mul_i32 s48, s27, 0x7f4a7c15\n\t"
      "s_add_u32 s13, s13, s48\n\t"
      "s_mul_i32 s12, s26, 0x7f4a7c15\n\t"
      "s_add_u32 s12, s12, 0xd192ed03\n\t"
      "s_addc_u32 s13, s13, 0xd1b54a32\n\t"
      "v_mov_b32_e32 v188, s12\n\t"
      "v_mov_b32_e32 v189, s13\n\t"
      "v_mov_b32_e32 v190, s26\n\t"
      "v_mov_b32_e32 v191, s27\n\t"
      "v_mov_b32_e32 v192, 163312\n\t"
      "s_mov_b64 exec, 1\n\t"
      "ds_write_b128 v192, v[188:191]\n\t"
      "s_mov_b64 exec, -1\n\t"
      "v_and_b32_e32 v210, s22, v210\n\t"
      "v_and_b32_e32 v211, s22, v211\n\t"
      "v_lshlrev_b32_e32 v160, 16, v204\n\t"
      "v_and_b32_e32 v161, 0xffff0000, v204\n\t"
      "v_lshlrev_b32_e32 v162, 16, v205\n\t"
      "v_and_b32_e32 v163, 0xffff0000, v205\n\t"
      "v_lshlrev_b32_e32 v164, 16, v206\n\t"
      "v_and_b32_e32 v165, 0xffff0000, v206\n\t"
      "v_lshlrev_b32_e32 v166, 16, v207\n\t"
      "v_and_b32_e32 v167, 0xffff0000, v207\n\t"
      "v_lshlrev_b32_e32 v168, 16, v208\n\t"
      "v_and_b32_e32 v169, 0xffff0000, v208\n\t"
      "v_lshlrev_b32_e32 v170, 16, v209\n\t"
      "v_and_b32_e32 v171, 0xffff0000, v209\n\t"
      "v_lshlrev_b32_e32 v172, 16, v210\n\t"
      "v_and_b32_e32 v173, 0xffff0000, v210\n\t"
      "v_lshlrev_b32_e32 v174, 16, v211\n\t"
      "v_and_b32_e32 v175, 0xffff0000, v211\n\t"
      "v_pk_add_f32 v[188:189], v[160:161], v[164:165]\n\t"
      "v_pk_mul_f32 v[192:193], v[160:161], v[160:161]\n\t"
      "v_pk_add_f32 v[190:191], v[162:163], v[166:167]\n\t"
      "v_pk_mul_f32 v[194:195], v[162:163], v[162:163]\n\t"
      "v_pk_fma_f32 v[192:193], v[164:165], v[164:165], v[192:193]\n\t"
      "v_pk_fma_f32 v[194:195], v[166:167], v[166:167], v[194:195]\n\t"
      "v_pk_add_f32 v[188:189], v[188:189], v[168:169]\n\t"
      "v_pk_fma_f32 v[192:193], v[168:169], v[168:169], v[192:193]\n\t"
      "v_pk_add_f32 v[190:191], v[190:191], v[170:171]\n\t"
      "v_pk_fma_f32 v[194:195], v[170:171], v[170:171], v[194:195]\n\t"
      "v_pk_add_f32 v[188:189], v[188:189], v[172:173]\n\t"
      "v_pk_fma_f32 v[192:193], v[172:173], v[172:173], v[192:193]\n\t"
      "v_pk_add_f32 v[190:191], v[190:191], v[174:175]\n\t"
      "v_pk_fma_f32 v[194:195], v[174:175], v[174:175], v[194:195]\n\t"
      "v_add_f32_dpp v196, v188, v188 row_ror:8 row_mask:0xf bank_mask:0x3\n\t"
      "s_nop 1\n\t"
      "v_add_f32_dpp v196, v190, v190 row_ror:8 row_mask:0xf bank_mask:0xc\n\t"
      "v_add_f32_dpp v197, v189, v189 row_ror:8 row_mask:0xf bank_mask:0x3\n\t"
      "s_nop 1\n\t"
      "v_add_f32_dpp v197, v191, v191 row_ror:8 row_mask:0xf bank_mask:0xc\n\t"
      "v_add_f32_dpp v198, v192, v192 row_ror:8 row_mask:0xf bank_mask:0x3\n\t"
      "s_nop 1\n\t"
      "v_add_f32_dpp v198, v194, v194 row_ror:8 row_mask:0xf bank_mask:0xc\n\t"
      "v_add_f32_dpp v199, v193, v193 row_ror:8 row_mask:0xf bank_mask:0x3\n\t"
      "s_nop 1\n\t"
      "v_add_f32_dpp v199, v195, v195 row_ror:8 row_mask:0xf bank_mask:0xc\n\t"
      "v_add_f32_dpp v188, v196, v196 row_shl:4 row_mask:0xf bank_mask:0x5\n\t"
      "s_nop 1\n\t"
      "v_add_f32_dpp v188, v197, v197 row_shr:4 row_mask:0xf bank_mask:0xa\n\t"
      "v_add_f32_dpp v189, v198, v198 row_shl:4 row_mask:0xf bank_mask:0x5\n\t"
      "s_nop 1\n\t"
      "v_add_f32_dpp v189, v199, v199 row_shr:4 row_mask:0xf bank_mask:0xa\n\t"
      "v_add_f32_dpp v188, v188, v188 quad_perm:[1,0,3,2] row_mask:0xf bank_mask:0xf\n\t"
      "s_nop 0\n\t"
      "v_add_f32_dpp v189, v189, v189 quad_perm:[1,0,3,2] row_mask:0xf bank_mask:0xf\n\t"
      "v_add_f32_dpp v188, v188, v188 quad_perm:[2,3,0,1] row_mask:0xf bank_mask:0xf\n\t"
      "s_nop 0\n\t"
      "v_add_f32_dpp v189, v189, v189 quad_perm:[2,3,0,1] row_mask:0xf bank_mask:0xf\n\t"
      "ds_write_b64 v232, v[188:189]\n\t"
      "s_waitcnt vmcnt(0)\n\t"
      "s_waitcnt lgkmcnt(0)\n\t"
      "s_barrier\n\t"
      "ds_read_b128 v[188:191], v233\n\t"
      "s_waitcnt lgkmcnt(0)\n\t"
      "v_pk_add_f32 v[188:189], v[188:189], v[190:191]\n\t"
      "s_nop 1\n\t"
      "v_add_f32_dpp v188, v188, v188 row_ror:4 row_mask:0xf bank_mask:0xf\n\t"
      "v_add_f32_dpp v189, v189, v189 row_ror:4 row_mask:0xf bank_mask:0xf\n\t"
      "s_nop 0\n\t"
      "v_add_f32_dpp v188, v188, v188 row_ror:8 row_mask:0xf bank_mask:0xf\n\t"
      "v_add_f32_dpp v189, v189, v189 row_ror:8 row_mask:0xf bank_mask:0xf\n\t"
      "v_mul_f32_e32 v188, 0x3b124925, v188\n\t"
      "v_mul_f32_e32 v189, 0x3b124925, v189\n\t"
      "v_fma_f32 v189, -v188, v188, v189\n\t"
      "v_max_f32_e32 v189, 0, v189\n\t"
      "s_bitcmp1_b32 s20, 4\n\t"
      "s_cbranch_scc0 GW_%=\n\t"
      "v_add_f32_e32 v189, 0x3727c5ac, v189\n\t"
      "v_rsq_f32_e32 v189, v189\n\t"
      "v_xor_b32_e32 v188, 0x80000000, v188\n\t"
      "v_xor_b32_e32 v190, s12, v188\n\t"
      "v_xor_b32_e32 v191, s13, v189\n\t"
      "s_mov_b32 s48, 0xf000f\n\t"
      "s_mov_b32 s49, 0xf000f\n\t"
      "s_mov_b64 exec, s[48:49]\n\t"
      "global_store_dwordx4 v234, v[188:191], s[8:9] sc1\n\t"
      "s_mov_b64 exec, -1\n\t"
      "GW_%=:\n\t"
      "s_mov_b32 m0, s36\n\t"
      "s_nop 0\n\t"
      "global_load_lds_dwordx4 v236, s[34:35]\n\t"
      "global_load_lds_dwordx4 v236, s[34:35] offset:1024\n\t"
      "s_bitcmp1_b32 s20, 4\n\t"
      "s_cbranch_scc0 PG_%=\n\t"
      "s_mov_b64 exec, 1\n\t"
      "s_mov_b32 m0, 163328\n\t"
      "s_nop 0\n\t"
      "global_load_lds_dword v235, s[18:19]\n\t"
      "s_mov_b32 m0, 163328\n\t"
      "s_nop 0\n\t"
      "global_load_lds_dword v238, s[4:5]\n\t"
      "global_load_lds_dword v236, s[32:33] sc1\n\t"
      "s_mov_b64 exec, 0xf\n\t"
      "v_lshlrev_b32_e32 v188, 3, v236\n\t"
      "global_load_lds_dword v188, s[28:29]\n\t"
      "global_load_lds_dword v188, s[30:31]\n\t"
      "s_mov_b64 exec, 0x3f\n\t"
      "v_lshlrev_b32_e32 v189, 16, v236\n\t"
      "global_load_lds_dword v189, s[14:15] sc1\n\t"
      "s_mov_b64 exec, -1\n\t"
      "PG_%=:\n\t"
      "s_add_u32 m0, s21, 0\n\t"
      "s_nop 0\n\t"
      "global_load_lds_dwordx4 v224, s[6:7]\n\t"
      "global_load_lds_dwordx4 v221, s[6:7] offset:2048\n\t"
      "s_add_u32 m0, s21, 4096\n\t"
      "s_nop 0\n\t"
      "global_load_lds_dwordx4 v222, s[6:7]\n\t"
      "s_cbranch_vccnz WR0_%=\n\t"
      "global_load_lds_dwordx4 v223, s[6:7] offset:2048\n\t"
      "WR0_%=:\n\t"
      "s_sub_u32 s23, s23, 1\n\t"
      "s_cselect_b32 s48, -13, 1\n\t"
      "s_cselect_b32 s49, -1, 0\n\t"
      "s_mul_i32 s48, s48, 28672\n\t"
      "s_add_u32 s6, s6, s48\n\t"
      "s_addc_u32 s7, s7, s49\n\t"
      "s_add_u32 m0, s21, 28672\n\t"
      "s_nop 0\n\t"
      "global_load_lds_dwordx4 v224, s[6:7]\n\t"
      "global_load_lds_dwordx4 v221, s[6:7] offset:2048\n\t"
      "s_add_u32 m0, s21, 32768\n\t"
      "s_nop 0\n\t"
      "global_load_lds_dwordx4 v222, s[6:7]\n\t"
      "s_cbranch_vccnz WR1_%=\n\t"
      "global_load_lds_dwordx4 v223, s[6:7] offset:2048\n\t"
      "WR1_%=:\n\t"
      "s_sub_u32 s23, s23, 1\n\t"
      "s_cselect_b32 s48, -13, 1\n\t"
      "s_cselect_b32 s49, -1, 0\n\t"
      "s_mul_i32 s48, s48, 28672\n\t"
      "s_add_u32 s6, s6, s48\n\t"
      "s_addc_u32 s7, s7, s49\n\t"
      "s_cbranch_vccnz WW0_%=\n\t"
      "s_waitcnt vmcnt(4)\n\t"
      "s_branch WX0_%=\n\t"
      "WW0_%=:\n\t"
      "s_waitcnt vmcnt(3)\n\t"
      "WX0_%=:\n\t"
      "s_waitcnt lgkmcnt(0)\n\t"
      "s_barrier\n\t"
      "ds_read_b64_tr_b16 v[0:1], v225\n\t"
      "ds_read_b64_tr_b16 v[2:3], v225 offset:512\n\t"
      "ds_read_b64_tr_b16 v[4:5], v226\n\t"
      "ds_read_b64_tr_b16 v[6:7], v226 offset:512\n\t"
      "ds_read_b64_tr_b16 v[8:9], v227\n\t"
      "ds_read_b64_tr_b16 v[10:11], v227 offset:512\n\t"
      "ds_read_b64_tr_b16 v[12:13], v228\n\t"
      "ds_read_b64_tr_b16 v[14:15], v228 offset:512\n\t"
      "s_add_u32 m0, s21, 57344\n\t"
      "s_nop 0\n\t"
      "global_load_lds_dwordx4 v224, s[6:7]\n\t"
      "global_load_lds_dwordx4 v221, s[6:7] offset:2048\n\t"
      "s_add_u32 m0, s21, 61440\n\t"
      "s_nop 0\n\t"
      "global_load_lds_dwordx4 v222, s[6:7]\n\t"
      "s_cbranch_vccnz WR2_%=\n\t"
      "global_load_lds_dwordx4 v223, s[6:7] offset:2048\n\t"
      "WR2_%=:\n\t"
      "s_sub_u32 s23, s23, 1\n\t"
      "s_cselect_b32 s48, -13, 1\n\t"
      "s_cselect_b32 s49, -1, 0\n\t"
      "s_mul_i32 s48, s48, 28672\n\t"
      "s_add_u32 s6, s6, s48\n\t"
      "s_addc_u32 s7, s7, s49\n\t"
      "s_cbranch_vccnz WW1_%=\n\t"
      "s_waitcnt vmcnt(4)\n\t"
      "s_branch WX1_%=\n\t"
      "WW1_%=:\n\t"
      "s_waitcnt vmcnt(3)\n\t"
      "WX1_%=:\n\t"
      "s_waitcnt lgkmcnt(0)\n\t"
      "s_barrier\n\t"
      "ds_read_b64_tr_b16 v[16:17], v225 offset:28672\n\t"
      "ds_read_b64_tr_b16 v[18:19], v225 offset:29184\n\t"
      "ds_read_b64_tr_b16 v[20:21], v226 offset:28672\n\t"
      "ds_read_b64_tr_b16 v[22:23], v226 offset:29184\n\t"
      "ds_read_b64_tr_b16 v[24:25], v227 offset:28672\n\t"
      "ds_read_b64_tr_b16 v[26:27], v227 offset:29184\n\t"
      "ds_read_b64_tr_b16 v[28:29], v228 offset:28672\n\t"
      "ds_read_b64_tr_b16 v[30:31], v228 offset:29184\n\t"
      "s_add_u32 m0, s21, 86016\n\t"
      "s_nop 0\n\t"
      "global_load_lds_dwordx4 v224, s[6:7]\n\t"
      "global_load_lds_dwordx4 v221, s[6:7] offset:2048\n\t"
      "s_add_u32 m0, s21, 90112\n\t"
      "s_nop 0\n\t"
      "global_load_lds_dwordx4 v222, s[6:7]\n\t"
      "s_cbranch_vccnz WR3_%=\n\t"
      "global_load_lds_dwordx4 v223, s[6:7] offset:2048\n\t"
      "WR3_%=:\n\t"
      "s_sub_u32 s23, s23, 1\n\t"
      "s_cselect_b32 s48, -13, 1\n\t"
      "s_cselect_b32 s49, -1, 0\n\t"
      "s_mul_i32 s48, s48, 28672\n\t"
      "s_add_u32 s6, s6, s48\n\t"
      "s_addc_u32 s7, s7, s49\n\t"
      "v_add_u32_e32 v225, 57344, v225\n\t"
      "v_add_u32_e32 v226, 57344, v226\n\t"
      "v_add_u32_e32 v227, 57344, v227\n\t"
      "v_add_u32_e32 v228, 57344, v228\n\t"
      "s_cbranch_vccnz WW2_%=\n\t"
      "s_waitcnt vmcnt(4)\n\t"
      "s_branch WX2_%=\n\t"
      "WW2_%=:\n\t"
      "s_waitcnt vmcnt(3)\n\t"
      "WX2_%=:\n\t"
      "s_waitcnt lgkmcnt(0)\n\t"
      "s_barrier\n\t"
      "ds_read_b64_tr_b16 v[32:33], v225\n\t"
      "ds_read_b64_tr_b16 v[34:35], v225 offset:512\n\t"
      "ds_read_b64_tr_b16 v[36:37], v226\n\t"
      "ds_read_b64_tr_b16 v[38:39], v226 offset:512\n\t"
      "ds_read_b64_tr_b16 v[40:41], v227\n\t"
      "ds_read_b64_tr_b16 v[42:43], v227 offset:512\n\t"
      "ds_read_b64_tr_b16 v[44:45], v228\n\t"
      "ds_read_b64_tr_b16 v[46:47], v228 offset:512\n\t"
      "s_add_u32 m0, s21, 0\n\t"
      "s_nop 0\n\t"
      "global_load_lds_dwordx4 v224, s[6:7]\n\t"
      "global_load_lds_dwordx4 v221, s[6:7] offset:2048\n\t"
      "s_add_u32 m0, s21, 4096\n\t"
      "s_nop 0\n\t"
      "global_load_lds_dwordx4 v222, s[6:7]\n\t"
      "s_cbranch_vccnz WR4_%=\n\t"
      "global_load_lds_dwordx4 v223, s[6:7] offset:2048\n\t"
      "WR4_%=:\n\t"
      "s_sub_u32 s23, s23, 1\n\t"
      "s_cselect_b32 s48, -13, 1\n\t"
      "s_cselect_b32 s49, -1, 0\n\t"
      "s_mul_i32 s48, s48, 28672\n\t"
      "s_add_u32 s6, s6, s48\n\t"
      "s_addc_u32 s7, s7, s49\n\t"
      "s_cbranch_vccnz WW3_%=\n\t"
      "s_waitcnt vmcnt(4)\n\t"
      "s_branch WX3_%=\n\t"
      "WW3_%=:\n\t"
      "s_waitcnt vmcnt(3)\n\t"
      "WX3_%=:\n\t"
      "s_waitcnt lgkmcnt(0)\n\t"
      "s_barrier\n\t"
      "ds_read_b64_tr_b16 v[48:49], v225 offset:28672\n\t"
      "ds_read_b64_tr_b16 v[50:51], v225 offset:29184\n\t"
      "ds_read_b64_tr_b16 v[52:53], v226 offset:28672\n\t"
      "ds_read_b64_tr_b16 v[54:55], v226 offset:29184\n\t"
      "ds_read_b64_tr_b16 v[56:57], v227 offset:28672\n\t"
      "ds_read_b64_tr_b16 v[58:59], v227 offset:29184\n\t"
      "ds_read_b64_tr_b16 v[60:61], v228 offset:28672\n\t"
      "ds_read_b64_tr_b16 v[62:63], v228 offset:29184\n\t"
      "s_add_u32 m0, s21, 28672\n\t"
      "s_nop 0\n\t"
      "global_load_lds_dwordx4 v224, s[6:7]\n\t"
      "global_load_lds_dwordx4 v221, s[6:7] offset:2048\n\t"
      "s_add_u32 m0, s21, 32768\n\t"
      "s_nop 0\n\t"
      "global_load_lds_dwordx4 v222, s[6:7]\n\t"
      "s_cbranch_vccnz WR5_%=\n\t"
      "global_load_lds_dwordx4 v223, s[6:7] offset:2048\n\t"
      "WR5_%=:\n\t"
      "s_sub_u32 s23, s23, 1\n\t"
      "s_cselect_b32 s48, -13, 1\n\t"
      "s_cselect_b32 s49, -1, 0\n\t"
      "s_mul_i32 s48, s48, 28672\n\t"
      "s_add_u32 s6, s6, s48\n\t"
      "s_addc_u32 s7, s7, s49\n\t"
      "v_add_u32_e32 v225, 4294909952, v225\n\t"
      "v_add_u32_e32 v226, 4294909952, v226\n\t"
      "v_add_u32_e32 v227, 4294909952, v227\n\t"
      "v_add_u32_e32 v228, 4294909952, v228\n\t"
      "s_cbranch_vccnz WW4_%=\n\t"
      "s_waitcnt vmcnt(4)\n\t"
      "s_branch WX4_%=\n\t"
      "WW4_%=:\n\t"
      "s_waitcnt vmcnt(3)\n\t"
      "WX4_%=:\n\t"
      "s_waitcnt lgkmcnt(0)\n\t"
      "s_barrier\n\t"
      "ds_read_b64_tr_b16 v[64:65], v225\n\t"
      "ds_read_b64_tr_b16 v[66:67], v225 offset:512\n\t"
      "ds_read_b64_tr_b16 v[68:69], v226\n\t"
      "ds_read_b64_tr_b16 v[70:71], v226 offset:512\n\t"
      "ds_read_b64_tr_b16 v[72:73], v227\n\t"
      "ds_read_b64_tr_b16 v[74:75], v227 offset:512\n\t"
      "ds_read_b64_tr_b16 v[76:77], v228\n\t"
      "ds_read_b64_tr_b16 v[78:79], v228 offset:512\n\t"
      "s_add_u32 m0, s21, 57344\n\t"
      "s_nop 0\n\t"
      "global_load_lds_dwordx4 v224, s[6:7]\n\t"
      "global_load_lds_dwordx4 v221, s[6:7] offset:2048\n\t"
      "s_add_u32 m0, s21, 61440\n\t"
      "s_nop 0\n\t"
      "global_load_lds_dwordx4 v222, s[6:7]\n\t"
      "s_cbranch_vccnz WR6_%=\n\t"
      "global_load_lds_dwordx4 v223, s[6:7] offset:2048\n\t"
      "WR6_%=:\n\t"
      "s_sub_u32 s23, s23, 1\n\t"
      "s_cselect_b32 s48, -13, 1\n\t"
      "s_cselect_b32 s49, -1, 0\n\t"
      "s_mul_i32 s48, s48, 28672\n\t"
      "s_add_u32 s6, s6, s48\n\t"
      "s_addc_u32 s7, s7, s49\n\t"
      "s_cbranch_vccnz WW5_%=\n\t"
      "s_waitcnt vmcnt(4)\n\t"
      "s_branch WX5_%=\n\t"
      "WW5_%=:\n\t"
      "s_waitcnt vmcnt(3)\n\t"
      "WX5_%=:\n\t"
      "s_waitcnt lgkmcnt(0)\n\t"
      "s_barrier\n\t"
      "ds_read_b64_tr_b16 v[80:81], v225 offset:28672\n\t"
      "ds_read_b64_tr_b16 v[82:83], v225 offset:29184\n\t"
      "ds_read_b64_tr_b16 v[84:85], v226 offset:28672\n\t"
      "ds_read_b64_tr_b16 v[86:87], v226 offset:29184\n\t"
      "ds_read_b64_tr_b16 v[88:89], v227 offset:28672\n\t"
      "ds_read_b64_tr_b16 v[90:91], v227 offset:29184\n\t"
      "ds_read_b64_tr_b16 v[92:93], v228 offset:28672\n\t"
      "ds_read_b64_tr_b16 v[94:95], v228 offset:29184\n\t"
      "s_add_u32 m0, s21, 86016\n\t"
      "s_nop 0\n\t"
      "global_load_lds_dwordx4 v224, s[6:7]\n\t"
      "global_load_lds_dwordx4 v221, s[6:7] offset:2048\n\t"
      "s_add_u32 m0, s21, 90112\n\t"
      "s_nop 0\n\t"
      "global_load_lds_dwordx4 v222, s[6:7]\n\t"
      "s_cbranch_vccnz WR7_%=\n\t"
      "global_load_lds_dwordx4 v223, s[6:7] offset:2048\n\t"
      "WR7_%=:\n\t"
      "s_sub_u32 s23, s23, 1\n\t"
      "s_cselect_b32 s48, -13, 1\n\t"
      "s_cselect_b32 s49, -1, 0\n\t"
      "s_mul_i32 s48, s48, 28672\n\t"
      "s_add_u32 s6, s6, s48\n\t"
      "s_addc_u32 s7, s7, s49\n\t"
      "v_add_u32_e32 v225, 57344, v225\n\t"
      "v_add_u32_e32 v226, 57344, v226\n\t"
      "v_add_u32_e32 v227, 57344, v227\n\t"
      "v_add_u32_e32 v228, 57344, v228\n\t"
      "s_cbranch_vccnz WW6_%=\n\t"
      "s_waitcnt vmcnt(4)\n\t"
      "s_branch WX6_%=\n\t"
      "WW6_%=:\n\t"
      "s_waitcnt vmcnt(3)\n\t"
      "WX6_%=:\n\t"
      "s_waitcnt lgkmcnt(0)\n\t"
      "s_barrier\n\t"
      "ds_read_b64_tr_b16 v[96:97], v225\n\t"
      "ds_read_b64_tr_b16 v[98:99], v225 offset:512\n\t"
      "ds_read_b64_tr_b16 v[100:101], v226\n\t"
      "ds_read_b64_tr_b16 v[102:103], v226 offset:512\n\t"
      "ds_read_b64_tr_b16 v[104:105], v227\n\t"
      "ds_read_b64_tr_b16 v[106:107], v227 offset:512\n\t"
      "ds_read_b64_tr_b16 v[108:109], v228\n\t"
      "ds_read_b64_tr_b16 v[110:111], v228 offset:512\n\t"
      "s_add_u32 m0, s21, 0\n\t"
      "s_nop 0\n\t"
      "global_load_lds_dwordx4 v224, s[6:7]\n\t"
      "global_load_lds_dwordx4 v221, s[6:7] offset:2048\n\t"
      "s_add_u32 m0, s21, 4096\n\t"
      "s_nop 0\n\t"
      "global_load_lds_dwordx4 v222, s[6:7]\n\t"
      "s_cbranch_vccnz WR8_%=\n\t"
      "global_load_lds_dwordx4 v223, s[6:7] offset:2048\n\t"
      "WR8_%=:\n\t"
      "s_sub_u32 s23, s23, 1\n\t"
      "s_cselect_b32 s48, -13, 1\n\t"
      "s_cselect_b32 s49, -1, 0\n\t"
      "s_mul_i32 s48, s48, 28672\n\t"
      "s_add_u32 s6, s6, s48\n\t"
      "s_addc_u32 s7, s7, s49\n\t"
      "s_cbranch_vccnz WW7_%=\n\t"
      "s_waitcnt vmcnt(4)\n\t"
      "s_branch WX7_%=\n\t"
      "WW7_%=:\n\t"
      "s_waitcnt vmcnt(3)\n\t"
      "WX7_%=:\n\t"
      "s_waitcnt lgkmcnt(0)\n\t"
      "s_barrier\n\t"
      "ds_read_b64_tr_b16 v[112:113], v225 offset:28672\n\t"
      "ds_read_b64_tr_b16 v[114:115], v225 offset:29184\n\t"
      "ds_read_b64_tr_b16 v[116:117], v226 offset:28672\n\t"
      "ds_read_b64_tr_b16 v[118:119], v226 offset:29184\n\t"
      "ds_read_b64_tr_b16 v[120:121], v227 offset:28672\n\t"
      "ds_read_b64_tr_b16 v[122:123], v227 offset:29184\n\t"
      "ds_read_b64_tr_b16 v[124:125], v228 offset:28672\n\t"
      "ds_read_b64_tr_b16 v[126:127], v228 offset:29184\n\t"
      "s_add_u32 m0, s21, 28672\n\t"
      "s_nop 0\n\t"
      "global_load_lds_dwordx4 v224, s[6:7]\n\t"
      "global_load_lds_dwordx4 v221, s[6:7] offset:2048\n\t"
      "s_add_u32 m0, s21, 32768\n\t"
      "s_nop 0\n\t"
      "global_load_lds_dwordx4 v222, s[6:7]\n\t"
      "s_cbranch_vccnz WR9_%=\n\t"
      "global_load_lds_dwordx4 v223, s[6:7] offset:2048\n\t"
      "WR9_%=:\n\t"
      "s_sub_u32 s23, s23, 1\n\t"
      "s_cselect_b32 s48, -13, 1\n\t"
      "s_cselect_b32 s49, -1, 0\n\t"
      "s_mul_i32 s48, s48, 28672\n\t"
      "s_add_u32 s6, s6, s48\n\t"
      "s_addc_u32 s7, s7, s49\n\t"
      "v_add_u32_e32 v225, 4294909952, v225\n\t"
      "v_add_u32_e32 v226, 4294909952, v226\n\t"
      "v_add_u32_e32 v227, 4294909952, v227\n\t"
      "v_add_u32_e32 v228, 4294909952, v228\n\t"
      "ds_read_b64_tr_b16 v[204:205], v229\n\t"
      "ds_read_b64_tr_b16 v[206:207], v229 offset:32\n\t"
      "ds_read_b64_tr_b16 v[208:209], v229 offset:64\n\t"
      "ds_read_b64_tr_b16 v[210:211], v229 offset:96\n\t"
      "s_waitcnt lgkmcnt(0)\n\t"
      "v_and_b32_e32 v210, s22, v210\n\t"
      "v_and_b32_e32 v211, s22, v211\n\t"
      "v_lshlrev_b32_e32 v160, 16, v204\n\t"
      "v_and_b32_e32 v161, 0xffff0000, v204\n\t"
      "v_lshlrev_b32_e32 v162, 16, v205\n\t"
      "v_and_b32_e32 v163, 0xffff0000, v205\n\t"
      "v_lshlrev_b32_e32 v164, 16, v206\n\t"
      "v_and_b32_e32 v165, 0xffff0000, v206\n\t"
      "v_lshlrev_b32_e32 v166, 16, v207\n\t"
      "v_and_b32_e32 v167, 0xffff0000, v207\n\t"
      "v_lshlrev_b32_e32 v168, 16, v208\n\t"
      "v_and_b32_e32 v169, 0xffff0000, v208\n\t"
      "v_lshlrev_b32_e32 v170, 16, v209\n\t"
      "v_and_b32_e32 v171, 0xffff0000, v209\n\t"
      "v_lshlrev_b32_e32 v172, 16, v210\n\t"
      "v_and_b32_e32 v173, 0xffff0000, v210\n\t"
      "v_lshlrev_b32_e32 v174, 16, v211\n\t"
      "v_and_b32_e32 v175, 0xffff0000, v211\n\t"
      "s_cbranch_vccnz WW8_%=\n\t"
      "s_waitcnt vmcnt(4)\n\t"
      "s_branch WX8_%=\n\t"
      "WW8_%=:\n\t"
      "s_waitcnt vmcnt(3)\n\t"
      "WX8_%=:\n\t"
      "s_waitcnt lgkmcnt(0)\n\t"
      "s_barrier\n\t"
      "ds_read_b64_tr_b16 v[128:129], v225\n\t"
      "ds_read_b64_tr_b16 v[130:131], v225 offset:512\n\t"
      "ds_read_b64_tr_b16 v[132:133], v226\n\t"
      "ds_read_b64_tr_b16 v[134:135], v226 offset:512\n\t"
      "ds_read_b64_tr_b16 v[136:137], v227\n\t"
      "ds_read_b64_tr_b16 v[138:139], v227 offset:512\n\t"
      "ds_read_b64_tr_b16 v[140:141], v228\n\t"
      "ds_read_b64_tr_b16 v[142:143], v228 offset:512\n\t"
      "s_add_u32 m0, s21, 57344\n\t"
      "s_nop 0\n\t"
      "global_load_lds_dwordx4 v224, s[6:7]\n\t"
      "global_load_lds_dwordx4 v221, s[6:7] offset:2048\n\t"
      "s_add_u32 m0, s21, 61440\n\t"
      "s_nop 0\n\t"
      "global_load_lds_dwordx4 v222, s[6:7]\n\t"
      "s_cbranch_vccnz WR10_%=\n\t"
      "global_load_lds_dwordx4 v223, s[6:7] offset:2048\n\t"
      "WR10_%=:\n\t"
      "s_sub_u32 s23, s23, 1\n\t"
      "s_cselect_b32 s48, -13, 1\n\t"
      "s_cselect_b32 s49, -1, 0\n\t"
      "s_mul_i32 s48, s48, 28672\n\t"
      "s_add_u32 s6, s6, s48\n\t"
      "s_addc_u32 s7, s7, s49\n\t"
      "s_cbranch_vccnz WW9_%=\n\t"
      "s_waitcnt vmcnt(4)\n\t"
      "s_branch WX9_%=\n\t"
      "WW9_%=:\n\t"
      "s_waitcnt vmcnt(3)\n\t"
      "WX9_%=:\n\t"
      "s_waitcnt lgkmcnt(0)\n\t"
      "s_barrier\n\t"
      "ds_read_b64_tr_b16 v[144:145], v225 offset:28672\n\t"
      "ds_read_b64_tr_b16 v[146:147], v225 offset:29184\n\t"
      "ds_read_b64_tr_b16 v[148:149], v226 offset:28672\n\t"
      "ds_read_b64_tr_b16 v[150:151], v226 offset:29184\n\t"
      "ds_read_b64_tr_b16 v[152:153], v227 offset:28672\n\t"
      "ds_read_b64_tr_b16 v[154:155], v227 offset:29184\n\t"
      "ds_read_b64_tr_b16 v[156:157], v228 offset:28672\n\t"
      "ds_read_b64_tr_b16 v[158:159], v228 offset:29184\n\t"
      "s_add_u32 m0, s21, 86016\n\t"
      "s_nop 0\n\t"
      "global_load_lds_dwordx4 v224, s[6:7]\n\t"
      "global_load_lds_dwordx4 v221, s[6:7] offset:2048\n\t"
      "s_add_u32 m0, s21, 90112\n\t"
      "s_nop 0\n\t"
      "global_load_lds_dwordx4 v222, s[6:7]\n\t"
      "s_cbranch_vccnz WR11_%=\n\t"
      "global_load_lds_dwordx4 v223, s[6:7] offset:2048\n\t"
      "WR11_%=:\n\t"
      "s_sub_u32 s23, s23, 1\n\t"
      "s_cselect_b32 s48, -13, 1\n\t"
      "s_cselect_b32 s49, -1, 0\n\t"
      "s_mul_i32 s48, s48, 28672\n\t"
      "s_add_u32 s6, s6, s48\n\t"
      "s_addc_u32 s7, s7, s49\n\t"
      "v_add_u32_e32 v225, 57344, v225\n\t"
      "v_add_u32_e32 v226, 57344, v226\n\t"
      "v_add_u32_e32 v227, 57344, v227\n\t"
      "v_add_u32_e32 v228, 57344, v228\n\t"
      "s_waitcnt lgkmcnt(0)\n\t"
      "s_barrier\n\t"
      "s_add_u32 m0, s21, 0\n\t"
      "s_nop 0\n\t"
      "global_load_lds_dwordx4 v224, s[6:7]\n\t"
      "global_load_lds_dwordx4 v221, s[6:7] offset:2048\n\t"
      "s_add_u32 m0, s21, 4096\n\t"
      "s_nop 0\n\t"
      "global_load_lds_dwordx4 v222, s[6:7]\n\t"
      "s_cbranch_vccnz WR12_%=\n\t"
      "global_load_lds_dwordx4 v223, s[6:7] offset:2048\n\t"
      "WR12_%=:\n\t"
      "s_sub_u32 s23, s23, 1\n\t"
      "s_cselect_b32 s48, -13, 1\n\t"
      "s_cselect_b32 s49, -1, 0\n\t"
      "s_mul_i32 s48, s48, 28672\n\t"
      "s_add_u32 s6, s6, s48\n\t"
      "s_addc_u32 s7, s7, s49\n\t"
      "s_add_u32 m0, s21, 28672\n\t"
      "s_nop 0\n\t"
      "global_load_lds_dwordx4 v224, s[6:7]\n\t"
      "global_load_lds_dwordx4 v221, s[6:7] offset:2048\n\t"
      "s_add_u32 m0, s21, 32768\n\t"
      "s_nop 0\n\t"
      "global_load_lds_dwordx4 v222, s[6:7]\n\t"
      "s_cbranch_vccnz WR13_%=\n\t"
      "global_load_lds_dwordx4 v223, s[6:7] offset:2048\n\t"
      "WR13_%=:\n\t"
      "s_waitcnt vmcnt(0)\n\t"
      "s_mov_b32 s51, 0\n\t"
      "s_branch QE_%=\n\t"
      ".p2align 6\n\t"
      "QL_%=:\n\t"
      "s_sleep 2\n\t"
      "s_add_u32 s51, s51, 1\n\t"
      "s_cmp_lt_u32 s51, 262144\n\t"
      "s_cbranch_scc0 QX_%=\n\t"
      "QE_%=:\n\t"
      "s_mov_b64 exec, 3\n\t"
      "v_mov_b64_e32 v[188:189], 0\n\t"
      "global_atomic_or_x2 v[188:189], v246, v[188:189], s[8:9] sc0 sc1\n\t"
      "s_waitcnt vmcnt(0)\n\t"
      "v_cmp_ne_u64_e32 vcc, s[12:13], v[188:189]\n\t"
      "s_mov_b64 exec, -1\n\t"
      "s_cbranch_vccnz QL_%=\n\t"
      "QX_%=:\n\t"
      "s_mov_b32 s51, 0\n\t"
      "s_branch OE_%=\n\t"
      "OL_%=:\n\t"
      "s_sleep 2\n\t"
      "s_add_u32 s51, s51, 1\n\t"
      "s_cmp_lt_u32 s51, 262144\n\t"
      "s_cbranch_scc0 OX_%=\n\t"
      "OE_%=:\n\t"
      "global_load_dwordx4 v[188:191], v236, s[10:11] sc1\n\t"
      "global_load_dwordx4 v[192:195], v236, s[10:11] offset:1024 sc1\n\t"
      "s_waitcnt vmcnt(0)\n\t"
      "v_max3_u32 v196, v188, v189, v190\n\t"
      "v_max3_u32 v197, v191, v192, v193\n\t"
      "v_max3_u32 v196, v196, v194, v195\n\t"
      "v_max_u32_e32 v196, v196, v197\n\t"
      "v_cmp_eq_u32_e32 vcc, -1, v196\n\t"
      "s_cbranch_vccnz OL_%=\n\t"
      "OX_%=:\n\t"
      "ds_write_b64 v240, v[188:189]\n\t"
      "ds_write_b64 v242, v[192:193]\n\t"
      "s_mov_b64 exec, s[44:45]\n\t"
      "ds_write_b64 v241, v[190:191]\n\t"
      "ds_write_b64 v243, v[194:195]\n\t"
      "s_mov_b64 exec, -1\n\t"
      "s_mov_b64 vcc, s[46:47]\n\t"
      "s_waitcnt lgkmcnt(0)\n\t"
      "s_barrier\n\t"
      "ds_read_b128 v[176:179], v230\n\t"
      "ds_read_b128 v[180:183], v230 offset:64\n\t"
      "ds_read_b128 v[184:187], v230 offset:128\n\t"
      "s_waitcnt lgkmcnt(2)\n\t"
      "v_mfma_f32_16x16x32_bf16 v[160:163], v[176:179], v[0:3], v[160:163]\n\t"
      "v_mfma_f32_16x16x32_bf16 v[164:167], v[176:179], v[4:7], v[164:167]\n\t"
      "v_mfma_f32_16x16x32_bf16 v[168:171], v[176:179], v[8:11], v[168:171]\n\t"
      "s_cbranch_vccnz M0_%=\n\t"
      "v_mfma_f32_16x16x32_bf16 v[172:175], v[176:179], v[12:15], v[172:175]\n\t"
      "M0_%=:\n\t"
      "global_store_dwordx4 v231, v[248:251], s[14:15] sc1\n\t"
      "ds_read_b128 v[176:179], v230 offset:192\n\t"
      "s_waitcnt lgkmcnt(2)\n\t"
      "v_mfma_f32_16x16x32_bf16 v[160:163], v[180:183], v[16:19], v[160:163]\n\t"
      "v_mfma_f32_16x16x32_bf16 v[164:167], v[180:183], v[20:23], v[164:167]\n\t"
      "v_mfma_f32_16x16x32_bf16 v[168:171], v[180:183], v[24:27], v[168:171]\n\t"
      "s_cbranch_vccnz M1_%=\n\t"
      "v_mfma_f32_16x16x32_bf16 v[172:175], v[180:183], v[28:31], v[172:175]\n\t"
      "M1_%=:\n\t"
      "s_mov_b64 exec, s[24:25]\n\t"
      "global_store_dwordx4 v237, v[248:251], s[14:15] sc1\n\t"
      "s_mov_b64 exec, -1\n\t"
      "ds_read_b128 v[180:183], v230 offset:256\n\t"
      "s_waitcnt lgkmcnt(2)\n\t"
      "v_mfma_f32_16x16x32_bf16 v[160:163], v[184:187], v[32:35], v[160:163]\n\t"
      "v_mfma_f32_16x16x32_bf16 v[164:167], v[184:187], v[36:39], v[164:167]\n\t"
      "v_mfma_f32_16x16x32_bf16 v[168:171], v[184:187], v[40:43], v[168:171]\n\t"
      "s_cbranch_vccnz M2_%=\n\t"
      "v_mfma_f32_16x16x32_bf16 v[172:175], v[184:187], v[44:47], v[172:175]\n\t"
      "M2_%=:\n\t"
      "ds_read_b64_tr_b16 v[188:189], v225\n\t"
      "ds_read_b64_tr_b16 v[190:191], v225 offset:512\n\t"
      "global_store_dwordx4 v236, v[248:251], s[16:17] sc1\n\t"
      "ds_read_b128 v[184:187], v230 offset:320\n\t"
      "s_waitcnt lgkmcnt(4)\n\t"
      "v_mfma_f32_16x16x32_bf16 v[160:163], v[176:179], v[48:51], v[160:163]\n\t"
      "v_mfma_f32_16x16x32_bf16 v[164:167], v[176:179], v[52:55], v[164:167]\n\t"
      "v_mfma_f32_16x16x32_bf16 v[168:171], v[176:179], v[56:59], v[168:171]\n\t"
      "s_cbranch_vccnz M3_%=\n\t"
      "v_mfma_f32_16x16x32_bf16 v[172:175], v[176:179], v[60:63], v[172:175]\n\t"
      "M3_%=:\n\t"
      "ds_read_b64_tr_b16 v[192:193], v226\n\t"
      "ds_read_b64_tr_b16 v[194:195], v226 offset:512\n\t"
      "s_mov_b32 exec_hi, 3\n\t"
      "global_store_dwordx4 v236, v[248:251], s[16:17] offset:1024 sc1\n\t"
      "s_mov_b32 exec_hi, -1\n\t"
      "ds_read_b128 v[176:179], v230 offset:384\n\t"
      "s_waitcnt lgkmcnt(6)\n\t"
      "v_mfma_f32_16x16x32_bf16 v[160:163], v[180:183], v[64:67], v[160:163]\n\t"
      "v_mfma_f32_16x16x32_bf16 v[164:167], v[180:183], v[68:71], v[164:167]\n\t"
      "v_mfma_f32_16x16x32_bf16 v[168:171], v[180:183], v[72:75], v[168:171]\n\t"
      "s_cbranch_vccnz M4_%=\n\t"
      "v_mfma_f32_16x16x32_bf16 v[172:175], v[180:183], v[76:79], v[172:175]\n\t"
      "M4_%=:\n\t"
      "ds_read_b64_tr_b16 v[196:197], v227\n\t"
      "ds_read_b64_tr_b16 v[198:199], v227 offset:512\n\t"
      "ds_read_b128 v[180:183], v230 offset:448\n\t"
      "s_waitcnt lgkmcnt(6)\n\t"
      "v_mfma_f32_16x16x32_bf16 v[160:163], v[184:187], v[80:83], v[160:163]\n\t"
      "v_mfma_f32_16x16x32_bf16 v[164:167], v[184:187], v[84:87], v[164:167]\n\t"
      "v_mfma_f32_16x16x32_bf16 v[168:171], v[184:187], v[88:91], v[168:171]\n\t"
      "s_cbranch_vccnz M5_%=\n\t"
      "v_mfma_f32_16x16x32_bf16 v[172:175], v[184:187], v[92:95], v[172:175]\n\t"
      "M5_%=:\n\t"
      "ds_read_b64_tr_b16 v[200:201], v228\n\t"
      "ds_read_b64_tr_b16 v[202:203], v228 offset:512\n\t"
      "ds_read_b128 v[184:187], v230 offset:512\n\t"
      "s_waitcnt lgkmcnt(6)\n\t"
      "v_mfma_f32_16x16x32_bf16 v[160:163], v[176:179], v[96:99], v[160:163]\n\t"
      "v_mfma_f32_16x16x32_bf16 v[164:167], v[176:179], v[100:103], v[164:167]\n\t"
      "v_mfma_f32_16x16x32_bf16 v[168:171], v[176:179], v[104:107], v[168:171]\n\t"
      "s_cbranch_vccnz M6_%=\n\t"
      "v_mfma_f32_16x16x32_bf16 v[172:175], v[176:179], v[108:111], v[172:175]\n\t"
      "M6_%=:\n\t"
      "ds_read_b64_tr_b16 v[204:205], v225 offset:28672\n\t"
      "ds_read_b64_tr_b16 v[206:207], v225 offset:29184\n\t"
      "ds_read_b128 v[176:179], v230 offset:576\n\t"
      "s_waitcnt lgkmcnt(6)\n\t"
      "v_mfma_f32_16x16x32_bf16 v[160:163], v[180:183], v[112:115], v[160:163]\n\t"
      "v_mfma_f32_16x16x32_bf16 v[164:167], v[180:183], v[116:119], v[164:167]\n\t"
      "v_mfma_f32_16x16x32_bf16 v[168:171], v[180:183], v[120:123], v[168:171]\n\t"
      "s_cbranch_vccnz M7_%=\n\t"
      "v_mfma_f32_16x16x32_bf16 v[172:175], v[180:183], v[124:127], v[172:175]\n\t"
      "M7_%=:\n\t"
      "ds_read_b64_tr_b16 v[208:209], v226 offset:28672\n\t"
      "ds_read_b64_tr_b16 v[210:211], v226 offset:29184\n\t"
      "ds_read_b128 v[180:183], v230 offset:640\n\t"
      "s_waitcnt lgkmcnt(6)\n\t"
      "v_mfma_f32_16x16x32_bf16 v[160:163], v[184:187], v[128:131], v[160:163]\n\t"
      "v_mfma_f32_16x16x32_bf16 v[164:167], v[184:187], v[132:135], v[164:167]\n\t"
      "v_mfma_f32_16x16x32_bf16 v[168:171], v[184:187], v[136:139], v[168:171]\n\t"
      "s_cbranch_vccnz M8_%=\n\t"
      "v_mfma_f32_16x16x32_bf16 v[172:175], v[184:187], v[140:143], v[172:175]\n\t"
      "M8_%=:\n\t"
      "ds_read_b64_tr_b16 v[212:213], v227 offset:28672\n\t"
      "ds_read_b64_tr_b16 v[214:215], v227 offset:29184\n\t"
      "ds_read_b128 v[184:187], v230 offset:704\n\t"
      "s_waitcnt lgkmcnt(6)\n\t"
      "v_mfma_f32_16x16x32_bf16 v[160:163], v[176:179], v[144:147], v[160:163]\n\t"
      "v_mfma_f32_16x16x32_bf16 v[164:167], v[176:179], v[148:151], v[164:167]\n\t"
      "v_mfma_f32_16x16x32_bf16 v[168:171], v[176:179], v[152:155], v[168:171]\n\t"
      "s_cbranch_vccnz M9_%=\n\t"
      "v_mfma_f32_16x16x32_bf16 v[172:175], v[176:179], v[156:159], v[172:175]\n\t"
      "M9_%=:\n\t"
      "ds_read_b64_tr_b16 v[216:217], v228 offset:28672\n\t"
      "ds_read_b64_tr_b16 v[218:219], v228 offset:29184\n\t"
      "ds_read_b128 v[176:179], v230 offset:768\n\t"
      "s_waitcnt lgkmcnt(6)\n\t"
      "v_mfma_f32_16x16x32_bf16 v[160:163], v[180:183], v[188:191], v[160:163]\n\t"
      "v_add_u32_e32 v225, 4294909952, v225\n\t"
      "v_add_u32_e32 v226, 4294909952, v226\n\t"
      "v_add_u32_e32 v227, 4294909952, v227\n\t"
      "v_add_u32_e32 v228, 4294909952, v228\n\t"
      "ds_read_b64_tr_b16 v[188:189], v225\n\t"
      "ds_read_b64_tr_b16 v[190:191], v225 offset:512\n\t"
      "v_mfma_f32_16x16x32_bf16 v[164:167], v[180:183], v[192:195], v[164:167]\n\t"
      "ds_read_b64_tr_b16 v[192:193], v226\n\t"
      "ds_read_b64_tr_b16 v[194:195], v226 offset:512\n\t"
      "v_mfma_f32_16x16x32_bf16 v[168:171], v[180:183], v[196:199], v[168:171]\n\t"
      "ds_read_b64_tr_b16 v[196:197], v227\n\t"
      "ds_read_b64_tr_b16 v[198:199], v227 offset:512\n\t"
      "s_cbranch_vccnz M10_%=\n\t"
      "v_mfma_f32_16x16x32_bf16 v[172:175], v[180:183], v[200:203], v[172:175]\n\t"
      "M10_%=:\n\t"
      "ds_read_b64_tr_b16 v[200:201], v228\n\t"
      "ds_read_b64_tr_b16 v[202:203], v228 offset:512\n\t"
      "ds_read_b128 v[180:183], v230 offset:832\n\t"
      "s_waitcnt lgkmcnt(12)\n\t"
      "v_mfma_f32_16x16x32_bf16 v[160:163], v[184:187], v[204:207], v[160:163]\n\t"
      "ds_read_b64_tr_b16 v[204:205], v225 offset:28672\n\t"
      "ds_read_b64_tr_b16 v[206:207], v225 offset:29184\n\t"
      "v_mfma_f32_16x16x32_bf16 v[164:167], v[184:187], v[208:211], v[164:167]\n\t"
      "ds_read_b64_tr_b16 v[208:209], v226 offset:28672\n\t"
      "ds_read_b64_tr_b16 v[210:211], v226 offset:29184\n\t"
      "v_mfma_f32_16x16x32_bf16 v[168:171], v[184:187], v[212:215], v[168:171]\n\t"
      "ds_read_b64_tr_b16 v[212:213], v227 offset:28672\n\t"
      "ds_read_b64_tr_b16 v[214:215], v227 offset:29184\n\t"
      "s_waitcnt lgkmcnt(14)\n\t"
      "s_cbranch_vccnz M11_%=\n\t"
      "v_mfma_f32_16x16x32_bf16 v[172:175], v[184:187], v[216:219], v[172:175]\n\t"
      "M11_%=:\n\t"
      "ds_read_b64_tr_b16 v[216:217], v228 offset:28672\n\t"
      "ds_read_b64_tr_b16 v[218:219], v228 offset:29184\n\t"
      "s_waitcnt lgkmcnt(14)\n\t"
      "s_waitcnt lgkmcnt(14)\n\t"
      "v_mfma_f32_16x16x32_bf16 v[160:163], v[176:179], v[188:191], v[160:163]\n\t"
      "s_waitcnt lgkmcnt(13)\n\t"
      "v_mfma_f32_16x16x32_bf16 v[164:167], v[176:179], v[192:195], v[164:167]\n\t"
      "s_waitcnt lgkmcnt(11)\n\t"
      "v_mfma_f32_16x16x32_bf16 v[168:171], v[176:179], v[196:199], v[168:171]\n\t"
      "s_waitcnt lgkmcnt(9)\n\t"
      "s_cbranch_vccnz M12_%=\n\t"
      "v_mfma_f32_16x16x32_bf16 v[172:175], v[176:179], v[200:203], v[172:175]\n\t"
      "M12_%=:\n\t"
      "s_waitcnt lgkmcnt(8)\n\t"
      "s_waitcnt lgkmcnt(6)\n\t"
      "v_mfma_f32_16x16x32_bf16 v[160:163], v[180:183], v[204:207], v[160:163]\n\t"
      "s_waitcnt lgkmcnt(4)\n\t"
      "v_mfma_f32_16x16x32_bf16 v[164:167], v[180:183], v[208:211], v[164:167]\n\t"
      "s_waitcnt lgkmcnt(2)\n\t"
      "v_mfma_f32_16x16x32_bf16 v[168:171], v[180:183], v[212:215], v[168:171]\n\t"
      "s_waitcnt lgkmcnt(0)\n\t"
      "s_cbranch_vccnz M13_%=\n\t"
      "v_mfma_f32_16x16x32_bf16 v[172:175], v[180:183], v[216:219], v[172:175]\n\t"
      "M13_%=:\n\t"
      "s_nop 3\n\t"
      "v_pk_add_f32 v[188:189], v[160:161], v[164:165]\n\t"
      "v_pk_mul_f32 v[192:193], v[160:161], v[160:161]\n\t"
      "v_pk_add_f32 v[190:191], v[162:163], v[166:167]\n\t"
      "v_pk_mul_f32 v[194:195], v[162:163], v[162:163]\n\t"
      "v_pk_fma_f32 v[192:193], v[164:165], v[164:165], v[192:193]\n\t"
      "v_pk_fma_f32 v[194:195], v[166:167], v[166:167], v[194:195]\n\t"
      "v_pk_add_f32 v[188:189], v[188:189], v[168:169]\n\t"
      "v_pk_fma_f32 v[192:193], v[168:169], v[168:169], v[192:193]\n\t"
      "v_pk_add_f32 v[190:191], v[190:191], v[170:171]\n\t"
      "v_pk_fma_f32 v[194:195], v[170:171], v[170:171], v[194:195]\n\t"
      "v_pk_add_f32 v[188:189], v[188:189], v[172:173]\n\t"
      "v_pk_fma_f32 v[192:193], v[172:173], v[172:173], v[192:193]\n\t"
      "v_pk_add_f32 v[190:191], v[190:191], v[174:175]\n\t"
      "v_pk_fma_f32 v[194:195], v[174:175], v[174:175], v[194:195]\n\t"
      "v_add_f32_dpp v196, v188, v188 row_ror:8 row_mask:0xf bank_mask:0x3\n\t"
      "s_nop 1\n\t"
      "v_add_f32_dpp v196, v190, v190 row_ror:8 row_mask:0xf bank_mask:0xc\n\t"
      "v_add_f32_dpp v197, v189, v189 row_ror:8 row_mask:0xf bank_mask:0x3\n\t"
      "s_nop 1\n\t"
      "v_add_f32_dpp v197, v191, v191 row_ror:8 row_mask:0xf bank_mask:0xc\n\t"
      "v_add_f32_dpp v198, v192, v192 row_ror:8 row_mask:0xf bank_mask:0x3\n\t"
      "s_nop 1\n\t"
      "v_add_f32_dpp v198, v194, v194 row_ror:8 row_mask:0xf bank_mask:0xc\n\t"
      "v_add_f32_dpp v199, v193, v193 row_ror:8 row_mask:0xf bank_mask:0x3\n\t"
      "s_nop 1\n\t"
      "v_add_f32_dpp v199, v195, v195 row_ror:8 row_mask:0xf bank_mask:0xc\n\t"
      "v_add_f32_dpp v188, v196, v196 row_shl:4 row_mask:0xf bank_mask:0x5\n\t"
      "s_nop 1\n\t"
      "v_add_f32_dpp v188, v197, v197 row_shr:4 row_mask:0xf bank_mask:0xa\n\t"
      "v_add_f32_dpp v189, v198, v198 row_shl:4 row_mask:0xf bank_mask:0x5\n\t"
      "s_nop 1\n\t"
      "v_add_f32_dpp v189, v199, v199 row_shr:4 row_mask:0xf bank_mask:0xa\n\t"
      "v_add_f32_dpp v188, v188, v188 quad_perm:[1,0,3,2] row_mask:0xf bank_mask:0xf\n\t"
      "s_nop 0\n\t"
      "v_add_f32_dpp v189, v189, v189 quad_perm:[1,0,3,2] row_mask:0xf bank_mask:0xf\n\t"
      "v_add_f32_dpp v188, v188, v188 quad_perm:[2,3,0,1] row_mask:0xf bank_mask:0xf\n\t"
      "s_nop 0\n\t"
      "v_add_f32_dpp v189, v189, v189 quad_perm:[2,3,0,1] row_mask:0xf bank_mask:0xf\n\t"
      "ds_write_b64 v232, v[188:189]\n\t"
      "v_cvt_pk_bf16_f32 v204, v160, v161\n\t"
      "v_cvt_pk_bf16_f32 v205, v162, v163\n\t"
      "v_cvt_pk_bf16_f32 v206, v164, v165\n\t"
      "v_cvt_pk_bf16_f32 v207, v166, v167\n\t"
      "s_waitcnt vmcnt(0)\n\t"
      "s_waitcnt lgkmcnt(0)\n\t"
      "s_barrier\n\t"
      "ds_read_b128 v[188:191], v233\n\t"
      "s_bitcmp1_b32 s20, 4\n\t"
      "s_cbranch_scc0 HI_%=\n\t"
      "v_mov_b32_e32 v220, s12\n\t"
      "v_mov_b32_e32 v221, s13\n\t"
      "s_mov_b64 exec, 255\n\t"
      "global_store_dwordx2 v239, v[220:221], s[8:9] sc1\n\t"
      "s_mov_b64 exec, -1\n\t"
      "HI_%=:\n\t"
      "v_cvt_pk_bf16_f32 v208, v168, v169\n\t"
      "v_cvt_pk_bf16_f32 v209, v170, v171\n\t"
      "v_cvt_pk_bf16_f32 v210, v172, v173\n\t"
      "v_cvt_pk_bf16_f32 v211, v174, v175\n\t"
      "s_waitcnt lgkmcnt(0)\n\t"
      "v_pk_add_f32 v[188:189], v[188:189], v[190:191]\n\t"
      "s_nop 1\n\t"
      "v_add_f32_dpp v188, v188, v188 row_ror:4 row_mask:0xf bank_mask:0xf\n\t"
      "v_add_f32_dpp v189, v189, v189 row_ror:4 row_mask:0xf bank_mask:0xf\n\t"
      "s_nop 0\n\t"
      "v_add_f32_dpp v188, v188, v188 row_ror:8 row_mask:0xf bank_mask:0xf\n\t"
      "v_add_f32_dpp v189, v189, v189 row_ror:8 row_mask:0xf bank_mask:0xf\n\t"
      "v_mul_f32_e32 v188, 0x3b124925, v188\n\t"
      "v_mul_f32_e32 v189, 0x3b124925, v189\n\t"
      "v_fma_f32 v189, -v188, v188, v189\n\t"
      "v_max_f32_e32 v189, 0, v189\n\t"
      "v_add_f32_e32 v190, 0x3727c5ac, v189\n\t"
      "v_fmac_f32_e32 v189, 0x3727c5ac, v190\n\t"
      "v_rsq_f32_e32 v189, v189\n\t"
      "s_nop 0\n\t"
      "v_mul_f32_e64 v188, -v188, v189\n\t"
      "v_mov_b32_dpp v192, v189 quad_perm:[0,0,0,0] row_mask:0xf bank_mask:0xf\n\t"
      "s_nop 0\n\t"
      "v_mov_b32_dpp v196, v188 quad_perm:[0,0,0,0] row_mask:0xf bank_mask:0xf\n\t"
      "v_mov_b32_dpp v193, v189 quad_perm:[1,1,1,1] row_mask:0xf bank_mask:0xf\n\t"
      "v_mov_b32_dpp v197, v188 quad_perm:[1,1,1,1] row_mask:0xf bank_mask:0xf\n\t"
      "v_mov_b32_dpp v194, v189 quad_perm:[2,2,2,2] row_mask:0xf bank_mask:0xf\n\t"
      "v_mov_b32_dpp v198, v188 quad_perm:[2,2,2,2] row_mask:0xf bank_mask:0xf\n\t"
      "v_mov_b32_dpp v195, v189 quad_perm:[3,3,3,3] row_mask:0xf bank_mask:0xf\n\t"
      "v_mov_b32_dpp v199, v188 quad_perm:[3,3,3,3] row_mask:0xf bank_mask:0xf\n\t"
      "v_pk_fma_f32 v[160:161], v[160:161], v[192:193], v[196:197]\n\t"
      "v_pk_fma_f32 v[162:163], v[162:163], v[194:195], v[198:199]\n\t"
      "v_pk_fma_f32 v[164:165], v[164:165], v[192:193], v[196:197]\n\t"
      "v_pk_fma_f32 v[166:167], v[166:167], v[194:195], v[198:199]\n\t"
      "v_pk_fma_f32 v[168:169], v[168:169], v[192:193], v[196:197]\n\t"
      "v_pk_fma_f32 v[170:171], v[170:171], v[194:195], v[198:199]\n\t"
      "v_pk_fma_f32 v[172:173], v[172:173], v[192:193], v[196:197]\n\t"
      "v_pk_fma_f32 v[174:175], v[174:175], v[194:195], v[198:199]\n\t"
      "v_cvt_pk_bf16_f32 v212, v160, v161\n\t"
      "v_cvt_pk_bf16_f32 v213, v162, v163\n\t"
      "v_cvt_pk_bf16_f32 v214, v164, v165\n\t"
      "v_cvt_pk_bf16_f32 v215, v166, v167\n\t"
      "v_cvt_pk_bf16_f32 v216, v168, v169\n\t"
      "v_cvt_pk_bf16_f32 v217, v170, v171\n\t"
      "v_cvt_pk_bf16_f32 v218, v172, v173\n\t"
      "v_cvt_pk_bf16_f32 v219, v174, v175\n\t"
      "v_min_u32_e32 v212, -2, v212\n\t"
      "v_min_u32_e32 v213, -2, v213\n\t"
      "v_min_u32_e32 v214, -2, v214\n\t"
      "v_min_u32_e32 v215, -2, v215\n\t"
      "v_min_u32_e32 v216, -2, v216\n\t"
      "v_min_u32_e32 v217, -2, v217\n\t"
      "v_min_u32_e32 v218, -2, v218\n\t"
      "v_min_u32_e32 v219, -2, v219\n\t"
      "v_permlane16_swap_b32_e32 v212, v214\n\t"
      "v_permlane16_swap_b32_e32 v213, v215\n\t"
      "v_permlane16_swap_b32_e32 v216, v218\n\t"
      "v_permlane16_swap_b32_e32 v217, v219\n\t"
      "global_store_dwordx4 v231, v[212:215], s[14:15] sc1\n\t"
      "s_mov_b64 exec, s[24:25]\n\t"
      "global_store_dwordx4 v237, v[216:219], s[14:15] sc1\n\t"
      "s_mov_b64 exec, -1\n\t"
      "v_permlane16_swap_b32_e32 v204, v206\n\t"
      "v_permlane16_swap_b32_e32 v205, v207\n\t"
      "v_permlane16_swap_b32_e32 v208, v210\n\t"
      "v_permlane16_swap_b32_e32 v209, v211\n\t"
      "global_store_dwordx4 v231, v[204:207], s[40:41] sc1\n\t"
      "s_mov_b64 exec, s[24:25]\n\t"
      "global_store_dwordx4 v237, v[208:211], s[40:41] sc1\n\t"
      "s_mov_b64 exec, -1\n\t"
      "s_nop 1\n\t"
      : "+{s[4:19]}"(s), "+{s[20:27]}"(s2), "+{s[28:29]}"(pb1), "+{s[30:31]}"(pb2), "+{s[32:33]}"(pob), "+{s[34:35]}"(px), "+{s36}"(lx), "+{v[224:239]}"(v), "+{v[240:243]}"(vo), "+{v246}"(vq)
      :
      : "memory", "vcc", "scc",
        "v0", "v1", "v2", "v3", "v4", "v5", "v6", "v7", "v8", "v9", "v10", "v11", "v12", "v13", "v14", "v15", "v16",
        "v17", "v18", "v19", "v20", "v21", "v22", "v23", "v24", "v25", "v26", "v27", "v28", "v29", "v30", "v31", "v32",
        "v33", "v34", "v35", "v36", "v37", "v38", "v39", "v40", "v41", "v42", "v43", "v44", "v45", "v46", "v47", "v48",
        "v49", "v50", "v51", "v52", "v53", "v54", "v55", "v56", "v57", "v58", "v59", "v60", "v61", "v62", "v63", "v64",
        "v65", "v66", "v67", "v68", "v69", "v70", "v71", "v72", "v73", "v74", "v75", "v76", "v77", "v78", "v79", "v80",
        "v81", "v82", "v83", "v84", "v85", "v86", "v87", "v88", "v89", "v90", "v91", "v92", "v93", "v94", "v95", "v96",
        "v97", "v98", "v99", "v100", "v101", "v102", "v103", "v104", "v105", "v106", "v107", "v108", "v109", "v110",
        "v111", "v112", "v113", "v114", "v115", "v116", "v117", "v118", "v119", "v120", "v121", "v122", "v123", "v124",
        "v125", "v126", "v127", "v128", "v129", "v130", "v131", "v132", "v133", "v134", "v135", "v136", "v137", "v138",
        "v139", "v140", "v141", "v142", "v143", "v144", "v145", "v146", "v147", "v148", "v149", "v150", "v151", "v152",
        "v153", "v154", "v155", "v156", "v157", "v158", "v159", "v160", "v161", "v162", "v163", "v164", "v165", "v166",
        "v167", "v168", "v169", "v170", "v171", "v172", "v173", "v174", "v175", "v176", "v177", "v178", "v179", "v180",
        "v181", "v182", "v183", "v184", "v185", "v186", "v187", "v188", "v189", "v190", "v191", "v192", "v193", "v194",
        "v195", "v196", "v197", "v198", "v199", "v200", "v201", "v202", "v203", "v204", "v205", "v206", "v207", "v208",
        "v209", "v210", "v211", "v212", "v213", "v214", "v215", "v216", "v217", "v218", "v219", "v220", "v221", "v222",
        "v223", "v248", "v249", "v250", "v251", "v252", "v253", "v254", "s40", "s41", "s42", "s43", "s44", "s45", "s46",
        "s47", "s48", "s49", "s50", "s51"
    );
    }
    PIN();
    LANE_BITS(__builtin_amdgcn_mbcnt_hi(~0u, __builtin_amdgcn_mbcnt_lo(~0u, 0u)));
    u32x8 v, s2;
    u32x16 s;
    v[0] = lane * 16;
    u64 kr = (u64)(6 - rot) | ((u64)rot << 32);
    v[1] = lr * 4096 + ((lc ^ swz_tr(lr)) << 4);
    v[2] = wave * G_WV + g * 1024 + q4 * 128 + (((2 * (wave & 3) + (p4 >> 1)) ^ swz_tr(8 * g + q4)) << 4) + (p4 & 1) * 8;
    v[3] = wave * 4096 + (wave & 3) * 1024 + ((lane * 16) ^ (g << 6));
    v[4] = (wave & 3) * 1024 + (((p4 * 16 + (lane >> 2)) * 16) ^ (p4 << 6));
    v[5] = (lane >> 2) * 896 + p4 * 8;
    v[6] = c * 2;
    v[7] = ST_H4 + (blk0 * 4 + ((7 * wave) >> 1) + p4) * 128 + xcd * 16384;
    put(s, 0, (u64)(w2 + st * 128 + (wave * 224 + rot * 32) * 4096));
    put(s, 2, (u64)(ws + WS_G + (blk0 * 56 + 7 * wave) * 1024));
    put(s, 4, (u64)(out + (64 * st + 16 * (wave & 3)) * 896 + blk0 * 32));
    put(s, 6, (u64)(b2 + (64 * st + 16 * (wave & 3)) * 2));
    put(s, 8, (u64)(ws + WS_ST));
    s[12] = bits;
    s[13] = wave * G_WV;
    put(s2, 0, (u64)(ws + WS_H + (blk0 * 64 + 2 * st + ((wave >> 1) & 1)) * 1024));
    s2[2] = ST_FL + (4 * st + (wave & 2)) * 128;
    s2[3] = ST_FL + t * 128;
    put(s2, 4, (u64)(ws + WS_EP));
    asm volatile(
      "s_waitcnt lgkmcnt(0)\n\ts_nop 4\n\t"
      "v_mov_b32_e32 v112, 163312\n\t"
      "ds_read_b128 v[112:115], v112\n\t"
      "v_xor_b32_e32 v248, 64, v241\n\t"
      "v_add_u32_e32 v249, 63488, v241\n\t"
      "v_add_u32_e32 v250, 95232, v248\n\t"
      "v_add_u32_e32 v248, 31744, v248\n\t"
      "v_xor_b32_e32 v251, 32, v242\n\t"
      "v_xor_b32_e32 v252, 64, v242\n\t"
      "v_xor_b32_e32 v253, 96, v242\n\t"
      "s_add_u32 m0, s17, 0\n\t"
      "s_nop 0\n\t"
      "global_load_lds_dwordx4 v241, s[4:5]\n\t"
      "global_load_lds_dwordx4 v248, s[4:5] offset:1024\n\t"
      "global_load_lds_dwordx4 v249, s[4:5] offset:2048\n\t"
      "global_load_lds_dwordx4 v250, s[4:5] offset:3072\n\t"
      "s_sub_u32 s28, s28, 1\n\t"
      "s_cselect_b32 s48, -6, 1\n\t"
      "s_cselect_b32 s49, -1, 0\n\t"
      "s_mul_i32 s48, s48, 131072\n\t"
      "s_add_u32 s4, s4, s48\n\t"
      "s_addc_u32 s5, s5, s49\n\t"
      "global_load_ushort v236, v246, s[10:11]\n\t"
      "s_waitcnt lgkmcnt(0)\n\t"
      "v_readfirstlane_b32 s14, v112\n\t"
      "v_readfirstlane_b32 s15, v113\n\t"
      "v_readfirstlane_b32 s26, v114\n\t"
      "v_readfirstlane_b32 s27, v115\n\t"
      "s_add_u32 s52, s6, 0\n\t"
      "s_addc_u32 s53, s7, 0\n\t"
      "s_add_u32 s54, s6, 57344\n\t"
      "s_addc_u32 s55, s7, 0\n\t"
      "s_add_u32 s56, s6, 114688\n\t"
      "s_addc_u32 s57, s7, 0\n\t"
      "s_add_u32 s58, s6, 172032\n\t"
      "s_addc_u32 s59, s7, 0\n\t"
      "s_add_u32 s60, s6, 229376\n\t"
      "s_addc_u32 s61, s7, 0\n\t"
      "s_add_u32 s62, s6, 286720\n\t"
      "s_addc_u32 s63, s7, 0\n\t"
      "s_add_u32 s64, s6, 344064\n\t"
      "s_addc_u32 s65, s7, 0\n\t"
      "s_add_u32 s48, s29, 0\n\t"
      "s_cmp_ge_u32 s48, 7\n\t"
      "s_cselect_b32 s49, 7, 0\n\t"
      "s_sub_u32 s48, s48, s49\n\t"
      "v_lshl_add_u32 v224, s48, 10, v240\n\t"
      "s_add_u32 s48, s29, 1\n\t"
      "s_cmp_ge_u32 s48, 7\n\t"
      "s_cselect_b32 s49, 7, 0\n\t"
      "s_sub_u32 s48, s48, s49\n\t"
      "v_lshl_add_u32 v225, s48, 10, v240\n\t"
      "s_add_u32 s48, s29, 2\n\t"
      "s_cmp_ge_u32 s48, 7\n\t"
      "s_cselect_b32 s49, 7, 0\n\t"
      "s_sub_u32 s48, s48, s49\n\t"
      "v_lshl_add_u32 v226, s48, 10, v240\n\t"
      "s_add_u32 s48, s29, 3\n\t"
      "s_cmp_ge_u32 s48, 7\n\t"
      "s_cselect_b32 s49, 7, 0\n\t"
      "s_sub_u32 s48, s48, s49\n\t"
      "v_lshl_add_u32 v227, s48, 10, v240\n\t"
      "s_add_u32 s48, s29, 4\n\t"
      "s_cmp_ge_u32 s48, 7\n\t"
      "s_cselect_b32 s49, 7, 0\n\t"
      "s_sub_u32 s48, s48, s49\n\t"
      "v_lshl_add_u32 v228, s48, 10, v240\n\t"
      "s_add_u32 s48, s29, 5\n\t"
      "s_cmp_ge_u32 s48, 7\n\t"
      "s_cselect_b32 s49, 7, 0\n\t"
      "s_sub_u32 s48, s48, s49\n\t"
      "v_lshl_add_u32 v229, s48, 10, v240\n\t"
      "s_add_u32 s48, s29, 6\n\t"
      "s_cmp_ge_u32 s48, 7\n\t"
      "s_cselect_b32 s49, 7, 0\n\t"
      "s_sub_u32 s48, s48, s49\n\t"
      "v_lshl_add_u32 v230, s48, 10, v240\n\t"
      "s_waitcnt vmcnt(5)\n\t"
      "s_barrier\n\t"
      "v_mov_b32_e32 v118, s23\n\t"
      "s_bitcmp1_b32 s16, 4\n\t"
      "s_cbranch_scc0 FW1_%=\n\t"
      "v_mov_b32_e32 v116, s14\n\t"
      "v_mov_b32_e32 v117, s15\n\t"
      "s_mov_b64 exec, 1\n\t"
      "global_store_dwordx2 v118, v[116:117], s[12:13] sc1\n\t"
      "s_mov_b64 exec, -1\n\t"
      "FW1_%=:\n\t"
      "s_waitcnt vmcnt(1)\n\t"
      "s_add_u32 m0, s17, 4096\n\t"
      "s_nop 0\n\t"
      "global_load_lds_dwordx4 v241, s[4:5]\n\t"
      "global_load_lds_dwordx4 v248, s[4:5] offset:1024\n\t"
      "global_load_lds_dwordx4 v249, s[4:5] offset:2048\n\t"
      "global_load_lds_dwordx4 v250, s[4:5] offset:3072\n\t"
      "s_sub_u32 s28, s28, 1\n\t"
      "s_cselect_b32 s48, -6, 1\n\t"
      "s_cselect_b32 s49, -1, 0\n\t"
      "s_mul_i32 s48, s48, 131072\n\t"
      "s_add_u32 s4, s4, s48\n\t"
      "s_addc_u32 s5, s5, s49\n\t"
      "ds_read_b64_tr_b16 v[0:1], v242\n\t"
      "ds_read_b64_tr_b16 v[2:3], v242 offset:512\n\t"
      "ds_read_b64_tr_b16 v[4:5], v251\n\t"
      "ds_read_b64_tr_b16 v[6:7], v251 offset:512\n\t"
      "ds_read_b64_tr_b16 v[8:9], v252\n\t"
      "ds_read_b64_tr_b16 v[10:11], v252 offset:512\n\t"
      "ds_read_b64_tr_b16 v[12:13], v253\n\t"
      "ds_read_b64_tr_b16 v[14:15], v253 offset:512\n\t"
      "s_waitcnt vmcnt(0)\n\t"
      "s_add_u32 m0, s17, 8192\n\t"
      "s_nop 0\n\t"
      "global_load_lds_dwordx4 v241, s[4:5]\n\t"
      "global_load_lds_dwordx4 v248, s[4:5] offset:1024\n\t"
      "global_load_lds_dwordx4 v249, s[4:5] offset:2048\n\t"
      "global_load_lds_dwordx4 v250, s[4:5] offset:3072\n\t"
      "s_sub_u32 s28, s28, 1\n\t"
      "s_cselect_b32 s48, -6, 1\n\t"
      "s_cselect_b32 s49, -1, 0\n\t"
      "s_mul_i32 s48, s48, 131072\n\t"
      "s_add_u32 s4, s4, s48\n\t"
      "s_addc_u32 s5, s5, s49\n\t"
      "ds_read_b64_tr_b16 v[16:17], v242 offset:4096\n\t"
      "ds_read_b64_tr_b16 v[18:19], v242 offset:4608\n\t"
      "ds_read_b64_tr_b16 v[20:21], v251 offset:4096\n\t"
      "ds_read_b64_tr_b16 v[22:23], v251 offset:4608\n\t"
      "ds_read_b64_tr_b16 v[24:25], v252 offset:4096\n\t"
      "ds_read_b64_tr_b16 v[26:27], v252 offset:4608\n\t"
      "ds_read_b64_tr_b16 v[28:29], v253 offset:4096\n\t"
      "ds_read_b64_tr_b16 v[30:31], v253 offset:4608\n\t"
      "v_xor_b32_e32 v220, 1024, v243\n\t"
      "v_xor_b32_e32 v221, 2048, v243\n\t"
      "v_xor_b32_e32 v222, 3072, v243\n\t"
      "v_lshlrev_b32_e32 v237, 16, v236\n\t"
      "v_lshlrev_b32_e32 v238, 16, v236\n\t"
      "v_lshlrev_b32_e32 v239, 16, v236\n\t"
      "v_lshlrev_b32_e32 v236, 16, v236\n\t"
      "v_lshrrev_b32_e32 v112, 4, v240\n\t"
      "v_and_b32_e32 v113, 15, v112\n\t"
      "v_lshrrev_b32_e32 v112, 4, v112\n\t"
      "s_lshr_b32 s48, s17, 10\n\t"
      "s_and_b32 s48, s48, 16\n\t"
      "v_lshlrev_b32_e32 v112, 3, v112\n\t"
      "v_sub_u32_e32 v112, v113, v112\n\t"
      "v_add_u32_e32 v112, s48, v112\n\t"
      "v_mov_b32_e32 v114, 0x3f80\n\t"
      "v_mov_b32_e32 v115, 0x3f800000\n\t"
      "v_cmp_eq_u32_e32 vcc, 0, v112\n\t"
      "s_nop 1\n\t"
      "v_cndmask_b32_e32 v216, 0, v114, vcc\n\t"
      "v_cmp_eq_u32_e32 vcc, 1, v112\n\t"
      "s_nop 1\n\t"
      "v_cndmask_b32_e32 v216, v216, v115, vcc\n\t"
      "v_cmp_eq_u32_e32 vcc, 2, v112\n\t"
      "s_nop 1\n\t"
      "v_cndmask_b32_e32 v217, 0, v114, vcc\n\t"
      "v_cmp_eq_u32_e32 vcc, 3, v112\n\t"
      "s_nop 1\n\t"
      "v_cndmask_b32_e32 v217, v217, v115, vcc\n\t"
      "v_cmp_eq_u32_e32 vcc, 4, v112\n\t"
      "s_nop 1\n\t"
      "v_cndmask_b32_e32 v218, 0, v114, vcc\n\t"
      "v_cmp_eq_u32_e32 vcc, 5, v112\n\t"
      "s_nop 1\n\t"
      "v_cndmask_b32_e32 v218, v218, v115, vcc\n\t"
      "v_cmp_eq_u32_e32 vcc, 6, v112\n\t"
      "s_nop 1\n\t"
      "v_cndmask_b32_e32 v219, 0, v114, vcc\n\t"
      "v_cmp_eq_u32_e32 vcc, 7, v112\n\t"
      "s_nop 1\n\t"
      "v_cndmask_b32_e32 v219, v219, v115, vcc\n\t"
      "v_and_b32_e32 v114, 16, v240\n\t"
      "v_lshl_add_u32 v114, v114, 3, s22\n\t"
      "s_waitcnt vmcnt(0)\n\t"
      "s_add_u32 m0, s17, 12288\n\t"
      "s_nop 0\n\t"
      "global_load_lds_dwordx4 v241, s[4:5]\n\t"
      "global_load_lds_dwordx4 v248, s[4:5] offset:1024\n\t"
      "global_load_lds_dwordx4 v249, s[4:5] offset:2048\n\t"
      "global_load_lds_dwordx4 v250, s[4:5] offset:3072\n\t"
      "s_sub_u32 s28, s28, 1\n\t"
      "s_cselect_b32 s48, -6, 1\n\t"
      "s_cselect_b32 s49, -1, 0\n\t"
      "s_mul_i32 s48, s48, 131072\n\t"
      "s_add_u32 s4, s4, s48\n\t"
      "s_addc_u32 s5, s5, s49\n\t"
      "ds_read_b64_tr_b16 v[32:33], v242 offset:8192\n\t"
      "ds_read_b64_tr_b16 v[34:35], v242 offset:8704\n\t"
      "ds_read_b64_tr_b16 v[36:37], v251 offset:8192\n\t"
      "ds_read_b64_tr_b16 v[38:39], v251 offset:8704\n\t"
      "ds_read_b64_tr_b16 v[40:41], v252 offset:8192\n\t"
      "ds_read_b64_tr_b16 v[42:43], v252 offset:8704\n\t"
      "ds_read_b64_tr_b16 v[44:45], v253 offset:8192\n\t"
      "ds_read_b64_tr_b16 v[46:47], v253 offset:8704\n\t"
      "s_waitcnt vmcnt(0)\n\t"
      "s_waitcnt lgkmcnt(14)\n\t"
      "s_add_u32 m0, s17, 0\n\t"
      "s_nop 0\n\t"
      "global_load_lds_dwordx4 v241, s[4:5]\n\t"
      "global_load_lds_dwordx4 v248, s[4:5] offset:1024\n\t"
      "global_load_lds_dwordx4 v249, s[4:5] offset:2048\n\t"
      "global_load_lds_dwordx4 v250, s[4:5] offset:3072\n\t"
      "s_sub_u32 s28, s28, 1\n\t"
      "s_cselect_b32 s48, -6, 1\n\t"
      "s_cselect_b32 s49, -1, 0\n\t"
      "s_mul_i32 s48, s48, 131072\n\t"
      "s_add_u32 s4, s4, s48\n\t"
      "s_addc_u32 s5, s5, s49\n\t"
      "ds_read_b64_tr_b16 v[48:49], v242 offset:12288\n\t"
      "ds_read_b64_tr_b16 v[50:51], v242 offset:12800\n\t"
      "ds_read_b64_tr_b16 v[52:53], v251 offset:12288\n\t"
      "ds_read_b64_tr_b16 v[54:55], v251 offset:12800\n\t"
      "ds_read_b64_tr_b16 v[56:57], v252 offset:12288\n\t"
      "ds_read_b64_tr_b16 v[58:59], v252 offset:12800\n\t"
      "ds_read_b64_tr_b16 v[60:61], v253 offset:12288\n\t"
      "ds_read_b64_tr_b16 v[62:63], v253 offset:12800\n\t"
      "s_waitcnt vmcnt(0)\n\t"
      "s_waitcnt lgkmcnt(14)\n\t"
      "s_add_u32 m0, s17, 4096\n\t"
      "s_nop 0\n\t"
      "global_load_lds_dwordx4 v241, s[4:5]\n\t"
      "global_load_lds_dwordx4 v248, s[4:5] offset:1024\n\t"
      "global_load_lds_dwordx4 v249, s[4:5] offset:2048\n\t"
      "global_load_lds_dwordx4 v250, s[4:5] offset:3072\n\t"
      "s_sub_u32 s28, s28, 1\n\t"
      "s_cselect_b32 s48, -6, 1\n\t"
      "s_cselect_b32 s49, -1, 0\n\t"
      "s_mul_i32 s48, s48, 131072\n\t"
      "s_add_u32 s4, s4, s48\n\t"
      "s_addc_u32 s5, s5, s49\n\t"
      "ds_read_b64_tr_b16 v[64:65], v242\n\t"
      "ds_read_b64_tr_b16 v[66:67], v242 offset:512\n\t"
      "ds_read_b64_tr_b16 v[68:69], v251\n\t"
      "ds_read_b64_tr_b16 v[70:71], v251 offset:512\n\t"
      "ds_read_b64_tr_b16 v[72:73], v252\n\t"
      "ds_read_b64_tr_b16 v[74:75], v252 offset:512\n\t"
      "ds_read_b64_tr_b16 v[76:77], v253\n\t"
      "ds_read_b64_tr_b16 v[78:79], v253 offset:512\n\t"
      "s_bitcmp1_b32 s16, 1\n\t"
      "s_cbranch_scc0 HWQ_%=\n\t"
      "global_load_dwordx2 v[112:113], v114, s[12:13] sc1\n\t"
      "HWQ_%=:\n\t"
      "s_waitcnt vmcnt(0)\n\t"
      "s_waitcnt lgkmcnt(14)\n\t"
      "s_add_u32 m0, s17, 8192\n\t"
      "s_nop 0\n\t"
      "global_load_lds_dwordx4 v241, s[4:5]\n\t"
      "global_load_lds_dwordx4 v248, s[4:5] offset:1024\n\t"
      "global_load_lds_dwordx4 v249, s[4:5] offset:2048\n\t"
      "global_load_lds_dwordx4 v250, s[4:5] offset:3072\n\t"
      "ds_read_b64_tr_b16 v[80:81], v242 offset:4096\n\t"
      "ds_read_b64_tr_b16 v[82:83], v242 offset:4608\n\t"
      "ds_read_b64_tr_b16 v[84:85], v251 offset:4096\n\t"
      "ds_read_b64_tr_b16 v[86:87], v251 offset:4608\n\t"
      "ds_read_b64_tr_b16 v[88:89], v252 offset:4096\n\t"
      "ds_read_b64_tr_b16 v[90:91], v252 offset:4608\n\t"
      "ds_read_b64_tr_b16 v[92:93], v253 offset:4096\n\t"
      "ds_read_b64_tr_b16 v[94:95], v253 offset:4608\n\t"
      "s_waitcnt vmcnt(0)\n\t"
      "ds_read_b64_tr_b16 v[96:97], v242 offset:8192\n\t"
      "ds_read_b64_tr_b16 v[98:99], v242 offset:8704\n\t"
      "ds_read_b64_tr_b16 v[100:101], v251 offset:8192\n\t"
      "ds_read_b64_tr_b16 v[102:103], v251 offset:8704\n\t"
      "ds_read_b64_tr_b16 v[104:105], v252 offset:8192\n\t"
      "ds_read_b64_tr_b16 v[106:107], v252 offset:8704\n\t"
      "ds_read_b64_tr_b16 v[108:109], v253 offset:8192\n\t"
      "ds_read_b64_tr_b16 v[110:111], v253 offset:8704\n\t"
      "s_mov_b64 exec, 0xf\n\t"
      "v_mov_b64_e32 v[232:233], 0\n\t"
      "global_atomic_or_x2 v[232:233], v247, v[232:233], s[12:13] sc0 sc1\n\t"
      "s_mov_b64 exec, -1\n\t"
      "s_bitcmp1_b32 s16, 1\n\t"
      "s_cbranch_scc0 HWE_%=\n\t"
      "v_cmp_ne_u64_e32 vcc, s[14:15], v[112:113]\n\t"
      "s_cbranch_vccnz HWR_%=\n\t"
      "HWB_%=:\n\t"
      "s_add_u32 s68, s20, 0\n\t"
      "s_addc_u32 s69, s21, 0\n\t"
      "global_load_dwordx4 v[184:187], v240, s[68:69] sc1\n\t"
      "s_add_u32 s68, s20, 65536\n\t"
      "s_addc_u32 s69, s21, 0\n\t"
      "global_load_dwordx4 v[188:191], v240, s[68:69] sc1\n\t"
      "s_add_u32 s68, s20, 131072\n\t"
      "s_addc_u32 s69, s21, 0\n\t"
      "global_load_dwordx4 v[192:195], v240, s[68:69] sc1\n\t"
      "s_add_u32 s68, s20, 196608\n\t"
      "s_addc_u32 s69, s21, 0\n\t"
      "global_load_dwordx4 v[196:199], v240, s[68:69] sc1\n\t"
      "s_add_u32 s68, s20, 262144\n\t"
      "s_addc_u32 s69, s21, 0\n\t"
      "global_load_dwordx4 v[200:203], v240, s[68:69] sc1\n\t"
      "s_add_u32 s68, s20, 327680\n\t"
      "s_addc_u32 s69, s21, 0\n\t"
      "global_load_dwordx4 v[204:207], v240, s[68:69] sc1\n\t"
      "s_add_u32 s68, s20, 393216\n\t"
      "s_addc_u32 s69, s21, 0\n\t"
      "global_load_dwordx4 v[208:211], v240, s[68:69] sc1\n\t"
      "s_waitcnt vmcnt(0)\n\t"
      "v_mfma_f32_16x16x32_bf16 v[156:159], v[184:187], v[216:219], v[236:239]\n\t"
      "s_waitcnt vmcnt(0)\n\t"
      "v_mfma_f32_16x16x32_bf16 v[160:163], v[188:191], v[216:219], v[236:239]\n\t"
      "s_waitcnt vmcnt(0)\n\t"
      "v_mfma_f32_16x16x32_bf16 v[164:167], v[192:195], v[216:219], v[236:239]\n\t"
      "s_waitcnt vmcnt(0)\n\t"
      "v_mfma_f32_16x16x32_bf16 v[168:171], v[196:199], v[216:219], v[236:239]\n\t"
      "s_waitcnt vmcnt(0)\n\t"
      "v_mfma_f32_16x16x32_bf16 v[172:175], v[200:203], v[216:219], v[236:239]\n\t"
      "s_waitcnt vmcnt(0)\n\t"
      "v_mfma_f32_16x16x32_bf16 v[176:179], v[204:207], v[216:219], v[236:239]\n\t"
      "s_waitcnt vmcnt(0)\n\t"
      "v_mfma_f32_16x16x32_bf16 v[180:183], v[208:211], v[216:219], v[236:239]\n\t"
      "HWE_%=:\n\t"
      "s_waitcnt lgkmcnt(0)\n\t"
      "s_barrier\n\t"
      "s_mov_b64 exec, 15\n\t"
      "s_waitcnt vmcnt(0)\n\t"
      "v_subrev_u32_e32 v232, s14, v232\n\t"
      "v_cmp_ne_u32_e64 s[48:49], s15, v233\n\t"
      "v_subrev_u32_e32 v232, 0, v232\n\t"
      "v_cmp_lt_u32_e32 vcc, 7, v232\n\t"
      "s_mov_b64 exec, -1\n\t"
      "s_or_b64 vcc, vcc, s[48:49]\n\t"
      "s_cbranch_vccz PRY_%=\n\t"
      "s_mov_b32 s51, 0\n\t"
      "s_branch PRE_%=\n\t"
      "PRL_%=:\n\t"
      "s_sleep 4\n\t"
      "s_add_u32 s51, s51, 1\n\t"
      "s_cmp_lt_u32 s51, 262144\n\t"
      "s_cbranch_scc0 PRX_%=\n\t"
      "PRE_%=:\n\t"
      "s_mov_b64 exec, 15\n\t"
      "v_mov_b64_e32 v[232:233], 0\n\t"
      "global_atomic_or_x2 v[232:233], v247, v[232:233], s[12:13] sc0 sc1\n\t"
      "s_waitcnt vmcnt(0)\n\t"
      "v_subrev_u32_e32 v232, s14, v232\n\t"
      "v_cmp_ne_u32_e64 s[48:49], s15, v233\n\t"
      "v_subrev_u32_e32 v232, 0, v232\n\t"
      "v_cmp_lt_u32_e32 vcc, 7, v232\n\t"
      "s_mov_b64 exec, -1\n\t"
      "s_or_b64 vcc, vcc, s[48:49]\n\t"
      "s_cbranch_vccnz PRL_%=\n\t"
      "PRX_%=:\n\t"
      "s_sleep 4\n\t"
      "PRY_%=:\n\t"
      "global_load_dwordx4 v[128:131], v224, s[52:53] sc1\n\t"
      "global_load_dwordx4 v[132:135], v225, s[52:53] sc1\n\t"
      "global_load_dwordx4 v[136:139], v226, s[52:53] sc1\n\t"
      "global_load_dwordx4 v[140:143], v227, s[52:53] sc1\n\t"
      "global_load_dwordx4 v[144:147], v228, s[52:53] sc1\n\t"
      "global_load_dwordx4 v[148:151], v229, s[52:53] sc1\n\t"
      "global_load_dwordx4 v[152:155], v230, s[52:53] sc1\n\t"
      "s_cmp_ge_u32 s17, 65536\n\t"
      "s_cbranch_scc1 GQ_%=\n\t"
      ".p2align 6\n\t"
      "s_setprio 1\n\t"
      "s_waitcnt vmcnt(6)\n\t"
      "v_max3_u32 v234, v128, v129, v130\n\t"
      "v_max_u32_e32 v234, v234, v131\n\t"
      "v_cmp_eq_u32_e32 vcc, -1, v234\n\t"
      "s_cbranch_vccnz FP0R_%=\n\t"
      "FP0B_%=:\n\t"
      "v_mfma_f32_16x16x32_bf16 v[184:187], v[128:131], v[0:3], 0\n\t"
      "v_mfma_f32_16x16x32_bf16 v[188:191], v[128:131], v[4:7], 0\n\t"
      "v_mfma_f32_16x16x32_bf16 v[192:195], v[128:131], v[8:11], 0\n\t"
      "v_mfma_f32_16x16x32_bf16 v[196:199], v[128:131], v[12:15], 0\n\t"
      "global_load_dwordx4 v[128:131], v224, s[54:55] sc1\n\t"
      "s_waitcnt vmcnt(6)\n\t"
      "v_max3_u32 v234, v132, v133, v134\n\t"
      "v_max_u32_e32 v234, v234, v135\n\t"
      "v_cmp_eq_u32_e32 vcc, -1, v234\n\t"
      "s_cbranch_vccnz FP1R_%=\n\t"
      "FP1B_%=:\n\t"
      "v_mfma_f32_16x16x32_bf16 v[184:187], v[132:135], v[16:19], v[184:187]\n\t"
      "v_mfma_f32_16x16x32_bf16 v[188:191], v[132:135], v[20:23], v[188:191]\n\t"
      "v_mfma_f32_16x16x32_bf16 v[192:195], v[132:135], v[24:27], v[192:195]\n\t"
      "v_mfma_f32_16x16x32_bf16 v[196:199], v[132:135], v[28:31], v[196:199]\n\t"
      "global_load_dwordx4 v[132:135], v225, s[54:55] sc1\n\t"
      "s_waitcnt vmcnt(6)\n\t"
      "v_max3_u32 v234, v136, v137, v138\n\t"
      "v_max_u32_e32 v234, v234, v139\n\t"
      "v_cmp_eq_u32_e32 vcc, -1, v234\n\t"
      "s_cbranch_vccnz FP2R_%=\n\t"
      "FP2B_%=:\n\t"
      "v_mfma_f32_16x16x32_bf16 v[184:187], v[136:139], v[32:35], v[184:187]\n\t"
      "v_mfma_f32_16x16x32_bf16 v[188:191], v[136:139], v[36:39], v[188:191]\n\t"
      "v_mfma_f32_16x16x32_bf16 v[192:195], v[136:139], v[40:43], v[192:195]\n\t"
      "v_mfma_f32_16x16x32_bf16 v[196:199], v[136:139], v[44:47], v[196:199]\n\t"
      "global_load_dwordx4 v[136:139], v226, s[54:55] sc1\n\t"
      "s_waitcnt vmcnt(6)\n\t"
      "v_max3_u32 v234, v140, v141, v142\n\t"
      "v_max_u32_e32 v234, v234, v143\n\t"
      "v_cmp_eq_u32_e32 vcc, -1, v234\n\t"
      "s_cbranch_vccnz FP3R_%=\n\t"
      "FP3B_%=:\n\t"
      "v_mfma_f32_16x16x32_bf16 v[184:187], v[140:143], v[48:51], v[184:187]\n\t"
      "v_mfma_f32_16x16x32_bf16 v[188:191], v[140:143], v[52:55], v[188:191]\n\t"
      "v_mfma_f32_16x16x32_bf16 v[192:195], v[140:143], v[56:59], v[192:195]\n\t"
      "v_mfma_f32_16x16x32_bf16 v[196:199], v[140:143], v[60:63], v[196:199]\n\t"
      "global_load_dwordx4 v[140:143], v227, s[54:55] sc1\n\t"
      "s_waitcnt vmcnt(6)\n\t"
      "v_max3_u32 v234, v144, v145, v146\n\t"
      "v_max_u32_e32 v234, v234, v147\n\t"
      "v_cmp_eq_u32_e32 vcc, -1, v234\n\t"
      "s_cbranch_vccnz FP4R_%=\n\t"
      "FP4B_%=:\n\t"
      "v_mfma_f32_16x16x32_bf16 v[184:187], v[144:147], v[64:67], v[184:187]\n\t"
      "v_mfma_f32_16x16x32_bf16 v[188:191], v[144:147], v[68:71], v[188:191]\n\t"
      "v_mfma_f32_16x16x32_bf16 v[192:195], v[144:147], v[72:75], v[192:195]\n\t"
      "v_mfma_f32_16x16x32_bf16 v[196:199], v[144:147], v[76:79], v[196:199]\n\t"
      "global_load_dwordx4 v[144:147], v228, s[54:55] sc1\n\t"
      "s_waitcnt vmcnt(6)\n\t"
      "v_max3_u32 v234, v148, v149, v150\n\t"
      "v_max_u32_e32 v234, v234, v151\n\t"
      "v_cmp_eq_u32_e32 vcc, -1, v234\n\t"
      "s_cbranch_vccnz FP5R_%=\n\t"
      "FP5B_%=:\n\t"
      "v_mfma_f32_16x16x32_bf16 v[184:187], v[148:151], v[80:83], v[184:187]\n\t"
      "v_mfma_f32_16x16x32_bf16 v[188:191], v[148:151], v[84:87], v[188:191]\n\t"
      "v_mfma_f32_16x16x32_bf16 v[192:195], v[148:151], v[88:91], v[192:195]\n\t"
      "v_mfma_f32_16x16x32_bf16 v[196:199], v[148:151], v[92:95], v[196:199]\n\t"
      "global_load_dwordx4 v[148:151], v229, s[54:55] sc1\n\t"
      "s_waitcnt vmcnt(6)\n\t"
      "v_max3_u32 v234, v152, v153, v154\n\t"
      "v_max_u32_e32 v234, v234, v155\n\t"
      "v_cmp_eq_u32_e32 vcc, -1, v234\n\t"
      "s_cbranch_vccnz FP6R_%=\n\t"
      "FP6B_%=:\n\t"
      "v_mfma_f32_16x16x32_bf16 v[184:187], v[152:155], v[96:99], v[184:187]\n\t"
      "v_mfma_f32_16x16x32_bf16 v[188:191], v[152:155], v[100:103], v[188:191]\n\t"
      "v_mfma_f32_16x16x32_bf16 v[192:195], v[152:155], v[104:107], v[192:195]\n\t"
      "v_mfma_f32_16x16x32_bf16 v[196:199], v[152:155], v[108:111], v[196:199]\n\t"
      "global_load_dwordx4 v[152:155], v230, s[54:55] sc1\n\t"
      "s_waitcnt vmcnt(6)\n\t"
      "v_cmp_eq_u32_e32 vcc, -1, v128\n\t"
      "s_cbranch_vccnz FP7R_%=\n\t"
      "FP7B_%=:\n\t"
      "v_mfma_f32_16x16x32_bf16 v[200:203], v[128:131], v[0:3], 0\n\t"
      "ds_write_b128 v243, v[184:187]\n\t"
      "v_mfma_f32_16x16x32_bf16 v[204:207], v[128:131], v[4:7], 0\n\t"
      "v_mfma_f32_16x16x32_bf16 v[208:211], v[128:131], v[8:11], 0\n\t"
      "v_mfma_f32_16x16x32_bf16 v[212:215], v[128:131], v[12:15], 0\n\t"
      "global_load_dwordx4 v[128:131], v224, s[56:57] sc1\n\t"
      "ds_write_b128 v220, v[188:191]\n\t"
      "s_waitcnt vmcnt(6)\n\t"
      "v_cmp_eq_u32_e32 vcc, -1, v132\n\t"
      "s_cbranch_vccnz FP8R_%=\n\t"
      "FP8B_%=:\n\t"
      "v_mfma_f32_16x16x32_bf16 v[200:203], v[132:135], v[16:19], v[200:203]\n\t"
      "v_mfma_f32_16x16x32_bf16 v[204:207], v[132:135], v[20:23], v[204:207]\n\t"
      "v_mfma_f32_16x16x32_bf16 v[208:211], v[132:135], v[24:27], v[208:211]\n\t"
      "ds_write_b128 v221, v[192:195]\n\t"
      "v_mfma_f32_16x16x32_bf16 v[212:215], v[132:135], v[28:31], v[212:215]\n\t"
      "global_load_dwordx4 v[132:135], v225, s[56:57] sc1\n\t"
      "s_waitcnt vmcnt(6)\n\t"
      "v_cmp_eq_u32_e32 vcc, -1, v136\n\t"
      "s_cbranch_vccnz FP9R_%=\n\t"
      "FP9B_%=:\n\t"
      "v_mfma_f32_16x16x32_bf16 v[200:203], v[136:139], v[32:35], v[200:203]\n\t"
      "v_mfma_f32_16x16x32_bf16 v[204:207], v[136:139], v[36:39], v[204:207]\n\t"
      "ds_write_b128 v222, v[196:199]\n\t"
      "v_mfma_f32_16x16x32_bf16 v[208:211], v[136:139], v[40:43], v[208:211]\n\t"
      "v_mfma_f32_16x16x32_bf16 v[212:215], v[136:139], v[44:47], v[212:215]\n\t"
      "global_load_dwordx4 v[136:139], v226, s[56:57] sc1\n\t"
      "s_waitcnt vmcnt(6)\n\t"
      "v_cmp_eq_u32_e32 vcc, -1, v140\n\t"
      "s_cbranch_vccnz FP10R_%=\n\t"
      "FP10B_%=:\n\t"
      "v_mfma_f32_16x16x32_bf16 v[200:203], v[140:143], v[48:51], v[200:203]\n\t"
      "s_waitcnt lgkmcnt(0)\n\t"
      "s_barrier\n\t"
      "ds_read_b128 v[112:115], v244\n\t"
      "v_mfma_f32_16x16x32_bf16 v[204:207], v[140:143], v[52:55], v[204:207]\n\t"
      "ds_read_b128 v[116:119], v244 offset:4096\n\t"
      "v_mfma_f32_16x16x32_bf16 v[208:211], v[140:143], v[56:59], v[208:211]\n\t"
      "ds_read_b128 v[120:123], v244 offset:8192\n\t"
      "v_mfma_f32_16x16x32_bf16 v[212:215], v[140:143], v[60:63], v[212:215]\n\t"
      "global_load_dwordx4 v[140:143], v227, s[56:57] sc1\n\t"
      "ds_read_b128 v[124:127], v244 offset:12288\n\t"
      "s_waitcnt vmcnt(6)\n\t"
      "v_cmp_eq_u32_e32 vcc, -1, v144\n\t"
      "s_cbranch_vccnz FP11R_%=\n\t"
      "FP11B_%=:\n\t"
      "v_mfma_f32_16x16x32_bf16 v[200:203], v[144:147], v[64:67], v[200:203]\n\t"
      "ds_read_b128 v[184:187], v244 offset:16384\n\t"
      "v_mfma_f32_16x16x32_bf16 v[204:207], v[144:147], v[68:71], v[204:207]\n\t"
      "ds_read_b128 v[188:191], v244 offset:20480\n\t"
      "v_mfma_f32_16x16x32_bf16 v[208:211], v[144:147], v[72:75], v[208:211]\n\t"
      "ds_read_b128 v[192:195], v244 offset:24576\n\t"
      "v_mfma_f32_16x16x32_bf16 v[212:215], v[144:147], v[76:79], v[212:215]\n\t"
      "global_load_dwordx4 v[144:147], v228, s[56:57] sc1\n\t"
      "ds_read_b128 v[196:199], v244 offset:28672\n\t"
      "s_waitcnt vmcnt(6)\n\t"
      "v_cmp_eq_u32_e32 vcc, -1, v148\n\t"
      "s_cbranch_vccnz FP12R_%=\n\t"
      "FP12B_%=:\n\t"
      "v_mfma_f32_16x16x32_bf16 v[200:203], v[148:151], v[80:83], v[200:203]\n\t"
      "v_mfma_f32_16x16x32_bf16 v[204:207], v[148:151], v[84:87], v[204:207]\n\t"
      "v_mfma_f32_16x16x32_bf16 v[208:211], v[148:151], v[88:91], v[208:211]\n\t"
      "s_waitcnt lgkmcnt(6)\n\t"
      "v_add_f32_e32 v112, v112, v116\n\t"
      "v_add_f32_e32 v113, v113, v117\n\t"
      "v_add_f32_e32 v114, v114, v118\n\t"
      "v_add_f32_e32 v115, v115, v119\n\t"
      "s_waitcnt lgkmcnt(4)\n\t"
      "v_add_f32_e32 v120, v120, v124\n\t"
      "v_add_f32_e32 v121, v121, v125\n\t"
      "v_add_f32_e32 v122, v122, v126\n\t"
      "v_add_f32_e32 v123, v123, v127\n\t"
      "s_waitcnt lgkmcnt(2)\n\t"
      "v_add_f32_e32 v184, v184, v188\n\t"
      "v_add_f32_e32 v185, v185, v189\n\t"
      "v_add_f32_e32 v186, v186, v190\n\t"
      "v_add_f32_e32 v187, v187, v191\n\t"
      "v_add_f32_e32 v112, v112, v120\n\t"
      "v_add_f32_e32 v113, v113, v121\n\t"
      "v_add_f32_e32 v114, v114, v122\n\t"
      "v_add_f32_e32 v115, v115, v123\n\t"
      "s_waitcnt lgkmcnt(0)\n\t"
      "v_add_f32_e32 v192, v192, v196\n\t"
      "v_add_f32_e32 v193, v193, v197\n\t"
      "v_add_f32_e32 v194, v194, v198\n\t"
      "v_add_f32_e32 v195, v195, v199\n\t"
      "v_add_f32_e32 v184, v184, v192\n\t"
      "v_add_f32_e32 v185, v185, v193\n\t"
      "v_add_f32_e32 v186, v186, v194\n\t"
      "v_add_f32_e32 v187, v187, v195\n\t"
      "v_add_f32_e32 v112, v112, v184\n\t"
      "v_add_f32_e32 v113, v113, v185\n\t"
      "v_add_f32_e32 v114, v114, v186\n\t"
      "v_add_f32_e32 v115, v115, v187\n\t"
      "v_cvt_pk_bf16_f32 v116, v112, v113\n\t"
      "v_cvt_pk_bf16_f32 v117, v114, v115\n\t"
      "global_store_dwordx2 v245, v[116:117], s[8:9]\n\t"
      "v_mfma_f32_16x16x32_bf16 v[212:215], v[148:151], v[92:95], v[212:215]\n\t"
      "global_load_dwordx4 v[148:151], v229, s[56:57] sc1\n\t"
      "s_waitcnt vmcnt(7)\n\t"
      "v_cmp_eq_u32_e32 vcc, -1, v152\n\t"
      "s_cbranch_vccnz FP13R_%=\n\t"
      "FP13B_%=:\n\t"
      "v_mfma_f32_16x16x32_bf16 v[200:203], v[152:155], v[96:99], v[200:203]\n\t"
      "v_mfma_f32_16x16x32_bf16 v[204:207], v[152:155], v[100:103], v[204:207]\n\t"
      "v_mfma_f32_16x16x32_bf16 v[208:211], v[152:155], v[104:107], v[208:211]\n\t"
      "v_mfma_f32_16x16x32_bf16 v[212:215], v[152:155], v[108:111], v[212:215]\n\t"
      "global_load_dwordx4 v[152:155], v230, s[56:57] sc1\n\t"
      "s_nop 3\n\t"
      "v_cmp_u_f32_e32 vcc, v200, v201\n\t"
      "v_cmp_u_f32_e64 s[48:49], v202, v203\n\t"
      "s_nop 0\n\t"
      "s_or_b64 vcc, vcc, s[48:49]\n\t"
      "s_cbranch_scc1 NP1R_%=\n\t"
      "NP1B_%=:\n\t"
      "s_waitcnt vmcnt(7)\n\t"
      "v_cmp_eq_u32_e32 vcc, -1, v128\n\t"
      "s_cbranch_vccnz FP14R_%=\n\t"
      "FP14B_%=:\n\t"
      "v_mfma_f32_16x16x32_bf16 v[184:187], v[128:131], v[0:3], 0\n\t"
      "ds_write_b128 v243, v[200:203] offset:32768\n\t"
      "v_mfma_f32_16x16x32_bf16 v[188:191], v[128:131], v[4:7], 0\n\t"
      "v_mfma_f32_16x16x32_bf16 v[192:195], v[128:131], v[8:11], 0\n\t"
      "v_mfma_f32_16x16x32_bf16 v[196:199], v[128:131], v[12:15], 0\n\t"
      "global_load_dwordx4 v[128:131], v224, s[58:59] sc1\n\t"
      "ds_write_b128 v220, v[204:207] offset:32768\n\t"
      "s_waitcnt vmcnt(7)\n\t"
      "v_cmp_eq_u32_e32 vcc, -1, v132\n\t"
      "s_cbranch_vccnz FP15R_%=\n\t"
      "FP15B_%=:\n\t"
      "v_mfma_f32_16x16x32_bf16 v[184:187], v[132:135], v[16:19], v[184:187]\n\t"
      "v_mfma_f32_16x16x32_bf16 v[188:191], v[132:135], v[20:23], v[188:191]\n\t"
      "v_mfma_f32_16x16x32_bf16 v[192:195], v[132:135], v[24:27], v[192:195]\n\t"
      "ds_write_b128 v221, v[208:211] offset:32768\n\t"
      "v_mfma_f32_16x16x32_bf16 v[196:199], v[132:135], v[28:31], v[196:199]\n\t"
      "global_load_dwordx4 v[132:135], v225, s[58:59] sc1\n\t"
      "s_waitcnt vmcnt(7)\n\t"
      "v_cmp_eq_u32_e32 vcc, -1, v136\n\t"
      "s_cbranch_vccnz FP16R_%=\n\t"
      "FP16B_%=:\n\t"
      "v_mfma_f32_16x16x32_bf16 v[184:187], v[136:139], v[32:35], v[184:187]\n\t"
      "v_mfma_f32_16x16x32_bf16 v[188:191], v[136:139], v[36:39], v[188:191]\n\t"
      "ds_write_b128 v222, v[212:215] offset:32768\n\t"
      "v_mfma_f32_16x16x32_bf16 v[192:195], v[136:139], v[40:43], v[192:195]\n\t"
      "v_mfma_f32_16x16x32_bf16 v[196:199], v[136:139], v[44:47], v[196:199]\n\t"
      "global_load_dwordx4 v[136:139], v226, s[58:59] sc1\n\t"
      "s_waitcnt vmcnt(7)\n\t"
      "v_cmp_eq_u32_e32 vcc, -1, v140\n\t"
      "s_cbranch_vccnz FP17R_%=\n\t"
      "FP17B_%=:\n\t"
      "v_mfma_f32_16x16x32_bf16 v[184:187], v[140:143], v[48:51], v[184:187]\n\t"
      "s_waitcnt lgkmcnt(0)\n\t"
      "s_barrier\n\t"
      "ds_read_b128 v[112:115], v244 offset:32768\n\t"
      "v_mfma_f32_16x16x32_bf16 v[188:191], v[140:143], v[52:55], v[188:191]\n\t"
      "ds_read_b128 v[116:119], v244 offset:36864\n\t"
      "v_mfma_f32_16x16x32_bf16 v[192:195], v[140:143], v[56:59], v[192:195]\n\t"
      "ds_read_b128 v[120:123], v244 offset:40960\n\t"
      "v_mfma_f32_16x16x32_bf16 v[196:199], v[140:143], v[60:63], v[196:199]\n\t"
      "global_load_dwordx4 v[140:143], v227, s[58:59] sc1\n\t"
      "ds_read_b128 v[124:127], v244 offset:45056\n\t"
      "s_waitcnt vmcnt(7)\n\t"
      "v_cmp_eq_u32_e32 vcc, -1, v144\n\t"
      "s_cbranch_vccnz FP18R_%=\n\t"
      "FP18B_%=:\n\t"
      "v_mfma_f32_16x16x32_bf16 v[184:187], v[144:147], v[64:67], v[184:187]\n\t"
      "ds_read_b128 v[200:203], v244 offset:49152\n\t"
      "v_mfma_f32_16x16x32_bf16 v[188:191], v[144:147], v[68:71], v[188:191]\n\t"
      "ds_read_b128 v[204:207], v244 offset:53248\n\t"
      "v_mfma_f32_16x16x32_bf16 v[192:195], v[144:147], v[72:75], v[192:195]\n\t"
      "ds_read_b128 v[208:211], v244 offset:57344\n\t"
      "v_mfma_f32_16x16x32_bf16 v[196:199], v[144:147], v[76:79], v[196:199]\n\t"
      "global_load_dwordx4 v[144:147], v228, s[58:59] sc1\n\t"
      "ds_read_b128 v[212:215], v244 offset:61440\n\t"
      "s_waitcnt vmcnt(6)\n\t"
      "v_cmp_eq_u32_e32 vcc, -1, v148\n\t"
      "s_cbranch_vccnz FP19R_%=\n\t"
      "FP19B_%=:\n\t"
      "v_mfma_f32_16x16x32_bf16 v[184:187], v[148:151], v[80:83], v[184:187]\n\t"
      "v_mfma_f32_16x16x32_bf16 v[188:191], v[148:151], v[84:87], v[188:191]\n\t"
      "v_mfma_f32_16x16x32_bf16 v[192:195], v[148:151], v[88:91], v[192:195]\n\t"
      "s_waitcnt lgkmcnt(6)\n\t"
      "v_add_f32_e32 v112, v112, v116\n\t"
      "v_add_f32_e32 v113, v113, v117\n\t"
      "v_add_f32_e32 v114, v114, v118\n\t"
      "v_add_f32_e32 v115, v115, v119\n\t"
      "s_waitcnt lgkmcnt(4)\n\t"
      "v_add_f32_e32 v120, v120, v124\n\t"
      "v_add_f32_e32 v121, v121, v125\n\t"
      "v_add_f32_e32 v122, v122, v126\n\t"
      "v_add_f32_e32 v123, v123, v127\n\t"
      "s_waitcnt lgkmcnt(2)\n\t"
      "v_add_f32_e32 v200, v200, v204\n\t"
      "v_add_f32_e32 v201, v201, v205\n\t"
      "v_add_f32_e32 v202, v202, v206\n\t"
      "v_add_f32_e32 v203, v203, v207\n\t"
      "v_add_f32_e32 v112, v112, v120\n\t"
      "v_add_f32_e32 v113, v113, v121\n\t"
      "v_add_f32_e32 v114, v114, v122\n\t"
      "v_add_f32_e32 v115, v115, v123\n\t"
      "s_waitcnt lgkmcnt(0)\n\t"
      "v_add_f32_e32 v208, v208, v212\n\t"
      "v_add_f32_e32 v209, v209, v213\n\t"
      "v_add_f32_e32 v210, v210, v214\n\t"
      "v_add_f32_e32 v211, v211, v215\n\t"
      "v_add_f32_e32 v200, v200, v208\n\t"
      "v_add_f32_e32 v201, v201, v209\n\t"
      "v_add_f32_e32 v202, v202, v210\n\t"
      "v_add_f32_e32 v203, v203, v211\n\t"
      "v_add_f32_e32 v112, v112, v200\n\t"
      "v_add_f32_e32 v113, v113, v201\n\t"
      "v_add_f32_e32 v114, v114, v202\n\t"
      "v_add_f32_e32 v115, v115, v203\n\t"
      "v_cvt_pk_bf16_f32 v116, v112, v113\n\t"
      "v_cvt_pk_bf16_f32 v117, v114, v115\n\t"
      "global_store_dwordx2 v245, v[116:117], s[8:9] offset:32\n\t"
      "v_mfma_f32_16x16x32_bf16 v[196:199], v[148:151], v[92:95], v[196:199]\n\t"
      "global_load_dwordx4 v[148:151], v229, s[58:59] sc1\n\t"
      "s_waitcnt vmcnt(7)\n\t"
      "v_cmp_eq_u32_e32 vcc, -1, v152\n\t"
      "s_cbranch_vccnz FP20R_%=\n\t"
      "FP20B_%=:\n\t"
      "v_mfma_f32_16x16x32_bf16 v[184:187], v[152:155], v[96:99], v[184:187]\n\t"
      "v_mfma_f32_16x16x32_bf16 v[188:191], v[152:155], v[100:103], v[188:191]\n\t"
      "v_mfma_f32_16x16x32_bf16 v[192:195], v[152:155], v[104:107], v[192:195]\n\t"
      "v_mfma_f32_16x16x32_bf16 v[196:199], v[152:155], v[108:111], v[196:199]\n\t"
      "global_load_dwordx4 v[152:155], v230, s[58:59] sc1\n\t"
      "s_nop 3\n\t"
      "v_cmp_u_f32_e32 vcc, v184, v185\n\t"
      "v_cmp_u_f32_e64 s[48:49], v186, v187\n\t"
      "s_nop 0\n\t"
      "s_or_b64 vcc, vcc, s[48:49]\n\t"
      "s_cbranch_scc1 NP2R_%=\n\t"
      "NP2B_%=:\n\t"
      "s_waitcnt vmcnt(7)\n\t"
      "v_cmp_eq_u32_e32 vcc, -1, v128\n\t"
      "s_cbranch_vccnz FP21R_%=\n\t"
      "FP21B_%=:\n\t"
      "v_mfma_f32_16x16x32_bf16 v[200:203], v[128:131], v[0:3], 0\n\t"
      "ds_write_b128 v243, v[184:187]\n\t"
      "v_mfma_f32_16x16x32_bf16 v[204:207], v[128:131], v[4:7], 0\n\t"
      "v_mfma_f32_16x16x32_bf16 v[208:211], v[128:131], v[8:11], 0\n\t"
      "v_mfma_f32_16x16x32_bf16 v[212:215], v[128:131], v[12:15], 0\n\t"
      "global_load_dwordx4 v[128:131], v224, s[60:61] sc1\n\t"
      "ds_write_b128 v220, v[188:191]\n\t"
      "s_waitcnt vmcnt(7)\n\t"
      "v_cmp_eq_u32_e32 vcc, -1, v132\n\t"
      "s_cbranch_vccnz FP22R_%=\n\t"
      "FP22B_%=:\n\t"
      "v_mfma_f32_16x16x32_bf16 v[200:203], v[132:135], v[16:19], v[200:203]\n\t"
      "v_mfma_f32_16x16x32_bf16 v[204:207], v[132:135], v[20:23], v[204:207]\n\t"
      "v_mfma_f32_16x16x32_bf16 v[208:211], v[132:135], v[24:27], v[208:211]\n\t"
      "ds_write_b128 v221, v[192:195]\n\t"
      "v_mfma_f32_16x16x32_bf16 v[212:215], v[132:135], v[28:31], v[212:215]\n\t"
      "global_load_dwordx4 v[132:135], v225, s[60:61] sc1\n\t"
      "s_waitcnt vmcnt(7)\n\t"
      "v_cmp_eq_u32_e32 vcc, -1, v136\n\t"
      "s_cbranch_vccnz FP23R_%=\n\t"
      "FP23B_%=:\n\t"
      "v_mfma_f32_16x16x32_bf16 v[200:203], v[136:139], v[32:35], v[200:203]\n\t"
      "v_mfma_f32_16x16x32_bf16 v[204:207], v[136:139], v[36:39], v[204:207]\n\t"
      "ds_write_b128 v222, v[196:199]\n\t"
      "v_mfma_f32_16x16x32_bf16 v[208:211], v[136:139], v[40:43], v[208:211]\n\t"
      "v_mfma_f32_16x16x32_bf16 v[212:215], v[136:139], v[44:47], v[212:215]\n\t"
      "global_load_dwordx4 v[136:139], v226, s[60:61] sc1\n\t"
      "s_waitcnt vmcnt(7)\n\t"
      "v_cmp_eq_u32_e32 vcc, -1, v140\n\t"
      "s_cbranch_vccnz FP24R_%=\n\t"
      "FP24B_%=:\n\t"
      "v_mfma_f32_16x16x32_bf16 v[200:203], v[140:143], v[48:51], v[200:203]\n\t"
      "s_waitcnt lgkmcnt(0)\n\t"
      "s_barrier\n\t"
      "ds_read_b128 v[112:115], v244\n\t"
      "v_mfma_f32_16x16x32_bf16 v[204:207], v[140:143], v[52:55], v[204:207]\n\t"
      "ds_read_b128 v[116:119], v244 offset:4096\n\t"
      "v_mfma_f32_16x16x32_bf16 v[208:211], v[140:143], v[56:59], v[208:211]\n\t"
      "ds_read_b128 v[120:123], v244 offset:8192\n\t"
      "v_mfma_f32_16x16x32_bf16 v[212:215], v[140:143], v[60:63], v[212:215]\n\t"
      "global_load_dwordx4 v[140:143], v227, s[60:61] sc1\n\t"
      "ds_read_b128 v[124:127], v244 offset:12288\n\t"
      "s_waitcnt vmcnt(7)\n\t"
      "v_cmp_eq_u32_e32 vcc, -1, v144\n\t"
      "s_cbranch_vccnz FP25R_%=\n\t"
      "FP25B_%=:\n\t"
      "v_mfma_f32_16x16x32_bf16 v[200:203], v[144:147], v[64:67], v[200:203]\n\t"
      "ds_read_b128 v[184:187], v244 offset:16384\n\t"
      "v_mfma_f32_16x16x32_bf16 v[204:207], v[144:147], v[68:71], v[204:207]\n\t"
      "ds_read_b128 v[188:191], v244 offset:20480\n\t"
      "v_mfma_f32_16x16x32_bf16 v[208:211], v[144:147], v[72:75], v[208:211]\n\t"
      "ds_read_b128 v[192:195], v244 offset:24576\n\t"
      "v_mfma_f32_16x16x32_bf16 v[212:215], v[144:147], v[76:79], v[212:215]\n\t"
      "global_load_dwordx4 v[144:147], v228, s[60:61] sc1\n\t"
      "ds_read_b128 v[196:199], v244 offset:28672\n\t"
      "s_waitcnt vmcnt(6)\n\t"
      "v_cmp_eq_u32_e32 vcc, -1, v148\n\t"
      "s_cbranch_vccnz FP26R_%=\n\t"
      "FP26B_%=:\n\t"
      "v_mfma_f32_16x16x32_bf16 v[200:203], v[148:151], v[80:83], v[200:203]\n\t"
      "v_mfma_f32_16x16x32_bf16 v[204:207], v[148:151], v[84:87], v[204:207]\n\t"
      "v_mfma_f32_16x16x32_bf16 v[208:211], v[148:151], v[88:91], v[208:211]\n\t"
      "s_waitcnt lgkmcnt(6)\n\t"
      "v_add_f32_e32 v112, v112, v116\n\t"
      "v_add_f32_e32 v113, v113, v117\n\t"
      "v_add_f32_e32 v114, v114, v118\n\t"
      "v_add_f32_e32 v115, v115, v119\n\t"
      "s_waitcnt lgkmcnt(4)\n\t"
      "v_add_f32_e32 v120, v120, v124\n\t"
      "v_add_f32_e32 v121, v121, v125\n\t"
      "v_add_f32_e32 v122, v122, v126\n\t"
      "v_add_f32_e32 v123, v123, v127\n\t"
      "s_waitcnt lgkmcnt(2)\n\t"
      "v_add_f32_e32 v184, v184, v188\n\t"
      "v_add_f32_e32 v185, v185, v189\n\t"
      "v_add_f32_e32 v186, v186, v190\n\t"
      "v_add_f32_e32 v187, v187, v191\n\t"
      "v_add_f32_e32 v112, v112, v120\n\t"
      "v_add_f32_e32 v113, v113, v121\n\t"
      "v_add_f32_e32 v114, v114, v122\n\t"
      "v_add_f32_e32 v115, v115, v123\n\t"
      "s_waitcnt lgkmcnt(0)\n\t"
      "v_add_f32_e32 v192, v192, v196\n\t"
      "v_add_f32_e32 v193, v193, v197\n\t"
      "v_add_f32_e32 v194, v194, v198\n\t"
      "v_add_f32_e32 v195, v195, v199\n\t"
      "v_add_f32_e32 v184, v184, v192\n\t"
      "v_add_f32_e32 v185, v185, v193\n\t"
      "v_add_f32_e32 v186, v186, v194\n\t"
      "v_add_f32_e32 v187, v187, v195\n\t"
      "v_add_f32_e32 v112, v112, v184\n\t"
      "v_add_f32_e32 v113, v113, v185\n\t"
      "v_add_f32_e32 v114, v114, v186\n\t"
      "v_add_f32_e32 v115, v115, v187\n\t"
      "v_cvt_pk_bf16_f32 v116, v112, v113\n\t"
      "v_cvt_pk_bf16_f32 v117, v114, v115\n\t"
      "global_store_dwordx2 v245, v[116:117], s[8:9] offset:64\n\t"
      "v_mfma_f32_16x16x32_bf16 v[212:215], v[148:151], v[92:95], v[212:215]\n\t"
      "global_load_dwordx4 v[148:151], v229, s[60:61] sc1\n\t"
      "s_waitcnt vmcnt(7)\n\t"
      "v_cmp_eq_u32_e32 vcc, -1, v152\n\t"
      "s_cbranch_vccnz FP27R_%=\n\t"
      "FP27B_%=:\n\t"
      "v_mfma_f32_16x16x32_bf16 v[200:203], v[152:155], v[96:99], v[200:203]\n\t"
      "v_mfma_f32_16x16x32_bf16 v[204:207], v[152:155], v[100:103], v[204:207]\n\t"
      "v_mfma_f32_16x16x32_bf16 v[208:211], v[152:155], v[104:107], v[208:211]\n\t"
      "v_mfma_f32_16x16x32_bf16 v[212:215], v[152:155], v[108:111], v[212:215]\n\t"
      "global_load_dwordx4 v[152:155], v230, s[60:61] sc1\n\t"
      "s_nop 3\n\t"
      "v_cmp_u_f32_e32 vcc, v200, v201\n\t"
      "v_cmp_u_f32_e64 s[48:49], v202, v203\n\t"
      "s_nop 0\n\t"
      "s_or_b64 vcc, vcc, s[48:49]\n\t"
      "s_cbranch_scc1 NP3R_%=\n\t"
      "NP3B_%=:\n\t"
      "s_waitcnt vmcnt(7)\n\t"
      "v_cmp_eq_u32_e32 vcc, -1, v128\n\t"
      "s_cbranch_vccnz FP28R_%=\n\t"
      "FP28B_%=:\n\t"
      "v_mfma_f32_16x16x32_bf16 v[184:187], v[128:131], v[0:3], 0\n\t"
      "ds_write_b128 v243, v[200:203] offset:32768\n\t"
      "v_mfma_f32_16x16x32_bf16 v[188:191], v[128:131], v[4:7], 0\n\t"
      "v_mfma_f32_16x16x32_bf16 v[192:195], v[128:131], v[8:11], 0\n\t"
      "v_mfma_f32_16x16x32_bf16 v[196:199], v[128:131], v[12:15], 0\n\t"
      "global_load_dwordx4 v[128:131], v224, s[62:63] sc1\n\t"
      "ds_write_b128 v220, v[204:207] offset:32768\n\t"
      "s_waitcnt vmcnt(7)\n\t"
      "v_cmp_eq_u32_e32 vcc, -1, v132\n\t"
      "s_cbranch_vccnz FP29R_%=\n\t"
      "FP29B_%=:\n\t"
      "v_mfma_f32_16x16x32_bf16 v[184:187], v[132:135], v[16:19], v[184:187]\n\t"
      "v_mfma_f32_16x16x32_bf16 v[188:191], v[132:135], v[20:23], v[188:191]\n\t"
      "v_mfma_f32_16x16x32_bf16 v[192:195], v[132:135], v[24:27], v[192:195]\n\t"
      "ds_write_b128 v221, v[208:211] offset:32768\n\t"
      "v_mfma_f32_16x16x32_bf16 v[196:199], v[132:135], v[28:31], v[196:199]\n\t"
      "global_load_dwordx4 v[132:135], v225, s[62:63] sc1\n\t"
      "s_waitcnt vmcnt(7)\n\t"
      "v_cmp_eq_u32_e32 vcc, -1, v136\n\t"
      "s_cbranch_vccnz FP30R_%=\n\t"
      "FP30B_%=:\n\t"
      "v_mfma_f32_16x16x32_bf16 v[184:187], v[136:139], v[32:35], v[184:187]\n\t"
      "v_mfma_f32_16x16x32_bf16 v[188:191], v[136:139], v[36:39], v[188:191]\n\t"
      "ds_write_b128 v222, v[212:215] offset:32768\n\t"
      "v_mfma_f32_16x16x32_bf16 v[192:195], v[136:139], v[40:43], v[192:195]\n\t"
      "v_mfma_f32_16x16x32_bf16 v[196:199], v[136:139], v[44:47], v[196:199]\n\t"
      "global_load_dwordx4 v[136:139], v226, s[62:63] sc1\n\t"
      "s_waitcnt vmcnt(7)\n\t"
      "v_cmp_eq_u32_e32 vcc, -1, v140\n\t"
      "s_cbranch_vccnz FP31R_%=\n\t"
      "FP31B_%=:\n\t"
      "v_mfma_f32_16x16x32_bf16 v[184:187], v[140:143], v[48:51], v[184:187]\n\t"
      "s_waitcnt lgkmcnt(0)\n\t"
      "s_barrier\n\t"
      "ds_read_b128 v[112:115], v244 offset:32768\n\t"
      "v_mfma_f32_16x16x32_bf16 v[188:191], v[140:143], v[52:55], v[188:191]\n\t"
      "ds_read_b128 v[116:119], v244 offset:36864\n\t"
      "v_mfma_f32_16x16x32_bf16 v[192:195], v[140:143], v[56:59], v[192:195]\n\t"
      "ds_read_b128 v[120:123], v244 offset:40960\n\t"
      "v_mfma_f32_16x16x32_bf16 v[196:199], v[140:143], v[60:63], v[196:199]\n\t"
      "global_load_dwordx4 v[140:143], v227, s[62:63] sc1\n\t"
      "ds_read_b128 v[124:127], v244 offset:45056\n\t"
      "s_waitcnt vmcnt(7)\n\t"
      "v_cmp_eq_u32_e32 vcc, -1, v144\n\t"
      "s_cbranch_vccnz FP32R_%=\n\t"
      "FP32B_%=:\n\t"
      "v_mfma_f32_16x16x32_bf16 v[184:187], v[144:147], v[64:67], v[184:187]\n\t"
      "ds_read_b128 v[200:203], v244 offset:49152\n\t"
      "v_mfma_f32_16x16x32_bf16 v[188:191], v[144:147], v[68:71], v[188:191]\n\t"
      "ds_read_b128 v[204:207], v244 offset:53248\n\t"
      "v_mfma_f32_16x16x32_bf16 v[192:195], v[144:147], v[72:75], v[192:195]\n\t"
      "ds_read_b128 v[208:211], v244 offset:57344\n\t"
      "v_mfma_f32_16x16x32_bf16 v[196:199], v[144:147], v[76:79], v[196:199]\n\t"
      "global_load_dwordx4 v[144:147], v228, s[62:63] sc1\n\t"
      "ds_read_b128 v[212:215], v244 offset:61440\n\t"
      "s_waitcnt vmcnt(6)\n\t"
      "v_cmp_eq_u32_e32 vcc, -1, v148\n\t"
      "s_cbranch_vccnz FP33R_%=\n\t"
      "FP33B_%=:\n\t"
      "v_mfma_f32_16x16x32_bf16 v[184:187], v[148:151], v[80:83], v[184:187]\n\t"
      "v_mfma_f32_16x16x32_bf16 v[188:191], v[148:151], v[84:87], v[188:191]\n\t"
      "v_mfma_f32_16x16x32_bf16 v[192:195], v[148:151], v[88:91], v[192:195]\n\t"
      "s_waitcnt lgkmcnt(6)\n\t"
      "v_add_f32_e32 v112, v112, v116\n\t"
      "v_add_f32_e32 v113, v113, v117\n\t"
      "v_add_f32_e32 v114, v114, v118\n\t"
      "v_add_f32_e32 v115, v115, v119\n\t"
      "s_waitcnt lgkmcnt(4)\n\t"
      "v_add_f32_e32 v120, v120, v124\n\t"
      "v_add_f32_e32 v121, v121, v125\n\t"
      "v_add_f32_e32 v122, v122, v126\n\t"
      "v_add_f32_e32 v123, v123, v127\n\t"
      "s_waitcnt lgkmcnt(2)\n\t"
      "v_add_f32_e32 v200, v200, v204\n\t"
      "v_add_f32_e32 v201, v201, v205\n\t"
      "v_add_f32_e32 v202, v202, v206\n\t"
      "v_add_f32_e32 v203, v203, v207\n\t"
      "v_add_f32_e32 v112, v112, v120\n\t"
      "v_add_f32_e32 v113, v113, v121\n\t"
      "v_add_f32_e32 v114, v114, v122\n\t"
      "v_add_f32_e32 v115, v115, v123\n\t"
      "s_waitcnt lgkmcnt(0)\n\t"
      "v_add_f32_e32 v208, v208, v212\n\t"
      "v_add_f32_e32 v209, v209, v213\n\t"
      "v_add_f32_e32 v210, v210, v214\n\t"
      "v_add_f32_e32 v211, v211, v215\n\t"
      "v_add_f32_e32 v200, v200, v208\n\t"
      "v_add_f32_e32 v201, v201, v209\n\t"
      "v_add_f32_e32 v202, v202, v210\n\t"
      "v_add_f32_e32 v203, v203, v211\n\t"
      "v_add_f32_e32 v112, v112, v200\n\t"
      "v_add_f32_e32 v113, v113, v201\n\t"
      "v_add_f32_e32 v114, v114, v202\n\t"
      "v_add_f32_e32 v115, v115, v203\n\t"
      "v_cvt_pk_bf16_f32 v116, v112, v113\n\t"
      "v_cvt_pk_bf16_f32 v117, v114, v115\n\t"
      "global_store_dwordx2 v245, v[116:117], s[8:9] offset:96\n\t"
      "v_mfma_f32_16x16x32_bf16 v[196:199], v[148:151], v[92:95], v[196:199]\n\t"
      "global_load_dwordx4 v[148:151], v229, s[62:63] sc1\n\t"
      "s_waitcnt vmcnt(7)\n\t"
      "v_cmp_eq_u32_e32 vcc, -1, v152\n\t"
      "s_cbranch_vccnz FP34R_%=\n\t"
      "FP34B_%=:\n\t"
      "v_mfma_f32_16x16x32_bf16 v[184:187], v[152:155], v[96:99], v[184:187]\n\t"
      "v_mfma_f32_16x16x32_bf16 v[188:191], v[152:155], v[100:103], v[188:191]\n\t"
      "v_mfma_f32_16x16x32_bf16 v[192:195], v[152:155], v[104:107], v[192:195]\n\t"
      "v_mfma_f32_16x16x32_bf16 v[196:199], v[152:155], v[108:111], v[196:199]\n\t"
      "global_load_dwordx4 v[152:155], v230, s[62:63] sc1\n\t"
      "s_nop 3\n\t"
      "v_cmp_u_f32_e32 vcc, v184, v185\n\t"
      "v_cmp_u_f32_e64 s[48:49], v186, v187\n\t"
      "s_nop 0\n\t"
      "s_or_b64 vcc, vcc, s[48:49]\n\t"
      "s_cbranch_scc1 NP4R_%=\n\t"
      "NP4B_%=:\n\t"
      "s_waitcnt vmcnt(7)\n\t"
      "v_cmp_eq_u32_e32 vcc, -1, v128\n\t"
      "s_cbranch_vccnz FP35R_%=\n\t"
      "FP35B_%=:\n\t"
      "v_mfma_f32_16x16x32_bf16 v[200:203], v[128:131], v[0:3], 0\n\t"
      "ds_write_b128 v243, v[184:187]\n\t"
      "v_mfma_f32_16x16x32_bf16 v[204:207], v[128:131], v[4:7], 0\n\t"
      "v_mfma_f32_16x16x32_bf16 v[208:211], v[128:131], v[8:11], 0\n\t"
      "v_mfma_f32_16x16x32_bf16 v[212:215], v[128:131], v[12:15], 0\n\t"
      "global_load_dwordx4 v[128:131], v224, s[64:65] sc1\n\t"
      "ds_write_b128 v220, v[188:191]\n\t"
      "s_waitcnt vmcnt(7)\n\t"
      "v_cmp_eq_u32_e32 vcc, -1, v132\n\t"
      "s_cbranch_vccnz FP36R_%=\n\t"
      "FP36B_%=:\n\t"
      "v_mfma_f32_16x16x32_bf16 v[200:203], v[132:135], v[16:19], v[200:203]\n\t"
      "v_mfma_f32_16x16x32_bf16 v[204:207], v[132:135], v[20:23], v[204:207]\n\t"
      "v_mfma_f32_16x16x32_bf16 v[208:211], v[132:135], v[24:27], v[208:211]\n\t"
      "ds_write_b128 v221, v[192:195]\n\t"
      "v_mfma_f32_16x16x32_bf16 v[212:215], v[132:135], v[28:31], v[212:215]\n\t"
      "global_load_dwordx4 v[132:135], v225, s[64:65] sc1\n\t"
      "s_waitcnt vmcnt(7)\n\t"
      "v_cmp_eq_u32_e32 vcc, -1, v136\n\t"
      "s_cbranch_vccnz FP37R_%=\n\t"
      "FP37B_%=:\n\t"
      "v_mfma_f32_16x16x32_bf16 v[200:203], v[136:139], v[32:35], v[200:203]\n\t"
      "v_mfma_f32_16x16x32_bf16 v[204:207], v[136:139], v[36:39], v[204:207]\n\t"
      "ds_write_b128 v222, v[196:199]\n\t"
      "v_mfma_f32_16x16x32_bf16 v[208:211], v[136:139], v[40:43], v[208:211]\n\t"
      "v_mfma_f32_16x16x32_bf16 v[212:215], v[136:139], v[44:47], v[212:215]\n\t"
      "global_load_dwordx4 v[136:139], v226, s[64:65] sc1\n\t"
      "s_waitcnt vmcnt(7)\n\t"
      "v_cmp_eq_u32_e32 vcc, -1, v140\n\t"
      "s_cbranch_vccnz FP38R_%=\n\t"
      "FP38B_%=:\n\t"
      "v_mfma_f32_16x16x32_bf16 v[200:203], v[140:143], v[48:51], v[200:203]\n\t"
      "s_waitcnt lgkmcnt(0)\n\t"
      "s_barrier\n\t"
      "ds_read_b128 v[112:115], v244\n\t"
      "v_mfma_f32_16x16x32_bf16 v[204:207], v[140:143], v[52:55], v[204:207]\n\t"
      "ds_read_b128 v[116:119], v244 offset:4096\n\t"
      "v_mfma_f32_16x16x32_bf16 v[208:211], v[140:143], v[56:59], v[208:211]\n\t"
      "ds_read_b128 v[120:123], v244 offset:8192\n\t"
      "v_mfma_f32_16x16x32_bf16 v[212:215], v[140:143], v[60:63], v[212:215]\n\t"
      "global_load_dwordx4 v[140:143], v227, s[64:65] sc1\n\t"
      "ds_read_b128 v[124:127], v244 offset:12288\n\t"
      "s_waitcnt vmcnt(7)\n\t"
      "v_cmp_eq_u32_e32 vcc, -1, v144\n\t"
      "s_cbranch_vccnz FP39R_%=\n\t"
      "FP39B_%=:\n\t"
      "v_mfma_f32_16x16x32_bf16 v[200:203], v[144:147], v[64:67], v[200:203]\n\t"
      "ds_read_b128 v[184:187], v244 offset:16384\n\t"
      "v_mfma_f32_16x16x32_bf16 v[204:207], v[144:147], v[68:71], v[204:207]\n\t"
      "ds_read_b128 v[188:191], v244 offset:20480\n\t"
      "v_mfma_f32_16x16x32_bf16 v[208:211], v[144:147], v[72:75], v[208:211]\n\t"
      "ds_read_b128 v[192:195], v244 offset:24576\n\t"
      "v_mfma_f32_16x16x32_bf16 v[212:215], v[144:147], v[76:79], v[212:215]\n\t"
      "global_load_dwordx4 v[144:147], v228, s[64:65] sc1\n\t"
      "ds_read_b128 v[196:199], v244 offset:28672\n\t"
      "s_waitcnt vmcnt(6)\n\t"
      "v_cmp_eq_u32_e32 vcc, -1, v148\n\t"
      "s_cbranch_vccnz FP40R_%=\n\t"
      "FP40B_%=:\n\t"
      "v_mfma_f32_16x16x32_bf16 v[200:203], v[148:151], v[80:83], v[200:203]\n\t"
      "v_mfma_f32_16x16x32_bf16 v[204:207], v[148:151], v[84:87], v[204:207]\n\t"
      "v_mfma_f32_16x16x32_bf16 v[208:211], v[148:151], v[88:91], v[208:211]\n\t"
      "s_waitcnt lgkmcnt(6)\n\t"
      "v_add_f32_e32 v112, v112, v116\n\t"
      "v_add_f32_e32 v113, v113, v117\n\t"
      "v_add_f32_e32 v114, v114, v118\n\t"
      "v_add_f32_e32 v115, v115, v119\n\t"
      "s_waitcnt lgkmcnt(4)\n\t"
      "v_add_f32_e32 v120, v120, v124\n\t"
      "v_add_f32_e32 v121, v121, v125\n\t"
      "v_add_f32_e32 v122, v122, v126\n\t"
      "v_add_f32_e32 v123, v123, v127\n\t"
      "s_waitcnt lgkmcnt(2)\n\t"
      "v_add_f32_e32 v184, v184, v188\n\t"
      "v_add_f32_e32 v185, v185, v189\n\t"
      "v_add_f32_e32 v186, v186, v190\n\t"
      "v_add_f32_e32 v187, v187, v191\n\t"
      "v_add_f32_e32 v112, v112, v120\n\t"
      "v_add_f32_e32 v113, v113, v121\n\t"
      "v_add_f32_e32 v114, v114, v122\n\t"
      "v_add_f32_e32 v115, v115, v123\n\t"
      "s_waitcnt lgkmcnt(0)\n\t"
      "v_add_f32_e32 v192, v192, v196\n\t"
      "v_add_f32_e32 v193, v193, v197\n\t"
      "v_add_f32_e32 v194, v194, v198\n\t"
      "v_add_f32_e32 v195, v195, v199\n\t"
      "v_add_f32_e32 v184, v184, v192\n\t"
      "v_add_f32_e32 v185, v185, v193\n\t"
      "v_add_f32_e32 v186, v186, v194\n\t"
      "v_add_f32_e32 v187, v187, v195\n\t"
      "v_add_f32_e32 v112, v112, v184\n\t"
      "v_add_f32_e32 v113, v113, v185\n\t"
      "v_add_f32_e32 v114, v114, v186\n\t"
      "v_add_f32_e32 v115, v115, v187\n\t"
      "v_cvt_pk_bf16_f32 v116, v112, v113\n\t"
      "v_cvt_pk_bf16_f32 v117, v114, v115\n\t"
      "global_store_dwordx2 v245, v[116:117], s[8:9] offset:128\n\t"
      "v_mfma_f32_16x16x32_bf16 v[212:215], v[148:151], v[92:95], v[212:215]\n\t"
      "global_load_dwordx4 v[148:151], v229, s[64:65] sc1\n\t"
      "s_waitcnt vmcnt(7)\n\t"
      "v_cmp_eq_u32_e32 vcc, -1, v152\n\t"
      "s_cbranch_vccnz FP41R_%=\n\t"
      "FP41B_%=:\n\t"
      "v_mfma_f32_16x16x32_bf16 v[200:203], v[152:155], v[96:99], v[200:203]\n\t"
      "v_mfma_f32_16x16x32_bf16 v[204:207], v[152:155], v[100:103], v[204:207]\n\t"
      "v_mfma_f32_16x16x32_bf16 v[208:211], v[152:155], v[104:107], v[208:211]\n\t"
      "v_mfma_f32_16x16x32_bf16 v[212:215], v[152:155], v[108:111], v[212:215]\n\t"
      "global_load_dwordx4 v[152:155], v230, s[64:65] sc1\n\t"
      "s_nop 3\n\t"
      "v_cmp_u_f32_e32 vcc, v200, v201\n\t"
      "v_cmp_u_f32_e64 s[48:49], v202, v203\n\t"
      "s_nop 0\n\t"
      "s_or_b64 vcc, vcc, s[48:49]\n\t"
      "s_cbranch_scc1 NP5R_%=\n\t"
      "NP5B_%=:\n\t"
      "s_waitcnt vmcnt(7)\n\t"
      "v_cmp_eq_u32_e32 vcc, -1, v128\n\t"
      "s_cbranch_vccnz FP42R_%=\n\t"
      "FP42B_%=:\n\t"
      "v_mfma_f32_16x16x32_bf16 v[184:187], v[128:131], v[0:3], 0\n\t"
      "ds_write_b128 v243, v[200:203] offset:32768\n\t"
      "v_mfma_f32_16x16x32_bf16 v[188:191], v[128:131], v[4:7], 0\n\t"
      "v_mfma_f32_16x16x32_bf16 v[192:195], v[128:131], v[8:11], 0\n\t"
      "v_mfma_f32_16x16x32_bf16 v[196:199], v[128:131], v[12:15], 0\n\t"
      "ds_write_b128 v220, v[204:207] offset:32768\n\t"
      "s_waitcnt vmcnt(6)\n\t"
      "v_cmp_eq_u32_e32 vcc, -1, v132\n\t"
      "s_cbranch_vccnz FP43R_%=\n\t"
      "FP43B_%=:\n\t"
      "v_mfma_f32_16x16x32_bf16 v[184:187], v[132:135], v[16:19], v[184:187]\n\t"
      "v_mfma_f32_16x16x32_bf16 v[188:191], v[132:135], v[20:23], v[188:191]\n\t"
      "v_mfma_f32_16x16x32_bf16 v[192:195], v[132:135], v[24:27], v[192:195]\n\t"
      "ds_write_b128 v221, v[208:211] offset:32768\n\t"
      "v_mfma_f32_16x16x32_bf16 v[196:199], v[132:135], v[28:31], v[196:199]\n\t"
      "s_waitcnt vmcnt(5)\n\t"
      "v_cmp_eq_u32_e32 vcc, -1, v136\n\t"
      "s_cbranch_vccnz FP44R_%=\n\t"
      "FP44B_%=:\n\t"
      "v_mfma_f32_16x16x32_bf16 v[184:187], v[136:139], v[32:35], v[184:187]\n\t"
      "v_mfma_f32_16x16x32_bf16 v[188:191], v[136:139], v[36:39], v[188:191]\n\t"
      "ds_write_b128 v222, v[212:215] offset:32768\n\t"
      "v_mfma_f32_16x16x32_bf16 v[192:195], v[136:139], v[40:43], v[192:195]\n\t"
      "v_mfma_f32_16x16x32_bf16 v[196:199], v[136:139], v[44:47], v[196:199]\n\t"
      "s_waitcnt vmcnt(4)\n\t"
      "v_cmp_eq_u32_e32 vcc, -1, v140\n\t"
      "s_cbranch_vccnz FP45R_%=\n\t"
      "FP45B_%=:\n\t"
      "v_mfma_f32_16x16x32_bf16 v[184:187], v[140:143], v[48:51], v[184:187]\n\t"
      "s_waitcnt lgkmcnt(0)\n\t"
      "s_barrier\n\t"
      "ds_read_b128 v[112:115], v244 offset:32768\n\t"
      "v_mfma_f32_16x16x32_bf16 v[188:191], v[140:143], v[52:55], v[188:191]\n\t"
      "ds_read_b128 v[116:119], v244 offset:36864\n\t"
      "v_mfma_f32_16x16x32_bf16 v[192:195], v[140:143], v[56:59], v[192:195]\n\t"
      "ds_read_b128 v[120:123], v244 offset:40960\n\t"
      "v_mfma_f32_16x16x32_bf16 v[196:199], v[140:143], v[60:63], v[196:199]\n\t"
      "ds_read_b128 v[124:127], v244 offset:45056\n\t"
      "s_waitcnt vmcnt(3)\n\t"
      "v_cmp_eq_u32_e32 vcc, -1, v144\n\t"
      "s_cbranch_vccnz FP46R_%=\n\t"
      "FP46B_%=:\n\t"
      "v_mfma_f32_16x16x32_bf16 v[184:187], v[144:147], v[64:67], v[184:187]\n\t"
      "ds_read_b128 v[200:203], v244 offset:49152\n\t"
      "v_mfma_f32_16x16x32_bf16 v[188:191], v[144:147], v[68:71], v[188:191]\n\t"
      "ds_read_b128 v[204:207], v244 offset:53248\n\t"
      "v_mfma_f32_16x16x32_bf16 v[192:195], v[144:147], v[72:75], v[192:195]\n\t"
      "ds_read_b128 v[208:211], v244 offset:57344\n\t"
      "v_mfma_f32_16x16x32_bf16 v[196:199], v[144:147], v[76:79], v[196:199]\n\t"
      "ds_read_b128 v[212:215], v244 offset:61440\n\t"
      "s_waitcnt vmcnt(1)\n\t"
      "v_cmp_eq_u32_e32 vcc, -1, v148\n\t"
      "s_cbranch_vccnz FP47R_%=\n\t"
      "FP47B_%=:\n\t"
      "v_mfma_f32_16x16x32_bf16 v[184:187], v[148:151], v[80:83], v[184:187]\n\t"
      "v_mfma_f32_16x16x32_bf16 v[188:191], v[148:151], v[84:87], v[188:191]\n\t"
      "v_mfma_f32_16x16x32_bf16 v[192:195], v[148:151], v[88:91], v[192:195]\n\t"
      "s_waitcnt lgkmcnt(6)\n\t"
      "v_add_f32_e32 v112, v112, v116\n\t"
      "v_add_f32_e32 v113, v113, v117\n\t"
      "v_add_f32_e32 v114, v114, v118\n\t"
      "v_add_f32_e32 v115, v115, v119\n\t"
      "s_waitcnt lgkmcnt(4)\n\t"
      "v_add_f32_e32 v120, v120, v124\n\t"
      "v_add_f32_e32 v121, v121, v125\n\t"
      "v_add_f32_e32 v122, v122, v126\n\t"
      "v_add_f32_e32 v123, v123, v127\n\t"
      "s_waitcnt lgkmcnt(2)\n\t"
      "v_add_f32_e32 v200, v200, v204\n\t"
      "v_add_f32_e32 v201, v201, v205\n\t"
      "v_add_f32_e32 v202, v202, v206\n\t"
      "v_add_f32_e32 v203, v203, v207\n\t"
      "v_add_f32_e32 v112, v112, v120\n\t"
      "v_add_f32_e32 v113, v113, v121\n\t"
      "v_add_f32_e32 v114, v114, v122\n\t"
      "v_add_f32_e32 v115, v115, v123\n\t"
      "s_waitcnt lgkmcnt(0)\n\t"
      "v_add_f32_e32 v208, v208, v212\n\t"
      "v_add_f32_e32 v209, v209, v213\n\t"
      "v_add_f32_e32 v210, v210, v214\n\t"
      "v_add_f32_e32 v211, v211, v215\n\t"
      "v_add_f32_e32 v200, v200, v208\n\t"
      "v_add_f32_e32 v201, v201, v209\n\t"
      "v_add_f32_e32 v202, v202, v210\n\t"
      "v_add_f32_e32 v203, v203, v211\n\t"
      "v_add_f32_e32 v112, v112, v200\n\t"
      "v_add_f32_e32 v113, v113, v201\n\t"
      "v_add_f32_e32 v114, v114, v202\n\t"
      "v_add_f32_e32 v115, v115, v203\n\t"
      "v_cvt_pk_bf16_f32 v116, v112, v113\n\t"
      "v_cvt_pk_bf16_f32 v117, v114, v115\n\t"
      "global_store_dwordx2 v245, v[116:117], s[8:9] offset:160\n\t"
      "v_mfma_f32_16x16x32_bf16 v[196:199], v[148:151], v[92:95], v[196:199]\n\t"
      "s_waitcnt vmcnt(1)\n\t"
      "v_cmp_eq_u32_e32 vcc, -1, v152\n\t"
      "s_cbranch_vccnz FP48R_%=\n\t"
      "FP48B_%=:\n\t"
      "v_mfma_f32_16x16x32_bf16 v[184:187], v[152:155], v[96:99], v[184:187]\n\t"
      "v_mfma_f32_16x16x32_bf16 v[188:191], v[152:155], v[100:103], v[188:191]\n\t"
      "v_mfma_f32_16x16x32_bf16 v[192:195], v[152:155], v[104:107], v[192:195]\n\t"
      "v_mfma_f32_16x16x32_bf16 v[196:199], v[152:155], v[108:111], v[196:199]\n\t"
      "s_nop 4\n\t"
      "v_cmp_u_f32_e32 vcc, v184, v185\n\t"
      "v_cmp_u_f32_e64 s[48:49], v186, v187\n\t"
      "s_nop 0\n\t"
      "s_or_b64 vcc, vcc, s[48:49]\n\t"
      "s_cbranch_scc1 NP6R_%=\n\t"
      "NP6B_%=:\n\t"
      "ds_write_b128 v243, v[184:187]\n\t"
      "ds_write_b128 v220, v[188:191]\n\t"
      "ds_write_b128 v221, v[192:195]\n\t"
      "ds_write_b128 v222, v[196:199]\n\t"
      "s_waitcnt lgkmcnt(0)\n\t"
      "s_barrier\n\t"
      "ds_read_b128 v[112:115], v244\n\t"
      "ds_read_b128 v[116:119], v244 offset:4096\n\t"
      "ds_read_b128 v[120:123], v244 offset:8192\n\t"
      "ds_read_b128 v[124:127], v244 offset:12288\n\t"
      "ds_read_b128 v[184:187], v244 offset:16384\n\t"
      "ds_read_b128 v[188:191], v244 offset:20480\n\t"
      "ds_read_b128 v[192:195], v244 offset:24576\n\t"
      "ds_read_b128 v[196:199], v244 offset:28672\n\t"
      "s_waitcnt lgkmcnt(6)\n\t"
      "v_add_f32_e32 v112, v112, v116\n\t"
      "v_add_f32_e32 v113, v113, v117\n\t"
      "v_add_f32_e32 v114, v114, v118\n\t"
      "v_add_f32_e32 v115, v115, v119\n\t"
      "s_waitcnt lgkmcnt(4)\n\t"
      "v_add_f32_e32 v120, v120, v124\n\t"
      "v_add_f32_e32 v121, v121, v125\n\t"
      "v_add_f32_e32 v122, v122, v126\n\t"
      "v_add_f32_e32 v123, v123, v127\n\t"
      "s_waitcnt lgkmcnt(2)\n\t"
      "v_add_f32_e32 v184, v184, v188\n\t"
      "v_add_f32_e32 v185, v185, v189\n\t"
      "v_add_f32_e32 v186, v186, v190\n\t"
      "v_add_f32_e32 v187, v187, v191\n\t"
      "v_add_f32_e32 v112, v112, v120\n\t"
      "v_add_f32_e32 v113, v113, v121\n\t"
      "v_add_f32_e32 v114, v114, v122\n\t"
      "v_add_f32_e32 v115, v115, v123\n\t"
      "s_waitcnt lgkmcnt(0)\n\t"
      "v_add_f32_e32 v192, v192, v196\n\t"
      "v_add_f32_e32 v193, v193, v197\n\t"
      "v_add_f32_e32 v194, v194, v198\n\t"
      "v_add_f32_e32 v195, v195, v199\n\t"
      "v_add_f32_e32 v184, v184, v192\n\t"
      "v_add_f32_e32 v185, v185, v193\n\t"
      "v_add_f32_e32 v186, v186, v194\n\t"
      "v_add_f32_e32 v187, v187, v195\n\t"
      "v_add_f32_e32 v112, v112, v184\n\t"
      "v_add_f32_e32 v113, v113, v185\n\t"
      "v_add_f32_e32 v114, v114, v186\n\t"
      "v_add_f32_e32 v115, v115, v187\n\t"
      "v_cvt_pk_bf16_f32 v116, v112, v113\n\t"
      "v_cvt_pk_bf16_f32 v117, v114, v115\n\t"
      "global_store_dwordx2 v245, v[116:117], s[8:9] offset:192\n\t"
      "GX_%=:\n\t"
      "s_bitcmp1_b32 s16, 5\n\t"
      "s_cbranch_scc0 EP_%=\n\t"
      "v_mov_b32_e32 v0, s26\n\t"
      "v_mov_b32_e32 v1, s27\n\t"
      "v_mov_b32_e32 v2, 0\n\t"
      "s_mov_b64 exec, 1\n\t"
      "global_store_dwordx2 v2, v[0:1], s[24:25] sc1\n\t"
      "s_mov_b64 exec, -1\n\t"
      "EP_%=:\n\t"
      "s_endpgm\n\t"
      ".p2align 6\n\t"
      "GQ_%=:\n\t"
      "s_waitcnt vmcnt(6)\n\t"
      "v_max3_u32 v234, v128, v129, v130\n\t"
      "v_max_u32_e32 v234, v234, v131\n\t"
      "v_cmp_eq_u32_e32 vcc, -1, v234\n\t"
      "s_cbranch_vccnz FQ0R_%=\n\t"
      "FQ0B_%=:\n\t"
      "v_mfma_f32_16x16x32_bf16 v[184:187], v[128:131], v[0:3], v[156:159]\n\t"
      "v_mfma_f32_16x16x32_bf16 v[188:191], v[128:131], v[4:7], 0\n\t"
      "v_mfma_f32_16x16x32_bf16 v[192:195], v[128:131], v[8:11], 0\n\t"
      "v_mfma_f32_16x16x32_bf16 v[196:199], v[128:131], v[12:15], 0\n\t"
      "global_load_dwordx4 v[128:131], v224, s[54:55] sc1\n\t"
      "s_waitcnt vmcnt(6)\n\t"
      "v_max3_u32 v234, v132, v133, v134\n\t"
      "v_max_u32_e32 v234, v234, v135\n\t"
      "v_cmp_eq_u32_e32 vcc, -1, v234\n\t"
      "s_cbranch_vccnz FQ1R_%=\n\t"
      "FQ1B_%=:\n\t"
      "v_mfma_f32_16x16x32_bf16 v[184:187], v[132:135], v[16:19], v[184:187]\n\t"
      "v_mfma_f32_16x16x32_bf16 v[188:191], v[132:135], v[20:23], v[188:191]\n\t"
      "v_mfma_f32_16x16x32_bf16 v[192:195], v[132:135], v[24:27], v[192:195]\n\t"
      "v_mfma_f32_16x16x32_bf16 v[196:199], v[132:135], v[28:31], v[196:199]\n\t"
      "global_load_dwordx4 v[132:135], v225, s[54:55] sc1\n\t"
      "s_waitcnt vmcnt(6)\n\t"
      "v_max3_u32 v234, v136, v137, v138\n\t"
      "v_max_u32_e32 v234, v234, v139\n\t"
      "v_cmp_eq_u32_e32 vcc, -1, v234\n\t"
      "s_cbranch_vccnz FQ2R_%=\n\t"
      "FQ2B_%=:\n\t"
      "v_mfma_f32_16x16x32_bf16 v[184:187], v[136:139], v[32:35], v[184:187]\n\t"
      "v_mfma_f32_16x16x32_bf16 v[188:191], v[136:139], v[36:39], v[188:191]\n\t"
      "v_mfma_f32_16x16x32_bf16 v[192:195], v[136:139], v[40:43], v[192:195]\n\t"
      "v_mfma_f32_16x16x32_bf16 v[196:199], v[136:139], v[44:47], v[196:199]\n\t"
      "global_load_dwordx4 v[136:139], v226, s[54:55] sc1\n\t"
      "s_waitcnt vmcnt(6)\n\t"
      "v_max3_u32 v234, v140, v141, v142\n\t"
      "v_max_u32_e32 v234, v234, v143\n\t"
      "v_cmp_eq_u32_e32 vcc, -1, v234\n\t"
      "s_cbranch_vccnz FQ3R_%=\n\t"
      "FQ3B_%=:\n\t"
      "v_mfma_f32_16x16x32_bf16 v[184:187], v[140:143], v[48:51], v[184:187]\n\t"
      "v_mfma_f32_16x16x32_bf16 v[188:191], v[140:143], v[52:55], v[188:191]\n\t"
      "v_mfma_f32_16x16x32_bf16 v[192:195], v[140:143], v[56:59], v[192:195]\n\t"
      "v_mfma_f32_16x16x32_bf16 v[196:199], v[140:143], v[60:63], v[196:199]\n\t"
      "global_load_dwordx4 v[140:143], v227, s[54:55] sc1\n\t"
      "s_waitcnt vmcnt(6)\n\t"
      "v_max3_u32 v234, v144, v145, v146\n\t"
      "v_max_u32_e32 v234, v234, v147\n\t"
      "v_cmp_eq_u32_e32 vcc, -1, v234\n\t"
      "s_cbranch_vccnz FQ4R_%=\n\t"
      "FQ4B_%=:\n\t"
      "v_mfma_f32_16x16x32_bf16 v[184:187], v[144:147], v[64:67], v[184:187]\n\t"
      "v_mfma_f32_16x16x32_bf16 v[188:191], v[144:147], v[68:71], v[188:191]\n\t"
      "v_mfma_f32_16x16x32_bf16 v[192:195], v[144:147], v[72:75], v[192:195]\n\t"
      "v_mfma_f32_16x16x32_bf16 v[196:199], v[144:147], v[76:79], v[196:199]\n\t"
      "global_load_dwordx4 v[144:147], v228, s[54:55] sc1\n\t"
      "s_waitcnt vmcnt(6)\n\t"
      "v_max3_u32 v234, v148, v149, v150\n\t"
      "v_max_u32_e32 v234, v234, v151\n\t"
      "v_cmp_eq_u32_e32 vcc, -1, v234\n\t"
      "s_cbranch_vccnz FQ5R_%=\n\t"
      "FQ5B_%=:\n\t"
      "v_mfma_f32_16x16x32_bf16 v[184:187], v[148:151], v[80:83], v[184:187]\n\t"
      "v_mfma_f32_16x16x32_bf16 v[188:191], v[148:151], v[84:87], v[188:191]\n\t"
      "v_mfma_f32_16x16x32_bf16 v[192:195], v[148:151], v[88:91], v[192:195]\n\t"
      "v_mfma_f32_16x16x32_bf16 v[196:199], v[148:151], v[92:95], v[196:199]\n\t"
      "global_load_dwordx4 v[148:151], v229, s[54:55] sc1\n\t"
      "s_waitcnt vmcnt(6)\n\t"
      "v_max3_u32 v234, v152, v153, v154\n\t"
      "v_max_u32_e32 v234, v234, v155\n\t"
      "v_cmp_eq_u32_e32 vcc, -1, v234\n\t"
      "s_cbranch_vccnz FQ6R_%=\n\t"
      "FQ6B_%=:\n\t"
      "v_mfma_f32_16x16x32_bf16 v[184:187], v[152:155], v[96:99], v[184:187]\n\t"
      "v_mfma_f32_16x16x32_bf16 v[188:191], v[152:155], v[100:103], v[188:191]\n\t"
      "v_mfma_f32_16x16x32_bf16 v[192:195], v[152:155], v[104:107], v[192:195]\n\t"
      "v_mfma_f32_16x16x32_bf16 v[196:199], v[152:155], v[108:111], v[196:199]\n\t"
      "global_load_dwordx4 v[152:155], v230, s[54:55] sc1\n\t"
      "s_waitcnt vmcnt(6)\n\t"
      "v_cmp_eq_u32_e32 vcc, -1, v128\n\t"
      "s_cbranch_vccnz FQ7R_%=\n\t"
      "FQ7B_%=:\n\t"
      "v_mfma_f32_16x16x32_bf16 v[200:203], v[128:131], v[0:3], v[160:163]\n\t"
      "v_mfma_f32_16x16x32_bf16 v[204:207], v[128:131], v[4:7], 0\n\t"
      "v_mfma_f32_16x16x32_bf16 v[208:211], v[128:131], v[8:11], 0\n\t"
      "v_mfma_f32_16x16x32_bf16 v[212:215], v[128:131], v[12:15], 0\n\t"
      "global_load_dwordx4 v[128:131], v224, s[56:57] sc1\n\t"
      "s_waitcnt vmcnt(6)\n\t"
      "v_cmp_eq_u32_e32 vcc, -1, v132\n\t"
      "s_cbranch_vccnz FQ8R_%=\n\t"
      "FQ8B_%=:\n\t"
      "v_mfma_f32_16x16x32_bf16 v[200:203], v[132:135], v[16:19], v[200:203]\n\t"
      "ds_write_b128 v243, v[184:187]\n\t"
      "v_mfma_f32_16x16x32_bf16 v[204:207], v[132:135], v[20:23], v[204:207]\n\t"
      "v_mfma_f32_16x16x32_bf16 v[208:211], v[132:135], v[24:27], v[208:211]\n\t"
      "v_mfma_f32_16x16x32_bf16 v[212:215], v[132:135], v[28:31], v[212:215]\n\t"
      "global_load_dwordx4 v[132:135], v225, s[56:57] sc1\n\t"
      "ds_write_b128 v220, v[188:191]\n\t"
      "s_waitcnt vmcnt(6)\n\t"
      "v_cmp_eq_u32_e32 vcc, -1, v136\n\t"
      "s_cbranch_vccnz FQ9R_%=\n\t"
      "FQ9B_%=:\n\t"
      "v_mfma_f32_16x16x32_bf16 v[200:203], v[136:139], v[32:35], v[200:203]\n\t"
      "v_mfma_f32_16x16x32_bf16 v[204:207], v[136:139], v[36:39], v[204:207]\n\t"
      "v_mfma_f32_16x16x32_bf16 v[208:211], v[136:139], v[40:43], v[208:211]\n\t"
      "ds_write_b128 v221, v[192:195]\n\t"
      "v_mfma_f32_16x16x32_bf16 v[212:215], v[136:139], v[44:47], v[212:215]\n\t"
      "global_load_dwordx4 v[136:139], v226, s[56:57] sc1\n\t"
      "s_waitcnt vmcnt(6)\n\t"
      "v_cmp_eq_u32_e32 vcc, -1, v140\n\t"
      "s_cbranch_vccnz FQ10R_%=\n\t"
      "FQ10B_%=:\n\t"
      "v_mfma_f32_16x16x32_bf16 v[200:203], v[140:143], v[48:51], v[200:203]\n\t"
      "v_mfma_f32_16x16x32_bf16 v[204:207], v[140:143], v[52:55], v[204:207]\n\t"
      "ds_write_b128 v222, v[196:199]\n\t"
      "v_mfma_f32_16x16x32_bf16 v[208:211], v[140:143], v[56:59], v[208:211]\n\t"
      "v_mfma_f32_16x16x32_bf16 v[212:215], v[140:143], v[60:63], v[212:215]\n\t"
      "global_load_dwordx4 v[140:143], v227, s[56:57] sc1\n\t"
      "s_waitcnt vmcnt(6)\n\t"
      "v_cmp_eq_u32_e32 vcc, -1, v144\n\t"
      "s_cbranch_vccnz FQ11R_%=\n\t"
      "FQ11B_%=:\n\t"
      "v_mfma_f32_16x16x32_bf16 v[200:203], v[144:147], v[64:67], v[200:203]\n\t"
      "s_waitcnt lgkmcnt(0)\n\t"
      "s_barrier\n\t"
      "v_mfma_f32_16x16x32_bf16 v[204:207], v[144:147], v[68:71], v[204:207]\n\t"
      "v_mfma_f32_16x16x32_bf16 v[208:211], v[144:147], v[72:75], v[208:211]\n\t"
      "v_mfma_f32_16x16x32_bf16 v[212:215], v[144:147], v[76:79], v[212:215]\n\t"
      "global_load_dwordx4 v[144:147], v228, s[56:57] sc1\n\t"
      "s_waitcnt vmcnt(6)\n\t"
      "v_cmp_eq_u32_e32 vcc, -1, v148\n\t"
      "s_cbranch_vccnz FQ12R_%=\n\t"
      "FQ12B_%=:\n\t"
      "v_mfma_f32_16x16x32_bf16 v[200:203], v[148:151], v[80:83], v[200:203]\n\t"
      "v_mfma_f32_16x16x32_bf16 v[204:207], v[148:151], v[84:87], v[204:207]\n\t"
      "v_mfma_f32_16x16x32_bf16 v[208:211], v[148:151], v[88:91], v[208:211]\n\t"
      "v_mfma_f32_16x16x32_bf16 v[212:215], v[148:151], v[92:95], v[212:215]\n\t"
      "global_load_dwordx4 v[148:151], v229, s[56:57] sc1\n\t"
      "s_waitcnt vmcnt(6)\n\t"
      "v_cmp_eq_u32_e32 vcc, -1, v152\n\t"
      "s_cbranch_vccnz FQ13R_%=\n\t"
      "FQ13B_%=:\n\t"
      "v_mfma_f32_16x16x32_bf16 v[200:203], v[152:155], v[96:99], v[200:203]\n\t"
      "v_mfma_f32_16x16x32_bf16 v[204:207], v[152:155], v[100:103], v[204:207]\n\t"
      "v_mfma_f32_16x16x32_bf16 v[208:211], v[152:155], v[104:107], v[208:211]\n\t"
      "v_mfma_f32_16x16x32_bf16 v[212:215], v[152:155], v[108:111], v[212:215]\n\t"
      "global_load_dwordx4 v[152:155], v230, s[56:57] sc1\n\t"
      "s_waitcnt vmcnt(6)\n\t"
      "v_cmp_eq_u32_e32 vcc, -1, v128\n\t"
      "s_cbranch_vccnz FQ14R_%=\n\t"
      "FQ14B_%=:\n\t"
      "v_mfma_f32_16x16x32_bf16 v[184:187], v[128:131], v[0:3], v[164:167]\n\t"
      "v_mfma_f32_16x16x32_bf16 v[188:191], v[128:131], v[4:7], 0\n\t"
      "v_mfma_f32_16x16x32_bf16 v[192:195], v[128:131], v[8:11], 0\n\t"
      "v_mfma_f32_16x16x32_bf16 v[196:199], v[128:131], v[12:15], 0\n\t"
      "global_load_dwordx4 v[128:131], v224, s[58:59] sc1\n\t"
      "v_cmp_u_f32_e32 vcc, v200, v201\n\t"
      "v_cmp_u_f32_e64 s[48:49], v202, v203\n\t"
      "s_nop 0\n\t"
      "s_or_b64 vcc, vcc, s[48:49]\n\t"
      "s_cbranch_scc1 NQ1R_%=\n\t"
      "NQ1B_%=:\n\t"
      "s_waitcnt vmcnt(6)\n\t"
      "v_cmp_eq_u32_e32 vcc, -1, v132\n\t"
      "s_cbranch_vccnz FQ15R_%=\n\t"
      "FQ15B_%=:\n\t"
      "v_mfma_f32_16x16x32_bf16 v[184:187], v[132:135], v[16:19], v[184:187]\n\t"
      "ds_write_b128 v243, v[200:203] offset:32768\n\t"
      "v_mfma_f32_16x16x32_bf16 v[188:191], v[132:135], v[20:23], v[188:191]\n\t"
      "v_mfma_f32_16x16x32_bf16 v[192:195], v[132:135], v[24:27], v[192:195]\n\t"
      "v_mfma_f32_16x16x32_bf16 v[196:199], v[132:135], v[28:31], v[196:199]\n\t"
      "global_load_dwordx4 v[132:135], v225, s[58:59] sc1\n\t"
      "ds_write_b128 v220, v[204:207] offset:32768\n\t"
      "s_waitcnt vmcnt(6)\n\t"
      "v_cmp_eq_u32_e32 vcc, -1, v136\n\t"
      "s_cbranch_vccnz FQ16R_%=\n\t"
      "FQ16B_%=:\n\t"
      "v_mfma_f32_16x16x32_bf16 v[184:187], v[136:139], v[32:35], v[184:187]\n\t"
      "v_mfma_f32_16x16x32_bf16 v[188:191], v[136:139], v[36:39], v[188:191]\n\t"
      "v_mfma_f32_16x16x32_bf16 v[192:195], v[136:139], v[40:43], v[192:195]\n\t"
      "ds_write_b128 v221, v[208:211] offset:32768\n\t"
      "v_mfma_f32_16x16x32_bf16 v[196:199], v[136:139], v[44:47], v[196:199]\n\t"
      "global_load_dwordx4 v[136:139], v226, s[58:59] sc1\n\t"
      "s_waitcnt vmcnt(6)\n\t"
      "v_cmp_eq_u32_e32 vcc, -1, v140\n\t"
      "s_cbranch_vccnz FQ17R_%=\n\t"
      "FQ17B_%=:\n\t"
      "v_mfma_f32_16x16x32_bf16 v[184:187], v[140:143], v[48:51], v[184:187]\n\t"
      "v_mfma_f32_16x16x32_bf16 v[188:191], v[140:143], v[52:55], v[188:191]\n\t"
      "ds_write_b128 v222, v[212:215] offset:32768\n\t"
      "v_mfma_f32_16x16x32_bf16 v[192:195], v[140:143], v[56:59], v[192:195]\n\t"
      "v_mfma_f32_16x16x32_bf16 v[196:199], v[140:143], v[60:63], v[196:199]\n\t"
      "global_load_dwordx4 v[140:143], v227, s[58:59] sc1\n\t"
      "s_waitcnt vmcnt(6)\n\t"
      "v_cmp_eq_u32_e32 vcc, -1, v144\n\t"
      "s_cbranch_vccnz FQ18R_%=\n\t"
      "FQ18B_%=:\n\t"
      "v_mfma_f32_16x16x32_bf16 v[184:187], v[144:147], v[64:67], v[184:187]\n\t"
      "s_waitcnt lgkmcnt(0)\n\t"
      "s_barrier\n\t"
      "v_mfma_f32_16x16x32_bf16 v[188:191], v[144:147], v[68:71], v[188:191]\n\t"
      "v_mfma_f32_16x16x32_bf16 v[192:195], v[144:147], v[72:75], v[192:195]\n\t"
      "v_mfma_f32_16x16x32_bf16 v[196:199], v[144:147], v[76:79], v[196:199]\n\t"
      "global_load_dwordx4 v[144:147], v228, s[58:59] sc1\n\t"
      "s_waitcnt vmcnt(6)\n\t"
      "v_cmp_eq_u32_e32 vcc, -1, v148\n\t"
      "s_cbranch_vccnz FQ19R_%=\n\t"
      "FQ19B_%=:\n\t"
      "v_mfma_f32_16x16x32_bf16 v[184:187], v[148:151], v[80:83], v[184:187]\n\t"
      "v_mfma_f32_16x16x32_bf16 v[188:191], v[148:151], v[84:87], v[188:191]\n\t"
      "v_mfma_f32_16x16x32_bf16 v[192:195], v[148:151], v[88:91], v[192:195]\n\t"
      "v_mfma_f32_16x16x32_bf16 v[196:199], v[148:151], v[92:95], v[196:199]\n\t"
      "global_load_dwordx4 v[148:151], v229, s[58:59] sc1\n\t"
      "s_waitcnt vmcnt(6)\n\t"
      "v_cmp_eq_u32_e32 vcc, -1, v152\n\t"
      "s_cbranch_vccnz FQ20R_%=\n\t"
      "FQ20B_%=:\n\t"
      "v_mfma_f32_16x16x32_bf16 v[184:187], v[152:155], v[96:99], v[184:187]\n\t"
      "v_mfma_f32_16x16x32_bf16 v[188:191], v[152:155], v[100:103], v[188:191]\n\t"
      "v_mfma_f32_16x16x32_bf16 v[192:195], v[152:155], v[104:107], v[192:195]\n\t"
      "v_mfma_f32_16x16x32_bf16 v[196:199], v[152:155], v[108:111], v[196:199]\n\t"
      "global_load_dwordx4 v[152:155], v230, s[58:59] sc1\n\t"
      "s_waitcnt vmcnt(6)\n\t"
      "v_cmp_eq_u32_e32 vcc, -1, v128\n\t"
      "s_cbranch_vccnz FQ21R_%=\n\t"
      "FQ21B_%=:\n\t"
      "v_mfma_f32_16x16x32_bf16 v[200:203], v[128:131], v[0:3], v[168:171]\n\t"
      "v_mfma_f32_16x16x32_bf16 v[204:207], v[128:131], v[4:7], 0\n\t"
      "v_mfma_f32_16x16x32_bf16 v[208:211], v[128:131], v[8:11], 0\n\t"
      "v_mfma_f32_16x16x32_bf16 v[212:215], v[128:131], v[12:15], 0\n\t"
      "global_load_dwordx4 v[128:131], v224, s[60:61] sc1\n\t"
      "v_cmp_u_f32_e32 vcc, v184, v185\n\t"
      "v_cmp_u_f32_e64 s[48:49], v186, v187\n\t"
      "s_nop 0\n\t"
      "s_or_b64 vcc, vcc, s[48:49]\n\t"
      "s_cbranch_scc1 NQ2R_%=\n\t"
      "NQ2B_%=:\n\t"
      "s_waitcnt vmcnt(6)\n\t"
      "v_cmp_eq_u32_e32 vcc, -1, v132\n\t"
      "s_cbranch_vccnz FQ22R_%=\n\t"
      "FQ22B_%=:\n\t"
      "v_mfma_f32_16x16x32_bf16 v[200:203], v[132:135], v[16:19], v[200:203]\n\t"
      "ds_write_b128 v243, v[184:187]\n\t"
      "v_mfma_f32_16x16x32_bf16 v[204:207], v[132:135], v[20:23], v[204:207]\n\t"
      "v_mfma_f32_16x16x32_bf16 v[208:211], v[132:135], v[24:27], v[208:211]\n\t"
      "v_mfma_f32_16x16x32_bf16 v[212:215], v[132:135], v[28:31], v[212:215]\n\t"
      "global_load_dwordx4 v[132:135], v225, s[60:61] sc1\n\t"
      "ds_write_b128 v220, v[188:191]\n\t"
      "s_waitcnt vmcnt(6)\n\t"
      "v_cmp_eq_u32_e32 vcc, -1, v136\n\t"
      "s_cbranch_vccnz FQ23R_%=\n\t"
      "FQ23B_%=:\n\t"
      "v_mfma_f32_16x16x32_bf16 v[200:203], v[136:139], v[32:35], v[200:203]\n\t"
      "v_mfma_f32_16x16x32_bf16 v[204:207], v[136:139], v[36:39], v[204:207]\n\t"
      "v_mfma_f32_16x16x32_bf16 v[208:211], v[136:139], v[40:43], v[208:211]\n\t"
      "ds_write_b128 v221, v[192:195]\n\t"
      "v_mfma_f32_16x16x32_bf16 v[212:215], v[136:139], v[44:47], v[212:215]\n\t"
      "global_load_dwordx4 v[136:139], v226, s[60:61] sc1\n\t"
      "s_waitcnt vmcnt(6)\n\t"
      "v_cmp_eq_u32_e32 vcc, -1, v140\n\t"
      "s_cbranch_vccnz FQ24R_%=\n\t"
      "FQ24B_%=:\n\t"
      "v_mfma_f32_16x16x32_bf16 v[200:203], v[140:143], v[48:51], v[200:203]\n\t"
      "v_mfma_f32_16x16x32_bf16 v[204:207], v[140:143], v[52:55], v[204:207]\n\t"
      "ds_write_b128 v222, v[196:199]\n\t"
      "v_mfma_f32_16x16x32_bf16 v[208:211], v[140:143], v[56:59], v[208:211]\n\t"
      "v_mfma_f32_16x16x32_bf16 v[212:215], v[140:143], v[60:63], v[212:215]\n\t"
      "global_load_dwordx4 v[140:143], v227, s[60:61] sc1\n\t"
      "s_waitcnt vmcnt(6)\n\t"
      "v_cmp_eq_u32_e32 vcc, -1, v144\n\t"
      "s_cbranch_vccnz FQ25R_%=\n\t"
      "FQ25B_%=:\n\t"
      "v_mfma_f32_16x16x32_bf16 v[200:203], v[144:147], v[64:67], v[200:203]\n\t"
      "s_waitcnt lgkmcnt(0)\n\t"
      "s_barrier\n\t"
      "v_mfma_f32_16x16x32_bf16 v[204:207], v[144:147], v[68:71], v[204:207]\n\t"
      "v_mfma_f32_16x16x32_bf16 v[208:211], v[144:147], v[72:75], v[208:211]\n\t"
      "v_mfma_f32_16x16x32_bf16 v[212:215], v[144:147], v[76:79], v[212:215]\n\t"
      "global_load_dwordx4 v[144:147], v228, s[60:61] sc1\n\t"
      "s_waitcnt vmcnt(6)\n\t"
      "v_cmp_eq_u32_e32 vcc, -1, v148\n\t"
      "s_cbranch_vccnz FQ26R_%=\n\t"
      "FQ26B_%=:\n\t"
      "v_mfma_f32_16x16x32_bf16 v[200:203], v[148:151], v[80:83], v[200:203]\n\t"
      "v_mfma_f32_16x16x32_bf16 v[204:207], v[148:151], v[84:87], v[204:207]\n\t"
      "v_mfma_f32_16x16x32_bf16 v[208:211], v[148:151], v[88:91], v[208:211]\n\t"
      "v_mfma_f32_16x16x32_bf16 v[212:215], v[148:151], v[92:95], v[212:215]\n\t"
      "global_load_dwordx4 v[148:151], v229, s[60:61] sc1\n\t"
      "s_waitcnt vmcnt(6)\n\t"
      "v_cmp_eq_u32_e32 vcc, -1, v152\n\t"
      "s_cbranch_vccnz FQ27R_%=\n\t"
      "FQ27B_%=:\n\t"
      "v_mfma_f32_16x16x32_bf16 v[200:203], v[152:155], v[96:99], v[200:203]\n\t"
      "v_mfma_f32_16x16x32_bf16 v[204:207], v[152:155], v[100:103], v[204:207]\n\t"
      "v_mfma_f32_16x16x32_bf16 v[208:211], v[152:155], v[104:107], v[208:211]\n\t"
      "v_mfma_f32_16x16x32_bf16 v[212:215], v[152:155], v[108:111], v[212:215]\n\t"
      "global_load_dwordx4 v[152:155], v230, s[60:61] sc1\n\t"
      "s_waitcnt vmcnt(6)\n\t"
      "v_cmp_eq_u32_e32 vcc, -1, v128\n\t"
      "s_cbranch_vccnz FQ28R_%=\n\t"
      "FQ28B_%=:\n\t"
      "v_mfma_f32_16x16x32_bf16 v[184:187], v[128:131], v[0:3], v[172:175]\n\t"
      "v_mfma_f32_16x16x32_bf16 v[188:191], v[128:131], v[4:7], 0\n\t"
      "v_mfma_f32_16x16x32_bf16 v[192:195], v[128:131], v[8:11], 0\n\t"
      "v_mfma_f32_16x16x32_bf16 v[196:199], v[128:131], v[12:15], 0\n\t"
      "global_load_dwordx4 v[128:131], v224, s[62:63] sc1\n\t"
      "v_cmp_u_f32_e32 vcc, v200, v201\n\t"
      "v_cmp_u_f32_e64 s[48:49], v202, v203\n\t"
      "s_nop 0\n\t"
      "s_or_b64 vcc, vcc, s[48:49]\n\t"
      "s_cbranch_scc1 NQ3R_%=\n\t"
      "NQ3B_%=:\n\t"
      "s_waitcnt vmcnt(6)\n\t"
      "v_cmp_eq_u32_e32 vcc, -1, v132\n\t"
      "s_cbranch_vccnz FQ29R_%=\n\t"
      "FQ29B_%=:\n\t"
      "v_mfma_f32_16x16x32_bf16 v[184:187], v[132:135], v[16:19], v[184:187]\n\t"
      "ds_write_b128 v243, v[200:203] offset:32768\n\t"
      "v_mfma_f32_16x16x32_bf16 v[188:191], v[132:135], v[20:23], v[188:191]\n\t"
      "v_mfma_f32_16x16x32_bf16 v[192:195], v[132:135], v[24:27], v[192:195]\n\t"
      "v_mfma_f32_16x16x32_bf16 v[196:199], v[132:135], v[28:31], v[196:199]\n\t"
      "global_load_dwordx4 v[132:135], v225, s[62:63] sc1\n\t"
      "ds_write_b128 v220, v[204:207] offset:32768\n\t"
      "s_waitcnt vmcnt(6)\n\t"
      "v_cmp_eq_u32_e32 vcc, -1, v136\n\t"
      "s_cbranch_vccnz FQ30R_%=\n\t"
      "FQ30B_%=:\n\t"
      "v_mfma_f32_16x16x32_bf16 v[184:187], v[136:139], v[32:35], v[184:187]\n\t"
      "v_mfma_f32_16x16x32_bf16 v[188:191], v[136:139], v[36:39], v[188:191]\n\t"
      "v_mfma_f32_16x16x32_bf16 v[192:195], v[136:139], v[40:43], v[192:195]\n\t"
      "ds_write_b128 v221, v[208:211] offset:32768\n\t"
      "v_mfma_f32_16x16x32_bf16 v[196:199], v[136:139], v[44:47], v[196:199]\n\t"
      "global_load_dwordx4 v[136:139], v226, s[62:63] sc1\n\t"
      "s_waitcnt vmcnt(6)\n\t"
      "v_cmp_eq_u32_e32 vcc, -1, v140\n\t"
      "s_cbranch_vccnz FQ31R_%=\n\t"
      "FQ31B_%=:\n\t"
      "v_mfma_f32_16x16x32_bf16 v[184:187], v[140:143], v[48:51], v[184:187]\n\t"
      "v_mfma_f32_16x16x32_bf16 v[188:191], v[140:143], v[52:55], v[188:191]\n\t"
      "ds_write_b128 v222, v[212:215] offset:32768\n\t"
      "v_mfma_f32_16x16x32_bf16 v[192:195], v[140:143], v[56:59], v[192:195]\n\t"
      "v_mfma_f32_16x16x32_bf16 v[196:199], v[140:143], v[60:63], v[196:199]\n\t"
      "global_load_dwordx4 v[140:143], v227, s[62:63] sc1\n\t"
      "s_waitcnt vmcnt(6)\n\t"
      "v_cmp_eq_u32_e32 vcc, -1, v144\n\t"
      "s_cbranch_vccnz FQ32R_%=\n\t"
      "FQ32B_%=:\n\t"
      "v_mfma_f32_16x16x32_bf16 v[184:187], v[144:147], v[64:67], v[184:187]\n\t"
      "s_waitcnt lgkmcnt(0)\n\t"
      "s_barrier\n\t"
      "v_mfma_f32_16x16x32_bf16 v[188:191], v[144:147], v[68:71], v[188:191]\n\t"
      "v_mfma_f32_16x16x32_bf16 v[192:195], v[144:147], v[72:75], v[192:195]\n\t"
      "v_mfma_f32_16x16x32_bf16 v[196:199], v[144:147], v[76:79], v[196:199]\n\t"
      "global_load_dwordx4 v[144:147], v228, s[62:63] sc1\n\t"
      "s_waitcnt vmcnt(6)\n\t"
      "v_cmp_eq_u32_e32 vcc, -1, v148\n\t"
      "s_cbranch_vccnz FQ33R_%=\n\t"
      "FQ33B_%=:\n\t"
      "v_mfma_f32_16x16x32_bf16 v[184:187], v[148:151], v[80:83], v[184:187]\n\t"
      "v_mfma_f32_16x16x32_bf16 v[188:191], v[148:151], v[84:87], v[188:191]\n\t"
      "v_mfma_f32_16x16x32_bf16 v[192:195], v[148:151], v[88:91], v[192:195]\n\t"
      "v_mfma_f32_16x16x32_bf16 v[196:199], v[148:151], v[92:95], v[196:199]\n\t"
      "global_load_dwordx4 v[148:151], v229, s[62:63] sc1\n\t"
      "s_waitcnt vmcnt(6)\n\t"
      "v_cmp_eq_u32_e32 vcc, -1, v152\n\t"
      "s_cbranch_vccnz FQ34R_%=\n\t"
      "FQ34B_%=:\n\t"
      "v_mfma_f32_16x16x32_bf16 v[184:187], v[152:155], v[96:99], v[184:187]\n\t"
      "v_mfma_f32_16x16x32_bf16 v[188:191], v[152:155], v[100:103], v[188:191]\n\t"
      "v_mfma_f32_16x16x32_bf16 v[192:195], v[152:155], v[104:107], v[192:195]\n\t"
      "v_mfma_f32_16x16x32_bf16 v[196:199], v[152:155], v[108:111], v[196:199]\n\t"
      "global_load_dwordx4 v[152:155], v230, s[62:63] sc1\n\t"
      "s_waitcnt vmcnt(6)\n\t"
      "v_cmp_eq_u32_e32 vcc, -1, v128\n\t"
      "s_cbranch_vccnz FQ35R_%=\n\t"
      "FQ35B_%=:\n\t"
      "v_mfma_f32_16x16x32_bf16 v[200:203], v[128:131], v[0:3], v[176:179]\n\t"
      "v_mfma_f32_16x16x32_bf16 v[204:207], v[128:131], v[4:7], 0\n\t"
      "v_mfma_f32_16x16x32_bf16 v[208:211], v[128:131], v[8:11], 0\n\t"
      "v_mfma_f32_16x16x32_bf16 v[212:215], v[128:131], v[12:15], 0\n\t"
      "global_load_dwordx4 v[128:131], v224, s[64:65] sc1\n\t"
      "v_cmp_u_f32_e32 vcc, v184, v185\n\t"
      "v_cmp_u_f32_e64 s[48:49], v186, v187\n\t"
      "s_nop 0\n\t"
      "s_or_b64 vcc, vcc, s[48:49]\n\t"
      "s_cbranch_scc1 NQ4R_%=\n\t"
      "NQ4B_%=:\n\t"
      "s_waitcnt vmcnt(6)\n\t"
      "v_cmp_eq_u32_e32 vcc, -1, v132\n\t"
      "s_cbranch_vccnz FQ36R_%=\n\t"
      "FQ36B_%=:\n\t"
      "v_mfma_f32_16x16x32_bf16 v[200:203], v[132:135], v[16:19], v[200:203]\n\t"
      "ds_write_b128 v243, v[184:187]\n\t"
      "v_mfma_f32_16x16x32_bf16 v[204:207], v[132:135], v[20:23], v[204:207]\n\t"
      "v_mfma_f32_16x16x32_bf16 v[208:211], v[132:135], v[24:27], v[208:211]\n\t"
      "v_mfma_f32_16x16x32_bf16 v[212:215], v[132:135], v[28:31], v[212:215]\n\t"
      "global_load_dwordx4 v[132:135], v225, s[64:65] sc1\n\t"
      "ds_write_b128 v220, v[188:191]\n\t"
      "s_waitcnt vmcnt(6)\n\t"
      "v_cmp_eq_u32_e32 vcc, -1, v136\n\t"
      "s_cbranch_vccnz FQ37R_%=\n\t"
      "FQ37B_%=:\n\t"
      "v_mfma_f32_16x16x32_bf16 v[200:203], v[136:139], v[32:35], v[200:203]\n\t"
      "v_mfma_f32_16x16x32_bf16 v[204:207], v[136:139], v[36:39], v[204:207]\n\t"
      "v_mfma_f32_16x16x32_bf16 v[208:211], v[136:139], v[40:43], v[208:211]\n\t"
      "ds_write_b128 v221, v[192:195]\n\t"
      "v_mfma_f32_16x16x32_bf16 v[212:215], v[136:139], v[44:47], v[212:215]\n\t"
      "global_load_dwordx4 v[136:139], v226, s[64:65] sc1\n\t"
      "s_waitcnt vmcnt(6)\n\t"
      "v_cmp_eq_u32_e32 vcc, -1, v140\n\t"
      "s_cbranch_vccnz FQ38R_%=\n\t"
      "FQ38B_%=:\n\t"
      "v_mfma_f32_16x16x32_bf16 v[200:203], v[140:143], v[48:51], v[200:203]\n\t"
      "v_mfma_f32_16x16x32_bf16 v[204:207], v[140:143], v[52:55], v[204:207]\n\t"
      "ds_write_b128 v222, v[196:199]\n\t"
      "v_mfma_f32_16x16x32_bf16 v[208:211], v[140:143], v[56:59], v[208:211]\n\t"
      "v_mfma_f32_16x16x32_bf16 v[212:215], v[140:143], v[60:63], v[212:215]\n\t"
      "global_load_dwordx4 v[140:143], v227, s[64:65] sc1\n\t"
      "s_waitcnt vmcnt(6)\n\t"
      "v_cmp_eq_u32_e32 vcc, -1, v144\n\t"
      "s_cbranch_vccnz FQ39R_%=\n\t"
      "FQ39B_%=:\n\t"
      "v_mfma_f32_16x16x32_bf16 v[200:203], v[144:147], v[64:67], v[200:203]\n\t"
      "s_waitcnt lgkmcnt(0)\n\t"
      "s_barrier\n\t"
      "v_mfma_f32_16x16x32_bf16 v[204:207], v[144:147], v[68:71], v[204:207]\n\t"
      "v_mfma_f32_16x16x32_bf16 v[208:211], v[144:147], v[72:75], v[208:211]\n\t"
      "v_mfma_f32_16x16x32_bf16 v[212:215], v[144:147], v[76:79], v[212:215]\n\t"
      "global_load_dwordx4 v[144:147], v228, s[64:65] sc1\n\t"
      "s_waitcnt vmcnt(6)\n\t"
      "v_cmp_eq_u32_e32 vcc, -1, v148\n\t"
      "s_cbranch_vccnz FQ40R_%=\n\t"
      "FQ40B_%=:\n\t"
      "v_mfma_f32_16x16x32_bf16 v[200:203], v[148:151], v[80:83], v[200:203]\n\t"
      "v_mfma_f32_16x16x32_bf16 v[204:207], v[148:151], v[84:87], v[204:207]\n\t"
      "v_mfma_f32_16x16x32_bf16 v[208:211], v[148:151], v[88:91], v[208:211]\n\t"
      "v_mfma_f32_16x16x32_bf16 v[212:215], v[148:151], v[92:95], v[212:215]\n\t"
      "global_load_dwordx4 v[148:151], v229, s[64:65] sc1\n\t"
      "s_waitcnt vmcnt(6)\n\t"
      "v_cmp_eq_u32_e32 vcc, -1, v152\n\t"
      "s_cbranch_vccnz FQ41R_%=\n\t"
      "FQ41B_%=:\n\t"
      "v_mfma_f32_16x16x32_bf16 v[200:203], v[152:155], v[96:99], v[200:203]\n\t"
      "v_mfma_f32_16x16x32_bf16 v[204:207], v[152:155], v[100:103], v[204:207]\n\t"
      "v_mfma_f32_16x16x32_bf16 v[208:211], v[152:155], v[104:107], v[208:211]\n\t"
      "v_mfma_f32_16x16x32_bf16 v[212:215], v[152:155], v[108:111], v[212:215]\n\t"
      "global_load_dwordx4 v[152:155], v230, s[64:65] sc1\n\t"
      "s_waitcnt vmcnt(6)\n\t"
      "v_cmp_eq_u32_e32 vcc, -1, v128\n\t"
      "s_cbranch_vccnz FQ42R_%=\n\t"
      "FQ42B_%=:\n\t"
      "v_mfma_f32_16x16x32_bf16 v[184:187], v[128:131], v[0:3], v[180:183]\n\t"
      "v_mfma_f32_16x16x32_bf16 v[188:191], v[128:131], v[4:7], 0\n\t"
      "v_mfma_f32_16x16x32_bf16 v[192:195], v[128:131], v[8:11], 0\n\t"
      "v_mfma_f32_16x16x32_bf16 v[196:199], v[128:131], v[12:15], 0\n\t"
      "v_cmp_u_f32_e32 vcc, v200, v201\n\t"
      "v_cmp_u_f32_e64 s[48:49], v202, v203\n\t"
      "s_nop 0\n\t"
      "s_or_b64 vcc, vcc, s[48:49]\n\t"
      "s_cbranch_scc1 NQ5R_%=\n\t"
      "NQ5B_%=:\n\t"
      "s_waitcnt vmcnt(5)\n\t"
      "v_cmp_eq_u32_e32 vcc, -1, v132\n\t"
      "s_cbranch_vccnz FQ43R_%=\n\t"
      "FQ43B_%=:\n\t"
      "v_mfma_f32_16x16x32_bf16 v[184:187], v[132:135], v[16:19], v[184:187]\n\t"
      "ds_write_b128 v243, v[200:203] offset:32768\n\t"
      "v_mfma_f32_16x16x32_bf16 v[188:191], v[132:135], v[20:23], v[188:191]\n\t"
      "v_mfma_f32_16x16x32_bf16 v[192:195], v[132:135], v[24:27], v[192:195]\n\t"
      "v_mfma_f32_16x16x32_bf16 v[196:199], v[132:135], v[28:31], v[196:199]\n\t"
      "ds_write_b128 v220, v[204:207] offset:32768\n\t"
      "s_waitcnt vmcnt(4)\n\t"
      "v_cmp_eq_u32_e32 vcc, -1, v136\n\t"
      "s_cbranch_vccnz FQ44R_%=\n\t"
      "FQ44B_%=:\n\t"
      "v_mfma_f32_16x16x32_bf16 v[184:187], v[136:139], v[32:35], v[184:187]\n\t"
      "v_mfma_f32_16x16x32_bf16 v[188:191], v[136:139], v[36:39], v[188:191]\n\t"
      "v_mfma_f32_16x16x32_bf16 v[192:195], v[136:139], v[40:43], v[192:195]\n\t"
      "ds_write_b128 v221, v[208:211] offset:32768\n\t"
      "v_mfma_f32_16x16x32_bf16 v[196:199], v[136:139], v[44:47], v[196:199]\n\t"
      "s_waitcnt vmcnt(3)\n\t"
      "v_cmp_eq_u32_e32 vcc, -1, v140\n\t"
      "s_cbranch_vccnz FQ45R_%=\n\t"
      "FQ45B_%=:\n\t"
      "v_mfma_f32_16x16x32_bf16 v[184:187], v[140:143], v[48:51], v[184:187]\n\t"
      "v_mfma_f32_16x16x32_bf16 v[188:191], v[140:143], v[52:55], v[188:191]\n\t"
      "ds_write_b128 v222, v[212:215] offset:32768\n\t"
      "v_mfma_f32_16x16x32_bf16 v[192:195], v[140:143], v[56:59], v[192:195]\n\t"
      "v_mfma_f32_16x16x32_bf16 v[196:199], v[140:143], v[60:63], v[196:199]\n\t"
      "s_waitcnt vmcnt(2)\n\t"
      "v_cmp_eq_u32_e32 vcc, -1, v144\n\t"
      "s_cbranch_vccnz FQ46R_%=\n\t"
      "FQ46B_%=:\n\t"
      "v_mfma_f32_16x16x32_bf16 v[184:187], v[144:147], v[64:67], v[184:187]\n\t"
      "s_waitcnt lgkmcnt(0)\n\t"
      "s_barrier\n\t"
      "v_mfma_f32_16x16x32_bf16 v[188:191], v[144:147], v[68:71], v[188:191]\n\t"
      "v_mfma_f32_16x16x32_bf16 v[192:195], v[144:147], v[72:75], v[192:195]\n\t"
      "v_mfma_f32_16x16x32_bf16 v[196:199], v[144:147], v[76:79], v[196:199]\n\t"
      "s_waitcnt vmcnt(1)\n\t"
      "v_cmp_eq_u32_e32 vcc, -1, v148\n\t"
      "s_cbranch_vccnz FQ47R_%=\n\t"
      "FQ47B_%=:\n\t"
      "v_mfma_f32_16x16x32_bf16 v[184:187], v[148:151], v[80:83], v[184:187]\n\t"
      "v_mfma_f32_16x16x32_bf16 v[188:191], v[148:151], v[84:87], v[188:191]\n\t"
      "v_mfma_f32_16x16x32_bf16 v[192:195], v[148:151], v[88:91], v[192:195]\n\t"
      "v_mfma_f32_16x16x32_bf16 v[196:199], v[148:151], v[92:95], v[196:199]\n\t"
      "s_waitcnt vmcnt(0)\n\t"
      "v_cmp_eq_u32_e32 vcc, -1, v152\n\t"
      "s_cbranch_vccnz FQ48R_%=\n\t"
      "FQ48B_%=:\n\t"
      "v_mfma_f32_16x16x32_bf16 v[184:187], v[152:155], v[96:99], v[184:187]\n\t"
      "v_mfma_f32_16x16x32_bf16 v[188:191], v[152:155], v[100:103], v[188:191]\n\t"
      "v_mfma_f32_16x16x32_bf16 v[192:195], v[152:155], v[104:107], v[192:195]\n\t"
      "v_mfma_f32_16x16x32_bf16 v[196:199], v[152:155], v[108:111], v[196:199]\n\t"
      "s_nop 4\n\t"
      "v_cmp_u_f32_e32 vcc, v184, v185\n\t"
      "v_cmp_u_f32_e64 s[48:49], v186, v187\n\t"
      "s_nop 0\n\t"
      "s_or_b64 vcc, vcc, s[48:49]\n\t"
      "s_cbranch_scc1 NQ6R_%=\n\t"
      "NQ6B_%=:\n\t"
      "ds_write_b128 v243, v[184:187]\n\t"
      "ds_write_b128 v220, v[188:191]\n\t"
      "ds_write_b128 v221, v[192:195]\n\t"
      "ds_write_b128 v222, v[196:199]\n\t"
      "s_waitcnt lgkmcnt(0)\n\t"
      "s_barrier\n\t"
      "s_branch GX_%=\n\t"
      "HWR_%=:\n\t"
      "s_mov_b32 s51, 0\n\t"
      "HWL_%=:\n\t"
      "s_add_u32 s51, s51, 1\n\t"
      "s_cmp_lt_u32 s51, 262144\n\t"
      "s_cbranch_scc0 HWX_%=\n\t"
      "s_sleep 2\n\t"
      "global_load_dwordx2 v[112:113], v114, s[12:13] sc1\n\t"
      "s_waitcnt vmcnt(0)\n\t"
      "v_cmp_ne_u64_e32 vcc, s[14:15], v[112:113]\n\t"
      "s_cbranch_vccnz HWL_%=\n\t"
      "HWX_%=:\n\t"
      "s_branch HWB_%=\n\t"
      "FQ0R_%=:\n\t"
      "s_mov_b32 s51, 0\n\t"
      "FQ0L_%=:\n\t"
      "s_add_u32 s51, s51, 1\n\t"
      "s_cmp_lt_u32 s51, 262144\n\t"
      "s_cbranch_scc0 FQ0X_%=\n\t"
      "s_waitcnt vmcnt(0)\n\t"
      "s_sleep 1\n\t"
      "global_load_dwordx4 v[128:131], v224, s[52:53] sc1\n\t"
      "global_load_dwordx4 v[132:135], v225, s[52:53] sc1\n\t"
      "global_load_dwordx4 v[136:139], v226, s[52:53] sc1\n\t"
      "global_load_dwordx4 v[140:143], v227, s[52:53] sc1\n\t"
      "global_load_dwordx4 v[144:147], v228, s[52:53] sc1\n\t"
      "global_load_dwordx4 v[148:151], v229, s[52:53] sc1\n\t"
      "global_load_dwordx4 v[152:155], v230, s[52:53] sc1\n\t"
      "s_waitcnt vmcnt(6)\n\t"
      "v_max3_u32 v234, v128, v129, v130\n\t"
      "v_max_u32_e32 v234, v234, v131\n\t"
      "v_cmp_eq_u32_e32 vcc, -1, v234\n\t"
      "s_cbranch_vccnz FQ0L_%=\n\t"
      "FQ0X_%=:\n\t"
      "s_branch FQ0B_%=\n\t"
      "FQ1R_%=:\n\t"
      "s_mov_b32 s51, 0\n\t"
      "FQ1L_%=:\n\t"
      "s_add_u32 s51, s51, 1\n\t"
      "s_cmp_lt_u32 s51, 262144\n\t"
      "s_cbranch_scc0 FQ1X_%=\n\t"
      "s_waitcnt vmcnt(0)\n\t"
      "s_sleep 1\n\t"
      "global_load_dwordx4 v[132:135], v225, s[52:53] sc1\n\t"
      "global_load_dwordx4 v[136:139], v226, s[52:53] sc1\n\t"
      "global_load_dwordx4 v[140:143], v227, s[52:53] sc1\n\t"
      "global_load_dwordx4 v[144:147], v228, s[52:53] sc1\n\t"
      "global_load_dwordx4 v[148:151], v229, s[52:53] sc1\n\t"
      "global_load_dwordx4 v[152:155], v230, s[52:53] sc1\n\t"
      "global_load_dwordx4 v[128:131], v224, s[54:55] sc1\n\t"
      "s_waitcnt vmcnt(6)\n\t"
      "v_max3_u32 v234, v132, v133, v134\n\t"
      "v_max_u32_e32 v234, v234, v135\n\t"
      "v_cmp_eq_u32_e32 vcc, -1, v234\n\t"
      "s_cbranch_vccnz FQ1L_%=\n\t"
      "FQ1X_%=:\n\t"
      "s_branch FQ1B_%=\n\t"
      "FQ2R_%=:\n\t"
      "s_mov_b32 s51, 0\n\t"
      "FQ2L_%=:\n\t"
      "s_add_u32 s51, s51, 1\n\t"
      "s_cmp_lt_u32 s51, 262144\n\t"
      "s_cbranch_scc0 FQ2X_%=\n\t"
      "s_waitcnt vmcnt(0)\n\t"
      "s_sleep 1\n\t"
      "global_load_dwordx4 v[136:139], v226, s[52:53] sc1\n\t"
      "global_load_dwordx4 v[140:143], v227, s[52:53] sc1\n\t"
      "global_load_dwordx4 v[144:147], v228, s[52:53] sc1\n\t"
      "global_load_dwordx4 v[148:151], v229, s[52:53] sc1\n\t"
      "global_load_dwordx4 v[152:155], v230, s[52:53] sc1\n\t"
      "global_load_dwordx4 v[128:131], v224, s[54:55] sc1\n\t"
      "global_load_dwordx4 v[132:135], v225, s[54:55] sc1\n\t"
      "s_waitcnt vmcnt(6)\n\t"
      "v_max3_u32 v234, v136, v137, v138\n\t"
      "v_max_u32_e32 v234, v234, v139\n\t"
      "v_cmp_eq_u32_e32 vcc, -1, v234\n\t"
      "s_cbranch_vccnz FQ2L_%=\n\t"
      "FQ2X_%=:\n\t"
      "s_branch FQ2B_%=\n\t"
      "FQ3R_%=:\n\t"
      "s_mov_b32 s51, 0\n\t"
      "FQ3L_%=:\n\t"
      "s_add_u32 s51, s51, 1\n\t"
      "s_cmp_lt_u32 s51, 262144\n\t"
      "s_cbranch_scc0 FQ3X_%=\n\t"
      "s_waitcnt vmcnt(0)\n\t"
      "s_sleep 1\n\t"
      "global_load_dwordx4 v[140:143], v227, s[52:53] sc1\n\t"
      "global_load_dwordx4 v[144:147], v228, s[52:53] sc1\n\t"
      "global_load_dwordx4 v[148:151], v229, s[52:53] sc1\n\t"
      "global_load_dwordx4 v[152:155], v230, s[52:53] sc1\n\t"
      "global_load_dwordx4 v[128:131], v224, s[54:55] sc1\n\t"
      "global_load_dwordx4 v[132:135], v225, s[54:55] sc1\n\t"
      "global_load_dwordx4 v[136:139], v226, s[54:55] sc1\n\t"
      "s_waitcnt vmcnt(6)\n\t"
      "v_max3_u32 v234, v140, v141, v142\n\t"
      "v_max_u32_e32 v234, v234, v143\n\t"
      "v_cmp_eq_u32_e32 vcc, -1, v234\n\t"
      "s_cbranch_vccnz FQ3L_%=\n\t"
      "FQ3X_%=:\n\t"
      "s_branch FQ3B_%=\n\t"
      "FQ4R_%=:\n\t"
      "s_mov_b32 s51, 0\n\t"
      "FQ4L_%=:\n\t"
      "s_add_u32 s51, s51, 1\n\t"
      "s_cmp_lt_u32 s51, 262144\n\t"
      "s_cbranch_scc0 FQ4X_%=\n\t"
      "s_waitcnt vmcnt(0)\n\t"
      "s_sleep 1\n\t"
      "global_load_dwordx4 v[144:147], v228, s[52:53] sc1\n\t"
      "global_load_dwordx4 v[148:151], v229, s[52:53] sc1\n\t"
      "global_load_dwordx4 v[152:155], v230, s[52:53] sc1\n\t"
      "global_load_dwordx4 v[128:131], v224, s[54:55] sc1\n\t"
      "global_load_dwordx4 v[132:135], v225, s[54:55] sc1\n\t"
      "global_load_dwordx4 v[136:139], v226, s[54:55] sc1\n\t"
      "global_load_dwordx4 v[140:143], v227, s[54:55] sc1\n\t"
      "s_waitcnt vmcnt(6)\n\t"
      "v_max3_u32 v234, v144, v145, v146\n\t"
      "v_max_u32_e32 v234, v234, v147\n\t"
      "v_cmp_eq_u32_e32 vcc, -1, v234\n\t"
      "s_cbranch_vccnz FQ4L_%=\n\t"
      "FQ4X_%=:\n\t"
      "s_branch FQ4B_%=\n\t"
      "FQ5R_%=:\n\t"
      "s_mov_b32 s51, 0\n\t"
      "FQ5L_%=:\n\t"
      "s_add_u32 s51, s51, 1\n\t"
      "s_cmp_lt_u32 s51, 262144\n\t"
      "s_cbranch_scc0 FQ5X_%=\n\t"
      "s_waitcnt vmcnt(0)\n\t"
      "s_sleep 1\n\t"
      "global_load_dwordx4 v[148:151], v229, s[52:53] sc1\n\t"
      "global_load_dwordx4 v[152:155], v230, s[52:53] sc1\n\t"
      "global_load_dwordx4 v[128:131], v224, s[54:55] sc1\n\t"
      "global_load_dwordx4 v[132:135], v225, s[54:55] sc1\n\t"
      "global_load_dwordx4 v[136:139], v226, s[54:55] sc1\n\t"
      "global_load_dwordx4 v[140:143], v227, s[54:55] sc1\n\t"
      "global_load_dwordx4 v[144:147], v228, s[54:55] sc1\n\t"
      "s_waitcnt vmcnt(6)\n\t"
      "v_max3_u32 v234, v148, v149, v150\n\t"
      "v_max_u32_e32 v234, v234, v151\n\t"
      "v_cmp_eq_u32_e32 vcc, -1, v234\n\t"
      "s_cbranch_vccnz FQ5L_%=\n\t"
      "FQ5X_%=:\n\t"
      "s_branch FQ5B_%=\n\t"
      "FQ6R_%=:\n\t"
      "s_mov_b32 s51, 0\n\t"
      "FQ6L_%=:\n\t"
      "s_add_u32 s51, s51, 1\n\t"
      "s_cmp_lt_u32 s51, 262144\n\t"
      "s_cbranch_scc0 FQ6X_%=\n\t"
      "s_waitcnt vmcnt(0)\n\t"
      "s_sleep 1\n\t"
      "global_load_dwordx4 v[152:155], v230, s[52:53] sc1\n\t"
      "global_load_dwordx4 v[128:131], v224, s[54:55] sc1\n\t"
      "global_load_dwordx4 v[132:135], v225, s[54:55] sc1\n\t"
      "global_load_dwordx4 v[136:139], v226, s[54:55] sc1\n\t"
      "global_load_dwordx4 v[140:143], v227, s[54:55] sc1\n\t"
      "global_load_dwordx4 v[144:147], v228, s[54:55] sc1\n\t"
      "global_load_dwordx4 v[148:151], v229, s[54:55] sc1\n\t"
      "s_waitcnt vmcnt(6)\n\t"
      "v_max3_u32 v234, v152, v153, v154\n\t"
      "v_max_u32_e32 v234, v234, v155\n\t"
      "v_cmp_eq_u32_e32 vcc, -1, v234\n\t"
      "s_cbranch_vccnz FQ6L_%=\n\t"
      "FQ6X_%=:\n\t"
      "s_branch FQ6B_%=\n\t"
      "FQ7R_%=:\n\t"
      "s_mov_b32 s51, 0\n\t"
      "FQ7L_%=:\n\t"
      "s_add_u32 s51, s51, 1\n\t"
      "s_cmp_lt_u32 s51, 262144\n\t"
      "s_cbranch_scc0 FQ7X_%=\n\t"
      "s_waitcnt vmcnt(0)\n\t"
      "s_sleep 4\n\t"
      "global_load_dwordx4 v[128:131], v224, s[54:55] sc1\n\t"
      "global_load_dwordx4 v[132:135], v225, s[54:55] sc1\n\t"
      "global_load_dwordx4 v[136:139], v226, s[54:55] sc1\n\t"
      "global_load_dwordx4 v[140:143], v227, s[54:55] sc1\n\t"
      "global_load_dwordx4 v[144:147], v228, s[54:55] sc1\n\t"
      "global_load_dwordx4 v[148:151], v229, s[54:55] sc1\n\t"
      "global_load_dwordx4 v[152:155], v230, s[54:55] sc1\n\t"
      "s_waitcnt vmcnt(6)\n\t"
      "v_max3_u32 v234, v128, v129, v130\n\t"
      "v_max_u32_e32 v234, v234, v131\n\t"
      "v_cmp_eq_u32_e32 vcc, -1, v234\n\t"
      "s_cbranch_vccnz FQ7L_%=\n\t"
      "FQ7X_%=:\n\t"
      "s_branch FQ7B_%=\n\t"
      "FQ8R_%=:\n\t"
      "s_mov_b32 s51, 0\n\t"
      "FQ8L_%=:\n\t"
      "s_add_u32 s51, s51, 1\n\t"
      "s_cmp_lt_u32 s51, 262144\n\t"
      "s_cbranch_scc0 FQ8X_%=\n\t"
      "s_waitcnt vmcnt(0)\n\t"
      "s_sleep 4\n\t"
      "global_load_dwordx4 v[132:135], v225, s[54:55] sc1\n\t"
      "global_load_dwordx4 v[136:139], v226, s[54:55] sc1\n\t"
      "global_load_dwordx4 v[140:143], v227, s[54:55] sc1\n\t"
      "global_load_dwordx4 v[144:147], v228, s[54:55] sc1\n\t"
      "global_load_dwordx4 v[148:151], v229, s[54:55] sc1\n\t"
      "global_load_dwordx4 v[152:155], v230, s[54:55] sc1\n\t"
      "global_load_dwordx4 v[128:131], v224, s[56:57] sc1\n\t"
      "s_waitcnt vmcnt(6)\n\t"
      "v_max3_u32 v234, v132, v133, v134\n\t"
      "v_max_u32_e32 v234, v234, v135\n\t"
      "v_cmp_eq_u32_e32 vcc, -1, v234\n\t"
      "s_cbranch_vccnz FQ8L_%=\n\t"
      "FQ8X_%=:\n\t"
      "s_branch FQ8B_%=\n\t"
      "FQ9R_%=:\n\t"
      "s_mov_b32 s51, 0\n\t"
      "FQ9L_%=:\n\t"
      "s_add_u32 s51, s51, 1\n\t"
      "s_cmp_lt_u32 s51, 262144\n\t"
      "s_cbranch_scc0 FQ9X_%=\n\t"
      "s_waitcnt vmcnt(0)\n\t"
      "s_sleep 4\n\t"
      "global_load_dwordx4 v[136:139], v226, s[54:55] sc1\n\t"
      "global_load_dwordx4 v[140:143], v227, s[54:55] sc1\n\t"
      "global_load_dwordx4 v[144:147], v228, s[54:55] sc1\n\t"
      "global_load_dwordx4 v[148:151], v229, s[54:55] sc1\n\t"
      "global_load_dwordx4 v[152:155], v230, s[54:55] sc1\n\t"
      "global_load_dwordx4 v[128:131], v224, s[56:57] sc1\n\t"
      "global_load_dwordx4 v[132:135], v225, s[56:57] sc1\n\t"
      "s_waitcnt vmcnt(6)\n\t"
      "v_max3_u32 v234, v136, v137, v138\n\t"
      "v_max_u32_e32 v234, v234, v139\n\t"
      "v_cmp_eq_u32_e32 vcc, -1, v234\n\t"
      "s_cbranch_vccnz FQ9L_%=\n\t"
      "FQ9X_%=:\n\t"
      "s_branch FQ9B_%=\n\t"
      "FQ10R_%=:\n\t"
      "s_mov_b32 s51, 0\n\t"
      "FQ10L_%=:\n\t"
      "s_add_u32 s51, s51, 1\n\t"
      "s_cmp_lt_u32 s51, 262144\n\t"
      "s_cbranch_scc0 FQ10X_%=\n\t"
      "s_waitcnt vmcnt(0)\n\t"
      "s_sleep 4\n\t"
      "global_load_dwordx4 v[140:143], v227, s[54:55] sc1\n\t"
      "global_load_dwordx4 v[144:147], v228, s[54:55] sc1\n\t"
      "global_load_dwordx4 v[148:151], v229, s[54:55] sc1\n\t"
      "global_load_dwordx4 v[152:155], v230, s[54:55] sc1\n\t"
      "global_load_dwordx4 v[128:131], v224, s[56:57] sc1\n\t"
      "global_load_dwordx4 v[132:135], v225, s[56:57] sc1\n\t"
      "global_load_dwordx4 v[136:139], v226, s[56:57] sc1\n\t"
      "s_waitcnt vmcnt(6)\n\t"
      "v_max3_u32 v234, v140, v141, v142\n\t"
      "v_max_u32_e32 v234, v234, v143\n\t"
      "v_cmp_eq_u32_e32 vcc, -1, v234\n\t"
      "s_cbranch_vccnz FQ10L_%=\n\t"
      "FQ10X_%=:\n\t"
      "s_branch FQ10B_%=\n\t"
      "FQ11R_%=:\n\t"
      "s_mov_b32 s51, 0\n\t"
      "FQ11L_%=:\n\t"
      "s_add_u32 s51, s51, 1\n\t"
      "s_cmp_lt_u32 s51, 262144\n\t"
      "s_cbranch_scc0 FQ11X_%=\n\t"
      "s_waitcnt vmcnt(0)\n\t"
      "s_sleep 4\n\t"
      "global_load_dwordx4 v[144:147], v228, s[54:55] sc1\n\t"
      "global_load_dwordx4 v[148:151], v229, s[54:55] sc1\n\t"
      "global_load_dwordx4 v[152:155], v230, s[54:55] sc1\n\t"
      "global_load_dwordx4 v[128:131], v224, s[56:57] sc1\n\t"
      "global_load_dwordx4 v[132:135], v225, s[56:57] sc1\n\t"
      "global_load_dwordx4 v[136:139], v226, s[56:57] sc1\n\t"
      "global_load_dwordx4 v[140:143], v227, s[56:57] sc1\n\t"
      "s_waitcnt vmcnt(6)\n\t"
      "v_max3_u32 v234, v144, v145, v146\n\t"
      "v_max_u32_e32 v234, v234, v147\n\t"
      "v_cmp_eq_u32_e32 vcc, -1, v234\n\t"
      "s_cbranch_vccnz FQ11L_%=\n\t"
      "FQ11X_%=:\n\t"
      "s_branch FQ11B_%=\n\t"
      "FQ12R_%=:\n\t"
      "s_mov_b32 s51, 0\n\t"
      "FQ12L_%=:\n\t"
      "s_add_u32 s51, s51, 1\n\t"
      "s_cmp_lt_u32 s51, 262144\n\t"
      "s_cbranch_scc0 FQ12X_%=\n\t"
      "s_waitcnt vmcnt(0)\n\t"
      "s_sleep 4\n\t"
      "global_load_dwordx4 v[148:151], v229, s[54:55] sc1\n\t"
      "global_load_dwordx4 v[152:155], v230, s[54:55] sc1\n\t"
      "global_load_dwordx4 v[128:131], v224, s[56:57] sc1\n\t"
      "global_load_dwordx4 v[132:135], v225, s[56:57] sc1\n\t"
      "global_load_dwordx4 v[136:139], v226, s[56:57] sc1\n\t"
      "global_load_dwordx4 v[140:143], v227, s[56:57] sc1\n\t"
      "global_load_dwordx4 v[144:147], v228, s[56:57] sc1\n\t"
      "s_waitcnt vmcnt(6)\n\t"
      "v_max3_u32 v234, v148, v149, v150\n\t"
      "v_max_u32_e32 v234, v234, v151\n\t"
      "v_cmp_eq_u32_e32 vcc, -1, v234\n\t"
      "s_cbranch_vccnz FQ12L_%=\n\t"
      "FQ12X_%=:\n\t"
      "s_branch FQ12B_%=\n\t"
      "FQ13R_%=:\n\t"
      "s_mov_b32 s51, 0\n\t"
      "FQ13L_%=:\n\t"
      "s_add_u32 s51, s51, 1\n\t"
      "s_cmp_lt_u32 s51, 262144\n\t"
      "s_cbranch_scc0 FQ13X_%=\n\t"
      "s_waitcnt vmcnt(0)\n\t"
      "s_sleep 4\n\t"
      "global_load_dwordx4 v[152:155], v230, s[54:55] sc1\n\t"
      "global_load_dwordx4 v[128:131], v224, s[56:57] sc1\n\t"
      "global_load_dwordx4 v[132:135], v225, s[56:57] sc1\n\t"
      "global_load_dwordx4 v[136:139], v226, s[56:57] sc1\n\t"
      "global_load_dwordx4 v[140:143], v227, s[56:57] sc1\n\t"
      "global_load_dwordx4 v[144:147], v228, s[56:57] sc1\n\t"
      "global_load_dwordx4 v[148:151], v229, s[56:57] sc1\n\t"
      "s_waitcnt vmcnt(6)\n\t"
      "v_max3_u32 v234, v152, v153, v154\n\t"
      "v_max_u32_e32 v234, v234, v155\n\t"
      "v_cmp_eq_u32_e32 vcc, -1, v234\n\t"
      "s_cbranch_vccnz FQ13L_%=\n\t"
      "FQ13X_%=:\n\t"
      "s_branch FQ13B_%=\n\t"
      "FQ14R_%=:\n\t"
      "s_mov_b32 s51, 0\n\t"
      "FQ14L_%=:\n\t"
      "s_add_u32 s51, s51, 1\n\t"
      "s_cmp_lt_u32 s51, 262144\n\t"
      "s_cbranch_scc0 FQ14X_%=\n\t"
      "s_waitcnt vmcnt(0)\n\t"
      "s_sleep 4\n\t"
      "global_load_dwordx4 v[128:131], v224, s[56:57] sc1\n\t"
      "global_load_dwordx4 v[132:135], v225, s[56:57] sc1\n\t"
      "global_load_dwordx4 v[136:139], v226, s[56:57] sc1\n\t"
      "global_load_dwordx4 v[140:143], v227, s[56:57] sc1\n\t"
      "global_load_dwordx4 v[144:147], v228, s[56:57] sc1\n\t"
      "global_load_dwordx4 v[148:151], v229, s[56:57] sc1\n\t"
      "global_load_dwordx4 v[152:155], v230, s[56:57] sc1\n\t"
      "s_waitcnt vmcnt(6)\n\t"
      "v_max3_u32 v234, v128, v129, v130\n\t"
      "v_max_u32_e32 v234, v234, v131\n\t"
      "v_cmp_eq_u32_e32 vcc, -1, v234\n\t"
      "s_cbranch_vccnz FQ14L_%=\n\t"
      "FQ14X_%=:\n\t"
      "s_branch FQ14B_%=\n\t"
      "NQ1R_%=:\n\t"
      "s_mov_b32 s51, 0\n\t"
      "NQ1L_%=:\n\t"
      "s_add_u32 s51, s51, 1\n\t"
      "s_cmp_lt_u32 s51, 262144\n\t"
      "s_cbranch_scc0 NQ1X_%=\n\t"
      "s_waitcnt vmcnt(0)\n\t"
      "s_sleep 1\n\t"
      "global_load_dwordx4 v[128:131], v224, s[54:55] sc1\n\t"
      "global_load_dwordx4 v[132:135], v225, s[54:55] sc1\n\t"
      "global_load_dwordx4 v[136:139], v226, s[54:55] sc1\n\t"
      "global_load_dwordx4 v[140:143], v227, s[54:55] sc1\n\t"
      "global_load_dwordx4 v[144:147], v228, s[54:55] sc1\n\t"
      "global_load_dwordx4 v[148:151], v229, s[54:55] sc1\n\t"
      "global_load_dwordx4 v[152:155], v230, s[54:55] sc1\n\t"
      "s_waitcnt vmcnt(0)\n\t"
      "v_max3_u32 v234, v128, v129, v130\n\t"
      "v_max3_u32 v234, v234, v131, v132\n\t"
      "v_max3_u32 v234, v234, v133, v134\n\t"
      "v_max3_u32 v234, v234, v135, v136\n\t"
      "v_max3_u32 v234, v234, v137, v138\n\t"
      "v_max3_u32 v234, v234, v139, v140\n\t"
      "v_max3_u32 v234, v234, v141, v142\n\t"
      "v_max3_u32 v234, v234, v143, v144\n\t"
      "v_max3_u32 v234, v234, v145, v146\n\t"
      "v_max3_u32 v234, v234, v147, v148\n\t"
      "v_max3_u32 v234, v234, v149, v150\n\t"
      "v_max3_u32 v234, v234, v151, v152\n\t"
      "v_max3_u32 v234, v234, v153, v154\n\t"
      "v_max_u32_e32 v234, v234, v155\n\t"
      "v_cmp_eq_u32_e32 vcc, -1, v234\n\t"
      "s_cbranch_vccnz NQ1L_%=\n\t"
      "NQ1X_%=:\n\t"
      "v_mfma_f32_16x16x32_bf16 v[200:203], v[128:131], v[0:3], v[160:163]\n\t"
      "v_mfma_f32_16x16x32_bf16 v[204:207], v[128:131], v[4:7], 0\n\t"
      "v_mfma_f32_16x16x32_bf16 v[208:211], v[128:131], v[8:11], 0\n\t"
      "v_mfma_f32_16x16x32_bf16 v[212:215], v[128:131], v[12:15], 0\n\t"
      "v_mfma_f32_16x16x32_bf16 v[200:203], v[132:135], v[16:19], v[200:203]\n\t"
      "v_mfma_f32_16x16x32_bf16 v[204:207], v[132:135], v[20:23], v[204:207]\n\t"
      "v_mfma_f32_16x16x32_bf16 v[208:211], v[132:135], v[24:27], v[208:211]\n\t"
      "v_mfma_f32_16x16x32_bf16 v[212:215], v[132:135], v[28:31], v[212:215]\n\t"
      "v_mfma_f32_16x16x32_bf16 v[200:203], v[136:139], v[32:35], v[200:203]\n\t"
      "v_mfma_f32_16x16x32_bf16 v[204:207], v[136:139], v[36:39], v[204:207]\n\t"
      "v_mfma_f32_16x16x32_bf16 v[208:211], v[136:139], v[40:43], v[208:211]\n\t"
      "v_mfma_f32_16x16x32_bf16 v[212:215], v[136:139], v[44:47], v[212:215]\n\t"
      "v_mfma_f32_16x16x32_bf16 v[200:203], v[140:143], v[48:51], v[200:203]\n\t"
      "v_mfma_f32_16x16x32_bf16 v[204:207], v[140:143], v[52:55], v[204:207]\n\t"
      "v_mfma_f32_16x16x32_bf16 v[208:211], v[140:143], v[56:59], v[208:211]\n\t"
      "v_mfma_f32_16x16x32_bf16 v[212:215], v[140:143], v[60:63], v[212:215]\n\t"
      "v_mfma_f32_16x16x32_bf16 v[200:203], v[144:147], v[64:67], v[200:203]\n\t"
      "v_mfma_f32_16x16x32_bf16 v[204:207], v[144:147], v[68:71], v[204:207]\n\t"
      "v_mfma_f32_16x16x32_bf16 v[208:211], v[144:147], v[72:75], v[208:211]\n\t"
      "v_mfma_f32_16x16x32_bf16 v[212:215], v[144:147], v[76:79], v[212:215]\n\t"
      "v_mfma_f32_16x16x32_bf16 v[200:203], v[148:151], v[80:83], v[200:203]\n\t"
      "v_mfma_f32_16x16x32_bf16 v[204:207], v[148:151], v[84:87], v[204:207]\n\t"
      "v_mfma_f32_16x16x32_bf16 v[208:211], v[148:151], v[88:91], v[208:211]\n\t"
      "v_mfma_f32_16x16x32_bf16 v[212:215], v[148:151], v[92:95], v[212:215]\n\t"
      "v_mfma_f32_16x16x32_bf16 v[200:203], v[152:155], v[96:99], v[200:203]\n\t"
      "v_mfma_f32_16x16x32_bf16 v[204:207], v[152:155], v[100:103], v[204:207]\n\t"
      "v_mfma_f32_16x16x32_bf16 v[208:211], v[152:155], v[104:107], v[208:211]\n\t"
      "v_mfma_f32_16x16x32_bf16 v[212:215], v[152:155], v[108:111], v[212:215]\n\t"
      "global_load_dwordx4 v[132:135], v225, s[56:57] sc1\n\t"
      "global_load_dwordx4 v[136:139], v226, s[56:57] sc1\n\t"
      "global_load_dwordx4 v[140:143], v227, s[56:57] sc1\n\t"
      "global_load_dwordx4 v[144:147], v228, s[56:57] sc1\n\t"
      "global_load_dwordx4 v[148:151], v229, s[56:57] sc1\n\t"
      "global_load_dwordx4 v[152:155], v230, s[56:57] sc1\n\t"
      "global_load_dwordx4 v[128:131], v224, s[58:59] sc1\n\t"
      "s_waitcnt vmcnt(0)\n\t"
      "s_nop 7\n\t"
      "s_branch NQ1B_%=\n\t"
      "FQ15R_%=:\n\t"
      "s_mov_b32 s51, 0\n\t"
      "FQ15L_%=:\n\t"
      "s_add_u32 s51, s51, 1\n\t"
      "s_cmp_lt_u32 s51, 262144\n\t"
      "s_cbranch_scc0 FQ15X_%=\n\t"
      "s_waitcnt vmcnt(0)\n\t"
      "s_sleep 4\n\t"
      "global_load_dwordx4 v[132:135], v225, s[56:57] sc1\n\t"
      "global_load_dwordx4 v[136:139], v226, s[56:57] sc1\n\t"
      "global_load_dwordx4 v[140:143], v227, s[56:57] sc1\n\t"
      "global_load_dwordx4 v[144:147], v228, s[56:57] sc1\n\t"
      "global_load_dwordx4 v[148:151], v229, s[56:57] sc1\n\t"
      "global_load_dwordx4 v[152:155], v230, s[56:57] sc1\n\t"
      "global_load_dwordx4 v[128:131], v224, s[58:59] sc1\n\t"
      "s_waitcnt vmcnt(6)\n\t"
      "v_max3_u32 v234, v132, v133, v134\n\t"
      "v_max_u32_e32 v234, v234, v135\n\t"
      "v_cmp_eq_u32_e32 vcc, -1, v234\n\t"
      "s_cbranch_vccnz FQ15L_%=\n\t"
      "FQ15X_%=:\n\t"
      "s_branch FQ15B_%=\n\t"
      "FQ16R_%=:\n\t"
      "s_mov_b32 s51, 0\n\t"
      "FQ16L_%=:\n\t"
      "s_add_u32 s51, s51, 1\n\t"
      "s_cmp_lt_u32 s51, 262144\n\t"
      "s_cbranch_scc0 FQ16X_%=\n\t"
      "s_waitcnt vmcnt(0)\n\t"
      "s_sleep 4\n\t"
      "global_load_dwordx4 v[136:139], v226, s[56:57] sc1\n\t"
      "global_load_dwordx4 v[140:143], v227, s[56:57] sc1\n\t"
      "global_load_dwordx4 v[144:147], v228, s[56:57] sc1\n\t"
      "global_load_dwordx4 v[148:151], v229, s[56:57] sc1\n\t"
      "global_load_dwordx4 v[152:155], v230, s[56:57] sc1\n\t"
      "global_load_dwordx4 v[128:131], v224, s[58:59] sc1\n\t"
      "global_load_dwordx4 v[132:135], v225, s[58:59] sc1\n\t"
      "s_waitcnt vmcnt(6)\n\t"
      "v_max3_u32 v234, v136, v137, v138\n\t"
      "v_max_u32_e32 v234, v234, v139\n\t"
      "v_cmp_eq_u32_e32 vcc, -1, v234\n\t"
      "s_cbranch_vccnz FQ16L_%=\n\t"
      "FQ16X_%=:\n\t"
      "s_branch FQ16B_%=\n\t"
      "FQ17R_%=:\n\t"
      "s_mov_b32 s51, 0\n\t"
      "FQ17L_%=:\n\t"
      "s_add_u32 s51, s51, 1\n\t"
      "s_cmp_lt_u32 s51, 262144\n\t"
      "s_cbranch_scc0 FQ17X_%=\n\t"
      "s_waitcnt vmcnt(0)\n\t"
      "s_sleep 4\n\t"
      "global_load_dwordx4 v[140:143], v227, s[56:57] sc1\n\t"
      "global_load_dwordx4 v[144:147], v228, s[56:57] sc1\n\t"
      "global_load_dwordx4 v[148:151], v229, s[56:57] sc1\n\t"
      "global_load_dwordx4 v[152:155], v230, s[56:57] sc1\n\t"
      "global_load_dwordx4 v[128:131], v224, s[58:59] sc1\n\t"
      "global_load_dwordx4 v[132:135], v225, s[58:59] sc1\n\t"
      "global_load_dwordx4 v[136:139], v226, s[58:59] sc1\n\t"
      "s_waitcnt vmcnt(6)\n\t"
      "v_max3_u32 v234, v140, v141, v142\n\t"
      "v_max_u32_e32 v234, v234, v143\n\t"
      "v_cmp_eq_u32_e32 vcc, -1, v234\n\t"
      "s_cbranch_vccnz FQ17L_%=\n\t"
      "FQ17X_%=:\n\t"
      "s_branch FQ17B_%=\n\t"
      "FQ18R_%=:\n\t"
      "s_mov_b32 s51, 0\n\t"
      "FQ18L_%=:\n\t"
      "s_add_u32 s51, s51, 1\n\t"
      "s_cmp_lt_u32 s51, 262144\n\t"
      "s_cbranch_scc0 FQ18X_%=\n\t"
      "s_waitcnt vmcnt(0)\n\t"
      "s_sleep 4\n\t"
      "global_load_dwordx4 v[144:147], v228, s[56:57] sc1\n\t"
      "global_load_dwordx4 v[148:151], v229, s[56:57] sc1\n\t"
      "global_load_dwordx4 v[152:155], v230, s[56:57] sc1\n\t"
      "global_load_dwordx4 v[128:131], v224, s[58:59] sc1\n\t"
      "global_load_dwordx4 v[132:135], v225, s[58:59] sc1\n\t"
      "global_load_dwordx4 v[136:139], v226, s[58:59] sc1\n\t"
      "global_load_dwordx4 v[140:143], v227, s[58:59] sc1\n\t"
      "s_waitcnt vmcnt(6)\n\t"
      "v_max3_u32 v234, v144, v145, v146\n\t"
      "v_max_u32_e32 v234, v234, v147\n\t"
      "v_cmp_eq_u32_e32 vcc, -1, v234\n\t"
      "s_cbranch_vccnz FQ18L_%=\n\t"
      "FQ18X_%=:\n\t"
      "s_branch FQ18B_%=\n\t"
      "FQ19R_%=:\n\t"
      "s_mov_b32 s51, 0\n\t"
      "FQ19L_%=:\n\t"
      "s_add_u32 s51, s51, 1\n\t"
      "s_cmp_lt_u32 s51, 262144\n\t"
      "s_cbranch_scc0 FQ19X_%=\n\t"
      "s_waitcnt vmcnt(0)\n\t"
      "s_sleep 4\n\t"
      "global_load_dwordx4 v[148:151], v229, s[56:57] sc1\n\t"
      "global_load_dwordx4 v[152:155], v230, s[56:57] sc1\n\t"
      "global_load_dwordx4 v[128:131], v224, s[58:59] sc1\n\t"
      "global_load_dwordx4 v[132:135], v225, s[58:59] sc1\n\t"
      "global_load_dwordx4 v[136:139], v226, s[58:59] sc1\n\t"
      "global_load_dwordx4 v[140:143], v227, s[58:59] sc1\n\t"
      "global_load_dwordx4 v[144:147], v228, s[58:59] sc1\n\t"
      "s_waitcnt vmcnt(6)\n\t"
      "v_max3_u32 v234, v148, v149, v150\n\t"
      "v_max_u32_e32 v234, v234, v151\n\t"
      "v_cmp_eq_u32_e32 vcc, -1, v234\n\t"
      "s_cbranch_vccnz FQ19L_%=\n\t"
      "FQ19X_%=:\n\t"
      "s_branch FQ19B_%=\n\t"
      "FQ20R_%=:\n\t"
      "s_mov_b32 s51, 0\n\t"
      "FQ20L_%=:\n\t"
      "s_add_u32 s51, s51, 1\n\t"
      "s_cmp_lt_u32 s51, 262144\n\t"
      "s_cbranch_scc0 FQ20X_%=\n\t"
      "s_waitcnt vmcnt(0)\n\t"
      "s_sleep 4\n\t"
      "global_load_dwordx4 v[152:155], v230, s[56:57] sc1\n\t"
      "global_load_dwordx4 v[128:131], v224, s[58:59] sc1\n\t"
      "global_load_dwordx4 v[132:135], v225, s[58:59] sc1\n\t"
      "global_load_dwordx4 v[136:139], v226, s[58:59] sc1\n\t"
      "global_load_dwordx4 v[140:143], v227, s[58:59] sc1\n\t"
      "global_load_dwordx4 v[144:147], v228, s[58:59] sc1\n\t"
      "global_load_dwordx4 v[148:151], v229, s[58:59] sc1\n\t"
      "s_waitcnt vmcnt(6)\n\t"
      "v_max3_u32 v234, v152, v153, v154\n\t"
      "v_max_u32_e32 v234, v234, v155\n\t"
      "v_cmp_eq_u32_e32 vcc, -1, v234\n\t"
      "s_cbranch_vccnz FQ20L_%=\n\t"
      "FQ20X_%=:\n\t"
      "s_branch FQ20B_%=\n\t"
      "FQ21R_%=:\n\t"
      "s_mov_b32 s51, 0\n\t"
      "FQ21L_%=:\n\t"
      "s_add_u32 s51, s51, 1\n\t"
      "s_cmp_lt_u32 s51, 262144\n\t"
      "s_cbranch_scc0 FQ21X_%=\n\t"
      "s_waitcnt vmcnt(0)\n\t"
      "s_sleep 4\n\t"
      "global_load_dwordx4 v[128:131], v224, s[58:59] sc1\n\t"
      "global_load_dwordx4 v[132:135], v225, s[58:59] sc1\n\t"
      "global_load_dwordx4 v[136:139], v226, s[58:59] sc1\n\t"
      "global_load_dwordx4 v[140:143], v227, s[58:59] sc1\n\t"
      "global_load_dwordx4 v[144:147], v228, s[58:59] sc1\n\t"
      "global_load_dwordx4 v[148:151], v229, s[58:59] sc1\n\t"
      "global_load_dwordx4 v[152:155], v230, s[58:59] sc1\n\t"
      "s_waitcnt vmcnt(6)\n\t"
      "v_max3_u32 v234, v128, v129, v130\n\t"
      "v_max_u32_e32 v234, v234, v131\n\t"
      "v_cmp_eq_u32_e32 vcc, -1, v234\n\t"
      "s_cbranch_vccnz FQ21L_%=\n\t"
      "FQ21X_%=:\n\t"
      "s_branch FQ21B_%=\n\t"
      "NQ2R_%=:\n\t"
      "s_mov_b32 s51, 0\n\t"
      "NQ2L_%=:\n\t"
      "s_add_u32 s51, s51, 1\n\t"
      "s_cmp_lt_u32 s51, 262144\n\t"
      "s_cbranch_scc0 NQ2X_%=\n\t"
      "s_waitcnt vmcnt(0)\n\t"
      "s_sleep 1\n\t"
      "global_load_dwordx4 v[128:131], v224, s[56:57] sc1\n\t"
      "global_load_dwordx4 v[132:135], v225, s[56:57] sc1\n\t"
      "global_load_dwordx4 v[136:139], v226, s[56:57] sc1\n\t"
      "global_load_dwordx4 v[140:143], v227, s[56:57] sc1\n\t"
      "global_load_dwordx4 v[144:147], v228, s[56:57] sc1\n\t"
      "global_load_dwordx4 v[148:151], v229, s[56:57] sc1\n\t"
      "global_load_dwordx4 v[152:155], v230, s[56:57] sc1\n\t"
      "s_waitcnt vmcnt(0)\n\t"
      "v_max3_u32 v234, v128, v129, v130\n\t"
      "v_max3_u32 v234, v234, v131, v132\n\t"
      "v_max3_u32 v234, v234, v133, v134\n\t"
      "v_max3_u32 v234, v234, v135, v136\n\t"
      "v_max3_u32 v234, v234, v137, v138\n\t"
      "v_max3_u32 v234, v234, v139, v140\n\t"
      "v_max3_u32 v234, v234, v141, v142\n\t"
      "v_max3_u32 v234, v234, v143, v144\n\t"
      "v_max3_u32 v234, v234, v145, v146\n\t"
      "v_max3_u32 v234, v234, v147, v148\n\t"
      "v_max3_u32 v234, v234, v149, v150\n\t"
      "v_max3_u32 v234, v234, v151, v152\n\t"
      "v_max3_u32 v234, v234, v153, v154\n\t"
      "v_max_u32_e32 v234, v234, v155\n\t"
      "v_cmp_eq_u32_e32 vcc, -1, v234\n\t"
      "s_cbranch_vccnz NQ2L_%=\n\t"
      "NQ2X_%=:\n\t"
      "v_mfma_f32_16x16x32_bf16 v[184:187], v[128:131], v[0:3], v[164:167]\n\t"
      "v_mfma_f32_16x16x32_bf16 v[188:191], v[128:131], v[4:7], 0\n\t"
      "v_mfma_f32_16x16x32_bf16 v[192:195], v[128:131], v[8:11], 0\n\t"
      "v_mfma_f32_16x16x32_bf16 v[196:199], v[128:131], v[12:15], 0\n\t"
      "v_mfma_f32_16x16x32_bf16 v[184:187], v[132:135], v[16:19], v[184:187]\n\t"
      "v_mfma_f32_16x16x32_bf16 v[188:191], v[132:135], v[20:23], v[188:191]\n\t"
      "v_mfma_f32_16x16x32_bf16 v[192:195], v[132:135], v[24:27], v[192:195]\n\t"
      "v_mfma_f32_16x16x32_bf16 v[196:199], v[132:135], v[28:31], v[196:199]\n\t"
      "v_mfma_f32_16x16x32_bf16 v[184:187], v[136:139], v[32:35], v[184:187]\n\t"
      "v_mfma_f32_16x16x32_bf16 v[188:191], v[136:139], v[36:39], v[188:191]\n\t"
      "v_mfma_f32_16x16x32_bf16 v[192:195], v[136:139], v[40:43], v[192:195]\n\t"
      "v_mfma_f32_16x16x32_bf16 v[196:199], v[136:139], v[44:47], v[196:199]\n\t"
      "v_mfma_f32_16x16x32_bf16 v[184:187], v[140:143], v[48:51], v[184:187]\n\t"
      "v_mfma_f32_16x16x32_bf16 v[188:191], v[140:143], v[52:55], v[188:191]\n\t"
      "v_mfma_f32_16x16x32_bf16 v[192:195], v[140:143], v[56:59], v[192:195]\n\t"
      "v_mfma_f32_16x16x32_bf16 v[196:199], v[140:143], v[60:63], v[196:199]\n\t"
      "v_mfma_f32_16x16x32_bf16 v[184:187], v[144:147], v[64:67], v[184:187]\n\t"
      "v_mfma_f32_16x16x32_bf16 v[188:191], v[144:147], v[68:71], v[188:191]\n\t"
      "v_mfma_f32_16x16x32_bf16 v[192:195], v[144:147], v[72:75], v[192:195]\n\t"
      "v_mfma_f32_16x16x32_bf16 v[196:199], v[144:147], v[76:79], v[196:199]\n\t"
      "v_mfma_f32_16x16x32_bf16 v[184:187], v[148:151], v[80:83], v[184:187]\n\t"
      "v_mfma_f32_16x16x32_bf16 v[188:191], v[148:151], v[84:87], v[188:191]\n\t"
      "v_mfma_f32_16x16x32_bf16 v[192:195], v[148:151], v[88:91], v[192:195]\n\t"
      "v_mfma_f32_16x16x32_bf16 v[196:199], v[148:151], v[92:95], v[196:199]\n\t"
      "v_mfma_f32_16x16x32_bf16 v[184:187], v[152:155], v[96:99], v[184:187]\n\t"
      "v_mfma_f32_16x16x32_bf16 v[188:191], v[152:155], v[100:103], v[188:191]\n\t"
      "v_mfma_f32_16x16x32_bf16 v[192:195], v[152:155], v[104:107], v[192:195]\n\t"
      "v_mfma_f32_16x16x32_bf16 v[196:199], v[152:155], v[108:111], v[196:199]\n\t"
      "global_load_dwordx4 v[132:135], v225, s[58:59] sc1\n\t"
      "global_load_dwordx4 v[136:139], v226, s[58:59] sc1\n\t"
      "global_load_dwordx4 v[140:143], v227, s[58:59] sc1\n\t"
      "global_load_dwordx4 v[144:147], v228, s[58:59] sc1\n\t"
      "global_load_dwordx4 v[148:151], v229, s[58:59] sc1\n\t"
      "global_load_dwordx4 v[152:155], v230, s[58:59] sc1\n\t"
      "global_load_dwordx4 v[128:131], v224, s[60:61] sc1\n\t"
      "s_waitcnt vmcnt(0)\n\t"
      "s_nop 7\n\t"
      "s_branch NQ2B_%=\n\t"
      "FQ22R_%=:\n\t"
      "s_mov_b32 s51, 0\n\t"
      "FQ22L_%=:\n\t"
      "s_add_u32 s51, s51, 1\n\t"
      "s_cmp_lt_u32 s51, 262144\n\t"
      "s_cbranch_scc0 FQ22X_%=\n\t"
      "s_waitcnt vmcnt(0)\n\t"
      "s_sleep 4\n\t"
      "global_load_dwordx4 v[132:135], v225, s[58:59] sc1\n\t"
      "global_load_dwordx4 v[136:139], v226, s[58:59] sc1\n\t"
      "global_load_dwordx4 v[140:143], v227, s[58:59] sc1\n\t"
      "global_load_dwordx4 v[144:147], v228, s[58:59] sc1\n\t"
      "global_load_dwordx4 v[148:151], v229, s[58:59] sc1\n\t"
      "global_load_dwordx4 v[152:155], v230, s[58:59] sc1\n\t"
      "global_load_dwordx4 v[128:131], v224, s[60:61] sc1\n\t"
      "s_waitcnt vmcnt(6)\n\t"
      "v_max3_u32 v234, v132, v133, v134\n\t"
      "v_max_u32_e32 v234, v234, v135\n\t"
      "v_cmp_eq_u32_e32 vcc, -1, v234\n\t"
      "s_cbranch_vccnz FQ22L_%=\n\t"
      "FQ22X_%=:\n\t"
      "s_branch FQ22B_%=\n\t"
      "FQ23R_%=:\n\t"
      "s_mov_b32 s51, 0\n\t"
      "FQ23L_%=:\n\t"
      "s_add_u32 s51, s51, 1\n\t"
      "s_cmp_lt_u32 s51, 262144\n\t"
      "s_cbranch_scc0 FQ23X_%=\n\t"
      "s_waitcnt vmcnt(0)\n\t"
      "s_sleep 4\n\t"
      "global_load_dwordx4 v[136:139], v226, s[58:59] sc1\n\t"
      "global_load_dwordx4 v[140:143], v227, s[58:59] sc1\n\t"
      "global_load_dwordx4 v[144:147], v228, s[58:59] sc1\n\t"
      "global_load_dwordx4 v[148:151], v229, s[58:59] sc1\n\t"
      "global_load_dwordx4 v[152:155], v230, s[58:59] sc1\n\t"
      "global_load_dwordx4 v[128:131], v224, s[60:61] sc1\n\t"
      "global_load_dwordx4 v[132:135], v225, s[60:61] sc1\n\t"
      "s_waitcnt vmcnt(6)\n\t"
      "v_max3_u32 v234, v136, v137, v138\n\t"
      "v_max_u32_e32 v234, v234, v139\n\t"
      "v_cmp_eq_u32_e32 vcc, -1, v234\n\t"
      "s_cbranch_vccnz FQ23L_%=\n\t"
      "FQ23X_%=:\n\t"
      "s_branch FQ23B_%=\n\t"
      "FQ24R_%=:\n\t"
      "s_mov_b32 s51, 0\n\t"
      "FQ24L_%=:\n\t"
      "s_add_u32 s51, s51, 1\n\t"
      "s_cmp_lt_u32 s51, 262144\n\t"
      "s_cbranch_scc0 FQ24X_%=\n\t"
      "s_waitcnt vmcnt(0)\n\t"
      "s_sleep 4\n\t"
      "global_load_dwordx4 v[140:143], v227, s[58:59] sc1\n\t"
      "global_load_dwordx4 v[144:147], v228, s[58:59] sc1\n\t"
      "global_load_dwordx4 v[148:151], v229, s[58:59] sc1\n\t"
      "global_load_dwordx4 v[152:155], v230, s[58:59] sc1\n\t"
      "global_load_dwordx4 v[128:131], v224, s[60:61] sc1\n\t"
      "global_load_dwordx4 v[132:135], v225, s[60:61] sc1\n\t"
      "global_load_dwordx4 v[136:139], v226, s[60:61] sc1\n\t"
      "s_waitcnt vmcnt(6)\n\t"
      "v_max3_u32 v234, v140, v141, v142\n\t"
      "v_max_u32_e32 v234, v234, v143\n\t"
      "v_cmp_eq_u32_e32 vcc, -1, v234\n\t"
      "s_cbranch_vccnz FQ24L_%=\n\t"
      "FQ24X_%=:\n\t"
      "s_branch FQ24B_%=\n\t"
      "FQ25R_%=:\n\t"
      "s_mov_b32 s51, 0\n\t"
      "FQ25L_%=:\n\t"
      "s_add_u32 s51, s51, 1\n\t"
      "s_cmp_lt_u32 s51, 262144\n\t"
      "s_cbranch_scc0 FQ25X_%=\n\t"
      "s_waitcnt vmcnt(0)\n\t"
      "s_sleep 4\n\t"
      "global_load_dwordx4 v[144:147], v228, s[58:59] sc1\n\t"
      "global_load_dwordx4 v[148:151], v229, s[58:59] sc1\n\t"
      "global_load_dwordx4 v[152:155], v230, s[58:59] sc1\n\t"
      "global_load_dwordx4 v[128:131], v224, s[60:61] sc1\n\t"
      "global_load_dwordx4 v[132:135], v225, s[60:61] sc1\n\t"
      "global_load_dwordx4 v[136:139], v226, s[60:61] sc1\n\t"
      "global_load_dwordx4 v[140:143], v227, s[60:61] sc1\n\t"
      "s_waitcnt vmcnt(6)\n\t"
      "v_max3_u32 v234, v144, v145, v146\n\t"
      "v_max_u32_e32 v234, v234, v147\n\t"
      "v_cmp_eq_u32_e32 vcc, -1, v234\n\t"
      "s_cbranch_vccnz FQ25L_%=\n\t"
      "FQ25X_%=:\n\t"
      "s_branch FQ25B_%=\n\t"
      "FQ26R_%=:\n\t"
      "s_mov_b32 s51, 0\n\t"
      "FQ26L_%=:\n\t"
      "s_add_u32 s51, s51, 1\n\t"
      "s_cmp_lt_u32 s51, 262144\n\t"
      "s_cbranch_scc0 FQ26X_%=\n\t"
      "s_waitcnt vmcnt(0)\n\t"
      "s_sleep 4\n\t"
      "global_load_dwordx4 v[148:151], v229, s[58:59] sc1\n\t"
      "global_load_dwordx4 v[152:155], v230, s[58:59] sc1\n\t"
      "global_load_dwordx4 v[128:131], v224, s[60:61] sc1\n\t"
      "global_load_dwordx4 v[132:135], v225, s[60:61] sc1\n\t"
      "global_load_dwordx4 v[136:139], v226, s[60:61] sc1\n\t"
      "global_load_dwordx4 v[140:143], v227, s[60:61] sc1\n\t"
      "global_load_dwordx4 v[144:147], v228, s[60:61] sc1\n\t"
      "s_waitcnt vmcnt(6)\n\t"
      "v_max3_u32 v234, v148, v149, v150\n\t"
      "v_max_u32_e32 v234, v234, v151\n\t"
      "v_cmp_eq_u32_e32 vcc, -1, v234\n\t"
      "s_cbranch_vccnz FQ26L_%=\n\t"
      "FQ26X_%=:\n\t"
      "s_branch FQ26B_%=\n\t"
      "FQ27R_%=:\n\t"
      "s_mov_b32 s51, 0\n\t"
      "FQ27L_%=:\n\t"
      "s_add_u32 s51, s51, 1\n\t"
      "s_cmp_lt_u32 s51, 262144\n\t"
      "s_cbranch_scc0 FQ27X_%=\n\t"
      "s_waitcnt vmcnt(0)\n\t"
      "s_sleep 4\n\t"
      "global_load_dwordx4 v[152:155], v230, s[58:59] sc1\n\t"
      "global_load_dwordx4 v[128:131], v224, s[60:61] sc1\n\t"
      "global_load_dwordx4 v[132:135], v225, s[60:61] sc1\n\t"
      "global_load_dwordx4 v[136:139], v226, s[60:61] sc1\n\t"
      "global_load_dwordx4 v[140:143], v227, s[60:61] sc1\n\t"
      "global_load_dwordx4 v[144:147], v228, s[60:61] sc1\n\t"
      "global_load_dwordx4 v[148:151], v229, s[60:61] sc1\n\t"
      "s_waitcnt vmcnt(6)\n\t"
      "v_max3_u32 v234, v152, v153, v154\n\t"
      "v_max_u32_e32 v234, v234, v155\n\t"
      "v_cmp_eq_u32_e32 vcc, -1, v234\n\t"
      "s_cbranch_vccnz FQ27L_%=\n\t"
      "FQ27X_%=:\n\t"
      "s_branch FQ27B_%=\n\t"
      "FQ28R_%=:\n\t"
      "s_mov_b32 s51, 0\n\t"
      "FQ28L_%=:\n\t"
      "s_add_u32 s51, s51, 1\n\t"
      "s_cmp_lt_u32 s51, 262144\n\t"
      "s_cbranch_scc0 FQ28X_%=\n\t"
      "s_waitcnt vmcnt(0)\n\t"
      "s_sleep 4\n\t"
      "global_load_dwordx4 v[128:131], v224, s[60:61] sc1\n\t"
      "global_load_dwordx4 v[132:135], v225, s[60:61] sc1\n\t"
      "global_load_dwordx4 v[136:139], v226, s[60:61] sc1\n\t"
      "global_load_dwordx4 v[140:143], v227, s[60:61] sc1\n\t"
      "global_load_dwordx4 v[144:147], v228, s[60:61] sc1\n\t"
      "global_load_dwordx4 v[148:151], v229, s[60:61] sc1\n\t"
      "global_load_dwordx4 v[152:155], v230, s[60:61] sc1\n\t"
      "s_waitcnt vmcnt(6)\n\t"
      "v_max3_u32 v234, v128, v129, v130\n\t"
      "v_max_u32_e32 v234, v234, v131\n\t"
      "v_cmp_eq_u32_e32 vcc, -1, v234\n\t"
      "s_cbranch_vccnz FQ28L_%=\n\t"
      "FQ28X_%=:\n\t"
      "s_branch FQ28B_%=\n\t"
      "NQ3R_%=:\n\t"
      "s_mov_b32 s51, 0\n\t"
      "NQ3L_%=:\n\t"
      "s_add_u32 s51, s51, 1\n\t"
      "s_cmp_lt_u32 s51, 262144\n\t"
      "s_cbranch_scc0 NQ3X_%=\n\t"
      "s_waitcnt vmcnt(0)\n\t"
      "s_sleep 1\n\t"
      "global_load_dwordx4 v[128:131], v224, s[58:59] sc1\n\t"
      "global_load_dwordx4 v[132:135], v225, s[58:59] sc1\n\t"
      "global_load_dwordx4 v[136:139], v226, s[58:59] sc1\n\t"
      "global_load_dwordx4 v[140:143], v227, s[58:59] sc1\n\t"
      "global_load_dwordx4 v[144:147], v228, s[58:59] sc1\n\t"
      "global_load_dwordx4 v[148:151], v229, s[58:59] sc1\n\t"
      "global_load_dwordx4 v[152:155], v230, s[58:59] sc1\n\t"
      "s_waitcnt vmcnt(0)\n\t"
      "v_max3_u32 v234, v128, v129, v130\n\t"
      "v_max3_u32 v234, v234, v131, v132\n\t"
      "v_max3_u32 v234, v234, v133, v134\n\t"
      "v_max3_u32 v234, v234, v135, v136\n\t"
      "v_max3_u32 v234, v234, v137, v138\n\t"
      "v_max3_u32 v234, v234, v139, v140\n\t"
      "v_max3_u32 v234, v234, v141, v142\n\t"
      "v_max3_u32 v234, v234, v143, v144\n\t"
      "v_max3_u32 v234, v234, v145, v146\n\t"
      "v_max3_u32 v234, v234, v147, v148\n\t"
      "v_max3_u32 v234, v234, v149, v150\n\t"
      "v_max3_u32 v234, v234, v151, v152\n\t"
      "v_max3_u32 v234, v234, v153, v154\n\t"
      "v_max_u32_e32 v234, v234, v155\n\t"
      "v_cmp_eq_u32_e32 vcc, -1, v234\n\t"
      "s_cbranch_vccnz NQ3L_%=\n\t"
      "NQ3X_%=:\n\t"
      "v_mfma_f32_16x16x32_bf16 v[200:203], v[128:131], v[0:3], v[168:171]\n\t"
      "v_mfma_f32_16x16x32_bf16 v[204:207], v[128:131], v[4:7], 0\n\t"
      "v_mfma_f32_16x16x32_bf16 v[208:211], v[128:131], v[8:11], 0\n\t"
      "v_mfma_f32_16x16x32_bf16 v[212:215], v[128:131], v[12:15], 0\n\t"
      "v_mfma_f32_16x16x32_bf16 v[200:203], v[132:135], v[16:19], v[200:203]\n\t"
      "v_mfma_f32_16x16x32_bf16 v[204:207], v[132:135], v[20:23], v[204:207]\n\t"
      "v_mfma_f32_16x16x32_bf16 v[208:211], v[132:135], v[24:27], v[208:211]\n\t"
      "v_mfma_f32_16x16x32_bf16 v[212:215], v[132:135], v[28:31], v[212:215]\n\t"
      "v_mfma_f32_16x16x32_bf16 v[200:203], v[136:139], v[32:35], v[200:203]\n\t"
      "v_mfma_f32_16x16x32_bf16 v[204:207], v[136:139], v[36:39], v[204:207]\n\t"
      "v_mfma_f32_16x16x32_bf16 v[208:211], v[136:139], v[40:43], v[208:211]\n\t"
      "v_mfma_f32_16x16x32_bf16 v[212:215], v[136:139], v[44:47], v[212:215]\n\t"
      "v_mfma_f32_16x16x32_bf16 v[200:203], v[140:143], v[48:51], v[200:203]\n\t"
      "v_mfma_f32_16x16x32_bf16 v[204:207], v[140:143], v[52:55], v[204:207]\n\t"
      "v_mfma_f32_16x16x32_bf16 v[208:211], v[140:143], v[56:59], v[208:211]\n\t"
      "v_mfma_f32_16x16x32_bf16 v[212:215], v[140:143], v[60:63], v[212:215]\n\t"
      "v_mfma_f32_16x16x32_bf16 v[200:203], v[144:147], v[64:67], v[200:203]\n\t"
      "v_mfma_f32_16x16x32_bf16 v[204:207], v[144:147], v[68:71], v[204:207]\n\t"
      "v_mfma_f32_16x16x32_bf16 v[208:211], v[144:147], v[72:75], v[208:211]\n\t"
      "v_mfma_f32_16x16x32_bf16 v[212:215], v[144:147], v[76:79], v[212:215]\n\t"
      "v_mfma_f32_16x16x32_bf16 v[200:203], v[148:151], v[80:83], v[200:203]\n\t"
      "v_mfma_f32_16x16x32_bf16 v[204:207], v[148:151], v[84:87], v[204:207]\n\t"
      "v_mfma_f32_16x16x32_bf16 v[208:211], v[148:151], v[88:91], v[208:211]\n\t"
      "v_mfma_f32_16x16x32_bf16 v[212:215], v[148:151], v[92:95], v[212:215]\n\t"
      "v_mfma_f32_16x16x32_bf16 v[200:203], v[152:155], v[96:99], v[200:203]\n\t"
      "v_mfma_f32_16x16x32_bf16 v[204:207], v[152:155], v[100:103], v[204:207]\n\t"
      "v_mfma_f32_16x16x32_bf16 v[208:211], v[152:155], v[104:107], v[208:211]\n\t"
      "v_mfma_f32_16x16x32_bf16 v[212:215], v[152:155], v[108:111], v[212:215]\n\t"
      "global_load_dwordx4 v[132:135], v225, s[60:61] sc1\n\t"
      "global_load_dwordx4 v[136:139], v226, s[60:61] sc1\n\t"
      "global_load_dwordx4 v[140:143], v227, s[60:61] sc1\n\t"
      "global_load_dwordx4 v[144:147], v228, s[60:61] sc1\n\t"
      "global_load_dwordx4 v[148:151], v229, s[60:61] sc1\n\t"
      "global_load_dwordx4 v[152:155], v230, s[60:61] sc1\n\t"
      "global_load_dwordx4 v[128:131], v224, s[62:63] sc1\n\t"
      "s_waitcnt vmcnt(0)\n\t"
      "s_nop 7\n\t"
      "s_branch NQ3B_%=\n\t"
      "FQ29R_%=:\n\t"
      "s_mov_b32 s51, 0\n\t"
      "FQ29L_%=:\n\t"
      "s_add_u32 s51, s51, 1\n\t"
      "s_cmp_lt_u32 s51, 262144\n\t"
      "s_cbranch_scc0 FQ29X_%=\n\t"
      "s_waitcnt vmcnt(0)\n\t"
      "s_sleep 4\n\t"
      "global_load_dwordx4 v[132:135], v225, s[60:61] sc1\n\t"
      "global_load_dwordx4 v[136:139], v226, s[60:61] sc1\n\t"
      "global_load_dwordx4 v[140:143], v227, s[60:61] sc1\n\t"
      "global_load_dwordx4 v[144:147], v228, s[60:61] sc1\n\t"
      "global_load_dwordx4 v[148:151], v229, s[60:61] sc1\n\t"
      "global_load_dwordx4 v[152:155], v230, s[60:61] sc1\n\t"
      "global_load_dwordx4 v[128:131], v224, s[62:63] sc1\n\t"
      "s_waitcnt vmcnt(6)\n\t"
      "v_max3_u32 v234, v132, v133, v134\n\t"
      "v_max_u32_e32 v234, v234, v135\n\t"
      "v_cmp_eq_u32_e32 vcc, -1, v234\n\t"
      "s_cbranch_vccnz FQ29L_%=\n\t"
      "FQ29X_%=:\n\t"
      "s_branch FQ29B_%=\n\t"
      "FQ30R_%=:\n\t"
      "s_mov_b32 s51, 0\n\t"
      "FQ30L_%=:\n\t"
      "s_add_u32 s51, s51, 1\n\t"
      "s_cmp_lt_u32 s51, 262144\n\t"
      "s_cbranch_scc0 FQ30X_%=\n\t"
      "s_waitcnt vmcnt(0)\n\t"
      "s_sleep 4\n\t"
      "global_load_dwordx4 v[136:139], v226, s[60:61] sc1\n\t"
      "global_load_dwordx4 v[140:143], v227, s[60:61] sc1\n\t"
      "global_load_dwordx4 v[144:147], v228, s[60:61] sc1\n\t"
      "global_load_dwordx4 v[148:151], v229, s[60:61] sc1\n\t"
      "global_load_dwordx4 v[152:155], v230, s[60:61] sc1\n\t"
      "global_load_dwordx4 v[128:131], v224, s[62:63] sc1\n\t"
      "global_load_dwordx4 v[132:135], v225, s[62:63] sc1\n\t"
      "s_waitcnt vmcnt(6)\n\t"
      "v_max3_u32 v234, v136, v137, v138\n\t"
      "v_max_u32_e32 v234, v234, v139\n\t"
      "v_cmp_eq_u32_e32 vcc, -1, v234\n\t"
      "s_cbranch_vccnz FQ30L_%=\n\t"
      "FQ30X_%=:\n\t"
      "s_branch FQ30B_%=\n\t"
      "FQ31R_%=:\n\t"
      "s_mov_b32 s51, 0\n\t"
      "FQ31L_%=:\n\t"
      "s_add_u32 s51, s51, 1\n\t"
      "s_cmp_lt_u32 s51, 262144\n\t"
      "s_cbranch_scc0 FQ31X_%=\n\t"
      "s_waitcnt vmcnt(0)\n\t"
      "s_sleep 4\n\t"
      "global_load_dwordx4 v[140:143], v227, s[60:61] sc1\n\t"
      "global_load_dwordx4 v[144:147], v228, s[60:61] sc1\n\t"
      "global_load_dwordx4 v[148:151], v229, s[60:61] sc1\n\t"
      "global_load_dwordx4 v[152:155], v230, s[60:61] sc1\n\t"
      "global_load_dwordx4 v[128:131], v224, s[62:63] sc1\n\t"
      "global_load_dwordx4 v[132:135], v225, s[62:63] sc1\n\t"
      "global_load_dwordx4 v[136:139], v226, s[62:63] sc1\n\t"
      "s_waitcnt vmcnt(6)\n\t"
      "v_max3_u32 v234, v140, v141, v142\n\t"
      "v_max_u32_e32 v234, v234, v143\n\t"
      "v_cmp_eq_u32_e32 vcc, -1, v234\n\t"
      "s_cbranch_vccnz FQ31L_%=\n\t"
      "FQ31X_%=:\n\t"
      "s_branch FQ31B_%=\n\t"
      "FQ32R_%=:\n\t"
      "s_mov_b32 s51, 0\n\t"
      "FQ32L_%=:\n\t"
      "s_add_u32 s51, s51, 1\n\t"
      "s_cmp_lt_u32 s51, 262144\n\t"
      "s_cbranch_scc0 FQ32X_%=\n\t"
      "s_waitcnt vmcnt(0)\n\t"
      "s_sleep 4\n\t"
      "global_load_dwordx4 v[144:147], v228, s[60:61] sc1\n\t"
      "global_load_dwordx4 v[148:151], v229, s[60:61] sc1\n\t"
      "global_load_dwordx4 v[152:155], v230, s[60:61] sc1\n\t"
      "global_load_dwordx4 v[128:131], v224, s[62:63] sc1\n\t"
      "global_load_dwordx4 v[132:135], v225, s[62:63] sc1\n\t"
      "global_load_dwordx4 v[136:139], v226, s[62:63] sc1\n\t"
      "global_load_dwordx4 v[140:143], v227, s[62:63] sc1\n\t"
      "s_waitcnt vmcnt(6)\n\t"
      "v_max3_u32 v234, v144, v145, v146\n\t"
      "v_max_u32_e32 v234, v234, v147\n\t"
      "v_cmp_eq_u32_e32 vcc, -1, v234\n\t"
      "s_cbranch_vccnz FQ32L_%=\n\t"
      "FQ32X_%=:\n\t"
      "s_branch FQ32B_%=\n\t"
      "FQ33R_%=:\n\t"
      "s_mov_b32 s51, 0\n\t"
      "FQ33L_%=:\n\t"
      "s_add_u32 s51, s51, 1\n\t"
      "s_cmp_lt_u32 s51, 262144\n\t"
      "s_cbranch_scc0 FQ33X_%=\n\t"
      "s_waitcnt vmcnt(0)\n\t"
      "s_sleep 4\n\t"
      "global_load_dwordx4 v[148:151], v229, s[60:61] sc1\n\t"
      "global_load_dwordx4 v[152:155], v230, s[60:61] sc1\n\t"
      "global_load_dwordx4 v[128:131], v224, s[62:63] sc1\n\t"
      "global_load_dwordx4 v[132:135], v225, s[62:63] sc1\n\t"
      "global_load_dwordx4 v[136:139], v226, s[62:63] sc1\n\t"
      "global_load_dwordx4 v[140:143], v227, s[62:63] sc1\n\t"
      "global_load_dwordx4 v[144:147], v228, s[62:63] sc1\n\t"
      "s_waitcnt vmcnt(6)\n\t"
      "v_max3_u32 v234, v148, v149, v150\n\t"
      "v_max_u32_e32 v234, v234, v151\n\t"
      "v_cmp_eq_u32_e32 vcc, -1, v234\n\t"
      "s_cbranch_vccnz FQ33L_%=\n\t"
      "FQ33X_%=:\n\t"
      "s_branch FQ33B_%=\n\t"
      "FQ34R_%=:\n\t"
      "s_mov_b32 s51, 0\n\t"
      "FQ34L_%=:\n\t"
      "s_add_u32 s51, s51, 1\n\t"
      "s_cmp_lt_u32 s51, 262144\n\t"
      "s_cbranch_scc0 FQ34X_%=\n\t"
      "s_waitcnt vmcnt(0)\n\t"
      "s_sleep 4\n\t"
      "global_load_dwordx4 v[152:155], v230, s[60:61] sc1\n\t"
      "global_load_dwordx4 v[128:131], v224, s[62:63] sc1\n\t"
      "global_load_dwordx4 v[132:135], v225, s[62:63] sc1\n\t"
      "global_load_dwordx4 v[136:139], v226, s[62:63] sc1\n\t"
      "global_load_dwordx4 v[140:143], v227, s[62:63] sc1\n\t"
      "global_load_dwordx4 v[144:147], v228, s[62:63] sc1\n\t"
      "global_load_dwordx4 v[148:151], v229, s[62:63] sc1\n\t"
      "s_waitcnt vmcnt(6)\n\t"
      "v_max3_u32 v234, v152, v153, v154\n\t"
      "v_max_u32_e32 v234, v234, v155\n\t"
      "v_cmp_eq_u32_e32 vcc, -1, v234\n\t"
      "s_cbranch_vccnz FQ34L_%=\n\t"
      "FQ34X_%=:\n\t"
      "s_branch FQ34B_%=\n\t"
      "FQ35R_%=:\n\t"
      "s_mov_b32 s51, 0\n\t"
      "FQ35L_%=:\n\t"
      "s_add_u32 s51, s51, 1\n\t"
      "s_cmp_lt_u32 s51, 262144\n\t"
      "s_cbranch_scc0 FQ35X_%=\n\t"
      "s_waitcnt vmcnt(0)\n\t"
      "s_sleep 4\n\t"
      "global_load_dwordx4 v[128:131], v224, s[62:63] sc1\n\t"
      "global_load_dwordx4 v[132:135], v225, s[62:63] sc1\n\t"
      "global_load_dwordx4 v[136:139], v226, s[62:63] sc1\n\t"
      "global_load_dwordx4 v[140:143], v227, s[62:63] sc1\n\t"
      "global_load_dwordx4 v[144:147], v228, s[62:63] sc1\n\t"
      "global_load_dwordx4 v[148:151], v229, s[62:63] sc1\n\t"
      "global_load_dwordx4 v[152:155], v230, s[62:63] sc1\n\t"
      "s_waitcnt vmcnt(6)\n\t"
      "v_max3_u32 v234, v128, v129, v130\n\t"
      "v_max_u32_e32 v234, v234, v131\n\t"
      "v_cmp_eq_u32_e32 vcc, -1, v234\n\t"
      "s_cbranch_vccnz FQ35L_%=\n\t"
      "FQ35X_%=:\n\t"
      "s_branch FQ35B_%=\n\t"
      "NQ4R_%=:\n\t"
      "s_mov_b32 s51, 0\n\t"
      "NQ4L_%=:\n\t"
      "s_add_u32 s51, s51, 1\n\t"
      "s_cmp_lt_u32 s51, 262144\n\t"
      "s_cbranch_scc0 NQ4X_%=\n\t"
      "s_waitcnt vmcnt(0)\n\t"
      "s_sleep 1\n\t"
      "global_load_dwordx4 v[128:131], v224, s[60:61] sc1\n\t"
      "global_load_dwordx4 v[132:135], v225, s[60:61] sc1\n\t"
      "global_load_dwordx4 v[136:139], v226, s[60:61] sc1\n\t"
      "global_load_dwordx4 v[140:143], v227, s[60:61] sc1\n\t"
      "global_load_dwordx4 v[144:147], v228, s[60:61] sc1\n\t"
      "global_load_dwordx4 v[148:151], v229, s[60:61] sc1\n\t"
      "global_load_dwordx4 v[152:155], v230, s[60:61] sc1\n\t"
      "s_waitcnt vmcnt(0)\n\t"
      "v_max3_u32 v234, v128, v129, v130\n\t"
      "v_max3_u32 v234, v234, v131, v132\n\t"
      "v_max3_u32 v234, v234, v133, v134\n\t"
      "v_max3_u32 v234, v234, v135, v136\n\t"
      "v_max3_u32 v234, v234, v137, v138\n\t"
      "v_max3_u32 v234, v234, v139, v140\n\t"
      "v_max3_u32 v234, v234, v141, v142\n\t"
      "v_max3_u32 v234, v234, v143, v144\n\t"
      "v_max3_u32 v234, v234, v145, v146\n\t"
      "v_max3_u32 v234, v234, v147, v148\n\t"
      "v_max3_u32 v234, v234, v149, v150\n\t"
      "v_max3_u32 v234, v234, v151, v152\n\t"
      "v_max3_u32 v234, v234, v153, v154\n\t"
      "v_max_u32_e32 v234, v234, v155\n\t"
      "v_cmp_eq_u32_e32 vcc, -1, v234\n\t"
      "s_cbranch_vccnz NQ4L_%=\n\t"
      "NQ4X_%=:\n\t"
      "v_mfma_f32_16x16x32_bf16 v[184:187], v[128:131], v[0:3], v[172:175]\n\t"
      "v_mfma_f32_16x16x32_bf16 v[188:191], v[128:131], v[4:7], 0\n\t"
      "v_mfma_f32_16x16x32_bf16 v[192:195], v[128:131], v[8:11], 0\n\t"
      "v_mfma_f32_16x16x32_bf16 v[196:199], v[128:131], v[12:15], 0\n\t"
      "v_mfma_f32_16x16x32_bf16 v[184:187], v[132:135], v[16:19], v[184:187]\n\t"
      "v_mfma_f32_16x16x32_bf16 v[188:191], v[132:135], v[20:23], v[188:191]\n\t"
      "v_mfma_f32_16x16x32_bf16 v[192:195], v[132:135], v[24:27], v[192:195]\n\t"
      "v_mfma_f32_16x16x32_bf16 v[196:199], v[132:135], v[28:31], v[196:199]\n\t"
      "v_mfma_f32_16x16x32_bf16 v[184:187], v[136:139], v[32:35], v[184:187]\n\t"
      "v_mfma_f32_16x16x32_bf16 v[188:191], v[136:139], v[36:39], v[188:191]\n\t"
      "v_mfma_f32_16x16x32_bf16 v[192:195], v[136:139], v[40:43], v[192:195]\n\t"
      "v_mfma_f32_16x16x32_bf16 v[196:199], v[136:139], v[44:47], v[196:199]\n\t"
      "v_mfma_f32_16x16x32_bf16 v[184:187], v[140:143], v[48:51], v[184:187]\n\t"
      "v_mfma_f32_16x16x32_bf16 v[188:191], v[140:143], v[52:55], v[188:191]\n\t"
      "v_mfma_f32_16x16x32_bf16 v[192:195], v[140:143], v[56:59], v[192:195]\n\t"
      "v_mfma_f32_16x16x32_bf16 v[196:199], v[140:143], v[60:63], v[196:199]\n\t"
      "v_mfma_f32_16x16x32_bf16 v[184:187], v[144:147], v[64:67], v[184:187]\n\t"
      "v_mfma_f32_16x16x32_bf16 v[188:191], v[144:147], v[68:71], v[188:191]\n\t"
      "v_mfma_f32_16x16x32_bf16 v[192:195], v[144:147], v[72:75], v[192:195]\n\t"
      "v_mfma_f32_16x16x32_bf16 v[196:199], v[144:147], v[76:79], v[196:199]\n\t"
      "v_mfma_f32_16x16x32_bf16 v[184:187], v[148:151], v[80:83], v[184:187]\n\t"
      "v_mfma_f32_16x16x32_bf16 v[188:191], v[148:151], v[84:87], v[188:191]\n\t"
      "v_mfma_f32_16x16x32_bf16 v[192:195], v[148:151], v[88:91], v[192:195]\n\t"
      "v_mfma_f32_16x16x32_bf16 v[196:199], v[148:151], v[92:95], v[196:199]\n\t"
      "v_mfma_f32_16x16x32_bf16 v[184:187], v[152:155], v[96:99], v[184:187]\n\t"
      "v_mfma_f32_16x16x32_bf16 v[188:191], v[152:155], v[100:103], v[188:191]\n\t"
      "v_mfma_f32_16x16x32_bf16 v[192:195], v[152:155], v[104:107], v[192:195]\n\t"
      "v_mfma_f32_16x16x32_bf16 v[196:199], v[152:155], v[108:111], v[196:199]\n\t"
      "global_load_dwordx4 v[132:135], v225, s[62:63] sc1\n\t"
      "global_load_dwordx4 v[136:139], v226, s[62:63] sc1\n\t"
      "global_load_dwordx4 v[140:143], v227, s[62:63] sc1\n\t"
      "global_load_dwordx4 v[144:147], v228, s[62:63] sc1\n\t"
      "global_load_dwordx4 v[148:151], v229, s[62:63] sc1\n\t"
      "global_load_dwordx4 v[152:155], v230, s[62:63] sc1\n\t"
      "global_load_dwordx4 v[128:131], v224, s[64:65] sc1\n\t"
      "s_waitcnt vmcnt(0)\n\t"
      "s_nop 7\n\t"
      "s_branch NQ4B_%=\n\t"
      "FQ36R_%=:\n\t"
      "s_mov_b32 s51, 0\n\t"
      "FQ36L_%=:\n\t"
      "s_add_u32 s51, s51, 1\n\t"
      "s_cmp_lt_u32 s51, 262144\n\t"
      "s_cbranch_scc0 FQ36X_%=\n\t"
      "s_waitcnt vmcnt(0)\n\t"
      "s_sleep 4\n\t"
      "global_load_dwordx4 v[132:135], v225, s[62:63] sc1\n\t"
      "global_load_dwordx4 v[136:139], v226, s[62:63] sc1\n\t"
      "global_load_dwordx4 v[140:143], v227, s[62:63] sc1\n\t"
      "global_load_dwordx4 v[144:147], v228, s[62:63] sc1\n\t"
      "global_load_dwordx4 v[148:151], v229, s[62:63] sc1\n\t"
      "global_load_dwordx4 v[152:155], v230, s[62:63] sc1\n\t"
      "global_load_dwordx4 v[128:131], v224, s[64:65] sc1\n\t"
      "s_waitcnt vmcnt(6)\n\t"
      "v_max3_u32 v234, v132, v133, v134\n\t"
      "v_max_u32_e32 v234, v234, v135\n\t"
      "v_cmp_eq_u32_e32 vcc, -1, v234\n\t"
      "s_cbranch_vccnz FQ36L_%=\n\t"
      "FQ36X_%=:\n\t"
      "s_branch FQ36B_%=\n\t"
      "FQ37R_%=:\n\t"
      "s_mov_b32 s51, 0\n\t"
      "FQ37L_%=:\n\t"
      "s_add_u32 s51, s51, 1\n\t"
      "s_cmp_lt_u32 s51, 262144\n\t"
      "s_cbranch_scc0 FQ37X_%=\n\t"
      "s_waitcnt vmcnt(0)\n\t"
      "s_sleep 4\n\t"
      "global_load_dwordx4 v[136:139], v226, s[62:63] sc1\n\t"
      "global_load_dwordx4 v[140:143], v227, s[62:63] sc1\n\t"
      "global_load_dwordx4 v[144:147], v228, s[62:63] sc1\n\t"
      "global_load_dwordx4 v[148:151], v229, s[62:63] sc1\n\t"
      "global_load_dwordx4 v[152:155], v230, s[62:63] sc1\n\t"
      "global_load_dwordx4 v[128:131], v224, s[64:65] sc1\n\t"
      "global_load_dwordx4 v[132:135], v225, s[64:65] sc1\n\t"
      "s_waitcnt vmcnt(6)\n\t"
      "v_max3_u32 v234, v136, v137, v138\n\t"
      "v_max_u32_e32 v234, v234, v139\n\t"
      "v_cmp_eq_u32_e32 vcc, -1, v234\n\t"
      "s_cbranch_vccnz FQ37L_%=\n\t"
      "FQ37X_%=:\n\t"
      "s_branch FQ37B_%=\n\t"
      "FQ38R_%=:\n\t"
      "s_mov_b32 s51, 0\n\t"
      "FQ38L_%=:\n\t"
      "s_add_u32 s51, s51, 1\n\t"
      "s_cmp_lt_u32 s51, 262144\n\t"
      "s_cbranch_scc0 FQ38X_%=\n\t"
      "s_waitcnt vmcnt(0)\n\t"
      "s_sleep 4\n\t"
      "global_load_dwordx4 v[140:143], v227, s[62:63] sc1\n\t"
      "global_load_dwordx4 v[144:147], v228, s[62:63] sc1\n\t"
      "global_load_dwordx4 v[148:151], v229, s[62:63] sc1\n\t"
      "global_load_dwordx4 v[152:155], v230, s[62:63] sc1\n\t"
      "global_load_dwordx4 v[128:131], v224, s[64:65] sc1\n\t"
      "global_load_dwordx4 v[132:135], v225, s[64:65] sc1\n\t"
      "global_load_dwordx4 v[136:139], v226, s[64:65] sc1\n\t"
      "s_waitcnt vmcnt(6)\n\t"
      "v_max3_u32 v234, v140, v141, v142\n\t"
      "v_max_u32_e32 v234, v234, v143\n\t"
      "v_cmp_eq_u32_e32 vcc, -1, v234\n\t"
      "s_cbranch_vccnz FQ38L_%=\n\t"
      "FQ38X_%=:\n\t"
      "s_branch FQ38B_%=\n\t"
      "FQ39R_%=:\n\t"
      "s_mov_b32 s51, 0\n\t"
      "FQ39L_%=:\n\t"
      "s_add_u32 s51, s51, 1\n\t"
      "s_cmp_lt_u32 s51, 262144\n\t"
      "s_cbranch_scc0 FQ39X_%=\n\t"
      "s_waitcnt vmcnt(0)\n\t"
      "s_sleep 4\n\t"
      "global_load_dwordx4 v[144:147], v228, s[62:63] sc1\n\t"
      "global_load_dwordx4 v[148:151], v229, s[62:63] sc1\n\t"
      "global_load_dwordx4 v[152:155], v230, s[62:63] sc1\n\t"
      "global_load_dwordx4 v[128:131], v224, s[64:65] sc1\n\t"
      "global_load_dwordx4 v[132:135], v225, s[64:65] sc1\n\t"
      "global_load_dwordx4 v[136:139], v226, s[64:65] sc1\n\t"
      "global_load_dwordx4 v[140:143], v227, s[64:65] sc1\n\t"
      "s_waitcnt vmcnt(6)\n\t"
      "v_max3_u32 v234, v144, v145, v146\n\t"
      "v_max_u32_e32 v234, v234, v147\n\t"
      "v_cmp_eq_u32_e32 vcc, -1, v234\n\t"
      "s_cbranch_vccnz FQ39L_%=\n\t"
      "FQ39X_%=:\n\t"
      "s_branch FQ39B_%=\n\t"
      "FQ40R_%=:\n\t"
      "s_mov_b32 s51, 0\n\t"
      "FQ40L_%=:\n\t"
      "s_add_u32 s51, s51, 1\n\t"
      "s_cmp_lt_u32 s51, 262144\n\t"
      "s_cbranch_scc0 FQ40X_%=\n\t"
      "s_waitcnt vmcnt(0)\n\t"
      "s_sleep 4\n\t"
      "global_load_dwordx4 v[148:151], v229, s[62:63] sc1\n\t"
      "global_load_dwordx4 v[152:155], v230, s[62:63] sc1\n\t"
      "global_load_dwordx4 v[128:131], v224, s[64:65] sc1\n\t"
      "global_load_dwordx4 v[132:135], v225, s[64:65] sc1\n\t"
      "global_load_dwordx4 v[136:139], v226, s[64:65] sc1\n\t"
      "global_load_dwordx4 v[140:143], v227, s[64:65] sc1\n\t"
      "global_load_dwordx4 v[144:147], v228, s[64:65] sc1\n\t"
      "s_waitcnt vmcnt(6)\n\t"
      "v_max3_u32 v234, v148, v149, v150\n\t"
      "v_max_u32_e32 v234, v234, v151\n\t"
      "v_cmp_eq_u32_e32 vcc, -1, v234\n\t"
      "s_cbranch_vccnz FQ40L_%=\n\t"
      "FQ40X_%=:\n\t"
      "s_branch FQ40B_%=\n\t"
      "FQ41R_%=:\n\t"
      "s_mov_b32 s51, 0\n\t"
      "FQ41L_%=:\n\t"
      "s_add_u32 s51, s51, 1\n\t"
      "s_cmp_lt_u32 s51, 262144\n\t"
      "s_cbranch_scc0 FQ41X_%=\n\t"
      "s_waitcnt vmcnt(0)\n\t"
      "s_sleep 4\n\t"
      "global_load_dwordx4 v[152:155], v230, s[62:63] sc1\n\t"
      "global_load_dwordx4 v[128:131], v224, s[64:65] sc1\n\t"
      "global_load_dwordx4 v[132:135], v225, s[64:65] sc1\n\t"
      "global_load_dwordx4 v[136:139], v226, s[64:65] sc1\n\t"
      "global_load_dwordx4 v[140:143], v227, s[64:65] sc1\n\t"
      "global_load_dwordx4 v[144:147], v228, s[64:65] sc1\n\t"
      "global_load_dwordx4 v[148:151], v229, s[64:65] sc1\n\t"
      "s_waitcnt vmcnt(6)\n\t"
      "v_max3_u32 v234, v152, v153, v154\n\t"
      "v_max_u32_e32 v234, v234, v155\n\t"
      "v_cmp_eq_u32_e32 vcc, -1, v234\n\t"
      "s_cbranch_vccnz FQ41L_%=\n\t"
      "FQ41X_%=:\n\t"
      "s_branch FQ41B_%=\n\t"
      "FQ42R_%=:\n\t"
      "s_mov_b32 s51, 0\n\t"
      "FQ42L_%=:\n\t"
      "s_add_u32 s51, s51, 1\n\t"
      "s_cmp_lt_u32 s51, 262144\n\t"
      "s_cbranch_scc0 FQ42X_%=\n\t"
      "s_sleep 1\n\t"
      "s_mov_b64 exec, 15\n\t"
      "v_mov_b64_e32 v[232:233], 0\n\t"
      "global_atomic_or_x2 v[232:233], v247, v[232:233], s[12:13] sc0 sc1\n\t"
      "s_waitcnt vmcnt(0)\n\t"
      "v_subrev_u32_e32 v232, s14, v232\n\t"
      "v_cmp_ne_u32_e64 s[48:49], s15, v233\n\t"
      "v_subrev_u32_e32 v232, 6, v232\n\t"
      "v_cmp_lt_u32_e32 vcc, 7, v232\n\t"
      "s_mov_b64 exec, -1\n\t"
      "s_or_b64 vcc, vcc, s[48:49]\n\t"
      "s_cbranch_vccnz FQ42L_%=\n\t"
      "FQ42X_%=:\n\t"
      "FQ42DR_%=:\n\t"
      "s_mov_b32 s51, 0\n\t"
      "FQ42DL_%=:\n\t"
      "s_add_u32 s51, s51, 1\n\t"
      "s_cmp_lt_u32 s51, 262144\n\t"
      "s_cbranch_scc0 FQ42DX_%=\n\t"
      "s_waitcnt vmcnt(0)\n\t"
      "s_sleep 1\n\t"
      "global_load_dwordx4 v[128:131], v224, s[64:65] sc1\n\t"
      "global_load_dwordx4 v[132:135], v225, s[64:65] sc1\n\t"
      "global_load_dwordx4 v[136:139], v226, s[64:65] sc1\n\t"
      "global_load_dwordx4 v[140:143], v227, s[64:65] sc1\n\t"
      "global_load_dwordx4 v[144:147], v228, s[64:65] sc1\n\t"
      "global_load_dwordx4 v[148:151], v229, s[64:65] sc1\n\t"
      "global_load_dwordx4 v[152:155], v230, s[64:65] sc1\n\t"
      "s_waitcnt vmcnt(6)\n\t"
      "v_max3_u32 v234, v128, v129, v130\n\t"
      "v_max_u32_e32 v234, v234, v131\n\t"
      "v_cmp_eq_u32_e32 vcc, -1, v234\n\t"
      "s_cbranch_vccnz FQ42DL_%=\n\t"
      "FQ42DX_%=:\n\t"
      "s_branch FQ42B_%=\n\t"
      "NQ5R_%=:\n\t"
      "s_mov_b32 s51, 0\n\t"
      "NQ5L_%=:\n\t"
      "s_add_u32 s51, s51, 1\n\t"
      "s_cmp_lt_u32 s51, 262144\n\t"
      "s_cbranch_scc0 NQ5X_%=\n\t"
      "s_waitcnt vmcnt(0)\n\t"
      "s_sleep 1\n\t"
      "global_load_dwordx4 v[128:131], v224, s[62:63] sc1\n\t"
      "global_load_dwordx4 v[132:135], v225, s[62:63] sc1\n\t"
      "global_load_dwordx4 v[136:139], v226, s[62:63] sc1\n\t"
      "global_load_dwordx4 v[140:143], v227, s[62:63] sc1\n\t"
      "global_load_dwordx4 v[144:147], v228, s[62:63] sc1\n\t"
      "global_load_dwordx4 v[148:151], v229, s[62:63] sc1\n\t"
      "global_load_dwordx4 v[152:155], v230, s[62:63] sc1\n\t"
      "s_waitcnt vmcnt(0)\n\t"
      "v_max3_u32 v234, v128, v129, v130\n\t"
      "v_max3_u32 v234, v234, v131, v132\n\t"
      "v_max3_u32 v234, v234, v133, v134\n\t"
      "v_max3_u32 v234, v234, v135, v136\n\t"
      "v_max3_u32 v234, v234, v137, v138\n\t"
      "v_max3_u32 v234, v234, v139, v140\n\t"
      "v_max3_u32 v234, v234, v141, v142\n\t"
      "v_max3_u32 v234, v234, v143, v144\n\t"
      "v_max3_u32 v234, v234, v145, v146\n\t"
      "v_max3_u32 v234, v234, v147, v148\n\t"
      "v_max3_u32 v234, v234, v149, v150\n\t"
      "v_max3_u32 v234, v234, v151, v152\n\t"
      "v_max3_u32 v234, v234, v153, v154\n\t"
      "v_max_u32_e32 v234, v234, v155\n\t"
      "v_cmp_eq_u32_e32 vcc, -1, v234\n\t"
      "s_cbranch_vccnz NQ5L_%=\n\t"
      "NQ5X_%=:\n\t"
      "v_mfma_f32_16x16x32_bf16 v[200:203], v[128:131], v[0:3], v[176:179]\n\t"
      "v_mfma_f32_16x16x32_bf16 v[204:207], v[128:131], v[4:7], 0\n\t"
      "v_mfma_f32_16x16x32_bf16 v[208:211], v[128:131], v[8:11], 0\n\t"
      "v_mfma_f32_16x16x32_bf16 v[212:215], v[128:131], v[12:15], 0\n\t"
      "v_mfma_f32_16x16x32_bf16 v[200:203], v[132:135], v[16:19], v[200:203]\n\t"
      "v_mfma_f32_16x16x32_bf16 v[204:207], v[132:135], v[20:23], v[204:207]\n\t"
      "v_mfma_f32_16x16x32_bf16 v[208:211], v[132:135], v[24:27], v[208:211]\n\t"
      "v_mfma_f32_16x16x32_bf16 v[212:215], v[132:135], v[28:31], v[212:215]\n\t"
      "v_mfma_f32_16x16x32_bf16 v[200:203], v[136:139], v[32:35], v[200:203]\n\t"
      "v_mfma_f32_16x16x32_bf16 v[204:207], v[136:139], v[36:39], v[204:207]\n\t"
      "v_mfma_f32_16x16x32_bf16 v[208:211], v[136:139], v[40:43], v[208:211]\n\t"
      "v_mfma_f32_16x16x32_bf16 v[212:215], v[136:139], v[44:47], v[212:215]\n\t"
      "v_mfma_f32_16x16x32_bf16 v[200:203], v[140:143], v[48:51], v[200:203]\n\t"
      "v_mfma_f32_16x16x32_bf16 v[204:207], v[140:143], v[52:55], v[204:207]\n\t"
      "v_mfma_f32_16x16x32_bf16 v[208:211], v[140:143], v[56:59], v[208:211]\n\t"
      "v_mfma_f32_16x16x32_bf16 v[212:215], v[140:143], v[60:63], v[212:215]\n\t"
      "v_mfma_f32_16x16x32_bf16 v[200:203], v[144:147], v[64:67], v[200:203]\n\t"
      "v_mfma_f32_16x16x32_bf16 v[204:207], v[144:147], v[68:71], v[204:207]\n\t"
      "v_mfma_f32_16x16x32_bf16 v[208:211], v[144:147], v[72:75], v[208:211]\n\t"
      "v_mfma_f32_16x16x32_bf16 v[212:215], v[144:147], v[76:79], v[212:215]\n\t"
      "v_mfma_f32_16x16x32_bf16 v[200:203], v[148:151], v[80:83], v[200:203]\n\t"
      "v_mfma_f32_16x16x32_bf16 v[204:207], v[148:151], v[84:87], v[204:207]\n\t"
      "v_mfma_f32_16x16x32_bf16 v[208:211], v[148:151], v[88:91], v[208:211]\n\t"
      "v_mfma_f32_16x16x32_bf16 v[212:215], v[148:151], v[92:95], v[212:215]\n\t"
      "v_mfma_f32_16x16x32_bf16 v[200:203], v[152:155], v[96:99], v[200:203]\n\t"
      "v_mfma_f32_16x16x32_bf16 v[204:207], v[152:155], v[100:103], v[204:207]\n\t"
      "v_mfma_f32_16x16x32_bf16 v[208:211], v[152:155], v[104:107], v[208:211]\n\t"
      "v_mfma_f32_16x16x32_bf16 v[212:215], v[152:155], v[108:111], v[212:215]\n\t"
      "global_load_dwordx4 v[132:135], v225, s[64:65] sc1\n\t"
      "global_load_dwordx4 v[136:139], v226, s[64:65] sc1\n\t"
      "global_load_dwordx4 v[140:143], v227, s[64:65] sc1\n\t"
      "global_load_dwordx4 v[144:147], v228, s[64:65] sc1\n\t"
      "global_load_dwordx4 v[148:151], v229, s[64:65] sc1\n\t"
      "global_load_dwordx4 v[152:155], v230, s[64:65] sc1\n\t"
      "s_waitcnt vmcnt(0)\n\t"
      "s_nop 7\n\t"
      "s_branch NQ5B_%=\n\t"
      "FQ43R_%=:\n\t"
      "s_mov_b32 s51, 0\n\t"
      "FQ43L_%=:\n\t"
      "s_add_u32 s51, s51, 1\n\t"
      "s_cmp_lt_u32 s51, 262144\n\t"
      "s_cbranch_scc0 FQ43X_%=\n\t"
      "s_sleep 1\n\t"
      "s_mov_b64 exec, 15\n\t"
      "v_mov_b64_e32 v[232:233], 0\n\t"
      "global_atomic_or_x2 v[232:233], v247, v[232:233], s[12:13] sc0 sc1\n\t"
      "s_waitcnt vmcnt(0)\n\t"
      "v_subrev_u32_e32 v232, s14, v232\n\t"
      "v_cmp_ne_u32_e64 s[48:49], s15, v233\n\t"
      "v_subrev_u32_e32 v232, 6, v232\n\t"
      "v_cmp_lt_u32_e32 vcc, 7, v232\n\t"
      "s_mov_b64 exec, -1\n\t"
      "s_or_b64 vcc, vcc, s[48:49]\n\t"
      "s_cbranch_vccnz FQ43L_%=\n\t"
      "FQ43X_%=:\n\t"
      "FQ43DR_%=:\n\t"
      "s_mov_b32 s51, 0\n\t"
      "FQ43DL_%=:\n\t"
      "s_add_u32 s51, s51, 1\n\t"
      "s_cmp_lt_u32 s51, 262144\n\t"
      "s_cbranch_scc0 FQ43DX_%=\n\t"
      "s_waitcnt vmcnt(0)\n\t"
      "s_sleep 1\n\t"
      "global_load_dwordx4 v[132:135], v225, s[64:65] sc1\n\t"
      "global_load_dwordx4 v[136:139], v226, s[64:65] sc1\n\t"
      "global_load_dwordx4 v[140:143], v227, s[64:65] sc1\n\t"
      "global_load_dwordx4 v[144:147], v228, s[64:65] sc1\n\t"
      "global_load_dwordx4 v[148:151], v229, s[64:65] sc1\n\t"
      "global_load_dwordx4 v[152:155], v230, s[64:65] sc1\n\t"
      "s_waitcnt vmcnt(5)\n\t"
      "v_max3_u32 v234, v132, v133, v134\n\t"
      "v_max_u32_e32 v234, v234, v135\n\t"
      "v_cmp_eq_u32_e32 vcc, -1, v234\n\t"
      "s_cbranch_vccnz FQ43DL_%=\n\t"
      "FQ43DX_%=:\n\t"
      "s_branch FQ43B_%=\n\t"
      "FQ44R_%=:\n\t"
      "s_mov_b32 s51, 0\n\t"
      "FQ44L_%=:\n\t"
      "s_add_u32 s51, s51, 1\n\t"
      "s_cmp_lt_u32 s51, 262144\n\t"
      "s_cbranch_scc0 FQ44X_%=\n\t"
      "s_sleep 1\n\t"
      "s_mov_b64 exec, 15\n\t"
      "v_mov_b64_e32 v[232:233], 0\n\t"
      "global_atomic_or_x2 v[232:233], v247, v[232:233], s[12:13] sc0 sc1\n\t"
      "s_waitcnt vmcnt(0)\n\t"
      "v_subrev_u32_e32 v232, s14, v232\n\t"
      "v_cmp_ne_u32_e64 s[48:49], s15, v233\n\t"
      "v_subrev_u32_e32 v232, 6, v232\n\t"
      "v_cmp_lt_u32_e32 vcc, 7, v232\n\t"
      "s_mov_b64 exec, -1\n\t"
      "s_or_b64 vcc, vcc, s[48:49]\n\t"
      "s_cbranch_vccnz FQ44L_%=\n\t"
      "FQ44X_%=:\n\t"
      "FQ44DR_%=:\n\t"
      "s_mov_b32 s51, 0\n\t"
      "FQ44DL_%=:\n\t"
      "s_add_u32 s51, s51, 1\n\t"
      "s_cmp_lt_u32 s51, 262144\n\t"
      "s_cbranch_scc0 FQ44DX_%=\n\t"
      "s_waitcnt vmcnt(0)\n\t"
      "s_sleep 1\n\t"
      "global_load_dwordx4 v[136:139], v226, s[64:65] sc1\n\t"
      "global_load_dwordx4 v[140:143], v227, s[64:65] sc1\n\t"
      "global_load_dwordx4 v[144:147], v228, s[64:65] sc1\n\t"
      "global_load_dwordx4 v[148:151], v229, s[64:65] sc1\n\t"
      "global_load_dwordx4 v[152:155], v230, s[64:65] sc1\n\t"
      "s_waitcnt vmcnt(4)\n\t"
      "v_max3_u32 v234, v136, v137, v138\n\t"
      "v_max_u32_e32 v234, v234, v139\n\t"
      "v_cmp_eq_u32_e32 vcc, -1, v234\n\t"
      "s_cbranch_vccnz FQ44DL_%=\n\t"
      "FQ44DX_%=:\n\t"
      "s_branch FQ44B_%=\n\t"
      "FQ45R_%=:\n\t"
      "s_mov_b32 s51, 0\n\t"
      "FQ45L_%=:\n\t"
      "s_add_u32 s51, s51, 1\n\t"
      "s_cmp_lt_u32 s51, 262144\n\t"
      "s_cbranch_scc0 FQ45X_%=\n\t"
      "s_sleep 1\n\t"
      "s_mov_b64 exec, 15\n\t"
      "v_mov_b64_e32 v[232:233], 0\n\t"
      "global_atomic_or_x2 v[232:233], v247, v[232:233], s[12:13] sc0 sc1\n\t"
      "s_waitcnt vmcnt(0)\n\t"
      "v_subrev_u32_e32 v232, s14, v232\n\t"
      "v_cmp_ne_u32_e64 s[48:49], s15, v233\n\t"
      "v_subrev_u32_e32 v232, 6, v232\n\t"
      "v_cmp_lt_u32_e32 vcc, 7, v232\n\t"
      "s_mov_b64 exec, -1\n\t"
      "s_or_b64 vcc, vcc, s[48:49]\n\t"
      "s_cbranch_vccnz FQ45L_%=\n\t"
      "FQ45X_%=:\n\t"
      "FQ45DR_%=:\n\t"
      "s_mov_b32 s51, 0\n\t"
      "FQ45DL_%=:\n\t"
      "s_add_u32 s51, s51, 1\n\t"
      "s_cmp_lt_u32 s51, 262144\n\t"
      "s_cbranch_scc0 FQ45DX_%=\n\t"
      "s_waitcnt vmcnt(0)\n\t"
      "s_sleep 1\n\t"
      "global_load_dwordx4 v[140:143], v227, s[64:65] sc1\n\t"
      "global_load_dwordx4 v[144:147], v228, s[64:65] sc1\n\t"
      "global_load_dwordx4 v[148:151], v229, s[64:65] sc1\n\t"
      "global_load_dwordx4 v[152:155], v230, s[64:65] sc1\n\t"
      "s_waitcnt vmcnt(3)\n\t"
      "v_max3_u32 v234, v140, v141, v142\n\t"
      "v_max_u32_e32 v234, v234, v143\n\t"
      "v_cmp_eq_u32_e32 vcc, -1, v234\n\t"
      "s_cbranch_vccnz FQ45DL_%=\n\t"
      "FQ45DX_%=:\n\t"
      "s_branch FQ45B_%=\n\t"
      "FQ46R_%=:\n\t"
      "s_mov_b32 s51, 0\n\t"
      "FQ46L_%=:\n\t"
      "s_add_u32 s51, s51, 1\n\t"
      "s_cmp_lt_u32 s51, 262144\n\t"
      "s_cbranch_scc0 FQ46X_%=\n\t"
      "s_sleep 1\n\t"
      "s_mov_b64 exec, 15\n\t"
      "v_mov_b64_e32 v[232:233], 0\n\t"
      "global_atomic_or_x2 v[232:233], v247, v[232:233], s[12:13] sc0 sc1\n\t"
      "s_waitcnt vmcnt(0)\n\t"
      "v_subrev_u32_e32 v232, s14, v232\n\t"
      "v_cmp_ne_u32_e64 s[48:49], s15, v233\n\t"
      "v_subrev_u32_e32 v232, 6, v232\n\t"
      "v_cmp_lt_u32_e32 vcc, 7, v232\n\t"
      "s_mov_b64 exec, -1\n\t"
      "s_or_b64 vcc, vcc, s[48:49]\n\t"
      "s_cbranch_vccnz FQ46L_%=\n\t"
      "FQ46X_%=:\n\t"
      "FQ46DR_%=:\n\t"
      "s_mov_b32 s51, 0\n\t"
      "FQ46DL_%=:\n\t"
      "s_add_u32 s51, s51, 1\n\t"
      "s_cmp_lt_u32 s51, 262144\n\t"
      "s_cbranch_scc0 FQ46DX_%=\n\t"
      "s_waitcnt vmcnt(0)\n\t"
      "s_sleep 1\n\t"
      "global_load_dwordx4 v[144:147], v228, s[64:65] sc1\n\t"
      "global_load_dwordx4 v[148:151], v229, s[64:65] sc1\n\t"
      "global_load_dwordx4 v[152:155], v230, s[64:65] sc1\n\t"
      "s_waitcnt vmcnt(2)\n\t"
      "v_max3_u32 v234, v144, v145, v146\n\t"
      "v_max_u32_e32 v234, v234, v147\n\t"
      "v_cmp_eq_u32_e32 vcc, -1, v234\n\t"
      "s_cbranch_vccnz FQ46DL_%=\n\t"
      "FQ46DX_%=:\n\t"
      "s_branch FQ46B_%=\n\t"
      "FQ47R_%=:\n\t"
      "s_mov_b32 s51, 0\n\t"
      "FQ47L_%=:\n\t"
      "s_add_u32 s51, s51, 1\n\t"
      "s_cmp_lt_u32 s51, 262144\n\t"
      "s_cbranch_scc0 FQ47X_%=\n\t"
      "s_sleep 1\n\t"
      "s_mov_b64 exec, 15\n\t"
      "v_mov_b64_e32 v[232:233], 0\n\t"
      "global_atomic_or_x2 v[232:233], v247, v[232:233], s[12:13] sc0 sc1\n\t"
      "s_waitcnt vmcnt(0)\n\t"
      "v_subrev_u32_e32 v232, s14, v232\n\t"
      "v_cmp_ne_u32_e64 s[48:49], s15, v233\n\t"
      "v_subrev_u32_e32 v232, 6, v232\n\t"
      "v_cmp_lt_u32_e32 vcc, 7, v232\n\t"
      "s_mov_b64 exec, -1\n\t"
      "s_or_b64 vcc, vcc, s[48:49]\n\t"
      "s_cbranch_vccnz FQ47L_%=\n\t"
      "FQ47X_%=:\n\t"
      "FQ47DR_%=:\n\t"
      "s_mov_b32 s51, 0\n\t"
      "FQ47DL_%=:\n\t"
      "s_add_u32 s51, s51, 1\n\t"
      "s_cmp_lt_u32 s51, 262144\n\t"
      "s_cbranch_scc0 FQ47DX_%=\n\t"
      "s_waitcnt vmcnt(0)\n\t"
      "s_sleep 1\n\t"
      "global_load_dwordx4 v[148:151], v229, s[64:65] sc1\n\t"
      "global_load_dwordx4 v[152:155], v230, s[64:65] sc1\n\t"
      "s_waitcnt vmcnt(1)\n\t"
      "v_max3_u32 v234, v148, v149, v150\n\t"
      "v_max_u32_e32 v234, v234, v151\n\t"
      "v_cmp_eq_u32_e32 vcc, -1, v234\n\t"
      "s_cbranch_vccnz FQ47DL_%=\n\t"
      "FQ47DX_%=:\n\t"
      "s_branch FQ47B_%=\n\t"
      "FQ48R_%=:\n\t"
      "s_mov_b32 s51, 0\n\t"
      "FQ48L_%=:\n\t"
      "s_add_u32 s51, s51, 1\n\t"
      "s_cmp_lt_u32 s51, 262144\n\t"
      "s_cbranch_scc0 FQ48X_%=\n\t"
      "s_sleep 1\n\t"
      "s_mov_b64 exec, 15\n\t"
      "v_mov_b64_e32 v[232:233], 0\n\t"
      "global_atomic_or_x2 v[232:233], v247, v[232:233], s[12:13] sc0 sc1\n\t"
      "s_waitcnt vmcnt(0)\n\t"
      "v_subrev_u32_e32 v232, s14, v232\n\t"
      "v_cmp_ne_u32_e64 s[48:49], s15, v233\n\t"
      "v_subrev_u32_e32 v232, 6, v232\n\t"
      "v_cmp_lt_u32_e32 vcc, 7, v232\n\t"
      "s_mov_b64 exec, -1\n\t"
      "s_or_b64 vcc, vcc, s[48:49]\n\t"
      "s_cbranch_vccnz FQ48L_%=\n\t"
      "FQ48X_%=:\n\t"
      "FQ48DR_%=:\n\t"
      "s_mov_b32 s51, 0\n\t"
      "FQ48DL_%=:\n\t"
      "s_add_u32 s51, s51, 1\n\t"
      "s_cmp_lt_u32 s51, 262144\n\t"
      "s_cbranch_scc0 FQ48DX_%=\n\t"
      "s_waitcnt vmcnt(0)\n\t"
      "s_sleep 1\n\t"
      "global_load_dwordx4 v[152:155], v230, s[64:65] sc1\n\t"
      "s_waitcnt vmcnt(0)\n\t"
      "v_max3_u32 v234, v152, v153, v154\n\t"
      "v_max_u32_e32 v234, v234, v155\n\t"
      "v_cmp_eq_u32_e32 vcc, -1, v234\n\t"
      "s_cbranch_vccnz FQ48DL_%=\n\t"
      "FQ48DX_%=:\n\t"
      "s_branch FQ48B_%=\n\t"
      "NQ6R_%=:\n\t"
      "s_mov_b32 s51, 0\n\t"
      "NQ6L_%=:\n\t"
      "s_add_u32 s51, s51, 1\n\t"
      "s_cmp_lt_u32 s51, 262144\n\t"
      "s_cbranch_scc0 NQ6X_%=\n\t"
      "s_waitcnt vmcnt(0)\n\t"
      "s_sleep 1\n\t"
      "global_load_dwordx4 v[128:131], v224, s[64:65] sc1\n\t"
      "global_load_dwordx4 v[132:135], v225, s[64:65] sc1\n\t"
      "global_load_dwordx4 v[136:139], v226, s[64:65] sc1\n\t"
      "global_load_dwordx4 v[140:143], v227, s[64:65] sc1\n\t"
      "global_load_dwordx4 v[144:147], v228, s[64:65] sc1\n\t"
      "global_load_dwordx4 v[148:151], v229, s[64:65] sc1\n\t"
      "global_load_dwordx4 v[152:155], v230, s[64:65] sc1\n\t"
      "s_waitcnt vmcnt(0)\n\t"
      "v_max3_u32 v234, v128, v129, v130\n\t"
      "v_max3_u32 v234, v234, v131, v132\n\t"
      "v_max3_u32 v234, v234, v133, v134\n\t"
      "v_max3_u32 v234, v234, v135, v136\n\t"
      "v_max3_u32 v234, v234, v137, v138\n\t"
      "v_max3_u32 v234, v234, v139, v140\n\t"
      "v_max3_u32 v234, v234, v141, v142\n\t"
      "v_max3_u32 v234, v234, v143, v144\n\t"
      "v_max3_u32 v234, v234, v145, v146\n\t"
      "v_max3_u32 v234, v234, v147, v148\n\t"
      "v_max3_u32 v234, v234, v149, v150\n\t"
      "v_max3_u32 v234, v234, v151, v152\n\t"
      "v_max3_u32 v234, v234, v153, v154\n\t"
      "v_max_u32_e32 v234, v234, v155\n\t"
      "v_cmp_eq_u32_e32 vcc, -1, v234\n\t"
      "s_cbranch_vccnz NQ6L_%=\n\t"
      "NQ6X_%=:\n\t"
      "v_mfma_f32_16x16x32_bf16 v[184:187], v[128:131], v[0:3], v[180:183]\n\t"
      "v_mfma_f32_16x16x32_bf16 v[188:191], v[128:131], v[4:7], 0\n\t"
      "v_mfma_f32_16x16x32_bf16 v[192:195], v[128:131], v[8:11], 0\n\t"
      "v_mfma_f32_16x16x32_bf16 v[196:199], v[128:131], v[12:15], 0\n\t"
      "v_mfma_f32_16x16x32_bf16 v[184:187], v[132:135], v[16:19], v[184:187]\n\t"
      "v_mfma_f32_16x16x32_bf16 v[188:191], v[132:135], v[20:23], v[188:191]\n\t"
      "v_mfma_f32_16x16x32_bf16 v[192:195], v[132:135], v[24:27], v[192:195]\n\t"
      "v_mfma_f32_16x16x32_bf16 v[196:199], v[132:135], v[28:31], v[196:199]\n\t"
      "v_mfma_f32_16x16x32_bf16 v[184:187], v[136:139], v[32:35], v[184:187]\n\t"
      "v_mfma_f32_16x16x32_bf16 v[188:191], v[136:139], v[36:39], v[188:191]\n\t"
      "v_mfma_f32_16x16x32_bf16 v[192:195], v[136:139], v[40:43], v[192:195]\n\t"
      "v_mfma_f32_16x16x32_bf16 v[196:199], v[136:139], v[44:47], v[196:199]\n\t"
      "v_mfma_f32_16x16x32_bf16 v[184:187], v[140:143], v[48:51], v[184:187]\n\t"
      "v_mfma_f32_16x16x32_bf16 v[188:191], v[140:143], v[52:55], v[188:191]\n\t"
      "v_mfma_f32_16x16x32_bf16 v[192:195], v[140:143], v[56:59], v[192:195]\n\t"
      "v_mfma_f32_16x16x32_bf16 v[196:199], v[140:143], v[60:63], v[196:199]\n\t"
      "v_mfma_f32_16x16x32_bf16 v[184:187], v[144:147], v[64:67], v[184:187]\n\t"
      "v_mfma_f32_16x16x32_bf16 v[188:191], v[144:147], v[68:71], v[188:191]\n\t"
      "v_mfma_f32_16x16x32_bf16 v[192:195], v[144:147], v[72:75], v[192:195]\n\t"
      "v_mfma_f32_16x16x32_bf16 v[196:199], v[144:147], v[76:79], v[196:199]\n\t"
      "v_mfma_f32_16x16x32_bf16 v[184:187], v[148:151], v[80:83], v[184:187]\n\t"
      "v_mfma_f32_16x16x32_bf16 v[188:191], v[148:151], v[84:87], v[188:191]\n\t"
      "v_mfma_f32_16x16x32_bf16 v[192:195], v[148:151], v[88:91], v[192:195]\n\t"
      "v_mfma_f32_16x16x32_bf16 v[196:199], v[148:151], v[92:95], v[196:199]\n\t"
      "v_mfma_f32_16x16x32_bf16 v[184:187], v[152:155], v[96:99], v[184:187]\n\t"
      "v_mfma_f32_16x16x32_bf16 v[188:191], v[152:155], v[100:103], v[188:191]\n\t"
      "v_mfma_f32_16x16x32_bf16 v[192:195], v[152:155], v[104:107], v[192:195]\n\t"
      "v_mfma_f32_16x16x32_bf16 v[196:199], v[152:155], v[108:111], v[196:199]\n\t"
      "s_waitcnt vmcnt(0)\n\t"
      "s_nop 7\n\t"
      "s_branch NQ6B_%=\n\t"
      "FP0R_%=:\n\t"
      "s_mov_b32 s51, 0\n\t"
      "FP0L_%=:\n\t"
      "s_add_u32 s51, s51, 1\n\t"
      "s_cmp_lt_u32 s51, 262144\n\t"
      "s_cbranch_scc0 FP0X_%=\n\t"
      "s_waitcnt vmcnt(0)\n\t"
      "s_sleep 1\n\t"
      "global_load_dwordx4 v[128:131], v224, s[52:53] sc1\n\t"
      "global_load_dwordx4 v[132:135], v225, s[52:53] sc1\n\t"
      "global_load_dwordx4 v[136:139], v226, s[52:53] sc1\n\t"
      "global_load_dwordx4 v[140:143], v227, s[52:53] sc1\n\t"
      "global_load_dwordx4 v[144:147], v228, s[52:53] sc1\n\t"
      "global_load_dwordx4 v[148:151], v229, s[52:53] sc1\n\t"
      "global_load_dwordx4 v[152:155], v230, s[52:53] sc1\n\t"
      "s_waitcnt vmcnt(6)\n\t"
      "v_max3_u32 v234, v128, v129, v130\n\t"
      "v_max_u32_e32 v234, v234, v131\n\t"
      "v_cmp_eq_u32_e32 vcc, -1, v234\n\t"
      "s_cbranch_vccnz FP0L_%=\n\t"
      "FP0X_%=:\n\t"
      "s_branch FP0B_%=\n\t"
      "FP1R_%=:\n\t"
      "s_mov_b32 s51, 0\n\t"
      "FP1L_%=:\n\t"
      "s_add_u32 s51, s51, 1\n\t"
      "s_cmp_lt_u32 s51, 262144\n\t"
      "s_cbranch_scc0 FP1X_%=\n\t"
      "s_waitcnt vmcnt(0)\n\t"
      "s_sleep 1\n\t"
      "global_load_dwordx4 v[132:135], v225, s[52:53] sc1\n\t"
      "global_load_dwordx4 v[136:139], v226, s[52:53] sc1\n\t"
      "global_load_dwordx4 v[140:143], v227, s[52:53] sc1\n\t"
      "global_load_dwordx4 v[144:147], v228, s[52:53] sc1\n\t"
      "global_load_dwordx4 v[148:151], v229, s[52:53] sc1\n\t"
      "global_load_dwordx4 v[152:155], v230, s[52:53] sc1\n\t"
      "global_load_dwordx4 v[128:131], v224, s[54:55] sc1\n\t"
      "s_waitcnt vmcnt(6)\n\t"
      "v_max3_u32 v234, v132, v133, v134\n\t"
      "v_max_u32_e32 v234, v234, v135\n\t"
      "v_cmp_eq_u32_e32 vcc, -1, v234\n\t"
      "s_cbranch_vccnz FP1L_%=\n\t"
      "FP1X_%=:\n\t"
      "s_branch FP1B_%=\n\t"
      "FP2R_%=:\n\t"
      "s_mov_b32 s51, 0\n\t"
      "FP2L_%=:\n\t"
      "s_add_u32 s51, s51, 1\n\t"
      "s_cmp_lt_u32 s51, 262144\n\t"
      "s_cbranch_scc0 FP2X_%=\n\t"
      "s_waitcnt vmcnt(0)\n\t"
      "s_sleep 1\n\t"
      "global_load_dwordx4 v[136:139], v226, s[52:53] sc1\n\t"
      "global_load_dwordx4 v[140:143], v227, s[52:53] sc1\n\t"
      "global_load_dwordx4 v[144:147], v228, s[52:53] sc1\n\t"
      "global_load_dwordx4 v[148:151], v229, s[52:53] sc1\n\t"
      "global_load_dwordx4 v[152:155], v230, s[52:53] sc1\n\t"
      "global_load_dwordx4 v[128:131], v224, s[54:55] sc1\n\t"
      "global_load_dwordx4 v[132:135], v225, s[54:55] sc1\n\t"
      "s_waitcnt vmcnt(6)\n\t"
      "v_max3_u32 v234, v136, v137, v138\n\t"
      "v_max_u32_e32 v234, v234, v139\n\t"
      "v_cmp_eq_u32_e32 vcc, -1, v234\n\t"
      "s_cbranch_vccnz FP2L_%=\n\t"
      "FP2X_%=:\n\t"
      "s_branch FP2B_%=\n\t"
      "FP3R_%=:\n\t"
      "s_mov_b32 s51, 0\n\t"
      "FP3L_%=:\n\t"
      "s_add_u32 s51, s51, 1\n\t"
      "s_cmp_lt_u32 s51, 262144\n\t"
      "s_cbranch_scc0 FP3X_%=\n\t"
      "s_waitcnt vmcnt(0)\n\t"
      "s_sleep 1\n\t"
      "global_load_dwordx4 v[140:143], v227, s[52:53] sc1\n\t"
      "global_load_dwordx4 v[144:147], v228, s[52:53] sc1\n\t"
      "global_load_dwordx4 v[148:151], v229, s[52:53] sc1\n\t"
      "global_load_dwordx4 v[152:155], v230, s[52:53] sc1\n\t"
      "global_load_dwordx4 v[128:131], v224, s[54:55] sc1\n\t"
      "global_load_dwordx4 v[132:135], v225, s[54:55] sc1\n\t"
      "global_load_dwordx4 v[136:139], v226, s[54:55] sc1\n\t"
      "s_waitcnt vmcnt(6)\n\t"
      "v_max3_u32 v234, v140, v141, v142\n\t"
      "v_max_u32_e32 v234, v234, v143\n\t"
      "v_cmp_eq_u32_e32 vcc, -1, v234\n\t"
      "s_cbranch_vccnz FP3L_%=\n\t"
      "FP3X_%=:\n\t"
      "s_branch FP3B_%=\n\t"
      "FP4R_%=:\n\t"
      "s_mov_b32 s51, 0\n\t"
      "FP4L_%=:\n\t"
      "s_add_u32 s51, s51, 1\n\t"
      "s_cmp_lt_u32 s51, 262144\n\t"
      "s_cbranch_scc0 FP4X_%=\n\t"
      "s_waitcnt vmcnt(0)\n\t"
      "s_sleep 1\n\t"
      "global_load_dwordx4 v[144:147], v228, s[52:53] sc1\n\t"
      "global_load_dwordx4 v[148:151], v229, s[52:53] sc1\n\t"
      "global_load_dwordx4 v[152:155], v230, s[52:53] sc1\n\t"
      "global_load_dwordx4 v[128:131], v224, s[54:55] sc1\n\t"
      "global_load_dwordx4 v[132:135], v225, s[54:55] sc1\n\t"
      "global_load_dwordx4 v[136:139], v226, s[54:55] sc1\n\t"
      "global_load_dwordx4 v[140:143], v227, s[54:55] sc1\n\t"
      "s_waitcnt vmcnt(6)\n\t"
      "v_max3_u32 v234, v144, v145, v146\n\t"
      "v_max_u32_e32 v234, v234, v147\n\t"
      "v_cmp_eq_u32_e32 vcc, -1, v234\n\t"
      "s_cbranch_vccnz FP4L_%=\n\t"
      "FP4X_%=:\n\t"
      "s_branch FP4B_%=\n\t"
      "FP5R_%=:\n\t"
      "s_mov_b32 s51, 0\n\t"
      "FP5L_%=:\n\t"
      "s_add_u32 s51, s51, 1\n\t"
      "s_cmp_lt_u32 s51, 262144\n\t"
      "s_cbranch_scc0 FP5X_%=\n\t"
      "s_waitcnt vmcnt(0)\n\t"
      "s_sleep 1\n\t"
      "global_load_dwordx4 v[148:151], v229, s[52:53] sc1\n\t"
      "global_load_dwordx4 v[152:155], v230, s[52:53] sc1\n\t"
      "global_load_dwordx4 v[128:131], v224, s[54:55] sc1\n\t"
      "global_load_dwordx4 v[132:135], v225, s[54:55] sc1\n\t"
      "global_load_dwordx4 v[136:139], v226, s[54:55] sc1\n\t"
      "global_load_dwordx4 v[140:143], v227, s[54:55] sc1\n\t"
      "global_load_dwordx4 v[144:147], v228, s[54:55] sc1\n\t"
      "s_waitcnt vmcnt(6)\n\t"
      "v_max3_u32 v234, v148, v149, v150\n\t"
      "v_max_u32_e32 v234, v234, v151\n\t"
      "v_cmp_eq_u32_e32 vcc, -1, v234\n\t"
      "s_cbranch_vccnz FP5L_%=\n\t"
      "FP5X_%=:\n\t"
      "s_branch FP5B_%=\n\t"
      "FP6R_%=:\n\t"
      "s_mov_b32 s51, 0\n\t"
      "FP6L_%=:\n\t"
      "s_add_u32 s51, s51, 1\n\t"
      "s_cmp_lt_u32 s51, 262144\n\t"
      "s_cbranch_scc0 FP6X_%=\n\t"
      "s_waitcnt vmcnt(0)\n\t"
      "s_sleep 1\n\t"
      "global_load_dwordx4 v[152:155], v230, s[52:53] sc1\n\t"
      "global_load_dwordx4 v[128:131], v224, s[54:55] sc1\n\t"
      "global_load_dwordx4 v[132:135], v225, s[54:55] sc1\n\t"
      "global_load_dwordx4 v[136:139], v226, s[54:55] sc1\n\t"
      "global_load_dwordx4 v[140:143], v227, s[54:55] sc1\n\t"
      "global_load_dwordx4 v[144:147], v228, s[54:55] sc1\n\t"
      "global_load_dwordx4 v[148:151], v229, s[54:55] sc1\n\t"
      "s_waitcnt vmcnt(6)\n\t"
      "v_max3_u32 v234, v152, v153, v154\n\t"
      "v_max_u32_e32 v234, v234, v155\n\t"
      "v_cmp_eq_u32_e32 vcc, -1, v234\n\t"
      "s_cbranch_vccnz FP6L_%=\n\t"
      "FP6X_%=:\n\t"
      "s_branch FP6B_%=\n\t"
      "FP7R_%=:\n\t"
      "s_mov_b32 s51, 0\n\t"
      "FP7L_%=:\n\t"
      "s_add_u32 s51, s51, 1\n\t"
      "s_cmp_lt_u32 s51, 262144\n\t"
      "s_cbranch_scc0 FP7X_%=\n\t"
      "s_waitcnt vmcnt(0)\n\t"
      "s_sleep 4\n\t"
      "global_load_dwordx4 v[128:131], v224, s[54:55] sc1\n\t"
      "global_load_dwordx4 v[132:135], v225, s[54:55] sc1\n\t"
      "global_load_dwordx4 v[136:139], v226, s[54:55] sc1\n\t"
      "global_load_dwordx4 v[140:143], v227, s[54:55] sc1\n\t"
      "global_load_dwordx4 v[144:147], v228, s[54:55] sc1\n\t"
      "global_load_dwordx4 v[148:151], v229, s[54:55] sc1\n\t"
      "global_load_dwordx4 v[152:155], v230, s[54:55] sc1\n\t"
      "s_waitcnt vmcnt(6)\n\t"
      "v_max3_u32 v234, v128, v129, v130\n\t"
      "v_max_u32_e32 v234, v234, v131\n\t"
      "v_cmp_eq_u32_e32 vcc, -1, v234\n\t"
      "s_cbranch_vccnz FP7L_%=\n\t"
      "FP7X_%=:\n\t"
      "s_branch FP7B_%=\n\t"
      "FP8R_%=:\n\t"
      "s_mov_b32 s51, 0\n\t"
      "FP8L_%=:\n\t"
      "s_add_u32 s51, s51, 1\n\t"
      "s_cmp_lt_u32 s51, 262144\n\t"
      "s_cbranch_scc0 FP8X_%=\n\t"
      "s_waitcnt vmcnt(0)\n\t"
      "s_sleep 4\n\t"
      "global_load_dwordx4 v[132:135], v225, s[54:55] sc1\n\t"
      "global_load_dwordx4 v[136:139], v226, s[54:55] sc1\n\t"
      "global_load_dwordx4 v[140:143], v227, s[54:55] sc1\n\t"
      "global_load_dwordx4 v[144:147], v228, s[54:55] sc1\n\t"
      "global_load_dwordx4 v[148:151], v229, s[54:55] sc1\n\t"
      "global_load_dwordx4 v[152:155], v230, s[54:55] sc1\n\t"
      "global_load_dwordx4 v[128:131], v224, s[56:57] sc1\n\t"
      "s_waitcnt vmcnt(6)\n\t"
      "v_max3_u32 v234, v132, v133, v134\n\t"
      "v_max_u32_e32 v234, v234, v135\n\t"
      "v_cmp_eq_u32_e32 vcc, -1, v234\n\t"
      "s_cbranch_vccnz FP8L_%=\n\t"
      "FP8X_%=:\n\t"
      "s_branch FP8B_%=\n\t"
      "FP9R_%=:\n\t"
      "s_mov_b32 s51, 0\n\t"
      "FP9L_%=:\n\t"
      "s_add_u32 s51, s51, 1\n\t"
      "s_cmp_lt_u32 s51, 262144\n\t"
      "s_cbranch_scc0 FP9X_%=\n\t"
      "s_waitcnt vmcnt(0)\n\t"
      "s_sleep 4\n\t"
      "global_load_dwordx4 v[136:139], v226, s[54:55] sc1\n\t"
      "global_load_dwordx4 v[140:143], v227, s[54:55] sc1\n\t"
      "global_load_dwordx4 v[144:147], v228, s[54:55] sc1\n\t"
      "global_load_dwordx4 v[148:151], v229, s[54:55] sc1\n\t"
      "global_load_dwordx4 v[152:155], v230, s[54:55] sc1\n\t"
      "global_load_dwordx4 v[128:131], v224, s[56:57] sc1\n\t"
      "global_load_dwordx4 v[132:135], v225, s[56:57] sc1\n\t"
      "s_waitcnt vmcnt(6)\n\t"
      "v_max3_u32 v234, v136, v137, v138\n\t"
      "v_max_u32_e32 v234, v234, v139\n\t"
      "v_cmp_eq_u32_e32 vcc, -1, v234\n\t"
      "s_cbranch_vccnz FP9L_%=\n\t"
      "FP9X_%=:\n\t"
      "s_branch FP9B_%=\n\t"
      "FP10R_%=:\n\t"
      "s_mov_b32 s51, 0\n\t"
      "FP10L_%=:\n\t"
      "s_add_u32 s51, s51, 1\n\t"
      "s_cmp_lt_u32 s51, 262144\n\t"
      "s_cbranch_scc0 FP10X_%=\n\t"
      "s_waitcnt vmcnt(0)\n\t"
      "s_sleep 4\n\t"
      "global_load_dwordx4 v[140:143], v227, s[54:55] sc1\n\t"
      "global_load_dwordx4 v[144:147], v228, s[54:55] sc1\n\t"
      "global_load_dwordx4 v[148:151], v229, s[54:55] sc1\n\t"
      "global_load_dwordx4 v[152:155], v230, s[54:55] sc1\n\t"
      "global_load_dwordx4 v[128:131], v224, s[56:57] sc1\n\t"
      "global_load_dwordx4 v[132:135], v225, s[56:57] sc1\n\t"
      "global_load_dwordx4 v[136:139], v226, s[56:57] sc1\n\t"
      "s_waitcnt vmcnt(6)\n\t"
      "v_max3_u32 v234, v140, v141, v142\n\t"
      "v_max_u32_e32 v234, v234, v143\n\t"
      "v_cmp_eq_u32_e32 vcc, -1, v234\n\t"
      "s_cbranch_vccnz FP10L_%=\n\t"
      "FP10X_%=:\n\t"
      "s_branch FP10B_%=\n\t"
      "FP11R_%=:\n\t"
      "s_mov_b32 s51, 0\n\t"
      "FP11L_%=:\n\t"
      "s_add_u32 s51, s51, 1\n\t"
      "s_cmp_lt_u32 s51, 262144\n\t"
      "s_cbranch_scc0 FP11X_%=\n\t"
      "s_waitcnt vmcnt(0)\n\t"
      "s_sleep 4\n\t"
      "global_load_dwordx4 v[144:147], v228, s[54:55] sc1\n\t"
      "global_load_dwordx4 v[148:151], v229, s[54:55] sc1\n\t"
      "global_load_dwordx4 v[152:155], v230, s[54:55] sc1\n\t"
      "global_load_dwordx4 v[128:131], v224, s[56:57] sc1\n\t"
      "global_load_dwordx4 v[132:135], v225, s[56:57] sc1\n\t"
      "global_load_dwordx4 v[136:139], v226, s[56:57] sc1\n\t"
      "global_load_dwordx4 v[140:143], v227, s[56:57] sc1\n\t"
      "s_waitcnt vmcnt(6)\n\t"
      "v_max3_u32 v234, v144, v145, v146\n\t"
      "v_max_u32_e32 v234, v234, v147\n\t"
      "v_cmp_eq_u32_e32 vcc, -1, v234\n\t"
      "s_cbranch_vccnz FP11L_%=\n\t"
      "FP11X_%=:\n\t"
      "s_branch FP11B_%=\n\t"
      "FP12R_%=:\n\t"
      "s_mov_b32 s51, 0\n\t"
      "FP12L_%=:\n\t"
      "s_add_u32 s51, s51, 1\n\t"
      "s_cmp_lt_u32 s51, 262144\n\t"
      "s_cbranch_scc0 FP12X_%=\n\t"
      "s_waitcnt vmcnt(0)\n\t"
      "s_sleep 4\n\t"
      "global_load_dwordx4 v[148:151], v229, s[54:55] sc1\n\t"
      "global_load_dwordx4 v[152:155], v230, s[54:55] sc1\n\t"
      "global_load_dwordx4 v[128:131], v224, s[56:57] sc1\n\t"
      "global_load_dwordx4 v[132:135], v225, s[56:57] sc1\n\t"
      "global_load_dwordx4 v[136:139], v226, s[56:57] sc1\n\t"
      "global_load_dwordx4 v[140:143], v227, s[56:57] sc1\n\t"
      "global_load_dwordx4 v[144:147], v228, s[56:57] sc1\n\t"
      "s_waitcnt vmcnt(6)\n\t"
      "v_max3_u32 v234, v148, v149, v150\n\t"
      "v_max_u32_e32 v234, v234, v151\n\t"
      "v_cmp_eq_u32_e32 vcc, -1, v234\n\t"
      "s_cbranch_vccnz FP12L_%=\n\t"
      "FP12X_%=:\n\t"
      "s_branch FP12B_%=\n\t"
      "FP13R_%=:\n\t"
      "s_mov_b32 s51, 0\n\t"
      "FP13L_%=:\n\t"
      "s_add_u32 s51, s51, 1\n\t"
      "s_cmp_lt_u32 s51, 262144\n\t"
      "s_cbranch_scc0 FP13X_%=\n\t"
      "s_waitcnt vmcnt(0)\n\t"
      "s_sleep 4\n\t"
      "global_load_dwordx4 v[152:155], v230, s[54:55] sc1\n\t"
      "global_load_dwordx4 v[128:131], v224, s[56:57] sc1\n\t"
      "global_load_dwordx4 v[132:135], v225, s[56:57] sc1\n\t"
      "global_load_dwordx4 v[136:139], v226, s[56:57] sc1\n\t"
      "global_load_dwordx4 v[140:143], v227, s[56:57] sc1\n\t"
      "global_load_dwordx4 v[144:147], v228, s[56:57] sc1\n\t"
      "global_load_dwordx4 v[148:151], v229, s[56:57] sc1\n\t"
      "s_waitcnt vmcnt(6)\n\t"
      "v_max3_u32 v234, v152, v153, v154\n\t"
      "v_max_u32_e32 v234, v234, v155\n\t"
      "v_cmp_eq_u32_e32 vcc, -1, v234\n\t"
      "s_cbranch_vccnz FP13L_%=\n\t"
      "FP13X_%=:\n\t"
      "s_waitcnt vmcnt(0)\n\t"
      "s_branch FP13B_%=\n\t"
      "NP1R_%=:\n\t"
      "s_mov_b32 s51, 0\n\t"
      "NP1L_%=:\n\t"
      "s_add_u32 s51, s51, 1\n\t"
      "s_cmp_lt_u32 s51, 262144\n\t"
      "s_cbranch_scc0 NP1X_%=\n\t"
      "s_waitcnt vmcnt(0)\n\t"
      "s_sleep 1\n\t"
      "global_load_dwordx4 v[128:131], v224, s[54:55] sc1\n\t"
      "global_load_dwordx4 v[132:135], v225, s[54:55] sc1\n\t"
      "global_load_dwordx4 v[136:139], v226, s[54:55] sc1\n\t"
      "global_load_dwordx4 v[140:143], v227, s[54:55] sc1\n\t"
      "global_load_dwordx4 v[144:147], v228, s[54:55] sc1\n\t"
      "global_load_dwordx4 v[148:151], v229, s[54:55] sc1\n\t"
      "global_load_dwordx4 v[152:155], v230, s[54:55] sc1\n\t"
      "s_waitcnt vmcnt(0)\n\t"
      "v_max3_u32 v234, v128, v129, v130\n\t"
      "v_max3_u32 v234, v234, v131, v132\n\t"
      "v_max3_u32 v234, v234, v133, v134\n\t"
      "v_max3_u32 v234, v234, v135, v136\n\t"
      "v_max3_u32 v234, v234, v137, v138\n\t"
      "v_max3_u32 v234, v234, v139, v140\n\t"
      "v_max3_u32 v234, v234, v141, v142\n\t"
      "v_max3_u32 v234, v234, v143, v144\n\t"
      "v_max3_u32 v234, v234, v145, v146\n\t"
      "v_max3_u32 v234, v234, v147, v148\n\t"
      "v_max3_u32 v234, v234, v149, v150\n\t"
      "v_max3_u32 v234, v234, v151, v152\n\t"
      "v_max3_u32 v234, v234, v153, v154\n\t"
      "v_max_u32_e32 v234, v234, v155\n\t"
      "v_cmp_eq_u32_e32 vcc, -1, v234\n\t"
      "s_cbranch_vccnz NP1L_%=\n\t"
      "NP1X_%=:\n\t"
      "v_mfma_f32_16x16x32_bf16 v[200:203], v[128:131], v[0:3], 0\n\t"
      "v_mfma_f32_16x16x32_bf16 v[204:207], v[128:131], v[4:7], 0\n\t"
      "v_mfma_f32_16x16x32_bf16 v[208:211], v[128:131], v[8:11], 0\n\t"
      "v_mfma_f32_16x16x32_bf16 v[212:215], v[128:131], v[12:15], 0\n\t"
      "v_mfma_f32_16x16x32_bf16 v[200:203], v[132:135], v[16:19], v[200:203]\n\t"
      "v_mfma_f32_16x16x32_bf16 v[204:207], v[132:135], v[20:23], v[204:207]\n\t"
      "v_mfma_f32_16x16x32_bf16 v[208:211], v[132:135], v[24:27], v[208:211]\n\t"
      "v_mfma_f32_16x16x32_bf16 v[212:215], v[132:135], v[28:31], v[212:215]\n\t"
      "v_mfma_f32_16x16x32_bf16 v[200:203], v[136:139], v[32:35], v[200:203]\n\t"
      "v_mfma_f32_16x16x32_bf16 v[204:207], v[136:139], v[36:39], v[204:207]\n\t"
      "v_mfma_f32_16x16x32_bf16 v[208:211], v[136:139], v[40:43], v[208:211]\n\t"
      "v_mfma_f32_16x16x32_bf16 v[212:215], v[136:139], v[44:47], v[212:215]\n\t"
      "v_mfma_f32_16x16x32_bf16 v[200:203], v[140:143], v[48:51], v[200:203]\n\t"
      "v_mfma_f32_16x16x32_bf16 v[204:207], v[140:143], v[52:55], v[204:207]\n\t"
      "v_mfma_f32_16x16x32_bf16 v[208:211], v[140:143], v[56:59], v[208:211]\n\t"
      "v_mfma_f32_16x16x32_bf16 v[212:215], v[140:143], v[60:63], v[212:215]\n\t"
      "v_mfma_f32_16x16x32_bf16 v[200:203], v[144:147], v[64:67], v[200:203]\n\t"
      "v_mfma_f32_16x16x32_bf16 v[204:207], v[144:147], v[68:71], v[204:207]\n\t"
      "v_mfma_f32_16x16x32_bf16 v[208:211], v[144:147], v[72:75], v[208:211]\n\t"
      "v_mfma_f32_16x16x32_bf16 v[212:215], v[144:147], v[76:79], v[212:215]\n\t"
      "v_mfma_f32_16x16x32_bf16 v[200:203], v[148:151], v[80:83], v[200:203]\n\t"
      "v_mfma_f32_16x16x32_bf16 v[204:207], v[148:151], v[84:87], v[204:207]\n\t"
      "v_mfma_f32_16x16x32_bf16 v[208:211], v[148:151], v[88:91], v[208:211]\n\t"
      "v_mfma_f32_16x16x32_bf16 v[212:215], v[148:151], v[92:95], v[212:215]\n\t"
      "v_mfma_f32_16x16x32_bf16 v[200:203], v[152:155], v[96:99], v[200:203]\n\t"
      "v_mfma_f32_16x16x32_bf16 v[204:207], v[152:155], v[100:103], v[204:207]\n\t"
      "v_mfma_f32_16x16x32_bf16 v[208:211], v[152:155], v[104:107], v[208:211]\n\t"
      "v_mfma_f32_16x16x32_bf16 v[212:215], v[152:155], v[108:111], v[212:215]\n\t"
      "global_load_dwordx4 v[128:131], v224, s[56:57] sc1\n\t"
      "global_load_dwordx4 v[132:135], v225, s[56:57] sc1\n\t"
      "global_load_dwordx4 v[136:139], v226, s[56:57] sc1\n\t"
      "global_load_dwordx4 v[140:143], v227, s[56:57] sc1\n\t"
      "global_load_dwordx4 v[144:147], v228, s[56:57] sc1\n\t"
      "global_load_dwordx4 v[148:151], v229, s[56:57] sc1\n\t"
      "global_load_dwordx4 v[152:155], v230, s[56:57] sc1\n\t"
      "s_waitcnt vmcnt(0)\n\t"
      "s_nop 7\n\t"
      "s_branch NP1B_%=\n\t"
      "FP14R_%=:\n\t"
      "s_mov_b32 s51, 0\n\t"
      "FP14L_%=:\n\t"
      "s_add_u32 s51, s51, 1\n\t"
      "s_cmp_lt_u32 s51, 262144\n\t"
      "s_cbranch_scc0 FP14X_%=\n\t"
      "s_waitcnt vmcnt(0)\n\t"
      "s_sleep 4\n\t"
      "global_load_dwordx4 v[128:131], v224, s[56:57] sc1\n\t"
      "global_load_dwordx4 v[132:135], v225, s[56:57] sc1\n\t"
      "global_load_dwordx4 v[136:139], v226, s[56:57] sc1\n\t"
      "global_load_dwordx4 v[140:143], v227, s[56:57] sc1\n\t"
      "global_load_dwordx4 v[144:147], v228, s[56:57] sc1\n\t"
      "global_load_dwordx4 v[148:151], v229, s[56:57] sc1\n\t"
      "global_load_dwordx4 v[152:155], v230, s[56:57] sc1\n\t"
      "s_waitcnt vmcnt(6)\n\t"
      "v_max3_u32 v234, v128, v129, v130\n\t"
      "v_max_u32_e32 v234, v234, v131\n\t"
      "v_cmp_eq_u32_e32 vcc, -1, v234\n\t"
      "s_cbranch_vccnz FP14L_%=\n\t"
      "FP14X_%=:\n\t"
      "s_waitcnt vmcnt(0)\n\t"
      "s_branch FP14B_%=\n\t"
      "FP15R_%=:\n\t"
      "s_mov_b32 s51, 0\n\t"
      "FP15L_%=:\n\t"
      "s_add_u32 s51, s51, 1\n\t"
      "s_cmp_lt_u32 s51, 262144\n\t"
      "s_cbranch_scc0 FP15X_%=\n\t"
      "s_waitcnt vmcnt(0)\n\t"
      "s_sleep 4\n\t"
      "global_load_dwordx4 v[132:135], v225, s[56:57] sc1\n\t"
      "global_load_dwordx4 v[136:139], v226, s[56:57] sc1\n\t"
      "global_load_dwordx4 v[140:143], v227, s[56:57] sc1\n\t"
      "global_load_dwordx4 v[144:147], v228, s[56:57] sc1\n\t"
      "global_load_dwordx4 v[148:151], v229, s[56:57] sc1\n\t"
      "global_load_dwordx4 v[152:155], v230, s[56:57] sc1\n\t"
      "global_load_dwordx4 v[128:131], v224, s[58:59] sc1\n\t"
      "s_waitcnt vmcnt(6)\n\t"
      "v_max3_u32 v234, v132, v133, v134\n\t"
      "v_max_u32_e32 v234, v234, v135\n\t"
      "v_cmp_eq_u32_e32 vcc, -1, v234\n\t"
      "s_cbranch_vccnz FP15L_%=\n\t"
      "FP15X_%=:\n\t"
      "s_waitcnt vmcnt(0)\n\t"
      "s_branch FP15B_%=\n\t"
      "FP16R_%=:\n\t"
      "s_mov_b32 s51, 0\n\t"
      "FP16L_%=:\n\t"
      "s_add_u32 s51, s51, 1\n\t"
      "s_cmp_lt_u32 s51, 262144\n\t"
      "s_cbranch_scc0 FP16X_%=\n\t"
      "s_waitcnt vmcnt(0)\n\t"
      "s_sleep 4\n\t"
      "global_load_dwordx4 v[136:139], v226, s[56:57] sc1\n\t"
      "global_load_dwordx4 v[140:143], v227, s[56:57] sc1\n\t"
      "global_load_dwordx4 v[144:147], v228, s[56:57] sc1\n\t"
      "global_load_dwordx4 v[148:151], v229, s[56:57] sc1\n\t"
      "global_load_dwordx4 v[152:155], v230, s[56:57] sc1\n\t"
      "global_load_dwordx4 v[128:131], v224, s[58:59] sc1\n\t"
      "global_load_dwordx4 v[132:135], v225, s[58:59] sc1\n\t"
      "s_waitcnt vmcnt(6)\n\t"
      "v_max3_u32 v234, v136, v137, v138\n\t"
      "v_max_u32_e32 v234, v234, v139\n\t"
      "v_cmp_eq_u32_e32 vcc, -1, v234\n\t"
      "s_cbranch_vccnz FP16L_%=\n\t"
      "FP16X_%=:\n\t"
      "s_waitcnt vmcnt(0)\n\t"
      "s_branch FP16B_%=\n\t"
      "FP17R_%=:\n\t"
      "s_mov_b32 s51, 0\n\t"
      "FP17L_%=:\n\t"
      "s_add_u32 s51, s51, 1\n\t"
      "s_cmp_lt_u32 s51, 262144\n\t"
      "s_cbranch_scc0 FP17X_%=\n\t"
      "s_waitcnt vmcnt(0)\n\t"
      "s_sleep 4\n\t"
      "global_load_dwordx4 v[140:143], v227, s[56:57] sc1\n\t"
      "global_load_dwordx4 v[144:147], v228, s[56:57] sc1\n\t"
      "global_load_dwordx4 v[148:151], v229, s[56:57] sc1\n\t"
      "global_load_dwordx4 v[152:155], v230, s[56:57] sc1\n\t"
      "global_load_dwordx4 v[128:131], v224, s[58:59] sc1\n\t"
      "global_load_dwordx4 v[132:135], v225, s[58:59] sc1\n\t"
      "global_load_dwordx4 v[136:139], v226, s[58:59] sc1\n\t"
      "s_waitcnt vmcnt(6)\n\t"
      "v_max3_u32 v234, v140, v141, v142\n\t"
      "v_max_u32_e32 v234, v234, v143\n\t"
      "v_cmp_eq_u32_e32 vcc, -1, v234\n\t"
      "s_cbranch_vccnz FP17L_%=\n\t"
      "FP17X_%=:\n\t"
      "s_waitcnt vmcnt(0)\n\t"
      "s_branch FP17B_%=\n\t"
      "FP18R_%=:\n\t"
      "s_mov_b32 s51, 0\n\t"
      "FP18L_%=:\n\t"
      "s_add_u32 s51, s51, 1\n\t"
      "s_cmp_lt_u32 s51, 262144\n\t"
      "s_cbranch_scc0 FP18X_%=\n\t"
      "s_waitcnt vmcnt(0)\n\t"
      "s_sleep 4\n\t"
      "global_load_dwordx4 v[144:147], v228, s[56:57] sc1\n\t"
      "global_load_dwordx4 v[148:151], v229, s[56:57] sc1\n\t"
      "global_load_dwordx4 v[152:155], v230, s[56:57] sc1\n\t"
      "global_load_dwordx4 v[128:131], v224, s[58:59] sc1\n\t"
      "global_load_dwordx4 v[132:135], v225, s[58:59] sc1\n\t"
      "global_load_dwordx4 v[136:139], v226, s[58:59] sc1\n\t"
      "global_load_dwordx4 v[140:143], v227, s[58:59] sc1\n\t"
      "s_waitcnt vmcnt(6)\n\t"
      "v_max3_u32 v234, v144, v145, v146\n\t"
      "v_max_u32_e32 v234, v234, v147\n\t"
      "v_cmp_eq_u32_e32 vcc, -1, v234\n\t"
      "s_cbranch_vccnz FP18L_%=\n\t"
      "FP18X_%=:\n\t"
      "s_waitcnt vmcnt(0)\n\t"
      "s_branch FP18B_%=\n\t"
      "FP19R_%=:\n\t"
      "s_mov_b32 s51, 0\n\t"
      "FP19L_%=:\n\t"
      "s_add_u32 s51, s51, 1\n\t"
      "s_cmp_lt_u32 s51, 262144\n\t"
      "s_cbranch_scc0 FP19X_%=\n\t"
      "s_waitcnt vmcnt(0)\n\t"
      "s_sleep 4\n\t"
      "global_load_dwordx4 v[148:151], v229, s[56:57] sc1\n\t"
      "global_load_dwordx4 v[152:155], v230, s[56:57] sc1\n\t"
      "global_load_dwordx4 v[128:131], v224, s[58:59] sc1\n\t"
      "global_load_dwordx4 v[132:135], v225, s[58:59] sc1\n\t"
      "global_load_dwordx4 v[136:139], v226, s[58:59] sc1\n\t"
      "global_load_dwordx4 v[140:143], v227, s[58:59] sc1\n\t"
      "global_load_dwordx4 v[144:147], v228, s[58:59] sc1\n\t"
      "s_waitcnt vmcnt(6)\n\t"
      "v_max3_u32 v234, v148, v149, v150\n\t"
      "v_max_u32_e32 v234, v234, v151\n\t"
      "v_cmp_eq_u32_e32 vcc, -1, v234\n\t"
      "s_cbranch_vccnz FP19L_%=\n\t"
      "FP19X_%=:\n\t"
      "s_branch FP19B_%=\n\t"
      "FP20R_%=:\n\t"
      "s_mov_b32 s51, 0\n\t"
      "FP20L_%=:\n\t"
      "s_add_u32 s51, s51, 1\n\t"
      "s_cmp_lt_u32 s51, 262144\n\t"
      "s_cbranch_scc0 FP20X_%=\n\t"
      "s_waitcnt vmcnt(0)\n\t"
      "s_sleep 4\n\t"
      "global_load_dwordx4 v[152:155], v230, s[56:57] sc1\n\t"
      "global_load_dwordx4 v[128:131], v224, s[58:59] sc1\n\t"
      "global_load_dwordx4 v[132:135], v225, s[58:59] sc1\n\t"
      "global_load_dwordx4 v[136:139], v226, s[58:59] sc1\n\t"
      "global_load_dwordx4 v[140:143], v227, s[58:59] sc1\n\t"
      "global_load_dwordx4 v[144:147], v228, s[58:59] sc1\n\t"
      "global_load_dwordx4 v[148:151], v229, s[58:59] sc1\n\t"
      "s_waitcnt vmcnt(6)\n\t"
      "v_max3_u32 v234, v152, v153, v154\n\t"
      "v_max_u32_e32 v234, v234, v155\n\t"
      "v_cmp_eq_u32_e32 vcc, -1, v234\n\t"
      "s_cbranch_vccnz FP20L_%=\n\t"
      "FP20X_%=:\n\t"
      "s_waitcnt vmcnt(0)\n\t"
      "s_branch FP20B_%=\n\t"
      "NP2R_%=:\n\t"
      "s_mov_b32 s51, 0\n\t"
      "NP2L_%=:\n\t"
      "s_add_u32 s51, s51, 1\n\t"
      "s_cmp_lt_u32 s51, 262144\n\t"
      "s_cbranch_scc0 NP2X_%=\n\t"
      "s_waitcnt vmcnt(0)\n\t"
      "s_sleep 1\n\t"
      "global_load_dwordx4 v[128:131], v224, s[56:57] sc1\n\t"
      "global_load_dwordx4 v[132:135], v225, s[56:57] sc1\n\t"
      "global_load_dwordx4 v[136:139], v226, s[56:57] sc1\n\t"
      "global_load_dwordx4 v[140:143], v227, s[56:57] sc1\n\t"
      "global_load_dwordx4 v[144:147], v228, s[56:57] sc1\n\t"
      "global_load_dwordx4 v[148:151], v229, s[56:57] sc1\n\t"
      "global_load_dwordx4 v[152:155], v230, s[56:57] sc1\n\t"
      "s_waitcnt vmcnt(0)\n\t"
      "v_max3_u32 v234, v128, v129, v130\n\t"
      "v_max3_u32 v234, v234, v131, v132\n\t"
      "v_max3_u32 v234, v234, v133, v134\n\t"
      "v_max3_u32 v234, v234, v135, v136\n\t"
      "v_max3_u32 v234, v234, v137, v138\n\t"
      "v_max3_u32 v234, v234, v139, v140\n\t"
      "v_max3_u32 v234, v234, v141, v142\n\t"
      "v_max3_u32 v234, v234, v143, v144\n\t"
      "v_max3_u32 v234, v234, v145, v146\n\t"
      "v_max3_u32 v234, v234, v147, v148\n\t"
      "v_max3_u32 v234, v234, v149, v150\n\t"
      "v_max3_u32 v234, v234, v151, v152\n\t"
      "v_max3_u32 v234, v234, v153, v154\n\t"
      "v_max_u32_e32 v234, v234, v155\n\t"
      "v_cmp_eq_u32_e32 vcc, -1, v234\n\t"
      "s_cbranch_vccnz NP2L_%=\n\t"
      "NP2X_%=:\n\t"
      "v_mfma_f32_16x16x32_bf16 v[184:187], v[128:131], v[0:3], 0\n\t"
      "v_mfma_f32_16x16x32_bf16 v[188:191], v[128:131], v[4:7], 0\n\t"
      "v_mfma_f32_16x16x32_bf16 v[192:195], v[128:131], v[8:11], 0\n\t"
      "v_mfma_f32_16x16x32_bf16 v[196:199], v[128:131], v[12:15], 0\n\t"
      "v_mfma_f32_16x16x32_bf16 v[184:187], v[132:135], v[16:19], v[184:187]\n\t"
      "v_mfma_f32_16x16x32_bf16 v[188:191], v[132:135], v[20:23], v[188:191]\n\t"
      "v_mfma_f32_16x16x32_bf16 v[192:195], v[132:135], v[24:27], v[192:195]\n\t"
      "v_mfma_f32_16x16x32_bf16 v[196:199], v[132:135], v[28:31], v[196:199]\n\t"
      "v_mfma_f32_16x16x32_bf16 v[184:187], v[136:139], v[32:35], v[184:187]\n\t"
      "v_mfma_f32_16x16x32_bf16 v[188:191], v[136:139], v[36:39], v[188:191]\n\t"
      "v_mfma_f32_16x16x32_bf16 v[192:195], v[136:139], v[40:43], v[192:195]\n\t"
      "v_mfma_f32_16x16x32_bf16 v[196:199], v[136:139], v[44:47], v[196:199]\n\t"
      "v_mfma_f32_16x16x32_bf16 v[184:187], v[140:143], v[48:51], v[184:187]\n\t"
      "v_mfma_f32_16x16x32_bf16 v[188:191], v[140:143], v[52:55], v[188:191]\n\t"
      "v_mfma_f32_16x16x32_bf16 v[192:195], v[140:143], v[56:59], v[192:195]\n\t"
      "v_mfma_f32_16x16x32_bf16 v[196:199], v[140:143], v[60:63], v[196:199]\n\t"
      "v_mfma_f32_16x16x32_bf16 v[184:187], v[144:147], v[64:67], v[184:187]\n\t"
      "v_mfma_f32_16x16x32_bf16 v[188:191], v[144:147], v[68:71], v[188:191]\n\t"
      "v_mfma_f32_16x16x32_bf16 v[192:195], v[144:147], v[72:75], v[192:195]\n\t"
      "v_mfma_f32_16x16x32_bf16 v[196:199], v[144:147], v[76:79], v[196:199]\n\t"
      "v_mfma_f32_16x16x32_bf16 v[184:187], v[148:151], v[80:83], v[184:187]\n\t"
      "v_mfma_f32_16x16x32_bf16 v[188:191], v[148:151], v[84:87], v[188:191]\n\t"
      "v_mfma_f32_16x16x32_bf16 v[192:195], v[148:151], v[88:91], v[192:195]\n\t"
      "v_mfma_f32_16x16x32_bf16 v[196:199], v[148:151], v[92:95], v[196:199]\n\t"
      "v_mfma_f32_16x16x32_bf16 v[184:187], v[152:155], v[96:99], v[184:187]\n\t"
      "v_mfma_f32_16x16x32_bf16 v[188:191], v[152:155], v[100:103], v[188:191]\n\t"
      "v_mfma_f32_16x16x32_bf16 v[192:195], v[152:155], v[104:107], v[192:195]\n\t"
      "v_mfma_f32_16x16x32_bf16 v[196:199], v[152:155], v[108:111], v[196:199]\n\t"
      "global_load_dwordx4 v[128:131], v224, s[58:59] sc1\n\t"
      "global_load_dwordx4 v[132:135], v225, s[58:59] sc1\n\t"
      "global_load_dwordx4 v[136:139], v226, s[58:59] sc1\n\t"
      "global_load_dwordx4 v[140:143], v227, s[58:59] sc1\n\t"
      "global_load_dwordx4 v[144:147], v228, s[58:59] sc1\n\t"
      "global_load_dwordx4 v[148:151], v229, s[58:59] sc1\n\t"
      "global_load_dwordx4 v[152:155], v230, s[58:59] sc1\n\t"
      "s_waitcnt vmcnt(0)\n\t"
      "s_nop 7\n\t"
      "s_branch NP2B_%=\n\t"
      "FP21R_%=:\n\t"
      "s_mov_b32 s51, 0\n\t"
      "FP21L_%=:\n\t"
      "s_add_u32 s51, s51, 1\n\t"
      "s_cmp_lt_u32 s51, 262144\n\t"
      "s_cbranch_scc0 FP21X_%=\n\t"
      "s_waitcnt vmcnt(0)\n\t"
      "s_sleep 4\n\t"
      "global_load_dwordx4 v[128:131], v224, s[58:59] sc1\n\t"
      "global_load_dwordx4 v[132:135], v225, s[58:59] sc1\n\t"
      "global_load_dwordx4 v[136:139], v226, s[58:59] sc1\n\t"
      "global_load_dwordx4 v[140:143], v227, s[58:59] sc1\n\t"
      "global_load_dwordx4 v[144:147], v228, s[58:59] sc1\n\t"
      "global_load_dwordx4 v[148:151], v229, s[58:59] sc1\n\t"
      "global_load_dwordx4 v[152:155], v230, s[58:59] sc1\n\t"
      "s_waitcnt vmcnt(6)\n\t"
      "v_max3_u32 v234, v128, v129, v130\n\t"
      "v_max_u32_e32 v234, v234, v131\n\t"
      "v_cmp_eq_u32_e32 vcc, -1, v234\n\t"
      "s_cbranch_vccnz FP21L_%=\n\t"
      "FP21X_%=:\n\t"
      "s_waitcnt vmcnt(0)\n\t"
      "s_branch FP21B_%=\n\t"
      "FP22R_%=:\n\t"
      "s_mov_b32 s51, 0\n\t"
      "FP22L_%=:\n\t"
      "s_add_u32 s51, s51, 1\n\t"
      "s_cmp_lt_u32 s51, 262144\n\t"
      "s_cbranch_scc0 FP22X_%=\n\t"
      "s_waitcnt vmcnt(0)\n\t"
      "s_sleep 4\n\t"
      "global_load_dwordx4 v[132:135], v225, s[58:59] sc1\n\t"
      "global_load_dwordx4 v[136:139], v226, s[58:59] sc1\n\t"
      "global_load_dwordx4 v[140:143], v227, s[58:59] sc1\n\t"
      "global_load_dwordx4 v[144:147], v228, s[58:59] sc1\n\t"
      "global_load_dwordx4 v[148:151], v229, s[58:59] sc1\n\t"
      "global_load_dwordx4 v[152:155], v230, s[58:59] sc1\n\t"
      "global_load_dwordx4 v[128:131], v224, s[60:61] sc1\n\t"
      "s_waitcnt vmcnt(6)\n\t"
      "v_max3_u32 v234, v132, v133, v134\n\t"
      "v_max_u32_e32 v234, v234, v135\n\t"
      "v_cmp_eq_u32_e32 vcc, -1, v234\n\t"
      "s_cbranch_vccnz FP22L_%=\n\t"
      "FP22X_%=:\n\t"
      "s_waitcnt vmcnt(0)\n\t"
      "s_branch FP22B_%=\n\t"
      "FP23R_%=:\n\t"
      "s_mov_b32 s51, 0\n\t"
      "FP23L_%=:\n\t"
      "s_add_u32 s51, s51, 1\n\t"
      "s_cmp_lt_u32 s51, 262144\n\t"
      "s_cbranch_scc0 FP23X_%=\n\t"
      "s_waitcnt vmcnt(0)\n\t"
      "s_sleep 4\n\t"
      "global_load_dwordx4 v[136:139], v226, s[58:59] sc1\n\t"
      "global_load_dwordx4 v[140:143], v227, s[58:59] sc1\n\t"
      "global_load_dwordx4 v[144:147], v228, s[58:59] sc1\n\t"
      "global_load_dwordx4 v[148:151], v229, s[58:59] sc1\n\t"
      "global_load_dwordx4 v[152:155], v230, s[58:59] sc1\n\t"
      "global_load_dwordx4 v[128:131], v224, s[60:61] sc1\n\t"
      "global_load_dwordx4 v[132:135], v225, s[60:61] sc1\n\t"
      "s_waitcnt vmcnt(6)\n\t"
      "v_max3_u32 v234, v136, v137, v138\n\t"
      "v_max_u32_e32 v234, v234, v139\n\t"
      "v_cmp_eq_u32_e32 vcc, -1, v234\n\t"
      "s_cbranch_vccnz FP23L_%=\n\t"
      "FP23X_%=:\n\t"
      "s_waitcnt vmcnt(0)\n\t"
      "s_branch FP23B_%=\n\t"
      "FP24R_%=:\n\t"
      "s_mov_b32 s51, 0\n\t"
      "FP24L_%=:\n\t"
      "s_add_u32 s51, s51, 1\n\t"
      "s_cmp_lt_u32 s51, 262144\n\t"
      "s_cbranch_scc0 FP24X_%=\n\t"
      "s_waitcnt vmcnt(0)\n\t"
      "s_sleep 4\n\t"
      "global_load_dwordx4 v[140:143], v227, s[58:59] sc1\n\t"
      "global_load_dwordx4 v[144:147], v228, s[58:59] sc1\n\t"
      "global_load_dwordx4 v[148:151], v229, s[58:59] sc1\n\t"
      "global_load_dwordx4 v[152:155], v230, s[58:59] sc1\n\t"
      "global_load_dwordx4 v[128:131], v224, s[60:61] sc1\n\t"
      "global_load_dwordx4 v[132:135], v225, s[60:61] sc1\n\t"
      "global_load_dwordx4 v[136:139], v226, s[60:61] sc1\n\t"
      "s_waitcnt vmcnt(6)\n\t"
      "v_max3_u32 v234, v140, v141, v142\n\t"
      "v_max_u32_e32 v234, v234, v143\n\t"
      "v_cmp_eq_u32_e32 vcc, -1, v234\n\t"
      "s_cbranch_vccnz FP24L_%=\n\t"
      "FP24X_%=:\n\t"
      "s_waitcnt vmcnt(0)\n\t"
      "s_branch FP24B_%=\n\t"
      "FP25R_%=:\n\t"
      "s_mov_b32 s51, 0\n\t"
      "FP25L_%=:\n\t"
      "s_add_u32 s51, s51, 1\n\t"
      "s_cmp_lt_u32 s51, 262144\n\t"
      "s_cbranch_scc0 FP25X_%=\n\t"
      "s_waitcnt vmcnt(0)\n\t"
      "s_sleep 4\n\t"
      "global_load_dwordx4 v[144:147], v228, s[58:59] sc1\n\t"
      "global_load_dwordx4 v[148:151], v229, s[58:59] sc1\n\t"
      "global_load_dwordx4 v[152:155], v230, s[58:59] sc1\n\t"
      "global_load_dwordx4 v[128:131], v224, s[60:61] sc1\n\t"
      "global_load_dwordx4 v[132:135], v225, s[60:61] sc1\n\t"
      "global_load_dwordx4 v[136:139], v226, s[60:61] sc1\n\t"
      "global_load_dwordx4 v[140:143], v227, s[60:61] sc1\n\t"
      "s_waitcnt vmcnt(6)\n\t"
      "v_max3_u32 v234, v144, v145, v146\n\t"
      "v_max_u32_e32 v234, v234, v147\n\t"
      "v_cmp_eq_u32_e32 vcc, -1, v234\n\t"
      "s_cbranch_vccnz FP25L_%=\n\t"
      "FP25X_%=:\n\t"
      "s_waitcnt vmcnt(0)\n\t"
      "s_branch FP25B_%=\n\t"
      "FP26R_%=:\n\t"
      "s_mov_b32 s51, 0\n\t"
      "FP26L_%=:\n\t"
      "s_add_u32 s51, s51, 1\n\t"
      "s_cmp_lt_u32 s51, 262144\n\t"
      "s_cbranch_scc0 FP26X_%=\n\t"
      "s_waitcnt vmcnt(0)\n\t"
      "s_sleep 4\n\t"
      "global_load_dwordx4 v[148:151], v229, s[58:59] sc1\n\t"
      "global_load_dwordx4 v[152:155], v230, s[58:59] sc1\n\t"
      "global_load_dwordx4 v[128:131], v224, s[60:61] sc1\n\t"
      "global_load_dwordx4 v[132:135], v225, s[60:61] sc1\n\t"
      "global_load_dwordx4 v[136:139], v226, s[60:61] sc1\n\t"
      "global_load_dwordx4 v[140:143], v227, s[60:61] sc1\n\t"
      "global_load_dwordx4 v[144:147], v228, s[60:61] sc1\n\t"
      "s_waitcnt vmcnt(6)\n\t"
      "v_max3_u32 v234, v148, v149, v150\n\t"
      "v_max_u32_e32 v234, v234, v151\n\t"
      "v_cmp_eq_u32_e32 vcc, -1, v234\n\t"
      "s_cbranch_vccnz FP26L_%=\n\t"
      "FP26X_%=:\n\t"
      "s_branch FP26B_%=\n\t"
      "FP27R_%=:\n\t"
      "s_mov_b32 s51, 0\n\t"
      "FP27L_%=:\n\t"
      "s_add_u32 s51, s51, 1\n\t"
      "s_cmp_lt_u32 s51, 262144\n\t"
      "s_cbranch_scc0 FP27X_%=\n\t"
      "s_waitcnt vmcnt(0)\n\t"
      "s_sleep 4\n\t"
      "global_load_dwordx4 v[152:155], v230, s[58:59] sc1\n\t"
      "global_load_dwordx4 v[128:131], v224, s[60:61] sc1\n\t"
      "global_load_dwordx4 v[132:135], v225, s[60:61] sc1\n\t"
      "global_load_dwordx4 v[136:139], v226, s[60:61] sc1\n\t"
      "global_load_dwordx4 v[140:143], v227, s[60:61] sc1\n\t"
      "global_load_dwordx4 v[144:147], v228, s[60:61] sc1\n\t"
      "global_load_dwordx4 v[148:151], v229, s[60:61] sc1\n\t"
      "s_waitcnt vmcnt(6)\n\t"
      "v_max3_u32 v234, v152, v153, v154\n\t"
      "v_max_u32_e32 v234, v234, v155\n\t"
      "v_cmp_eq_u32_e32 vcc, -1, v234\n\t"
      "s_cbranch_vccnz FP27L_%=\n\t"
      "FP27X_%=:\n\t"
      "s_waitcnt vmcnt(0)\n\t"
      "s_branch FP27B_%=\n\t"
      "NP3R_%=:\n\t"
      "s_mov_b32 s51, 0\n\t"
      "NP3L_%=:\n\t"
      "s_add_u32 s51, s51, 1\n\t"
      "s_cmp_lt_u32 s51, 262144\n\t"
      "s_cbranch_scc0 NP3X_%=\n\t"
      "s_waitcnt vmcnt(0)\n\t"
      "s_sleep 1\n\t"
      "global_load_dwordx4 v[128:131], v224, s[58:59] sc1\n\t"
      "global_load_dwordx4 v[132:135], v225, s[58:59] sc1\n\t"
      "global_load_dwordx4 v[136:139], v226, s[58:59] sc1\n\t"
      "global_load_dwordx4 v[140:143], v227, s[58:59] sc1\n\t"
      "global_load_dwordx4 v[144:147], v228, s[58:59] sc1\n\t"
      "global_load_dwordx4 v[148:151], v229, s[58:59] sc1\n\t"
      "global_load_dwordx4 v[152:155], v230, s[58:59] sc1\n\t"
      "s_waitcnt vmcnt(0)\n\t"
      "v_max3_u32 v234, v128, v129, v130\n\t"
      "v_max3_u32 v234, v234, v131, v132\n\t"
      "v_max3_u32 v234, v234, v133, v134\n\t"
      "v_max3_u32 v234, v234, v135, v136\n\t"
      "v_max3_u32 v234, v234, v137, v138\n\t"
      "v_max3_u32 v234, v234, v139, v140\n\t"
      "v_max3_u32 v234, v234, v141, v142\n\t"
      "v_max3_u32 v234, v234, v143, v144\n\t"
      "v_max3_u32 v234, v234, v145, v146\n\t"
      "v_max3_u32 v234, v234, v147, v148\n\t"
      "v_max3_u32 v234, v234, v149, v150\n\t"
      "v_max3_u32 v234, v234, v151, v152\n\t"
      "v_max3_u32 v234, v234, v153, v154\n\t"
      "v_max_u32_e32 v234, v234, v155\n\t"
      "v_cmp_eq_u32_e32 vcc, -1, v234\n\t"
      "s_cbranch_vccnz NP3L_%=\n\t"
      "NP3X_%=:\n\t"
      "v_mfma_f32_16x16x32_bf16 v[200:203], v[128:131], v[0:3], 0\n\t"
      "v_mfma_f32_16x16x32_bf16 v[204:207], v[128:131], v[4:7], 0\n\t"
      "v_mfma_f32_16x16x32_bf16 v[208:211], v[128:131], v[8:11], 0\n\t"
      "v_mfma_f32_16x16x32_bf16 v[212:215], v[128:131], v[12:15], 0\n\t"
      "v_mfma_f32_16x16x32_bf16 v[200:203], v[132:135], v[16:19], v[200:203]\n\t"
      "v_mfma_f32_16x16x32_bf16 v[204:207], v[132:135], v[20:23], v[204:207]\n\t"
      "v_mfma_f32_16x16x32_bf16 v[208:211], v[132:135], v[24:27], v[208:211]\n\t"
      "v_mfma_f32_16x16x32_bf16 v[212:215], v[132:135], v[28:31], v[212:215]\n\t"
      "v_mfma_f32_16x16x32_bf16 v[200:203], v[136:139], v[32:35], v[200:203]\n\t"
      "v_mfma_f32_16x16x32_bf16 v[204:207], v[136:139], v[36:39], v[204:207]\n\t"
      "v_mfma_f32_16x16x32_bf16 v[208:211], v[136:139], v[40:43], v[208:211]\n\t"
      "v_mfma_f32_16x16x32_bf16 v[212:215], v[136:139], v[44:47], v[212:215]\n\t"
      "v_mfma_f32_16x16x32_bf16 v[200:203], v[140:143], v[48:51], v[200:203]\n\t"
      "v_mfma_f32_16x16x32_bf16 v[204:207], v[140:143], v[52:55], v[204:207]\n\t"
      "v_mfma_f32_16x16x32_bf16 v[208:211], v[140:143], v[56:59], v[208:211]\n\t"
      "v_mfma_f32_16x16x32_bf16 v[212:215], v[140:143], v[60:63], v[212:215]\n\t"
      "v_mfma_f32_16x16x32_bf16 v[200:203], v[144:147], v[64:67], v[200:203]\n\t"
      "v_mfma_f32_16x16x32_bf16 v[204:207], v[144:147], v[68:71], v[204:207]\n\t"
      "v_mfma_f32_16x16x32_bf16 v[208:211], v[144:147], v[72:75], v[208:211]\n\t"
      "v_mfma_f32_16x16x32_bf16 v[212:215], v[144:147], v[76:79], v[212:215]\n\t"
      "v_mfma_f32_16x16x32_bf16 v[200:203], v[148:151], v[80:83], v[200:203]\n\t"
      "v_mfma_f32_16x16x32_bf16 v[204:207], v[148:151], v[84:87], v[204:207]\n\t"
      "v_mfma_f32_16x16x32_bf16 v[208:211], v[148:151], v[88:91], v[208:211]\n\t"
      "v_mfma_f32_16x16x32_bf16 v[212:215], v[148:151], v[92:95], v[212:215]\n\t"
      "v_mfma_f32_16x16x32_bf16 v[200:203], v[152:155], v[96:99], v[200:203]\n\t"
      "v_mfma_f32_16x16x32_bf16 v[204:207], v[152:155], v[100:103], v[204:207]\n\t"
      "v_mfma_f32_16x16x32_bf16 v[208:211], v[152:155], v[104:107], v[208:211]\n\t"
      "v_mfma_f32_16x16x32_bf16 v[212:215], v[152:155], v[108:111], v[212:215]\n\t"
      "global_load_dwordx4 v[128:131], v224, s[60:61] sc1\n\t"
      "global_load_dwordx4 v[132:135], v225, s[60:61] sc1\n\t"
      "global_load_dwordx4 v[136:139], v226, s[60:61] sc1\n\t"
      "global_load_dwordx4 v[140:143], v227, s[60:61] sc1\n\t"
      "global_load_dwordx4 v[144:147], v228, s[60:61] sc1\n\t"
      "global_load_dwordx4 v[148:151], v229, s[60:61] sc1\n\t"
      "global_load_dwordx4 v[152:155], v230, s[60:61] sc1\n\t"
      "s_waitcnt vmcnt(0)\n\t"
      "s_nop 7\n\t"
      "s_branch NP3B_%=\n\t"
      "FP28R_%=:\n\t"
      "s_mov_b32 s51, 0\n\t"
      "FP28L_%=:\n\t"
      "s_add_u32 s51, s51, 1\n\t"
      "s_cmp_lt_u32 s51, 262144\n\t"
      "s_cbranch_scc0 FP28X_%=\n\t"
      "s_waitcnt vmcnt(0)\n\t"
      "s_sleep 4\n\t"
      "global_load_dwordx4 v[128:131], v224, s[60:61] sc1\n\t"
      "global_load_dwordx4 v[132:135], v225, s[60:61] sc1\n\t"
      "global_load_dwordx4 v[136:139], v226, s[60:61] sc1\n\t"
      "global_load_dwordx4 v[140:143], v227, s[60:61] sc1\n\t"
      "global_load_dwordx4 v[144:147], v228, s[60:61] sc1\n\t"
      "global_load_dwordx4 v[148:151], v229, s[60:61] sc1\n\t"
      "global_load_dwordx4 v[152:155], v230, s[60:61] sc1\n\t"
      "s_waitcnt vmcnt(6)\n\t"
      "v_max3_u32 v234, v128, v129, v130\n\t"
      "v_max_u32_e32 v234, v234, v131\n\t"
      "v_cmp_eq_u32_e32 vcc, -1, v234\n\t"
      "s_cbranch_vccnz FP28L_%=\n\t"
      "FP28X_%=:\n\t"
      "s_waitcnt vmcnt(0)\n\t"
      "s_branch FP28B_%=\n\t"
      "FP29R_%=:\n\t"
      "s_mov_b32 s51, 0\n\t"
      "FP29L_%=:\n\t"
      "s_add_u32 s51, s51, 1\n\t"
      "s_cmp_lt_u32 s51, 262144\n\t"
      "s_cbranch_scc0 FP29X_%=\n\t"
      "s_waitcnt vmcnt(0)\n\t"
      "s_sleep 4\n\t"
      "global_load_dwordx4 v[132:135], v225, s[60:61] sc1\n\t"
      "global_load_dwordx4 v[136:139], v226, s[60:61] sc1\n\t"
      "global_load_dwordx4 v[140:143], v227, s[60:61] sc1\n\t"
      "global_load_dwordx4 v[144:147], v228, s[60:61] sc1\n\t"
      "global_load_dwordx4 v[148:151], v229, s[60:61] sc1\n\t"
      "global_load_dwordx4 v[152:155], v230, s[60:61] sc1\n\t"
      "global_load_dwordx4 v[128:131], v224, s[62:63] sc1\n\t"
      "s_waitcnt vmcnt(6)\n\t"
      "v_max3_u32 v234, v132, v133, v134\n\t"
      "v_max_u32_e32 v234, v234, v135\n\t"
      "v_cmp_eq_u32_e32 vcc, -1, v234\n\t"
      "s_cbranch_vccnz FP29L_%=\n\t"
      "FP29X_%=:\n\t"
      "s_waitcnt vmcnt(0)\n\t"
      "s_branch FP29B_%=\n\t"
      "FP30R_%=:\n\t"
      "s_mov_b32 s51, 0\n\t"
      "FP30L_%=:\n\t"
      "s_add_u32 s51, s51, 1\n\t"
      "s_cmp_lt_u32 s51, 262144\n\t"
      "s_cbranch_scc0 FP30X_%=\n\t"
      "s_waitcnt vmcnt(0)\n\t"
      "s_sleep 4\n\t"
      "global_load_dwordx4 v[136:139], v226, s[60:61] sc1\n\t"
      "global_load_dwordx4 v[140:143], v227, s[60:61] sc1\n\t"
      "global_load_dwordx4 v[144:147], v228, s[60:61] sc1\n\t"
      "global_load_dwordx4 v[148:151], v229, s[60:61] sc1\n\t"
      "global_load_dwordx4 v[152:155], v230, s[60:61] sc1\n\t"
      "global_load_dwordx4 v[128:131], v224, s[62:63] sc1\n\t"
      "global_load_dwordx4 v[132:135], v225, s[62:63] sc1\n\t"
      "s_waitcnt vmcnt(6)\n\t"
      "v_max3_u32 v234, v136, v137, v138\n\t"
      "v_max_u32_e32 v234, v234, v139\n\t"
      "v_cmp_eq_u32_e32 vcc, -1, v234\n\t"
      "s_cbranch_vccnz FP30L_%=\n\t"
      "FP30X_%=:\n\t"
      "s_waitcnt vmcnt(0)\n\t"
      "s_branch FP30B_%=\n\t"
      "FP31R_%=:\n\t"
      "s_mov_b32 s51, 0\n\t"
      "FP31L_%=:\n\t"
      "s_add_u32 s51, s51, 1\n\t"
      "s_cmp_lt_u32 s51, 262144\n\t"
      "s_cbranch_scc0 FP31X_%=\n\t"
      "s_waitcnt vmcnt(0)\n\t"
      "s_sleep 4\n\t"
      "global_load_dwordx4 v[140:143], v227, s[60:61] sc1\n\t"
      "global_load_dwordx4 v[144:147], v228, s[60:61] sc1\n\t"
      "global_load_dwordx4 v[148:151], v229, s[60:61] sc1\n\t"
      "global_load_dwordx4 v[152:155], v230, s[60:61] sc1\n\t"
      "global_load_dwordx4 v[128:131], v224, s[62:63] sc1\n\t"
      "global_load_dwordx4 v[132:135], v225, s[62:63] sc1\n\t"
      "global_load_dwordx4 v[136:139], v226, s[62:63] sc1\n\t"
      "s_waitcnt vmcnt(6)\n\t"
      "v_max3_u32 v234, v140, v141, v142\n\t"
      "v_max_u32_e32 v234, v234, v143\n\t"
      "v_cmp_eq_u32_e32 vcc, -1, v234\n\t"
      "s_cbranch_vccnz FP31L_%=\n\t"
      "FP31X_%=:\n\t"
      "s_waitcnt vmcnt(0)\n\t"
      "s_branch FP31B_%=\n\t"
      "FP32R_%=:\n\t"
      "s_mov_b32 s51, 0\n\t"
      "FP32L_%=:\n\t"
      "s_add_u32 s51, s51, 1\n\t"
      "s_cmp_lt_u32 s51, 262144\n\t"
      "s_cbranch_scc0 FP32X_%=\n\t"
      "s_waitcnt vmcnt(0)\n\t"
      "s_sleep 4\n\t"
      "global_load_dwordx4 v[144:147], v228, s[60:61] sc1\n\t"
      "global_load_dwordx4 v[148:151], v229, s[60:61] sc1\n\t"
      "global_load_dwordx4 v[152:155], v230, s[60:61] sc1\n\t"
      "global_load_dwordx4 v[128:131], v224, s[62:63] sc1\n\t"
      "global_load_dwordx4 v[132:135], v225, s[62:63] sc1\n\t"
      "global_load_dwordx4 v[136:139], v226, s[62:63] sc1\n\t"
      "global_load_dwordx4 v[140:143], v227, s[62:63] sc1\n\t"
      "s_waitcnt vmcnt(6)\n\t"
      "v_max3_u32 v234, v144, v145, v146\n\t"
      "v_max_u32_e32 v234, v234, v147\n\t"
      "v_cmp_eq_u32_e32 vcc, -1, v234\n\t"
      "s_cbranch_vccnz FP32L_%=\n\t"
      "FP32X_%=:\n\t"
      "s_waitcnt vmcnt(0)\n\t"
      "s_branch FP32B_%=\n\t"
      "FP33R_%=:\n\t"
      "s_mov_b32 s51, 0\n\t"
      "FP33L_%=:\n\t"
      "s_add_u32 s51, s51, 1\n\t"
      "s_cmp_lt_u32 s51, 262144\n\t"
      "s_cbranch_scc0 FP33X_%=\n\t"
      "s_waitcnt vmcnt(0)\n\t"
      "s_sleep 4\n\t"
      "global_load_dwordx4 v[148:151], v229, s[60:61] sc1\n\t"
      "global_load_dwordx4 v[152:155], v230, s[60:61] sc1\n\t"
      "global_load_dwordx4 v[128:131], v224, s[62:63] sc1\n\t"
      "global_load_dwordx4 v[132:135], v225, s[62:63] sc1\n\t"
      "global_load_dwordx4 v[136:139], v226, s[62:63] sc1\n\t"
      "global_load_dwordx4 v[140:143], v227, s[62:63] sc1\n\t"
      "global_load_dwordx4 v[144:147], v228, s[62:63] sc1\n\t"
      "s_waitcnt vmcnt(6)\n\t"
      "v_max3_u32 v234, v148, v149, v150\n\t"
      "v_max_u32_e32 v234, v234, v151\n\t"
      "v_cmp_eq_u32_e32 vcc, -1, v234\n\t"
      "s_cbranch_vccnz FP33L_%=\n\t"
      "FP33X_%=:\n\t"
      "s_branch FP33B_%=\n\t"
      "FP34R_%=:\n\t"
      "s_mov_b32 s51, 0\n\t"
      "FP34L_%=:\n\t"
      "s_add_u32 s51, s51, 1\n\t"
      "s_cmp_lt_u32 s51, 262144\n\t"
      "s_cbranch_scc0 FP34X_%=\n\t"
      "s_waitcnt vmcnt(0)\n\t"
      "s_sleep 4\n\t"
      "global_load_dwordx4 v[152:155], v230, s[60:61] sc1\n\t"
      "global_load_dwordx4 v[128:131], v224, s[62:63] sc1\n\t"
      "global_load_dwordx4 v[132:135], v225, s[62:63] sc1\n\t"
      "global_load_dwordx4 v[136:139], v226, s[62:63] sc1\n\t"
      "global_load_dwordx4 v[140:143], v227, s[62:63] sc1\n\t"
      "global_load_dwordx4 v[144:147], v228, s[62:63] sc1\n\t"
      "global_load_dwordx4 v[148:151], v229, s[62:63] sc1\n\t"
      "s_waitcnt vmcnt(6)\n\t"
      "v_max3_u32 v234, v152, v153, v154\n\t"
      "v_max_u32_e32 v234, v234, v155\n\t"
      "v_cmp_eq_u32_e32 vcc, -1, v234\n\t"
      "s_cbranch_vccnz FP34L_%=\n\t"
      "FP34X_%=:\n\t"
      "s_waitcnt vmcnt(0)\n\t"
      "s_branch FP34B_%=\n\t"
      "NP4R_%=:\n\t"
      "s_mov_b32 s51, 0\n\t"
      "NP4L_%=:\n\t"
      "s_add_u32 s51, s51, 1\n\t"
      "s_cmp_lt_u32 s51, 262144\n\t"
      "s_cbranch_scc0 NP4X_%=\n\t"
      "s_waitcnt vmcnt(0)\n\t"
      "s_sleep 1\n\t"
      "global_load_dwordx4 v[128:131], v224, s[60:61] sc1\n\t"
      "global_load_dwordx4 v[132:135], v225, s[60:61] sc1\n\t"
      "global_load_dwordx4 v[136:139], v226, s[60:61] sc1\n\t"
      "global_load_dwordx4 v[140:143], v227, s[60:61] sc1\n\t"
      "global_load_dwordx4 v[144:147], v228, s[60:61] sc1\n\t"
      "global_load_dwordx4 v[148:151], v229, s[60:61] sc1\n\t"
      "global_load_dwordx4 v[152:155], v230, s[60:61] sc1\n\t"
      "s_waitcnt vmcnt(0)\n\t"
      "v_max3_u32 v234, v128, v129, v130\n\t"
      "v_max3_u32 v234, v234, v131, v132\n\t"
      "v_max3_u32 v234, v234, v133, v134\n\t"
      "v_max3_u32 v234, v234, v135, v136\n\t"
      "v_max3_u32 v234, v234, v137, v138\n\t"
      "v_max3_u32 v234, v234, v139, v140\n\t"
      "v_max3_u32 v234, v234, v141, v142\n\t"
      "v_max3_u32 v234, v234, v143, v144\n\t"
      "v_max3_u32 v234, v234, v145, v146\n\t"
      "v_max3_u32 v234, v234, v147, v148\n\t"
      "v_max3_u32 v234, v234, v149, v150\n\t"
      "v_max3_u32 v234, v234, v151, v152\n\t"
      "v_max3_u32 v234, v234, v153, v154\n\t"
      "v_max_u32_e32 v234, v234, v155\n\t"
      "v_cmp_eq_u32_e32 vcc, -1, v234\n\t"
      "s_cbranch_vccnz NP4L_%=\n\t"
      "NP4X_%=:\n\t"
      "v_mfma_f32_16x16x32_bf16 v[184:187], v[128:131], v[0:3], 0\n\t"
      "v_mfma_f32_16x16x32_bf16 v[188:191], v[128:131], v[4:7], 0\n\t"
      "v_mfma_f32_16x16x32_bf16 v[192:195], v[128:131], v[8:11], 0\n\t"
      "v_mfma_f32_16x16x32_bf16 v[196:199], v[128:131], v[12:15], 0\n\t"
      "v_mfma_f32_16x16x32_bf16 v[184:187], v[132:135], v[16:19], v[184:187]\n\t"
      "v_mfma_f32_16x16x32_bf16 v[188:191], v[132:135], v[20:23], v[188:191]\n\t"
      "v_mfma_f32_16x16x32_bf16 v[192:195], v[132:135], v[24:27], v[192:195]\n\t"
      "v_mfma_f32_16x16x32_bf16 v[196:199], v[132:135], v[28:31], v[196:199]\n\t"
      "v_mfma_f32_16x16x32_bf16 v[184:187], v[136:139], v[32:35], v[184:187]\n\t"
      "v_mfma_f32_16x16x32_bf16 v[188:191], v[136:139], v[36:39], v[188:191]\n\t"
      "v_mfma_f32_16x16x32_bf16 v[192:195], v[136:139], v[40:43], v[192:195]\n\t"
      "v_mfma_f32_16x16x32_bf16 v[196:199], v[136:139], v[44:47], v[196:199]\n\t"
      "v_mfma_f32_16x16x32_bf16 v[184:187], v[140:143], v[48:51], v[184:187]\n\t"
      "v_mfma_f32_16x16x32_bf16 v[188:191], v[140:143], v[52:55], v[188:191]\n\t"
      "v_mfma_f32_16x16x32_bf16 v[192:195], v[140:143], v[56:59], v[192:195]\n\t"
      "v_mfma_f32_16x16x32_bf16 v[196:199], v[140:143], v[60:63], v[196:199]\n\t"
      "v_mfma_f32_16x16x32_bf16 v[184:187], v[144:147], v[64:67], v[184:187]\n\t"
      "v_mfma_f32_16x16x32_bf16 v[188:191], v[144:147], v[68:71], v[188:191]\n\t"
      "v_mfma_f32_16x16x32_bf16 v[192:195], v[144:147], v[72:75], v[192:195]\n\t"
      "v_mfma_f32_16x16x32_bf16 v[196:199], v[144:147], v[76:79], v[196:199]\n\t"
      "v_mfma_f32_16x16x32_bf16 v[184:187], v[148:151], v[80:83], v[184:187]\n\t"
      "v_mfma_f32_16x16x32_bf16 v[188:191], v[148:151], v[84:87], v[188:191]\n\t"
      "v_mfma_f32_16x16x32_bf16 v[192:195], v[148:151], v[88:91], v[192:195]\n\t"
      "v_mfma_f32_16x16x32_bf16 v[196:199], v[148:151], v[92:95], v[196:199]\n\t"
      "v_mfma_f32_16x16x32_bf16 v[184:187], v[152:155], v[96:99], v[184:187]\n\t"
      "v_mfma_f32_16x16x32_bf16 v[188:191], v[152:155], v[100:103], v[188:191]\n\t"
      "v_mfma_f32_16x16x32_bf16 v[192:195], v[152:155], v[104:107], v[192:195]\n\t"
      "v_mfma_f32_16x16x32_bf16 v[196:199], v[152:155], v[108:111], v[196:199]\n\t"
      "global_load_dwordx4 v[128:131], v224, s[62:63] sc1\n\t"
      "global_load_dwordx4 v[132:135], v225, s[62:63] sc1\n\t"
      "global_load_dwordx4 v[136:139], v226, s[62:63] sc1\n\t"
      "global_load_dwordx4 v[140:143], v227, s[62:63] sc1\n\t"
      "global_load_dwordx4 v[144:147], v228, s[62:63] sc1\n\t"
      "global_load_dwordx4 v[148:151], v229, s[62:63] sc1\n\t"
      "global_load_dwordx4 v[152:155], v230, s[62:63] sc1\n\t"
      "s_waitcnt vmcnt(0)\n\t"
      "s_nop 7\n\t"
      "s_branch NP4B_%=\n\t"
      "FP35R_%=:\n\t"
      "s_mov_b32 s51, 0\n\t"
      "FP35L_%=:\n\t"
      "s_add_u32 s51, s51, 1\n\t"
      "s_cmp_lt_u32 s51, 262144\n\t"
      "s_cbranch_scc0 FP35X_%=\n\t"
      "s_waitcnt vmcnt(0)\n\t"
      "s_sleep 4\n\t"
      "global_load_dwordx4 v[128:131], v224, s[62:63] sc1\n\t"
      "global_load_dwordx4 v[132:135], v225, s[62:63] sc1\n\t"
      "global_load_dwordx4 v[136:139], v226, s[62:63] sc1\n\t"
      "global_load_dwordx4 v[140:143], v227, s[62:63] sc1\n\t"
      "global_load_dwordx4 v[144:147], v228, s[62:63] sc1\n\t"
      "global_load_dwordx4 v[148:151], v229, s[62:63] sc1\n\t"
      "global_load_dwordx4 v[152:155], v230, s[62:63] sc1\n\t"
      "s_waitcnt vmcnt(6)\n\t"
      "v_max3_u32 v234, v128, v129, v130\n\t"
      "v_max_u32_e32 v234, v234, v131\n\t"
      "v_cmp_eq_u32_e32 vcc, -1, v234\n\t"
      "s_cbranch_vccnz FP35L_%=\n\t"
      "FP35X_%=:\n\t"
      "s_waitcnt vmcnt(0)\n\t"
      "s_branch FP35B_%=\n\t"
      "FP36R_%=:\n\t"
      "s_mov_b32 s51, 0\n\t"
      "FP36L_%=:\n\t"
      "s_add_u32 s51, s51, 1\n\t"
      "s_cmp_lt_u32 s51, 262144\n\t"
      "s_cbranch_scc0 FP36X_%=\n\t"
      "s_waitcnt vmcnt(0)\n\t"
      "s_sleep 4\n\t"
      "global_load_dwordx4 v[132:135], v225, s[62:63] sc1\n\t"
      "global_load_dwordx4 v[136:139], v226, s[62:63] sc1\n\t"
      "global_load_dwordx4 v[140:143], v227, s[62:63] sc1\n\t"
      "global_load_dwordx4 v[144:147], v228, s[62:63] sc1\n\t"
      "global_load_dwordx4 v[148:151], v229, s[62:63] sc1\n\t"
      "global_load_dwordx4 v[152:155], v230, s[62:63] sc1\n\t"
      "global_load_dwordx4 v[128:131], v224, s[64:65] sc1\n\t"
      "s_waitcnt vmcnt(6)\n\t"
      "v_max3_u32 v234, v132, v133, v134\n\t"
      "v_max_u32_e32 v234, v234, v135\n\t"
      "v_cmp_eq_u32_e32 vcc, -1, v234\n\t"
      "s_cbranch_vccnz FP36L_%=\n\t"
      "FP36X_%=:\n\t"
      "s_waitcnt vmcnt(0)\n\t"
      "s_branch FP36B_%=\n\t"
      "FP37R_%=:\n\t"
      "s_mov_b32 s51, 0\n\t"
      "FP37L_%=:\n\t"
      "s_add_u32 s51, s51, 1\n\t"
      "s_cmp_lt_u32 s51, 262144\n\t"
      "s_cbranch_scc0 FP37X_%=\n\t"
      "s_waitcnt vmcnt(0)\n\t"
      "s_sleep 4\n\t"
      "global_load_dwordx4 v[136:139], v226, s[62:63] sc1\n\t"
      "global_load_dwordx4 v[140:143], v227, s[62:63] sc1\n\t"
      "global_load_dwordx4 v[144:147], v228, s[62:63] sc1\n\t"
      "global_load_dwordx4 v[148:151], v229, s[62:63] sc1\n\t"
      "global_load_dwordx4 v[152:155], v230, s[62:63] sc1\n\t"
      "global_load_dwordx4 v[128:131], v224, s[64:65] sc1\n\t"
      "global_load_dwordx4 v[132:135], v225, s[64:65] sc1\n\t"
      "s_waitcnt vmcnt(6)\n\t"
      "v_max3_u32 v234, v136, v137, v138\n\t"
      "v_max_u32_e32 v234, v234, v139\n\t"
      "v_cmp_eq_u32_e32 vcc, -1, v234\n\t"
      "s_cbranch_vccnz FP37L_%=\n\t"
      "FP37X_%=:\n\t"
      "s_waitcnt vmcnt(0)\n\t"
      "s_branch FP37B_%=\n\t"
      "FP38R_%=:\n\t"
      "s_mov_b32 s51, 0\n\t"
      "FP38L_%=:\n\t"
      "s_add_u32 s51, s51, 1\n\t"
      "s_cmp_lt_u32 s51, 262144\n\t"
      "s_cbranch_scc0 FP38X_%=\n\t"
      "s_waitcnt vmcnt(0)\n\t"
      "s_sleep 4\n\t"
      "global_load_dwordx4 v[140:143], v227, s[62:63] sc1\n\t"
      "global_load_dwordx4 v[144:147], v228, s[62:63] sc1\n\t"
      "global_load_dwordx4 v[148:151], v229, s[62:63] sc1\n\t"
      "global_load_dwordx4 v[152:155], v230, s[62:63] sc1\n\t"
      "global_load_dwordx4 v[128:131], v224, s[64:65] sc1\n\t"
      "global_load_dwordx4 v[132:135], v225, s[64:65] sc1\n\t"
      "global_load_dwordx4 v[136:139], v226, s[64:65] sc1\n\t"
      "s_waitcnt vmcnt(6)\n\t"
      "v_max3_u32 v234, v140, v141, v142\n\t"
      "v_max_u32_e32 v234, v234, v143\n\t"
      "v_cmp_eq_u32_e32 vcc, -1, v234\n\t"
      "s_cbranch_vccnz FP38L_%=\n\t"
      "FP38X_%=:\n\t"
      "s_waitcnt vmcnt(0)\n\t"
      "s_branch FP38B_%=\n\t"
      "FP39R_%=:\n\t"
      "s_mov_b32 s51, 0\n\t"
      "FP39L_%=:\n\t"
      "s_add_u32 s51, s51, 1\n\t"
      "s_cmp_lt_u32 s51, 262144\n\t"
      "s_cbranch_scc0 FP39X_%=\n\t"
      "s_waitcnt vmcnt(0)\n\t"
      "s_sleep 4\n\t"
      "global_load_dwordx4 v[144:147], v228, s[62:63] sc1\n\t"
      "global_load_dwordx4 v[148:151], v229, s[62:63] sc1\n\t"
      "global_load_dwordx4 v[152:155], v230, s[62:63] sc1\n\t"
      "global_load_dwordx4 v[128:131], v224, s[64:65] sc1\n\t"
      "global_load_dwordx4 v[132:135], v225, s[64:65] sc1\n\t"
      "global_load_dwordx4 v[136:139], v226, s[64:65] sc1\n\t"
      "global_load_dwordx4 v[140:143], v227, s[64:65] sc1\n\t"
      "s_waitcnt vmcnt(6)\n\t"
      "v_max3_u32 v234, v144, v145, v146\n\t"
      "v_max_u32_e32 v234, v234, v147\n\t"
      "v_cmp_eq_u32_e32 vcc, -1, v234\n\t"
      "s_cbranch_vccnz FP39L_%=\n\t"
      "FP39X_%=:\n\t"
      "s_waitcnt vmcnt(0)\n\t"
      "s_branch FP39B_%=\n\t"
      "FP40R_%=:\n\t"
      "s_mov_b32 s51, 0\n\t"
      "FP40L_%=:\n\t"
      "s_add_u32 s51, s51, 1\n\t"
      "s_cmp_lt_u32 s51, 262144\n\t"
      "s_cbranch_scc0 FP40X_%=\n\t"
      "s_waitcnt vmcnt(0)\n\t"
      "s_sleep 4\n\t"
      "global_load_dwordx4 v[148:151], v229, s[62:63] sc1\n\t"
      "global_load_dwordx4 v[152:155], v230, s[62:63] sc1\n\t"
      "global_load_dwordx4 v[128:131], v224, s[64:65] sc1\n\t"
      "global_load_dwordx4 v[132:135], v225, s[64:65] sc1\n\t"
      "global_load_dwordx4 v[136:139], v226, s[64:65] sc1\n\t"
      "global_load_dwordx4 v[140:143], v227, s[64:65] sc1\n\t"
      "global_load_dwordx4 v[144:147], v228, s[64:65] sc1\n\t"
      "s_waitcnt vmcnt(6)\n\t"
      "v_max3_u32 v234, v148, v149, v150\n\t"
      "v_max_u32_e32 v234, v234, v151\n\t"
      "v_cmp_eq_u32_e32 vcc, -1, v234\n\t"
      "s_cbranch_vccnz FP40L_%=\n\t"
      "FP40X_%=:\n\t"
      "s_branch FP40B_%=\n\t"
      "FP41R_%=:\n\t"
      "s_mov_b32 s51, 0\n\t"
      "FP41L_%=:\n\t"
      "s_add_u32 s51, s51, 1\n\t"
      "s_cmp_lt_u32 s51, 262144\n\t"
      "s_cbranch_scc0 FP41X_%=\n\t"
      "s_waitcnt vmcnt(0)\n\t"
      "s_sleep 4\n\t"
      "global_load_dwordx4 v[152:155], v230, s[62:63] sc1\n\t"
      "global_load_dwordx4 v[128:131], v224, s[64:65] sc1\n\t"
      "global_load_dwordx4 v[132:135], v225, s[64:65] sc1\n\t"
      "global_load_dwordx4 v[136:139], v226, s[64:65] sc1\n\t"
      "global_load_dwordx4 v[140:143], v227, s[64:65] sc1\n\t"
      "global_load_dwordx4 v[144:147], v228, s[64:65] sc1\n\t"
      "global_load_dwordx4 v[148:151], v229, s[64:65] sc1\n\t"
      "s_waitcnt vmcnt(6)\n\t"
      "v_max3_u32 v234, v152, v153, v154\n\t"
      "v_max_u32_e32 v234, v234, v155\n\t"
      "v_cmp_eq_u32_e32 vcc, -1, v234\n\t"
      "s_cbranch_vccnz FP41L_%=\n\t"
      "FP41X_%=:\n\t"
      "s_waitcnt vmcnt(0)\n\t"
      "s_branch FP41B_%=\n\t"
      "NP5R_%=:\n\t"
      "s_mov_b32 s51, 0\n\t"
      "NP5L_%=:\n\t"
      "s_add_u32 s51, s51, 1\n\t"
      "s_cmp_lt_u32 s51, 262144\n\t"
      "s_cbranch_scc0 NP5X_%=\n\t"
      "s_waitcnt vmcnt(0)\n\t"
      "s_sleep 1\n\t"
      "global_load_dwordx4 v[128:131], v224, s[62:63] sc1\n\t"
      "global_load_dwordx4 v[132:135], v225, s[62:63] sc1\n\t"
      "global_load_dwordx4 v[136:139], v226, s[62:63] sc1\n\t"
      "global_load_dwordx4 v[140:143], v227, s[62:63] sc1\n\t"
      "global_load_dwordx4 v[144:147], v228, s[62:63] sc1\n\t"
      "global_load_dwordx4 v[148:151], v229, s[62:63] sc1\n\t"
      "global_load_dwordx4 v[152:155], v230, s[62:63] sc1\n\t"
      "s_waitcnt vmcnt(0)\n\t"
      "v_max3_u32 v234, v128, v129, v130\n\t"
      "v_max3_u32 v234, v234, v131, v132\n\t"
      "v_max3_u32 v234, v234, v133, v134\n\t"
      "v_max3_u32 v234, v234, v135, v136\n\t"
      "v_max3_u32 v234, v234, v137, v138\n\t"
      "v_max3_u32 v234, v234, v139, v140\n\t"
      "v_max3_u32 v234, v234, v141, v142\n\t"
      "v_max3_u32 v234, v234, v143, v144\n\t"
      "v_max3_u32 v234, v234, v145, v146\n\t"
      "v_max3_u32 v234, v234, v147, v148\n\t"
      "v_max3_u32 v234, v234, v149, v150\n\t"
      "v_max3_u32 v234, v234, v151, v152\n\t"
      "v_max3_u32 v234, v234, v153, v154\n\t"
      "v_max_u32_e32 v234, v234, v155\n\t"
      "v_cmp_eq_u32_e32 vcc, -1, v234\n\t"
      "s_cbranch_vccnz NP5L_%=\n\t"
      "NP5X_%=:\n\t"
      "v_mfma_f32_16x16x32_bf16 v[200:203], v[128:131], v[0:3], 0\n\t"
      "v_mfma_f32_16x16x32_bf16 v[204:207], v[128:131], v[4:7], 0\n\t"
      "v_mfma_f32_16x16x32_bf16 v[208:211], v[128:131], v[8:11], 0\n\t"
      "v_mfma_f32_16x16x32_bf16 v[212:215], v[128:131], v[12:15], 0\n\t"
      "v_mfma_f32_16x16x32_bf16 v[200:203], v[132:135], v[16:19], v[200:203]\n\t"
      "v_mfma_f32_16x16x32_bf16 v[204:207], v[132:135], v[20:23], v[204:207]\n\t"
      "v_mfma_f32_16x16x32_bf16 v[208:211], v[132:135], v[24:27], v[208:211]\n\t"
      "v_mfma_f32_16x16x32_bf16 v[212:215], v[132:135], v[28:31], v[212:215]\n\t"
      "v_mfma_f32_16x16x32_bf16 v[200:203], v[136:139], v[32:35], v[200:203]\n\t"
      "v_mfma_f32_16x16x32_bf16 v[204:207], v[136:139], v[36:39], v[204:207]\n\t"
      "v_mfma_f32_16x16x32_bf16 v[208:211], v[136:139], v[40:43], v[208:211]\n\t"
      "v_mfma_f32_16x16x32_bf16 v[212:215], v[136:139], v[44:47], v[212:215]\n\t"
      "v_mfma_f32_16x16x32_bf16 v[200:203], v[140:143], v[48:51], v[200:203]\n\t"
      "v_mfma_f32_16x16x32_bf16 v[204:207], v[140:143], v[52:55], v[204:207]\n\t"
      "v_mfma_f32_16x16x32_bf16 v[208:211], v[140:143], v[56:59], v[208:211]\n\t"
      "v_mfma_f32_16x16x32_bf16 v[212:215], v[140:143], v[60:63], v[212:215]\n\t"
      "v_mfma_f32_16x16x32_bf16 v[200:203], v[144:147], v[64:67], v[200:203]\n\t"
      "v_mfma_f32_16x16x32_bf16 v[204:207], v[144:147], v[68:71], v[204:207]\n\t"
      "v_mfma_f32_16x16x32_bf16 v[208:211], v[144:147], v[72:75], v[208:211]\n\t"
      "v_mfma_f32_16x16x32_bf16 v[212:215], v[144:147], v[76:79], v[212:215]\n\t"
      "v_mfma_f32_16x16x32_bf16 v[200:203], v[148:151], v[80:83], v[200:203]\n\t"
      "v_mfma_f32_16x16x32_bf16 v[204:207], v[148:151], v[84:87], v[204:207]\n\t"
      "v_mfma_f32_16x16x32_bf16 v[208:211], v[148:151], v[88:91], v[208:211]\n\t"
      "v_mfma_f32_16x16x32_bf16 v[212:215], v[148:151], v[92:95], v[212:215]\n\t"
      "v_mfma_f32_16x16x32_bf16 v[200:203], v[152:155], v[96:99], v[200:203]\n\t"
      "v_mfma_f32_16x16x32_bf16 v[204:207], v[152:155], v[100:103], v[204:207]\n\t"
      "v_mfma_f32_16x16x32_bf16 v[208:211], v[152:155], v[104:107], v[208:211]\n\t"
      "v_mfma_f32_16x16x32_bf16 v[212:215], v[152:155], v[108:111], v[212:215]\n\t"
      "global_load_dwordx4 v[128:131], v224, s[64:65] sc1\n\t"
      "global_load_dwordx4 v[132:135], v225, s[64:65] sc1\n\t"
      "global_load_dwordx4 v[136:139], v226, s[64:65] sc1\n\t"
      "global_load_dwordx4 v[140:143], v227, s[64:65] sc1\n\t"
      "global_load_dwordx4 v[144:147], v228, s[64:65] sc1\n\t"
      "global_load_dwordx4 v[148:151], v229, s[64:65] sc1\n\t"
      "global_load_dwordx4 v[152:155], v230, s[64:65] sc1\n\t"
      "s_waitcnt vmcnt(0)\n\t"
      "s_nop 7\n\t"
      "s_branch NP5B_%=\n\t"
      "FP42R_%=:\n\t"
      "s_mov_b32 s51, 0\n\t"
      "FP42L_%=:\n\t"
      "s_add_u32 s51, s51, 1\n\t"
      "s_cmp_lt_u32 s51, 262144\n\t"
      "s_cbranch_scc0 FP42X_%=\n\t"
      "s_sleep 1\n\t"
      "s_mov_b64 exec, 15\n\t"
      "v_mov_b64_e32 v[232:233], 0\n\t"
      "global_atomic_or_x2 v[232:233], v247, v[232:233], s[12:13] sc0 sc1\n\t"
      "s_waitcnt vmcnt(0)\n\t"
      "v_subrev_u32_e32 v232, s14, v232\n\t"
      "v_cmp_ne_u32_e64 s[48:49], s15, v233\n\t"
      "v_subrev_u32_e32 v232, 6, v232\n\t"
      "v_cmp_lt_u32_e32 vcc, 7, v232\n\t"
      "s_mov_b64 exec, -1\n\t"
      "s_or_b64 vcc, vcc, s[48:49]\n\t"
      "s_cbranch_vccnz FP42L_%=\n\t"
      "FP42X_%=:\n\t"
      "FP42DR_%=:\n\t"
      "s_mov_b32 s51, 0\n\t"
      "FP42DL_%=:\n\t"
      "s_add_u32 s51, s51, 1\n\t"
      "s_cmp_lt_u32 s51, 262144\n\t"
      "s_cbranch_scc0 FP42DX_%=\n\t"
      "s_waitcnt vmcnt(0)\n\t"
      "s_sleep 1\n\t"
      "global_load_dwordx4 v[128:131], v224, s[64:65] sc1\n\t"
      "global_load_dwordx4 v[132:135], v225, s[64:65] sc1\n\t"
      "global_load_dwordx4 v[136:139], v226, s[64:65] sc1\n\t"
      "global_load_dwordx4 v[140:143], v227, s[64:65] sc1\n\t"
      "global_load_dwordx4 v[144:147], v228, s[64:65] sc1\n\t"
      "global_load_dwordx4 v[148:151], v229, s[64:65] sc1\n\t"
      "global_load_dwordx4 v[152:155], v230, s[64:65] sc1\n\t"
      "s_waitcnt vmcnt(6)\n\t"
      "v_max3_u32 v234, v128, v129, v130\n\t"
      "v_max_u32_e32 v234, v234, v131\n\t"
      "v_cmp_eq_u32_e32 vcc, -1, v234\n\t"
      "s_cbranch_vccnz FP42DL_%=\n\t"
      "FP42DX_%=:\n\t"
      "s_waitcnt vmcnt(0)\n\t"
      "s_branch FP42B_%=\n\t"
      "FP43R_%=:\n\t"
      "s_mov_b32 s51, 0\n\t"
      "FP43L_%=:\n\t"
      "s_add_u32 s51, s51, 1\n\t"
      "s_cmp_lt_u32 s51, 262144\n\t"
      "s_cbranch_scc0 FP43X_%=\n\t"
      "s_sleep 1\n\t"
      "s_mov_b64 exec, 15\n\t"
      "v_mov_b64_e32 v[232:233], 0\n\t"
      "global_atomic_or_x2 v[232:233], v247, v[232:233], s[12:13] sc0 sc1\n\t"
      "s_waitcnt vmcnt(0)\n\t"
      "v_subrev_u32_e32 v232, s14, v232\n\t"
      "v_cmp_ne_u32_e64 s[48:49], s15, v233\n\t"
      "v_subrev_u32_e32 v232, 6, v232\n\t"
      "v_cmp_lt_u32_e32 vcc, 7, v232\n\t"
      "s_mov_b64 exec, -1\n\t"
      "s_or_b64 vcc, vcc, s[48:49]\n\t"
      "s_cbranch_vccnz FP43L_%=\n\t"
      "FP43X_%=:\n\t"
      "FP43DR_%=:\n\t"
      "s_mov_b32 s51, 0\n\t"
      "FP43DL_%=:\n\t"
      "s_add_u32 s51, s51, 1\n\t"
      "s_cmp_lt_u32 s51, 262144\n\t"
      "s_cbranch_scc0 FP43DX_%=\n\t"
      "s_waitcnt vmcnt(0)\n\t"
      "s_sleep 1\n\t"
      "global_load_dwordx4 v[132:135], v225, s[64:65] sc1\n\t"
      "global_load_dwordx4 v[136:139], v226, s[64:65] sc1\n\t"
      "global_load_dwordx4 v[140:143], v227, s[64:65] sc1\n\t"
      "global_load_dwordx4 v[144:147], v228, s[64:65] sc1\n\t"
      "global_load_dwordx4 v[148:151], v229, s[64:65] sc1\n\t"
      "global_load_dwordx4 v[152:155], v230, s[64:65] sc1\n\t"
      "s_waitcnt vmcnt(5)\n\t"
      "v_max3_u32 v234, v132, v133, v134\n\t"
      "v_max_u32_e32 v234, v234, v135\n\t"
      "v_cmp_eq_u32_e32 vcc, -1, v234\n\t"
      "s_cbranch_vccnz FP43DL_%=\n\t"
      "FP43DX_%=:\n\t"
      "s_waitcnt vmcnt(0)\n\t"
      "s_branch FP43B_%=\n\t"
      "FP44R_%=:\n\t"
      "s_mov_b32 s51, 0\n\t"
      "FP44L_%=:\n\t"
      "s_add_u32 s51, s51, 1\n\t"
      "s_cmp_lt_u32 s51, 262144\n\t"
      "s_cbranch_scc0 FP44X_%=\n\t"
      "s_sleep 1\n\t"
      "s_mov_b64 exec, 15\n\t"
      "v_mov_b64_e32 v[232:233], 0\n\t"
      "global_atomic_or_x2 v[232:233], v247, v[232:233], s[12:13] sc0 sc1\n\t"
      "s_waitcnt vmcnt(0)\n\t"
      "v_subrev_u32_e32 v232, s14, v232\n\t"
      "v_cmp_ne_u32_e64 s[48:49], s15, v233\n\t"
      "v_subrev_u32_e32 v232, 6, v232\n\t"
      "v_cmp_lt_u32_e32 vcc, 7, v232\n\t"
      "s_mov_b64 exec, -1\n\t"
      "s_or_b64 vcc, vcc, s[48:49]\n\t"
      "s_cbranch_vccnz FP44L_%=\n\t"
      "FP44X_%=:\n\t"
      "FP44DR_%=:\n\t"
      "s_mov_b32 s51, 0\n\t"
      "FP44DL_%=:\n\t"
      "s_add_u32 s51, s51, 1\n\t"
      "s_cmp_lt_u32 s51, 262144\n\t"
      "s_cbranch_scc0 FP44DX_%=\n\t"
      "s_waitcnt vmcnt(0)\n\t"
      "s_sleep 1\n\t"
      "global_load_dwordx4 v[136:139], v226, s[64:65] sc1\n\t"
      "global_load_dwordx4 v[140:143], v227, s[64:65] sc1\n\t"
      "global_load_dwordx4 v[144:147], v228, s[64:65] sc1\n\t"
      "global_load_dwordx4 v[148:151], v229, s[64:65] sc1\n\t"
      "global_load_dwordx4 v[152:155], v230, s[64:65] sc1\n\t"
      "s_waitcnt vmcnt(4)\n\t"
      "v_max3_u32 v234, v136, v137, v138\n\t"
      "v_max_u32_e32 v234, v234, v139\n\t"
      "v_cmp_eq_u32_e32 vcc, -1, v234\n\t"
      "s_cbranch_vccnz FP44DL_%=\n\t"
      "FP44DX_%=:\n\t"
      "s_waitcnt vmcnt(0)\n\t"
      "s_branch FP44B_%=\n\t"
      "FP45R_%=:\n\t"
      "s_mov_b32 s51, 0\n\t"
      "FP45L_%=:\n\t"
      "s_add_u32 s51, s51, 1\n\t"
      "s_cmp_lt_u32 s51, 262144\n\t"
      "s_cbranch_scc0 FP45X_%=\n\t"
      "s_sleep 1\n\t"
      "s_mov_b64 exec, 15\n\t"
      "v_mov_b64_e32 v[232:233], 0\n\t"
      "global_atomic_or_x2 v[232:233], v247, v[232:233], s[12:13] sc0 sc1\n\t"
      "s_waitcnt vmcnt(0)\n\t"
      "v_subrev_u32_e32 v232, s14, v232\n\t"
      "v_cmp_ne_u32_e64 s[48:49], s15, v233\n\t"
      "v_subrev_u32_e32 v232, 6, v232\n\t"
      "v_cmp_lt_u32_e32 vcc, 7, v232\n\t"
      "s_mov_b64 exec, -1\n\t"
      "s_or_b64 vcc, vcc, s[48:49]\n\t"
      "s_cbranch_vccnz FP45L_%=\n\t"
      "FP45X_%=:\n\t"
      "FP45DR_%=:\n\t"
      "s_mov_b32 s51, 0\n\t"
      "FP45DL_%=:\n\t"
      "s_add_u32 s51, s51, 1\n\t"
      "s_cmp_lt_u32 s51, 262144\n\t"
      "s_cbranch_scc0 FP45DX_%=\n\t"
      "s_waitcnt vmcnt(0)\n\t"
      "s_sleep 1\n\t"
      "global_load_dwordx4 v[140:143], v227, s[64:65] sc1\n\t"
      "global_load_dwordx4 v[144:147], v228, s[64:65] sc1\n\t"
      "global_load_dwordx4 v[148:151], v229, s[64:65] sc1\n\t"
      "global_load_dwordx4 v[152:155], v230, s[64:65] sc1\n\t"
      "s_waitcnt vmcnt(3)\n\t"
      "v_max3_u32 v234, v140, v141, v142\n\t"
      "v_max_u32_e32 v234, v234, v143\n\t"
      "v_cmp_eq_u32_e32 vcc, -1, v234\n\t"
      "s_cbranch_vccnz FP45DL_%=\n\t"
      "FP45DX_%=:\n\t"
      "s_waitcnt vmcnt(0)\n\t"
      "s_branch FP45B_%=\n\t"
      "FP46R_%=:\n\t"
      "s_mov_b32 s51, 0\n\t"
      "FP46L_%=:\n\t"
      "s_add_u32 s51, s51, 1\n\t"
      "s_cmp_lt_u32 s51, 262144\n\t"
      "s_cbranch_scc0 FP46X_%=\n\t"
      "s_sleep 1\n\t"
      "s_mov_b64 exec, 15\n\t"
      "v_mov_b64_e32 v[232:233], 0\n\t"
      "global_atomic_or_x2 v[232:233], v247, v[232:233], s[12:13] sc0 sc1\n\t"
      "s_waitcnt vmcnt(0)\n\t"
      "v_subrev_u32_e32 v232, s14, v232\n\t"
      "v_cmp_ne_u32_e64 s[48:49], s15, v233\n\t"
      "v_subrev_u32_e32 v232, 6, v232\n\t"
      "v_cmp_lt_u32_e32 vcc, 7, v232\n\t"
      "s_mov_b64 exec, -1\n\t"
      "s_or_b64 vcc, vcc, s[48:49]\n\t"
      "s_cbranch_vccnz FP46L_%=\n\t"
      "FP46X_%=:\n\t"
      "FP46DR_%=:\n\t"
      "s_mov_b32 s51, 0\n\t"
      "FP46DL_%=:\n\t"
      "s_add_u32 s51, s51, 1\n\t"
      "s_cmp_lt_u32 s51, 262144\n\t"
      "s_cbranch_scc0 FP46DX_%=\n\t"
      "s_waitcnt vmcnt(0)\n\t"
      "s_sleep 1\n\t"
      "global_load_dwordx4 v[144:147], v228, s[64:65] sc1\n\t"
      "global_load_dwordx4 v[148:151], v229, s[64:65] sc1\n\t"
      "global_load_dwordx4 v[152:155], v230, s[64:65] sc1\n\t"
      "s_waitcnt vmcnt(2)\n\t"
      "v_max3_u32 v234, v144, v145, v146\n\t"
      "v_max_u32_e32 v234, v234, v147\n\t"
      "v_cmp_eq_u32_e32 vcc, -1, v234\n\t"
      "s_cbranch_vccnz FP46DL_%=\n\t"
      "FP46DX_%=:\n\t"
      "s_waitcnt vmcnt(0)\n\t"
      "s_branch FP46B_%=\n\t"
      "FP47R_%=:\n\t"
      "s_mov_b32 s51, 0\n\t"
      "FP47L_%=:\n\t"
      "s_add_u32 s51, s51, 1\n\t"
      "s_cmp_lt_u32 s51, 262144\n\t"
      "s_cbranch_scc0 FP47X_%=\n\t"
      "s_sleep 1\n\t"
      "s_mov_b64 exec, 15\n\t"
      "v_mov_b64_e32 v[232:233], 0\n\t"
      "global_atomic_or_x2 v[232:233], v247, v[232:233], s[12:13] sc0 sc1\n\t"
      "s_waitcnt vmcnt(0)\n\t"
      "v_subrev_u32_e32 v232, s14, v232\n\t"
      "v_cmp_ne_u32_e64 s[48:49], s15, v233\n\t"
      "v_subrev_u32_e32 v232, 6, v232\n\t"
      "v_cmp_lt_u32_e32 vcc, 7, v232\n\t"
      "s_mov_b64 exec, -1\n\t"
      "s_or_b64 vcc, vcc, s[48:49]\n\t"
      "s_cbranch_vccnz FP47L_%=\n\t"
      "FP47X_%=:\n\t"
      "FP47DR_%=:\n\t"
      "s_mov_b32 s51, 0\n\t"
      "FP47DL_%=:\n\t"
      "s_add_u32 s51, s51, 1\n\t"
      "s_cmp_lt_u32 s51, 262144\n\t"
      "s_cbranch_scc0 FP47DX_%=\n\t"
      "s_waitcnt vmcnt(0)\n\t"
      "s_sleep 1\n\t"
      "global_load_dwordx4 v[148:151], v229, s[64:65] sc1\n\t"
      "global_load_dwordx4 v[152:155], v230, s[64:65] sc1\n\t"
      "s_waitcnt vmcnt(1)\n\t"
      "v_max3_u32 v234, v148, v149, v150\n\t"
      "v_max_u32_e32 v234, v234, v151\n\t"
      "v_cmp_eq_u32_e32 vcc, -1, v234\n\t"
      "s_cbranch_vccnz FP47DL_%=\n\t"
      "FP47DX_%=:\n\t"
      "s_branch FP47B_%=\n\t"
      "FP48R_%=:\n\t"
      "s_mov_b32 s51, 0\n\t"
      "FP48L_%=:\n\t"
      "s_add_u32 s51, s51, 1\n\t"
      "s_cmp_lt_u32 s51, 262144\n\t"
      "s_cbranch_scc0 FP48X_%=\n\t"
      "s_sleep 1\n\t"
      "s_mov_b64 exec, 15\n\t"
      "v_mov_b64_e32 v[232:233], 0\n\t"
      "global_atomic_or_x2 v[232:233], v247, v[232:233], s[12:13] sc0 sc1\n\t"
      "s_waitcnt vmcnt(0)\n\t"
      "v_subrev_u32_e32 v232, s14, v232\n\t"
      "v_cmp_ne_u32_e64 s[48:49], s15, v233\n\t"
      "v_subrev_u32_e32 v232, 6, v232\n\t"
      "v_cmp_lt_u32_e32 vcc, 7, v232\n\t"
      "s_mov_b64 exec, -1\n\t"
      "s_or_b64 vcc, vcc, s[48:49]\n\t"
      "s_cbranch_vccnz FP48L_%=\n\t"
      "FP48X_%=:\n\t"
      "FP48DR_%=:\n\t"
      "s_mov_b32 s51, 0\n\t"
      "FP48DL_%=:\n\t"
      "s_add_u32 s51, s51, 1\n\t"
      "s_cmp_lt_u32 s51, 262144\n\t"
      "s_cbranch_scc0 FP48DX_%=\n\t"
      "s_waitcnt vmcnt(0)\n\t"
      "s_sleep 1\n\t"
      "global_load_dwordx4 v[152:155], v230, s[64:65] sc1\n\t"
      "s_waitcnt vmcnt(0)\n\t"
      "v_max3_u32 v234, v152, v153, v154\n\t"
      "v_max_u32_e32 v234, v234, v155\n\t"
      "v_cmp_eq_u32_e32 vcc, -1, v234\n\t"
      "s_cbranch_vccnz FP48DL_%=\n\t"
      "FP48DX_%=:\n\t"
      "s_waitcnt vmcnt(0)\n\t"
      "s_branch FP48B_%=\n\t"
      "NP6R_%=:\n\t"
      "s_mov_b32 s51, 0\n\t"
      "NP6L_%=:\n\t"
      "s_add_u32 s51, s51, 1\n\t"
      "s_cmp_lt_u32 s51, 262144\n\t"
      "s_cbranch_scc0 NP6X_%=\n\t"
      "s_waitcnt vmcnt(0)\n\t"
      "s_sleep 1\n\t"
      "global_load_dwordx4 v[128:131], v224, s[64:65] sc1\n\t"
      "global_load_dwordx4 v[132:135], v225, s[64:65] sc1\n\t"
      "global_load_dwordx4 v[136:139], v226, s[64:65] sc1\n\t"
      "global_load_dwordx4 v[140:143], v227, s[64:65] sc1\n\t"
      "global_load_dwordx4 v[144:147], v228, s[64:65] sc1\n\t"
      "global_load_dwordx4 v[148:151], v229, s[64:65] sc1\n\t"
      "global_load_dwordx4 v[152:155], v230, s[64:65] sc1\n\t"
      "s_waitcnt vmcnt(0)\n\t"
      "v_max3_u32 v234, v128, v129, v130\n\t"
      "v_max3_u32 v234, v234, v131, v132\n\t"
      "v_max3_u32 v234, v234, v133, v134\n\t"
      "v_max3_u32 v234, v234, v135, v136\n\t"
      "v_max3_u32 v234, v234, v137, v138\n\t"
      "v_max3_u32 v234, v234, v139, v140\n\t"
      "v_max3_u32 v234, v234, v141, v142\n\t"
      "v_max3_u32 v234, v234, v143, v144\n\t"
      "v_max3_u32 v234, v234, v145, v146\n\t"
      "v_max3_u32 v234, v234, v147, v148\n\t"
      "v_max3_u32 v234, v234, v149, v150\n\t"
      "v_max3_u32 v234, v234, v151, v152\n\t"
      "v_max3_u32 v234, v234, v153, v154\n\t"
      "v_max_u32_e32 v234, v234, v155\n\t"
      "v_cmp_eq_u32_e32 vcc, -1, v234\n\t"
      "s_cbranch_vccnz NP6L_%=\n\t"
      "NP6X_%=:\n\t"
      "v_mfma_f32_16x16x32_bf16 v[184:187], v[128:131], v[0:3], 0\n\t"
      "v_mfma_f32_16x16x32_bf16 v[188:191], v[128:131], v[4:7], 0\n\t"
      "v_mfma_f32_16x16x32_bf16 v[192:195], v[128:131], v[8:11], 0\n\t"
      "v_mfma_f32_16x16x32_bf16 v[196:199], v[128:131], v[12:15], 0\n\t"
      "v_mfma_f32_16x16x32_bf16 v[184:187], v[132:135], v[16:19], v[184:187]\n\t"
      "v_mfma_f32_16x16x32_bf16 v[188:191], v[132:135], v[20:23], v[188:191]\n\t"
      "v_mfma_f32_16x16x32_bf16 v[192:195], v[132:135], v[24:27], v[192:195]\n\t"
      "v_mfma_f32_16x16x32_bf16 v[196:199], v[132:135], v[28:31], v[196:199]\n\t"
      "v_mfma_f32_16x16x32_bf16 v[184:187], v[136:139], v[32:35], v[184:187]\n\t"
      "v_mfma_f32_16x16x32_bf16 v[188:191], v[136:139], v[36:39], v[188:191]\n\t"
      "v_mfma_f32_16x16x32_bf16 v[192:195], v[136:139], v[40:43], v[192:195]\n\t"
      "v_mfma_f32_16x16x32_bf16 v[196:199], v[136:139], v[44:47], v[196:199]\n\t"
      "v_mfma_f32_16x16x32_bf16 v[184:187], v[140:143], v[48:51], v[184:187]\n\t"
      "v_mfma_f32_16x16x32_bf16 v[188:191], v[140:143], v[52:55], v[188:191]\n\t"
      "v_mfma_f32_16x16x32_bf16 v[192:195], v[140:143], v[56:59], v[192:195]\n\t"
      "v_mfma_f32_16x16x32_bf16 v[196:199], v[140:143], v[60:63], v[196:199]\n\t"
      "v_mfma_f32_16x16x32_bf16 v[184:187], v[144:147], v[64:67], v[184:187]\n\t"
      "v_mfma_f32_16x16x32_bf16 v[188:191], v[144:147], v[68:71], v[188:191]\n\t"
      "v_mfma_f32_16x16x32_bf16 v[192:195], v[144:147], v[72:75], v[192:195]\n\t"
      "v_mfma_f32_16x16x32_bf16 v[196:199], v[144:147], v[76:79], v[196:199]\n\t"
      "v_mfma_f32_16x16x32_bf16 v[184:187], v[148:151], v[80:83], v[184:187]\n\t"
      "v_mfma_f32_16x16x32_bf16 v[188:191], v[148:151], v[84:87], v[188:191]\n\t"
      "v_mfma_f32_16x16x32_bf16 v[192:195], v[148:151], v[88:91], v[192:195]\n\t"
      "v_mfma_f32_16x16x32_bf16 v[196:199], v[148:151], v[92:95], v[196:199]\n\t"
      "v_mfma_f32_16x16x32_bf16 v[184:187], v[152:155], v[96:99], v[184:187]\n\t"
      "v_mfma_f32_16x16x32_bf16 v[188:191], v[152:155], v[100:103], v[188:191]\n\t"
      "v_mfma_f32_16x16x32_bf16 v[192:195], v[152:155], v[104:107], v[192:195]\n\t"
      "v_mfma_f32_16x16x32_bf16 v[196:199], v[152:155], v[108:111], v[196:199]\n\t"
      "s_waitcnt vmcnt(0)\n\t"
      "s_nop 7\n\t"
      "s_branch NP6B_%=\n\t"
      : "+{s[4:19]}"(s), "+{s[20:27]}"(s2), "+{s[28:29]}"(kr), "+{v[240:247]}"(v)
      :
      : "memory", "vcc", "scc",
        "v0", "v1", "v2", "v3", "v4", "v5", "v6", "v7", "v8", "v9", "v10", "v11", "v12", "v13", "v14", "v15", "v16",
        "v17", "v18", "v19", "v20", "v21", "v22", "v23", "v24", "v25", "v26", "v27", "v28", "v29", "v30", "v31", "v32",
        "v33", "v34", "v35", "v36", "v37", "v38", "v39", "v40", "v41", "v42", "v43", "v44", "v45", "v46", "v47", "v48",
        "v49", "v50", "v51", "v52", "v53", "v54", "v55", "v56", "v57", "v58", "v59", "v60", "v61", "v62", "v63", "v64",
        "v65", "v66", "v67", "v68", "v69", "v70", "v71", "v72", "v73", "v74", "v75", "v76", "v77", "v78", "v79", "v80",
        "v81", "v82", "v83", "v84", "v85", "v86", "v87", "v88", "v89", "v90", "v91", "v92", "v93", "v94", "v95", "v96",
        "v97", "v98", "v99", "v100", "v101", "v102", "v103", "v104", "v105", "v106", "v107", "v108", "v109", "v110",
        "v111", "v112", "v113", "v114", "v115", "v116", "v117", "v118", "v119", "v120", "v121", "v122", "v123", "v124",
        "v125", "v126", "v127", "v128", "v129", "v130", "v131", "v132", "v133", "v134", "v135", "v136", "v137", "v138",
        "v139", "v140", "v141", "v142", "v143", "v144", "v145", "v146", "v147", "v148", "v149", "v150", "v151", "v152",
        "v153", "v154", "v155", "v156", "v157", "v158", "v159", "v160", "v161", "v162", "v163", "v164", "v165", "v166",
        "v167", "v168", "v169", "v170", "v171", "v172", "v173", "v174", "v175", "v176", "v177", "v178", "v179", "v180",
        "v181", "v182", "v183", "v184", "v185", "v186", "v187", "v188", "v189", "v190", "v191", "v192", "v193", "v194",
        "v195", "v196", "v197", "v198", "v199", "v200", "v201", "v202", "v203", "v204", "v205", "v206", "v207", "v208",
        "v209", "v210", "v211", "v212", "v213", "v214", "v215", "v216", "v217", "v218", "v219", "v220", "v221", "v222",
        "v223", "v224", "v225", "v226", "v227", "v228", "v229", "v230", "v231", "v232", "v233", "v234", "v235", "v236",
        "v237", "v238", "v239", "v248", "v249", "v250", "v251", "v252", "v253", "s44", "s45", "s46", "s47", "s48",
        "s49", "s50", "s51", "s52", "s53", "s54", "s55", "s56", "s57", "s58", "s59", "s60", "s61", "s62", "s63", "s64",
        "s65", "s66", "s67", "s68", "s69", "s70"
    );
    __builtin_amdgcn_endpgm();
  }
}


__global__ void probe(const char* p, unsigned stride, unsigned* sink) {
  unsigned off = 0, acc = 0;
  for (int i = 0; i < 40; ++i) {
    unsigned v;
    asm volatile("global_load_dword %0, %1, %2\n\ts_waitcnt vmcnt(0)" : "=v"(v) : "v"(off), "s"(p) : "memory");
    acc += v; off += stride + (v == 0x12345678u ? 128 : 0) * (acc == 77u ? 1 : 0);
  }
  if (acc == 0x9abcdef1u && off == 5) *sink = acc;
}
extern "C" void kernel_launch(void* const* d_in, const int* in_sizes, int n_in, void* d_out, int out_size,
                              void* d_ws, size_t ws_size, hipStream_t stream) {
  (void)hipFuncSetAttribute((const void*)fused_block, hipFuncAttributeMaxDynamicSharedMemorySize, LDS_BYTES);
  probe<<<1, 64, 0, stream>>>((const char*)d_in[0], 44928u, (unsigned*)d_ws + (20u << 20));
  fused_block<<<256, 512, LDS_BYTES, stream>>>((const char*)d_in[0], (const char*)d_in[1], (const char*)d_in[2],
                                               (const char*)d_in[4], (const char*)d_in[5], (const char*)d_in[6],
                                               (const char*)d_in[7], (char*)d_out, (char*)d_ws);
}
```

```cpp
#include <hip/hip_runtime.h>
#include <stdint.h>

#define WS_O 0
#define WS_HN 2097152
#define WS_H 4194304
#define WS_G 6291456
#define WS_ST 8388608
#define ST_FL 65536
#define ST_H3 262144
#define ST_H4 393216
#define ST_H2 32768
#define ST_CK 524288
#define WS_EP 8519680
#define A_UNIT 5120
#define A_HALO 40960
#define A_SLOT 43008
#define A_CS 131072
#define A_ST 132096
#define A_EX 142336
#define A_FL 154624
#define B_UNIT 7168
#define B_SLOT 57344
#define B_X 114688
#define B_OP 928
#define B_PS 131072
#define G_WV 16384
#define A_W0 49152
#define A_WD 40960
#define G_RED 0
#define G_RED2 32768
#define LDS_KEY 163312
#define LDS_SINK 163328
#define LDS_BYTES 163840
#define XO 3
#define KROT 14
#define KMUL 1
#define PRE 0
#define HP4 1
#define SWP 0
#define M3 0
#define M4 0
#define RF3 0
#define RF4 0
#define TW0 128
#define TW1 448
#define TWN 53
#define TWN1 1

typedef unsigned short u16;
typedef unsigned long long u64;
typedef __attribute__((ext_vector_type(4))) unsigned int u32x4;
typedef __attribute__((ext_vector_type(8))) unsigned int u32x8;
typedef __attribute__((ext_vector_type(16))) unsigned int u32x16;
#define UNI(x) (unsigned)__builtin_amdgcn_readfirstlane((int)(x))
#define PIN() __builtin_amdgcn_sched_barrier(0)

__device__ __forceinline__ int swz_tr(int row) { return (((row >> 1) & 1) | (((row >> 3) & 1) << 1)) << 1; }
__device__ __forceinline__ int swz_row(int row) { return (row >> 1) & 7; }
template <class T>
__device__ __forceinline__ void put(T& s, int i, u64 p) { s[i] = (unsigned)p; s[i + 1] = (unsigned)(p >> 32); }
template <class T>
__device__ __forceinline__ void put2(T& s, int i, unsigned lo, unsigned hi) { s[i] = UNI(lo); s[i + 1] = UNI(hi); }

__global__ __launch_bounds__(512, 2) void fused_block(const char* __restrict__ x, const char* __restrict__ wqkv,
                                                   const char* __restrict__ wo, const char* __restrict__ w1,
                                                   const char* __restrict__ b1, const char* __restrict__ w2,
                                                   const char* __restrict__ b2, char* __restrict__ out,
                                                   char* __restrict__ ws) {
  u32x8 f;
  asm volatile(
      "s_load_dwordx4 s[4:7], s[0:1], 0x0\n\t"
      "v_readfirstlane_b32 s3, v0\n\t"
      "s_getpc_b64 s[40:41]\n\t"
      "s_lshr_b32 s12, s2, 3\n\t"
      "s_or_b32 s13, s3, s12\n\t"
      "s_and_b32 s13, s13, 0x1d0\n\t"
      "s_cmp_lg_u32 s13, 16\n\t"
      "s_cbranch_scc1 ET2_%=\n\t"
      "s_and_b32 s12, s12, 7\n\t"
      "s_mov_b32 m0, 163328\n\t"
      "v_lshl_add_u32 v3, s12, 6, v0\n\t"
      "v_min_u32_e32 v3, 496, v3\n\t"
      "v_lshlrev_b32_e32 v3, 7, v3\n\t"
      "global_load_lds_dword v3, s[40:41]\n\t"
      "ET2_%=:\n\t"
      "v_lshrrev_b32_e32 v1, 3, v0\n\t"
      "v_and_b32_e32 v2, 7, v0\n\t"
      "s_and_b32 s17, s2, 7\n\t"
      "s_lshr_b32 s18, s2, 3\n\t"
      "s_lshr_b32 s3, s3, 6\n\t"
      "s_cmpk_gt_u32 s2, 0x7f\n\t"
      "s_cbranch_scc1 EB_%=\n\t"
      "v_and_b32_e32 v3, 2, v1\n\t"
      "v_lshrrev_b32_e32 v4, 1, v1\n\t"
      "s_lshr_b32 s19, s18, 3\n\t"
      "s_lshl1_add_u32 s19, s17, s19\n\t"
      "v_and_or_b32 v3, v4, 4, v3\n\t"
      "v_xor_b32_e32 v3, v3, v2\n\t"
      "v_min_u32_e32 v3, 6, v3\n\t"
      "v_mul_u32_u24_e32 v5, 2688, v1\n\t"
      "v_lshl_add_u32 v3, v3, 4, v5\n\t"
      "v_add_u32_e32 v195, 2048, v3\n\t"
      "v_add_u32_e32 v196, 1920, v3\n\t"
      "v_add_u32_e32 v197, 5888, v3\n\t"
      "s_mul_i32 s12, s19, 114688\n\t"
      "v_and_b32_e32 v5, 0x38, v1\n\t"
      "v_add_u32_e32 v5, v5, v1\n\t"
      "v_mul_u32_u24_e32 v5, 896, v5\n\t"
      "s_and_b32 s13, s18, 7\n\t"
      "s_mul_i32 s13, s13, 112\n\t"
      "v_and_b32_e32 v6, 3, v4\n\t"
      "v_xor_b32_e32 v6, v6, v2\n\t"
      "v_lshl_add_u32 v5, v6, 4, v5\n\t"
      "s_sub_u32 s12, s12, 15360\n\t"
      "s_ashr_i32 s14, s12, 31\n\t"
      "s_sub_u32 s13, s13, 4096\n\t"
      "v_add_u32_e32 v192, 15360, v5\n\t"
      "v_xor_b32_e32 v5, 64, v5\n\t"
      "v_add_u32_e32 v193, 21504, v5\n\t"
      "s_mul_i32 s9, s3, 5120\n\t"
      "s_waitcnt lgkmcnt(0)\n\t"
      "s_add_u32 s4, s4, s12\n\t"
      "s_addc_u32 s5, s5, s14\n\t"
      "s_add_u32 s6, s6, s13\n\t"
      "s_addc_u32 s7, s7, -1\n\t"
      "s_add_u32 m0, s9, 0\n\t"
      "s_nop 0\n\t"
      "global_load_lds_dwordx4 v195, s[6:7] offset:2048\n\t"
      "global_load_lds_dwordx4 v192, s[4:5]\n\t"
      "global_load_lds_dwordx4 v193, s[4:5] offset:1024\n\t"
      "global_load_lds_dwordx4 v196, s[6:7] offset:3072\n\t"
      "s_add_u32 m0, s9, 4096\n\t"
      "s_nop 0\n\t"
      "global_load_lds_dwordx4 v197, s[6:7]\n\t"
      "s_sub_u32 s3, s3, 4\n\t"
      "s_cbranch_scc1 EX_%=\n\t"
      "s_cmp_eq_u32 s19, 0\n\t"
      "s_cselect_b32 s13, 16, 0\n\t"
      "s_lshl2_add_u32 s13, s3, s13\n\t"
      "v_and_or_b32 v3, v1, 3, s13\n\t"
      "v_bfe_u32 v4, v3, 1, 3\n\t"
      "v_mul_u32_u24_e32 v3, 896, v3\n\t"
      "s_cmp_eq_u32 s3, 0\n\t"
      "s_cselect_b32 s10, 0xffffff00, -1\n\t"
      "s_mov_b32 s11, 0\n\t"
      "v_xor_b32_e32 v4, v4, v2\n\t"
      "v_lshl_add_u32 v3, v4, 4, v3\n\t"
      "v_add_u32_e32 v194, 1024, v3\n\t"
      "s_lshl_b32 s12, s3, 9\n\t"
      "s_add_u32 s12, s12, 40960\n\t"
      "s_add_u32 m0, s12, 0\n\t"
      "s_mov_b64 exec, s[10:11]\n\t"
      "global_load_lds_dwordx4 v194, s[4:5]\n\t"
      "s_mov_b64 exec, -1\n\t"
      "s_sub_u32 s40, s4, 57344\n\t"
      "s_subb_u32 s41, s5, 0\n\t"
      "s_sub_u32 s42, s6, 86016\n\t"
      "s_subb_u32 s43, s7, 0\n\t"
      "s_add_u32 s40, s40, 128\n\t"
      "s_addc_u32 s41, s41, 0\n\t"
      "s_add_u32 s42, s42, 172032\n\t"
      "s_addc_u32 s43, s43, 0\n\t"
      "s_add_u32 m0, s9, 22528\n\t"
      "s_nop 0\n\t"
      "global_load_lds_dwordx4 v195, s[42:43] offset:2048\n\t"
      "global_load_lds_dwordx4 v192, s[40:41]\n\t"
      "global_load_lds_dwordx4 v193, s[40:41] offset:1024\n\t"
      "global_load_lds_dwordx4 v196, s[42:43] offset:3072\n\t"
      "s_add_u32 m0, s9, 26624\n\t"
      "s_nop 0\n\t"
      "global_load_lds_dwordx4 v197, s[42:43]\n\t"
      "s_branch EX_%=\n\t"
      "EB_%=:\n\t"
      "v_and_b32_e32 v3, 63, v0\n\t"
      "v_lshlrev_b32_e32 v3, 4, v3\n\t"
      "s_sub_u32 s18, s18, 16\n\t"
      "s_min_u32 s16, s3, 6\n\t"
      "s_lshl4_add_u32 s15, s17, s18\n\t"
      "s_mul_i32 s15, s15, 14336\n\t"
      "s_lshl_b32 s12, s16, 7\n\t"
      "s_add_u32 s15, s15, s12\n\t"
      "v_mul_u32_u24_e32 v6, 56, v3\n\t"
      "v_add_u32_e32 v6, s15, v6\n\t"
      "s_add_u32 s19, s17, 3\n\t"
      "s_and_b32 s19, s19, 7\n\t"
      "s_lshl4_add_u32 s19, s19, s18\n\t"
      "s_lshl3_add_u32 s12, s19, s3\n\t"
      "s_lshl_b32 s12, s12, 11\n\t"
      "s_mul_i32 s19, s19, 14336\n\t"
      "s_lshl_b32 s16, s16, 11\n\t"
      "s_add_u32 s19, s19, s16\n\t"
      "s_add_u32 s8, s16, 114688\n\t"
      "v_add_u32_e32 v1, s19, v3\n\t"
      "s_lshl4_add_u32 s16, s3, s18\n\t"
      "s_mul_i32 s13, s16, 6784\n\t"
      "s_add_u32 s13, s13, 344064\n\t"
      "s_mul_i32 s14, s16, 2688\n\t"
      "s_add_u32 s14, s14, 0\n\t"
      "v_min_u32_e32 v4, 832, v3\n\t"
      "v_lshl_add_u32 v4, v4, 3, s13\n\t"
      "v_min_u32_e32 v4, 1204096, v4\n\t"
      "v_min_u32_e32 v5, 320, v3\n\t"
      "v_lshl_add_u32 v5, v5, 3, s14\n\t"
      "s_mov_b32 m0, 163328\n\t"
      "s_waitcnt lgkmcnt(0)\n\t"
      "s_mov_b64 exec, 0xffff\n\t"
      "global_load_lds_dword v6, s[4:5]\n\t"
      "s_mov_b64 exec, -1\n\t"
      "global_load_lds_dword v5, s[6:7]\n\t"
      "s_mov_b32 m0, s8\n\t"
      "s_nop 0\n\t"
      "global_load_lds_dwordx4 v1, s[4:5]\n\t"
      "global_load_lds_dwordx4 v1, s[4:5] offset:1024\n\t"
      "s_mov_b32 m0, 163328\n\t"
      "s_barrier\n\t"
      "global_load_lds_dword v4, s[6:7]\n\t"
      "EX_%=:\n\t"
      : "={v[192:199]}"(f)
      : "{s[0:1]}"(__builtin_amdgcn_kernarg_segment_ptr()), "{s2}"(blockIdx.x), "{v0}"(threadIdx.x)
      : "memory", "scc", "s3", "s4", "s5", "s6", "s7", "s8", "s9", "s10", "s11", "s12", "s13", "s14", "s15", "s16", "s17", "s18", "s19", "v1", "v2",
        "v3", "v4", "v5", "v6", "s40", "s41", "s42", "s43", "s48", "s49", "s50"
      );
  PIN();
  const int wave = __builtin_amdgcn_readfirstlane(threadIdx.x >> 6);
  const int xcd = blockIdx.x & 7, idx = blockIdx.x >> 3;
#define LANE_BITS(l) \
  const int lane = (l), g = lane >> 4, c = lane & 15, q4 = c >> 2, p4 = lane & 3, lr = lane >> 3, lc = lane & 7

  if (__builtin_expect(idx < 16, 1)) {
    const int rt = 2 * xcd + (idx >> 3), hp = idx & 7, hh = (wave >> 1) & 1, half = wave & 1;
    const unsigned sta = A_ST + half * 1024;
    if (wave < 4) {
      LANE_BITS(threadIdx.x & 63);
      u32x16 v;
      u32x8 s;
      v[0] = 4 * half * A_UNIT + c * 128 + ((g ^ swz_row(c)) << 4);
      v[1] = A_HALO + c * 128 + ((g ^ swz_row(c)) << 4);
#pragma unroll
      for (int j = 0; j < 2; ++j) {
        const int by = 56 * hh + 32 * j + 8 * p4;
        v[2 + j] = g * A_UNIT + 2048 + q4 * 128 + (((by >> 4) ^ swz_tr(8 * g + q4)) << 4) + (by & 8);
      }
      const unsigned csa = A_CS + hh * 384, exa = A_EX + hh * 3 * 2048;
      v[4] = csa + g * 16;
      v[5] = csa + c * 4;
      v[6] = v[4] + 64 * half;
      v[7] = v[5] + 64 * half;
      v[8] = A_FL + (wave ^ 1) * 8;
      v[9] = sta + g * 16;
      v[10] = sta + c * 4;
      const int key = 4 * g + (c & 1);
      v[11] = sta + 384 + (key <= c ? 128 : 64) + key * 4;
      unsigned vk1 = sta + 384 + (key + 2 <= c ? 128 : 64) + key * 4 + 8;
      v[12] = exa + half * 2048 + lane * 16;
      v[13] = exa + half * 2048 + lane * 8;
      v[14] = exa + lane * 16 + 8 * half;
      v[15] = lane * 16;
      put(s, 0, (u64)(ws + WS_O + ((8 * rt + 4 * half) * 16 + 2 * hp + hh) * 1024));
      s[2] = half;
      s[3] = UNI(rt == 0 && half == 0 ? 0xff800000u : 0u);
      s[4] = exa + 1024 + 512 * half;
      s[5] = A_FL + 32 + 4 * half;
      put(s, 6, (u64)(ws + WS_ST));
      unsigned hint = ST_H2 + (((rt * 8 + hp) * 2 + half) * 2 + hh) * 64;
      unsigned chk = ST_CK + ((rt * 2 + half) * 16 + 2 * hp + hh) * 256;
    asm volatile(
      "s_waitcnt lgkmcnt(0)\n\ts_nop 4\n\t"
      "v_xor_b32_e32 v248, 64, v224\n\t"
      "v_xor_b32_e32 v249, 64, v225\n\t"
      "s_mov_b32 s36, 0xaaaaaaaa\n\t"
      "s_mov_b32 s37, 0xaaaaaaaa\n\t"
      "s_mov_b32 s38, 0xfff0ffff\n\t"
      "s_mov_b32 s39, 0xf000ff00\n\t"
      "s_mov_b32 s40, 0xffc0fffc\n\t"
      "s_mov_b32 s41, 0xc000fc00\n\t"
      "s_mov_b32 s42, 0x0\n\t"
      "s_mov_b32 s43, 0xffff0000\n\t"
      "s_mov_b32 s44, 0x10001\n\t"
      "s_mov_b32 s45, 0x10001\n\t"
      "v_mov_b32_e32 v216, 0xffff\n\t"
      "v_mov_b32_e32 v217, 0xffff0000\n\t"
      "v_cndmask_b32_e64 v216, v216, v217, s[36:37]\n\t"
      "v_cndmask_b32_e64 v240, v216, 0, s[38:39]\n\t"
      "v_cndmask_b32_e64 v241, v216, 0, s[40:41]\n\t"
      "v_cndmask_b32_e64 v242, 0, v216, s[38:39]\n\t"
      "v_cndmask_b32_e64 v243, 0, v216, s[40:41]\n\t"
      "v_mov_b32_e32 v244, s7\n\t"
      "v_mov_b32_e32 v245, s7\n\t"
      "v_mov_b32_e32 v246, s7\n\t"
      "v_mov_b32_e32 v247, s7\n\t"
      "v_mov_b64_e32 v[0:1], 0\n\t"
      "v_mov_b64_e32 v[2:3], 0\n\t"
      "v_mov_b64_e32 v[4:5], 0\n\t"
      "v_mov_b64_e32 v[6:7], 0\n\t"
      "v_mov_b64_e32 v[8:9], 0\n\t"
      "v_mov_b64_e32 v[10:11], 0\n\t"
      "v_mov_b64_e32 v[12:13], 0\n\t"
      "v_mov_b64_e32 v[14:15], 0\n\t"
      "v_mov_b64_e32 v[16:17], 0\n\t"
      "v_mov_b64_e32 v[18:19], 0\n\t"
      "v_mov_b64_e32 v[20:21], 0\n\t"
      "v_mov_b64_e32 v[22:23], 0\n\t"
      "v_mov_b64_e32 v[24:25], 0\n\t"
      "v_mov_b64_e32 v[26:27], 0\n\t"
      "v_mov_b64_e32 v[28:29], 0\n\t"
      "v_mov_b64_e32 v[30:31], 0\n\t"
      "v_mov_b64_e32 v[32:33], 0\n\t"
      "v_mov_b64_e32 v[34:35], 0\n\t"
      "v_mov_b64_e32 v[36:37], 0\n\t"
      "v_mov_b64_e32 v[38:39], 0\n\t"
      "v_mov_b64_e32 v[40:41], 0\n\t"
      "v_mov_b64_e32 v[42:43], 0\n\t"
      "v_mov_b64_e32 v[44:45], 0\n\t"
      "v_mov_b64_e32 v[46:47], 0\n\t"
      "v_mov_b64_e32 v[48:49], 0\n\t"
      "v_mov_b64_e32 v[50:51], 0\n\t"
      "v_mov_b64_e32 v[52:53], 0\n\t"
      "v_mov_b64_e32 v[54:55], 0\n\t"
      "v_mov_b64_e32 v[56:57], 0\n\t"
      "v_mov_b64_e32 v[58:59], 0\n\t"
      "v_mov_b64_e32 v[60:61], 0\n\t"
      "v_mov_b64_e32 v[62:63], 0\n\t"
      "v_mov_b64_e32 v[64:65], 0\n\t"
      "v_mov_b64_e32 v[66:67], 0\n\t"
      "v_mov_b64_e32 v[68:69], 0\n\t"
      "v_mov_b64_e32 v[70:71], 0\n\t"
      "v_mov_b64_e32 v[72:73], 0\n\t"
      "v_mov_b64_e32 v[74:75], 0\n\t"
      "v_mov_b64_e32 v[76:77], 0\n\t"
      "v_mov_b64_e32 v[78:79], 0\n\t"
      "v_mov_b64_e32 v[80:81], 0\n\t"
      "v_mov_b64_e32 v[82:83], 0\n\t"
      "v_mov_b64_e32 v[84:85], 0\n\t"
      "v_mov_b64_e32 v[86:87], 0\n\t"
      "v_mov_b64_e32 v[88:89], 0\n\t"
      "v_mov_b64_e32 v[90:91], 0\n\t"
      "v_mov_b64_e32 v[92:93], 0\n\t"
      "v_mov_b64_e32 v[94:95], 0\n\t"
      "v_mov_b64_e32 v[96:97], 0\n\t"
      "v_mov_b64_e32 v[98:99], 0\n\t"
      "v_mov_b64_e32 v[100:101], 0\n\t"
      "v_mov_b64_e32 v[102:103], 0\n\t"
      "v_mov_b64_e32 v[104:105], 0\n\t"
      "v_mov_b64_e32 v[106:107], 0\n\t"
      "s_mov_b32 s47, 6\n\t"
      "s_cmp_eq_u32 s6, 0\n\t"
      "s_cbranch_scc0 LOOP1_%=\n\t"
      "s_waitcnt vmcnt(0)\n\t"
      "s_barrier\n\t"
      "ds_read_b64_tr_b16 v[108:109], v226\n\t"
      "ds_read_b64_tr_b16 v[110:111], v226 offset:512\n\t"
      "ds_read_b128 v[132:135], v224\n\t"
      "ds_read_b128 v[136:139], v224 offset:5120\n\t"
      "ds_read_b128 v[140:143], v224 offset:10240\n\t"
      "ds_read_b128 v[144:147], v224 offset:15360\n\t"
      "ds_read_b64_tr_b16 v[112:113], v227\n\t"
      "ds_read_b64_tr_b16 v[114:115], v227 offset:512\n\t"
      "ds_read_b128 v[148:151], v225\n\t"
      "ds_read_b64_tr_b16 v[116:117], v226 offset:1024\n\t"
      "ds_read_b64_tr_b16 v[118:119], v226 offset:1536\n\t"
      "ds_read_b64_tr_b16 v[120:121], v227 offset:1024\n\t"
      "ds_read_b64_tr_b16 v[122:123], v227 offset:1536\n\t"
      "ds_read_b64_tr_b16 v[124:125], v226 offset:2048\n\t"
      "ds_read_b64_tr_b16 v[126:127], v226 offset:2560\n\t"
      "ds_read_b64_tr_b16 v[128:129], v227 offset:2048\n\t"
      "ds_read_b64_tr_b16 v[130:131], v227 offset:2560\n\t"
      "s_branch EVEN0_%=\n\t"
      "s_waitcnt lgkmcnt(0)\n\t"
      ".p2align 6\n\t"
      "ODD0_%=:\n\t"
      "s_waitcnt lgkmcnt(0)\n\t"
      "v_mfma_f32_16x16x32_bf16 v[0:3], v[152:155], v[176:179], v[0:3]\n\t"
      "v_add_u32_e32 v224, s46, v224\n\t"
      "v_add_u32_e32 v226, s46, v226\n\t"
      "v_mfma_f32_16x16x32_bf16 v[24:27], v[152:155], v[180:183], v[24:27]\n\t"
      "v_add_u32_e32 v227, s46, v227\n\t"
      "v_add_u32_e32 v225, s46, v225\n\t"
      "v_mfma_f32_16x16x32_bf16 v[48:51], v[152:155], v[184:187], v[48:51]\n\t"
      "s_barrier\n\t"
      "ds_read_b64_tr_b16 v[108:109], v226\n\t"
      "v_mfma_f32_16x16x32_bf16 v[72:75], v[152:155], v[188:191], v[72:75]\n\t"
      "ds_read_b64_tr_b16 v[110:111], v226 offset:512\n\t"
      "v_mfma_f32_16x16x32_bf16 v[4:7], v[156:159], v[176:179], v[4:7]\n\t"
      "ds_read_b128 v[132:135], v224\n\t"
      "v_mfma_f32_16x16x32_bf16 v[28:31], v[156:159], v[180:183], v[28:31]\n\t"
      "ds_read_b128 v[136:139], v224 offset:5120\n\t"
      "v_mfma_f32_16x16x32_bf16 v[52:55], v[156:159], v[184:187], v[52:55]\n\t"
      "ds_read_b128 v[140:143], v224 offset:10240\n\t"
      "v_mfma_f32_16x16x32_bf16 v[76:79], v[156:159], v[188:191], v[76:79]\n\t"
      "ds_read_b128 v[144:147], v224 offset:15360\n\t"
      "v_mfma_f32_16x16x32_bf16 v[96:99], v[152:155], v[192:195], v[96:99]\n\t"
      "v_mfma_f32_16x16x32_bf16 v[8:11], v[160:163], v[176:179], v[8:11]\n\t"
      "ds_read_b64_tr_b16 v[112:113], v227\n\t"
      "v_mfma_f32_16x16x32_bf16 v[32:35], v[160:163], v[180:183], v[32:35]\n\t"
      "ds_read_b64_tr_b16 v[114:115], v227 offset:512\n\t"
      "v_mfma_f32_16x16x32_bf16 v[56:59], v[160:163], v[184:187], v[56:59]\n\t"
      "ds_read_b128 v[148:151], v225\n\t"
      "v_mfma_f32_16x16x32_bf16 v[80:83], v[160:163], v[188:191], v[80:83]\n\t"
      "ds_read_b64_tr_b16 v[116:117], v226 offset:1024\n\t"
      "v_mfma_f32_16x16x32_bf16 v[12:15], v[164:167], v[176:179], v[12:15]\n\t"
      "ds_read_b64_tr_b16 v[118:119], v226 offset:1536\n\t"
      "v_mfma_f32_16x16x32_bf16 v[36:39], v[164:167], v[180:183], v[36:39]\n\t"
      "ds_read_b64_tr_b16 v[120:121], v227 offset:1024\n\t"
      "v_mfma_f32_16x16x32_bf16 v[60:63], v[164:167], v[184:187], v[60:63]\n\t"
      "v_mfma_f32_16x16x32_bf16 v[84:87], v[164:167], v[188:191], v[84:87]\n\t"
      "ds_read_b64_tr_b16 v[122:123], v227 offset:1536\n\t"
      "v_mfma_f32_16x16x32_bf16 v[100:103], v[160:163], v[192:195], v[100:103]\n\t"
      "ds_read_b64_tr_b16 v[124:125], v226 offset:2048\n\t"
      "v_mfma_f32_16x16x32_bf16 v[16:19], v[176:179], v[168:171], v[16:19]\n\t"
      "ds_read_b64_tr_b16 v[126:127], v226 offset:2560\n\t"
      "v_mfma_f32_16x16x32_bf16 v[40:43], v[180:183], v[168:171], v[40:43]\n\t"
      "ds_read_b64_tr_b16 v[128:129], v227 offset:2048\n\t"
      "v_mfma_f32_16x16x32_bf16 v[64:67], v[184:187], v[168:171], v[64:67]\n\t"
      "ds_read_b64_tr_b16 v[130:131], v227 offset:2560\n\t"
      "v_mfma_f32_16x16x32_bf16 v[88:91], v[188:191], v[168:171], v[88:91]\n\t"
      "v_mfma_f32_16x16x32_bf16 v[20:23], v[176:179], v[172:175], v[20:23]\n\t"
      "v_mfma_f32_16x16x32_bf16 v[44:47], v[180:183], v[172:175], v[44:47]\n\t"
      "v_add_u32_e32 v248, s46, v248\n\t"
      "v_add_u32_e32 v249, s46, v249\n\t"
      "v_mfma_f32_16x16x32_bf16 v[68:71], v[184:187], v[172:175], v[68:71]\n\t"
      "v_mfma_f32_16x16x32_bf16 v[92:95], v[188:191], v[172:175], v[92:95]\n\t"
      "v_mfma_f32_16x16x32_bf16 v[104:107], v[192:195], v[168:171], v[104:107]\n\t"
      "EVEN0_%=:\n\t"
      "s_waitcnt lgkmcnt(0)\n\t"
      "v_mfma_f32_16x16x32_bf16 v[0:3], v[108:111], v[132:135], v[0:3]\n\t"
      "ds_read_b64_tr_b16 v[152:153], v226 offset:20480\n\t"
      "v_mfma_f32_16x16x32_bf16 v[24:27], v[108:111], v[136:139], v[24:27]\n\t"
      "ds_read_b64_tr_b16 v[154:155], v226 offset:20992\n\t"
      "v_mfma_f32_16x16x32_bf16 v[48:51], v[108:111], v[140:143], v[48:51]\n\t"
      "ds_read_b128 v[176:179], v248\n\t"
      "v_mfma_f32_16x16x32_bf16 v[72:75], v[108:111], v[144:147], v[72:75]\n\t"
      "ds_read_b128 v[180:183], v248 offset:5120\n\t"
      "v_mfma_f32_16x16x32_bf16 v[4:7], v[112:115], v[132:135], v[4:7]\n\t"
      "v_mfma_f32_16x16x32_bf16 v[28:31], v[112:115], v[136:139], v[28:31]\n\t"
      "ds_read_b128 v[184:187], v248 offset:10240\n\t"
      "v_mfma_f32_16x16x32_bf16 v[52:55], v[112:115], v[140:143], v[52:55]\n\t"
      "ds_read_b128 v[188:191], v248 offset:15360\n\t"
      "v_mfma_f32_16x16x32_bf16 v[76:79], v[112:115], v[144:147], v[76:79]\n\t"
      "ds_read_b64_tr_b16 v[156:157], v227 offset:20480\n\t"
      "v_mfma_f32_16x16x32_bf16 v[96:99], v[108:111], v[148:151], v[96:99]\n\t"
      "v_mfma_f32_16x16x32_bf16 v[8:11], v[116:119], v[132:135], v[8:11]\n\t"
      "ds_read_b64_tr_b16 v[158:159], v227 offset:20992\n\t"
      "v_mfma_f32_16x16x32_bf16 v[32:35], v[116:119], v[136:139], v[32:35]\n\t"
      "ds_read_b128 v[192:195], v249\n\t"
      "v_mfma_f32_16x16x32_bf16 v[56:59], v[116:119], v[140:143], v[56:59]\n\t"
      "ds_read_b64_tr_b16 v[160:161], v226 offset:21504\n\t"
      "v_mfma_f32_16x16x32_bf16 v[80:83], v[116:119], v[144:147], v[80:83]\n\t"
      "ds_read_b64_tr_b16 v[162:163], v226 offset:22016\n\t"
      "v_mfma_f32_16x16x32_bf16 v[12:15], v[120:123], v[132:135], v[12:15]\n\t"
      "v_mfma_f32_16x16x32_bf16 v[36:39], v[120:123], v[136:139], v[36:39]\n\t"
      "ds_read_b64_tr_b16 v[164:165], v227 offset:21504\n\t"
      "v_mfma_f32_16x16x32_bf16 v[60:63], v[120:123], v[140:143], v[60:63]\n\t"
      "ds_read_b64_tr_b16 v[166:167], v227 offset:22016\n\t"
      "v_mfma_f32_16x16x32_bf16 v[84:87], v[120:123], v[144:147], v[84:87]\n\t"
      "ds_read_b64_tr_b16 v[168:169], v226 offset:22528\n\t"
      "v_mfma_f32_16x16x32_bf16 v[100:103], v[116:119], v[148:151], v[100:103]\n\t"
      "v_mfma_f32_16x16x32_bf16 v[16:19], v[132:135], v[124:127], v[16:19]\n\t"
      "ds_read_b64_tr_b16 v[170:171], v226 offset:23040\n\t"
      "v_mfma_f32_16x16x32_bf16 v[40:43], v[136:139], v[124:127], v[40:43]\n\t"
      "ds_read_b64_tr_b16 v[172:173], v227 offset:22528\n\t"
      "v_mfma_f32_16x16x32_bf16 v[64:67], v[140:143], v[124:127], v[64:67]\n\t"
      "ds_read_b64_tr_b16 v[174:175], v227 offset:23040\n\t"
      "s_cmp_eq_u32 s47, 4\n\t"
      "s_cselect_b32 s46, -2, 1\n\t"
      "v_mfma_f32_16x16x32_bf16 v[88:91], v[144:147], v[124:127], v[88:91]\n\t"
      "s_cmp_eq_u32 s47, 1\n\t"
      "s_cselect_b32 s46, -2, s46\n\t"
      "v_mfma_f32_16x16x32_bf16 v[20:23], v[132:135], v[128:131], v[20:23]\n\t"
      "s_mul_i32 s46, s46, 43008\n\t"
      "v_mfma_f32_16x16x32_bf16 v[44:47], v[136:139], v[128:131], v[44:47]\n\t"
      "v_mfma_f32_16x16x32_bf16 v[68:71], v[140:143], v[128:131], v[68:71]\n\t"
      "v_mfma_f32_16x16x32_bf16 v[92:95], v[144:147], v[128:131], v[92:95]\n\t"
      "s_sub_u32 s47, s47, 1\n\t"
      "v_mfma_f32_16x16x32_bf16 v[104:107], v[148:151], v[124:127], v[104:107]\n\t"
      "s_cbranch_scc0 ODD0_%=\n\t"
      "s_waitcnt lgkmcnt(0)\n\t"
      "v_mfma_f32_16x16x32_bf16 v[96:99], v[152:155], v[192:195], v[96:99]\n\t"
      "v_mov_b32_e32 v212, s9\n\t"
      "ds_read_b32 v212, v212\n\t"
      "ds_read2_b32 v[128:129], v234 offset0:32 offset1:32\n\t"
      "v_mfma_f32_16x16x32_bf16 v[100:103], v[160:163], v[192:195], v[100:103]\n\t"
      "ds_read2_b32 v[130:131], v234 offset0:48 offset1:48\n\t"
      "v_mfma_f32_16x16x32_bf16 v[104:107], v[192:195], v[168:171], v[104:107]\n\t"
      "ds_read2_b32 v[132:133], v234 offset0:64 offset1:64\n\t"
      "ds_read2_b32 v[134:135], v234 offset0:80 offset1:80\n\t"
      "v_mfma_f32_16x16x32_bf16 v[80:83], v[160:163], v[188:191], v[80:83]\n\t"
      "ds_read2_b32 v[248:249], v234 offset0:0 offset1:0\n\t"
      "v_mfma_f32_16x16x32_bf16 v[84:87], v[164:167], v[188:191], v[84:87]\n\t"
      "ds_read_b128 v[196:199], v233\n\t"
      "ds_read_b128 v[136:139], v233 offset:128\n\t"
      "v_mfma_f32_16x16x32_bf16 v[88:91], v[188:191], v[168:171], v[88:91]\n\t"
      "ds_read_b128 v[140:143], v233 offset:192\n\t"
      "v_mfma_f32_16x16x32_bf16 v[92:95], v[188:191], v[172:175], v[92:95]\n\t"
      "ds_read_b128 v[144:147], v233 offset:256\n\t"
      "ds_read_b128 v[148:151], v233 offset:320\n\t"
      "v_mfma_f32_16x16x32_bf16 v[56:59], v[160:163], v[184:187], v[56:59]\n\t"
      "s_mov_b64 exec, s[44:45]\n\t"
      "ds_write_b128 v230, v[96:99]\n\t"
      "ds_write_b128 v230, v[100:103] offset:128\n\t"
      "s_mov_b64 exec, 0xffff\n\t"
      "ds_write_b32 v231, v104 offset:256\n\t"
      "v_xor_b32_e32 v227, 8, v232\n\t"
      "ds_write_b32 v227, v227\n\t"
      "s_mov_b64 exec, -1\n\t"
      "v_mfma_f32_16x16x32_bf16 v[60:63], v[164:167], v[184:187], v[60:63]\n\t"
      "v_mfma_f32_16x16x32_bf16 v[64:67], v[184:187], v[168:171], v[64:67]\n\t"
      "v_mfma_f32_16x16x32_bf16 v[68:71], v[184:187], v[172:175], v[68:71]\n\t"
      "s_waitcnt lgkmcnt(4)\n\t"
      "v_mfma_f32_16x16x32_bf16 v[32:35], v[160:163], v[180:183], v[32:35]\n\t"
      "ds_read_b32 v226, v232\n\t"
      "ds_read_b128 v[116:119], v228 offset:128\n\t"
      "v_mfma_f32_16x16x32_bf16 v[36:39], v[164:167], v[180:183], v[36:39]\n\t"
      "ds_read_b128 v[120:123], v228 offset:192\n\t"
      "ds_read2_b32 v[250:251], v229 offset0:64 offset1:64\n\t"
      "v_mfma_f32_16x16x32_bf16 v[40:43], v[180:183], v[168:171], v[40:43]\n\t"
      "ds_read2_b32 v[252:253], v229 offset0:80 offset1:80\n\t"
      "ds_read_b128 v[124:127], v230 offset:128\n\t"
      "v_mfma_f32_16x16x32_bf16 v[44:47], v[180:183], v[172:175], v[44:47]\n\t"
      "ds_read2_b32 v[224:225], v231 offset0:64 offset1:64\n\t"
      "ds_read_b128 v[108:111], v228\n\t"
      "v_mfma_f32_16x16x32_bf16 v[8:11], v[160:163], v[176:179], v[8:11]\n\t"
      "ds_read_b128 v[112:115], v228 offset:64\n\t"
      "ds_read2_b32 v[204:205], v235 offset0:0 offset1:16\n\t"
      "v_mfma_f32_16x16x32_bf16 v[12:15], v[164:167], v[176:179], v[12:15]\n\t"
      "ds_read2_b32 v[206:207], v235 offset0:32 offset1:48\n\t"
      "ds_read2_b32 v[208:209], v254 offset0:0 offset1:16\n\t"
      "v_mfma_f32_16x16x32_bf16 v[16:19], v[176:179], v[168:171], v[16:19]\n\t"
      "ds_read2_b32 v[210:211], v254 offset0:32 offset1:48\n\t"
      "ds_read2_b32 v[200:201], v234 offset0:192 offset1:208\n\t"
      "v_mfma_f32_16x16x32_bf16 v[20:23], v[176:179], v[172:175], v[20:23]\n\t"
      "ds_read2_b32 v[202:203], v234 offset0:224 offset1:240\n\t"
      "LOOPX_%=:\n\t"
      "s_waitcnt lgkmcnt(14)\n\t"
      "v_min_u32_e32 v226, v226, v212\n\t"
      "v_cmp_eq_u32_e32 vcc, 0, v226\n\t"
      "s_cbranch_vccnz XAR_%=\n\t"
      "XAB_%=:\n\t"
      "s_waitcnt lgkmcnt(12)\n\t"
      "v_pk_fma_f32 v[80:81], v[134:135], v[116:117], v[80:81]\n\t"
      "v_pk_fma_f32 v[82:83], v[134:135], v[118:119], v[82:83]\n\t"
      "v_pk_fma_f32 v[84:85], v[134:135], v[120:121], v[84:85]\n\t"
      "v_pk_fma_f32 v[86:87], v[134:135], v[122:123], v[86:87]\n\t"
      "v_mfma_f32_16x16x32_bf16 v[72:75], v[152:155], v[188:191], v[72:75]\n\t"
      "v_cvt_pk_bf16_f32 v80, v80, v81\n\t"
      "v_cvt_pk_bf16_f32 v81, v82, v83\n\t"
      "v_cvt_pk_bf16_f32 v82, v84, v85\n\t"
      "v_cvt_pk_bf16_f32 v83, v86, v87\n\t"
      "v_mfma_f32_16x16x32_bf16 v[76:79], v[156:159], v[188:191], v[76:79]\n\t"
      "ds_write_b128 v236, v[80:83] offset:2048\n\t"
      "s_waitcnt lgkmcnt(11)\n\t"
      "v_pk_fma_f32 v[88:89], v[148:149], v[250:251], v[88:89]\n\t"
      "v_pk_fma_f32 v[90:91], v[150:151], v[250:251], v[90:91]\n\t"
      "v_pk_fma_f32 v[92:93], v[148:149], v[252:253], v[92:93]\n\t"
      "v_pk_fma_f32 v[94:95], v[150:151], v[252:253], v[94:95]\n\t"
      "v_mfma_f32_16x16x32_bf16 v[48:51], v[152:155], v[184:187], v[48:51]\n\t"
      "v_cvt_pk_bf16_f32 v216, v88, v89\n\t"
      "v_cvt_pk_bf16_f32 v217, v90, v91\n\t"
      "v_cvt_pk_bf16_f32 v218, v92, v93\n\t"
      "v_cvt_pk_bf16_f32 v219, v94, v95\n\t"
      "v_mfma_f32_16x16x32_bf16 v[52:55], v[156:159], v[184:187], v[52:55]\n\t"
      "ds_write_b64 v237, v[216:217] offset:3072\n\t"
      "ds_write_b64 v237, v[218:219] offset:3584\n\t"
      "s_waitcnt lgkmcnt(11)\n\t"
      "v_pk_fma_f32 v[100:101], v[248:249], v[124:125], v[100:101]\n\t"
      "v_pk_fma_f32 v[104:105], v[196:197], v[224:225], v[104:105]\n\t"
      "v_pk_fma_f32 v[102:103], v[248:249], v[126:127], v[102:103]\n\t"
      "v_pk_fma_f32 v[106:107], v[198:199], v[224:225], v[106:107]\n\t"
      "v_mfma_f32_16x16x32_bf16 v[24:27], v[152:155], v[180:183], v[24:27]\n\t"
      "v_cvt_pk_bf16_f32 v220, v100, v101\n\t"
      "v_cvt_pk_bf16_f32 v221, v102, v103\n\t"
      "v_cvt_pk_bf16_f32 v222, v104, v105\n\t"
      "v_cvt_pk_bf16_f32 v223, v106, v107\n\t"
      "v_mfma_f32_16x16x32_bf16 v[28:31], v[156:159], v[180:183], v[28:31]\n\t"
      "ds_write_b64 v238, v[220:221]\n\t"
      "v_lshrrev_b32_e32 v96, 1, v239\n\t"
      "v_add_u32_e32 v96, s8, v96\n\t"
      "ds_write_b64 v96, v[222:223]\n\t"
      "v_xor_b32_e32 v227, 8, v232\n\t"
      "ds_write_b32 v227, v227 offset:4\n\t"
      "v_pk_fma_f32 v[56:57], v[132:133], v[116:117], v[56:57]\n\t"
      "v_pk_fma_f32 v[58:59], v[132:133], v[118:119], v[58:59]\n\t"
      "v_pk_fma_f32 v[60:61], v[132:133], v[120:121], v[60:61]\n\t"
      "v_pk_fma_f32 v[62:63], v[132:133], v[122:123], v[62:63]\n\t"
      "v_mfma_f32_16x16x32_bf16 v[0:3], v[152:155], v[176:179], v[0:3]\n\t"
      "v_cvt_pk_bf16_f32 v56, v56, v57\n\t"
      "v_cvt_pk_bf16_f32 v57, v58, v59\n\t"
      "v_cvt_pk_bf16_f32 v58, v60, v61\n\t"
      "v_cvt_pk_bf16_f32 v59, v62, v63\n\t"
      "v_mfma_f32_16x16x32_bf16 v[4:7], v[156:159], v[176:179], v[4:7]\n\t"
      "v_mov_b32_e32 v164, v216\n\t"
      "v_mov_b32_e32 v165, v217\n\t"
      "v_mov_b32_e32 v174, v218\n\t"
      "v_mov_b32_e32 v175, v219\n\t"
      "s_waitcnt lgkmcnt(12)\n\t"
      "v_pk_fma_f32 v[72:73], v[134:135], v[108:109], v[72:73]\n\t"
      "v_pk_fma_f32 v[74:75], v[134:135], v[110:111], v[74:75]\n\t"
      "v_pk_fma_f32 v[76:77], v[134:135], v[112:113], v[76:77]\n\t"
      "v_pk_fma_f32 v[78:79], v[134:135], v[114:115], v[78:79]\n\t"
      "v_cvt_pk_bf16_f32 v72, v72, v73\n\t"
      "v_cvt_pk_bf16_f32 v73, v74, v75\n\t"
      "v_cvt_pk_bf16_f32 v74, v76, v77\n\t"
      "v_cvt_pk_bf16_f32 v75, v78, v79\n\t"
      "v_cndmask_b32_e64 v74, v74, 0, s[42:43]\n\t"
      "v_cndmask_b32_e64 v75, v75, 0, s[42:43]\n\t"
      "s_nop 1\n\t"
      "v_mfma_f32_16x16x32_bf16 v[76:79], v[56:59], v[72:75], 0\n\t"
      "v_mfma_f32_16x16x32_bf16 v[84:87], v[80:83], v[72:75], 0\n\t"
      "v_pk_fma_f32 v[32:33], v[130:131], v[116:117], v[32:33]\n\t"
      "v_pk_fma_f32 v[34:35], v[130:131], v[118:119], v[34:35]\n\t"
      "v_pk_fma_f32 v[36:37], v[130:131], v[120:121], v[36:37]\n\t"
      "v_pk_fma_f32 v[38:39], v[130:131], v[122:123], v[38:39]\n\t"
      "v_cvt_pk_bf16_f32 v32, v32, v33\n\t"
      "v_cvt_pk_bf16_f32 v33, v34, v35\n\t"
      "v_cvt_pk_bf16_f32 v34, v36, v37\n\t"
      "v_cvt_pk_bf16_f32 v35, v38, v39\n\t"
      "v_pk_fma_f32 v[48:49], v[132:133], v[108:109], v[48:49]\n\t"
      "v_pk_fma_f32 v[50:51], v[132:133], v[110:111], v[50:51]\n\t"
      "v_pk_fma_f32 v[52:53], v[132:133], v[112:113], v[52:53]\n\t"
      "v_pk_fma_f32 v[54:55], v[132:133], v[114:115], v[54:55]\n\t"
      "v_cvt_pk_bf16_f32 v48, v48, v49\n\t"
      "v_cvt_pk_bf16_f32 v49, v50, v51\n\t"
      "v_cvt_pk_bf16_f32 v50, v52, v53\n\t"
      "v_cvt_pk_bf16_f32 v51, v54, v55\n\t"
      "v_cndmask_b32_e64 v50, v50, 0, s[42:43]\n\t"
      "v_cndmask_b32_e64 v51, v51, 0, s[42:43]\n\t"
      "v_pk_fma_f32 v[64:65], v[144:145], v[250:251], v[64:65]\n\t"
      "v_pk_fma_f32 v[66:67], v[146:147], v[250:251], v[66:67]\n\t"
      "v_pk_fma_f32 v[68:69], v[144:145], v[252:253], v[68:69]\n\t"
      "v_pk_fma_f32 v[70:71], v[146:147], v[252:253], v[70:71]\n\t"
      "v_cvt_pk_bf16_f32 v162, v64, v65\n\t"
      "v_cvt_pk_bf16_f32 v163, v66, v67\n\t"
      "v_cvt_pk_bf16_f32 v172, v68, v69\n\t"
      "v_cvt_pk_bf16_f32 v173, v70, v71\n\t"
      "ds_read_b32 v226, v232 offset:4\n\t"
      "ds_read_b128 v[152:155], v236\n\t"
      "ds_read_b64 v[156:157], v237 offset:1024\n\t"
      "ds_read_b64 v[166:167], v237 offset:1536\n\t"
      "v_mfma_f32_16x16x32_bf16 v[52:55], v[32:35], v[48:51], 0\n\t"
      "v_mfma_f32_16x16x32_bf16 v[60:63], v[56:59], v[48:51], 0\n\t"
      "v_pk_fma_f32 v[8:9], v[128:129], v[116:117], v[8:9]\n\t"
      "v_pk_fma_f32 v[10:11], v[128:129], v[118:119], v[10:11]\n\t"
      "v_pk_fma_f32 v[12:13], v[128:129], v[120:121], v[12:13]\n\t"
      "v_pk_fma_f32 v[14:15], v[128:129], v[122:123], v[14:15]\n\t"
      "v_cvt_pk_bf16_f32 v8, v8, v9\n\t"
      "v_cvt_pk_bf16_f32 v9, v10, v11\n\t"
      "v_cvt_pk_bf16_f32 v10, v12, v13\n\t"
      "v_cvt_pk_bf16_f32 v11, v14, v15\n\t"
      "v_pk_fma_f32 v[24:25], v[130:131], v[108:109], v[24:25]\n\t"
      "v_pk_fma_f32 v[26:27], v[130:131], v[110:111], v[26:27]\n\t"
      "v_pk_fma_f32 v[28:29], v[130:131], v[112:113], v[28:29]\n\t"
      "v_pk_fma_f32 v[30:31], v[130:131], v[114:115], v[30:31]\n\t"
      "v_cvt_pk_bf16_f32 v24, v24, v25\n\t"
      "v_cvt_pk_bf16_f32 v25, v26, v27\n\t"
      "v_cvt_pk_bf16_f32 v26, v28, v29\n\t"
      "v_cvt_pk_bf16_f32 v27, v30, v31\n\t"
      "v_cndmask_b32_e64 v26, v26, 0, s[42:43]\n\t"
      "v_cndmask_b32_e64 v27, v27, 0, s[42:43]\n\t"
      "v_pk_fma_f32 v[40:41], v[140:141], v[250:251], v[40:41]\n\t"
      "v_pk_fma_f32 v[42:43], v[142:143], v[250:251], v[42:43]\n\t"
      "v_pk_fma_f32 v[44:45], v[140:141], v[252:253], v[44:45]\n\t"
      "v_pk_fma_f32 v[46:47], v[142:143], v[252:253], v[46:47]\n\t"
      "v_cvt_pk_bf16_f32 v160, v40, v41\n\t"
      "v_cvt_pk_bf16_f32 v161, v42, v43\n\t"
      "v_cvt_pk_bf16_f32 v170, v44, v45\n\t"
      "v_cvt_pk_bf16_f32 v171, v46, v47\n\t"
      "v_mfma_f32_16x16x32_bf16 v[28:31], v[8:11], v[24:27], 0\n\t"
      "v_mfma_f32_16x16x32_bf16 v[36:39], v[32:35], v[24:27], 0\n\t"
      "v_pk_fma_f32 v[0:1], v[128:129], v[108:109], v[0:1]\n\t"
      "v_pk_fma_f32 v[2:3], v[128:129], v[110:111], v[2:3]\n\t"
      "v_pk_fma_f32 v[4:5], v[128:129], v[112:113], v[4:5]\n\t"
      "v_pk_fma_f32 v[6:7], v[128:129], v[114:115], v[6:7]\n\t"
      "v_cvt_pk_bf16_f32 v0, v0, v1\n\t"
      "v_cvt_pk_bf16_f32 v1, v2, v3\n\t"
      "v_cvt_pk_bf16_f32 v2, v4, v5\n\t"
      "v_cvt_pk_bf16_f32 v3, v6, v7\n\t"
      "v_cndmask_b32_e64 v2, v2, 0, s[42:43]\n\t"
      "v_cndmask_b32_e64 v3, v3, 0, s[42:43]\n\t"
      "v_pk_fma_f32 v[16:17], v[136:137], v[250:251], v[16:17]\n\t"
      "v_pk_fma_f32 v[18:19], v[138:139], v[250:251], v[18:19]\n\t"
      "v_pk_fma_f32 v[20:21], v[136:137], v[252:253], v[20:21]\n\t"
      "v_pk_fma_f32 v[22:23], v[138:139], v[252:253], v[22:23]\n\t"
      "v_cvt_pk_bf16_f32 v158, v16, v17\n\t"
      "v_cvt_pk_bf16_f32 v159, v18, v19\n\t"
      "v_cvt_pk_bf16_f32 v168, v20, v21\n\t"
      "v_cvt_pk_bf16_f32 v169, v22, v23\n\t"
      "s_waitcnt lgkmcnt(3)\n\t"
      "v_cmp_eq_u32_e32 vcc, 0, v226\n\t"
      "s_cbranch_vccnz XBR_%=\n\t"
      "XBB_%=:\n\t"
      "s_waitcnt lgkmcnt(2)\n\t"
      "v_mfma_f32_16x16x32_bf16 v[4:7], v[152:155], v[0:3], v[244:247]\n\t"
      "v_mfma_f32_16x16x32_bf16 v[12:15], v[8:11], v[0:3], 0\n\t"
      "v_mov_b32_e32 v252, 163312\n\t"
      "ds_read_b64 v[250:251], v252\n\t"
      "v_cndmask_b32_e64 v76, v76, v77, s[36:37]\n\t"
      "v_cndmask_b32_e64 v84, v84, v85, s[36:37]\n\t"
      "v_cndmask_b32_e64 v78, v78, v79, s[36:37]\n\t"
      "v_cndmask_b32_e64 v86, v86, v87, s[36:37]\n\t"
      "v_cndmask_b32_e64 v179, v76, v84, s[38:39]\n\t"
      "v_cndmask_b32_e64 v183, v78, v86, s[40:41]\n\t"
      "v_cndmask_b32_e64 v52, v52, v53, s[36:37]\n\t"
      "v_cndmask_b32_e64 v60, v60, v61, s[36:37]\n\t"
      "v_cndmask_b32_e64 v54, v54, v55, s[36:37]\n\t"
      "v_cndmask_b32_e64 v62, v62, v63, s[36:37]\n\t"
      "v_cndmask_b32_e64 v178, v52, v60, s[38:39]\n\t"
      "v_cndmask_b32_e64 v182, v54, v62, s[40:41]\n\t"
      "v_pk_mul_f32 v[178:179], v[178:179], v[206:207]\n\t"
      "v_pk_mul_f32 v[182:183], v[182:183], v[210:211]\n\t"
      "v_max_f32_e32 v219, v179, v183\n\t"
      "v_max_f32_e32 v218, v178, v182\n\t"
      "v_cndmask_b32_e64 v28, v28, v29, s[36:37]\n\t"
      "v_cndmask_b32_e64 v36, v36, v37, s[36:37]\n\t"
      "v_cndmask_b32_e64 v30, v30, v31, s[36:37]\n\t"
      "v_cndmask_b32_e64 v38, v38, v39, s[36:37]\n\t"
      "v_cndmask_b32_e64 v177, v28, v36, s[38:39]\n\t"
      "v_cndmask_b32_e64 v181, v30, v38, s[40:41]\n\t"
      "v_cndmask_b32_e64 v4, v4, v5, s[36:37]\n\t"
      "v_cndmask_b32_e64 v12, v12, v13, s[36:37]\n\t"
      "v_cndmask_b32_e64 v6, v6, v7, s[36:37]\n\t"
      "v_cndmask_b32_e64 v14, v14, v15, s[36:37]\n\t"
      "v_cndmask_b32_e64 v176, v4, v12, s[38:39]\n\t"
      "v_cndmask_b32_e64 v180, v6, v14, s[40:41]\n\t"
      "v_pk_mul_f32 v[176:177], v[176:177], v[204:205]\n\t"
      "v_pk_mul_f32 v[180:181], v[180:181], v[208:209]\n\t"
      "v_max_f32_e32 v217, v177, v181\n\t"
      "v_max_f32_e32 v216, v176, v180\n\t"
      "v_permlane16_swap_b32_e32 v218, v219\n\t"
      "s_nop 0\n\t"
      "v_permlane16_swap_b32_e32 v216, v217\n\t"
      "v_max_f32_e32 v218, v218, v219\n\t"
      "v_max_f32_e32 v216, v216, v217\n\t"
      "v_mov_b32_e32 v219, v218\n\t"
      "v_mov_b32_e32 v217, v216\n\t"
      "s_nop 0\n\t"
      "v_permlane32_swap_b32_e32 v218, v219\n\t"
      "v_permlane32_swap_b32_e32 v216, v217\n\t"
      "v_max_f32_e32 v218, v218, v219\n\t"
      "v_max_f32_e32 v216, v216, v217\n\t"
      "v_mov_b32_e32 v219, v218\n\t"
      "v_mov_b32_e32 v217, v216\n\t"
      "s_nop 0\n\t"
      "v_permlane16_swap_b32_e32 v218, v219\n\t"
      "v_permlane16_swap_b32_e32 v216, v217\n\t"
      "v_pk_add_f32 v[178:179], v[178:179], v[218:219] neg_lo:[0,1] neg_hi:[0,1]\n\t"
      "v_pk_add_f32 v[182:183], v[182:183], v[218:219] neg_lo:[0,1] neg_hi:[0,1]\n\t"
      "v_pk_add_f32 v[176:177], v[176:177], v[216:217] neg_lo:[0,1] neg_hi:[0,1]\n\t"
      "v_pk_add_f32 v[180:181], v[180:181], v[216:217] neg_lo:[0,1] neg_hi:[0,1]\n\t"
      "v_pk_mul_f32 v[178:179], v[178:179], v[202:203]\n\t"
      "v_pk_mul_f32 v[182:183], v[182:183], v[202:203]\n\t"
      "v_pk_mul_f32 v[176:177], v[176:177], v[200:201]\n\t"
      "v_pk_mul_f32 v[180:181], v[180:181], v[200:201]\n\t"
      "v_exp_f32_e32 v179, v179\n\t"
      "v_exp_f32_e32 v183, v183\n\t"
      "v_exp_f32_e32 v178, v178\n\t"
      "v_exp_f32_e32 v182, v182\n\t"
      "v_exp_f32_e32 v177, v177\n\t"
      "v_exp_f32_e32 v181, v181\n\t"
      "v_exp_f32_e32 v176, v176\n\t"
      "v_exp_f32_e32 v180, v180\n\t"
      "s_waitcnt lgkmcnt(0)\n\t"
      "v_mov_b32_e32 v253, s12\n\t"
      "s_mov_b64 exec, 1\n\t"
      "global_store_dwordx2 v253, v[250:251], s[10:11] sc1\n\t"
      "s_mov_b64 exec, -1\n\t"
      "v_pk_add_f32 v[218:219], v[178:179], v[182:183]\n\t"
      "v_pk_mul_f32 v[178:179], v[178:179], v[206:207]\n\t"
      "v_pk_mul_f32 v[182:183], v[182:183], v[210:211]\n\t"
      "v_cvt_pk_bf16_f32 v179, v179, v179\n\t"
      "v_cvt_pk_bf16_f32 v183, v183, v183\n\t"
      "v_and_b32_e32 v72, v240, v179\n\t"
      "v_and_b32_e32 v73, v241, v183\n\t"
      "v_and_b32_e32 v74, v242, v179\n\t"
      "v_and_b32_e32 v75, v243, v183\n\t"
      "v_cvt_pk_bf16_f32 v178, v178, v178\n\t"
      "v_cvt_pk_bf16_f32 v182, v182, v182\n\t"
      "v_and_b32_e32 v48, v240, v178\n\t"
      "v_and_b32_e32 v49, v241, v182\n\t"
      "v_and_b32_e32 v50, v242, v178\n\t"
      "v_and_b32_e32 v51, v243, v182\n\t"
      "v_mfma_f32_16x16x32_bf16 v[88:91], v[162:165], v[72:75], 0\n\t"
      "v_mfma_f32_16x16x32_bf16 v[92:95], v[172:175], v[72:75], 0\n\t"
      "v_pk_add_f32 v[216:217], v[176:177], v[180:181]\n\t"
      "v_pk_mul_f32 v[176:177], v[176:177], v[204:205]\n\t"
      "v_pk_mul_f32 v[180:181], v[180:181], v[208:209]\n\t"
      "v_cvt_pk_bf16_f32 v177, v177, v177\n\t"
      "v_cvt_pk_bf16_f32 v181, v181, v181\n\t"
      "v_and_b32_e32 v24, v240, v177\n\t"
      "v_and_b32_e32 v25, v241, v181\n\t"
      "v_and_b32_e32 v26, v242, v177\n\t"
      "v_and_b32_e32 v27, v243, v181\n\t"
      "v_mfma_f32_16x16x32_bf16 v[64:67], v[160:163], v[48:51], 0\n\t"
      "v_mfma_f32_16x16x32_bf16 v[68:71], v[170:173], v[48:51], 0\n\t"
      "v_cvt_pk_bf16_f32 v176, v176, v176\n\t"
      "v_cvt_pk_bf16_f32 v180, v180, v180\n\t"
      "v_and_b32_e32 v0, v240, v176\n\t"
      "v_and_b32_e32 v1, v241, v180\n\t"
      "v_and_b32_e32 v2, v242, v176\n\t"
      "v_and_b32_e32 v3, v243, v180\n\t"
      "v_mfma_f32_16x16x32_bf16 v[40:43], v[158:161], v[24:27], 0\n\t"
      "v_mfma_f32_16x16x32_bf16 v[44:47], v[168:171], v[24:27], 0\n\t"
      "s_add_u32 s48, s4, 49152\n\t"
      "s_addc_u32 s49, s5, 0\n\t"
      "v_mfma_f32_16x16x32_bf16 v[16:19], v[156:159], v[0:3], 0\n\t"
      "v_mfma_f32_16x16x32_bf16 v[20:23], v[166:169], v[0:3], 0\n\t"
      "v_permlane16_swap_b32_e32 v218, v219\n\t"
      "v_permlane16_swap_b32_e32 v216, v217\n\t"
      "v_add_f32_e32 v218, v218, v219\n\t"
      "v_add_f32_e32 v216, v216, v217\n\t"
      "v_mov_b32_e32 v219, v218\n\t"
      "v_mov_b32_e32 v217, v216\n\t"
      "s_nop 0\n\t"
      "v_permlane32_swap_b32_e32 v218, v219\n\t"
      "v_permlane32_swap_b32_e32 v216, v217\n\t"
      "v_add_f32_e32 v218, v218, v219\n\t"
      "v_add_f32_e32 v216, v216, v217\n\t"
      "v_mov_b32_e32 v219, v218\n\t"
      "v_mov_b32_e32 v217, v216\n\t"
      "s_nop 0\n\t"
      "v_permlane16_swap_b32_e32 v218, v219\n\t"
      "v_permlane16_swap_b32_e32 v216, v217\n\t"
      "v_rcp_f32_e32 v190, v219\n\t"
      "v_rcp_f32_e32 v188, v218\n\t"
      "v_rcp_f32_e32 v186, v217\n\t"
      "v_rcp_f32_e32 v184, v216\n\t"
      "v_mov_b32_e32 v191, v190\n\t"
      "v_mov_b32_e32 v189, v188\n\t"
      "v_mov_b32_e32 v187, v186\n\t"
      "v_mov_b32_e32 v185, v184\n\t"
      "v_lshrrev_b32_e32 v252, 2, v239\n\t"
      "v_add_u32_e32 v252, s13, v252\n\t"
      "v_pk_mul_f32 v[88:89], v[190:191], v[88:89]\n\t"
      "v_pk_mul_f32 v[90:91], v[190:191], v[90:91]\n\t"
      "v_pk_mul_f32 v[92:93], v[190:191], v[92:93]\n\t"
      "v_pk_mul_f32 v[94:95], v[190:191], v[94:95]\n\t"
      "v_cvt_pk_bf16_f32 v80, v88, v89\n\t"
      "v_cvt_pk_bf16_f32 v81, v90, v91\n\t"
      "v_cvt_pk_bf16_f32 v82, v92, v93\n\t"
      "v_cvt_pk_bf16_f32 v83, v94, v95\n\t"
      "global_store_dwordx4 v239, v[80:83], s[48:49] sc1\n\t"
      "s_sub_u32 s48, s48, 16384\n\t"
      "s_subb_u32 s49, s49, 0\n\t"
      "v_add3_u32 v219, v80, v81, v82\n\t"
      "v_add_u32_e32 v219, v219, v83\n\t"
      "v_pk_mul_f32 v[64:65], v[188:189], v[64:65]\n\t"
      "v_pk_mul_f32 v[66:67], v[188:189], v[66:67]\n\t"
      "v_pk_mul_f32 v[68:69], v[188:189], v[68:69]\n\t"
      "v_pk_mul_f32 v[70:71], v[188:189], v[70:71]\n\t"
      "v_cvt_pk_bf16_f32 v56, v64, v65\n\t"
      "v_cvt_pk_bf16_f32 v57, v66, v67\n\t"
      "v_cvt_pk_bf16_f32 v58, v68, v69\n\t"
      "v_cvt_pk_bf16_f32 v59, v70, v71\n\t"
      "global_store_dwordx4 v239, v[56:59], s[48:49] sc1\n\t"
      "s_sub_u32 s48, s48, 16384\n\t"
      "s_subb_u32 s49, s49, 0\n\t"
      "v_add3_u32 v218, v56, v57, v58\n\t"
      "v_add_u32_e32 v218, v218, v59\n\t"
      "v_pk_mul_f32 v[40:41], v[186:187], v[40:41]\n\t"
      "v_pk_mul_f32 v[42:43], v[186:187], v[42:43]\n\t"
      "v_pk_mul_f32 v[44:45], v[186:187], v[44:45]\n\t"
      "v_pk_mul_f32 v[46:47], v[186:187], v[46:47]\n\t"
      "v_cvt_pk_bf16_f32 v32, v40, v41\n\t"
      "v_cvt_pk_bf16_f32 v33, v42, v43\n\t"
      "v_cvt_pk_bf16_f32 v34, v44, v45\n\t"
      "v_cvt_pk_bf16_f32 v35, v46, v47\n\t"
      "global_store_dwordx4 v239, v[32:35], s[48:49] sc1\n\t"
      "s_sub_u32 s48, s48, 16384\n\t"
      "s_subb_u32 s49, s49, 0\n\t"
      "v_add3_u32 v217, v32, v33, v34\n\t"
      "v_add_u32_e32 v217, v217, v35\n\t"
      "s_nop 1\n\t"
      "v_permlane32_swap_b32_e32 v217, v219\n\t"
      "v_add_u32_e32 v217, v217, v219\n\t"
      "v_pk_mul_f32 v[16:17], v[184:185], v[16:17]\n\t"
      "v_pk_mul_f32 v[18:19], v[184:185], v[18:19]\n\t"
      "v_pk_mul_f32 v[20:21], v[184:185], v[20:21]\n\t"
      "v_pk_mul_f32 v[22:23], v[184:185], v[22:23]\n\t"
      "v_cvt_pk_bf16_f32 v8, v16, v17\n\t"
      "v_cvt_pk_bf16_f32 v9, v18, v19\n\t"
      "v_cvt_pk_bf16_f32 v10, v20, v21\n\t"
      "v_cvt_pk_bf16_f32 v11, v22, v23\n\t"
      "global_store_dwordx4 v239, v[8:11], s[48:49] sc1\n\t"
      "v_add3_u32 v216, v8, v9, v10\n\t"
      "v_add_u32_e32 v216, v216, v11\n\t"
      "s_nop 1\n\t"
      "v_permlane32_swap_b32_e32 v216, v218\n\t"
      "v_add_u32_e32 v216, v216, v218\n\t"
      "s_nop 1\n\t"
      "v_permlane16_swap_b32_e32 v216, v217\n\t"
      "v_add3_u32 v216, v216, v217, v250\n\t"
      "global_store_dword v252, v216, s[10:11] sc1\n\t"
      "s_barrier\n\t"
      "s_nop 1\n\t"
      "s_branch CEND_%=\n\t"
      "LOOP1_%=:\n\t"
      "s_waitcnt vmcnt(0)\n\t"
      "s_barrier\n\t"
      "ds_read_b64_tr_b16 v[108:109], v226\n\t"
      "ds_read_b64_tr_b16 v[110:111], v226 offset:512\n\t"
      "ds_read_b128 v[132:135], v224\n\t"
      "ds_read_b128 v[136:139], v224 offset:5120\n\t"
      "ds_read_b128 v[140:143], v224 offset:10240\n\t"
      "ds_read_b128 v[144:147], v224 offset:15360\n\t"
      "ds_read_b64_tr_b16 v[112:113], v227\n\t"
      "ds_read_b64_tr_b16 v[114:115], v227 offset:512\n\t"
      "ds_read_b128 v[148:151], v225\n\t"
      "ds_read_b64_tr_b16 v[116:117], v226 offset:1024\n\t"
      "ds_read_b64_tr_b16 v[118:119], v226 offset:1536\n\t"
      "ds_read_b64_tr_b16 v[120:121], v227 offset:1024\n\t"
      "ds_read_b64_tr_b16 v[122:123], v227 offset:1536\n\t"
      "ds_read_b64_tr_b16 v[124:125], v226 offset:2048\n\t"
      "ds_read_b64_tr_b16 v[126:127], v226 offset:2560\n\t"
      "ds_read_b64_tr_b16 v[128:129], v227 offset:2048\n\t"
      "ds_read_b64_tr_b16 v[130:131], v227 offset:2560\n\t"
      "s_branch EVEN1_%=\n\t"
      "s_waitcnt lgkmcnt(0)\n\t"
      ".p2align 6\n\t"
      "ODD1_%=:\n\t"
      "s_waitcnt lgkmcnt(0)\n\t"
      "v_mfma_f32_16x16x32_bf16 v[0:3], v[152:155], v[176:179], v[0:3]\n\t"
      "v_add_u32_e32 v224, s46, v224\n\t"
      "v_add_u32_e32 v226, s46, v226\n\t"
      "v_mfma_f32_16x16x32_bf16 v[24:27], v[152:155], v[180:183], v[24:27]\n\t"
      "v_add_u32_e32 v227, s46, v227\n\t"
      "v_add_u32_e32 v225, s46, v225\n\t"
      "v_mfma_f32_16x16x32_bf16 v[48:51], v[152:155], v[184:187], v[48:51]\n\t"
      "s_barrier\n\t"
      "ds_read_b64_tr_b16 v[108:109], v226\n\t"
      "v_mfma_f32_16x16x32_bf16 v[72:75], v[152:155], v[188:191], v[72:75]\n\t"
      "ds_read_b64_tr_b16 v[110:111], v226 offset:512\n\t"
      "v_mfma_f32_16x16x32_bf16 v[4:7], v[156:159], v[176:179], v[4:7]\n\t"
      "ds_read_b128 v[132:135], v224\n\t"
      "v_mfma_f32_16x16x32_bf16 v[28:31], v[156:159], v[180:183], v[28:31]\n\t"
      "ds_read_b128 v[136:139], v224 offset:5120\n\t"
      "v_mfma_f32_16x16x32_bf16 v[52:55], v[156:159], v[184:187], v[52:55]\n\t"
      "ds_read_b128 v[140:143], v224 offset:10240\n\t"
      "v_mfma_f32_16x16x32_bf16 v[76:79], v[156:159], v[188:191], v[76:79]\n\t"
      "ds_read_b128 v[144:147], v224 offset:15360\n\t"
      "v_mfma_f32_16x16x32_bf16 v[96:99], v[156:159], v[192:195], v[96:99]\n\t"
      "v_mfma_f32_16x16x32_bf16 v[8:11], v[160:163], v[176:179], v[8:11]\n\t"
      "ds_read_b64_tr_b16 v[112:113], v227\n\t"
      "v_mfma_f32_16x16x32_bf16 v[32:35], v[160:163], v[180:183], v[32:35]\n\t"
      "ds_read_b64_tr_b16 v[114:115], v227 offset:512\n\t"
      "v_mfma_f32_16x16x32_bf16 v[56:59], v[160:163], v[184:187], v[56:59]\n\t"
      "ds_read_b128 v[148:151], v225\n\t"
      "v_mfma_f32_16x16x32_bf16 v[80:83], v[160:163], v[188:191], v[80:83]\n\t"
      "ds_read_b64_tr_b16 v[116:117], v226 offset:1024\n\t"
      "v_mfma_f32_16x16x32_bf16 v[12:15], v[164:167], v[176:179], v[12:15]\n\t"
      "ds_read_b64_tr_b16 v[118:119], v226 offset:1536\n\t"
      "v_mfma_f32_16x16x32_bf16 v[36:39], v[164:167], v[180:183], v[36:39]\n\t"
      "ds_read_b64_tr_b16 v[120:121], v227 offset:1024\n\t"
      "v_mfma_f32_16x16x32_bf16 v[60:63], v[164:167], v[184:187], v[60:63]\n\t"
      "v_mfma_f32_16x16x32_bf16 v[84:87], v[164:167], v[188:191], v[84:87]\n\t"
      "ds_read_b64_tr_b16 v[122:123], v227 offset:1536\n\t"
      "v_mfma_f32_16x16x32_bf16 v[100:103], v[164:167], v[192:195], v[100:103]\n\t"
      "ds_read_b64_tr_b16 v[124:125], v226 offset:2048\n\t"
      "v_mfma_f32_16x16x32_bf16 v[16:19], v[176:179], v[168:171], v[16:19]\n\t"
      "ds_read_b64_tr_b16 v[126:127], v226 offset:2560\n\t"
      "v_mfma_f32_16x16x32_bf16 v[40:43], v[180:183], v[168:171], v[40:43]\n\t"
      "ds_read_b64_tr_b16 v[128:129], v227 offset:2048\n\t"
      "v_mfma_f32_16x16x32_bf16 v[64:67], v[184:187], v[168:171], v[64:67]\n\t"
      "ds_read_b64_tr_b16 v[130:131], v227 offset:2560\n\t"
      "v_mfma_f32_16x16x32_bf16 v[88:91], v[188:191], v[168:171], v[88:91]\n\t"
      "v_mfma_f32_16x16x32_bf16 v[20:23], v[176:179], v[172:175], v[20:23]\n\t"
      "v_mfma_f32_16x16x32_bf16 v[44:47], v[180:183], v[172:175], v[44:47]\n\t"
      "v_add_u32_e32 v248, s46, v248\n\t"
      "v_add_u32_e32 v249, s46, v249\n\t"
      "v_mfma_f32_16x16x32_bf16 v[68:71], v[184:187], v[172:175], v[68:71]\n\t"
      "v_mfma_f32_16x16x32_bf16 v[92:95], v[188:191], v[172:175], v[92:95]\n\t"
      "v_mfma_f32_16x16x32_bf16 v[104:107], v[192:195], v[172:175], v[104:107]\n\t"
      "EVEN1_%=:\n\t"
      "s_waitcnt lgkmcnt(0)\n\t"
      "v_mfma_f32_16x16x32_bf16 v[0:3], v[108:111], v[132:135], v[0:3]\n\t"
      "ds_read_b64_tr_b16 v[152:153], v226 offset:20480\n\t"
      "v_mfma_f32_16x16x32_bf16 v[24:27], v[108:111], v[136:139], v[24:27]\n\t"
      "ds_read_b64_tr_b16 v[154:155], v226 offset:20992\n\t"
      "v_mfma_f32_16x16x32_bf16 v[48:51], v[108:111], v[140:143], v[48:51]\n\t"
      "ds_read_b128 v[176:179], v248\n\t"
      "v_mfma_f32_16x16x32_bf16 v[72:75], v[108:111], v[144:147], v[72:75]\n\t"
      "ds_read_b128 v[180:183], v248 offset:5120\n\t"
      "v_mfma_f32_16x16x32_bf16 v[4:7], v[112:115], v[132:135], v[4:7]\n\t"
      "v_mfma_f32_16x16x32_bf16 v[28:31], v[112:115], v[136:139], v[28:31]\n\t"
      "ds_read_b128 v[184:187], v248 offset:10240\n\t"
      "v_mfma_f32_16x16x32_bf16 v[52:55], v[112:115], v[140:143], v[52:55]\n\t"
      "ds_read_b128 v[188:191], v248 offset:15360\n\t"
      "v_mfma_f32_16x16x32_bf16 v[76:79], v[112:115], v[144:147], v[76:79]\n\t"
      "ds_read_b64_tr_b16 v[156:157], v227 offset:20480\n\t"
      "v_mfma_f32_16x16x32_bf16 v[96:99], v[112:115], v[148:151], v[96:99]\n\t"
      "v_mfma_f32_16x16x32_bf16 v[8:11], v[116:119], v[132:135], v[8:11]\n\t"
      "ds_read_b64_tr_b16 v[158:159], v227 offset:20992\n\t"
      "v_mfma_f32_16x16x32_bf16 v[32:35], v[116:119], v[136:139], v[32:35]\n\t"
      "ds_read_b128 v[192:195], v249\n\t"
      "v_mfma_f32_16x16x32_bf16 v[56:59], v[116:119], v[140:143], v[56:59]\n\t"
      "ds_read_b64_tr_b16 v[160:161], v226 offset:21504\n\t"
      "v_mfma_f32_16x16x32_bf16 v[80:83], v[116:119], v[144:147], v[80:83]\n\t"
      "ds_read_b64_tr_b16 v[162:163], v226 offset:22016\n\t"
      "v_mfma_f32_16x16x32_bf16 v[12:15], v[120:123], v[132:135], v[12:15]\n\t"
      "v_mfma_f32_16x16x32_bf16 v[36:39], v[120:123], v[136:139], v[36:39]\n\t"
      "ds_read_b64_tr_b16 v[164:165], v227 offset:21504\n\t"
      "v_mfma_f32_16x16x32_bf16 v[60:63], v[120:123], v[140:143], v[60:63]\n\t"
      "ds_read_b64_tr_b16 v[166:167], v227 offset:22016\n\t"
      "v_mfma_f32_16x16x32_bf16 v[84:87], v[120:123], v[144:147], v[84:87]\n\t"
      "ds_read_b64_tr_b16 v[168:169], v226 offset:22528\n\t"
      "v_mfma_f32_16x16x32_bf16 v[100:103], v[120:123], v[148:151], v[100:103]\n\t"
      "v_mfma_f32_16x16x32_bf16 v[16:19], v[132:135], v[124:127], v[16:19]\n\t"
      "ds_read_b64_tr_b16 v[170:171], v226 offset:23040\n\t"
      "v_mfma_f32_16x16x32_bf16 v[40:43], v[136:139], v[124:127], v[40:43]\n\t"
      "ds_read_b64_tr_b16 v[172:173], v227 offset:22528\n\t"
      "v_mfma_f32_16x16x32_bf16 v[64:67], v[140:143], v[124:127], v[64:67]\n\t"
      "ds_read_b64_tr_b16 v[174:175], v227 offset:23040\n\t"
      "s_cmp_eq_u32 s47, 4\n\t"
      "s_cselect_b32 s46, -2, 1\n\t"
      "v_mfma_f32_16x16x32_bf16 v[88:91], v[144:147], v[124:127], v[88:91]\n\t"
      "s_cmp_eq_u32 s47, 1\n\t"
      "s_cselect_b32 s46, -2, s46\n\t"
      "v_mfma_f32_16x16x32_bf16 v[20:23], v[132:135], v[128:131], v[20:23]\n\t"
      "s_mul_i32 s46, s46, 43008\n\t"
      "v_mfma_f32_16x16x32_bf16 v[44:47], v[136:139], v[128:131], v[44:47]\n\t"
      "v_mfma_f32_16x16x32_bf16 v[68:71], v[140:143], v[128:131], v[68:71]\n\t"
      "v_mfma_f32_16x16x32_bf16 v[92:95], v[144:147], v[128:131], v[92:95]\n\t"
      "s_sub_u32 s47, s47, 1\n\t"
      "v_mfma_f32_16x16x32_bf16 v[104:107], v[148:151], v[128:131], v[104:107]\n\t"
      "s_cbranch_scc0 ODD1_%=\n\t"
      "s_waitcnt lgkmcnt(0)\n\t"
      "v_mfma_f32_16x16x32_bf16 v[96:99], v[156:159], v[192:195], v[96:99]\n\t"
      "v_mov_b32_e32 v212, s9\n\t"
      "ds_read_b32 v212, v212\n\t"
      "ds_read2_b32 v[128:129], v234 offset0:32 offset1:32\n\t"
      "v_mfma_f32_16x16x32_bf16 v[100:103], v[164:167], v[192:195], v[100:103]\n\t"
      "ds_read2_b32 v[130:131], v234 offset0:48 offset1:48\n\t"
      "v_mfma_f32_16x16x32_bf16 v[104:107], v[192:195], v[172:175], v[104:107]\n\t"
      "ds_read2_b32 v[132:133], v234 offset0:64 offset1:64\n\t"
      "ds_read2_b32 v[134:135], v234 offset0:80 offset1:80\n\t"
      "v_mfma_f32_16x16x32_bf16 v[80:83], v[160:163], v[188:191], v[80:83]\n\t"
      "ds_read2_b32 v[248:249], v234 offset0:0 offset1:0\n\t"
      "v_mfma_f32_16x16x32_bf16 v[84:87], v[164:167], v[188:191], v[84:87]\n\t"
      "ds_read_b128 v[196:199], v233\n\t"
      "ds_read_b128 v[136:139], v233 offset:128\n\t"
      "v_mfma_f32_16x16x32_bf16 v[88:91], v[188:191], v[168:171], v[88:91]\n\t"
      "ds_read_b128 v[140:143], v233 offset:192\n\t"
      "v_mfma_f32_16x16x32_bf16 v[92:95], v[188:191], v[172:175], v[92:95]\n\t"
      "ds_read_b128 v[144:147], v233 offset:256\n\t"
      "ds_read_b128 v[148:151], v233 offset:320\n\t"
      "v_mfma_f32_16x16x32_bf16 v[56:59], v[160:163], v[184:187], v[56:59]\n\t"
      "s_mov_b64 exec, s[44:45]\n\t"
      "ds_write_b128 v230, v[96:99]\n\t"
      "ds_write_b128 v230, v[100:103] offset:128\n\t"
      "s_mov_b64 exec, 0xffff\n\t"
      "ds_write_b32 v231, v104 offset:256\n\t"
      "v_xor_b32_e32 v227, 8, v232\n\t"
      "ds_write_b32 v227, v227\n\t"
      "s_mov_b64 exec, -1\n\t"
      "v_mfma_f32_16x16x32_bf16 v[60:63], v[164:167], v[184:187], v[60:63]\n\t"
      "v_mfma_f32_16x16x32_bf16 v[64:67], v[184:187], v[168:171], v[64:67]\n\t"
      "v_mfma_f32_16x16x32_bf16 v[68:71], v[184:187], v[172:175], v[68:71]\n\t"
      "s_waitcnt lgkmcnt(4)\n\t"
      "v_mfma_f32_16x16x32_bf16 v[32:35], v[160:163], v[180:183], v[32:35]\n\t"
      "ds_read_b32 v226, v232\n\t"
      "ds_read_b128 v[116:119], v228 offset:128\n\t"
      "v_mfma_f32_16x16x32_bf16 v[36:39], v[164:167], v[180:183], v[36:39]\n\t"
      "ds_read_b128 v[120:123], v228 offset:192\n\t"
      "ds_read2_b32 v[250:251], v229 offset0:64 offset1:64\n\t"
      "v_mfma_f32_16x16x32_bf16 v[40:43], v[180:183], v[168:171], v[40:43]\n\t"
      "ds_read2_b32 v[252:253], v229 offset0:80 offset1:80\n\t"
      "ds_read_b128 v[124:127], v230 offset:128\n\t"
      "v_mfma_f32_16x16x32_bf16 v[44:47], v[180:183], v[172:175], v[44:47]\n\t"
      "ds_read2_b32 v[224:225], v231 offset0:64 offset1:64\n\t"
      "ds_read_b128 v[108:111], v228\n\t"
      "v_mfma_f32_16x16x32_bf16 v[8:11], v[160:163], v[176:179], v[8:11]\n\t"
      "ds_read_b128 v[112:115], v228 offset:64\n\t"
      "ds_read2_b32 v[204:205], v235 offset0:0 offset1:16\n\t"
      "v_mfma_f32_16x16x32_bf16 v[12:15], v[164:167], v[176:179], v[12:15]\n\t"
      "ds_read2_b32 v[206:207], v235 offset0:32 offset1:48\n\t"
      "ds_read2_b32 v[208:209], v254 offset0:0 offset1:16\n\t"
      "v_mfma_f32_16x16x32_bf16 v[16:19], v[176:179], v[168:171], v[16:19]\n\t"
      "ds_read2_b32 v[210:211], v254 offset0:32 offset1:48\n\t"
      "ds_read2_b32 v[200:201], v234 offset0:192 offset1:208\n\t"
      "v_mfma_f32_16x16x32_bf16 v[20:23], v[176:179], v[172:175], v[20:23]\n\t"
      "ds_read2_b32 v[202:203], v234 offset0:224 offset1:240\n\t"
      "s_branch LOOPX_%=\n\t"
      "XAR_%=:\n\t"
      "s_mov_b32 s51, 0\n\t"
      "XAL_%=:\n\t"
      "s_add_u32 s51, s51, 1\n\t"
      "s_cmp_lt_u32 s51, 262144\n\t"
      "s_cbranch_scc0 XAX_%=\n\t"
      "s_waitcnt lgkmcnt(0)\n\t"
      "v_mov_b32_e32 v212, s9\n\t"
      "ds_read_b32 v212, v212\n\t"
      "ds_read2_b32 v[128:129], v234 offset0:32 offset1:32\n\t"
      "ds_read2_b32 v[130:131], v234 offset0:48 offset1:48\n\t"
      "ds_read2_b32 v[132:133], v234 offset0:64 offset1:64\n\t"
      "ds_read2_b32 v[134:135], v234 offset0:80 offset1:80\n\t"
      "ds_read2_b32 v[248:249], v234 offset0:0 offset1:0\n\t"
      "ds_read_b128 v[196:199], v233\n\t"
      "ds_read_b128 v[136:139], v233 offset:128\n\t"
      "ds_read_b128 v[140:143], v233 offset:192\n\t"
      "ds_read_b128 v[144:147], v233 offset:256\n\t"
      "ds_read_b128 v[148:151], v233 offset:320\n\t"
      "s_waitcnt lgkmcnt(0)\n\t"
      "ds_read_b32 v226, v232\n\t"
      "ds_read_b128 v[116:119], v228 offset:128\n\t"
      "ds_read_b128 v[120:123], v228 offset:192\n\t"
      "ds_read2_b32 v[250:251], v229 offset0:64 offset1:64\n\t"
      "ds_read2_b32 v[252:253], v229 offset0:80 offset1:80\n\t"
      "ds_read_b128 v[124:127], v230 offset:128\n\t"
      "ds_read2_b32 v[224:225], v231 offset0:64 offset1:64\n\t"
      "ds_read_b128 v[108:111], v228\n\t"
      "ds_read_b128 v[112:115], v228 offset:64\n\t"
      "ds_read2_b32 v[204:205], v235 offset0:0 offset1:16\n\t"
      "ds_read2_b32 v[206:207], v235 offset0:32 offset1:48\n\t"
      "ds_read2_b32 v[208:209], v254 offset0:0 offset1:16\n\t"
      "ds_read2_b32 v[210:211], v254 offset0:32 offset1:48\n\t"
      "ds_read2_b32 v[200:201], v234 offset0:192 offset1:208\n\t"
      "ds_read2_b32 v[202:203], v234 offset0:224 offset1:240\n\t"
      "s_waitcnt lgkmcnt(0)\n\t"
      "v_min_u32_e32 v226, v226, v212\n\t"
      "v_cmp_eq_u32_e32 vcc, 0, v226\n\t"
      "s_cbranch_vccnz XAL_%=\n\t"
      "XAX_%=:\n\t"
      "s_branch XAB_%=\n\t"
      "XBR_%=:\n\t"
      "s_mov_b32 s51, 0\n\t"
      "XBL_%=:\n\t"
      "s_add_u32 s51, s51, 1\n\t"
      "s_cmp_lt_u32 s51, 262144\n\t"
      "s_cbranch_scc0 XBX_%=\n\t"
      "s_waitcnt lgkmcnt(0)\n\t"
      "ds_read_b32 v226, v232 offset:4\n\t"
      "ds_read_b128 v[152:155], v236\n\t"
      "ds_read_b64 v[156:157], v237 offset:1024\n\t"
      "ds_read_b64 v[166:167], v237 offset:1536\n\t"
      "s_waitcnt lgkmcnt(0)\n\t"
      "v_cmp_eq_u32_e32 vcc, 0, v226\n\t"
      "s_cbranch_vccnz XBL_%=\n\t"
      "XBX_%=:\n\t"
      "s_branch XBB_%=\n\t"
      "CEND_%=:\n\t"
      : "+{s[4:11]}"(s), "+{s12}"(hint), "+{s13}"(chk), "+{v[224:239]}"(v), "+{v254}"(vk1)
      :
      : "memory", "vcc", "scc",
        "v0", "v1", "v2", "v3", "v4", "v5", "v6", "v7", "v8", "v9", "v10", "v11", "v12", "v13", "v14", "v15", "v16",
        "v17", "v18", "v19", "v20", "v21", "v22", "v23", "v24", "v25", "v26", "v27", "v28", "v29", "v30", "v31", "v32",
        "v33", "v34", "v35", "v36", "v37", "v38", "v39", "v40", "v41", "v42", "v43", "v44", "v45", "v46", "v47", "v48",
        "v49", "v50", "v51", "v52", "v53", "v54", "v55", "v56", "v57", "v58", "v59", "v60", "v61", "v62", "v63", "v64",
        "v65", "v66", "v67", "v68", "v69", "v70", "v71", "v72", "v73", "v74", "v75", "v76", "v77", "v78", "v79", "v80",
        "v81", "v82", "v83", "v84", "v85", "v86", "v87", "v88", "v89", "v90", "v91", "v92", "v93", "v94", "v95", "v96",
        "v97", "v98", "v99", "v100", "v101", "v102", "v103", "v104", "v105", "v106", "v107", "v108", "v109", "v110",
        "v111", "v112", "v113", "v114", "v115", "v116", "v117", "v118", "v119", "v120", "v121", "v122", "v123", "v124",
        "v125", "v126", "v127", "v128", "v129", "v130", "v131", "v132", "v133", "v134", "v135", "v136", "v137", "v138",
        "v139", "v140", "v141", "v142", "v143", "v144", "v145", "v146", "v147", "v148", "v149", "v150", "v151", "v152",
        "v153", "v154", "v155", "v156", "v157", "v158", "v159", "v160", "v161", "v162", "v163", "v164", "v165", "v166",
        "v167", "v168", "v169", "v170", "v171", "v172", "v173", "v174", "v175", "v176", "v177", "v178", "v179", "v180",
        "v181", "v182", "v183", "v184", "v185", "v186", "v187", "v188", "v189", "v190", "v191", "v192", "v193", "v194",
        "v195", "v196", "v197", "v198", "v199", "v200", "v201", "v202", "v203", "v204", "v205", "v206", "v207", "v208",
        "v209", "v210", "v211", "v212", "v213", "v214", "v215", "v216", "v217", "v218", "v219", "v220", "v221", "v222",
        "v223", "v240", "v241", "v242", "v243", "v244", "v245", "v246", "v247", "v248", "v249", "v250", "v251", "v252",
        "v253", "s36", "s37", "s38", "s39", "s40", "s41", "s42", "s43", "s44", "s45", "s46", "s47", "s48", "s49", "s50",
        "s51", "s52", "s53", "s54", "s55"
    );
    } else {
      LANE_BITS(threadIdx.x & 63);
      u32x8 v;
      u32x16 s;
      v[0] = ((8 * rt + 4 * half + g) * 16 + c) * 16;
      v[1] = (max(8 * rt + ((g & 1) ? 0 : 4 * half) - 1, 0) * 16 + c) * 16;
      v[2] = sta + 128 + lane * 4;
      v[3] = sta + ((g & 1) ? 0 : 64) + c * 4;
      v[4] = lr * A_SLOT + A_HALO + lc * 16;
      const int n3 = 14 * xcd + idx - (idx >= 14 && (xcd & 1) ? 16 : 0);
      v[5] = lr * 3584 + ((lc ^ swz_tr(lr)) << 4) + (n3 >> 2) * 128 + ((wave - 4) * 256 + ((2 * n3 + 2 * (n3 >> 2)) & 7) * 32) * 3584;
      v[6] = ((idx * 4 + wave - 4) * 16 + (lane >> 2)) * 3584 + (p4 + ((7 * xcd) >> 1)) * 128;
      u64 tw = (u64)w1;
      s[4] = A_W0 + (wave - 4) * 4096;
      put(s, 0, (u64)(x + (rt * 128 * 896 + 128) - (16 * 896 + 1024)));
      put(s, 2, (u64)(wqkv + (hp * 112 + 64 * 2688) - 4096));
      s[5] = wave * A_UNIT;
      put2(s, 6, wave > 4 ? 0xffffffffu : 0xffffff00u, 0u);
      s[8] = A_HALO + (wave - 4) * 512;
      s[9] = A_FL + 32 + 4 * half;
      put(s, 10, (u64)(ws + WS_ST));
    asm volatile(
      "s_waitcnt lgkmcnt(0)\n\ts_nop 4\n\t"
      "s_load_dwordx2 s[18:19], s[14:15], 0x20000\n\t"
      "s_sub_u32 s40, s4, 57344\n\t"
      "s_subb_u32 s41, s5, 0\n\t"
      "s_sub_u32 s42, s6, 86016\n\t"
      "s_subb_u32 s43, s7, 0\n\t"
      "s_setprio 2\n\t"
      "v_mov_b32_e32 v0, 0x3f803f80\n\t"
      "v_mov_b32_e32 v1, 0x3f803f80\n\t"
      "v_mov_b32_e32 v2, 0x3f803f80\n\t"
      "v_mov_b32_e32 v3, 0x3f803f80\n\t"
      "s_mov_b64 exec, 0xffffff\n\t"
      "ds_write_b128 v204, v[0:3]\n\t"
      "s_mov_b64 exec, -1\n\t"
      "v_mov_b32_e32 v32, 154624\n\t"
      "v_mov_b32_e32 v33, s13\n\t"
      "v_mov_b32_e32 v36, 0\n\t"
      "v_mov_b32_e32 v37, 0\n\t"
      "v_mov_b32_e32 v38, 0\n\t"
      "v_mov_b32_e32 v39, 0\n\t"
      "ds_write_b128 v32, v[36:39]\n\t"
      "ds_write_b128 v32, v[36:39] offset:16\n\t"
      "ds_write_b128 v32, v[36:39] offset:32\n\t"
      "s_add_u32 m0, s9, 43008\n\t"
      "s_nop 0\n\t"
      "global_load_lds_dwordx4 v195, s[6:7] offset:2048\n\t"
      "global_load_lds_dwordx4 v192, s[4:5]\n\t"
      "global_load_lds_dwordx4 v193, s[4:5] offset:1024\n\t"
      "global_load_lds_dwordx4 v196, s[6:7] offset:3072\n\t"
      "s_add_u32 m0, s9, 47104\n\t"
      "s_nop 0\n\t"
      "global_load_lds_dwordx4 v197, s[6:7]\n\t"
      "s_add_u32 m0, s12, 43008\n\t"
      "s_mov_b64 exec, s[10:11]\n\t"
      "global_load_lds_dwordx4 v194, s[4:5]\n\t"
      "s_mov_b64 exec, -1\n\t"
      "v_xor_b32_e32 v21, 64, v205\n\t"
      "v_add_u32_e32 v22, 55296, v205\n\t"
      "v_add_u32_e32 v23, 82944, v21\n\t"
      "v_add_u32_e32 v21, 27648, v21\n\t"
      "s_waitcnt lgkmcnt(0)\n\t"
      "s_add_u32 s18, s18, 1\n\t"
      "s_addc_u32 s19, s19, 0\n\t"
      "s_mul_i32 s48, s18, 0x9e3779b9\n\t"
      "s_mul_hi_u32 s17, s18, 0x7f4a7c15\n\t"
      "s_add_u32 s17, s17, s48\n\t"
      "s_mul_i32 s48, s19, 0x7f4a7c15\n\t"
      "s_add_u32 s17, s17, s48\n\t"
      "s_mul_i32 s16, s18, 0x7f4a7c15\n\t"
      "s_add_u32 s16, s16, 0xd192ed03\n\t"
      "s_addc_u32 s17, s17, 0xd1b54a32\n\t"
      "v_mov_b32_e32 v12, s16\n\t"
      "v_mov_b32_e32 v13, s17\n\t"
      "v_mov_b32_e32 v14, s18\n\t"
      "v_mov_b32_e32 v15, s19\n\t"
      "v_mov_b32_e32 v16, 163312\n\t"
      "s_mov_b64 exec, 1\n\t"
      "ds_write_b128 v16, v[12:15]\n\t"
      "s_mov_b64 exec, -1\n\t"
      "s_waitcnt vmcnt(11) lgkmcnt(0)\n\t"
      "s_barrier\n\t"
      "s_add_u32 s4, s4, 128\n\t"
      "s_addc_u32 s5, s5, 0\n\t"
      "s_add_u32 s6, s6, 172032\n\t"
      "s_addc_u32 s7, s7, 0\n\t"
      "s_add_u32 s40, s40, 128\n\t"
      "s_addc_u32 s41, s41, 0\n\t"
      "s_add_u32 s42, s42, 172032\n\t"
      "s_addc_u32 s43, s43, 0\n\t"
      "s_add_u32 m0, s9, 65536\n\t"
      "s_nop 0\n\t"
      "global_load_lds_dwordx4 v195, s[42:43] offset:2048\n\t"
      "global_load_lds_dwordx4 v192, s[40:41]\n\t"
      "global_load_lds_dwordx4 v193, s[40:41] offset:1024\n\t"
      "global_load_lds_dwordx4 v196, s[42:43] offset:3072\n\t"
      "s_add_u32 m0, s9, 69632\n\t"
      "s_nop 0\n\t"
      "global_load_lds_dwordx4 v197, s[42:43]\n\t"
      "s_add_u32 m0, s9, 86016\n\t"
      "s_nop 0\n\t"
      "global_load_lds_dwordx4 v195, s[6:7] offset:2048\n\t"
      "global_load_lds_dwordx4 v192, s[4:5]\n\t"
      "global_load_lds_dwordx4 v193, s[4:5] offset:1024\n\t"
      "global_load_lds_dwordx4 v196, s[6:7] offset:3072\n\t"
      "s_add_u32 m0, s9, 90112\n\t"
      "s_nop 0\n\t"
      "global_load_lds_dwordx4 v197, s[6:7]\n\t"
      "s_add_u32 m0, s12, 86016\n\t"
      "s_mov_b64 exec, s[10:11]\n\t"
      "global_load_lds_dwordx4 v194, s[4:5]\n\t"
      "s_mov_b64 exec, -1\n\t"
      "s_waitcnt vmcnt(11)\n\t"
      "s_barrier\n\t"
      "s_add_u32 s4, s4, 128\n\t"
      "s_addc_u32 s5, s5, 0\n\t"
      "s_add_u32 s6, s6, 172032\n\t"
      "s_addc_u32 s7, s7, 0\n\t"
      "s_add_u32 s40, s40, 128\n\t"
      "s_addc_u32 s41, s41, 0\n\t"
      "s_add_u32 s42, s42, 172032\n\t"
      "s_addc_u32 s43, s43, 0\n\t"
      "s_add_u32 m0, s9, 4294946816\n\t"
      "s_nop 0\n\t"
      "global_load_lds_dwordx4 v195, s[42:43] offset:2048\n\t"
      "global_load_lds_dwordx4 v192, s[40:41]\n\t"
      "global_load_lds_dwordx4 v193, s[40:41] offset:1024\n\t"
      "global_load_lds_dwordx4 v196, s[42:43] offset:3072\n\t"
      "s_add_u32 m0, s9, 4294950912\n\t"
      "s_nop 0\n\t"
      "global_load_lds_dwordx4 v197, s[42:43]\n\t"
      "s_add_u32 m0, s9, 0\n\t"
      "s_nop 0\n\t"
      "global_load_lds_dwordx4 v195, s[6:7] offset:2048\n\t"
      "global_load_lds_dwordx4 v192, s[4:5]\n\t"
      "global_load_lds_dwordx4 v193, s[4:5] offset:1024\n\t"
      "global_load_lds_dwordx4 v196, s[6:7] offset:3072\n\t"
      "s_add_u32 m0, s9, 4096\n\t"
      "s_nop 0\n\t"
      "global_load_lds_dwordx4 v197, s[6:7]\n\t"
      "s_add_u32 m0, s12, 0\n\t"
      "s_mov_b64 exec, s[10:11]\n\t"
      "global_load_lds_dwordx4 v194, s[4:5]\n\t"
      "s_mov_b64 exec, -1\n\t"
      "s_waitcnt vmcnt(11)\n\t"
      "s_barrier\n\t"
      "s_add_u32 s4, s4, 128\n\t"
      "s_addc_u32 s5, s5, 0\n\t"
      "s_add_u32 s6, s6, 172032\n\t"
      "s_addc_u32 s7, s7, 0\n\t"
      "s_add_u32 s40, s40, 128\n\t"
      "s_addc_u32 s41, s41, 0\n\t"
      "s_add_u32 s42, s42, 172032\n\t"
      "s_addc_u32 s43, s43, 0\n\t"
      "s_add_u32 m0, s9, 22528\n\t"
      "s_nop 0\n\t"
      "global_load_lds_dwordx4 v195, s[42:43] offset:2048\n\t"
      "global_load_lds_dwordx4 v192, s[40:41]\n\t"
      "global_load_lds_dwordx4 v193, s[40:41] offset:1024\n\t"
      "global_load_lds_dwordx4 v196, s[42:43] offset:3072\n\t"
      "s_add_u32 m0, s9, 26624\n\t"
      "s_nop 0\n\t"
      "global_load_lds_dwordx4 v197, s[42:43]\n\t"
      "s_add_u32 m0, s9, 43008\n\t"
      "s_nop 0\n\t"
      "global_load_lds_dwordx4 v195, s[6:7] offset:2048\n\t"
      "global_load_lds_dwordx4 v192, s[4:5]\n\t"
      "global_load_lds_dwordx4 v193, s[4:5] offset:1024\n\t"
      "global_load_lds_dwordx4 v196, s[6:7] offset:3072\n\t"
      "s_add_u32 m0, s9, 47104\n\t"
      "s_nop 0\n\t"
      "global_load_lds_dwordx4 v197, s[6:7]\n\t"
      "s_add_u32 m0, s12, 43008\n\t"
      "s_mov_b64 exec, s[10:11]\n\t"
      "global_load_lds_dwordx4 v194, s[4:5]\n\t"
      "s_mov_b64 exec, -1\n\t"
      "s_waitcnt vmcnt(11)\n\t"
      "s_barrier\n\t"
      "s_add_u32 s4, s4, 128\n\t"
      "s_addc_u32 s5, s5, 0\n\t"
      "s_add_u32 s6, s6, 172032\n\t"
      "s_addc_u32 s7, s7, 0\n\t"
      "s_add_u32 s40, s40, 128\n\t"
      "s_addc_u32 s41, s41, 0\n\t"
      "s_add_u32 s42, s42, 172032\n\t"
      "s_addc_u32 s43, s43, 0\n\t"
      "s_add_u32 m0, s9, 65536\n\t"
      "s_nop 0\n\t"
      "global_load_lds_dwordx4 v195, s[42:43] offset:2048\n\t"
      "global_load_lds_dwordx4 v192, s[40:41]\n\t"
      "global_load_lds_dwordx4 v193, s[40:41] offset:1024\n\t"
      "global_load_lds_dwordx4 v196, s[42:43] offset:3072\n\t"
      "s_add_u32 m0, s9, 69632\n\t"
      "s_nop 0\n\t"
      "global_load_lds_dwordx4 v197, s[42:43]\n\t"
      "s_add_u32 m0, s9, 86016\n\t"
      "s_nop 0\n\t"
      "global_load_lds_dwordx4 v195, s[6:7] offset:2048\n\t"
      "global_load_lds_dwordx4 v192, s[4:5]\n\t"
      "global_load_lds_dwordx4 v193, s[4:5] offset:1024\n\t"
      "global_load_lds_dwordx4 v196, s[6:7] offset:3072\n\t"
      "s_add_u32 m0, s9, 90112\n\t"
      "s_nop 0\n\t"
      "global_load_lds_dwordx4 v197, s[6:7]\n\t"
      "s_add_u32 m0, s12, 86016\n\t"
      "s_mov_b64 exec, s[10:11]\n\t"
      "global_load_lds_dwordx4 v194, s[4:5]\n\t"
      "s_mov_b64 exec, -1\n\t"
      "s_waitcnt vmcnt(11)\n\t"
      "s_barrier\n\t"
      "s_add_u32 s4, s4, 128\n\t"
      "s_addc_u32 s5, s5, 0\n\t"
      "s_add_u32 s6, s6, 172032\n\t"
      "s_addc_u32 s7, s7, 0\n\t"
      "s_add_u32 s40, s40, 128\n\t"
      "s_addc_u32 s41, s41, 0\n\t"
      "s_add_u32 s42, s42, 172032\n\t"
      "s_addc_u32 s43, s43, 0\n\t"
      "s_add_u32 m0, s9, 4294946816\n\t"
      "s_nop 0\n\t"
      "global_load_lds_dwordx4 v195, s[42:43] offset:2048\n\t"
      "global_load_lds_dwordx4 v192, s[40:41]\n\t"
      "global_load_lds_dwordx4 v193, s[40:41] offset:1024\n\t"
      "global_load_lds_dwordx4 v196, s[42:43] offset:3072\n\t"
      "s_add_u32 m0, s9, 4294950912\n\t"
      "s_nop 0\n\t"
      "global_load_lds_dwordx4 v197, s[42:43]\n\t"
      "s_add_u32 m0, s9, 0\n\t"
      "s_nop 0\n\t"
      "global_load_lds_dwordx4 v195, s[6:7] offset:2048\n\t"
      "global_load_lds_dwordx4 v192, s[4:5]\n\t"
      "global_load_lds_dwordx4 v193, s[4:5] offset:1024\n\t"
      "global_load_lds_dwordx4 v196, s[6:7] offset:3072\n\t"
      "s_add_u32 m0, s9, 4096\n\t"
      "s_nop 0\n\t"
      "global_load_lds_dwordx4 v197, s[6:7]\n\t"
      "s_add_u32 m0, s12, 0\n\t"
      "s_mov_b64 exec, s[10:11]\n\t"
      "global_load_lds_dwordx4 v194, s[4:5]\n\t"
      "s_mov_b64 exec, -1\n\t"
      "global_load_dwordx4 v[4:7], v200, s[14:15] sc1\n\t"
      "global_load_dwordx4 v[8:11], v201, s[14:15] sc1\n\t"
      "s_waitcnt vmcnt(13)\n\t"
      "s_barrier\n\t"
      "s_mov_b32 m0, 163328\n\t"
      "s_add_u32 s44, s20, 0\n\t"
      "s_addc_u32 s45, s21, 0\n\t"
      "global_load_lds_dword v206, s[44:45]\n\t"
      "s_waitcnt vmcnt(3)\n\t"
      "s_barrier\n\t"
      "s_waitcnt vmcnt(1)\n\t"
      "v_xor_b32_e32 v6, v6, v4\n\t"
      "v_xor_b32_e32 v7, v7, v5\n\t"
      "v_xor_b32_e32 v10, v10, v8\n\t"
      "v_xor_b32_e32 v11, v11, v9\n\t"
      "v_cmp_ne_u64_e64 s[48:49], s[16:17], v[6:7]\n\t"
      "v_cmp_ne_u64_e32 vcc, s[16:17], v[10:11]\n\t"
      "s_nop 0\n\t"
      "s_or_b64 vcc, vcc, s[48:49]\n\t"
      "s_cbranch_scc1 GRR_%=\n\t"
      "GRB_%=:\n\t"
      "v_mul_f32_e32 v6, 0x3e8b97f6, v5\n\t"
      "ds_write2_b32 v202, v4, v5 offset0:0 offset1:96\n\t"
      "ds_write2_b32 v203, v8, v9 offset0:0 offset1:96\n\t"
      "ds_write_b32 v202, v6 offset:640\n\t"
      "s_mov_b64 exec, 1\n\t"
      "ds_write_b32 v33, v33\n\t"
      "s_mov_b64 exec, -1\n\t"
      "s_setprio 0\n\t"
      "s_waitcnt vmcnt(0)\n\t"
      "s_mov_b32 m0, 163328\n\t"
      "s_add_u32 s44, s20, 3670016\n\t"
      "s_addc_u32 s45, s21, 0\n\t"
      "global_load_lds_dword v206, s[44:45]\n\t"
      "s_waitcnt lgkmcnt(0)\n\t"
      "s_branch HEND_%=\n\t"
      "GRR_%=:\n\t"
      "s_mov_b32 s51, 0\n\t"
      "GRL_%=:\n\t"
      "s_add_u32 s51, s51, 1\n\t"
      "s_cmp_lt_u32 s51, 262144\n\t"
      "s_cbranch_scc0 GRX_%=\n\t"
      "s_sleep 2\n\t"
      "global_load_dwordx4 v[4:7], v200, s[14:15] sc1\n\t"
      "global_load_dwordx4 v[8:11], v201, s[14:15] sc1\n\t"
      "s_waitcnt vmcnt(0)\n\t"
      "v_xor_b32_e32 v6, v6, v4\n\t"
      "v_xor_b32_e32 v7, v7, v5\n\t"
      "v_xor_b32_e32 v10, v10, v8\n\t"
      "v_xor_b32_e32 v11, v11, v9\n\t"
      "v_cmp_ne_u64_e64 s[48:49], s[16:17], v[6:7]\n\t"
      "v_cmp_ne_u64_e32 vcc, s[16:17], v[10:11]\n\t"
      "s_nop 0\n\t"
      "s_or_b64 vcc, vcc, s[48:49]\n\t"
      "s_cbranch_scc1 GRL_%=\n\t"
      "GRX_%=:\n\t"
      "s_branch GRB_%=\n\t"
      "HEND_%=:\n\t"
      : "+{s[4:19]}"(s), "+{s[20:21]}"(tw), "+{v[192:199]}"(f), "+{v[200:207]}"(v)
      :
      : "memory", "vcc", "scc",
        "v0", "v1", "v2", "v3", "v4", "v5", "v6", "v7", "v8", "v9", "v10", "v11", "v12", "v13", "v14", "v15", "v16",
        "v17", "v18", "v19", "v20", "v21", "v22", "v23", "v24", "v25", "v26", "v27", "v28", "v29", "v30", "v31", "v32",
        "v33", "v34", "v35", "v36", "v37", "v38", "v39", "s40", "s41", "s42", "s43", "s44", "s45", "s46", "s47", "s48",
        "s49", "s50", "s51"
    );
    }
    PIN();
    LANE_BITS(__builtin_amdgcn_mbcnt_hi(~0u, __builtin_amdgcn_mbcnt_lo(~0u, 0u)));
    const bool has3 = idx < 14;
    const int ft = has3 ? (14 * xcd + idx) >> 2 : 0, blk0 = 7 * ((14 * xcd + idx) & 3);
    u32x8 v;
    u32x16 s;
    v[0] = lane * 16;
    const int rot = (M3 ? ((RF3 * (idx >> 1)) >> 1) + 4 * (idx & 1) : 2 * (14 * xcd + idx) + (SWP ? 2 * (ft & 2) : RF3 * ft)) & 7, ks = SWP ? wave ^ (4 * (ft & 1)) : wave;
    u64 kr = (u64)(7 - rot) | ((u64)rot << 32);
    v[1] = has3 ? lr * 3584 + ((lc ^ swz_tr(lr)) << 4) : M4 ? (lr + 8 * (xcd & 1)) * 4096 + lc * 128 + (xcd >> 1) * 1024 : (lane >> 2) * 4096 + p4 * 128 + xcd * 512;
    v[2] = (PRE ? A_W0 + wave * 4096 : wave * G_WV) + g * 1024 + q4 * 128 + (((2 * (wave & 3) + (p4 >> 1)) ^ swz_tr(8 * g + q4)) << 4) + (p4 & 1) * 8;
    v[3] = wave * 4096 + (wave & 3) * 1024 + ((lane * 16) ^ ((g & 1) << 7));
    const int gg = 2 * (lane >> 5) + (lane & 1);
    v[4] = (wave & 3) * 1024 + (((gg * 16 + ((lane >> 1) & 15)) * 16) ^ ((gg & 1) << 7));
    v[5] = (wave & 3) * 512 + lane * 8;
    v[6] = g * 8;
    v[7] = ST_H3 + (ks * 16 + c) * 128 + xcd * 16384;
    put(s, 0, has3 ? (u64)(w1 + ft * 128 + (ks * 256 + rot * 32) * 3584) : (u64)(w2 + (wave * 224 + (idx & 1) * 16) * 4096));
    put(s, 2, (u64)(ws + WS_HN + (blk0 * 64 + 8 * ks) * 1024));
    put(s, 4, (u64)(ws + WS_G + (blk0 * 56 + 2 * ft) * 1024));
    put(s, 6, (u64)(b1 + (64 * ft + 16 * (wave & 3)) * 2));
    put(s, 8, (u64)(ws + WS_ST));
    u64 hint = (u64)(ST_H4 + (blk0 * 4 + ft) * 128) | ((u64)(ST_FL + (14 * xcd + idx) * 256) << 32);
    s[12] = UNI((has3 ? 4 : 0) | (ks == ((xcd + XO) & 7) ? 16 : 0));
    s[13] = wave * G_WV;
    asm volatile(
      "s_waitcnt lgkmcnt(0)\n\ts_nop 4\n\t"
      "s_bitcmp0_b32 s16, 2\n\t"
      "s_cbranch_scc1 ASP_%=\n\t"
      "v_mov_b32_e32 v216, 163312\n\t"
      "ds_read_b128 v[216:219], v216\n\t"
      "v_xor_b32_e32 v248, 64, v241\n\t"
      "v_add_u32_e32 v249, 55296, v241\n\t"
      "v_add_u32_e32 v250, 82944, v248\n\t"
      "v_add_u32_e32 v248, 27648, v248\n\t"
      "v_xor_b32_e32 v251, 32, v242\n\t"
      "v_xor_b32_e32 v252, 64, v242\n\t"
      "v_xor_b32_e32 v253, 96, v242\n\t"
      "s_cmp_ge_u32 s17, 65536\n\t"
      "s_cbranch_scc0 BOH_%=\n\t"
      "s_barrier\n\t"
      "BOH_%=:\n\t"
      "s_add_u32 m0, s17, 0\n\t"
      "s_nop 0\n\t"
      "global_load_lds_dwordx4 v241, s[4:5]\n\t"
      "global_load_lds_dwordx4 v248, s[4:5] offset:1024\n\t"
      "global_load_lds_dwordx4 v249, s[4:5] offset:2048\n\t"
      "global_load_lds_dwordx4 v250, s[4:5] offset:3072\n\t"
      "s_sub_u32 s20, s20, 1\n\t"
      "s_cselect_b32 s48, -7, 1\n\t"
      "s_cselect_b32 s49, -1, 0\n\t"
      "s_mul_i32 s48, s48, 114688\n\t"
      "s_add_u32 s4, s4, s48\n\t"
      "s_addc_u32 s5, s5, s49\n\t"
      "global_load_dwordx2 v[238:239], v246, s[10:11]\n\t"
      "s_waitcnt lgkmcnt(0)\n\t"
      "v_readfirstlane_b32 s14, v216\n\t"
      "v_readfirstlane_b32 s15, v217\n\t"
      "v_readfirstlane_b32 s46, v218\n\t"
      "v_readfirstlane_b32 s47, v219\n\t"
      "s_add_u32 s52, s6, 0\n\t"
      "s_addc_u32 s53, s7, 0\n\t"
      "s_add_u32 s54, s6, 65536\n\t"
      "s_addc_u32 s55, s7, 0\n\t"
      "s_add_u32 s56, s6, 131072\n\t"
      "s_addc_u32 s57, s7, 0\n\t"
      "s_add_u32 s58, s6, 196608\n\t"
      "s_addc_u32 s59, s7, 0\n\t"
      "s_add_u32 s60, s6, 262144\n\t"
      "s_addc_u32 s61, s7, 0\n\t"
      "s_add_u32 s62, s6, 327680\n\t"
      "s_addc_u32 s63, s7, 0\n\t"
      "s_add_u32 s64, s6, 393216\n\t"
      "s_addc_u32 s65, s7, 0\n\t"
      "s_add_u32 s48, s21, 0\n\t"
      "s_cmp_ge_u32 s48, 8\n\t"
      "s_cselect_b32 s49, 8, 0\n\t"
      "s_sub_u32 s48, s48, s49\n\t"
      "v_lshl_add_u32 v176, s48, 10, v240\n\t"
      "s_add_u32 s48, s21, 1\n\t"
      "s_cmp_ge_u32 s48, 8\n\t"
      "s_cselect_b32 s49, 8, 0\n\t"
      "s_sub_u32 s48, s48, s49\n\t"
      "v_lshl_add_u32 v177, s48, 10, v240\n\t"
      "s_add_u32 s48, s21, 2\n\t"
      "s_cmp_ge_u32 s48, 8\n\t"
      "s_cselect_b32 s49, 8, 0\n\t"
      "s_sub_u32 s48, s48, s49\n\t"
      "v_lshl_add_u32 v178, s48, 10, v240\n\t"
      "s_add_u32 s48, s21, 3\n\t"
      "s_cmp_ge_u32 s48, 8\n\t"
      "s_cselect_b32 s49, 8, 0\n\t"
      "s_sub_u32 s48, s48, s49\n\t"
      "v_lshl_add_u32 v179, s48, 10, v240\n\t"
      "s_add_u32 s48, s21, 4\n\t"
      "s_cmp_ge_u32 s48, 8\n\t"
      "s_cselect_b32 s49, 8, 0\n\t"
      "s_sub_u32 s48, s48, s49\n\t"
      "v_lshl_add_u32 v180, s48, 10, v240\n\t"
      "s_add_u32 s48, s21, 5\n\t"
      "s_cmp_ge_u32 s48, 8\n\t"
      "s_cselect_b32 s49, 8, 0\n\t"
      "s_sub_u32 s48, s48, s49\n\t"
      "v_lshl_add_u32 v181, s48, 10, v240\n\t"
      "s_add_u32 s48, s21, 6\n\t"
      "s_cmp_ge_u32 s48, 8\n\t"
      "s_cselect_b32 s49, 8, 0\n\t"
      "s_sub_u32 s48, s48, s49\n\t"
      "v_lshl_add_u32 v182, s48, 10, v240\n\t"
      "s_add_u32 s48, s21, 7\n\t"
      "s_cmp_ge_u32 s48, 8\n\t"
      "s_cselect_b32 s49, 8, 0\n\t"
      "s_sub_u32 s48, s48, s49\n\t"
      "v_lshl_add_u32 v183, s48, 10, v240\n\t"
      "s_add_u32 m0, s17, 4096\n\t"
      "s_nop 0\n\t"
      "global_load_lds_dwordx4 v241, s[4:5]\n\t"
      "global_load_lds_dwordx4 v248, s[4:5] offset:1024\n\t"
      "global_load_lds_dwordx4 v249, s[4:5] offset:2048\n\t"
      "global_load_lds_dwordx4 v250, s[4:5] offset:3072\n\t"
      "s_sub_u32 s20, s20, 1\n\t"
      "s_cselect_b32 s48, -7, 1\n\t"
      "s_cselect_b32 s49, -1, 0\n\t"
      "s_mul_i32 s48, s48, 114688\n\t"
      "s_add_u32 s4, s4, s48\n\t"
      "s_addc_u32 s5, s5, s49\n\t"
      "s_waitcnt vmcnt(5)\n\t"
      "s_add_u32 m0, s17, 8192\n\t"
      "s_nop 0\n\t"
      "global_load_lds_dwordx4 v241, s[4:5]\n\t"
      "global_load_lds_dwordx4 v248, s[4:5] offset:1024\n\t"
      "global_load_lds_dwordx4 v249, s[4:5] offset:2048\n\t"
      "global_load_lds_dwordx4 v250, s[4:5] offset:3072\n\t"
      "s_sub_u32 s20, s20, 1\n\t"
      "s_cselect_b32 s48, -7, 1\n\t"
      "s_cselect_b32 s49, -1, 0\n\t"
      "s_mul_i32 s48, s48, 114688\n\t"
      "s_add_u32 s4, s4, s48\n\t"
      "s_addc_u32 s5, s5, s49\n\t"
      "ds_read_b64_tr_b16 v[0:1], v242\n\t"
      "ds_read_b64_tr_b16 v[2:3], v242 offset:512\n\t"
      "ds_read_b64_tr_b16 v[4:5], v251\n\t"
      "ds_read_b64_tr_b16 v[6:7], v251 offset:512\n\t"
      "ds_read_b64_tr_b16 v[8:9], v252\n\t"
      "ds_read_b64_tr_b16 v[10:11], v252 offset:512\n\t"
      "ds_read_b64_tr_b16 v[12:13], v253\n\t"
      "ds_read_b64_tr_b16 v[14:15], v253 offset:512\n\t"
      "s_waitcnt vmcnt(4)\n\t"
      "s_add_u32 m0, s17, 12288\n\t"
      "s_nop 0\n\t"
      "global_load_lds_dwordx4 v241, s[4:5]\n\t"
      "global_load_lds_dwordx4 v248, s[4:5] offset:1024\n\t"
      "global_load_lds_dwordx4 v249, s[4:5] offset:2048\n\t"
      "global_load_lds_dwordx4 v250, s[4:5] offset:3072\n\t"
      "s_sub_u32 s20, s20, 1\n\t"
      "s_cselect_b32 s48, -7, 1\n\t"
      "s_cselect_b32 s49, -1, 0\n\t"
      "s_mul_i32 s48, s48, 114688\n\t"
      "s_add_u32 s4, s4, s48\n\t"
      "s_addc_u32 s5, s5, s49\n\t"
      "ds_read_b64_tr_b16 v[16:17], v242 offset:4096\n\t"
      "ds_read_b64_tr_b16 v[18:19], v242 offset:4608\n\t"
      "ds_read_b64_tr_b16 v[20:21], v251 offset:4096\n\t"
      "ds_read_b64_tr_b16 v[22:23], v251 offset:4608\n\t"
      "ds_read_b64_tr_b16 v[24:25], v252 offset:4096\n\t"
      "ds_read_b64_tr_b16 v[26:27], v252 offset:4608\n\t"
      "ds_read_b64_tr_b16 v[28:29], v253 offset:4096\n\t"
      "ds_read_b64_tr_b16 v[30:31], v253 offset:4608\n\t"
      "v_xor_b32_e32 v164, 1024, v243\n\t"
      "v_xor_b32_e32 v165, 2048, v243\n\t"
      "v_xor_b32_e32 v166, 3072, v243\n\t"
      "v_lshlrev_b32_e32 v236, 16, v238\n\t"
      "v_and_b32_e32 v237, 0xffff0000, v238\n\t"
      "v_lshlrev_b32_e32 v238, 16, v239\n\t"
      "v_and_b32_e32 v239, 0xffff0000, v239\n\t"
      "v_mov_b32_e32 v235, 0xbdd2d3e8\n\t"
      "s_waitcnt vmcnt(4)\n\t"
      "s_waitcnt lgkmcnt(8)\n\t"
      "s_add_u32 m0, s17, 0\n\t"
      "s_nop 0\n\t"
      "global_load_lds_dwordx4 v241, s[4:5]\n\t"
      "global_load_lds_dwordx4 v248, s[4:5] offset:1024\n\t"
      "global_load_lds_dwordx4 v249, s[4:5] offset:2048\n\t"
      "global_load_lds_dwordx4 v250, s[4:5] offset:3072\n\t"
      "s_sub_u32 s20, s20, 1\n\t"
      "s_cselect_b32 s48, -7, 1\n\t"
      "s_cselect_b32 s49, -1, 0\n\t"
      "s_mul_i32 s48, s48, 114688\n\t"
      "s_add_u32 s4, s4, s48\n\t"
      "s_addc_u32 s5, s5, s49\n\t"
      "ds_read_b64_tr_b16 v[32:33], v242 offset:8192\n\t"
      "ds_read_b64_tr_b16 v[34:35], v242 offset:8704\n\t"
      "ds_read_b64_tr_b16 v[36:37], v251 offset:8192\n\t"
      "ds_read_b64_tr_b16 v[38:39], v251 offset:8704\n\t"
      "ds_read_b64_tr_b16 v[40:41], v252 offset:8192\n\t"
      "ds_read_b64_tr_b16 v[42:43], v252 offset:8704\n\t"
      "ds_read_b64_tr_b16 v[44:45], v253 offset:8192\n\t"
      "ds_read_b64_tr_b16 v[46:47], v253 offset:8704\n\t"
      "s_waitcnt vmcnt(4)\n\t"
      "s_waitcnt lgkmcnt(8)\n\t"
      "s_add_u32 m0, s17, 4096\n\t"
      "s_nop 0\n\t"
      "global_load_lds_dwordx4 v241, s[4:5]\n\t"
      "global_load_lds_dwordx4 v248, s[4:5] offset:1024\n\t"
      "global_load_lds_dwordx4 v249, s[4:5] offset:2048\n\t"
      "global_load_lds_dwordx4 v250, s[4:5] offset:3072\n\t"
      "s_sub_u32 s20, s20, 1\n\t"
      "s_cselect_b32 s48, -7, 1\n\t"
      "s_cselect_b32 s49, -1, 0\n\t"
      "s_mul_i32 s48, s48, 114688\n\t"
      "s_add_u32 s4, s4, s48\n\t"
      "s_addc_u32 s5, s5, s49\n\t"
      "ds_read_b64_tr_b16 v[48:49], v242 offset:12288\n\t"
      "ds_read_b64_tr_b16 v[50:51], v242 offset:12800\n\t"
      "ds_read_b64_tr_b16 v[52:53], v251 offset:12288\n\t"
      "ds_read_b64_tr_b16 v[54:55], v251 offset:12800\n\t"
      "ds_read_b64_tr_b16 v[56:57], v252 offset:12288\n\t"
      "ds_read_b64_tr_b16 v[58:59], v252 offset:12800\n\t"
      "ds_read_b64_tr_b16 v[60:61], v253 offset:12288\n\t"
      "ds_read_b64_tr_b16 v[62:63], v253 offset:12800\n\t"
      "s_waitcnt vmcnt(4)\n\t"
      "s_waitcnt lgkmcnt(8)\n\t"
      "s_add_u32 m0, s17, 8192\n\t"
      "s_nop 0\n\t"
      "global_load_lds_dwordx4 v241, s[4:5]\n\t"
      "global_load_lds_dwordx4 v248, s[4:5] offset:1024\n\t"
      "global_load_lds_dwordx4 v249, s[4:5] offset:2048\n\t"
      "global_load_lds_dwordx4 v250, s[4:5] offset:3072\n\t"
      "s_sub_u32 s20, s20, 1\n\t"
      "s_cselect_b32 s48, -7, 1\n\t"
      "s_cselect_b32 s49, -1, 0\n\t"
      "s_mul_i32 s48, s48, 114688\n\t"
      "s_add_u32 s4, s4, s48\n\t"
      "s_addc_u32 s5, s5, s49\n\t"
      "ds_read_b64_tr_b16 v[64:65], v242\n\t"
      "ds_read_b64_tr_b16 v[66:67], v242 offset:512\n\t"
      "ds_read_b64_tr_b16 v[68:69], v251\n\t"
      "ds_read_b64_tr_b16 v[70:71], v251 offset:512\n\t"
      "ds_read_b64_tr_b16 v[72:73], v252\n\t"
      "ds_read_b64_tr_b16 v[74:75], v252 offset:512\n\t"
      "ds_read_b64_tr_b16 v[76:77], v253\n\t"
      "ds_read_b64_tr_b16 v[78:79], v253 offset:512\n\t"
      "s_waitcnt vmcnt(4)\n\t"
      "s_waitcnt lgkmcnt(8)\n\t"
      "s_add_u32 m0, s17, 12288\n\t"
      "s_nop 0\n\t"
      "global_load_lds_dwordx4 v241, s[4:5]\n\t"
      "global_load_lds_dwordx4 v248, s[4:5] offset:1024\n\t"
      "global_load_lds_dwordx4 v249, s[4:5] offset:2048\n\t"
      "global_load_lds_dwordx4 v250, s[4:5] offset:3072\n\t"
      "ds_read_b64_tr_b16 v[80:81], v242 offset:4096\n\t"
      "ds_read_b64_tr_b16 v[82:83], v242 offset:4608\n\t"
      "ds_read_b64_tr_b16 v[84:85], v251 offset:4096\n\t"
      "ds_read_b64_tr_b16 v[86:87], v251 offset:4608\n\t"
      "ds_read_b64_tr_b16 v[88:89], v252 offset:4096\n\t"
      "ds_read_b64_tr_b16 v[90:91], v252 offset:4608\n\t"
      "ds_read_b64_tr_b16 v[92:93], v253 offset:4096\n\t"
      "ds_read_b64_tr_b16 v[94:95], v253 offset:4608\n\t"
      "s_waitcnt vmcnt(4)\n\t"
      "ds_read_b64_tr_b16 v[96:97], v242 offset:8192\n\t"
      "ds_read_b64_tr_b16 v[98:99], v242 offset:8704\n\t"
      "ds_read_b64_tr_b16 v[100:101], v251 offset:8192\n\t"
      "ds_read_b64_tr_b16 v[102:103], v251 offset:8704\n\t"
      "ds_read_b64_tr_b16 v[104:105], v252 offset:8192\n\t"
      "ds_read_b64_tr_b16 v[106:107], v252 offset:8704\n\t"
      "ds_read_b64_tr_b16 v[108:109], v253 offset:8192\n\t"
      "ds_read_b64_tr_b16 v[110:111], v253 offset:8704\n\t"
      "s_waitcnt vmcnt(0)\n\t"
      "ds_read_b64_tr_b16 v[112:113], v242 offset:12288\n\t"
      "ds_read_b64_tr_b16 v[114:115], v242 offset:12800\n\t"
      "ds_read_b64_tr_b16 v[116:117], v251 offset:12288\n\t"
      "ds_read_b64_tr_b16 v[118:119], v251 offset:12800\n\t"
      "ds_read_b64_tr_b16 v[120:121], v252 offset:12288\n\t"
      "ds_read_b64_tr_b16 v[122:123], v252 offset:12800\n\t"
      "ds_read_b64_tr_b16 v[124:125], v253 offset:12288\n\t"
      "ds_read_b64_tr_b16 v[126:127], v253 offset:12800\n\t"
      "s_waitcnt lgkmcnt(0)\n\t"
      "s_barrier\n\t"
      "s_mov_b32 s51, 0\n\t"
      "s_branch PRE_%=\n\t"
      "PRL_%=:\n\t"
      "s_sleep 4\n\t"
      "s_add_u32 s51, s51, 1\n\t"
      "s_cmp_lt_u32 s51, 262144\n\t"
      "s_cbranch_scc0 PRX_%=\n\t"
      "PRE_%=:\n\t"
      "s_mov_b64 exec, 65535\n\t"
      "s_bitcmp1_b32 s16, 4\n\t"
      "s_cbranch_scc1 PM_%=\n\t"
      "global_load_dwordx2 v[232:233], v247, s[12:13] sc1\n\t"
      "s_waitcnt vmcnt(0)\n\t"
      "PM_%=:\n\t"
      "s_cbranch_scc0 PN_%=\n\t"
      "v_mov_b64_e32 v[232:233], 0\n\t"
      "global_atomic_or_x2 v[232:233], v247, v[232:233], s[12:13] sc0 sc1\n\t"
      "s_waitcnt vmcnt(0)\n\t"
      "PN_%=:\n\t"
      "v_subrev_u32_e32 v232, s14, v232\n\t"
      "v_cmp_ne_u32_e64 s[48:49], s15, v233\n\t"
      "v_subrev_u32_e32 v232, 0, v232\n\t"
      "v_cmp_lt_u32_e32 vcc, 7, v232\n\t"
      "s_mov_b64 exec, -1\n\t"
      "s_or_b64 vcc, vcc, s[48:49]\n\t"
      "s_cbranch_vccnz PRL_%=\n\t"
      "PRX_%=:\n\t"
      "PRY_%=:\n\t"
      "global_load_dwordx4 v[128:131], v176, s[52:53] sc1\n\t"
      "global_load_dwordx4 v[132:135], v177, s[52:53] sc1\n\t"
      "global_load_dwordx4 v[136:139], v178, s[52:53] sc1\n\t"
      "global_load_dwordx4 v[140:143], v179, s[52:53] sc1\n\t"
      "global_load_dwordx4 v[144:147], v180, s[52:53] sc1\n\t"
      "global_load_dwordx4 v[148:151], v181, s[52:53] sc1\n\t"
      "global_load_dwordx4 v[152:155], v182, s[52:53] sc1\n\t"
      "global_load_dwordx4 v[156:159], v183, s[52:53] sc1\n\t"
      "s_cmp_ge_u32 s17, 65536\n\t"
      "s_cbranch_scc1 GQ_%=\n\t"
      ".p2align 6\n\t"
      "s_setprio 1\n\t"
      "v_mov_b32_e32 v232, s14\n\t"
      "v_mov_b32_e32 v233, s15\n\t"
      "v_lshl_add_u32 v247, v240, 10, s22\n\t"
      "s_waitcnt vmcnt(7)\n\t"
      "v_max3_u32 v234, v128, v129, v130\n\t"
      "v_max_u32_e32 v234, v234, v131\n\t"
      "v_cmp_eq_u32_e32 vcc, -1, v234\n\t"
      "s_cbranch_vccnz FP0R_%=\n\t"
      "FP0B_%=:\n\t"
      "v_mfma_f32_16x16x32_bf16 v[184:187], v[0:3], v[128:131], 0\n\t"
      "v_mfma_f32_16x16x32_bf16 v[188:191], v[4:7], v[128:131], 0\n\t"
      "v_mfma_f32_16x16x32_bf16 v[192:195], v[8:11], v[128:131], 0\n\t"
      "v_mfma_f32_16x16x32_bf16 v[196:199], v[12:15], v[128:131], 0\n\t"
      "global_load_dwordx4 v[128:131], v176, s[54:55] sc1\n\t"
      "s_waitcnt vmcnt(7)\n\t"
      "v_max3_u32 v234, v132, v133, v134\n\t"
      "v_max_u32_e32 v234, v234, v135\n\t"
      "v_cmp_eq_u32_e32 vcc, -1, v234\n\t"
      "s_cbranch_vccnz FP1R_%=\n\t"
      "FP1B_%=:\n\t"
      "v_mfma_f32_16x16x32_bf16 v[184:187], v[16:19], v[132:135], v[184:187]\n\t"
      "v_mfma_f32_16x16x32_bf16 v[188:191], v[20:23], v[132:135], v[188:191]\n\t"
      "v_mfma_f32_16x16x32_bf16 v[192:195], v[24:27], v[132:135], v[192:195]\n\t"
      "v_mfma_f32_16x16x32_bf16 v[196:199], v[28:31], v[132:135], v[196:199]\n\t"
      "global_load_dwordx4 v[132:135], v177, s[54:55] sc1\n\t"
      "s_waitcnt vmcnt(7)\n\t"
      "v_max3_u32 v234, v136, v137, v138\n\t"
      "v_max_u32_e32 v234, v234, v139\n\t"
      "v_cmp_eq_u32_e32 vcc, -1, v234\n\t"
      "s_cbranch_vccnz FP2R_%=\n\t"
      "FP2B_%=:\n\t"
      "v_mfma_f32_16x16x32_bf16 v[184:187], v[32:35], v[136:139], v[184:187]\n\t"
      "v_mfma_f32_16x16x32_bf16 v[188:191], v[36:39], v[136:139], v[188:191]\n\t"
      "v_mfma_f32_16x16x32_bf16 v[192:195], v[40:43], v[136:139], v[192:195]\n\t"
      "v_mfma_f32_16x16x32_bf16 v[196:199], v[44:47], v[136:139], v[196:199]\n\t"
      "global_load_dwordx4 v[136:139], v178, s[54:55] sc1\n\t"
      "s_waitcnt vmcnt(7)\n\t"
      "v_max3_u32 v234, v140, v141, v142\n\t"
      "v_max_u32_e32 v234, v234, v143\n\t"
      "v_cmp_eq_u32_e32 vcc, -1, v234\n\t"
      "s_cbranch_vccnz FP3R_%=\n\t"
      "FP3B_%=:\n\t"
      "v_mfma_f32_16x16x32_bf16 v[184:187], v[48:51], v[140:143], v[184:187]\n\t"
      "v_mfma_f32_16x16x32_bf16 v[188:191], v[52:55], v[140:143], v[188:191]\n\t"
      "v_mfma_f32_16x16x32_bf16 v[192:195], v[56:59], v[140:143], v[192:195]\n\t"
      "v_mfma_f32_16x16x32_bf16 v[196:199], v[60:63], v[140:143], v[196:199]\n\t"
      "global_load_dwordx4 v[140:143], v179, s[54:55] sc1\n\t"
      "s_waitcnt vmcnt(7)\n\t"
      "v_max3_u32 v234, v144, v145, v146\n\t"
      "v_max_u32_e32 v234, v234, v147\n\t"
      "v_cmp_eq_u32_e32 vcc, -1, v234\n\t"
      "s_cbranch_vccnz FP4R_%=\n\t"
      "FP4B_%=:\n\t"
      "v_mfma_f32_16x16x32_bf16 v[184:187], v[64:67], v[144:147], v[184:187]\n\t"
      "v_mfma_f32_16x16x32_bf16 v[188:191], v[68:71], v[144:147], v[188:191]\n\t"
      "v_mfma_f32_16x16x32_bf16 v[192:195], v[72:75], v[144:147], v[192:195]\n\t"
      "v_mfma_f32_16x16x32_bf16 v[196:199], v[76:79], v[144:147], v[196:199]\n\t"
      "global_load_dwordx4 v[144:147], v180, s[54:55] sc1\n\t"
      "s_waitcnt vmcnt(7)\n\t"
      "v_max3_u32 v234, v148, v149, v150\n\t"
      "v_max_u32_e32 v234, v234, v151\n\t"
      "v_cmp_eq_u32_e32 vcc, -1, v234\n\t"
      "s_cbranch_vccnz FP5R_%=\n\t"
      "FP5B_%=:\n\t"
      "v_mfma_f32_16x16x32_bf16 v[184:187], v[80:83], v[148:151], v[184:187]\n\t"
      "v_mfma_f32_16x16x32_bf16 v[188:191], v[84:87], v[148:151], v[188:191]\n\t"
      "v_mfma_f32_16x16x32_bf16 v[192:195], v[88:91], v[148:151], v[192:195]\n\t"
      "v_mfma_f32_16x16x32_bf16 v[196:199], v[92:95], v[148:151], v[196:199]\n\t"
      "global_load_dwordx4 v[148:151], v181, s[54:55] sc1\n\t"
      "s_waitcnt vmcnt(7)\n\t"
      "v_max3_u32 v234, v152, v153, v154\n\t"
      "v_max_u32_e32 v234, v234, v155\n\t"
      "v_cmp_eq_u32_e32 vcc, -1, v234\n\t"
      "s_cbranch_vccnz FP6R_%=\n\t"
      "FP6B_%=:\n\t"
      "v_mfma_f32_16x16x32_bf16 v[184:187], v[96:99], v[152:155], v[184:187]\n\t"
      "v_mfma_f32_16x16x32_bf16 v[188:191], v[100:103], v[152:155], v[188:191]\n\t"
      "v_mfma_f32_16x16x32_bf16 v[192:195], v[104:107], v[152:155], v[192:195]\n\t"
      "v_mfma_f32_16x16x32_bf16 v[196:199], v[108:111], v[152:155], v[196:199]\n\t"
      "global_load_dwordx4 v[152:155], v182, s[54:55] sc1\n\t"
      "s_waitcnt vmcnt(7)\n\t"
      "v_max3_u32 v234, v156, v157, v158\n\t"
      "v_max_u32_e32 v234, v234, v159\n\t"
      "v_cmp_eq_u32_e32 vcc, -1, v234\n\t"
      "s_cbranch_vccnz FP7R_%=\n\t"
      "FP7B_%=:\n\t"
      "v_mfma_f32_16x16x32_bf16 v[184:187], v[112:115], v[156:159], v[184:187]\n\t"
      "v_mfma_f32_16x16x32_bf16 v[188:191], v[116:119], v[156:159], v[188:191]\n\t"
      "v_mfma_f32_16x16x32_bf16 v[192:195], v[120:123], v[156:159], v[192:195]\n\t"
      "v_mfma_f32_16x16x32_bf16 v[196:199], v[124:127], v[156:159], v[196:199]\n\t"
      "global_load_dwordx4 v[156:159], v183, s[54:55] sc1\n\t"
      "s_nop 3\n\t"
      "ds_write_b128 v243, v[184:187]\n\t"
      "ds_write_b128 v164, v[188:191]\n\t"
      "ds_write_b128 v165, v[192:195]\n\t"
      "ds_write_b128 v166, v[196:199]\n\t"
      "s_waitcnt lgkmcnt(0)\n\t"
      "s_barrier\n\t"
      "ds_read_b128 v[216:219], v244\n\t"
      "ds_read_b128 v[220:223], v244 offset:4096\n\t"
      "ds_read_b128 v[224:227], v244 offset:8192\n\t"
      "ds_read_b128 v[228:231], v244 offset:12288\n\t"
      "ds_read_b128 v[184:187], v244 offset:16384\n\t"
      "ds_read_b128 v[188:191], v244 offset:20480\n\t"
      "ds_read_b128 v[192:195], v244 offset:24576\n\t"
      "ds_read_b128 v[196:199], v244 offset:28672\n\t"
      "s_waitcnt lgkmcnt(6)\n\t"
      "v_add_f32_e32 v216, v216, v220\n\t"
      "v_add_f32_e32 v217, v217, v221\n\t"
      "v_add_f32_e32 v218, v218, v222\n\t"
      "v_add_f32_e32 v219, v219, v223\n\t"
      "s_waitcnt lgkmcnt(4)\n\t"
      "v_add_f32_e32 v224, v224, v228\n\t"
      "v_add_f32_e32 v225, v225, v229\n\t"
      "v_add_f32_e32 v226, v226, v230\n\t"
      "v_add_f32_e32 v227, v227, v231\n\t"
      "s_waitcnt lgkmcnt(2)\n\t"
      "v_add_f32_e32 v184, v184, v188\n\t"
      "v_add_f32_e32 v185, v185, v189\n\t"
      "v_add_f32_e32 v186, v186, v190\n\t"
      "v_add_f32_e32 v187, v187, v191\n\t"
      "v_add_f32_e32 v216, v216, v224\n\t"
      "v_add_f32_e32 v217, v217, v225\n\t"
      "v_add_f32_e32 v218, v218, v226\n\t"
      "v_add_f32_e32 v219, v219, v227\n\t"
      "s_waitcnt lgkmcnt(0)\n\t"
      "v_add_f32_e32 v192, v192, v196\n\t"
      "s_waitcnt vmcnt(7)\n\t"
      "v_mfma_f32_16x16x32_bf16 v[200:203], v[0:3], v[128:131], 0\n\t"
      "v_add_f32_e32 v193, v193, v197\n\t"
      "v_add_f32_e32 v194, v194, v198\n\t"
      "v_mfma_f32_16x16x32_bf16 v[204:207], v[4:7], v[128:131], 0\n\t"
      "v_add_f32_e32 v195, v195, v199\n\t"
      "v_add_f32_e32 v184, v184, v192\n\t"
      "v_mfma_f32_16x16x32_bf16 v[208:211], v[8:11], v[128:131], 0\n\t"
      "v_add_f32_e32 v185, v185, v193\n\t"
      "v_add_f32_e32 v186, v186, v194\n\t"
      "v_mfma_f32_16x16x32_bf16 v[212:215], v[12:15], v[128:131], 0\n\t"
      "global_load_dwordx4 v[128:131], v176, s[56:57] sc1\n\t"
      "v_add_f32_e32 v187, v187, v195\n\t"
      "s_waitcnt vmcnt(7)\n\t"
      "v_mfma_f32_16x16x32_bf16 v[200:203], v[16:19], v[132:135], v[200:203]\n\t"
      "v_add_f32_e32 v216, v216, v184\n\t"
      "v_add_f32_e32 v217, v217, v185\n\t"
      "v_mfma_f32_16x16x32_bf16 v[204:207], v[20:23], v[132:135], v[204:207]\n\t"
      "v_add_f32_e32 v218, v218, v186\n\t"
      "v_add_f32_e32 v219, v219, v187\n\t"
      "v_mfma_f32_16x16x32_bf16 v[208:211], v[24:27], v[132:135], v[208:211]\n\t"
      "v_mul_f32_e32 v220, v216, v216\n\t"
      "v_mul_f32_e32 v221, v217, v217\n\t"
      "v_mfma_f32_16x16x32_bf16 v[212:215], v[28:31], v[132:135], v[212:215]\n\t"
      "global_load_dwordx4 v[132:135], v177, s[56:57] sc1\n\t"
      "v_mul_f32_e32 v222, v218, v218\n\t"
      "s_waitcnt vmcnt(7)\n\t"
      "v_mfma_f32_16x16x32_bf16 v[200:203], v[32:35], v[136:139], v[200:203]\n\t"
      "v_mul_f32_e32 v223, v219, v219\n\t"
      "v_fmaak_f32 v220, v235, v220, 0xc0135761\n\t"
      "v_mfma_f32_16x16x32_bf16 v[204:207], v[36:39], v[136:139], v[204:207]\n\t"
      "v_fmaak_f32 v221, v235, v221, 0xc0135761\n\t"
      "v_fmaak_f32 v222, v235, v222, 0xc0135761\n\t"
      "v_mfma_f32_16x16x32_bf16 v[208:211], v[40:43], v[136:139], v[208:211]\n\t"
      "v_fmaak_f32 v223, v235, v223, 0xc0135761\n\t"
      "v_mul_f32_e32 v220, v216, v220\n\t"
      "v_mfma_f32_16x16x32_bf16 v[212:215], v[44:47], v[136:139], v[212:215]\n\t"
      "global_load_dwordx4 v[136:139], v178, s[56:57] sc1\n\t"
      "v_mul_f32_e32 v221, v217, v221\n\t"
      "s_waitcnt vmcnt(7)\n\t"
      "v_mfma_f32_16x16x32_bf16 v[200:203], v[48:51], v[140:143], v[200:203]\n\t"
      "v_mul_f32_e32 v222, v218, v222\n\t"
      "v_mul_f32_e32 v223, v219, v223\n\t"
      "v_mfma_f32_16x16x32_bf16 v[204:207], v[52:55], v[140:143], v[204:207]\n\t"
      "v_exp_f32_e32 v220, v220\n\t"
      "v_mfma_f32_16x16x32_bf16 v[208:211], v[56:59], v[140:143], v[208:211]\n\t"
      "v_exp_f32_e32 v221, v221\n\t"
      "v_mfma_f32_16x16x32_bf16 v[212:215], v[60:63], v[140:143], v[212:215]\n\t"
      "global_load_dwordx4 v[140:143], v179, s[56:57] sc1\n\t"
      "s_waitcnt vmcnt(7)\n\t"
      "v_mfma_f32_16x16x32_bf16 v[200:203], v[64:67], v[144:147], v[200:203]\n\t"
      "v_exp_f32_e32 v222, v222\n\t"
      "v_mfma_f32_16x16x32_bf16 v[204:207], v[68:71], v[144:147], v[204:207]\n\t"
      "v_exp_f32_e32 v223, v223\n\t"
      "v_mfma_f32_16x16x32_bf16 v[208:211], v[72:75], v[144:147], v[208:211]\n\t"
      "v_add_f32_e32 v220, 1.0, v220\n\t"
      "v_mfma_f32_16x16x32_bf16 v[212:215], v[76:79], v[144:147], v[212:215]\n\t"
      "global_load_dwordx4 v[144:147], v180, s[56:57] sc1\n\t"
      "v_add_f32_e32 v221, 1.0, v221\n\t"
      "s_waitcnt vmcnt(7)\n\t"
      "v_mfma_f32_16x16x32_bf16 v[200:203], v[80:83], v[148:151], v[200:203]\n\t"
      "v_add_f32_e32 v222, 1.0, v222\n\t"
      "v_add_f32_e32 v223, 1.0, v223\n\t"
      "v_mfma_f32_16x16x32_bf16 v[204:207], v[84:87], v[148:151], v[204:207]\n\t"
      "v_rcp_f32_e32 v220, v220\n\t"
      "v_mfma_f32_16x16x32_bf16 v[208:211], v[88:91], v[148:151], v[208:211]\n\t"
      "v_rcp_f32_e32 v221, v221\n\t"
      "v_mfma_f32_16x16x32_bf16 v[212:215], v[92:95], v[148:151], v[212:215]\n\t"
      "global_load_dwordx4 v[148:151], v181, s[56:57] sc1\n\t"
      "s_waitcnt vmcnt(7)\n\t"
      "v_mfma_f32_16x16x32_bf16 v[200:203], v[96:99], v[152:155], v[200:203]\n\t"
      "v_rcp_f32_e32 v222, v222\n\t"
      "v_mfma_f32_16x16x32_bf16 v[204:207], v[100:103], v[152:155], v[204:207]\n\t"
      "v_rcp_f32_e32 v223, v223\n\t"
      "v_mfma_f32_16x16x32_bf16 v[208:211], v[104:107], v[152:155], v[208:211]\n\t"
      "v_mul_f32_e32 v216, v216, v220\n\t"
      "v_mfma_f32_16x16x32_bf16 v[212:215], v[108:111], v[152:155], v[212:215]\n\t"
      "global_load_dwordx4 v[152:155], v182, s[56:57] sc1\n\t"
      "v_mul_f32_e32 v217, v217, v221\n\t"
      "s_waitcnt vmcnt(7)\n\t"
      "v_mfma_f32_16x16x32_bf16 v[200:203], v[112:115], v[156:159], v[200:203]\n\t"
      "v_mul_f32_e32 v218, v218, v222\n\t"
      "v_mul_f32_e32 v219, v219, v223\n\t"
      "v_mfma_f32_16x16x32_bf16 v[204:207], v[116:119], v[156:159], v[204:207]\n\t"
      "v_cvt_pk_bf16_f32 v220, v216, v217\n\t"
      "v_cvt_pk_bf16_f32 v221, v218, v219\n\t"
      "v_mfma_f32_16x16x32_bf16 v[208:211], v[120:123], v[156:159], v[208:211]\n\t"
      "v_min_u32_e32 v220, -2, v220\n\t"
      "v_min_u32_e32 v221, -2, v221\n\t"
      "v_mfma_f32_16x16x32_bf16 v[212:215], v[124:127], v[156:159], v[212:215]\n\t"
      "global_load_dwordx4 v[156:159], v183, s[56:57] sc1\n\t"
      "global_store_dwordx2 v245, v[220:221], s[8:9] sc1\n\t"
      "s_add_u32 s8, s8, 57344\n\t"
      "s_addc_u32 s9, s9, 0\n\t"
      "v_cmp_u_f32_e32 vcc, v200, v200\n\t"
      "s_cbranch_vccnz NP1R_%=\n\t"
      "NP1B_%=:\n\t"
      "ds_write_b128 v243, v[200:203] offset:32768\n\t"
      "ds_write_b128 v164, v[204:207] offset:32768\n\t"
      "ds_write_b128 v165, v[208:211] offset:32768\n\t"
      "ds_write_b128 v166, v[212:215] offset:32768\n\t"
      "s_waitcnt lgkmcnt(0)\n\t"
      "s_barrier\n\t"
      "ds_read_b128 v[216:219], v244 offset:32768\n\t"
      "ds_read_b128 v[220:223], v244 offset:36864\n\t"
      "ds_read_b128 v[224:227], v244 offset:40960\n\t"
      "ds_read_b128 v[228:231], v244 offset:45056\n\t"
      "ds_read_b128 v[200:203], v244 offset:49152\n\t"
      "ds_read_b128 v[204:207], v244 offset:53248\n\t"
      "ds_read_b128 v[208:211], v244 offset:57344\n\t"
      "ds_read_b128 v[212:215], v244 offset:61440\n\t"
      "s_waitcnt lgkmcnt(6)\n\t"
      "v_add_f32_e32 v216, v216, v220\n\t"
      "v_add_f32_e32 v217, v217, v221\n\t"
      "v_add_f32_e32 v218, v218, v222\n\t"
      "v_add_f32_e32 v219, v219, v223\n\t"
      "s_waitcnt lgkmcnt(4)\n\t"
      "v_add_f32_e32 v224, v224, v228\n\t"
      "v_add_f32_e32 v225, v225, v229\n\t"
      "v_add_f32_e32 v226, v226, v230\n\t"
      "v_add_f32_e32 v227, v227, v231\n\t"
      "s_waitcnt lgkmcnt(2)\n\t"
      "v_add_f32_e32 v200, v200, v204\n\t"
      "v_add_f32_e32 v201, v201, v205\n\t"
      "v_add_f32_e32 v202, v202, v206\n\t"
      "v_add_f32_e32 v203, v203, v207\n\t"
      "v_add_f32_e32 v216, v216, v224\n\t"
      "v_add_f32_e32 v217, v217, v225\n\t"
      "v_add_f32_e32 v218, v218, v226\n\t"
      "v_add_f32_e32 v219, v219, v227\n\t"
      "s_waitcnt lgkmcnt(0)\n\t"
      "v_add_f32_e32 v208, v208, v212\n\t"
      "s_waitcnt vmcnt(8)\n\t"
      "v_mfma_f32_16x16x32_bf16 v[184:187], v[0:3], v[128:131], 0\n\t"
      "v_add_f32_e32 v209, v209, v213\n\t"
      "v_add_f32_e32 v210, v210, v214\n\t"
      "v_mfma_f32_16x16x32_bf16 v[188:191], v[4:7], v[128:131], 0\n\t"
      "v_add_f32_e32 v211, v211, v215\n\t"
      "v_add_f32_e32 v200, v200, v208\n\t"
      "v_mfma_f32_16x16x32_bf16 v[192:195], v[8:11], v[128:131], 0\n\t"
      "v_add_f32_e32 v201, v201, v209\n\t"
      "v_add_f32_e32 v202, v202, v210\n\t"
      "v_mfma_f32_16x16x32_bf16 v[196:199], v[12:15], v[128:131], 0\n\t"
      "global_load_dwordx4 v[128:131], v176, s[58:59] sc1\n\t"
      "v_add_f32_e32 v203, v203, v211\n\t"
      "s_waitcnt vmcnt(8)\n\t"
      "v_mfma_f32_16x16x32_bf16 v[184:187], v[16:19], v[132:135], v[184:187]\n\t"
      "v_add_f32_e32 v216, v216, v200\n\t"
      "v_add_f32_e32 v217, v217, v201\n\t"
      "s_cmp_eq_u32 s17, 0\n\t"
      "s_cbranch_scc0 HI0_%=\n\t"
      "s_mov_b64 exec, 0xff\n\t"
      "global_store_dwordx2 v247, v[232:233], s[12:13] sc1\n\t"
      "s_mov_b64 exec, -1\n\t"
      "s_nop 0\n\t"
      "v_add_u32_e32 v232, 6, v232\n\t"
      "HI0_%=:\n\t"
      "v_mfma_f32_16x16x32_bf16 v[188:191], v[20:23], v[132:135], v[188:191]\n\t"
      "v_add_f32_e32 v218, v218, v202\n\t"
      "v_add_f32_e32 v219, v219, v203\n\t"
      "v_mfma_f32_16x16x32_bf16 v[192:195], v[24:27], v[132:135], v[192:195]\n\t"
      "v_mul_f32_e32 v220, v216, v216\n\t"
      "v_mul_f32_e32 v221, v217, v217\n\t"
      "v_mfma_f32_16x16x32_bf16 v[196:199], v[28:31], v[132:135], v[196:199]\n\t"
      "global_load_dwordx4 v[132:135], v177, s[58:59] sc1\n\t"
      "v_mul_f32_e32 v222, v218, v218\n\t"
      "s_waitcnt vmcnt(8)\n\t"
      "v_mfma_f32_16x16x32_bf16 v[184:187], v[32:35], v[136:139], v[184:187]\n\t"
      "v_mul_f32_e32 v223, v219, v219\n\t"
      "v_fmaak_f32 v220, v235, v220, 0xc0135761\n\t"
      "v_mfma_f32_16x16x32_bf16 v[188:191], v[36:39], v[136:139], v[188:191]\n\t"
      "v_fmaak_f32 v221, v235, v221, 0xc0135761\n\t"
      "v_fmaak_f32 v222, v235, v222, 0xc0135761\n\t"
      "v_mfma_f32_16x16x32_bf16 v[192:195], v[40:43], v[136:139], v[192:195]\n\t"
      "v_fmaak_f32 v223, v235, v223, 0xc0135761\n\t"
      "v_mul_f32_e32 v220, v216, v220\n\t"
      "v_mfma_f32_16x16x32_bf16 v[196:199], v[44:47], v[136:139], v[196:199]\n\t"
      "global_load_dwordx4 v[136:139], v178, s[58:59] sc1\n\t"
      "v_mul_f32_e32 v221, v217, v221\n\t"
      "s_waitcnt vmcnt(8)\n\t"
      "v_mfma_f32_16x16x32_bf16 v[184:187], v[48:51], v[140:143], v[184:187]\n\t"
      "v_mul_f32_e32 v222, v218, v222\n\t"
      "v_mul_f32_e32 v223, v219, v223\n\t"
      "v_mfma_f32_16x16x32_bf16 v[188:191], v[52:55], v[140:143], v[188:191]\n\t"
      "v_exp_f32_e32 v220, v220\n\t"
      "v_mfma_f32_16x16x32_bf16 v[192:195], v[56:59], v[140:143], v[192:195]\n\t"
      "v_exp_f32_e32 v221, v221\n\t"
      "v_mfma_f32_16x16x32_bf16 v[196:199], v[60:63], v[140:143], v[196:199]\n\t"
      "global_load_dwordx4 v[140:143], v179, s[58:59] sc1\n\t"
      "s_waitcnt vmcnt(8)\n\t"
      "v_mfma_f32_16x16x32_bf16 v[184:187], v[64:67], v[144:147], v[184:187]\n\t"
      "v_exp_f32_e32 v222, v222\n\t"
      "v_mfma_f32_16x16x32_bf16 v[188:191], v[68:71], v[144:147], v[188:191]\n\t"
      "v_exp_f32_e32 v223, v223\n\t"
      "v_mfma_f32_16x16x32_bf16 v[192:195], v[72:75], v[144:147], v[192:195]\n\t"
      "v_add_f32_e32 v220, 1.0, v220\n\t"
      "v_mfma_f32_16x16x32_bf16 v[196:199], v[76:79], v[144:147], v[196:199]\n\t"
      "global_load_dwordx4 v[144:147], v180, s[58:59] sc1\n\t"
      "v_add_f32_e32 v221, 1.0, v221\n\t"
      "s_waitcnt vmcnt(8)\n\t"
      "v_mfma_f32_16x16x32_bf16 v[184:187], v[80:83], v[148:151], v[184:187]\n\t"
      "v_add_f32_e32 v222, 1.0, v222\n\t"
      "v_add_f32_e32 v223, 1.0, v223\n\t"
      "v_mfma_f32_16x16x32_bf16 v[188:191], v[84:87], v[148:151], v[188:191]\n\t"
      "v_rcp_f32_e32 v220, v220\n\t"
      "v_mfma_f32_16x16x32_bf16 v[192:195], v[88:91], v[148:151], v[192:195]\n\t"
      "v_rcp_f32_e32 v221, v221\n\t"
      "v_mfma_f32_16x16x32_bf16 v[196:199], v[92:95], v[148:151], v[196:199]\n\t"
      "global_load_dwordx4 v[148:151], v181, s[58:59] sc1\n\t"
      "s_waitcnt vmcnt(8)\n\t"
      "v_mfma_f32_16x16x32_bf16 v[184:187], v[96:99], v[152:155], v[184:187]\n\t"
      "v_rcp_f32_e32 v222, v222\n\t"
      "v_mfma_f32_16x16x32_bf16 v[188:191], v[100:103], v[152:155], v[188:191]\n\t"
      "v_rcp_f32_e32 v223, v223\n\t"
      "v_mfma_f32_16x16x32_bf16 v[192:195], v[104:107], v[152:155], v[192:195]\n\t"
      "v_mul_f32_e32 v216, v216, v220\n\t"
      "v_mfma_f32_16x16x32_bf16 v[196:199], v[108:111], v[152:155], v[196:199]\n\t"
      "global_load_dwordx4 v[152:155], v182, s[58:59] sc1\n\t"
      "v_mul_f32_e32 v217, v217, v221\n\t"
      "s_waitcnt vmcnt(8)\n\t"
      "v_mfma_f32_16x16x32_bf16 v[184:187], v[112:115], v[156:159], v[184:187]\n\t"
      "v_mul_f32_e32 v218, v218, v222\n\t"
      "v_mul_f32_e32 v219, v219, v223\n\t"
      "v_mfma_f32_16x16x32_bf16 v[188:191], v[116:119], v[156:159], v[188:191]\n\t"
      "v_cvt_pk_bf16_f32 v220, v216, v217\n\t"
      "v_cvt_pk_bf16_f32 v221, v218, v219\n\t"
      "v_mfma_f32_16x16x32_bf16 v[192:195], v[120:123], v[156:159], v[192:195]\n\t"
      "v_min_u32_e32 v220, -2, v220\n\t"
      "v_min_u32_e32 v221, -2, v221\n\t"
      "v_mfma_f32_16x16x32_bf16 v[196:199], v[124:127], v[156:159], v[196:199]\n\t"
      "global_load_dwordx4 v[156:159], v183, s[58:59] sc1\n\t"
      "global_store_dwordx2 v245, v[220:221], s[8:9] sc1\n\t"
      "s_add_u32 s8, s8, 57344\n\t"
      "s_addc_u32 s9, s9, 0\n\t"
      "v_cmp_u_f32_e32 vcc, v184, v184\n\t"
      "s_cbranch_vccnz NP2R_%=\n\t"
      "NP2B_%=:\n\t"
      "ds_write_b128 v243, v[184:187]\n\t"
      "ds_write_b128 v164, v[188:191]\n\t"
      "ds_write_b128 v165, v[192:195]\n\t"
      "ds_write_b128 v166, v[196:199]\n\t"
      "s_waitcnt lgkmcnt(0)\n\t"
      "s_barrier\n\t"
      "ds_read_b128 v[216:219], v244\n\t"
      "ds_read_b128 v[220:223], v244 offset:4096\n\t"
      "ds_read_b128 v[224:227], v244 offset:8192\n\t"
      "ds_read_b128 v[228:231], v244 offset:12288\n\t"
      "ds_read_b128 v[184:187], v244 offset:16384\n\t"
      "ds_read_b128 v[188:191], v244 offset:20480\n\t"
      "ds_read_b128 v[192:195], v244 offset:24576\n\t"
      "ds_read_b128 v[196:199], v244 offset:28672\n\t"
      "s_waitcnt lgkmcnt(6)\n\t"
      "v_add_f32_e32 v216, v216, v220\n\t"
      "v_add_f32_e32 v217, v217, v221\n\t"
      "v_add_f32_e32 v218, v218, v222\n\t"
      "v_add_f32_e32 v219, v219, v223\n\t"
      "s_waitcnt lgkmcnt(4)\n\t"
      "v_add_f32_e32 v224, v224, v228\n\t"
      "v_add_f32_e32 v225, v225, v229\n\t"
      "v_add_f32_e32 v226, v226, v230\n\t"
      "v_add_f32_e32 v227, v227, v231\n\t"
      "s_waitcnt lgkmcnt(2)\n\t"
      "v_add_f32_e32 v184, v184, v188\n\t"
      "v_add_f32_e32 v185, v185, v189\n\t"
      "v_add_f32_e32 v186, v186, v190\n\t"
      "v_add_f32_e32 v187, v187, v191\n\t"
      "v_add_f32_e32 v216, v216, v224\n\t"
      "v_add_f32_e32 v217, v217, v225\n\t"
      "v_add_f32_e32 v218, v218, v226\n\t"
      "v_add_f32_e32 v219, v219, v227\n\t"
      "s_waitcnt lgkmcnt(0)\n\t"
      "v_add_f32_e32 v192, v192, v196\n\t"
      "s_waitcnt vmcnt(8)\n\t"
      "v_mfma_f32_16x16x32_bf16 v[200:203], v[0:3], v[128:131], 0\n\t"
      "v_add_f32_e32 v193, v193, v197\n\t"
      "v_add_f32_e32 v194, v194, v198\n\t"
      "v_mfma_f32_16x16x32_bf16 v[204:207], v[4:7], v[128:131], 0\n\t"
      "v_add_f32_e32 v195, v195, v199\n\t"
      "v_add_f32_e32 v184, v184, v192\n\t"
      "v_mfma_f32_16x16x32_bf16 v[208:211], v[8:11], v[128:131], 0\n\t"
      "v_add_f32_e32 v185, v185, v193\n\t"
      "v_add_f32_e32 v186, v186, v194\n\t"
      "v_mfma_f32_16x16x32_bf16 v[212:215], v[12:15], v[128:131], 0\n\t"
      "global_load_dwordx4 v[128:131], v176, s[60:61] sc1\n\t"
      "v_add_f32_e32 v187, v187, v195\n\t"
      "s_waitcnt vmcnt(8)\n\t"
      "v_mfma_f32_16x16x32_bf16 v[200:203], v[16:19], v[132:135], v[200:203]\n\t"
      "v_add_f32_e32 v216, v216, v184\n\t"
      "v_add_f32_e32 v217, v217, v185\n\t"
      "v_mfma_f32_16x16x32_bf16 v[204:207], v[20:23], v[132:135], v[204:207]\n\t"
      "v_add_f32_e32 v218, v218, v186\n\t"
      "v_add_f32_e32 v219, v219, v187\n\t"
      "v_mfma_f32_16x16x32_bf16 v[208:211], v[24:27], v[132:135], v[208:211]\n\t"
      "v_mul_f32_e32 v220, v216, v216\n\t"
      "v_mul_f32_e32 v221, v217, v217\n\t"
      "v_mfma_f32_16x16x32_bf16 v[212:215], v[28:31], v[132:135], v[212:215]\n\t"
      "global_load_dwordx4 v[132:135], v177, s[60:61] sc1\n\t"
      "v_mul_f32_e32 v222, v218, v218\n\t"
      "s_waitcnt vmcnt(8)\n\t"
      "v_mfma_f32_16x16x32_bf16 v[200:203], v[32:35], v[136:139], v[200:203]\n\t"
      "v_mul_f32_e32 v223, v219, v219\n\t"
      "v_fmaak_f32 v220, v235, v220, 0xc0135761\n\t"
      "v_mfma_f32_16x16x32_bf16 v[204:207], v[36:39], v[136:139], v[204:207]\n\t"
      "v_fmaak_f32 v221, v235, v221, 0xc0135761\n\t"
      "v_fmaak_f32 v222, v235, v222, 0xc0135761\n\t"
      "v_mfma_f32_16x16x32_bf16 v[208:211], v[40:43], v[136:139], v[208:211]\n\t"
      "v_fmaak_f32 v223, v235, v223, 0xc0135761\n\t"
      "v_mul_f32_e32 v220, v216, v220\n\t"
      "v_mfma_f32_16x16x32_bf16 v[212:215], v[44:47], v[136:139], v[212:215]\n\t"
      "global_load_dwordx4 v[136:139], v178, s[60:61] sc1\n\t"
      "v_mul_f32_e32 v221, v217, v221\n\t"
      "s_waitcnt vmcnt(8)\n\t"
      "v_mfma_f32_16x16x32_bf16 v[200:203], v[48:51], v[140:143], v[200:203]\n\t"
      "v_mul_f32_e32 v222, v218, v222\n\t"
      "v_mul_f32_e32 v223, v219, v223\n\t"
      "v_mfma_f32_16x16x32_bf16 v[204:207], v[52:55], v[140:143], v[204:207]\n\t"
      "v_exp_f32_e32 v220, v220\n\t"
      "v_mfma_f32_16x16x32_bf16 v[208:211], v[56:59], v[140:143], v[208:211]\n\t"
      "v_exp_f32_e32 v221, v221\n\t"
      "v_mfma_f32_16x16x32_bf16 v[212:215], v[60:63], v[140:143], v[212:215]\n\t"
      "global_load_dwordx4 v[140:143], v179, s[60:61] sc1\n\t"
      "s_waitcnt vmcnt(8)\n\t"
      "v_mfma_f32_16x16x32_bf16 v[200:203], v[64:67], v[144:147], v[200:203]\n\t"
      "v_exp_f32_e32 v222, v222\n\t"
      "v_mfma_f32_16x16x32_bf16 v[204:207], v[68:71], v[144:147], v[204:207]\n\t"
      "v_exp_f32_e32 v223, v223\n\t"
      "v_mfma_f32_16x16x32_bf16 v[208:211], v[72:75], v[144:147], v[208:211]\n\t"
      "v_add_f32_e32 v220, 1.0, v220\n\t"
      "v_mfma_f32_16x16x32_bf16 v[212:215], v[76:79], v[144:147], v[212:215]\n\t"
      "global_load_dwordx4 v[144:147], v180, s[60:61] sc1\n\t"
      "v_add_f32_e32 v221, 1.0, v221\n\t"
      "s_waitcnt vmcnt(8)\n\t"
      "v_mfma_f32_16x16x32_bf16 v[200:203], v[80:83], v[148:151], v[200:203]\n\t"
      "v_add_f32_e32 v222, 1.0, v222\n\t"
      "v_add_f32_e32 v223, 1.0, v223\n\t"
      "v_mfma_f32_16x16x32_bf16 v[204:207], v[84:87], v[148:151], v[204:207]\n\t"
      "v_rcp_f32_e32 v220, v220\n\t"
      "v_mfma_f32_16x16x32_bf16 v[208:211], v[88:91], v[148:151], v[208:211]\n\t"
      "v_rcp_f32_e32 v221, v221\n\t"
      "v_mfma_f32_16x16x32_bf16 v[212:215], v[92:95], v[148:151], v[212:215]\n\t"
      "global_load_dwordx4 v[148:151], v181, s[60:61] sc1\n\t"
      "s_waitcnt vmcnt(8)\n\t"
      "v_mfma_f32_16x16x32_bf16 v[200:203], v[96:99], v[152:155], v[200:203]\n\t"
      "v_rcp_f32_e32 v222, v222\n\t"
      "v_mfma_f32_16x16x32_bf16 v[204:207], v[100:103], v[152:155], v[204:207]\n\t"
      "v_rcp_f32_e32 v223, v223\n\t"
      "v_mfma_f32_16x16x32_bf16 v[208:211], v[104:107], v[152:155], v[208:211]\n\t"
      "v_mul_f32_e32 v216, v216, v220\n\t"
      "v_mfma_f32_16x16x32_bf16 v[212:215], v[108:111], v[152:155], v[212:215]\n\t"
      "global_load_dwordx4 v[152:155], v182, s[60:61] sc1\n\t"
      "v_mul_f32_e32 v217, v217, v221\n\t"
      "s_waitcnt vmcnt(8)\n\t"
      "v_mfma_f32_16x16x32_bf16 v[200:203], v[112:115], v[156:159], v[200:203]\n\t"
      "v_mul_f32_e32 v218, v218, v222\n\t"
      "v_mul_f32_e32 v219, v219, v223\n\t"
      "v_mfma_f32_16x16x32_bf16 v[204:207], v[116:119], v[156:159], v[204:207]\n\t"
      "v_cvt_pk_bf16_f32 v220, v216, v217\n\t"
      "v_cvt_pk_bf16_f32 v221, v218, v219\n\t"
      "v_mfma_f32_16x16x32_bf16 v[208:211], v[120:123], v[156:159], v[208:211]\n\t"
      "v_min_u32_e32 v220, -2, v220\n\t"
      "v_min_u32_e32 v221, -2, v221\n\t"
      "v_mfma_f32_16x16x32_bf16 v[212:215], v[124:127], v[156:159], v[212:215]\n\t"
      "global_load_dwordx4 v[156:159], v183, s[60:61] sc1\n\t"
      "global_store_dwordx2 v245, v[220:221], s[8:9] sc1\n\t"
      "s_add_u32 s8, s8, 57344\n\t"
      "s_addc_u32 s9, s9, 0\n\t"
      "v_cmp_u_f32_e32 vcc, v200, v200\n\t"
      "s_cbranch_vccnz NP3R_%=\n\t"
      "NP3B_%=:\n\t"
      "ds_write_b128 v243, v[200:203] offset:32768\n\t"
      "ds_write_b128 v164, v[204:207] offset:32768\n\t"
      "ds_write_b128 v165, v[208:211] offset:32768\n\t"
      "ds_write_b128 v166, v[212:215] offset:32768\n\t"
      "s_waitcnt lgkmcnt(0)\n\t"
      "s_barrier\n\t"
      "ds_read_b128 v[216:219], v244 offset:32768\n\t"
      "ds_read_b128 v[220:223], v244 offset:36864\n\t"
      "ds_read_b128 v[224:227], v244 offset:40960\n\t"
      "ds_read_b128 v[228:231], v244 offset:45056\n\t"
      "ds_read_b128 v[200:203], v244 offset:49152\n\t"
      "ds_read_b128 v[204:207], v244 offset:53248\n\t"
      "ds_read_b128 v[208:211], v244 offset:57344\n\t"
      "ds_read_b128 v[212:215], v244 offset:61440\n\t"
      "s_waitcnt lgkmcnt(6)\n\t"
      "v_add_f32_e32 v216, v216, v220\n\t"
      "v_add_f32_e32 v217, v217, v221\n\t"
      "v_add_f32_e32 v218, v218, v222\n\t"
      "v_add_f32_e32 v219, v219, v223\n\t"
      "s_waitcnt lgkmcnt(4)\n\t"
      "v_add_f32_e32 v224, v224, v228\n\t"
      "v_add_f32_e32 v225, v225, v229\n\t"
      "v_add_f32_e32 v226, v226, v230\n\t"
      "v_add_f32_e32 v227, v227, v231\n\t"
      "s_waitcnt lgkmcnt(2)\n\t"
      "v_add_f32_e32 v200, v200, v204\n\t"
      "v_add_f32_e32 v201, v201, v205\n\t"
      "v_add_f32_e32 v202, v202, v206\n\t"
      "v_add_f32_e32 v203, v203, v207\n\t"
      "v_add_f32_e32 v216, v216, v224\n\t"
      "v_add_f32_e32 v217, v217, v225\n\t"
      "v_add_f32_e32 v218, v218, v226\n\t"
      "v_add_f32_e32 v219, v219, v227\n\t"
      "s_waitcnt lgkmcnt(0)\n\t"
      "v_add_f32_e32 v208, v208, v212\n\t"
      "s_waitcnt vmcnt(8)\n\t"
      "v_mfma_f32_16x16x32_bf16 v[184:187], v[0:3], v[128:131], 0\n\t"
      "v_add_f32_e32 v209, v209, v213\n\t"
      "v_add_f32_e32 v210, v210, v214\n\t"
      "v_mfma_f32_16x16x32_bf16 v[188:191], v[4:7], v[128:131], 0\n\t"
      "v_add_f32_e32 v211, v211, v215\n\t"
      "v_add_f32_e32 v200, v200, v208\n\t"
      "v_mfma_f32_16x16x32_bf16 v[192:195], v[8:11], v[128:131], 0\n\t"
      "v_add_f32_e32 v201, v201, v209\n\t"
      "v_add_f32_e32 v202, v202, v210\n\t"
      "v_mfma_f32_16x16x32_bf16 v[196:199], v[12:15], v[128:131], 0\n\t"
      "global_load_dwordx4 v[128:131], v176, s[62:63] sc1\n\t"
      "v_add_f32_e32 v203, v203, v211\n\t"
      "s_waitcnt vmcnt(8)\n\t"
      "v_mfma_f32_16x16x32_bf16 v[184:187], v[16:19], v[132:135], v[184:187]\n\t"
      "v_add_f32_e32 v216, v216, v200\n\t"
      "v_add_f32_e32 v217, v217, v201\n\t"
      "v_mfma_f32_16x16x32_bf16 v[188:191], v[20:23], v[132:135], v[188:191]\n\t"
      "v_add_f32_e32 v218, v218, v202\n\t"
      "v_add_f32_e32 v219, v219, v203\n\t"
      "v_mfma_f32_16x16x32_bf16 v[192:195], v[24:27], v[132:135], v[192:195]\n\t"
      "v_mul_f32_e32 v220, v216, v216\n\t"
      "v_mul_f32_e32 v221, v217, v217\n\t"
      "v_mfma_f32_16x16x32_bf16 v[196:199], v[28:31], v[132:135], v[196:199]\n\t"
      "global_load_dwordx4 v[132:135], v177, s[62:63] sc1\n\t"
      "v_mul_f32_e32 v222, v218, v218\n\t"
      "s_waitcnt vmcnt(8)\n\t"
      "v_mfma_f32_16x16x32_bf16 v[184:187], v[32:35], v[136:139], v[184:187]\n\t"
      "v_mul_f32_e32 v223, v219, v219\n\t"
      "v_fmaak_f32 v220, v235, v220, 0xc0135761\n\t"
      "v_mfma_f32_16x16x32_bf16 v[188:191], v[36:39], v[136:139], v[188:191]\n\t"
      "v_fmaak_f32 v221, v235, v221, 0xc0135761\n\t"
      "v_fmaak_f32 v222, v235, v222, 0xc0135761\n\t"
      "v_mfma_f32_16x16x32_bf16 v[192:195], v[40:43], v[136:139], v[192:195]\n\t"
      "v_fmaak_f32 v223, v235, v223, 0xc0135761\n\t"
      "v_mul_f32_e32 v220, v216, v220\n\t"
      "v_mfma_f32_16x16x32_bf16 v[196:199], v[44:47], v[136:139], v[196:199]\n\t"
      "global_load_dwordx4 v[136:139], v178, s[62:63] sc1\n\t"
      "v_mul_f32_e32 v221, v217, v221\n\t"
      "s_waitcnt vmcnt(8)\n\t"
      "v_mfma_f32_16x16x32_bf16 v[184:187], v[48:51], v[140:143], v[184:187]\n\t"
      "v_mul_f32_e32 v222, v218, v222\n\t"
      "v_mul_f32_e32 v223, v219, v223\n\t"
      "v_mfma_f32_16x16x32_bf16 v[188:191], v[52:55], v[140:143], v[188:191]\n\t"
      "v_exp_f32_e32 v220, v220\n\t"
      "v_mfma_f32_16x16x32_bf16 v[192:195], v[56:59], v[140:143], v[192:195]\n\t"
      "v_exp_f32_e32 v221, v221\n\t"
      "v_mfma_f32_16x16x32_bf16 v[196:199], v[60:63], v[140:143], v[196:199]\n\t"
      "global_load_dwordx4 v[140:143], v179, s[62:63] sc1\n\t"
      "s_waitcnt vmcnt(8)\n\t"
      "v_mfma_f32_16x16x32_bf16 v[184:187], v[64:67], v[144:147], v[184:187]\n\t"
      "v_exp_f32_e32 v222, v222\n\t"
      "v_mfma_f32_16x16x32_bf16 v[188:191], v[68:71], v[144:147], v[188:191]\n\t"
      "v_exp_f32_e32 v223, v223\n\t"
      "v_mfma_f32_16x16x32_bf16 v[192:195], v[72:75], v[144:147], v[192:195]\n\t"
      "v_add_f32_e32 v220, 1.0, v220\n\t"
      "v_mfma_f32_16x16x32_bf16 v[196:199], v[76:79], v[144:147], v[196:199]\n\t"
      "global_load_dwordx4 v[144:147], v180, s[62:63] sc1\n\t"
      "v_add_f32_e32 v221, 1.0, v221\n\t"
      "s_waitcnt vmcnt(8)\n\t"
      "v_mfma_f32_16x16x32_bf16 v[184:187], v[80:83], v[148:151], v[184:187]\n\t"
      "v_add_f32_e32 v222, 1.0, v222\n\t"
      "v_add_f32_e32 v223, 1.0, v223\n\t"
      "v_mfma_f32_16x16x32_bf16 v[188:191], v[84:87], v[148:151], v[188:191]\n\t"
      "v_rcp_f32_e32 v220, v220\n\t"
      "v_mfma_f32_16x16x32_bf16 v[192:195], v[88:91], v[148:151], v[192:195]\n\t"
      "v_rcp_f32_e32 v221, v221\n\t"
      "v_mfma_f32_16x16x32_bf16 v[196:199], v[92:95], v[148:151], v[196:199]\n\t"
      "global_load_dwordx4 v[148:151], v181, s[62:63] sc1\n\t"
      "s_waitcnt vmcnt(8)\n\t"
      "v_mfma_f32_16x16x32_bf16 v[184:187], v[96:99], v[152:155], v[184:187]\n\t"
      "v_rcp_f32_e32 v222, v222\n\t"
      "v_mfma_f32_16x16x32_bf16 v[188:191], v[100:103], v[152:155], v[188:191]\n\t"
      "v_rcp_f32_e32 v223, v223\n\t"
      "v_mfma_f32_16x16x32_bf16 v[192:195], v[104:107], v[152:155], v[192:195]\n\t"
      "v_mul_f32_e32 v216, v216, v220\n\t"
      "v_mfma_f32_16x16x32_bf16 v[196:199], v[108:111], v[152:155], v[196:199]\n\t"
      "global_load_dwordx4 v[152:155], v182, s[62:63] sc1\n\t"
      "v_mul_f32_e32 v217, v217, v221\n\t"
      "s_waitcnt vmcnt(8)\n\t"
      "v_mfma_f32_16x16x32_bf16 v[184:187], v[112:115], v[156:159], v[184:187]\n\t"
      "v_mul_f32_e32 v218, v218, v222\n\t"
      "v_mul_f32_e32 v219, v219, v223\n\t"
      "v_mfma_f32_16x16x32_bf16 v[188:191], v[116:119], v[156:159], v[188:191]\n\t"
      "v_cvt_pk_bf16_f32 v220, v216, v217\n\t"
      "v_cvt_pk_bf16_f32 v221, v218, v219\n\t"
      "v_mfma_f32_16x16x32_bf16 v[192:195], v[120:123], v[156:159], v[192:195]\n\t"
      "v_min_u32_e32 v220, -2, v220\n\t"
      "v_min_u32_e32 v221, -2, v221\n\t"
      "v_mfma_f32_16x16x32_bf16 v[196:199], v[124:127], v[156:159], v[196:199]\n\t"
      "global_load_dwordx4 v[156:159], v183, s[62:63] sc1\n\t"
      "global_store_dwordx2 v245, v[220:221], s[8:9] sc1\n\t"
      "s_add_u32 s8, s8, 57344\n\t"
      "s_addc_u32 s9, s9, 0\n\t"
      "v_cmp_u_f32_e32 vcc, v184, v184\n\t"
      "s_cbranch_vccnz NP4R_%=\n\t"
      "NP4B_%=:\n\t"
      "ds_write_b128 v243, v[184:187]\n\t"
      "ds_write_b128 v164, v[188:191]\n\t"
      "ds_write_b128 v165, v[192:195]\n\t"
      "ds_write_b128 v166, v[196:199]\n\t"
      "s_waitcnt lgkmcnt(0)\n\t"
      "s_barrier\n\t"
      "ds_read_b128 v[216:219], v244\n\t"
      "ds_read_b128 v[220:223], v244 offset:4096\n\t"
      "ds_read_b128 v[224:227], v244 offset:8192\n\t"
      "ds_read_b128 v[228:231], v244 offset:12288\n\t"
      "ds_read_b128 v[184:187], v244 offset:16384\n\t"
      "ds_read_b128 v[188:191], v244 offset:20480\n\t"
      "ds_read_b128 v[192:195], v244 offset:24576\n\t"
      "ds_read_b128 v[196:199], v244 offset:28672\n\t"
      "s_waitcnt lgkmcnt(6)\n\t"
      "v_add_f32_e32 v216, v216, v220\n\t"
      "v_add_f32_e32 v217, v217, v221\n\t"
      "v_add_f32_e32 v218, v218, v222\n\t"
      "v_add_f32_e32 v219, v219, v223\n\t"
      "s_waitcnt lgkmcnt(4)\n\t"
      "v_add_f32_e32 v224, v224, v228\n\t"
      "v_add_f32_e32 v225, v225, v229\n\t"
      "v_add_f32_e32 v226, v226, v230\n\t"
      "v_add_f32_e32 v227, v227, v231\n\t"
      "s_waitcnt lgkmcnt(2)\n\t"
      "v_add_f32_e32 v184, v184, v188\n\t"
      "v_add_f32_e32 v185, v185, v189\n\t"
      "v_add_f32_e32 v186, v186, v190\n\t"
      "v_add_f32_e32 v187, v187, v191\n\t"
      "v_add_f32_e32 v216, v216, v224\n\t"
      "v_add_f32_e32 v217, v217, v225\n\t"
      "v_add_f32_e32 v218, v218, v226\n\t"
      "v_add_f32_e32 v219, v219, v227\n\t"
      "s_waitcnt lgkmcnt(0)\n\t"
      "v_add_f32_e32 v192, v192, v196\n\t"
      "s_waitcnt vmcnt(8)\n\t"
      "v_mfma_f32_16x16x32_bf16 v[200:203], v[0:3], v[128:131], 0\n\t"
      "v_add_f32_e32 v193, v193, v197\n\t"
      "v_add_f32_e32 v194, v194, v198\n\t"
      "v_mfma_f32_16x16x32_bf16 v[204:207], v[4:7], v[128:131], 0\n\t"
      "v_add_f32_e32 v195, v195, v199\n\t"
      "v_add_f32_e32 v184, v184, v192\n\t"
      "v_mfma_f32_16x16x32_bf16 v[208:211], v[8:11], v[128:131], 0\n\t"
      "v_add_f32_e32 v185, v185, v193\n\t"
      "v_add_f32_e32 v186, v186, v194\n\t"
      "v_mfma_f32_16x16x32_bf16 v[212:215], v[12:15], v[128:131], 0\n\t"
      "global_load_dwordx4 v[128:131], v176, s[64:65] sc1\n\t"
      "v_add_f32_e32 v187, v187, v195\n\t"
      "s_waitcnt vmcnt(8)\n\t"
      "v_mfma_f32_16x16x32_bf16 v[200:203], v[16:19], v[132:135], v[200:203]\n\t"
      "v_add_f32_e32 v216, v216, v184\n\t"
      "v_add_f32_e32 v217, v217, v185\n\t"
      "v_mfma_f32_16x16x32_bf16 v[204:207], v[20:23], v[132:135], v[204:207]\n\t"
      "v_add_f32_e32 v218, v218, v186\n\t"
      "v_add_f32_e32 v219, v219, v187\n\t"
      "v_mfma_f32_16x16x32_bf16 v[208:211], v[24:27], v[132:135], v[208:211]\n\t"
      "v_mul_f32_e32 v220, v216, v216\n\t"
      "v_mul_f32_e32 v221, v217, v217\n\t"
      "v_mfma_f32_16x16x32_bf16 v[212:215], v[28:31], v[132:135], v[212:215]\n\t"
      "global_load_dwordx4 v[132:135], v177, s[64:65] sc1\n\t"
      "v_mul_f32_e32 v222, v218, v218\n\t"
      "s_waitcnt vmcnt(8)\n\t"
      "v_mfma_f32_16x16x32_bf16 v[200:203], v[32:35], v[136:139], v[200:203]\n\t"
      "v_mul_f32_e32 v223, v219, v219\n\t"
      "v_fmaak_f32 v220, v235, v220, 0xc0135761\n\t"
      "v_mfma_f32_16x16x32_bf16 v[204:207], v[36:39], v[136:139], v[204:207]\n\t"
      "v_fmaak_f32 v221, v235, v221, 0xc0135761\n\t"
      "v_fmaak_f32 v222, v235, v222, 0xc0135761\n\t"
      "v_mfma_f32_16x16x32_bf16 v[208:211], v[40:43], v[136:139], v[208:211]\n\t"
      "v_fmaak_f32 v223, v235, v223, 0xc0135761\n\t"
      "v_mul_f32_e32 v220, v216, v220\n\t"
      "v_mfma_f32_16x16x32_bf16 v[212:215], v[44:47], v[136:139], v[212:215]\n\t"
      "global_load_dwordx4 v[136:139], v178, s[64:65] sc1\n\t"
      "v_mul_f32_e32 v221, v217, v221\n\t"
      "s_waitcnt vmcnt(8)\n\t"
      "v_mfma_f32_16x16x32_bf16 v[200:203], v[48:51], v[140:143], v[200:203]\n\t"
      "v_mul_f32_e32 v222, v218, v222\n\t"
      "v_mul_f32_e32 v223, v219, v223\n\t"
      "v_mfma_f32_16x16x32_bf16 v[204:207], v[52:55], v[140:143], v[204:207]\n\t"
      "v_exp_f32_e32 v220, v220\n\t"
      "v_mfma_f32_16x16x32_bf16 v[208:211], v[56:59], v[140:143], v[208:211]\n\t"
      "v_exp_f32_e32 v221, v221\n\t"
      "v_mfma_f32_16x16x32_bf16 v[212:215], v[60:63], v[140:143], v[212:215]\n\t"
      "global_load_dwordx4 v[140:143], v179, s[64:65] sc1\n\t"
      "s_waitcnt vmcnt(8)\n\t"
      "v_mfma_f32_16x16x32_bf16 v[200:203], v[64:67], v[144:147], v[200:203]\n\t"
      "v_exp_f32_e32 v222, v222\n\t"
      "v_mfma_f32_16x16x32_bf16 v[204:207], v[68:71], v[144:147], v[204:207]\n\t"
      "v_exp_f32_e32 v223, v223\n\t"
      "v_mfma_f32_16x16x32_bf16 v[208:211], v[72:75], v[144:147], v[208:211]\n\t"
      "v_add_f32_e32 v220, 1.0, v220\n\t"
      "v_mfma_f32_16x16x32_bf16 v[212:215], v[76:79], v[144:147], v[212:215]\n\t"
      "global_load_dwordx4 v[144:147], v180, s[64:65] sc1\n\t"
      "v_add_f32_e32 v221, 1.0, v221\n\t"
      "s_waitcnt vmcnt(8)\n\t"
      "v_mfma_f32_16x16x32_bf16 v[200:203], v[80:83], v[148:151], v[200:203]\n\t"
      "v_add_f32_e32 v222, 1.0, v222\n\t"
      "v_add_f32_e32 v223, 1.0, v223\n\t"
      "v_mfma_f32_16x16x32_bf16 v[204:207], v[84:87], v[148:151], v[204:207]\n\t"
      "v_rcp_f32_e32 v220, v220\n\t"
      "v_mfma_f32_16x16x32_bf16 v[208:211], v[88:91], v[148:151], v[208:211]\n\t"
      "v_rcp_f32_e32 v221, v221\n\t"
      "v_mfma_f32_16x16x32_bf16 v[212:215], v[92:95], v[148:151], v[212:215]\n\t"
      "global_load_dwordx4 v[148:151], v181, s[64:65] sc1\n\t"
      "s_waitcnt vmcnt(8)\n\t"
      "v_mfma_f32_16x16x32_bf16 v[200:203], v[96:99], v[152:155], v[200:203]\n\t"
      "v_rcp_f32_e32 v222, v222\n\t"
      "v_mfma_f32_16x16x32_bf16 v[204:207], v[100:103], v[152:155], v[204:207]\n\t"
      "v_rcp_f32_e32 v223, v223\n\t"
      "v_mfma_f32_16x16x32_bf16 v[208:211], v[104:107], v[152:155], v[208:211]\n\t"
      "v_mul_f32_e32 v216, v216, v220\n\t"
      "v_mfma_f32_16x16x32_bf16 v[212:215], v[108:111], v[152:155], v[212:215]\n\t"
      "global_load_dwordx4 v[152:155], v182, s[64:65] sc1\n\t"
      "v_mul_f32_e32 v217, v217, v221\n\t"
      "s_waitcnt vmcnt(8)\n\t"
      "v_mfma_f32_16x16x32_bf16 v[200:203], v[112:115], v[156:159], v[200:203]\n\t"
      "v_mul_f32_e32 v218, v218, v222\n\t"
      "v_mul_f32_e32 v219, v219, v223\n\t"
      "v_mfma_f32_16x16x32_bf16 v[204:207], v[116:119], v[156:159], v[204:207]\n\t"
      "v_cvt_pk_bf16_f32 v220, v216, v217\n\t"
      "v_cvt_pk_bf16_f32 v221, v218, v219\n\t"
      "v_mfma_f32_16x16x32_bf16 v[208:211], v[120:123], v[156:159], v[208:211]\n\t"
      "v_min_u32_e32 v220, -2, v220\n\t"
      "v_min_u32_e32 v221, -2, v221\n\t"
      "v_mfma_f32_16x16x32_bf16 v[212:215], v[124:127], v[156:159], v[212:215]\n\t"
      "global_load_dwordx4 v[156:159], v183, s[64:65] sc1\n\t"
      "global_store_dwordx2 v245, v[220:221], s[8:9] sc1\n\t"
      "s_add_u32 s8, s8, 57344\n\t"
      "s_addc_u32 s9, s9, 0\n\t"
      "v_cmp_u_f32_e32 vcc, v200, v200\n\t"
      "s_cbranch_vccnz NP5R_%=\n\t"
      "NP5B_%=:\n\t"
      "ds_write_b128 v243, v[200:203] offset:32768\n\t"
      "ds_write_b128 v164, v[204:207] offset:32768\n\t"
      "ds_write_b128 v165, v[208:211] offset:32768\n\t"
      "ds_write_b128 v166, v[212:215] offset:32768\n\t"
      "s_waitcnt lgkmcnt(0)\n\t"
      "s_barrier\n\t"
      "ds_read_b128 v[216:219], v244 offset:32768\n\t"
      "ds_read_b128 v[220:223], v244 offset:36864\n\t"
      "ds_read_b128 v[224:227], v244 offset:40960\n\t"
      "ds_read_b128 v[228:231], v244 offset:45056\n\t"
      "ds_read_b128 v[200:203], v244 offset:49152\n\t"
      "ds_read_b128 v[204:207], v244 offset:53248\n\t"
      "ds_read_b128 v[208:211], v244 offset:57344\n\t"
      "ds_read_b128 v[212:215], v244 offset:61440\n\t"
      "s_waitcnt lgkmcnt(6)\n\t"
      "v_add_f32_e32 v216, v216, v220\n\t"
      "v_add_f32_e32 v217, v217, v221\n\t"
      "v_add_f32_e32 v218, v218, v222\n\t"
      "v_add_f32_e32 v219, v219, v223\n\t"
      "s_waitcnt lgkmcnt(4)\n\t"
      "v_add_f32_e32 v224, v224, v228\n\t"
      "v_add_f32_e32 v225, v225, v229\n\t"
      "s_waitcnt vmcnt(8)\n\t"
      "v_mfma_f32_16x16x32_bf16 v[184:187], v[0:3], v[128:131], 0\n\t"
      "v_add_f32_e32 v226, v226, v230\n\t"
      "v_add_f32_e32 v227, v227, v231\n\t"
      "v_mfma_f32_16x16x32_bf16 v[188:191], v[4:7], v[128:131], 0\n\t"
      "s_waitcnt lgkmcnt(2)\n\t"
      "v_add_f32_e32 v200, v200, v204\n\t"
      "v_add_f32_e32 v201, v201, v205\n\t"
      "v_mfma_f32_16x16x32_bf16 v[192:195], v[8:11], v[128:131], 0\n\t"
      "v_add_f32_e32 v202, v202, v206\n\t"
      "v_add_f32_e32 v203, v203, v207\n\t"
      "v_mfma_f32_16x16x32_bf16 v[196:199], v[12:15], v[128:131], 0\n\t"
      "v_add_f32_e32 v216, v216, v224\n\t"
      "v_add_f32_e32 v217, v217, v225\n\t"
      "s_waitcnt vmcnt(7)\n\t"
      "v_mfma_f32_16x16x32_bf16 v[184:187], v[16:19], v[132:135], v[184:187]\n\t"
      "v_add_f32_e32 v218, v218, v226\n\t"
      "v_add_f32_e32 v219, v219, v227\n\t"
      "v_mfma_f32_16x16x32_bf16 v[188:191], v[20:23], v[132:135], v[188:191]\n\t"
      "s_waitcnt lgkmcnt(0)\n\t"
      "v_add_f32_e32 v208, v208, v212\n\t"
      "v_add_f32_e32 v209, v209, v213\n\t"
      "v_mfma_f32_16x16x32_bf16 v[192:195], v[24:27], v[132:135], v[192:195]\n\t"
      "v_add_f32_e32 v210, v210, v214\n\t"
      "v_add_f32_e32 v211, v211, v215\n\t"
      "v_mfma_f32_16x16x32_bf16 v[196:199], v[28:31], v[132:135], v[196:199]\n\t"
      "v_add_f32_e32 v200, v200, v208\n\t"
      "v_add_f32_e32 v201, v201, v209\n\t"
      "s_waitcnt vmcnt(6)\n\t"
      "v_mfma_f32_16x16x32_bf16 v[184:187], v[32:35], v[136:139], v[184:187]\n\t"
      "v_add_f32_e32 v202, v202, v210\n\t"
      "v_add_f32_e32 v203, v203, v211\n\t"
      "v_mfma_f32_16x16x32_bf16 v[188:191], v[36:39], v[136:139], v[188:191]\n\t"
      "v_add_f32_e32 v216, v216, v200\n\t"
      "v_add_f32_e32 v217, v217, v201\n\t"
      "v_mfma_f32_16x16x32_bf16 v[192:195], v[40:43], v[136:139], v[192:195]\n\t"
      "v_add_f32_e32 v218, v218, v202\n\t"
      "v_add_f32_e32 v219, v219, v203\n\t"
      "v_mfma_f32_16x16x32_bf16 v[196:199], v[44:47], v[136:139], v[196:199]\n\t"
      "v_mul_f32_e32 v220, v216, v216\n\t"
      "v_mul_f32_e32 v221, v217, v217\n\t"
      "s_waitcnt vmcnt(5)\n\t"
      "v_mfma_f32_16x16x32_bf16 v[184:187], v[48:51], v[140:143], v[184:187]\n\t"
      "v_mul_f32_e32 v222, v218, v218\n\t"
      "v_mul_f32_e32 v223, v219, v219\n\t"
      "v_mfma_f32_16x16x32_bf16 v[188:191], v[52:55], v[140:143], v[188:191]\n\t"
      "v_fmaak_f32 v220, v235, v220, 0xc0135761\n\t"
      "v_fmaak_f32 v221, v235, v221, 0xc0135761\n\t"
      "v_mfma_f32_16x16x32_bf16 v[192:195], v[56:59], v[140:143], v[192:195]\n\t"
      "v_fmaak_f32 v222, v235, v222, 0xc0135761\n\t"
      "v_fmaak_f32 v223, v235, v223, 0xc0135761\n\t"
      "v_mfma_f32_16x16x32_bf16 v[196:199], v[60:63], v[140:143], v[196:199]\n\t"
      "v_mul_f32_e32 v220, v216, v220\n\t"
      "v_mul_f32_e32 v221, v217, v221\n\t"
      "s_waitcnt vmcnt(4)\n\t"
      "v_mfma_f32_16x16x32_bf16 v[184:187], v[64:67], v[144:147], v[184:187]\n\t"
      "v_mul_f32_e32 v222, v218, v222\n\t"
      "v_mul_f32_e32 v223, v219, v223\n\t"
      "v_mfma_f32_16x16x32_bf16 v[188:191], v[68:71], v[144:147], v[188:191]\n\t"
      "v_exp_f32_e32 v220, v220\n\t"
      "v_mfma_f32_16x16x32_bf16 v[192:195], v[72:75], v[144:147], v[192:195]\n\t"
      "v_exp_f32_e32 v221, v221\n\t"
      "v_mfma_f32_16x16x32_bf16 v[196:199], v[76:79], v[144:147], v[196:199]\n\t"
      "v_exp_f32_e32 v222, v222\n\t"
      "s_waitcnt vmcnt(3)\n\t"
      "v_mfma_f32_16x16x32_bf16 v[184:187], v[80:83], v[148:151], v[184:187]\n\t"
      "v_exp_f32_e32 v223, v223\n\t"
      "v_mfma_f32_16x16x32_bf16 v[188:191], v[84:87], v[148:151], v[188:191]\n\t"
      "v_add_f32_e32 v220, 1.0, v220\n\t"
      "v_add_f32_e32 v221, 1.0, v221\n\t"
      "v_mfma_f32_16x16x32_bf16 v[192:195], v[88:91], v[148:151], v[192:195]\n\t"
      "v_add_f32_e32 v222, 1.0, v222\n\t"
      "v_add_f32_e32 v223, 1.0, v223\n\t"
      "v_mfma_f32_16x16x32_bf16 v[196:199], v[92:95], v[148:151], v[196:199]\n\t"
      "v_rcp_f32_e32 v220, v220\n\t"
      "s_waitcnt vmcnt(2)\n\t"
      "v_mfma_f32_16x16x32_bf16 v[184:187], v[96:99], v[152:155], v[184:187]\n\t"
      "v_rcp_f32_e32 v221, v221\n\t"
      "v_mfma_f32_16x16x32_bf16 v[188:191], v[100:103], v[152:155], v[188:191]\n\t"
      "v_rcp_f32_e32 v222, v222\n\t"
      "v_mfma_f32_16x16x32_bf16 v[192:195], v[104:107], v[152:155], v[192:195]\n\t"
      "v_rcp_f32_e32 v223, v223\n\t"
      "v_mfma_f32_16x16x32_bf16 v[196:199], v[108:111], v[152:155], v[196:199]\n\t"
      "v_mul_f32_e32 v216, v216, v220\n\t"
      "s_waitcnt vmcnt(1)\n\t"
      "v_mfma_f32_16x16x32_bf16 v[184:187], v[112:115], v[156:159], v[184:187]\n\t"
      "v_mul_f32_e32 v217, v217, v221\n\t"
      "v_mul_f32_e32 v218, v218, v222\n\t"
      "v_mfma_f32_16x16x32_bf16 v[188:191], v[116:119], v[156:159], v[188:191]\n\t"
      "v_mul_f32_e32 v219, v219, v223\n\t"
      "v_cvt_pk_bf16_f32 v220, v216, v217\n\t"
      "v_mfma_f32_16x16x32_bf16 v[192:195], v[120:123], v[156:159], v[192:195]\n\t"
      "v_cvt_pk_bf16_f32 v221, v218, v219\n\t"
      "v_min_u32_e32 v220, -2, v220\n\t"
      "v_mfma_f32_16x16x32_bf16 v[196:199], v[124:127], v[156:159], v[196:199]\n\t"
      "v_min_u32_e32 v221, -2, v221\n\t"
      "global_store_dwordx2 v245, v[220:221], s[8:9] sc1\n\t"
      "s_add_u32 s8, s8, 57344\n\t"
      "s_addc_u32 s9, s9, 0\n\t"
      "v_cmp_u_f32_e32 vcc, v184, v184\n\t"
      "s_cbranch_vccnz NP6R_%=\n\t"
      "NP6B_%=:\n\t"
      "ds_write_b128 v243, v[184:187]\n\t"
      "ds_write_b128 v164, v[188:191]\n\t"
      "ds_write_b128 v165, v[192:195]\n\t"
      "ds_write_b128 v166, v[196:199]\n\t"
      "s_waitcnt lgkmcnt(0)\n\t"
      "s_barrier\n\t"
      "ds_read_b128 v[216:219], v244\n\t"
      "ds_read_b128 v[220:223], v244 offset:4096\n\t"
      "ds_read_b128 v[224:227], v244 offset:8192\n\t"
      "ds_read_b128 v[228:231], v244 offset:12288\n\t"
      "ds_read_b128 v[184:187], v244 offset:16384\n\t"
      "ds_read_b128 v[188:191], v244 offset:20480\n\t"
      "ds_read_b128 v[192:195], v244 offset:24576\n\t"
      "ds_read_b128 v[196:199], v244 offset:28672\n\t"
      "s_waitcnt lgkmcnt(6)\n\t"
      "v_add_f32_e32 v216, v216, v220\n\t"
      "v_add_f32_e32 v217, v217, v221\n\t"
      "v_add_f32_e32 v218, v218, v222\n\t"
      "v_add_f32_e32 v219, v219, v223\n\t"
      "s_waitcnt lgkmcnt(4)\n\t"
      "v_add_f32_e32 v224, v224, v228\n\t"
      "v_add_f32_e32 v225, v225, v229\n\t"
      "v_add_f32_e32 v226, v226, v230\n\t"
      "v_add_f32_e32 v227, v227, v231\n\t"
      "s_waitcnt lgkmcnt(2)\n\t"
      "v_add_f32_e32 v184, v184, v188\n\t"
      "v_add_f32_e32 v185, v185, v189\n\t"
      "v_add_f32_e32 v186, v186, v190\n\t"
      "v_add_f32_e32 v187, v187, v191\n\t"
      "v_add_f32_e32 v216, v216, v224\n\t"
      "v_add_f32_e32 v217, v217, v225\n\t"
      "v_add_f32_e32 v218, v218, v226\n\t"
      "v_add_f32_e32 v219, v219, v227\n\t"
      "s_waitcnt lgkmcnt(0)\n\t"
      "v_add_f32_e32 v192, v192, v196\n\t"
      "v_add_f32_e32 v193, v193, v197\n\t"
      "v_add_f32_e32 v194, v194, v198\n\t"
      "v_add_f32_e32 v195, v195, v199\n\t"
      "v_add_f32_e32 v184, v184, v192\n\t"
      "v_add_f32_e32 v185, v185, v193\n\t"
      "v_add_f32_e32 v186, v186, v194\n\t"
      "v_add_f32_e32 v187, v187, v195\n\t"
      "v_add_f32_e32 v216, v216, v184\n\t"
      "v_add_f32_e32 v217, v217, v185\n\t"
      "v_add_f32_e32 v218, v218, v186\n\t"
      "v_add_f32_e32 v219, v219, v187\n\t"
      "v_mul_f32_e32 v220, v216, v216\n\t"
      "v_mul_f32_e32 v221, v217, v217\n\t"
      "v_mul_f32_e32 v222, v218, v218\n\t"
      "v_mul_f32_e32 v223, v219, v219\n\t"
      "v_fmaak_f32 v220, v235, v220, 0xc0135761\n\t"
      "v_fmaak_f32 v221, v235, v221, 0xc0135761\n\t"
      "v_fmaak_f32 v222, v235, v222, 0xc0135761\n\t"
      "v_fmaak_f32 v223, v235, v223, 0xc0135761\n\t"
      "v_mul_f32_e32 v220, v216, v220\n\t"
      "v_mul_f32_e32 v221, v217, v221\n\t"
      "v_mul_f32_e32 v222, v218, v222\n\t"
      "v_mul_f32_e32 v223, v219, v223\n\t"
      "v_exp_f32_e32 v220, v220\n\t"
      "v_exp_f32_e32 v221, v221\n\t"
      "v_exp_f32_e32 v222, v222\n\t"
      "v_exp_f32_e32 v223, v223\n\t"
      "v_add_f32_e32 v220, 1.0, v220\n\t"
      "v_add_f32_e32 v221, 1.0, v221\n\t"
      "v_add_f32_e32 v222, 1.0, v222\n\t"
      "v_add_f32_e32 v223, 1.0, v223\n\t"
      "v_rcp_f32_e32 v220, v220\n\t"
      "v_rcp_f32_e32 v221, v221\n\t"
      "v_rcp_f32_e32 v222, v222\n\t"
      "v_rcp_f32_e32 v223, v223\n\t"
      "v_mul_f32_e32 v216, v216, v220\n\t"
      "v_mul_f32_e32 v217, v217, v221\n\t"
      "v_mul_f32_e32 v218, v218, v222\n\t"
      "v_mul_f32_e32 v219, v219, v223\n\t"
      "v_cvt_pk_bf16_f32 v220, v216, v217\n\t"
      "v_cvt_pk_bf16_f32 v221, v218, v219\n\t"
      "v_min_u32_e32 v220, -2, v220\n\t"
      "v_min_u32_e32 v221, -2, v221\n\t"
      "s_cmp_eq_u32 s17, 0\n\t"
      "s_cbranch_scc0 HI6_%=\n\t"
      "s_mov_b64 exec, 0xff\n\t"
      "global_store_dwordx2 v247, v[232:233], s[12:13] sc1\n\t"
      "s_mov_b64 exec, -1\n\t"
      "HI6_%=:\n\t"
      "global_store_dwordx2 v245, v[220:221], s[8:9] sc1\n\t"
      "GX_%=:\n\t"
      "AEND_%=:\n\t"
      "s_endpgm\n\t"
      ".p2align 6\n\t"
      "GQ_%=:\n\t"
      "s_waitcnt vmcnt(7)\n\t"
      "v_max3_u32 v234, v128, v129, v130\n\t"
      "v_max_u32_e32 v234, v234, v131\n\t"
      "v_cmp_eq_u32_e32 vcc, -1, v234\n\t"
      "s_cbranch_vccnz FQ0R_%=\n\t"
      "FQ0B_%=:\n\t"
      "v_mfma_f32_16x16x32_bf16 v[184:187], v[0:3], v[128:131], v[236:239]\n\t"
      "v_mfma_f32_16x16x32_bf16 v[188:191], v[4:7], v[128:131], 0\n\t"
      "v_mfma_f32_16x16x32_bf16 v[192:195], v[8:11], v[128:131], 0\n\t"
      "v_mfma_f32_16x16x32_bf16 v[196:199], v[12:15], v[128:131], 0\n\t"
      "global_load_dwordx4 v[128:131], v176, s[54:55] sc1\n\t"
      "s_waitcnt vmcnt(7)\n\t"
      "v_max3_u32 v234, v132, v133, v134\n\t"
      "v_max_u32_e32 v234, v234, v135\n\t"
      "v_cmp_eq_u32_e32 vcc, -1, v234\n\t"
      "s_cbranch_vccnz FQ1R_%=\n\t"
      "FQ1B_%=:\n\t"
      "v_mfma_f32_16x16x32_bf16 v[184:187], v[16:19], v[132:135], v[184:187]\n\t"
      "v_mfma_f32_16x16x32_bf16 v[188:191], v[20:23], v[132:135], v[188:191]\n\t"
      "v_mfma_f32_16x16x32_bf16 v[192:195], v[24:27], v[132:135], v[192:195]\n\t"
      "v_mfma_f32_16x16x32_bf16 v[196:199], v[28:31], v[132:135], v[196:199]\n\t"
      "global_load_dwordx4 v[132:135], v177, s[54:55] sc1\n\t"
      "s_waitcnt vmcnt(7)\n\t"
      "v_max3_u32 v234, v136, v137, v138\n\t"
      "v_max_u32_e32 v234, v234, v139\n\t"
      "v_cmp_eq_u32_e32 vcc, -1, v234\n\t"
      "s_cbranch_vccnz FQ2R_%=\n\t"
      "FQ2B_%=:\n\t"
      "v_mfma_f32_16x16x32_bf16 v[184:187], v[32:35], v[136:139], v[184:187]\n\t"
      "v_mfma_f32_16x16x32_bf16 v[188:191], v[36:39], v[136:139], v[188:191]\n\t"
      "v_mfma_f32_16x16x32_bf16 v[192:195], v[40:43], v[136:139], v[192:195]\n\t"
      "v_mfma_f32_16x16x32_bf16 v[196:199], v[44:47], v[136:139], v[196:199]\n\t"
      "global_load_dwordx4 v[136:139], v178, s[54:55] sc1\n\t"
      "s_waitcnt vmcnt(7)\n\t"
      "v_max3_u32 v234, v140, v141, v142\n\t"
      "v_max_u32_e32 v234, v234, v143\n\t"
      "v_cmp_eq_u32_e32 vcc, -1, v234\n\t"
      "s_cbranch_vccnz FQ3R_%=\n\t"
      "FQ3B_%=:\n\t"
      "v_mfma_f32_16x16x32_bf16 v[184:187], v[48:51], v[140:143], v[184:187]\n\t"
      "v_mfma_f32_16x16x32_bf16 v[188:191], v[52:55], v[140:143], v[188:191]\n\t"
      "v_mfma_f32_16x16x32_bf16 v[192:195], v[56:59], v[140:143], v[192:195]\n\t"
      "v_mfma_f32_16x16x32_bf16 v[196:199], v[60:63], v[140:143], v[196:199]\n\t"
      "global_load_dwordx4 v[140:143], v179, s[54:55] sc1\n\t"
      "s_waitcnt vmcnt(7)\n\t"
      "v_max3_u32 v234, v144, v145, v146\n\t"
      "v_max_u32_e32 v234, v234, v147\n\t"
      "v_cmp_eq_u32_e32 vcc, -1, v234\n\t"
      "s_cbranch_vccnz FQ4R_%=\n\t"
      "FQ4B_%=:\n\t"
      "v_mfma_f32_16x16x32_bf16 v[184:187], v[64:67], v[144:147], v[184:187]\n\t"
      "v_mfma_f32_16x16x32_bf16 v[188:191], v[68:71], v[144:147], v[188:191]\n\t"
      "v_mfma_f32_16x16x32_bf16 v[192:195], v[72:75], v[144:147], v[192:195]\n\t"
      "v_mfma_f32_16x16x32_bf16 v[196:199], v[76:79], v[144:147], v[196:199]\n\t"
      "global_load_dwordx4 v[144:147], v180, s[54:55] sc1\n\t"
      "s_waitcnt vmcnt(7)\n\t"
      "v_max3_u32 v234, v148, v149, v150\n\t"
      "v_max_u32_e32 v234, v234, v151\n\t"
      "v_cmp_eq_u32_e32 vcc, -1, v234\n\t"
      "s_cbranch_vccnz FQ5R_%=\n\t"
      "FQ5B_%=:\n\t"
      "v_mfma_f32_16x16x32_bf16 v[184:187], v[80:83], v[148:151], v[184:187]\n\t"
      "v_mfma_f32_16x16x32_bf16 v[188:191], v[84:87], v[148:151], v[188:191]\n\t"
      "v_mfma_f32_16x16x32_bf16 v[192:195], v[88:91], v[148:151], v[192:195]\n\t"
      "v_mfma_f32_16x16x32_bf16 v[196:199], v[92:95], v[148:151], v[196:199]\n\t"
      "global_load_dwordx4 v[148:151], v181, s[54:55] sc1\n\t"
      "s_waitcnt vmcnt(7)\n\t"
      "v_max3_u32 v234, v152, v153, v154\n\t"
      "v_max_u32_e32 v234, v234, v155\n\t"
      "v_cmp_eq_u32_e32 vcc, -1, v234\n\t"
      "s_cbranch_vccnz FQ6R_%=\n\t"
      "FQ6B_%=:\n\t"
      "v_mfma_f32_16x16x32_bf16 v[184:187], v[96:99], v[152:155], v[184:187]\n\t"
      "v_mfma_f32_16x16x32_bf16 v[188:191], v[100:103], v[152:155], v[188:191]\n\t"
      "v_mfma_f32_16x16x32_bf16 v[192:195], v[104:107], v[152:155], v[192:195]\n\t"
      "v_mfma_f32_16x16x32_bf16 v[196:199], v[108:111], v[152:155], v[196:199]\n\t"
      "global_load_dwordx4 v[152:155], v182, s[54:55] sc1\n\t"
      "s_waitcnt vmcnt(7)\n\t"
      "v_max3_u32 v234, v156, v157, v158\n\t"
      "v_max_u32_e32 v234, v234, v159\n\t"
      "v_cmp_eq_u32_e32 vcc, -1, v234\n\t"
      "s_cbranch_vccnz FQ7R_%=\n\t"
      "FQ7B_%=:\n\t"
      "v_mfma_f32_16x16x32_bf16 v[184:187], v[112:115], v[156:159], v[184:187]\n\t"
      "v_mfma_f32_16x16x32_bf16 v[188:191], v[116:119], v[156:159], v[188:191]\n\t"
      "v_mfma_f32_16x16x32_bf16 v[192:195], v[120:123], v[156:159], v[192:195]\n\t"
      "v_mfma_f32_16x16x32_bf16 v[196:199], v[124:127], v[156:159], v[196:199]\n\t"
      "global_load_dwordx4 v[156:159], v183, s[54:55] sc1\n\t"
      "s_nop 3\n\t"
      "ds_write_b128 v243, v[184:187]\n\t"
      "ds_write_b128 v164, v[188:191]\n\t"
      "ds_write_b128 v165, v[192:195]\n\t"
      "ds_write_b128 v166, v[196:199]\n\t"
      "s_waitcnt vmcnt(7)\n\t"
      "v_mfma_f32_16x16x32_bf16 v[200:203], v[0:3], v[128:131], v[236:239]\n\t"
      "v_mfma_f32_16x16x32_bf16 v[204:207], v[4:7], v[128:131], 0\n\t"
      "v_mfma_f32_16x16x32_bf16 v[208:211], v[8:11], v[128:131], 0\n\t"
      "v_mfma_f32_16x16x32_bf16 v[212:215], v[12:15], v[128:131], 0\n\t"
      "global_load_dwordx4 v[128:131], v176, s[56:57] sc1\n\t"
      "s_waitcnt vmcnt(7)\n\t"
      "v_mfma_f32_16x16x32_bf16 v[200:203], v[16:19], v[132:135], v[200:203]\n\t"
      "v_mfma_f32_16x16x32_bf16 v[204:207], v[20:23], v[132:135], v[204:207]\n\t"
      "v_mfma_f32_16x16x32_bf16 v[208:211], v[24:27], v[132:135], v[208:211]\n\t"
      "v_mfma_f32_16x16x32_bf16 v[212:215], v[28:31], v[132:135], v[212:215]\n\t"
      "global_load_dwordx4 v[132:135], v177, s[56:57] sc1\n\t"
      "s_waitcnt vmcnt(7)\n\t"
      "v_mfma_f32_16x16x32_bf16 v[200:203], v[32:35], v[136:139], v[200:203]\n\t"
      "v_mfma_f32_16x16x32_bf16 v[204:207], v[36:39], v[136:139], v[204:207]\n\t"
      "v_mfma_f32_16x16x32_bf16 v[208:211], v[40:43], v[136:139], v[208:211]\n\t"
      "v_mfma_f32_16x16x32_bf16 v[212:215], v[44:47], v[136:139], v[212:215]\n\t"
      "global_load_dwordx4 v[136:139], v178, s[56:57] sc1\n\t"
      "s_waitcnt vmcnt(7)\n\t"
      "v_mfma_f32_16x16x32_bf16 v[200:203], v[48:51], v[140:143], v[200:203]\n\t"
      "v_mfma_f32_16x16x32_bf16 v[204:207], v[52:55], v[140:143], v[204:207]\n\t"
      "v_mfma_f32_16x16x32_bf16 v[208:211], v[56:59], v[140:143], v[208:211]\n\t"
      "v_mfma_f32_16x16x32_bf16 v[212:215], v[60:63], v[140:143], v[212:215]\n\t"
      "global_load_dwordx4 v[140:143], v179, s[56:57] sc1\n\t"
      "s_waitcnt vmcnt(7)\n\t"
      "v_mfma_f32_16x16x32_bf16 v[200:203], v[64:67], v[144:147], v[200:203]\n\t"
      "v_mfma_f32_16x16x32_bf16 v[204:207], v[68:71], v[144:147], v[204:207]\n\t"
      "v_mfma_f32_16x16x32_bf16 v[208:211], v[72:75], v[144:147], v[208:211]\n\t"
      "v_mfma_f32_16x16x32_bf16 v[212:215], v[76:79], v[144:147], v[212:215]\n\t"
      "global_load_dwordx4 v[144:147], v180, s[56:57] sc1\n\t"
      "s_waitcnt vmcnt(7)\n\t"
      "v_mfma_f32_16x16x32_bf16 v[200:203], v[80:83], v[148:151], v[200:203]\n\t"
      "v_mfma_f32_16x16x32_bf16 v[204:207], v[84:87], v[148:151], v[204:207]\n\t"
      "v_mfma_f32_16x16x32_bf16 v[208:211], v[88:91], v[148:151], v[208:211]\n\t"
      "v_mfma_f32_16x16x32_bf16 v[212:215], v[92:95], v[148:151], v[212:215]\n\t"
      "global_load_dwordx4 v[148:151], v181, s[56:57] sc1\n\t"
      "s_waitcnt lgkmcnt(0)\n\t"
      "s_barrier\n\t"
      "s_setprio 2\n\t"
      "s_waitcnt vmcnt(7)\n\t"
      "v_mfma_f32_16x16x32_bf16 v[200:203], v[96:99], v[152:155], v[200:203]\n\t"
      "v_mfma_f32_16x16x32_bf16 v[204:207], v[100:103], v[152:155], v[204:207]\n\t"
      "v_mfma_f32_16x16x32_bf16 v[208:211], v[104:107], v[152:155], v[208:211]\n\t"
      "v_mfma_f32_16x16x32_bf16 v[212:215], v[108:111], v[152:155], v[212:215]\n\t"
      "global_load_dwordx4 v[152:155], v182, s[56:57] sc1\n\t"
      "s_waitcnt vmcnt(7)\n\t"
      "v_mfma_f32_16x16x32_bf16 v[200:203], v[112:115], v[156:159], v[200:203]\n\t"
      "v_mfma_f32_16x16x32_bf16 v[204:207], v[116:119], v[156:159], v[204:207]\n\t"
      "v_mfma_f32_16x16x32_bf16 v[208:211], v[120:123], v[156:159], v[208:211]\n\t"
      "v_mfma_f32_16x16x32_bf16 v[212:215], v[124:127], v[156:159], v[212:215]\n\t"
      "global_load_dwordx4 v[156:159], v183, s[56:57] sc1\n\t"
      "s_setprio 0\n\t"
      "s_nop 2\n\t"
      "v_cmp_u_f32_e32 vcc, v200, v200\n\t"
      "s_cbranch_vccnz NQ1R_%=\n\t"
      "NQ1B_%=:\n\t"
      "ds_write_b128 v243, v[200:203] offset:32768\n\t"
      "ds_write_b128 v164, v[204:207] offset:32768\n\t"
      "ds_write_b128 v165, v[208:211] offset:32768\n\t"
      "ds_write_b128 v166, v[212:215] offset:32768\n\t"
      "s_waitcnt vmcnt(7)\n\t"
      "v_mfma_f32_16x16x32_bf16 v[184:187], v[0:3], v[128:131], v[236:239]\n\t"
      "v_mfma_f32_16x16x32_bf16 v[188:191], v[4:7], v[128:131], 0\n\t"
      "v_mfma_f32_16x16x32_bf16 v[192:195], v[8:11], v[128:131], 0\n\t"
      "v_mfma_f32_16x16x32_bf16 v[196:199], v[12:15], v[128:131], 0\n\t"
      "global_load_dwordx4 v[128:131], v176, s[58:59] sc1\n\t"
      "s_waitcnt vmcnt(7)\n\t"
      "v_mfma_f32_16x16x32_bf16 v[184:187], v[16:19], v[132:135], v[184:187]\n\t"
      "v_mfma_f32_16x16x32_bf16 v[188:191], v[20:23], v[132:135], v[188:191]\n\t"
      "v_mfma_f32_16x16x32_bf16 v[192:195], v[24:27], v[132:135], v[192:195]\n\t"
      "v_mfma_f32_16x16x32_bf16 v[196:199], v[28:31], v[132:135], v[196:199]\n\t"
      "global_load_dwordx4 v[132:135], v177, s[58:59] sc1\n\t"
      "s_waitcnt vmcnt(7)\n\t"
      "v_mfma_f32_16x16x32_bf16 v[184:187], v[32:35], v[136:139], v[184:187]\n\t"
      "v_mfma_f32_16x16x32_bf16 v[188:191], v[36:39], v[136:139], v[188:191]\n\t"
      "v_mfma_f32_16x16x32_bf16 v[192:195], v[40:43], v[136:139], v[192:195]\n\t"
      "v_mfma_f32_16x16x32_bf16 v[196:199], v[44:47], v[136:139], v[196:199]\n\t"
      "global_load_dwordx4 v[136:139], v178, s[58:59] sc1\n\t"
      "s_waitcnt vmcnt(7)\n\t"
      "v_mfma_f32_16x16x32_bf16 v[184:187], v[48:51], v[140:143], v[184:187]\n\t"
      "v_mfma_f32_16x16x32_bf16 v[188:191], v[52:55], v[140:143], v[188:191]\n\t"
      "v_mfma_f32_16x16x32_bf16 v[192:195], v[56:59], v[140:143], v[192:195]\n\t"
      "v_mfma_f32_16x16x32_bf16 v[196:199], v[60:63], v[140:143], v[196:199]\n\t"
      "global_load_dwordx4 v[140:143], v179, s[58:59] sc1\n\t"
      "s_waitcnt vmcnt(7)\n\t"
      "v_mfma_f32_16x16x32_bf16 v[184:187], v[64:67], v[144:147], v[184:187]\n\t"
      "v_mfma_f32_16x16x32_bf16 v[188:191], v[68:71], v[144:147], v[188:191]\n\t"
      "v_mfma_f32_16x16x32_bf16 v[192:195], v[72:75], v[144:147], v[192:195]\n\t"
      "v_mfma_f32_16x16x32_bf16 v[196:199], v[76:79], v[144:147], v[196:199]\n\t"
      "global_load_dwordx4 v[144:147], v180, s[58:59] sc1\n\t"
      "s_waitcnt vmcnt(7)\n\t"
      "v_mfma_f32_16x16x32_bf16 v[184:187], v[80:83], v[148:151], v[184:187]\n\t"
      "v_mfma_f32_16x16x32_bf16 v[188:191], v[84:87], v[148:151], v[188:191]\n\t"
      "v_mfma_f32_16x16x32_bf16 v[192:195], v[88:91], v[148:151], v[192:195]\n\t"
      "v_mfma_f32_16x16x32_bf16 v[196:199], v[92:95], v[148:151], v[196:199]\n\t"
      "global_load_dwordx4 v[148:151], v181, s[58:59] sc1\n\t"
      "s_waitcnt lgkmcnt(0)\n\t"
      "s_barrier\n\t"
      "s_setprio 2\n\t"
      "s_waitcnt vmcnt(7)\n\t"
      "v_mfma_f32_16x16x32_bf16 v[184:187], v[96:99], v[152:155], v[184:187]\n\t"
      "v_mfma_f32_16x16x32_bf16 v[188:191], v[100:103], v[152:155], v[188:191]\n\t"
      "v_mfma_f32_16x16x32_bf16 v[192:195], v[104:107], v[152:155], v[192:195]\n\t"
      "v_mfma_f32_16x16x32_bf16 v[196:199], v[108:111], v[152:155], v[196:199]\n\t"
      "global_load_dwordx4 v[152:155], v182, s[58:59] sc1\n\t"
      "s_waitcnt vmcnt(7)\n\t"
      "v_mfma_f32_16x16x32_bf16 v[184:187], v[112:115], v[156:159], v[184:187]\n\t"
      "v_mfma_f32_16x16x32_bf16 v[188:191], v[116:119], v[156:159], v[188:191]\n\t"
      "v_mfma_f32_16x16x32_bf16 v[192:195], v[120:123], v[156:159], v[192:195]\n\t"
      "v_mfma_f32_16x16x32_bf16 v[196:199], v[124:127], v[156:159], v[196:199]\n\t"
      "global_load_dwordx4 v[156:159], v183, s[58:59] sc1\n\t"
      "s_setprio 0\n\t"
      "s_nop 2\n\t"
      "v_cmp_u_f32_e32 vcc, v184, v184\n\t"
      "s_cbranch_vccnz NQ2R_%=\n\t"
      "NQ2B_%=:\n\t"
      "ds_write_b128 v243, v[184:187]\n\t"
      "ds_write_b128 v164, v[188:191]\n\t"
      "ds_write_b128 v165, v[192:195]\n\t"
      "ds_write_b128 v166, v[196:199]\n\t"
      "s_waitcnt vmcnt(7)\n\t"
      "v_mfma_f32_16x16x32_bf16 v[200:203], v[0:3], v[128:131], v[236:239]\n\t"
      "v_mfma_f32_16x16x32_bf16 v[204:207], v[4:7], v[128:131], 0\n\t"
      "v_mfma_f32_16x16x32_bf16 v[208:211], v[8:11], v[128:131], 0\n\t"
      "v_mfma_f32_16x16x32_bf16 v[212:215], v[12:15], v[128:131], 0\n\t"
      "global_load_dwordx4 v[128:131], v176, s[60:61] sc1\n\t"
      "s_waitcnt vmcnt(7)\n\t"
      "v_mfma_f32_16x16x32_bf16 v[200:203], v[16:19], v[132:135], v[200:203]\n\t"
      "v_mfma_f32_16x16x32_bf16 v[204:207], v[20:23], v[132:135], v[204:207]\n\t"
      "v_mfma_f32_16x16x32_bf16 v[208:211], v[24:27], v[132:135], v[208:211]\n\t"
      "v_mfma_f32_16x16x32_bf16 v[212:215], v[28:31], v[132:135], v[212:215]\n\t"
      "global_load_dwordx4 v[132:135], v177, s[60:61] sc1\n\t"
      "s_waitcnt vmcnt(7)\n\t"
      "v_mfma_f32_16x16x32_bf16 v[200:203], v[32:35], v[136:139], v[200:203]\n\t"
      "v_mfma_f32_16x16x32_bf16 v[204:207], v[36:39], v[136:139], v[204:207]\n\t"
      "v_mfma_f32_16x16x32_bf16 v[208:211], v[40:43], v[136:139], v[208:211]\n\t"
      "v_mfma_f32_16x16x32_bf16 v[212:215], v[44:47], v[136:139], v[212:215]\n\t"
      "global_load_dwordx4 v[136:139], v178, s[60:61] sc1\n\t"
      "s_waitcnt vmcnt(7)\n\t"
      "v_mfma_f32_16x16x32_bf16 v[200:203], v[48:51], v[140:143], v[200:203]\n\t"
      "v_mfma_f32_16x16x32_bf16 v[204:207], v[52:55], v[140:143], v[204:207]\n\t"
      "v_mfma_f32_16x16x32_bf16 v[208:211], v[56:59], v[140:143], v[208:211]\n\t"
      "v_mfma_f32_16x16x32_bf16 v[212:215], v[60:63], v[140:143], v[212:215]\n\t"
      "global_load_dwordx4 v[140:143], v179, s[60:61] sc1\n\t"
      "s_waitcnt vmcnt(7)\n\t"
      "v_mfma_f32_16x16x32_bf16 v[200:203], v[64:67], v[144:147], v[200:203]\n\t"
      "v_mfma_f32_16x16x32_bf16 v[204:207], v[68:71], v[144:147], v[204:207]\n\t"
      "v_mfma_f32_16x16x32_bf16 v[208:211], v[72:75], v[144:147], v[208:211]\n\t"
      "v_mfma_f32_16x16x32_bf16 v[212:215], v[76:79], v[144:147], v[212:215]\n\t"
      "global_load_dwordx4 v[144:147], v180, s[60:61] sc1\n\t"
      "s_waitcnt vmcnt(7)\n\t"
      "v_mfma_f32_16x16x32_bf16 v[200:203], v[80:83], v[148:151], v[200:203]\n\t"
      "v_mfma_f32_16x16x32_bf16 v[204:207], v[84:87], v[148:151], v[204:207]\n\t"
      "v_mfma_f32_16x16x32_bf16 v[208:211], v[88:91], v[148:151], v[208:211]\n\t"
      "v_mfma_f32_16x16x32_bf16 v[212:215], v[92:95], v[148:151], v[212:215]\n\t"
      "global_load_dwordx4 v[148:151], v181, s[60:61] sc1\n\t"
      "s_waitcnt lgkmcnt(0)\n\t"
      "s_barrier\n\t"
      "s_setprio 2\n\t"
      "s_waitcnt vmcnt(7)\n\t"
      "v_mfma_f32_16x16x32_bf16 v[200:203], v[96:99], v[152:155], v[200:203]\n\t"
      "v_mfma_f32_16x16x32_bf16 v[204:207], v[100:103], v[152:155], v[204:207]\n\t"
      "v_mfma_f32_16x16x32_bf16 v[208:211], v[104:107], v[152:155], v[208:211]\n\t"
      "v_mfma_f32_16x16x32_bf16 v[212:215], v[108:111], v[152:155], v[212:215]\n\t"
      "global_load_dwordx4 v[152:155], v182, s[60:61] sc1\n\t"
      "s_waitcnt vmcnt(7)\n\t"
      "v_mfma_f32_16x16x32_bf16 v[200:203], v[112:115], v[156:159], v[200:203]\n\t"
      "v_mfma_f32_16x16x32_bf16 v[204:207], v[116:119], v[156:159], v[204:207]\n\t"
      "v_mfma_f32_16x16x32_bf16 v[208:211], v[120:123], v[156:159], v[208:211]\n\t"
      "v_mfma_f32_16x16x32_bf16 v[212:215], v[124:127], v[156:159], v[212:215]\n\t"
      "global_load_dwordx4 v[156:159], v183, s[60:61] sc1\n\t"
      "s_setprio 0\n\t"
      "s_nop 2\n\t"
      "v_cmp_u_f32_e32 vcc, v200, v200\n\t"
      "s_cbranch_vccnz NQ3R_%=\n\t"
      "NQ3B_%=:\n\t"
      "ds_write_b128 v243, v[200:203] offset:32768\n\t"
      "ds_write_b128 v164, v[204:207] offset:32768\n\t"
      "ds_write_b128 v165, v[208:211] offset:32768\n\t"
      "ds_write_b128 v166, v[212:215] offset:32768\n\t"
      "s_waitcnt vmcnt(7)\n\t"
      "v_mfma_f32_16x16x32_bf16 v[184:187], v[0:3], v[128:131], v[236:239]\n\t"
      "v_mfma_f32_16x16x32_bf16 v[188:191], v[4:7], v[128:131], 0\n\t"
      "v_mfma_f32_16x16x32_bf16 v[192:195], v[8:11], v[128:131], 0\n\t"
      "v_mfma_f32_16x16x32_bf16 v[196:199], v[12:15], v[128:131], 0\n\t"
      "global_load_dwordx4 v[128:131], v176, s[62:63] sc1\n\t"
      "s_waitcnt vmcnt(7)\n\t"
      "v_mfma_f32_16x16x32_bf16 v[184:187], v[16:19], v[132:135], v[184:187]\n\t"
      "v_mfma_f32_16x16x32_bf16 v[188:191], v[20:23], v[132:135], v[188:191]\n\t"
      "v_mfma_f32_16x16x32_bf16 v[192:195], v[24:27], v[132:135], v[192:195]\n\t"
      "v_mfma_f32_16x16x32_bf16 v[196:199], v[28:31], v[132:135], v[196:199]\n\t"
      "global_load_dwordx4 v[132:135], v177, s[62:63] sc1\n\t"
      "s_waitcnt vmcnt(7)\n\t"
      "v_mfma_f32_16x16x32_bf16 v[184:187], v[32:35], v[136:139], v[184:187]\n\t"
      "v_mfma_f32_16x16x32_bf16 v[188:191], v[36:39], v[136:139], v[188:191]\n\t"
      "v_mfma_f32_16x16x32_bf16 v[192:195], v[40:43], v[136:139], v[192:195]\n\t"
      "v_mfma_f32_16x16x32_bf16 v[196:199], v[44:47], v[136:139], v[196:199]\n\t"
      "global_load_dwordx4 v[136:139], v178, s[62:63] sc1\n\t"
      "s_waitcnt vmcnt(7)\n\t"
      "v_mfma_f32_16x16x32_bf16 v[184:187], v[48:51], v[140:143], v[184:187]\n\t"
      "v_mfma_f32_16x16x32_bf16 v[188:191], v[52:55], v[140:143], v[188:191]\n\t"
      "v_mfma_f32_16x16x32_bf16 v[192:195], v[56:59], v[140:143], v[192:195]\n\t"
      "v_mfma_f32_16x16x32_bf16 v[196:199], v[60:63], v[140:143], v[196:199]\n\t"
      "global_load_dwordx4 v[140:143], v179, s[62:63] sc1\n\t"
      "s_waitcnt vmcnt(7)\n\t"
      "v_mfma_f32_16x16x32_bf16 v[184:187], v[64:67], v[144:147], v[184:187]\n\t"
      "v_mfma_f32_16x16x32_bf16 v[188:191], v[68:71], v[144:147], v[188:191]\n\t"
      "v_mfma_f32_16x16x32_bf16 v[192:195], v[72:75], v[144:147], v[192:195]\n\t"
      "v_mfma_f32_16x16x32_bf16 v[196:199], v[76:79], v[144:147], v[196:199]\n\t"
      "global_load_dwordx4 v[144:147], v180, s[62:63] sc1\n\t"
      "s_waitcnt vmcnt(7)\n\t"
      "v_mfma_f32_16x16x32_bf16 v[184:187], v[80:83], v[148:151], v[184:187]\n\t"
      "v_mfma_f32_16x16x32_bf16 v[188:191], v[84:87], v[148:151], v[188:191]\n\t"
      "v_mfma_f32_16x16x32_bf16 v[192:195], v[88:91], v[148:151], v[192:195]\n\t"
      "v_mfma_f32_16x16x32_bf16 v[196:199], v[92:95], v[148:151], v[196:199]\n\t"
      "global_load_dwordx4 v[148:151], v181, s[62:63] sc1\n\t"
      "s_waitcnt lgkmcnt(0)\n\t"
      "s_barrier\n\t"
      "s_setprio 2\n\t"
      "s_waitcnt vmcnt(7)\n\t"
      "v_mfma_f32_16x16x32_bf16 v[184:187], v[96:99], v[152:155], v[184:187]\n\t"
      "v_mfma_f32_16x16x32_bf16 v[188:191], v[100:103], v[152:155], v[188:191]\n\t"
      "v_mfma_f32_16x16x32_bf16 v[192:195], v[104:107], v[152:155], v[192:195]\n\t"
      "v_mfma_f32_16x16x32_bf16 v[196:199], v[108:111], v[152:155], v[196:199]\n\t"
      "global_load_dwordx4 v[152:155], v182, s[62:63] sc1\n\t"
      "s_waitcnt vmcnt(7)\n\t"
      "v_mfma_f32_16x16x32_bf16 v[184:187], v[112:115], v[156:159], v[184:187]\n\t"
      "v_mfma_f32_16x16x32_bf16 v[188:191], v[116:119], v[156:159], v[188:191]\n\t"
      "v_mfma_f32_16x16x32_bf16 v[192:195], v[120:123], v[156:159], v[192:195]\n\t"
      "v_mfma_f32_16x16x32_bf16 v[196:199], v[124:127], v[156:159], v[196:199]\n\t"
      "global_load_dwordx4 v[156:159], v183, s[62:63] sc1\n\t"
      "s_setprio 0\n\t"
      "s_nop 2\n\t"
      "v_cmp_u_f32_e32 vcc, v184, v184\n\t"
      "s_cbranch_vccnz NQ4R_%=\n\t"
      "NQ4B_%=:\n\t"
      "ds_write_b128 v243, v[184:187]\n\t"
      "ds_write_b128 v164, v[188:191]\n\t"
      "ds_write_b128 v165, v[192:195]\n\t"
      "ds_write_b128 v166, v[196:199]\n\t"
      "s_waitcnt vmcnt(7)\n\t"
      "v_mfma_f32_16x16x32_bf16 v[200:203], v[0:3], v[128:131], v[236:239]\n\t"
      "v_mfma_f32_16x16x32_bf16 v[204:207], v[4:7], v[128:131], 0\n\t"
      "v_mfma_f32_16x16x32_bf16 v[208:211], v[8:11], v[128:131], 0\n\t"
      "v_mfma_f32_16x16x32_bf16 v[212:215], v[12:15], v[128:131], 0\n\t"
      "global_load_dwordx4 v[128:131], v176, s[64:65] sc1\n\t"
      "s_waitcnt vmcnt(7)\n\t"
      "v_mfma_f32_16x16x32_bf16 v[200:203], v[16:19], v[132:135], v[200:203]\n\t"
      "v_mfma_f32_16x16x32_bf16 v[204:207], v[20:23], v[132:135], v[204:207]\n\t"
      "v_mfma_f32_16x16x32_bf16 v[208:211], v[24:27], v[132:135], v[208:211]\n\t"
      "v_mfma_f32_16x16x32_bf16 v[212:215], v[28:31], v[132:135], v[212:215]\n\t"
      "global_load_dwordx4 v[132:135], v177, s[64:65] sc1\n\t"
      "s_waitcnt vmcnt(7)\n\t"
      "v_mfma_f32_16x16x32_bf16 v[200:203], v[32:35], v[136:139], v[200:203]\n\t"
      "v_mfma_f32_16x16x32_bf16 v[204:207], v[36:39], v[136:139], v[204:207]\n\t"
      "v_mfma_f32_16x16x32_bf16 v[208:211], v[40:43], v[136:139], v[208:211]\n\t"
      "v_mfma_f32_16x16x32_bf16 v[212:215], v[44:47], v[136:139], v[212:215]\n\t"
      "global_load_dwordx4 v[136:139], v178, s[64:65] sc1\n\t"
      "s_waitcnt vmcnt(7)\n\t"
      "v_mfma_f32_16x16x32_bf16 v[200:203], v[48:51], v[140:143], v[200:203]\n\t"
      "v_mfma_f32_16x16x32_bf16 v[204:207], v[52:55], v[140:143], v[204:207]\n\t"
      "v_mfma_f32_16x16x32_bf16 v[208:211], v[56:59], v[140:143], v[208:211]\n\t"
      "v_mfma_f32_16x16x32_bf16 v[212:215], v[60:63], v[140:143], v[212:215]\n\t"
      "global_load_dwordx4 v[140:143], v179, s[64:65] sc1\n\t"
      "s_waitcnt vmcnt(7)\n\t"
      "v_mfma_f32_16x16x32_bf16 v[200:203], v[64:67], v[144:147], v[200:203]\n\t"
      "v_mfma_f32_16x16x32_bf16 v[204:207], v[68:71], v[144:147], v[204:207]\n\t"
      "v_mfma_f32_16x16x32_bf16 v[208:211], v[72:75], v[144:147], v[208:211]\n\t"
      "v_mfma_f32_16x16x32_bf16 v[212:215], v[76:79], v[144:147], v[212:215]\n\t"
      "global_load_dwordx4 v[144:147], v180, s[64:65] sc1\n\t"
      "s_waitcnt vmcnt(7)\n\t"
      "v_mfma_f32_16x16x32_bf16 v[200:203], v[80:83], v[148:151], v[200:203]\n\t"
      "v_mfma_f32_16x16x32_bf16 v[204:207], v[84:87], v[148:151], v[204:207]\n\t"
      "v_mfma_f32_16x16x32_bf16 v[208:211], v[88:91], v[148:151], v[208:211]\n\t"
      "v_mfma_f32_16x16x32_bf16 v[212:215], v[92:95], v[148:151], v[212:215]\n\t"
      "global_load_dwordx4 v[148:151], v181, s[64:65] sc1\n\t"
      "s_waitcnt lgkmcnt(0)\n\t"
      "s_barrier\n\t"
      "s_setprio 2\n\t"
      "s_waitcnt vmcnt(7)\n\t"
      "v_mfma_f32_16x16x32_bf16 v[200:203], v[96:99], v[152:155], v[200:203]\n\t"
      "v_mfma_f32_16x16x32_bf16 v[204:207], v[100:103], v[152:155], v[204:207]\n\t"
      "v_mfma_f32_16x16x32_bf16 v[208:211], v[104:107], v[152:155], v[208:211]\n\t"
      "v_mfma_f32_16x16x32_bf16 v[212:215], v[108:111], v[152:155], v[212:215]\n\t"
      "global_load_dwordx4 v[152:155], v182, s[64:65] sc1\n\t"
      "s_waitcnt vmcnt(7)\n\t"
      "v_mfma_f32_16x16x32_bf16 v[200:203], v[112:115], v[156:159], v[200:203]\n\t"
      "v_mfma_f32_16x16x32_bf16 v[204:207], v[116:119], v[156:159], v[204:207]\n\t"
      "v_mfma_f32_16x16x32_bf16 v[208:211], v[120:123], v[156:159], v[208:211]\n\t"
      "v_mfma_f32_16x16x32_bf16 v[212:215], v[124:127], v[156:159], v[212:215]\n\t"
      "global_load_dwordx4 v[156:159], v183, s[64:65] sc1\n\t"
      "s_setprio 0\n\t"
      "s_nop 2\n\t"
      "v_cmp_u_f32_e32 vcc, v200, v200\n\t"
      "s_cbranch_vccnz NQ5R_%=\n\t"
      "NQ5B_%=:\n\t"
      "ds_write_b128 v243, v[200:203] offset:32768\n\t"
      "ds_write_b128 v164, v[204:207] offset:32768\n\t"
      "ds_write_b128 v165, v[208:211] offset:32768\n\t"
      "ds_write_b128 v166, v[212:215] offset:32768\n\t"
      "s_waitcnt vmcnt(7)\n\t"
      "v_mfma_f32_16x16x32_bf16 v[184:187], v[0:3], v[128:131], v[236:239]\n\t"
      "v_mfma_f32_16x16x32_bf16 v[188:191], v[4:7], v[128:131], 0\n\t"
      "v_mfma_f32_16x16x32_bf16 v[192:195], v[8:11], v[128:131], 0\n\t"
      "v_mfma_f32_16x16x32_bf16 v[196:199], v[12:15], v[128:131], 0\n\t"
      "s_waitcnt vmcnt(6)\n\t"
      "v_mfma_f32_16x16x32_bf16 v[184:187], v[16:19], v[132:135], v[184:187]\n\t"
      "v_mfma_f32_16x16x32_bf16 v[188:191], v[20:23], v[132:135], v[188:191]\n\t"
      "v_mfma_f32_16x16x32_bf16 v[192:195], v[24:27], v[132:135], v[192:195]\n\t"
      "v_mfma_f32_16x16x32_bf16 v[196:199], v[28:31], v[132:135], v[196:199]\n\t"
      "s_waitcnt vmcnt(5)\n\t"
      "v_mfma_f32_16x16x32_bf16 v[184:187], v[32:35], v[136:139], v[184:187]\n\t"
      "v_mfma_f32_16x16x32_bf16 v[188:191], v[36:39], v[136:139], v[188:191]\n\t"
      "v_mfma_f32_16x16x32_bf16 v[192:195], v[40:43], v[136:139], v[192:195]\n\t"
      "v_mfma_f32_16x16x32_bf16 v[196:199], v[44:47], v[136:139], v[196:199]\n\t"
      "s_waitcnt vmcnt(4)\n\t"
      "v_mfma_f32_16x16x32_bf16 v[184:187], v[48:51], v[140:143], v[184:187]\n\t"
      "v_mfma_f32_16x16x32_bf16 v[188:191], v[52:55], v[140:143], v[188:191]\n\t"
      "v_mfma_f32_16x16x32_bf16 v[192:195], v[56:59], v[140:143], v[192:195]\n\t"
      "v_mfma_f32_16x16x32_bf16 v[196:199], v[60:63], v[140:143], v[196:199]\n\t"
      "s_waitcnt vmcnt(3)\n\t"
      "v_mfma_f32_16x16x32_bf16 v[184:187], v[64:67], v[144:147], v[184:187]\n\t"
      "v_mfma_f32_16x16x32_bf16 v[188:191], v[68:71], v[144:147], v[188:191]\n\t"
      "v_mfma_f32_16x16x32_bf16 v[192:195], v[72:75], v[144:147], v[192:195]\n\t"
      "v_mfma_f32_16x16x32_bf16 v[196:199], v[76:79], v[144:147], v[196:199]\n\t"
      "s_waitcnt vmcnt(2)\n\t"
      "v_mfma_f32_16x16x32_bf16 v[184:187], v[80:83], v[148:151], v[184:187]\n\t"
      "v_mfma_f32_16x16x32_bf16 v[188:191], v[84:87], v[148:151], v[188:191]\n\t"
      "v_mfma_f32_16x16x32_bf16 v[192:195], v[88:91], v[148:151], v[192:195]\n\t"
      "v_mfma_f32_16x16x32_bf16 v[196:199], v[92:95], v[148:151], v[196:199]\n\t"
      "s_waitcnt lgkmcnt(0)\n\t"
      "s_barrier\n\t"
      "s_setprio 2\n\t"
      "s_waitcnt vmcnt(1)\n\t"
      "v_mfma_f32_16x16x32_bf16 v[184:187], v[96:99], v[152:155], v[184:187]\n\t"
      "v_mfma_f32_16x16x32_bf16 v[188:191], v[100:103], v[152:155], v[188:191]\n\t"
      "v_mfma_f32_16x16x32_bf16 v[192:195], v[104:107], v[152:155], v[192:195]\n\t"
      "v_mfma_f32_16x16x32_bf16 v[196:199], v[108:111], v[152:155], v[196:199]\n\t"
      "s_waitcnt vmcnt(0)\n\t"
      "v_mfma_f32_16x16x32_bf16 v[184:187], v[112:115], v[156:159], v[184:187]\n\t"
      "v_mfma_f32_16x16x32_bf16 v[188:191], v[116:119], v[156:159], v[188:191]\n\t"
      "v_mfma_f32_16x16x32_bf16 v[192:195], v[120:123], v[156:159], v[192:195]\n\t"
      "v_mfma_f32_16x16x32_bf16 v[196:199], v[124:127], v[156:159], v[196:199]\n\t"
      "s_nop 4\n\t"
      "v_cmp_u_f32_e32 vcc, v184, v184\n\t"
      "s_cbranch_vccnz NQ6R_%=\n\t"
      "NQ6B_%=:\n\t"
      "ds_write_b128 v243, v[184:187]\n\t"
      "ds_write_b128 v164, v[188:191]\n\t"
      "ds_write_b128 v165, v[192:195]\n\t"
      "ds_write_b128 v166, v[196:199]\n\t"
      "s_waitcnt lgkmcnt(0)\n\t"
      "s_barrier\n\t"
      "s_branch GX_%=\n\t"
      "ASP_%=:\n\t"
      "s_mov_b32 m0, 163328\n\t"
      "s_cmp_ge_u32 s17, 65536\n\t"
      "s_cbranch_scc0 BOT_%=\n\t"
      "s_barrier\n\t"
      "BOT_%=:\n\t"
      "global_load_lds_dword v241, s[4:5]\n\t"
      "s_add_u32 s4, s4, 131072\n\t"
      "s_addc_u32 s5, s5, 0\n\t"
      "global_load_lds_dword v241, s[4:5]\n\t"
      "s_add_u32 s4, s4, 131072\n\t"
      "s_addc_u32 s5, s5, 0\n\t"
      "global_load_lds_dword v241, s[4:5]\n\t"
      "s_add_u32 s4, s4, 131072\n\t"
      "s_addc_u32 s5, s5, 0\n\t"
      "s_branch AEND_%=\n\t"
      "FQ0R_%=:\n\t"
      "s_mov_b32 s51, 0\n\t"
      "FQ0L_%=:\n\t"
      "s_add_u32 s51, s51, 1\n\t"
      "s_cmp_lt_u32 s51, 262144\n\t"
      "s_cbranch_scc0 FQ0X_%=\n\t"
      "s_waitcnt vmcnt(0)\n\t"
      "s_sleep 1\n\t"
      "global_load_dwordx4 v[128:131], v176, s[52:53] sc1\n\t"
      "global_load_dwordx4 v[132:135], v177, s[52:53] sc1\n\t"
      "global_load_dwordx4 v[136:139], v178, s[52:53] sc1\n\t"
      "global_load_dwordx4 v[140:143], v179, s[52:53] sc1\n\t"
      "global_load_dwordx4 v[144:147], v180, s[52:53] sc1\n\t"
      "global_load_dwordx4 v[148:151], v181, s[52:53] sc1\n\t"
      "global_load_dwordx4 v[152:155], v182, s[52:53] sc1\n\t"
      "global_load_dwordx4 v[156:159], v183, s[52:53] sc1\n\t"
      "s_waitcnt vmcnt(7)\n\t"
      "v_max3_u32 v234, v128, v129, v130\n\t"
      "v_max_u32_e32 v234, v234, v131\n\t"
      "v_cmp_eq_u32_e32 vcc, -1, v234\n\t"
      "s_cbranch_vccnz FQ0L_%=\n\t"
      "FQ0X_%=:\n\t"
      "s_branch FQ0B_%=\n\t"
      "FQ1R_%=:\n\t"
      "s_mov_b32 s51, 0\n\t"
      "FQ1L_%=:\n\t"
      "s_add_u32 s51, s51, 1\n\t"
      "s_cmp_lt_u32 s51, 262144\n\t"
      "s_cbranch_scc0 FQ1X_%=\n\t"
      "s_waitcnt vmcnt(0)\n\t"
      "s_sleep 1\n\t"
      "global_load_dwordx4 v[132:135], v177, s[52:53] sc1\n\t"
      "global_load_dwordx4 v[136:139], v178, s[52:53] sc1\n\t"
      "global_load_dwordx4 v[140:143], v179, s[52:53] sc1\n\t"
      "global_load_dwordx4 v[144:147], v180, s[52:53] sc1\n\t"
      "global_load_dwordx4 v[148:151], v181, s[52:53] sc1\n\t"
      "global_load_dwordx4 v[152:155], v182, s[52:53] sc1\n\t"
      "global_load_dwordx4 v[156:159], v183, s[52:53] sc1\n\t"
      "global_load_dwordx4 v[128:131], v176, s[54:55] sc1\n\t"
      "s_waitcnt vmcnt(7)\n\t"
      "v_max3_u32 v234, v132, v133, v134\n\t"
      "v_max_u32_e32 v234, v234, v135\n\t"
      "v_cmp_eq_u32_e32 vcc, -1, v234\n\t"
      "s_cbranch_vccnz FQ1L_%=\n\t"
      "FQ1X_%=:\n\t"
      "s_branch FQ1B_%=\n\t"
      "FQ2R_%=:\n\t"
      "s_mov_b32 s51, 0\n\t"
      "FQ2L_%=:\n\t"
      "s_add_u32 s51, s51, 1\n\t"
      "s_cmp_lt_u32 s51, 262144\n\t"
      "s_cbranch_scc0 FQ2X_%=\n\t"
      "s_waitcnt vmcnt(0)\n\t"
      "s_sleep 1\n\t"
      "global_load_dwordx4 v[136:139], v178, s[52:53] sc1\n\t"
      "global_load_dwordx4 v[140:143], v179, s[52:53] sc1\n\t"
      "global_load_dwordx4 v[144:147], v180, s[52:53] sc1\n\t"
      "global_load_dwordx4 v[148:151], v181, s[52:53] sc1\n\t"
      "global_load_dwordx4 v[152:155], v182, s[52:53] sc1\n\t"
      "global_load_dwordx4 v[156:159], v183, s[52:53] sc1\n\t"
      "global_load_dwordx4 v[128:131], v176, s[54:55] sc1\n\t"
      "global_load_dwordx4 v[132:135], v177, s[54:55] sc1\n\t"
      "s_waitcnt vmcnt(7)\n\t"
      "v_max3_u32 v234, v136, v137, v138\n\t"
      "v_max_u32_e32 v234, v234, v139\n\t"
      "v_cmp_eq_u32_e32 vcc, -1, v234\n\t"
      "s_cbranch_vccnz FQ2L_%=\n\t"
      "FQ2X_%=:\n\t"
      "s_branch FQ2B_%=\n\t"
      "FQ3R_%=:\n\t"
      "s_mov_b32 s51, 0\n\t"
      "FQ3L_%=:\n\t"
      "s_add_u32 s51, s51, 1\n\t"
      "s_cmp_lt_u32 s51, 262144\n\t"
      "s_cbranch_scc0 FQ3X_%=\n\t"
      "s_waitcnt vmcnt(0)\n\t"
      "s_sleep 1\n\t"
      "global_load_dwordx4 v[140:143], v179, s[52:53] sc1\n\t"
      "global_load_dwordx4 v[144:147], v180, s[52:53] sc1\n\t"
      "global_load_dwordx4 v[148:151], v181, s[52:53] sc1\n\t"
      "global_load_dwordx4 v[152:155], v182, s[52:53] sc1\n\t"
      "global_load_dwordx4 v[156:159], v183, s[52:53] sc1\n\t"
      "global_load_dwordx4 v[128:131], v176, s[54:55] sc1\n\t"
      "global_load_dwordx4 v[132:135], v177, s[54:55] sc1\n\t"
      "global_load_dwordx4 v[136:139], v178, s[54:55] sc1\n\t"
      "s_waitcnt vmcnt(7)\n\t"
      "v_max3_u32 v234, v140, v141, v142\n\t"
      "v_max_u32_e32 v234, v234, v143\n\t"
      "v_cmp_eq_u32_e32 vcc, -1, v234\n\t"
      "s_cbranch_vccnz FQ3L_%=\n\t"
      "FQ3X_%=:\n\t"
      "s_branch FQ3B_%=\n\t"
      "FQ4R_%=:\n\t"
      "s_mov_b32 s51, 0\n\t"
      "FQ4L_%=:\n\t"
      "s_add_u32 s51, s51, 1\n\t"
      "s_cmp_lt_u32 s51, 262144\n\t"
      "s_cbranch_scc0 FQ4X_%=\n\t"
      "s_waitcnt vmcnt(0)\n\t"
      "s_sleep 1\n\t"
      "global_load_dwordx4 v[144:147], v180, s[52:53] sc1\n\t"
      "global_load_dwordx4 v[148:151], v181, s[52:53] sc1\n\t"
      "global_load_dwordx4 v[152:155], v182, s[52:53] sc1\n\t"
      "global_load_dwordx4 v[156:159], v183, s[52:53] sc1\n\t"
      "global_load_dwordx4 v[128:131], v176, s[54:55] sc1\n\t"
      "global_load_dwordx4 v[132:135], v177, s[54:55] sc1\n\t"
      "global_load_dwordx4 v[136:139], v178, s[54:55] sc1\n\t"
      "global_load_dwordx4 v[140:143], v179, s[54:55] sc1\n\t"
      "s_waitcnt vmcnt(7)\n\t"
      "v_max3_u32 v234, v144, v145, v146\n\t"
      "v_max_u32_e32 v234, v234, v147\n\t"
      "v_cmp_eq_u32_e32 vcc, -1, v234\n\t"
      "s_cbranch_vccnz FQ4L_%=\n\t"
      "FQ4X_%=:\n\t"
      "s_branch FQ4B_%=\n\t"
      "FQ5R_%=:\n\t"
      "s_mov_b32 s51, 0\n\t"
      "FQ5L_%=:\n\t"
      "s_add_u32 s51, s51, 1\n\t"
      "s_cmp_lt_u32 s51, 262144\n\t"
      "s_cbranch_scc0 FQ5X_%=\n\t"
      "s_waitcnt vmcnt(0)\n\t"
      "s_sleep 1\n\t"
      "global_load_dwordx4 v[148:151], v181, s[52:53] sc1\n\t"
      "global_load_dwordx4 v[152:155], v182, s[52:53] sc1\n\t"
      "global_load_dwordx4 v[156:159], v183, s[52:53] sc1\n\t"
      "global_load_dwordx4 v[128:131], v176, s[54:55] sc1\n\t"
      "global_load_dwordx4 v[132:135], v177, s[54:55] sc1\n\t"
      "global_load_dwordx4 v[136:139], v178, s[54:55] sc1\n\t"
      "global_load_dwordx4 v[140:143], v179, s[54:55] sc1\n\t"
      "global_load_dwordx4 v[144:147], v180, s[54:55] sc1\n\t"
      "s_waitcnt vmcnt(7)\n\t"
      "v_max3_u32 v234, v148, v149, v150\n\t"
      "v_max_u32_e32 v234, v234, v151\n\t"
      "v_cmp_eq_u32_e32 vcc, -1, v234\n\t"
      "s_cbranch_vccnz FQ5L_%=\n\t"
      "FQ5X_%=:\n\t"
      "s_branch FQ5B_%=\n\t"
      "FQ6R_%=:\n\t"
      "s_mov_b32 s51, 0\n\t"
      "FQ6L_%=:\n\t"
      "s_add_u32 s51, s51, 1\n\t"
      "s_cmp_lt_u32 s51, 262144\n\t"
      "s_cbranch_scc0 FQ6X_%=\n\t"
      "s_waitcnt vmcnt(0)\n\t"
      "s_sleep 1\n\t"
      "global_load_dwordx4 v[152:155], v182, s[52:53] sc1\n\t"
      "global_load_dwordx4 v[156:159], v183, s[52:53] sc1\n\t"
      "global_load_dwordx4 v[128:131], v176, s[54:55] sc1\n\t"
      "global_load_dwordx4 v[132:135], v177, s[54:55] sc1\n\t"
      "global_load_dwordx4 v[136:139], v178, s[54:55] sc1\n\t"
      "global_load_dwordx4 v[140:143], v179, s[54:55] sc1\n\t"
      "global_load_dwordx4 v[144:147], v180, s[54:55] sc1\n\t"
      "global_load_dwordx4 v[148:151], v181, s[54:55] sc1\n\t"
      "s_waitcnt vmcnt(7)\n\t"
      "v_max3_u32 v234, v152, v153, v154\n\t"
      "v_max_u32_e32 v234, v234, v155\n\t"
      "v_cmp_eq_u32_e32 vcc, -1, v234\n\t"
      "s_cbranch_vccnz FQ6L_%=\n\t"
      "FQ6X_%=:\n\t"
      "s_branch FQ6B_%=\n\t"
      "FQ7R_%=:\n\t"
      "s_mov_b32 s51, 0\n\t"
      "FQ7L_%=:\n\t"
      "s_add_u32 s51, s51, 1\n\t"
      "s_cmp_lt_u32 s51, 262144\n\t"
      "s_cbranch_scc0 FQ7X_%=\n\t"
      "s_waitcnt vmcnt(0)\n\t"
      "s_sleep 1\n\t"
      "global_load_dwordx4 v[156:159], v183, s[52:53] sc1\n\t"
      "global_load_dwordx4 v[128:131], v176, s[54:55] sc1\n\t"
      "global_load_dwordx4 v[132:135], v177, s[54:55] sc1\n\t"
      "global_load_dwordx4 v[136:139], v178, s[54:55] sc1\n\t"
      "global_load_dwordx4 v[140:143], v179, s[54:55] sc1\n\t"
      "global_load_dwordx4 v[144:147], v180, s[54:55] sc1\n\t"
      "global_load_dwordx4 v[148:151], v181, s[54:55] sc1\n\t"
      "global_load_dwordx4 v[152:155], v182, s[54:55] sc1\n\t"
      "s_waitcnt vmcnt(7)\n\t"
      "v_max3_u32 v234, v156, v157, v158\n\t"
      "v_max_u32_e32 v234, v234, v159\n\t"
      "v_cmp_eq_u32_e32 vcc, -1, v234\n\t"
      "s_cbranch_vccnz FQ7L_%=\n\t"
      "FQ7X_%=:\n\t"
      "s_branch FQ7B_%=\n\t"
      "NQ1R_%=:\n\t"
      "s_mov_b32 s51, 0\n\t"
      "NQ1L_%=:\n\t"
      "s_add_u32 s51, s51, 1\n\t"
      "s_cmp_lt_u32 s51, 262144\n\t"
      "s_cbranch_scc0 NQ1X_%=\n\t"
      "s_waitcnt vmcnt(0)\n\t"
      "s_sleep 1\n\t"
      "global_load_dwordx4 v[128:131], v176, s[54:55] sc1\n\t"
      "global_load_dwordx4 v[132:135], v177, s[54:55] sc1\n\t"
      "global_load_dwordx4 v[136:139], v178, s[54:55] sc1\n\t"
      "global_load_dwordx4 v[140:143], v179, s[54:55] sc1\n\t"
      "global_load_dwordx4 v[144:147], v180, s[54:55] sc1\n\t"
      "global_load_dwordx4 v[148:151], v181, s[54:55] sc1\n\t"
      "global_load_dwordx4 v[152:155], v182, s[54:55] sc1\n\t"
      "global_load_dwordx4 v[156:159], v183, s[54:55] sc1\n\t"
      "s_waitcnt vmcnt(0)\n\t"
      "v_max3_u32 v234, v128, v129, v130\n\t"
      "v_max3_u32 v234, v234, v131, v132\n\t"
      "v_max3_u32 v234, v234, v133, v134\n\t"
      "v_max3_u32 v234, v234, v135, v136\n\t"
      "v_max3_u32 v234, v234, v137, v138\n\t"
      "v_max3_u32 v234, v234, v139, v140\n\t"
      "v_max3_u32 v234, v234, v141, v142\n\t"
      "v_max3_u32 v234, v234, v143, v144\n\t"
      "v_max3_u32 v234, v234, v145, v146\n\t"
      "v_max3_u32 v234, v234, v147, v148\n\t"
      "v_max3_u32 v234, v234, v149, v150\n\t"
      "v_max3_u32 v234, v234, v151, v152\n\t"
      "v_max3_u32 v234, v234, v153, v154\n\t"
      "v_max3_u32 v234, v234, v155, v156\n\t"
      "v_max3_u32 v234, v234, v157, v158\n\t"
      "v_max_u32_e32 v234, v234, v159\n\t"
      "v_cmp_eq_u32_e32 vcc, -1, v234\n\t"
      "s_cbranch_vccnz NQ1L_%=\n\t"
      "NQ1X_%=:\n\t"
      "v_mfma_f32_16x16x32_bf16 v[200:203], v[0:3], v[128:131], v[236:239]\n\t"
      "v_mfma_f32_16x16x32_bf16 v[204:207], v[4:7], v[128:131], 0\n\t"
      "v_mfma_f32_16x16x32_bf16 v[208:211], v[8:11], v[128:131], 0\n\t"
      "v_mfma_f32_16x16x32_bf16 v[212:215], v[12:15], v[128:131], 0\n\t"
      "v_mfma_f32_16x16x32_bf16 v[200:203], v[16:19], v[132:135], v[200:203]\n\t"
      "v_mfma_f32_16x16x32_bf16 v[204:207], v[20:23], v[132:135], v[204:207]\n\t"
      "v_mfma_f32_16x16x32_bf16 v[208:211], v[24:27], v[132:135], v[208:211]\n\t"
      "v_mfma_f32_16x16x32_bf16 v[212:215], v[28:31], v[132:135], v[212:215]\n\t"
      "v_mfma_f32_16x16x32_bf16 v[200:203], v[32:35], v[136:139], v[200:203]\n\t"
      "v_mfma_f32_16x16x32_bf16 v[204:207], v[36:39], v[136:139], v[204:207]\n\t"
      "v_mfma_f32_16x16x32_bf16 v[208:211], v[40:43], v[136:139], v[208:211]\n\t"
      "v_mfma_f32_16x16x32_bf16 v[212:215], v[44:47], v[136:139], v[212:215]\n\t"
      "v_mfma_f32_16x16x32_bf16 v[200:203], v[48:51], v[140:143], v[200:203]\n\t"
      "v_mfma_f32_16x16x32_bf16 v[204:207], v[52:55], v[140:143], v[204:207]\n\t"
      "v_mfma_f32_16x16x32_bf16 v[208:211], v[56:59], v[140:143], v[208:211]\n\t"
      "v_mfma_f32_16x16x32_bf16 v[212:215], v[60:63], v[140:143], v[212:215]\n\t"
      "v_mfma_f32_16x16x32_bf16 v[200:203], v[64:67], v[144:147], v[200:203]\n\t"
      "v_mfma_f32_16x16x32_bf16 v[204:207], v[68:71], v[144:147], v[204:207]\n\t"
      "v_mfma_f32_16x16x32_bf16 v[208:211], v[72:75], v[144:147], v[208:211]\n\t"
      "v_mfma_f32_16x16x32_bf16 v[212:215], v[76:79], v[144:147], v[212:215]\n\t"
      "v_mfma_f32_16x16x32_bf16 v[200:203], v[80:83], v[148:151], v[200:203]\n\t"
      "v_mfma_f32_16x16x32_bf16 v[204:207], v[84:87], v[148:151], v[204:207]\n\t"
      "v_mfma_f32_16x16x32_bf16 v[208:211], v[88:91], v[148:151], v[208:211]\n\t"
      "v_mfma_f32_16x16x32_bf16 v[212:215], v[92:95], v[148:151], v[212:215]\n\t"
      "v_mfma_f32_16x16x32_bf16 v[200:203], v[96:99], v[152:155], v[200:203]\n\t"
      "v_mfma_f32_16x16x32_bf16 v[204:207], v[100:103], v[152:155], v[204:207]\n\t"
      "v_mfma_f32_16x16x32_bf16 v[208:211], v[104:107], v[152:155], v[208:211]\n\t"
      "v_mfma_f32_16x16x32_bf16 v[212:215], v[108:111], v[152:155], v[212:215]\n\t"
      "v_mfma_f32_16x16x32_bf16 v[200:203], v[112:115], v[156:159], v[200:203]\n\t"
      "v_mfma_f32_16x16x32_bf16 v[204:207], v[116:119], v[156:159], v[204:207]\n\t"
      "v_mfma_f32_16x16x32_bf16 v[208:211], v[120:123], v[156:159], v[208:211]\n\t"
      "v_mfma_f32_16x16x32_bf16 v[212:215], v[124:127], v[156:159], v[212:215]\n\t"
      "global_load_dwordx4 v[128:131], v176, s[56:57] sc1\n\t"
      "global_load_dwordx4 v[132:135], v177, s[56:57] sc1\n\t"
      "global_load_dwordx4 v[136:139], v178, s[56:57] sc1\n\t"
      "global_load_dwordx4 v[140:143], v179, s[56:57] sc1\n\t"
      "global_load_dwordx4 v[144:147], v180, s[56:57] sc1\n\t"
      "global_load_dwordx4 v[148:151], v181, s[56:57] sc1\n\t"
      "global_load_dwordx4 v[152:155], v182, s[56:57] sc1\n\t"
      "global_load_dwordx4 v[156:159], v183, s[56:57] sc1\n\t"
      "s_waitcnt vmcnt(0)\n\t"
      "s_nop 7\n\t"
      "s_branch NQ1B_%=\n\t"
      "NQ2R_%=:\n\t"
      "s_mov_b32 s51, 0\n\t"
      "NQ2L_%=:\n\t"
      "s_add_u32 s51, s51, 1\n\t"
      "s_cmp_lt_u32 s51, 262144\n\t"
      "s_cbranch_scc0 NQ2X_%=\n\t"
      "s_waitcnt vmcnt(0)\n\t"
      "s_sleep 1\n\t"
      "global_load_dwordx4 v[128:131], v176, s[56:57] sc1\n\t"
      "global_load_dwordx4 v[132:135], v177, s[56:57] sc1\n\t"
      "global_load_dwordx4 v[136:139], v178, s[56:57] sc1\n\t"
      "global_load_dwordx4 v[140:143], v179, s[56:57] sc1\n\t"
      "global_load_dwordx4 v[144:147], v180, s[56:57] sc1\n\t"
      "global_load_dwordx4 v[148:151], v181, s[56:57] sc1\n\t"
      "global_load_dwordx4 v[152:155], v182, s[56:57] sc1\n\t"
      "global_load_dwordx4 v[156:159], v183, s[56:57] sc1\n\t"
      "s_waitcnt vmcnt(0)\n\t"
      "v_max3_u32 v234, v128, v129, v130\n\t"
      "v_max3_u32 v234, v234, v131, v132\n\t"
      "v_max3_u32 v234, v234, v133, v134\n\t"
      "v_max3_u32 v234, v234, v135, v136\n\t"
      "v_max3_u32 v234, v234, v137, v138\n\t"
      "v_max3_u32 v234, v234, v139, v140\n\t"
      "v_max3_u32 v234, v234, v141, v142\n\t"
      "v_max3_u32 v234, v234, v143, v144\n\t"
      "v_max3_u32 v234, v234, v145, v146\n\t"
      "v_max3_u32 v234, v234, v147, v148\n\t"
      "v_max3_u32 v234, v234, v149, v150\n\t"
      "v_max3_u32 v234, v234, v151, v152\n\t"
      "v_max3_u32 v234, v234, v153, v154\n\t"
      "v_max3_u32 v234, v234, v155, v156\n\t"
      "v_max3_u32 v234, v234, v157, v158\n\t"
      "v_max_u32_e32 v234, v234, v159\n\t"
      "v_cmp_eq_u32_e32 vcc, -1, v234\n\t"
      "s_cbranch_vccnz NQ2L_%=\n\t"
      "NQ2X_%=:\n\t"
      "v_mfma_f32_16x16x32_bf16 v[184:187], v[0:3], v[128:131], v[236:239]\n\t"
      "v_mfma_f32_16x16x32_bf16 v[188:191], v[4:7], v[128:131], 0\n\t"
      "v_mfma_f32_16x16x32_bf16 v[192:195], v[8:11], v[128:131], 0\n\t"
      "v_mfma_f32_16x16x32_bf16 v[196:199], v[12:15], v[128:131], 0\n\t"
      "v_mfma_f32_16x16x32_bf16 v[184:187], v[16:19], v[132:135], v[184:187]\n\t"
      "v_mfma_f32_16x16x32_bf16 v[188:191], v[20:23], v[132:135], v[188:191]\n\t"
      "v_mfma_f32_16x16x32_bf16 v[192:195], v[24:27], v[132:135], v[192:195]\n\t"
      "v_mfma_f32_16x16x32_bf16 v[196:199], v[28:31], v[132:135], v[196:199]\n\t"
      "v_mfma_f32_16x16x32_bf16 v[184:187], v[32:35], v[136:139], v[184:187]\n\t"
      "v_mfma_f32_16x16x32_bf16 v[188:191], v[36:39], v[136:139], v[188:191]\n\t"
      "v_mfma_f32_16x16x32_bf16 v[192:195], v[40:43], v[136:139], v[192:195]\n\t"
      "v_mfma_f32_16x16x32_bf16 v[196:199], v[44:47], v[136:139], v[196:199]\n\t"
      "v_mfma_f32_16x16x32_bf16 v[184:187], v[48:51], v[140:143], v[184:187]\n\t"
      "v_mfma_f32_16x16x32_bf16 v[188:191], v[52:55], v[140:143], v[188:191]\n\t"
      "v_mfma_f32_16x16x32_bf16 v[192:195], v[56:59], v[140:143], v[192:195]\n\t"
      "v_mfma_f32_16x16x32_bf16 v[196:199], v[60:63], v[140:143], v[196:199]\n\t"
      "v_mfma_f32_16x16x32_bf16 v[184:187], v[64:67], v[144:147], v[184:187]\n\t"
      "v_mfma_f32_16x16x32_bf16 v[188:191], v[68:71], v[144:147], v[188:191]\n\t"
      "v_mfma_f32_16x16x32_bf16 v[192:195], v[72:75], v[144:147], v[192:195]\n\t"
      "v_mfma_f32_16x16x32_bf16 v[196:199], v[76:79], v[144:147], v[196:199]\n\t"
      "v_mfma_f32_16x16x32_bf16 v[184:187], v[80:83], v[148:151], v[184:187]\n\t"
      "v_mfma_f32_16x16x32_bf16 v[188:191], v[84:87], v[148:151], v[188:191]\n\t"
      "v_mfma_f32_16x16x32_bf16 v[192:195], v[88:91], v[148:151], v[192:195]\n\t"
      "v_mfma_f32_16x16x32_bf16 v[196:199], v[92:95], v[148:151], v[196:199]\n\t"
      "v_mfma_f32_16x16x32_bf16 v[184:187], v[96:99], v[152:155], v[184:187]\n\t"
      "v_mfma_f32_16x16x32_bf16 v[188:191], v[100:103], v[152:155], v[188:191]\n\t"
      "v_mfma_f32_16x16x32_bf16 v[192:195], v[104:107], v[152:155], v[192:195]\n\t"
      "v_mfma_f32_16x16x32_bf16 v[196:199], v[108:111], v[152:155], v[196:199]\n\t"
      "v_mfma_f32_16x16x32_bf16 v[184:187], v[112:115], v[156:159], v[184:187]\n\t"
      "v_mfma_f32_16x16x32_bf16 v[188:191], v[116:119], v[156:159], v[188:191]\n\t"
      "v_mfma_f32_16x16x32_bf16 v[192:195], v[120:123], v[156:159], v[192:195]\n\t"
      "v_mfma_f32_16x16x32_bf16 v[196:199], v[124:127], v[156:159], v[196:199]\n\t"
      "global_load_dwordx4 v[128:131], v176, s[58:59] sc1\n\t"
      "global_load_dwordx4 v[132:135], v177, s[58:59] sc1\n\t"
      "global_load_dwordx4 v[136:139], v178, s[58:59] sc1\n\t"
      "global_load_dwordx4 v[140:143], v179, s[58:59] sc1\n\t"
      "global_load_dwordx4 v[144:147], v180, s[58:59] sc1\n\t"
      "global_load_dwordx4 v[148:151], v181, s[58:59] sc1\n\t"
      "global_load_dwordx4 v[152:155], v182, s[58:59] sc1\n\t"
      "global_load_dwordx4 v[156:159], v183, s[58:59] sc1\n\t"
      "s_waitcnt vmcnt(0)\n\t"
      "s_nop 7\n\t"
      "s_branch NQ2B_%=\n\t"
      "NQ3R_%=:\n\t"
      "s_mov_b32 s51, 0\n\t"
      "NQ3L_%=:\n\t"
      "s_add_u32 s51, s51, 1\n\t"
      "s_cmp_lt_u32 s51, 262144\n\t"
      "s_cbranch_scc0 NQ3X_%=\n\t"
      "s_waitcnt vmcnt(0)\n\t"
      "s_sleep 1\n\t"
      "global_load_dwordx4 v[128:131], v176, s[58:59] sc1\n\t"
      "global_load_dwordx4 v[132:135], v177, s[58:59] sc1\n\t"
      "global_load_dwordx4 v[136:139], v178, s[58:59] sc1\n\t"
      "global_load_dwordx4 v[140:143], v179, s[58:59] sc1\n\t"
      "global_load_dwordx4 v[144:147], v180, s[58:59] sc1\n\t"
      "global_load_dwordx4 v[148:151], v181, s[58:59] sc1\n\t"
      "global_load_dwordx4 v[152:155], v182, s[58:59] sc1\n\t"
      "global_load_dwordx4 v[156:159], v183, s[58:59] sc1\n\t"
      "s_waitcnt vmcnt(0)\n\t"
      "v_max3_u32 v234, v128, v129, v130\n\t"
      "v_max3_u32 v234, v234, v131, v132\n\t"
      "v_max3_u32 v234, v234, v133, v134\n\t"
      "v_max3_u32 v234, v234, v135, v136\n\t"
      "v_max3_u32 v234, v234, v137, v138\n\t"
      "v_max3_u32 v234, v234, v139, v140\n\t"
      "v_max3_u32 v234, v234, v141, v142\n\t"
      "v_max3_u32 v234, v234, v143, v144\n\t"
      "v_max3_u32 v234, v234, v145, v146\n\t"
      "v_max3_u32 v234, v234, v147, v148\n\t"
      "v_max3_u32 v234, v234, v149, v150\n\t"
      "v_max3_u32 v234, v234, v151, v152\n\t"
      "v_max3_u32 v234, v234, v153, v154\n\t"
      "v_max3_u32 v234, v234, v155, v156\n\t"
      "v_max3_u32 v234, v234, v157, v158\n\t"
      "v_max_u32_e32 v234, v234, v159\n\t"
      "v_cmp_eq_u32_e32 vcc, -1, v234\n\t"
      "s_cbranch_vccnz NQ3L_%=\n\t"
      "NQ3X_%=:\n\t"
      "v_mfma_f32_16x16x32_bf16 v[200:203], v[0:3], v[128:131], v[236:239]\n\t"
      "v_mfma_f32_16x16x32_bf16 v[204:207], v[4:7], v[128:131], 0\n\t"
      "v_mfma_f32_16x16x32_bf16 v[208:211], v[8:11], v[128:131], 0\n\t"
      "v_mfma_f32_16x16x32_bf16 v[212:215], v[12:15], v[128:131], 0\n\t"
      "v_mfma_f32_16x16x32_bf16 v[200:203], v[16:19], v[132:135], v[200:203]\n\t"
      "v_mfma_f32_16x16x32_bf16 v[204:207], v[20:23], v[132:135], v[204:207]\n\t"
      "v_mfma_f32_16x16x32_bf16 v[208:211], v[24:27], v[132:135], v[208:211]\n\t"
      "v_mfma_f32_16x16x32_bf16 v[212:215], v[28:31], v[132:135], v[212:215]\n\t"
      "v_mfma_f32_16x16x32_bf16 v[200:203], v[32:35], v[136:139], v[200:203]\n\t"
      "v_mfma_f32_16x16x32_bf16 v[204:207], v[36:39], v[136:139], v[204:207]\n\t"
      "v_mfma_f32_16x16x32_bf16 v[208:211], v[40:43], v[136:139], v[208:211]\n\t"
      "v_mfma_f32_16x16x32_bf16 v[212:215], v[44:47], v[136:139], v[212:215]\n\t"
      "v_mfma_f32_16x16x32_bf16 v[200:203], v[48:51], v[140:143], v[200:203]\n\t"
      "v_mfma_f32_16x16x32_bf16 v[204:207], v[52:55], v[140:143], v[204:207]\n\t"
      "v_mfma_f32_16x16x32_bf16 v[208:211], v[56:59], v[140:143], v[208:211]\n\t"
      "v_mfma_f32_16x16x32_bf16 v[212:215], v[60:63], v[140:143], v[212:215]\n\t"
      "v_mfma_f32_16x16x32_bf16 v[200:203], v[64:67], v[144:147], v[200:203]\n\t"
      "v_mfma_f32_16x16x32_bf16 v[204:207], v[68:71], v[144:147], v[204:207]\n\t"
      "v_mfma_f32_16x16x32_bf16 v[208:211], v[72:75], v[144:147], v[208:211]\n\t"
      "v_mfma_f32_16x16x32_bf16 v[212:215], v[76:79], v[144:147], v[212:215]\n\t"
      "v_mfma_f32_16x16x32_bf16 v[200:203], v[80:83], v[148:151], v[200:203]\n\t"
      "v_mfma_f32_16x16x32_bf16 v[204:207], v[84:87], v[148:151], v[204:207]\n\t"
      "v_mfma_f32_16x16x32_bf16 v[208:211], v[88:91], v[148:151], v[208:211]\n\t"
      "v_mfma_f32_16x16x32_bf16 v[212:215], v[92:95], v[148:151], v[212:215]\n\t"
      "v_mfma_f32_16x16x32_bf16 v[200:203], v[96:99], v[152:155], v[200:203]\n\t"
      "v_mfma_f32_16x16x32_bf16 v[204:207], v[100:103], v[152:155], v[204:207]\n\t"
      "v_mfma_f32_16x16x32_bf16 v[208:211], v[104:107], v[152:155], v[208:211]\n\t"
      "v_mfma_f32_16x16x32_bf16 v[212:215], v[108:111], v[152:155], v[212:215]\n\t"
      "v_mfma_f32_16x16x32_bf16 v[200:203], v[112:115], v[156:159], v[200:203]\n\t"
      "v_mfma_f32_16x16x32_bf16 v[204:207], v[116:119], v[156:159], v[204:207]\n\t"
      "v_mfma_f32_16x16x32_bf16 v[208:211], v[120:123], v[156:159], v[208:211]\n\t"
      "v_mfma_f32_16x16x32_bf16 v[212:215], v[124:127], v[156:159], v[212:215]\n\t"
      "global_load_dwordx4 v[128:131], v176, s[60:61] sc1\n\t"
      "global_load_dwordx4 v[132:135], v177, s[60:61] sc1\n\t"
      "global_load_dwordx4 v[136:139], v178, s[60:61] sc1\n\t"
      "global_load_dwordx4 v[140:143], v179, s[60:61] sc1\n\t"
      "global_load_dwordx4 v[144:147], v180, s[60:61] sc1\n\t"
      "global_load_dwordx4 v[148:151], v181, s[60:61] sc1\n\t"
      "global_load_dwordx4 v[152:155], v182, s[60:61] sc1\n\t"
      "global_load_dwordx4 v[156:159], v183, s[60:61] sc1\n\t"
      "s_waitcnt vmcnt(0)\n\t"
      "s_nop 7\n\t"
      "s_branch NQ3B_%=\n\t"
      "NQ4R_%=:\n\t"
      "s_mov_b32 s51, 0\n\t"
      "NQ4L_%=:\n\t"
      "s_add_u32 s51, s51, 1\n\t"
      "s_cmp_lt_u32 s51, 262144\n\t"
      "s_cbranch_scc0 NQ4X_%=\n\t"
      "s_waitcnt vmcnt(0)\n\t"
      "s_sleep 1\n\t"
      "global_load_dwordx4 v[128:131], v176, s[60:61] sc1\n\t"
      "global_load_dwordx4 v[132:135], v177, s[60:61] sc1\n\t"
      "global_load_dwordx4 v[136:139], v178, s[60:61] sc1\n\t"
      "global_load_dwordx4 v[140:143], v179, s[60:61] sc1\n\t"
      "global_load_dwordx4 v[144:147], v180, s[60:61] sc1\n\t"
      "global_load_dwordx4 v[148:151], v181, s[60:61] sc1\n\t"
      "global_load_dwordx4 v[152:155], v182, s[60:61] sc1\n\t"
      "global_load_dwordx4 v[156:159], v183, s[60:61] sc1\n\t"
      "s_waitcnt vmcnt(0)\n\t"
      "v_max3_u32 v234, v128, v129, v130\n\t"
      "v_max3_u32 v234, v234, v131, v132\n\t"
      "v_max3_u32 v234, v234, v133, v134\n\t"
      "v_max3_u32 v234, v234, v135, v136\n\t"
      "v_max3_u32 v234, v234, v137, v138\n\t"
      "v_max3_u32 v234, v234, v139, v140\n\t"
      "v_max3_u32 v234, v234, v141, v142\n\t"
      "v_max3_u32 v234, v234, v143, v144\n\t"
      "v_max3_u32 v234, v234, v145, v146\n\t"
      "v_max3_u32 v234, v234, v147, v148\n\t"
      "v_max3_u32 v234, v234, v149, v150\n\t"
      "v_max3_u32 v234, v234, v151, v152\n\t"
      "v_max3_u32 v234, v234, v153, v154\n\t"
      "v_max3_u32 v234, v234, v155, v156\n\t"
      "v_max3_u32 v234, v234, v157, v158\n\t"
      "v_max_u32_e32 v234, v234, v159\n\t"
      "v_cmp_eq_u32_e32 vcc, -1, v234\n\t"
      "s_cbranch_vccnz NQ4L_%=\n\t"
      "NQ4X_%=:\n\t"
      "v_mfma_f32_16x16x32_bf16 v[184:187], v[0:3], v[128:131], v[236:239]\n\t"
      "v_mfma_f32_16x16x32_bf16 v[188:191], v[4:7], v[128:131], 0\n\t"
      "v_mfma_f32_16x16x32_bf16 v[192:195], v[8:11], v[128:131], 0\n\t"
      "v_mfma_f32_16x16x32_bf16 v[196:199], v[12:15], v[128:131], 0\n\t"
      "v_mfma_f32_16x16x32_bf16 v[184:187], v[16:19], v[132:135], v[184:187]\n\t"
      "v_mfma_f32_16x16x32_bf16 v[188:191], v[20:23], v[132:135], v[188:191]\n\t"
      "v_mfma_f32_16x16x32_bf16 v[192:195], v[24:27], v[132:135], v[192:195]\n\t"
      "v_mfma_f32_16x16x32_bf16 v[196:199], v[28:31], v[132:135], v[196:199]\n\t"
      "v_mfma_f32_16x16x32_bf16 v[184:187], v[32:35], v[136:139], v[184:187]\n\t"
      "v_mfma_f32_16x16x32_bf16 v[188:191], v[36:39], v[136:139], v[188:191]\n\t"
      "v_mfma_f32_16x16x32_bf16 v[192:195], v[40:43], v[136:139], v[192:195]\n\t"
      "v_mfma_f32_16x16x32_bf16 v[196:199], v[44:47], v[136:139], v[196:199]\n\t"
      "v_mfma_f32_16x16x32_bf16 v[184:187], v[48:51], v[140:143], v[184:187]\n\t"
      "v_mfma_f32_16x16x32_bf16 v[188:191], v[52:55], v[140:143], v[188:191]\n\t"
      "v_mfma_f32_16x16x32_bf16 v[192:195], v[56:59], v[140:143], v[192:195]\n\t"
      "v_mfma_f32_16x16x32_bf16 v[196:199], v[60:63], v[140:143], v[196:199]\n\t"
      "v_mfma_f32_16x16x32_bf16 v[184:187], v[64:67], v[144:147], v[184:187]\n\t"
      "v_mfma_f32_16x16x32_bf16 v[188:191], v[68:71], v[144:147], v[188:191]\n\t"
      "v_mfma_f32_16x16x32_bf16 v[192:195], v[72:75], v[144:147], v[192:195]\n\t"
      "v_mfma_f32_16x16x32_bf16 v[196:199], v[76:79], v[144:147], v[196:199]\n\t"
      "v_mfma_f32_16x16x32_bf16 v[184:187], v[80:83], v[148:151], v[184:187]\n\t"
      "v_mfma_f32_16x16x32_bf16 v[188:191], v[84:87], v[148:151], v[188:191]\n\t"
      "v_mfma_f32_16x16x32_bf16 v[192:195], v[88:91], v[148:151], v[192:195]\n\t"
      "v_mfma_f32_16x16x32_bf16 v[196:199], v[92:95], v[148:151], v[196:199]\n\t"
      "v_mfma_f32_16x16x32_bf16 v[184:187], v[96:99], v[152:155], v[184:187]\n\t"
      "v_mfma_f32_16x16x32_bf16 v[188:191], v[100:103], v[152:155], v[188:191]\n\t"
      "v_mfma_f32_16x16x32_bf16 v[192:195], v[104:107], v[152:155], v[192:195]\n\t"
      "v_mfma_f32_16x16x32_bf16 v[196:199], v[108:111], v[152:155], v[196:199]\n\t"
      "v_mfma_f32_16x16x32_bf16 v[184:187], v[112:115], v[156:159], v[184:187]\n\t"
      "v_mfma_f32_16x16x32_bf16 v[188:191], v[116:119], v[156:159], v[188:191]\n\t"
      "v_mfma_f32_16x16x32_bf16 v[192:195], v[120:123], v[156:159], v[192:195]\n\t"
      "v_mfma_f32_16x16x32_bf16 v[196:199], v[124:127], v[156:159], v[196:199]\n\t"
      "global_load_dwordx4 v[128:131], v176, s[62:63] sc1\n\t"
      "global_load_dwordx4 v[132:135], v177, s[62:63] sc1\n\t"
      "global_load_dwordx4 v[136:139], v178, s[62:63] sc1\n\t"
      "global_load_dwordx4 v[140:143], v179, s[62:63] sc1\n\t"
      "global_load_dwordx4 v[144:147], v180, s[62:63] sc1\n\t"
      "global_load_dwordx4 v[148:151], v181, s[62:63] sc1\n\t"
      "global_load_dwordx4 v[152:155], v182, s[62:63] sc1\n\t"
      "global_load_dwordx4 v[156:159], v183, s[62:63] sc1\n\t"
      "s_waitcnt vmcnt(0)\n\t"
      "s_nop 7\n\t"
      "s_branch NQ4B_%=\n\t"
      "NQ5R_%=:\n\t"
      "s_mov_b32 s51, 0\n\t"
      "NQ5L_%=:\n\t"
      "s_add_u32 s51, s51, 1\n\t"
      "s_cmp_lt_u32 s51, 262144\n\t"
      "s_cbranch_scc0 NQ5X_%=\n\t"
      "s_waitcnt vmcnt(0)\n\t"
      "s_sleep 1\n\t"
      "global_load_dwordx4 v[128:131], v176, s[62:63] sc1\n\t"
      "global_load_dwordx4 v[132:135], v177, s[62:63] sc1\n\t"
      "global_load_dwordx4 v[136:139], v178, s[62:63] sc1\n\t"
      "global_load_dwordx4 v[140:143], v179, s[62:63] sc1\n\t"
      "global_load_dwordx4 v[144:147], v180, s[62:63] sc1\n\t"
      "global_load_dwordx4 v[148:151], v181, s[62:63] sc1\n\t"
      "global_load_dwordx4 v[152:155], v182, s[62:63] sc1\n\t"
      "global_load_dwordx4 v[156:159], v183, s[62:63] sc1\n\t"
      "s_waitcnt vmcnt(0)\n\t"
      "v_max3_u32 v234, v128, v129, v130\n\t"
      "v_max3_u32 v234, v234, v131, v132\n\t"
      "v_max3_u32 v234, v234, v133, v134\n\t"
      "v_max3_u32 v234, v234, v135, v136\n\t"
      "v_max3_u32 v234, v234, v137, v138\n\t"
      "v_max3_u32 v234, v234, v139, v140\n\t"
      "v_max3_u32 v234, v234, v141, v142\n\t"
      "v_max3_u32 v234, v234, v143, v144\n\t"
      "v_max3_u32 v234, v234, v145, v146\n\t"
      "v_max3_u32 v234, v234, v147, v148\n\t"
      "v_max3_u32 v234, v234, v149, v150\n\t"
      "v_max3_u32 v234, v234, v151, v152\n\t"
      "v_max3_u32 v234, v234, v153, v154\n\t"
      "v_max3_u32 v234, v234, v155, v156\n\t"
      "v_max3_u32 v234, v234, v157, v158\n\t"
      "v_max_u32_e32 v234, v234, v159\n\t"
      "v_cmp_eq_u32_e32 vcc, -1, v234\n\t"
      "s_cbranch_vccnz NQ5L_%=\n\t"
      "NQ5X_%=:\n\t"
      "v_mfma_f32_16x16x32_bf16 v[200:203], v[0:3], v[128:131], v[236:239]\n\t"
      "v_mfma_f32_16x16x32_bf16 v[204:207], v[4:7], v[128:131], 0\n\t"
      "v_mfma_f32_16x16x32_bf16 v[208:211], v[8:11], v[128:131], 0\n\t"
      "v_mfma_f32_16x16x32_bf16 v[212:215], v[12:15], v[128:131], 0\n\t"
      "v_mfma_f32_16x16x32_bf16 v[200:203], v[16:19], v[132:135], v[200:203]\n\t"
      "v_mfma_f32_16x16x32_bf16 v[204:207], v[20:23], v[132:135], v[204:207]\n\t"
      "v_mfma_f32_16x16x32_bf16 v[208:211], v[24:27], v[132:135], v[208:211]\n\t"
      "v_mfma_f32_16x16x32_bf16 v[212:215], v[28:31], v[132:135], v[212:215]\n\t"
      "v_mfma_f32_16x16x32_bf16 v[200:203], v[32:35], v[136:139], v[200:203]\n\t"
      "v_mfma_f32_16x16x32_bf16 v[204:207], v[36:39], v[136:139], v[204:207]\n\t"
      "v_mfma_f32_16x16x32_bf16 v[208:211], v[40:43], v[136:139], v[208:211]\n\t"
      "v_mfma_f32_16x16x32_bf16 v[212:215], v[44:47], v[136:139], v[212:215]\n\t"
      "v_mfma_f32_16x16x32_bf16 v[200:203], v[48:51], v[140:143], v[200:203]\n\t"
      "v_mfma_f32_16x16x32_bf16 v[204:207], v[52:55], v[140:143], v[204:207]\n\t"
      "v_mfma_f32_16x16x32_bf16 v[208:211], v[56:59], v[140:143], v[208:211]\n\t"
      "v_mfma_f32_16x16x32_bf16 v[212:215], v[60:63], v[140:143], v[212:215]\n\t"
      "v_mfma_f32_16x16x32_bf16 v[200:203], v[64:67], v[144:147], v[200:203]\n\t"
      "v_mfma_f32_16x16x32_bf16 v[204:207], v[68:71], v[144:147], v[204:207]\n\t"
      "v_mfma_f32_16x16x32_bf16 v[208:211], v[72:75], v[144:147], v[208:211]\n\t"
      "v_mfma_f32_16x16x32_bf16 v[212:215], v[76:79], v[144:147], v[212:215]\n\t"
      "v_mfma_f32_16x16x32_bf16 v[200:203], v[80:83], v[148:151], v[200:203]\n\t"
      "v_mfma_f32_16x16x32_bf16 v[204:207], v[84:87], v[148:151], v[204:207]\n\t"
      "v_mfma_f32_16x16x32_bf16 v[208:211], v[88:91], v[148:151], v[208:211]\n\t"
      "v_mfma_f32_16x16x32_bf16 v[212:215], v[92:95], v[148:151], v[212:215]\n\t"
      "v_mfma_f32_16x16x32_bf16 v[200:203], v[96:99], v[152:155], v[200:203]\n\t"
      "v_mfma_f32_16x16x32_bf16 v[204:207], v[100:103], v[152:155], v[204:207]\n\t"
      "v_mfma_f32_16x16x32_bf16 v[208:211], v[104:107], v[152:155], v[208:211]\n\t"
      "v_mfma_f32_16x16x32_bf16 v[212:215], v[108:111], v[152:155], v[212:215]\n\t"
      "v_mfma_f32_16x16x32_bf16 v[200:203], v[112:115], v[156:159], v[200:203]\n\t"
      "v_mfma_f32_16x16x32_bf16 v[204:207], v[116:119], v[156:159], v[204:207]\n\t"
      "v_mfma_f32_16x16x32_bf16 v[208:211], v[120:123], v[156:159], v[208:211]\n\t"
      "v_mfma_f32_16x16x32_bf16 v[212:215], v[124:127], v[156:159], v[212:215]\n\t"
      "global_load_dwordx4 v[128:131], v176, s[64:65] sc1\n\t"
      "global_load_dwordx4 v[132:135], v177, s[64:65] sc1\n\t"
      "global_load_dwordx4 v[136:139], v178, s[64:65] sc1\n\t"
      "global_load_dwordx4 v[140:143], v179, s[64:65] sc1\n\t"
      "global_load_dwordx4 v[144:147], v180, s[64:65] sc1\n\t"
      "global_load_dwordx4 v[148:151], v181, s[64:65] sc1\n\t"
      "global_load_dwordx4 v[152:155], v182, s[64:65] sc1\n\t"
      "global_load_dwordx4 v[156:159], v183, s[64:65] sc1\n\t"
      "s_waitcnt vmcnt(0)\n\t"
      "s_nop 7\n\t"
      "s_branch NQ5B_%=\n\t"
      "NQ6R_%=:\n\t"
      "s_mov_b32 s51, 0\n\t"
      "NQ6L_%=:\n\t"
      "s_add_u32 s51, s51, 1\n\t"
      "s_cmp_lt_u32 s51, 262144\n\t"
      "s_cbranch_scc0 NQ6X_%=\n\t"
      "s_waitcnt vmcnt(0)\n\t"
      "s_sleep 1\n\t"
      "global_load_dwordx4 v[128:131], v176, s[64:65] sc1\n\t"
      "global_load_dwordx4 v[132:135], v177, s[64:65] sc1\n\t"
      "global_load_dwordx4 v[136:139], v178, s[64:65] sc1\n\t"
      "global_load_dwordx4 v[140:143], v179, s[64:65] sc1\n\t"
      "global_load_dwordx4 v[144:147], v180, s[64:65] sc1\n\t"
      "global_load_dwordx4 v[148:151], v181, s[64:65] sc1\n\t"
      "global_load_dwordx4 v[152:155], v182, s[64:65] sc1\n\t"
      "global_load_dwordx4 v[156:159], v183, s[64:65] sc1\n\t"
      "s_waitcnt vmcnt(0)\n\t"
      "v_max3_u32 v234, v128, v129, v130\n\t"
      "v_max3_u32 v234, v234, v131, v132\n\t"
      "v_max3_u32 v234, v234, v133, v134\n\t"
      "v_max3_u32 v234, v234, v135, v136\n\t"
      "v_max3_u32 v234, v234, v137, v138\n\t"
      "v_max3_u32 v234, v234, v139, v140\n\t"
      "v_max3_u32 v234, v234, v141, v142\n\t"
      "v_max3_u32 v234, v234, v143, v144\n\t"
      "v_max3_u32 v234, v234, v145, v146\n\t"
      "v_max3_u32 v234, v234, v147, v148\n\t"
      "v_max3_u32 v234, v234, v149, v150\n\t"
      "v_max3_u32 v234, v234, v151, v152\n\t"
      "v_max3_u32 v234, v234, v153, v154\n\t"
      "v_max3_u32 v234, v234, v155, v156\n\t"
      "v_max3_u32 v234, v234, v157, v158\n\t"
      "v_max_u32_e32 v234, v234, v159\n\t"
      "v_cmp_eq_u32_e32 vcc, -1, v234\n\t"
      "s_cbranch_vccnz NQ6L_%=\n\t"
      "NQ6X_%=:\n\t"
      "v_mfma_f32_16x16x32_bf16 v[184:187], v[0:3], v[128:131], v[236:239]\n\t"
      "v_mfma_f32_16x16x32_bf16 v[188:191], v[4:7], v[128:131], 0\n\t"
      "v_mfma_f32_16x16x32_bf16 v[192:195], v[8:11], v[128:131], 0\n\t"
      "v_mfma_f32_16x16x32_bf16 v[196:199], v[12:15], v[128:131], 0\n\t"
      "v_mfma_f32_16x16x32_bf16 v[184:187], v[16:19], v[132:135], v[184:187]\n\t"
      "v_mfma_f32_16x16x32_bf16 v[188:191], v[20:23], v[132:135], v[188:191]\n\t"
      "v_mfma_f32_16x16x32_bf16 v[192:195], v[24:27], v[132:135], v[192:195]\n\t"
      "v_mfma_f32_16x16x32_bf16 v[196:199], v[28:31], v[132:135], v[196:199]\n\t"
      "v_mfma_f32_16x16x32_bf16 v[184:187], v[32:35], v[136:139], v[184:187]\n\t"
      "v_mfma_f32_16x16x32_bf16 v[188:191], v[36:39], v[136:139], v[188:191]\n\t"
      "v_mfma_f32_16x16x32_bf16 v[192:195], v[40:43], v[136:139], v[192:195]\n\t"
      "v_mfma_f32_16x16x32_bf16 v[196:199], v[44:47], v[136:139], v[196:199]\n\t"
      "v_mfma_f32_16x16x32_bf16 v[184:187], v[48:51], v[140:143], v[184:187]\n\t"
      "v_mfma_f32_16x16x32_bf16 v[188:191], v[52:55], v[140:143], v[188:191]\n\t"
      "v_mfma_f32_16x16x32_bf16 v[192:195], v[56:59], v[140:143], v[192:195]\n\t"
      "v_mfma_f32_16x16x32_bf16 v[196:199], v[60:63], v[140:143], v[196:199]\n\t"
      "v_mfma_f32_16x16x32_bf16 v[184:187], v[64:67], v[144:147], v[184:187]\n\t"
      "v_mfma_f32_16x16x32_bf16 v[188:191], v[68:71], v[144:147], v[188:191]\n\t"
      "v_mfma_f32_16x16x32_bf16 v[192:195], v[72:75], v[144:147], v[192:195]\n\t"
      "v_mfma_f32_16x16x32_bf16 v[196:199], v[76:79], v[144:147], v[196:199]\n\t"
      "v_mfma_f32_16x16x32_bf16 v[184:187], v[80:83], v[148:151], v[184:187]\n\t"
      "v_mfma_f32_16x16x32_bf16 v[188:191], v[84:87], v[148:151], v[188:191]\n\t"
      "v_mfma_f32_16x16x32_bf16 v[192:195], v[88:91], v[148:151], v[192:195]\n\t"
      "v_mfma_f32_16x16x32_bf16 v[196:199], v[92:95], v[148:151], v[196:199]\n\t"
      "v_mfma_f32_16x16x32_bf16 v[184:187], v[96:99], v[152:155], v[184:187]\n\t"
      "v_mfma_f32_16x16x32_bf16 v[188:191], v[100:103], v[152:155], v[188:191]\n\t"
      "v_mfma_f32_16x16x32_bf16 v[192:195], v[104:107], v[152:155], v[192:195]\n\t"
      "v_mfma_f32_16x16x32_bf16 v[196:199], v[108:111], v[152:155], v[196:199]\n\t"
      "v_mfma_f32_16x16x32_bf16 v[184:187], v[112:115], v[156:159], v[184:187]\n\t"
      "v_mfma_f32_16x16x32_bf16 v[188:191], v[116:119], v[156:159], v[188:191]\n\t"
      "v_mfma_f32_16x16x32_bf16 v[192:195], v[120:123], v[156:159], v[192:195]\n\t"
      "v_mfma_f32_16x16x32_bf16 v[196:199], v[124:127], v[156:159], v[196:199]\n\t"
      "s_waitcnt vmcnt(0)\n\t"
      "s_nop 7\n\t"
      "s_branch NQ6B_%=\n\t"
      "FP0R_%=:\n\t"
      "s_mov_b32 s51, 0\n\t"
      "FP0L_%=:\n\t"
      "s_add_u32 s51, s51, 1\n\t"
      "s_cmp_lt_u32 s51, 262144\n\t"
      "s_cbranch_scc0 FP0X_%=\n\t"
      "s_waitcnt vmcnt(0)\n\t"
      "s_sleep 1\n\t"
      "global_load_dwordx4 v[128:131], v176, s[52:53] sc1\n\t"
      "global_load_dwordx4 v[132:135], v177, s[52:53] sc1\n\t"
      "global_load_dwordx4 v[136:139], v178, s[52:53] sc1\n\t"
      "global_load_dwordx4 v[140:143], v179, s[52:53] sc1\n\t"
      "global_load_dwordx4 v[144:147], v180, s[52:53] sc1\n\t"
      "global_load_dwordx4 v[148:151], v181, s[52:53] sc1\n\t"
      "global_load_dwordx4 v[152:155], v182, s[52:53] sc1\n\t"
      "global_load_dwordx4 v[156:159], v183, s[52:53] sc1\n\t"
      "s_waitcnt vmcnt(7)\n\t"
      "v_max3_u32 v234, v128, v129, v130\n\t"
      "v_max_u32_e32 v234, v234, v131\n\t"
      "v_cmp_eq_u32_e32 vcc, -1, v234\n\t"
      "s_cbranch_vccnz FP0L_%=\n\t"
      "FP0X_%=:\n\t"
      "s_branch FP0B_%=\n\t"
      "FP1R_%=:\n\t"
      "s_mov_b32 s51, 0\n\t"
      "FP1L_%=:\n\t"
      "s_add_u32 s51, s51, 1\n\t"
      "s_cmp_lt_u32 s51, 262144\n\t"
      "s_cbranch_scc0 FP1X_%=\n\t"
      "s_waitcnt vmcnt(0)\n\t"
      "s_sleep 1\n\t"
      "global_load_dwordx4 v[132:135], v177, s[52:53] sc1\n\t"
      "global_load_dwordx4 v[136:139], v178, s[52:53] sc1\n\t"
      "global_load_dwordx4 v[140:143], v179, s[52:53] sc1\n\t"
      "global_load_dwordx4 v[144:147], v180, s[52:53] sc1\n\t"
      "global_load_dwordx4 v[148:151], v181, s[52:53] sc1\n\t"
      "global_load_dwordx4 v[152:155], v182, s[52:53] sc1\n\t"
      "global_load_dwordx4 v[156:159], v183, s[52:53] sc1\n\t"
      "global_load_dwordx4 v[128:131], v176, s[54:55] sc1\n\t"
      "s_waitcnt vmcnt(7)\n\t"
      "v_max3_u32 v234, v132, v133, v134\n\t"
      "v_max_u32_e32 v234, v234, v135\n\t"
      "v_cmp_eq_u32_e32 vcc, -1, v234\n\t"
      "s_cbranch_vccnz FP1L_%=\n\t"
      "FP1X_%=:\n\t"
      "s_branch FP1B_%=\n\t"
      "FP2R_%=:\n\t"
      "s_mov_b32 s51, 0\n\t"
      "FP2L_%=:\n\t"
      "s_add_u32 s51, s51, 1\n\t"
      "s_cmp_lt_u32 s51, 262144\n\t"
      "s_cbranch_scc0 FP2X_%=\n\t"
      "s_waitcnt vmcnt(0)\n\t"
      "s_sleep 1\n\t"
      "global_load_dwordx4 v[136:139], v178, s[52:53] sc1\n\t"
      "global_load_dwordx4 v[140:143], v179, s[52:53] sc1\n\t"
      "global_load_dwordx4 v[144:147], v180, s[52:53] sc1\n\t"
      "global_load_dwordx4 v[148:151], v181, s[52:53] sc1\n\t"
      "global_load_dwordx4 v[152:155], v182, s[52:53] sc1\n\t"
      "global_load_dwordx4 v[156:159], v183, s[52:53] sc1\n\t"
      "global_load_dwordx4 v[128:131], v176, s[54:55] sc1\n\t"
      "global_load_dwordx4 v[132:135], v177, s[54:55] sc1\n\t"
      "s_waitcnt vmcnt(7)\n\t"
      "v_max3_u32 v234, v136, v137, v138\n\t"
      "v_max_u32_e32 v234, v234, v139\n\t"
      "v_cmp_eq_u32_e32 vcc, -1, v234\n\t"
      "s_cbranch_vccnz FP2L_%=\n\t"
      "FP2X_%=:\n\t"
      "s_branch FP2B_%=\n\t"
      "FP3R_%=:\n\t"
      "s_mov_b32 s51, 0\n\t"
      "FP3L_%=:\n\t"
      "s_add_u32 s51, s51, 1\n\t"
      "s_cmp_lt_u32 s51, 262144\n\t"
      "s_cbranch_scc0 FP3X_%=\n\t"
      "s_waitcnt vmcnt(0)\n\t"
      "s_sleep 1\n\t"
      "global_load_dwordx4 v[140:143], v179, s[52:53] sc1\n\t"
      "global_load_dwordx4 v[144:147], v180, s[52:53] sc1\n\t"
      "global_load_dwordx4 v[148:151], v181, s[52:53] sc1\n\t"
      "global_load_dwordx4 v[152:155], v182, s[52:53] sc1\n\t"
      "global_load_dwordx4 v[156:159], v183, s[52:53] sc1\n\t"
      "global_load_dwordx4 v[128:131], v176, s[54:55] sc1\n\t"
      "global_load_dwordx4 v[132:135], v177, s[54:55] sc1\n\t"
      "global_load_dwordx4 v[136:139], v178, s[54:55] sc1\n\t"
      "s_waitcnt vmcnt(7)\n\t"
      "v_max3_u32 v234, v140, v141, v142\n\t"
      "v_max_u32_e32 v234, v234, v143\n\t"
      "v_cmp_eq_u32_e32 vcc, -1, v234\n\t"
      "s_cbranch_vccnz FP3L_%=\n\t"
      "FP3X_%=:\n\t"
      "s_branch FP3B_%=\n\t"
      "FP4R_%=:\n\t"
      "s_mov_b32 s51, 0\n\t"
      "FP4L_%=:\n\t"
      "s_add_u32 s51, s51, 1\n\t"
      "s_cmp_lt_u32 s51, 262144\n\t"
      "s_cbranch_scc0 FP4X_%=\n\t"
      "s_waitcnt vmcnt(0)\n\t"
      "s_sleep 1\n\t"
      "global_load_dwordx4 v[144:147], v180, s[52:53] sc1\n\t"
      "global_load_dwordx4 v[148:151], v181, s[52:53] sc1\n\t"
      "global_load_dwordx4 v[152:155], v182, s[52:53] sc1\n\t"
      "global_load_dwordx4 v[156:159], v183, s[52:53] sc1\n\t"
      "global_load_dwordx4 v[128:131], v176, s[54:55] sc1\n\t"
      "global_load_dwordx4 v[132:135], v177, s[54:55] sc1\n\t"
      "global_load_dwordx4 v[136:139], v178, s[54:55] sc1\n\t"
      "global_load_dwordx4 v[140:143], v179, s[54:55] sc1\n\t"
      "s_waitcnt vmcnt(7)\n\t"
      "v_max3_u32 v234, v144, v145, v146\n\t"
      "v_max_u32_e32 v234, v234, v147\n\t"
      "v_cmp_eq_u32_e32 vcc, -1, v234\n\t"
      "s_cbranch_vccnz FP4L_%=\n\t"
      "FP4X_%=:\n\t"
      "s_branch FP4B_%=\n\t"
      "FP5R_%=:\n\t"
      "s_mov_b32 s51, 0\n\t"
      "FP5L_%=:\n\t"
      "s_add_u32 s51, s51, 1\n\t"
      "s_cmp_lt_u32 s51, 262144\n\t"
      "s_cbranch_scc0 FP5X_%=\n\t"
      "s_waitcnt vmcnt(0)\n\t"
      "s_sleep 1\n\t"
      "global_load_dwordx4 v[148:151], v181, s[52:53] sc1\n\t"
      "global_load_dwordx4 v[152:155], v182, s[52:53] sc1\n\t"
      "global_load_dwordx4 v[156:159], v183, s[52:53] sc1\n\t"
      "global_load_dwordx4 v[128:131], v176, s[54:55] sc1\n\t"
      "global_load_dwordx4 v[132:135], v177, s[54:55] sc1\n\t"
      "global_load_dwordx4 v[136:139], v178, s[54:55] sc1\n\t"
      "global_load_dwordx4 v[140:143], v179, s[54:55] sc1\n\t"
      "global_load_dwordx4 v[144:147], v180, s[54:55] sc1\n\t"
      "s_waitcnt vmcnt(7)\n\t"
      "v_max3_u32 v234, v148, v149, v150\n\t"
      "v_max_u32_e32 v234, v234, v151\n\t"
      "v_cmp_eq_u32_e32 vcc, -1, v234\n\t"
      "s_cbranch_vccnz FP5L_%=\n\t"
      "FP5X_%=:\n\t"
      "s_branch FP5B_%=\n\t"
      "FP6R_%=:\n\t"
      "s_mov_b32 s51, 0\n\t"
      "FP6L_%=:\n\t"
      "s_add_u32 s51, s51, 1\n\t"
      "s_cmp_lt_u32 s51, 262144\n\t"
      "s_cbranch_scc0 FP6X_%=\n\t"
      "s_waitcnt vmcnt(0)\n\t"
      "s_sleep 1\n\t"
      "global_load_dwordx4 v[152:155], v182, s[52:53] sc1\n\t"
      "global_load_dwordx4 v[156:159], v183, s[52:53] sc1\n\t"
      "global_load_dwordx4 v[128:131], v176, s[54:55] sc1\n\t"
      "global_load_dwordx4 v[132:135], v177, s[54:55] sc1\n\t"
      "global_load_dwordx4 v[136:139], v178, s[54:55] sc1\n\t"
      "global_load_dwordx4 v[140:143], v179, s[54:55] sc1\n\t"
      "global_load_dwordx4 v[144:147], v180, s[54:55] sc1\n\t"
      "global_load_dwordx4 v[148:151], v181, s[54:55] sc1\n\t"
      "s_waitcnt vmcnt(7)\n\t"
      "v_max3_u32 v234, v152, v153, v154\n\t"
      "v_max_u32_e32 v234, v234, v155\n\t"
      "v_cmp_eq_u32_e32 vcc, -1, v234\n\t"
      "s_cbranch_vccnz FP6L_%=\n\t"
      "FP6X_%=:\n\t"
      "s_branch FP6B_%=\n\t"
      "FP7R_%=:\n\t"
      "s_mov_b32 s51, 0\n\t"
      "FP7L_%=:\n\t"
      "s_add_u32 s51, s51, 1\n\t"
      "s_cmp_lt_u32 s51, 262144\n\t"
      "s_cbranch_scc0 FP7X_%=\n\t"
      "s_waitcnt vmcnt(0)\n\t"
      "s_sleep 1\n\t"
      "global_load_dwordx4 v[156:159], v183, s[52:53] sc1\n\t"
      "global_load_dwordx4 v[128:131], v176, s[54:55] sc1\n\t"
      "global_load_dwordx4 v[132:135], v177, s[54:55] sc1\n\t"
      "global_load_dwordx4 v[136:139], v178, s[54:55] sc1\n\t"
      "global_load_dwordx4 v[140:143], v179, s[54:55] sc1\n\t"
      "global_load_dwordx4 v[144:147], v180, s[54:55] sc1\n\t"
      "global_load_dwordx4 v[148:151], v181, s[54:55] sc1\n\t"
      "global_load_dwordx4 v[152:155], v182, s[54:55] sc1\n\t"
      "s_waitcnt vmcnt(7)\n\t"
      "v_max3_u32 v234, v156, v157, v158\n\t"
      "v_max_u32_e32 v234, v234, v159\n\t"
      "v_cmp_eq_u32_e32 vcc, -1, v234\n\t"
      "s_cbranch_vccnz FP7L_%=\n\t"
      "FP7X_%=:\n\t"
      "s_branch FP7B_%=\n\t"
      "NP1R_%=:\n\t"
      "s_mov_b32 s51, 0\n\t"
      "NP1L_%=:\n\t"
      "s_add_u32 s51, s51, 1\n\t"
      "s_cmp_lt_u32 s51, 262144\n\t"
      "s_cbranch_scc0 NP1X_%=\n\t"
      "s_waitcnt vmcnt(0)\n\t"
      "s_sleep 1\n\t"
      "global_load_dwordx4 v[128:131], v176, s[54:55] sc1\n\t"
      "global_load_dwordx4 v[132:135], v177, s[54:55] sc1\n\t"
      "global_load_dwordx4 v[136:139], v178, s[54:55] sc1\n\t"
      "global_load_dwordx4 v[140:143], v179, s[54:55] sc1\n\t"
      "global_load_dwordx4 v[144:147], v180, s[54:55] sc1\n\t"
      "global_load_dwordx4 v[148:151], v181, s[54:55] sc1\n\t"
      "global_load_dwordx4 v[152:155], v182, s[54:55] sc1\n\t"
      "global_load_dwordx4 v[156:159], v183, s[54:55] sc1\n\t"
      "s_waitcnt vmcnt(0)\n\t"
      "v_max3_u32 v234, v128, v129, v130\n\t"
      "v_max3_u32 v234, v234, v131, v132\n\t"
      "v_max3_u32 v234, v234, v133, v134\n\t"
      "v_max3_u32 v234, v234, v135, v136\n\t"
      "v_max3_u32 v234, v234, v137, v138\n\t"
      "v_max3_u32 v234, v234, v139, v140\n\t"
      "v_max3_u32 v234, v234, v141, v142\n\t"
      "v_max3_u32 v234, v234, v143, v144\n\t"
      "v_max3_u32 v234, v234, v145, v146\n\t"
      "v_max3_u32 v234, v234, v147, v148\n\t"
      "v_max3_u32 v234, v234, v149, v150\n\t"
      "v_max3_u32 v234, v234, v151, v152\n\t"
      "v_max3_u32 v234, v234, v153, v154\n\t"
      "v_max3_u32 v234, v234, v155, v156\n\t"
      "v_max3_u32 v234, v234, v157, v158\n\t"
      "v_max_u32_e32 v234, v234, v159\n\t"
      "v_cmp_eq_u32_e32 vcc, -1, v234\n\t"
      "s_cbranch_vccnz NP1L_%=\n\t"
      "NP1X_%=:\n\t"
      "v_mfma_f32_16x16x32_bf16 v[200:203], v[0:3], v[128:131], 0\n\t"
      "v_mfma_f32_16x16x32_bf16 v[204:207], v[4:7], v[128:131], 0\n\t"
      "v_mfma_f32_16x16x32_bf16 v[208:211], v[8:11], v[128:131], 0\n\t"
      "v_mfma_f32_16x16x32_bf16 v[212:215], v[12:15], v[128:131], 0\n\t"
      "v_mfma_f32_16x16x32_bf16 v[200:203], v[16:19], v[132:135], v[200:203]\n\t"
      "v_mfma_f32_16x16x32_bf16 v[204:207], v[20:23], v[132:135], v[204:207]\n\t"
      "v_mfma_f32_16x16x32_bf16 v[208:211], v[24:27], v[132:135], v[208:211]\n\t"
      "v_mfma_f32_16x16x32_bf16 v[212:215], v[28:31], v[132:135], v[212:215]\n\t"
      "v_mfma_f32_16x16x32_bf16 v[200:203], v[32:35], v[136:139], v[200:203]\n\t"
      "v_mfma_f32_16x16x32_bf16 v[204:207], v[36:39], v[136:139], v[204:207]\n\t"
      "v_mfma_f32_16x16x32_bf16 v[208:211], v[40:43], v[136:139], v[208:211]\n\t"
      "v_mfma_f32_16x16x32_bf16 v[212:215], v[44:47], v[136:139], v[212:215]\n\t"
      "v_mfma_f32_16x16x32_bf16 v[200:203], v[48:51], v[140:143], v[200:203]\n\t"
      "v_mfma_f32_16x16x32_bf16 v[204:207], v[52:55], v[140:143], v[204:207]\n\t"
      "v_mfma_f32_16x16x32_bf16 v[208:211], v[56:59], v[140:143], v[208:211]\n\t"
      "v_mfma_f32_16x16x32_bf16 v[212:215], v[60:63], v[140:143], v[212:215]\n\t"
      "v_mfma_f32_16x16x32_bf16 v[200:203], v[64:67], v[144:147], v[200:203]\n\t"
      "v_mfma_f32_16x16x32_bf16 v[204:207], v[68:71], v[144:147], v[204:207]\n\t"
      "v_mfma_f32_16x16x32_bf16 v[208:211], v[72:75], v[144:147], v[208:211]\n\t"
      "v_mfma_f32_16x16x32_bf16 v[212:215], v[76:79], v[144:147], v[212:215]\n\t"
      "v_mfma_f32_16x16x32_bf16 v[200:203], v[80:83], v[148:151], v[200:203]\n\t"
      "v_mfma_f32_16x16x32_bf16 v[204:207], v[84:87], v[148:151], v[204:207]\n\t"
      "v_mfma_f32_16x16x32_bf16 v[208:211], v[88:91], v[148:151], v[208:211]\n\t"
      "v_mfma_f32_16x16x32_bf16 v[212:215], v[92:95], v[148:151], v[212:215]\n\t"
      "v_mfma_f32_16x16x32_bf16 v[200:203], v[96:99], v[152:155], v[200:203]\n\t"
      "v_mfma_f32_16x16x32_bf16 v[204:207], v[100:103], v[152:155], v[204:207]\n\t"
      "v_mfma_f32_16x16x32_bf16 v[208:211], v[104:107], v[152:155], v[208:211]\n\t"
      "v_mfma_f32_16x16x32_bf16 v[212:215], v[108:111], v[152:155], v[212:215]\n\t"
      "v_mfma_f32_16x16x32_bf16 v[200:203], v[112:115], v[156:159], v[200:203]\n\t"
      "v_mfma_f32_16x16x32_bf16 v[204:207], v[116:119], v[156:159], v[204:207]\n\t"
      "v_mfma_f32_16x16x32_bf16 v[208:211], v[120:123], v[156:159], v[208:211]\n\t"
      "v_mfma_f32_16x16x32_bf16 v[212:215], v[124:127], v[156:159], v[212:215]\n\t"
      "global_load_dwordx4 v[128:131], v176, s[56:57] sc1\n\t"
      "global_load_dwordx4 v[132:135], v177, s[56:57] sc1\n\t"
      "global_load_dwordx4 v[136:139], v178, s[56:57] sc1\n\t"
      "global_load_dwordx4 v[140:143], v179, s[56:57] sc1\n\t"
      "global_load_dwordx4 v[144:147], v180, s[56:57] sc1\n\t"
      "global_load_dwordx4 v[148:151], v181, s[56:57] sc1\n\t"
      "global_load_dwordx4 v[152:155], v182, s[56:57] sc1\n\t"
      "global_load_dwordx4 v[156:159], v183, s[56:57] sc1\n\t"
      "s_waitcnt vmcnt(0)\n\t"
      "s_nop 7\n\t"
      "s_branch NP1B_%=\n\t"
      "NP2R_%=:\n\t"
      "s_mov_b32 s51, 0\n\t"
      "NP2L_%=:\n\t"
      "s_add_u32 s51, s51, 1\n\t"
      "s_cmp_lt_u32 s51, 262144\n\t"
      "s_cbranch_scc0 NP2X_%=\n\t"
      "s_waitcnt vmcnt(0)\n\t"
      "s_sleep 1\n\t"
      "global_load_dwordx4 v[128:131], v176, s[56:57] sc1\n\t"
      "global_load_dwordx4 v[132:135], v177, s[56:57] sc1\n\t"
      "global_load_dwordx4 v[136:139], v178, s[56:57] sc1\n\t"
      "global_load_dwordx4 v[140:143], v179, s[56:57] sc1\n\t"
      "global_load_dwordx4 v[144:147], v180, s[56:57] sc1\n\t"
      "global_load_dwordx4 v[148:151], v181, s[56:57] sc1\n\t"
      "global_load_dwordx4 v[152:155], v182, s[56:57] sc1\n\t"
      "global_load_dwordx4 v[156:159], v183, s[56:57] sc1\n\t"
      "s_waitcnt vmcnt(0)\n\t"
      "v_max3_u32 v234, v128, v129, v130\n\t"
      "v_max3_u32 v234, v234, v131, v132\n\t"
      "v_max3_u32 v234, v234, v133, v134\n\t"
      "v_max3_u32 v234, v234, v135, v136\n\t"
      "v_max3_u32 v234, v234, v137, v138\n\t"
      "v_max3_u32 v234, v234, v139, v140\n\t"
      "v_max3_u32 v234, v234, v141, v142\n\t"
      "v_max3_u32 v234, v234, v143, v144\n\t"
      "v_max3_u32 v234, v234, v145, v146\n\t"
      "v_max3_u32 v234, v234, v147, v148\n\t"
      "v_max3_u32 v234, v234, v149, v150\n\t"
      "v_max3_u32 v234, v234, v151, v152\n\t"
      "v_max3_u32 v234, v234, v153, v154\n\t"
      "v_max3_u32 v234, v234, v155, v156\n\t"
      "v_max3_u32 v234, v234, v157, v158\n\t"
      "v_max_u32_e32 v234, v234, v159\n\t"
      "v_cmp_eq_u32_e32 vcc, -1, v234\n\t"
      "s_cbranch_vccnz NP2L_%=\n\t"
      "NP2X_%=:\n\t"
      "v_mfma_f32_16x16x32_bf16 v[184:187], v[0:3], v[128:131], 0\n\t"
      "v_mfma_f32_16x16x32_bf16 v[188:191], v[4:7], v[128:131], 0\n\t"
      "v_mfma_f32_16x16x32_bf16 v[192:195], v[8:11], v[128:131], 0\n\t"
      "v_mfma_f32_16x16x32_bf16 v[196:199], v[12:15], v[128:131], 0\n\t"
      "v_mfma_f32_16x16x32_bf16 v[184:187], v[16:19], v[132:135], v[184:187]\n\t"
      "v_mfma_f32_16x16x32_bf16 v[188:191], v[20:23], v[132:135], v[188:191]\n\t"
      "v_mfma_f32_16x16x32_bf16 v[192:195], v[24:27], v[132:135], v[192:195]\n\t"
      "v_mfma_f32_16x16x32_bf16 v[196:199], v[28:31], v[132:135], v[196:199]\n\t"
      "v_mfma_f32_16x16x32_bf16 v[184:187], v[32:35], v[136:139], v[184:187]\n\t"
      "v_mfma_f32_16x16x32_bf16 v[188:191], v[36:39], v[136:139], v[188:191]\n\t"
      "v_mfma_f32_16x16x32_bf16 v[192:195], v[40:43], v[136:139], v[192:195]\n\t"
      "v_mfma_f32_16x16x32_bf16 v[196:199], v[44:47], v[136:139], v[196:199]\n\t"
      "v_mfma_f32_16x16x32_bf16 v[184:187], v[48:51], v[140:143], v[184:187]\n\t"
      "v_mfma_f32_16x16x32_bf16 v[188:191], v[52:55], v[140:143], v[188:191]\n\t"
      "v_mfma_f32_16x16x32_bf16 v[192:195], v[56:59], v[140:143], v[192:195]\n\t"
      "v_mfma_f32_16x16x32_bf16 v[196:199], v[60:63], v[140:143], v[196:199]\n\t"
      "v_mfma_f32_16x16x32_bf16 v[184:187], v[64:67], v[144:147], v[184:187]\n\t"
      "v_mfma_f32_16x16x32_bf16 v[188:191], v[68:71], v[144:147], v[188:191]\n\t"
      "v_mfma_f32_16x16x32_bf16 v[192:195], v[72:75], v[144:147], v[192:195]\n\t"
      "v_mfma_f32_16x16x32_bf16 v[196:199], v[76:79], v[144:147], v[196:199]\n\t"
      "v_mfma_f32_16x16x32_bf16 v[184:187], v[80:83], v[148:151], v[184:187]\n\t"
      "v_mfma_f32_16x16x32_bf16 v[188:191], v[84:87], v[148:151], v[188:191]\n\t"
      "v_mfma_f32_16x16x32_bf16 v[192:195], v[88:91], v[148:151], v[192:195]\n\t"
      "v_mfma_f32_16x16x32_bf16 v[196:199], v[92:95], v[148:151], v[196:199]\n\t"
      "v_mfma_f32_16x16x32_bf16 v[184:187], v[96:99], v[152:155], v[184:187]\n\t"
      "v_mfma_f32_16x16x32_bf16 v[188:191], v[100:103], v[152:155], v[188:191]\n\t"
      "v_mfma_f32_16x16x32_bf16 v[192:195], v[104:107], v[152:155], v[192:195]\n\t"
      "v_mfma_f32_16x16x32_bf16 v[196:199], v[108:111], v[152:155], v[196:199]\n\t"
      "v_mfma_f32_16x16x32_bf16 v[184:187], v[112:115], v[156:159], v[184:187]\n\t"
      "v_mfma_f32_16x16x32_bf16 v[188:191], v[116:119], v[156:159], v[188:191]\n\t"
      "v_mfma_f32_16x16x32_bf16 v[192:195], v[120:123], v[156:159], v[192:195]\n\t"
      "v_mfma_f32_16x16x32_bf16 v[196:199], v[124:127], v[156:159], v[196:199]\n\t"
      "global_load_dwordx4 v[128:131], v176, s[58:59] sc1\n\t"
      "global_load_dwordx4 v[132:135], v177, s[58:59] sc1\n\t"
      "global_load_dwordx4 v[136:139], v178, s[58:59] sc1\n\t"
      "global_load_dwordx4 v[140:143], v179, s[58:59] sc1\n\t"
      "global_load_dwordx4 v[144:147], v180, s[58:59] sc1\n\t"
      "global_load_dwordx4 v[148:151], v181, s[58:59] sc1\n\t"
      "global_load_dwordx4 v[152:155], v182, s[58:59] sc1\n\t"
      "global_load_dwordx4 v[156:159], v183, s[58:59] sc1\n\t"
      "s_waitcnt vmcnt(0)\n\t"
      "s_nop 7\n\t"
      "s_branch NP2B_%=\n\t"
      "NP3R_%=:\n\t"
      "s_mov_b32 s51, 0\n\t"
      "NP3L_%=:\n\t"
      "s_add_u32 s51, s51, 1\n\t"
      "s_cmp_lt_u32 s51, 262144\n\t"
      "s_cbranch_scc0 NP3X_%=\n\t"
      "s_waitcnt vmcnt(0)\n\t"
      "s_sleep 1\n\t"
      "global_load_dwordx4 v[128:131], v176, s[58:59] sc1\n\t"
      "global_load_dwordx4 v[132:135], v177, s[58:59] sc1\n\t"
      "global_load_dwordx4 v[136:139], v178, s[58:59] sc1\n\t"
      "global_load_dwordx4 v[140:143], v179, s[58:59] sc1\n\t"
      "global_load_dwordx4 v[144:147], v180, s[58:59] sc1\n\t"
      "global_load_dwordx4 v[148:151], v181, s[58:59] sc1\n\t"
      "global_load_dwordx4 v[152:155], v182, s[58:59] sc1\n\t"
      "global_load_dwordx4 v[156:159], v183, s[58:59] sc1\n\t"
      "s_waitcnt vmcnt(0)\n\t"
      "v_max3_u32 v234, v128, v129, v130\n\t"
      "v_max3_u32 v234, v234, v131, v132\n\t"
      "v_max3_u32 v234, v234, v133, v134\n\t"
      "v_max3_u32 v234, v234, v135, v136\n\t"
      "v_max3_u32 v234, v234, v137, v138\n\t"
      "v_max3_u32 v234, v234, v139, v140\n\t"
      "v_max3_u32 v234, v234, v141, v142\n\t"
      "v_max3_u32 v234, v234, v143, v144\n\t"
      "v_max3_u32 v234, v234, v145, v146\n\t"
      "v_max3_u32 v234, v234, v147, v148\n\t"
      "v_max3_u32 v234, v234, v149, v150\n\t"
      "v_max3_u32 v234, v234, v151, v152\n\t"
      "v_max3_u32 v234, v234, v153, v154\n\t"
      "v_max3_u32 v234, v234, v155, v156\n\t"
      "v_max3_u32 v234, v234, v157, v158\n\t"
      "v_max_u32_e32 v234, v234, v159\n\t"
      "v_cmp_eq_u32_e32 vcc, -1, v234\n\t"
      "s_cbranch_vccnz NP3L_%=\n\t"
      "NP3X_%=:\n\t"
      "v_mfma_f32_16x16x32_bf16 v[200:203], v[0:3], v[128:131], 0\n\t"
      "v_mfma_f32_16x16x32_bf16 v[204:207], v[4:7], v[128:131], 0\n\t"
      "v_mfma_f32_16x16x32_bf16 v[208:211], v[8:11], v[128:131], 0\n\t"
      "v_mfma_f32_16x16x32_bf16 v[212:215], v[12:15], v[128:131], 0\n\t"
      "v_mfma_f32_16x16x32_bf16 v[200:203], v[16:19], v[132:135], v[200:203]\n\t"
      "v_mfma_f32_16x16x32_bf16 v[204:207], v[20:23], v[132:135], v[204:207]\n\t"
      "v_mfma_f32_16x16x32_bf16 v[208:211], v[24:27], v[132:135], v[208:211]\n\t"
      "v_mfma_f32_16x16x32_bf16 v[212:215], v[28:31], v[132:135], v[212:215]\n\t"
      "v_mfma_f32_16x16x32_bf16 v[200:203], v[32:35], v[136:139], v[200:203]\n\t"
      "v_mfma_f32_16x16x32_bf16 v[204:207], v[36:39], v[136:139], v[204:207]\n\t"
      "v_mfma_f32_16x16x32_bf16 v[208:211], v[40:43], v[136:139], v[208:211]\n\t"
      "v_mfma_f32_16x16x32_bf16 v[212:215], v[44:47], v[136:139], v[212:215]\n\t"
      "v_mfma_f32_16x16x32_bf16 v[200:203], v[48:51], v[140:143], v[200:203]\n\t"
      "v_mfma_f32_16x16x32_bf16 v[204:207], v[52:55], v[140:143], v[204:207]\n\t"
      "v_mfma_f32_16x16x32_bf16 v[208:211], v[56:59], v[140:143], v[208:211]\n\t"
      "v_mfma_f32_16x16x32_bf16 v[212:215], v[60:63], v[140:143], v[212:215]\n\t"
      "v_mfma_f32_16x16x32_bf16 v[200:203], v[64:67], v[144:147], v[200:203]\n\t"
      "v_mfma_f32_16x16x32_bf16 v[204:207], v[68:71], v[144:147], v[204:207]\n\t"
      "v_mfma_f32_16x16x32_bf16 v[208:211], v[72:75], v[144:147], v[208:211]\n\t"
      "v_mfma_f32_16x16x32_bf16 v[212:215], v[76:79], v[144:147], v[212:215]\n\t"
      "v_mfma_f32_16x16x32_bf16 v[200:203], v[80:83], v[148:151], v[200:203]\n\t"
      "v_mfma_f32_16x16x32_bf16 v[204:207], v[84:87], v[148:151], v[204:207]\n\t"
      "v_mfma_f32_16x16x32_bf16 v[208:211], v[88:91], v[148:151], v[208:211]\n\t"
      "v_mfma_f32_16x16x32_bf16 v[212:215], v[92:95], v[148:151], v[212:215]\n\t"
      "v_mfma_f32_16x16x32_bf16 v[200:203], v[96:99], v[152:155], v[200:203]\n\t"
      "v_mfma_f32_16x16x32_bf16 v[204:207], v[100:103], v[152:155], v[204:207]\n\t"
      "v_mfma_f32_16x16x32_bf16 v[208:211], v[104:107], v[152:155], v[208:211]\n\t"
      "v_mfma_f32_16x16x32_bf16 v[212:215], v[108:111], v[152:155], v[212:215]\n\t"
      "v_mfma_f32_16x16x32_bf16 v[200:203], v[112:115], v[156:159], v[200:203]\n\t"
      "v_mfma_f32_16x16x32_bf16 v[204:207], v[116:119], v[156:159], v[204:207]\n\t"
      "v_mfma_f32_16x16x32_bf16 v[208:211], v[120:123], v[156:159], v[208:211]\n\t"
      "v_mfma_f32_16x16x32_bf16 v[212:215], v[124:127], v[156:159], v[212:215]\n\t"
      "global_load_dwordx4 v[128:131], v176, s[60:61] sc1\n\t"
      "global_load_dwordx4 v[132:135], v177, s[60:61] sc1\n\t"
      "global_load_dwordx4 v[136:139], v178, s[60:61] sc1\n\t"
      "global_load_dwordx4 v[140:143], v179, s[60:61] sc1\n\t"
      "global_load_dwordx4 v[144:147], v180, s[60:61] sc1\n\t"
      "global_load_dwordx4 v[148:151], v181, s[60:61] sc1\n\t"
      "global_load_dwordx4 v[152:155], v182, s[60:61] sc1\n\t"
      "global_load_dwordx4 v[156:159], v183, s[60:61] sc1\n\t"
      "s_waitcnt vmcnt(0)\n\t"
      "s_nop 7\n\t"
      "s_branch NP3B_%=\n\t"
      "NP4R_%=:\n\t"
      "s_mov_b32 s51, 0\n\t"
      "NP4L_%=:\n\t"
      "s_add_u32 s51, s51, 1\n\t"
      "s_cmp_lt_u32 s51, 262144\n\t"
      "s_cbranch_scc0 NP4X_%=\n\t"
      "s_waitcnt vmcnt(0)\n\t"
      "s_sleep 1\n\t"
      "global_load_dwordx4 v[128:131], v176, s[60:61] sc1\n\t"
      "global_load_dwordx4 v[132:135], v177, s[60:61] sc1\n\t"
      "global_load_dwordx4 v[136:139], v178, s[60:61] sc1\n\t"
      "global_load_dwordx4 v[140:143], v179, s[60:61] sc1\n\t"
      "global_load_dwordx4 v[144:147], v180, s[60:61] sc1\n\t"
      "global_load_dwordx4 v[148:151], v181, s[60:61] sc1\n\t"
      "global_load_dwordx4 v[152:155], v182, s[60:61] sc1\n\t"
      "global_load_dwordx4 v[156:159], v183, s[60:61] sc1\n\t"
      "s_waitcnt vmcnt(0)\n\t"
      "v_max3_u32 v234, v128, v129, v130\n\t"
      "v_max3_u32 v234, v234, v131, v132\n\t"
      "v_max3_u32 v234, v234, v133, v134\n\t"
      "v_max3_u32 v234, v234, v135, v136\n\t"
      "v_max3_u32 v234, v234, v137, v138\n\t"
      "v_max3_u32 v234, v234, v139, v140\n\t"
      "v_max3_u32 v234, v234, v141, v142\n\t"
      "v_max3_u32 v234, v234, v143, v144\n\t"
      "v_max3_u32 v234, v234, v145, v146\n\t"
      "v_max3_u32 v234, v234, v147, v148\n\t"
      "v_max3_u32 v234, v234, v149, v150\n\t"
      "v_max3_u32 v234, v234, v151, v152\n\t"
      "v_max3_u32 v234, v234, v153, v154\n\t"
      "v_max3_u32 v234, v234, v155, v156\n\t"
      "v_max3_u32 v234, v234, v157, v158\n\t"
      "v_max_u32_e32 v234, v234, v159\n\t"
      "v_cmp_eq_u32_e32 vcc, -1, v234\n\t"
      "s_cbranch_vccnz NP4L_%=\n\t"
      "NP4X_%=:\n\t"
      "v_mfma_f32_16x16x32_bf16 v[184:187], v[0:3], v[128:131], 0\n\t"
      "v_mfma_f32_16x16x32_bf16 v[188:191], v[4:7], v[128:131], 0\n\t"
      "v_mfma_f32_16x16x32_bf16 v[192:195], v[8:11], v[128:131], 0\n\t"
      "v_mfma_f32_16x16x32_bf16 v[196:199], v[12:15], v[128:131], 0\n\t"
      "v_mfma_f32_16x16x32_bf16 v[184:187], v[16:19], v[132:135], v[184:187]\n\t"
      "v_mfma_f32_16x16x32_bf16 v[188:191], v[20:23], v[132:135], v[188:191]\n\t"
      "v_mfma_f32_16x16x32_bf16 v[192:195], v[24:27], v[132:135], v[192:195]\n\t"
      "v_mfma_f32_16x16x32_bf16 v[196:199], v[28:31], v[132:135], v[196:199]\n\t"
      "v_mfma_f32_16x16x32_bf16 v[184:187], v[32:35], v[136:139], v[184:187]\n\t"
      "v_mfma_f32_16x16x32_bf16 v[188:191], v[36:39], v[136:139], v[188:191]\n\t"
      "v_mfma_f32_16x16x32_bf16 v[192:195], v[40:43], v[136:139], v[192:195]\n\t"
      "v_mfma_f32_16x16x32_bf16 v[196:199], v[44:47], v[136:139], v[196:199]\n\t"
      "v_mfma_f32_16x16x32_bf16 v[184:187], v[48:51], v[140:143], v[184:187]\n\t"
      "v_mfma_f32_16x16x32_bf16 v[188:191], v[52:55], v[140:143], v[188:191]\n\t"
      "v_mfma_f32_16x16x32_bf16 v[192:195], v[56:59], v[140:143], v[192:195]\n\t"
      "v_mfma_f32_16x16x32_bf16 v[196:199], v[60:63], v[140:143], v[196:199]\n\t"
      "v_mfma_f32_16x16x32_bf16 v[184:187], v[64:67], v[144:147], v[184:187]\n\t"
      "v_mfma_f32_16x16x32_bf16 v[188:191], v[68:71], v[144:147], v[188:191]\n\t"
      "v_mfma_f32_16x16x32_bf16 v[192:195], v[72:75], v[144:147], v[192:195]\n\t"
      "v_mfma_f32_16x16x32_bf16 v[196:199], v[76:79], v[144:147], v[196:199]\n\t"
      "v_mfma_f32_16x16x32_bf16 v[184:187], v[80:83], v[148:151], v[184:187]\n\t"
      "v_mfma_f32_16x16x32_bf16 v[188:191], v[84:87], v[148:151], v[188:191]\n\t"
      "v_mfma_f32_16x16x32_bf16 v[192:195], v[88:91], v[148:151], v[192:195]\n\t"
      "v_mfma_f32_16x16x32_bf16 v[196:199], v[92:95], v[148:151], v[196:199]\n\t"
      "v_mfma_f32_16x16x32_bf16 v[184:187], v[96:99], v[152:155], v[184:187]\n\t"
      "v_mfma_f32_16x16x32_bf16 v[188:191], v[100:103], v[152:155], v[188:191]\n\t"
      "v_mfma_f32_16x16x32_bf16 v[192:195], v[104:107], v[152:155], v[192:195]\n\t"
      "v_mfma_f32_16x16x32_bf16 v[196:199], v[108:111], v[152:155], v[196:199]\n\t"
      "v_mfma_f32_16x16x32_bf16 v[184:187], v[112:115], v[156:159], v[184:187]\n\t"
      "v_mfma_f32_16x16x32_bf16 v[188:191], v[116:119], v[156:159], v[188:191]\n\t"
      "v_mfma_f32_16x16x32_bf16 v[192:195], v[120:123], v[156:159], v[192:195]\n\t"
      "v_mfma_f32_16x16x32_bf16 v[196:199], v[124:127], v[156:159], v[196:199]\n\t"
      "global_load_dwordx4 v[128:131], v176, s[62:63] sc1\n\t"
      "global_load_dwordx4 v[132:135], v177, s[62:63] sc1\n\t"
      "global_load_dwordx4 v[136:139], v178, s[62:63] sc1\n\t"
      "global_load_dwordx4 v[140:143], v179, s[62:63] sc1\n\t"
      "global_load_dwordx4 v[144:147], v180, s[62:63] sc1\n\t"
      "global_load_dwordx4 v[148:151], v181, s[62:63] sc1\n\t"
      "global_load_dwordx4 v[152:155], v182, s[62:63] sc1\n\t"
      "global_load_dwordx4 v[156:159], v183, s[62:63] sc1\n\t"
      "s_waitcnt vmcnt(0)\n\t"
      "s_nop 7\n\t"
      "s_branch NP4B_%=\n\t"
      "NP5R_%=:\n\t"
      "s_mov_b32 s51, 0\n\t"
      "NP5L_%=:\n\t"
      "s_add_u32 s51, s51, 1\n\t"
      "s_cmp_lt_u32 s51, 262144\n\t"
      "s_cbranch_scc0 NP5X_%=\n\t"
      "s_waitcnt vmcnt(0)\n\t"
      "s_sleep 1\n\t"
      "global_load_dwordx4 v[128:131], v176, s[62:63] sc1\n\t"
      "global_load_dwordx4 v[132:135], v177, s[62:63] sc1\n\t"
      "global_load_dwordx4 v[136:139], v178, s[62:63] sc1\n\t"
      "global_load_dwordx4 v[140:143], v179, s[62:63] sc1\n\t"
      "global_load_dwordx4 v[144:147], v180, s[62:63] sc1\n\t"
      "global_load_dwordx4 v[148:151], v181, s[62:63] sc1\n\t"
      "global_load_dwordx4 v[152:155], v182, s[62:63] sc1\n\t"
      "global_load_dwordx4 v[156:159], v183, s[62:63] sc1\n\t"
      "s_waitcnt vmcnt(0)\n\t"
      "v_max3_u32 v234, v128, v129, v130\n\t"
      "v_max3_u32 v234, v234, v131, v132\n\t"
      "v_max3_u32 v234, v234, v133, v134\n\t"
      "v_max3_u32 v234, v234, v135, v136\n\t"
      "v_max3_u32 v234, v234, v137, v138\n\t"
      "v_max3_u32 v234, v234, v139, v140\n\t"
      "v_max3_u32 v234, v234, v141, v142\n\t"
      "v_max3_u32 v234, v234, v143, v144\n\t"
      "v_max3_u32 v234, v234, v145, v146\n\t"
      "v_max3_u32 v234, v234, v147, v148\n\t"
      "v_max3_u32 v234, v234, v149, v150\n\t"
      "v_max3_u32 v234, v234, v151, v152\n\t"
      "v_max3_u32 v234, v234, v153, v154\n\t"
      "v_max3_u32 v234, v234, v155, v156\n\t"
      "v_max3_u32 v234, v234, v157, v158\n\t"
      "v_max_u32_e32 v234, v234, v159\n\t"
      "v_cmp_eq_u32_e32 vcc, -1, v234\n\t"
      "s_cbranch_vccnz NP5L_%=\n\t"
      "NP5X_%=:\n\t"
      "v_mfma_f32_16x16x32_bf16 v[200:203], v[0:3], v[128:131], 0\n\t"
      "v_mfma_f32_16x16x32_bf16 v[204:207], v[4:7], v[128:131], 0\n\t"
      "v_mfma_f32_16x16x32_bf16 v[208:211], v[8:11], v[128:131], 0\n\t"
      "v_mfma_f32_16x16x32_bf16 v[212:215], v[12:15], v[128:131], 0\n\t"
      "v_mfma_f32_16x16x32_bf16 v[200:203], v[16:19], v[132:135], v[200:203]\n\t"
      "v_mfma_f32_16x16x32_bf16 v[204:207], v[20:23], v[132:135], v[204:207]\n\t"
      "v_mfma_f32_16x16x32_bf16 v[208:211], v[24:27], v[132:135], v[208:211]\n\t"
      "v_mfma_f32_16x16x32_bf16 v[212:215], v[28:31], v[132:135], v[212:215]\n\t"
      "v_mfma_f32_16x16x32_bf16 v[200:203], v[32:35], v[136:139], v[200:203]\n\t"
      "v_mfma_f32_16x16x32_bf16 v[204:207], v[36:39], v[136:139], v[204:207]\n\t"
      "v_mfma_f32_16x16x32_bf16 v[208:211], v[40:43], v[136:139], v[208:211]\n\t"
      "v_mfma_f32_16x16x32_bf16 v[212:215], v[44:47], v[136:139], v[212:215]\n\t"
      "v_mfma_f32_16x16x32_bf16 v[200:203], v[48:51], v[140:143], v[200:203]\n\t"
      "v_mfma_f32_16x16x32_bf16 v[204:207], v[52:55], v[140:143], v[204:207]\n\t"
      "v_mfma_f32_16x16x32_bf16 v[208:211], v[56:59], v[140:143], v[208:211]\n\t"
      "v_mfma_f32_16x16x32_bf16 v[212:215], v[60:63], v[140:143], v[212:215]\n\t"
      "v_mfma_f32_16x16x32_bf16 v[200:203], v[64:67], v[144:147], v[200:203]\n\t"
      "v_mfma_f32_16x16x32_bf16 v[204:207], v[68:71], v[144:147], v[204:207]\n\t"
      "v_mfma_f32_16x16x32_bf16 v[208:211], v[72:75], v[144:147], v[208:211]\n\t"
      "v_mfma_f32_16x16x32_bf16 v[212:215], v[76:79], v[144:147], v[212:215]\n\t"
      "v_mfma_f32_16x16x32_bf16 v[200:203], v[80:83], v[148:151], v[200:203]\n\t"
      "v_mfma_f32_16x16x32_bf16 v[204:207], v[84:87], v[148:151], v[204:207]\n\t"
      "v_mfma_f32_16x16x32_bf16 v[208:211], v[88:91], v[148:151], v[208:211]\n\t"
      "v_mfma_f32_16x16x32_bf16 v[212:215], v[92:95], v[148:151], v[212:215]\n\t"
      "v_mfma_f32_16x16x32_bf16 v[200:203], v[96:99], v[152:155], v[200:203]\n\t"
      "v_mfma_f32_16x16x32_bf16 v[204:207], v[100:103], v[152:155], v[204:207]\n\t"
      "v_mfma_f32_16x16x32_bf16 v[208:211], v[104:107], v[152:155], v[208:211]\n\t"
      "v_mfma_f32_16x16x32_bf16 v[212:215], v[108:111], v[152:155], v[212:215]\n\t"
      "v_mfma_f32_16x16x32_bf16 v[200:203], v[112:115], v[156:159], v[200:203]\n\t"
      "v_mfma_f32_16x16x32_bf16 v[204:207], v[116:119], v[156:159], v[204:207]\n\t"
      "v_mfma_f32_16x16x32_bf16 v[208:211], v[120:123], v[156:159], v[208:211]\n\t"
      "v_mfma_f32_16x16x32_bf16 v[212:215], v[124:127], v[156:159], v[212:215]\n\t"
      "global_load_dwordx4 v[128:131], v176, s[64:65] sc1\n\t"
      "global_load_dwordx4 v[132:135], v177, s[64:65] sc1\n\t"
      "global_load_dwordx4 v[136:139], v178, s[64:65] sc1\n\t"
      "global_load_dwordx4 v[140:143], v179, s[64:65] sc1\n\t"
      "global_load_dwordx4 v[144:147], v180, s[64:65] sc1\n\t"
      "global_load_dwordx4 v[148:151], v181, s[64:65] sc1\n\t"
      "global_load_dwordx4 v[152:155], v182, s[64:65] sc1\n\t"
      "global_load_dwordx4 v[156:159], v183, s[64:65] sc1\n\t"
      "s_waitcnt vmcnt(0)\n\t"
      "s_nop 7\n\t"
      "s_branch NP5B_%=\n\t"
      "NP6R_%=:\n\t"
      "s_mov_b32 s51, 0\n\t"
      "NP6L_%=:\n\t"
      "s_add_u32 s51, s51, 1\n\t"
      "s_cmp_lt_u32 s51, 262144\n\t"
      "s_cbranch_scc0 NP6X_%=\n\t"
      "s_waitcnt vmcnt(0)\n\t"
      "s_sleep 1\n\t"
      "global_load_dwordx4 v[128:131], v176, s[64:65] sc1\n\t"
      "global_load_dwordx4 v[132:135], v177, s[64:65] sc1\n\t"
      "global_load_dwordx4 v[136:139], v178, s[64:65] sc1\n\t"
      "global_load_dwordx4 v[140:143], v179, s[64:65] sc1\n\t"
      "global_load_dwordx4 v[144:147], v180, s[64:65] sc1\n\t"
      "global_load_dwordx4 v[148:151], v181, s[64:65] sc1\n\t"
      "global_load_dwordx4 v[152:155], v182, s[64:65] sc1\n\t"
      "global_load_dwordx4 v[156:159], v183, s[64:65] sc1\n\t"
      "s_waitcnt vmcnt(0)\n\t"
      "v_max3_u32 v234, v128, v129, v130\n\t"
      "v_max3_u32 v234, v234, v131, v132\n\t"
      "v_max3_u32 v234, v234, v133, v134\n\t"
      "v_max3_u32 v234, v234, v135, v136\n\t"
      "v_max3_u32 v234, v234, v137, v138\n\t"
      "v_max3_u32 v234, v234, v139, v140\n\t"
      "v_max3_u32 v234, v234, v141, v142\n\t"
      "v_max3_u32 v234, v234, v143, v144\n\t"
      "v_max3_u32 v234, v234, v145, v146\n\t"
      "v_max3_u32 v234, v234, v147, v148\n\t"
      "v_max3_u32 v234, v234, v149, v150\n\t"
      "v_max3_u32 v234, v234, v151, v152\n\t"
      "v_max3_u32 v234, v234, v153, v154\n\t"
      "v_max3_u32 v234, v234, v155, v156\n\t"
      "v_max3_u32 v234, v234, v157, v158\n\t"
      "v_max_u32_e32 v234, v234, v159\n\t"
      "v_cmp_eq_u32_e32 vcc, -1, v234\n\t"
      "s_cbranch_vccnz NP6L_%=\n\t"
      "NP6X_%=:\n\t"
      "v_mfma_f32_16x16x32_bf16 v[184:187], v[0:3], v[128:131], 0\n\t"
      "v_mfma_f32_16x16x32_bf16 v[188:191], v[4:7], v[128:131], 0\n\t"
      "v_mfma_f32_16x16x32_bf16 v[192:195], v[8:11], v[128:131], 0\n\t"
      "v_mfma_f32_16x16x32_bf16 v[196:199], v[12:15], v[128:131], 0\n\t"
      "v_mfma_f32_16x16x32_bf16 v[184:187], v[16:19], v[132:135], v[184:187]\n\t"
      "v_mfma_f32_16x16x32_bf16 v[188:191], v[20:23], v[132:135], v[188:191]\n\t"
      "v_mfma_f32_16x16x32_bf16 v[192:195], v[24:27], v[132:135], v[192:195]\n\t"
      "v_mfma_f32_16x16x32_bf16 v[196:199], v[28:31], v[132:135], v[196:199]\n\t"
      "v_mfma_f32_16x16x32_bf16 v[184:187], v[32:35], v[136:139], v[184:187]\n\t"
      "v_mfma_f32_16x16x32_bf16 v[188:191], v[36:39], v[136:139], v[188:191]\n\t"
      "v_mfma_f32_16x16x32_bf16 v[192:195], v[40:43], v[136:139], v[192:195]\n\t"
      "v_mfma_f32_16x16x32_bf16 v[196:199], v[44:47], v[136:139], v[196:199]\n\t"
      "v_mfma_f32_16x16x32_bf16 v[184:187], v[48:51], v[140:143], v[184:187]\n\t"
      "v_mfma_f32_16x16x32_bf16 v[188:191], v[52:55], v[140:143], v[188:191]\n\t"
      "v_mfma_f32_16x16x32_bf16 v[192:195], v[56:59], v[140:143], v[192:195]\n\t"
      "v_mfma_f32_16x16x32_bf16 v[196:199], v[60:63], v[140:143], v[196:199]\n\t"
      "v_mfma_f32_16x16x32_bf16 v[184:187], v[64:67], v[144:147], v[184:187]\n\t"
      "v_mfma_f32_16x16x32_bf16 v[188:191], v[68:71], v[144:147], v[188:191]\n\t"
      "v_mfma_f32_16x16x32_bf16 v[192:195], v[72:75], v[144:147], v[192:195]\n\t"
      "v_mfma_f32_16x16x32_bf16 v[196:199], v[76:79], v[144:147], v[196:199]\n\t"
      "v_mfma_f32_16x16x32_bf16 v[184:187], v[80:83], v[148:151], v[184:187]\n\t"
      "v_mfma_f32_16x16x32_bf16 v[188:191], v[84:87], v[148:151], v[188:191]\n\t"
      "v_mfma_f32_16x16x32_bf16 v[192:195], v[88:91], v[148:151], v[192:195]\n\t"
      "v_mfma_f32_16x16x32_bf16 v[196:199], v[92:95], v[148:151], v[196:199]\n\t"
      "v_mfma_f32_16x16x32_bf16 v[184:187], v[96:99], v[152:155], v[184:187]\n\t"
      "v_mfma_f32_16x16x32_bf16 v[188:191], v[100:103], v[152:155], v[188:191]\n\t"
      "v_mfma_f32_16x16x32_bf16 v[192:195], v[104:107], v[152:155], v[192:195]\n\t"
      "v_mfma_f32_16x16x32_bf16 v[196:199], v[108:111], v[152:155], v[196:199]\n\t"
      "v_mfma_f32_16x16x32_bf16 v[184:187], v[112:115], v[156:159], v[184:187]\n\t"
      "v_mfma_f32_16x16x32_bf16 v[188:191], v[116:119], v[156:159], v[188:191]\n\t"
      "v_mfma_f32_16x16x32_bf16 v[192:195], v[120:123], v[156:159], v[192:195]\n\t"
      "v_mfma_f32_16x16x32_bf16 v[196:199], v[124:127], v[156:159], v[196:199]\n\t"
      "s_waitcnt vmcnt(0)\n\t"
      "s_nop 7\n\t"
      "s_branch NP6B_%=\n\t"
      : "+{s[4:19]}"(s), "+{s[20:21]}"(kr), "+{s[22:23]}"(hint), "+{v[240:247]}"(v)
      :
      : "memory", "vcc", "scc",
        "v0", "v1", "v2", "v3", "v4", "v5", "v6", "v7", "v8", "v9", "v10", "v11", "v12", "v13", "v14", "v15", "v16",
        "v17", "v18", "v19", "v20", "v21", "v22", "v23", "v24", "v25", "v26", "v27", "v28", "v29", "v30", "v31", "v32",
        "v33", "v34", "v35", "v36", "v37", "v38", "v39", "v40", "v41", "v42", "v43", "v44", "v45", "v46", "v47", "v48",
        "v49", "v50", "v51", "v52", "v53", "v54", "v55", "v56", "v57", "v58", "v59", "v60", "v61", "v62", "v63", "v64",
        "v65", "v66", "v67", "v68", "v69", "v70", "v71", "v72", "v73", "v74", "v75", "v76", "v77", "v78", "v79", "v80",
        "v81", "v82", "v83", "v84", "v85", "v86", "v87", "v88", "v89", "v90", "v91", "v92", "v93", "v94", "v95", "v96",
        "v97", "v98", "v99", "v100", "v101", "v102", "v103", "v104", "v105", "v106", "v107", "v108", "v109", "v110",
        "v111", "v112", "v113", "v114", "v115", "v116", "v117", "v118", "v119", "v120", "v121", "v122", "v123", "v124",
        "v125", "v126", "v127", "v128", "v129", "v130", "v131", "v132", "v133", "v134", "v135", "v136", "v137", "v138",
        "v139", "v140", "v141", "v142", "v143", "v144", "v145", "v146", "v147", "v148", "v149", "v150", "v151", "v152",
        "v153", "v154", "v155", "v156", "v157", "v158", "v159", "v160", "v161", "v162", "v163", "v164", "v165", "v166",
        "v167", "v168", "v169", "v170", "v171", "v172", "v173", "v174", "v175", "v176", "v177", "v178", "v179", "v180",
        "v181", "v182", "v183", "v184", "v185", "v186", "v187", "v188", "v189", "v190", "v191", "v192", "v193", "v194",
        "v195", "v196", "v197", "v198", "v199", "v200", "v201", "v202", "v203", "v204", "v205", "v206", "v207", "v208",
        "v209", "v210", "v211", "v212", "v213", "v214", "v215", "v216", "v217", "v218", "v219", "v220", "v221", "v222",
        "v223", "v224", "v225", "v226", "v227", "v228", "v229", "v230", "v231", "v232", "v233", "v234", "v235", "v236",
        "v237", "v238", "v239", "v248", "v249", "v250", "v251", "v252", "v253", "s44", "s45", "s46", "s47", "s48",
        "s49", "s50", "s51", "s52", "s53", "s54", "s55", "s56", "s57", "s58", "s59", "s60", "s61", "s62", "s63", "s64",
        "s65", "s66", "s67", "s68", "s69", "s70"
    );
    __builtin_amdgcn_endpgm();
  } else {
    const int ib = idx - 16, t = 16 * ((xcd + XO) & 7) + ib;
    const int st = M4 ? 8 * (xcd >> 1) + (ib >> 1) : 4 * xcd + (ib >> 2), blk0 = 7 * (M4 ? 2 * (xcd & 1) + (ib & 1) : ib & 3);
    const int rot = (M4 ? ((RF4 * (ib >> 1)) >> 1) + 4 * (ib & 1) + 2 * (xcd & 1) : 2 * (ib & 3) + (SWP ? 2 * (st & 2) : RF4 == 4 ? 4 * (st & 1) : RF4 * st)) % 7, ks = SWP ? wave ^ (4 * (st & 1)) : wave;
    const unsigned bits = UNI((wave >= 4 ? 1 : 0) | ((wave >= 4) != HP4 ? 2 : 0) | (wave == 0 ? 16 : 0) |
                              (t == 0 && wave == 0 ? 32 : 0) | (wave < 7 && t < 112 ? 64 : 0));
    u32x4 vo;
    unsigned vq;
    {
      LANE_BITS(threadIdx.x & 63);
      const int cb0 = wave < 4 ? 4 * wave : 4 + 3 * wave;
      const int k0 = (ib + 2 * xcd) % KROT * KMUL;
      u32x16 v, s;
      u32x8 s2;
      v[0] = (8 * (wave & 3) + lr) * 896 + (wave >> 2) * 128 + ((lc ^ swz_tr(8 * wave + lr)) << 4) + 4096;
#pragma unroll
      for (int b = 0; b < 4; ++b) {
        const int cb = min(cb0 + b, 27);
        v[1 + b] = g * B_UNIT + (cb >> 2) * 1024 + q4 * 128 + (((2 * (cb & 3) + (p4 >> 1)) ^ swz_tr(8 * g + q4)) << 4) + (p4 & 1) * 8;
        const int by = 112 * wave + 56 * (b >> 1) + 32 * (b & 1) + 8 * g - 64 * k0;
        vo[b] = B_X + c * B_OP + (by < 0 ? by + 896 : by);
      }
      v[5] = B_X + (4 * g + q4) * 896 + cb0 * 32 + p4 * 8;
      v[6] = B_X + c * B_OP + 16 * g;
      v[7] = ((cb0 + (g & 1)) * 64 + (t >> 1)) * 1024 + (2 * (t & 1) + (g >> 1)) * 256 + c * 16;
      v[8] = B_PS + (4 * g + q4) * 64 + wave * 8;
      v[9] = B_PS + (4 * g + p4) * 64 + q4 * 16;
      v[10] = (t * 16 + 4 * g + p4) * 16;
      v[11] = (lane >> 2) * 3584 + (p4 + ((7 * xcd) >> 1)) * 128;
      v[12] = lane * 16;
      v[14] = (wave * 224 + 96 - 96 * (lane >> 5) + ib * (8 - 2 * (lane >> 5)) + min((lane >> 2) & 7, 7 - 2 * (lane >> 5))) * 4096 + p4 * 128 + xcd * 512;
      v[15] = ST_H3 + t * 128 + lc * 16384;
      put(s, 0, (u64)w2);
      put(s, 2, (u64)(wo + k0 * 32 * 896 - 4096));
      put(s, 4, (u64)(ws + WS_ST));
      put(s, 6, (u64)(ws + WS_O + (t * 16 + 2 * wave) * 1024));
      put(s, 10, (u64)(ws + WS_HN));
      put(s, 12, (u64)(ws + WS_G + ((7 * (t & 3) + wave) * 56 + 2 * (t >> 2)) * 1024));
      put(s, 14, (u64)(w1 + (ib * 8 + wave) * 16 * 3584));
      u64 px = (u64)(x + t * 14336 + min(wave, 6) * 2048);
      unsigned lx = B_X + min(wave, 6) * 2048;
      u64 pb1 = (u64)(b1 + min(ib, 6) * 512), pb2 = (u64)(b2 + (ib & 7) * 512), pob = (u64)(out + ib * 114688);
      vq = ST_H2 + ((((t >> 3) * 8 + wave) * 2 + ((t >> 2) & 1)) * 2 + (lane & 1)) * 64;
      u64 pw = (u64)wqkv;
      unsigned vtw = min(TW0 * 2688 + ((wave * 16 + ib) * TWN + min(lane, TWN - 1)) * 128, 448 * 2688 - 128);
      unsigned vtw2 = min(TW1 * 2688 + ((wave * 16 + ib) * TWN1 + min(lane, TWN1 - 1)) * 128, 448 * 2688 - 128);
      unsigned vto = min(((wave * 16 + ib) * 25 + min(lane, 24)) * 128, 448 * 896 - 128) + 4096;
      unsigned vck = ST_CK + (((t >> 2) * 16 + 2 * wave + (g >> 1)) * 4 + (t & 3)) * 64 + c * 4;
      s2[0] = bits;
      s2[1] = (wave & 3) * B_UNIT + (wave >> 2) * 1024;
      s2[2] = UNI(wave < 4 ? ~0u : 0u);
      s2[3] = 13 - k0;
      put2(s2, 4, wave < 4 ? ~0u : 0xffffu, wave < 4 ? ~0u : 0xffffu);
    asm volatile(
      "s_waitcnt lgkmcnt(0)\n\ts_nop 4\n\t"
      "s_load_dwordx2 s[26:27], s[8:9], 0x20000\n\t"
      "v_mov_b32_e32 v248, -1\n\t"
      "v_mov_b32_e32 v249, -1\n\t"
      "v_mov_b32_e32 v250, -1\n\t"
      "v_mov_b32_e32 v251, -1\n\t"
      "v_add_u32_e32 v221, 4294965504, v224\n\t"
      "v_add_u32_e32 v222, 512, v224\n\t"
      "v_add_u32_e32 v223, 4294966016, v224\n\t"
      "s_bitcmp1_b32 s20, 0\n\t"
      "s_cselect_b64 s[46:47], -1, 0\n\t"
      "s_mov_b64 vcc, s[46:47]\n\t"
      "v_add_u32_e32 v237, 131072, v231\n\t"
      "s_mov_b32 s44, 0xffffffff\n\t"
      "s_mov_b32 s45, 0xffff\n\t"
      "s_waitcnt vmcnt(1)\n\t"
      "s_barrier\n\t"
      "ds_read_b64_tr_b16 v[204:205], v229\n\t"
      "ds_read_b64_tr_b16 v[206:207], v229 offset:32\n\t"
      "ds_read_b64_tr_b16 v[208:209], v229 offset:64\n\t"
      "ds_read_b64_tr_b16 v[210:211], v229 offset:96\n\t"
      "s_waitcnt lgkmcnt(0)\n\t"
      "s_add_u32 s26, s26, 1\n\t"
      "s_addc_u32 s27, s27, 0\n\t"
      "s_mul_i32 s48, s26, 0x9e3779b9\n\t"
      "s_mul_hi_u32 s13, s26, 0x7f4a7c15\n\t"
      "s_add_u32 s13, s13, s48\n\t"
      "s_mul_i32 s48, s27, 0x7f4a7c15\n\t"
      "s_add_u32 s13, s13, s48\n\t"
      "s_mul_i32 s12, s26, 0x7f4a7c15\n\t"
      "s_add_u32 s12, s12, 0xd192ed03\n\t"
      "s_addc_u32 s13, s13, 0xd1b54a32\n\t"
      "v_mov_b32_e32 v188, s12\n\t"
      "v_mov_b32_e32 v189, s13\n\t"
      "v_mov_b32_e32 v190, s26\n\t"
      "v_mov_b32_e32 v191, s27\n\t"
      "v_mov_b32_e32 v192, 163312\n\t"
      "s_mov_b64 exec, 1\n\t"
      "ds_write_b128 v192, v[188:191]\n\t"
      "s_mov_b64 exec, -1\n\t"
      "v_and_b32_e32 v210, s22, v210\n\t"
      "v_and_b32_e32 v211, s22, v211\n\t"
      "v_lshlrev_b32_e32 v160, 16, v204\n\t"
      "v_and_b32_e32 v161, 0xffff0000, v204\n\t"
      "v_lshlrev_b32_e32 v162, 16, v205\n\t"
      "v_and_b32_e32 v163, 0xffff0000, v205\n\t"
      "v_lshlrev_b32_e32 v164, 16, v206\n\t"
      "v_and_b32_e32 v165, 0xffff0000, v206\n\t"
      "v_lshlrev_b32_e32 v166, 16, v207\n\t"
      "v_and_b32_e32 v167, 0xffff0000, v207\n\t"
      "v_lshlrev_b32_e32 v168, 16, v208\n\t"
      "v_and_b32_e32 v169, 0xffff0000, v208\n\t"
      "v_lshlrev_b32_e32 v170, 16, v209\n\t"
      "v_and_b32_e32 v171, 0xffff0000, v209\n\t"
      "v_lshlrev_b32_e32 v172, 16, v210\n\t"
      "v_and_b32_e32 v173, 0xffff0000, v210\n\t"
      "v_lshlrev_b32_e32 v174, 16, v211\n\t"
      "v_and_b32_e32 v175, 0xffff0000, v211\n\t"
      "v_pk_add_f32 v[188:189], v[160:161], v[164:165]\n\t"
      "v_pk_mul_f32 v[192:193], v[160:161], v[160:161]\n\t"
      "v_pk_add_f32 v[190:191], v[162:163], v[166:167]\n\t"
      "v_pk_mul_f32 v[194:195], v[162:163], v[162:163]\n\t"
      "v_pk_fma_f32 v[192:193], v[164:165], v[164:165], v[192:193]\n\t"
      "v_pk_fma_f32 v[194:195], v[166:167], v[166:167], v[194:195]\n\t"
      "v_pk_add_f32 v[188:189], v[188:189], v[168:169]\n\t"
      "v_pk_fma_f32 v[192:193], v[168:169], v[168:169], v[192:193]\n\t"
      "v_pk_add_f32 v[190:191], v[190:191], v[170:171]\n\t"
      "v_pk_fma_f32 v[194:195], v[170:171], v[170:171], v[194:195]\n\t"
      "v_pk_add_f32 v[188:189], v[188:189], v[172:173]\n\t"
      "v_pk_fma_f32 v[192:193], v[172:173], v[172:173], v[192:193]\n\t"
      "v_pk_add_f32 v[190:191], v[190:191], v[174:175]\n\t"
      "v_pk_fma_f32 v[194:195], v[174:175], v[174:175], v[194:195]\n\t"
      "v_add_f32_dpp v196, v188, v188 row_ror:8 row_mask:0xf bank_mask:0x3\n\t"
      "s_nop 1\n\t"
      "v_add_f32_dpp v196, v190, v190 row_ror:8 row_mask:0xf bank_mask:0xc\n\t"
      "v_add_f32_dpp v197, v189, v189 row_ror:8 row_mask:0xf bank_mask:0x3\n\t"
      "s_nop 1\n\t"
      "v_add_f32_dpp v197, v191, v191 row_ror:8 row_mask:0xf bank_mask:0xc\n\t"
      "v_add_f32_dpp v198, v192, v192 row_ror:8 row_mask:0xf bank_mask:0x3\n\t"
      "s_nop 1\n\t"
      "v_add_f32_dpp v198, v194, v194 row_ror:8 row_mask:0xf bank_mask:0xc\n\t"
      "v_add_f32_dpp v199, v193, v193 row_ror:8 row_mask:0xf bank_mask:0x3\n\t"
      "s_nop 1\n\t"
      "v_add_f32_dpp v199, v195, v195 row_ror:8 row_mask:0xf bank_mask:0xc\n\t"
      "v_add_f32_dpp v188, v196, v196 row_shl:4 row_mask:0xf bank_mask:0x5\n\t"
      "s_nop 1\n\t"
      "v_add_f32_dpp v188, v197, v197 row_shr:4 row_mask:0xf bank_mask:0xa\n\t"
      "v_add_f32_dpp v189, v198, v198 row_shl:4 row_mask:0xf bank_mask:0x5\n\t"
      "s_nop 1\n\t"
      "v_add_f32_dpp v189, v199, v199 row_shr:4 row_mask:0xf bank_mask:0xa\n\t"
      "v_add_f32_dpp v188, v188, v188 quad_perm:[1,0,3,2] row_mask:0xf bank_mask:0xf\n\t"
      "s_nop 0\n\t"
      "v_add_f32_dpp v189, v189, v189 quad_perm:[1,0,3,2] row_mask:0xf bank_mask:0xf\n\t"
      "v_add_f32_dpp v188, v188, v188 quad_perm:[2,3,0,1] row_mask:0xf bank_mask:0xf\n\t"
      "s_nop 0\n\t"
      "v_add_f32_dpp v189, v189, v189 quad_perm:[2,3,0,1] row_mask:0xf bank_mask:0xf\n\t"
      "ds_write_b64 v232, v[188:189]\n\t"
      "s_waitcnt lgkmcnt(0)\n\t"
      "s_barrier\n\t"
      "ds_read_b128 v[188:191], v233\n\t"
      "s_waitcnt lgkmcnt(0)\n\t"
      "v_pk_add_f32 v[188:189], v[188:189], v[190:191]\n\t"
      "s_nop 1\n\t"
      "v_add_f32_dpp v188, v188, v188 row_ror:4 row_mask:0xf bank_mask:0xf\n\t"
      "v_add_f32_dpp v189, v189, v189 row_ror:4 row_mask:0xf bank_mask:0xf\n\t"
      "s_nop 0\n\t"
      "v_add_f32_dpp v188, v188, v188 row_ror:8 row_mask:0xf bank_mask:0xf\n\t"
      "v_add_f32_dpp v189, v189, v189 row_ror:8 row_mask:0xf bank_mask:0xf\n\t"
      "v_mul_f32_e32 v188, 0x3b124925, v188\n\t"
      "v_mul_f32_e32 v189, 0x3b124925, v189\n\t"
      "v_fma_f32 v189, -v188, v188, v189\n\t"
      "v_max_f32_e32 v189, 0, v189\n\t"
      "s_bitcmp1_b32 s20, 4\n\t"
      "s_cbranch_scc0 GW_%=\n\t"
      "v_add_f32_e32 v189, 0x3727c5ac, v189\n\t"
      "v_rsq_f32_e32 v189, v189\n\t"
      "v_xor_b32_e32 v188, 0x80000000, v188\n\t"
      "v_xor_b32_e32 v190, s12, v188\n\t"
      "v_xor_b32_e32 v191, s13, v189\n\t"
      "s_mov_b32 s48, 0xf000f\n\t"
      "s_mov_b32 s49, 0xf000f\n\t"
      "s_mov_b64 exec, s[48:49]\n\t"
      "global_store_dwordx4 v234, v[188:191], s[8:9] sc1\n\t"
      "s_mov_b64 exec, -1\n\t"
      "GW_%=:\n\t"
      "s_add_u32 m0, s21, 0\n\t"
      "s_nop 0\n\t"
      "global_load_lds_dwordx4 v224, s[6:7]\n\t"
      "global_load_lds_dwordx4 v221, s[6:7] offset:2048\n\t"
      "s_add_u32 m0, s21, 4096\n\t"
      "s_nop 0\n\t"
      "global_load_lds_dwordx4 v222, s[6:7]\n\t"
      "s_cbranch_vccnz WR0_%=\n\t"
      "global_load_lds_dwordx4 v223, s[6:7] offset:2048\n\t"
      "WR0_%=:\n\t"
      "s_sub_u32 s23, s23, 1\n\t"
      "s_cselect_b32 s48, -13, 1\n\t"
      "s_cselect_b32 s49, -1, 0\n\t"
      "s_mul_i32 s48, s48, 28672\n\t"
      "s_add_u32 s6, s6, s48\n\t"
      "s_addc_u32 s7, s7, s49\n\t"
      "s_add_u32 m0, s21, 28672\n\t"
      "s_nop 0\n\t"
      "global_load_lds_dwordx4 v224, s[6:7]\n\t"
      "global_load_lds_dwordx4 v221, s[6:7] offset:2048\n\t"
      "s_add_u32 m0, s21, 32768\n\t"
      "s_nop 0\n\t"
      "global_load_lds_dwordx4 v222, s[6:7]\n\t"
      "s_cbranch_vccnz WR1_%=\n\t"
      "global_load_lds_dwordx4 v223, s[6:7] offset:2048\n\t"
      "WR1_%=:\n\t"
      "s_sub_u32 s23, s23, 1\n\t"
      "s_cselect_b32 s48, -13, 1\n\t"
      "s_cselect_b32 s49, -1, 0\n\t"
      "s_mul_i32 s48, s48, 28672\n\t"
      "s_add_u32 s6, s6, s48\n\t"
      "s_addc_u32 s7, s7, s49\n\t"
      "s_cbranch_vccnz WW0_%=\n\t"
      "s_waitcnt vmcnt(4)\n\t"
      "s_branch WX0_%=\n\t"
      "WW0_%=:\n\t"
      "s_waitcnt vmcnt(3)\n\t"
      "WX0_%=:\n\t"
      "s_waitcnt lgkmcnt(0)\n\t"
      "s_barrier\n\t"
      "ds_read_b64_tr_b16 v[0:1], v225\n\t"
      "ds_read_b64_tr_b16 v[2:3], v225 offset:512\n\t"
      "ds_read_b64_tr_b16 v[4:5], v226\n\t"
      "ds_read_b64_tr_b16 v[6:7], v226 offset:512\n\t"
      "ds_read_b64_tr_b16 v[8:9], v227\n\t"
      "ds_read_b64_tr_b16 v[10:11], v227 offset:512\n\t"
      "ds_read_b64_tr_b16 v[12:13], v228\n\t"
      "ds_read_b64_tr_b16 v[14:15], v228 offset:512\n\t"
      "s_add_u32 m0, s21, 57344\n\t"
      "s_nop 0\n\t"
      "global_load_lds_dwordx4 v224, s[6:7]\n\t"
      "global_load_lds_dwordx4 v221, s[6:7] offset:2048\n\t"
      "s_add_u32 m0, s21, 61440\n\t"
      "s_nop 0\n\t"
      "global_load_lds_dwordx4 v222, s[6:7]\n\t"
      "s_cbranch_vccnz WR2_%=\n\t"
      "global_load_lds_dwordx4 v223, s[6:7] offset:2048\n\t"
      "WR2_%=:\n\t"
      "s_sub_u32 s23, s23, 1\n\t"
      "s_cselect_b32 s48, -13, 1\n\t"
      "s_cselect_b32 s49, -1, 0\n\t"
      "s_mul_i32 s48, s48, 28672\n\t"
      "s_add_u32 s6, s6, s48\n\t"
      "s_addc_u32 s7, s7, s49\n\t"
      "s_cbranch_vccnz WW1_%=\n\t"
      "s_waitcnt vmcnt(4)\n\t"
      "s_branch WX1_%=\n\t"
      "WW1_%=:\n\t"
      "s_waitcnt vmcnt(3)\n\t"
      "WX1_%=:\n\t"
      "s_waitcnt lgkmcnt(0)\n\t"
      "s_barrier\n\t"
      "ds_read_b64_tr_b16 v[16:17], v225 offset:28672\n\t"
      "ds_read_b64_tr_b16 v[18:19], v225 offset:29184\n\t"
      "ds_read_b64_tr_b16 v[20:21], v226 offset:28672\n\t"
      "ds_read_b64_tr_b16 v[22:23], v226 offset:29184\n\t"
      "ds_read_b64_tr_b16 v[24:25], v227 offset:28672\n\t"
      "ds_read_b64_tr_b16 v[26:27], v227 offset:29184\n\t"
      "ds_read_b64_tr_b16 v[28:29], v228 offset:28672\n\t"
      "ds_read_b64_tr_b16 v[30:31], v228 offset:29184\n\t"
      "s_add_u32 m0, s21, 86016\n\t"
      "s_nop 0\n\t"
      "global_load_lds_dwordx4 v224, s[6:7]\n\t"
      "global_load_lds_dwordx4 v221, s[6:7] offset:2048\n\t"
      "s_add_u32 m0, s21, 90112\n\t"
      "s_nop 0\n\t"
      "global_load_lds_dwordx4 v222, s[6:7]\n\t"
      "s_cbranch_vccnz WR3_%=\n\t"
      "global_load_lds_dwordx4 v223, s[6:7] offset:2048\n\t"
      "WR3_%=:\n\t"
      "s_sub_u32 s23, s23, 1\n\t"
      "s_cselect_b32 s48, -13, 1\n\t"
      "s_cselect_b32 s49, -1, 0\n\t"
      "s_mul_i32 s48, s48, 28672\n\t"
      "s_add_u32 s6, s6, s48\n\t"
      "s_addc_u32 s7, s7, s49\n\t"
      "v_add_u32_e32 v225, 57344, v225\n\t"
      "v_add_u32_e32 v226, 57344, v226\n\t"
      "v_add_u32_e32 v227, 57344, v227\n\t"
      "v_add_u32_e32 v228, 57344, v228\n\t"
      "s_cbranch_vccnz WW2_%=\n\t"
      "s_waitcnt vmcnt(4)\n\t"
      "s_branch WX2_%=\n\t"
      "WW2_%=:\n\t"
      "s_waitcnt vmcnt(3)\n\t"
      "WX2_%=:\n\t"
      "s_waitcnt lgkmcnt(0)\n\t"
      "s_barrier\n\t"
      "ds_read_b64_tr_b16 v[32:33], v225\n\t"
      "ds_read_b64_tr_b16 v[34:35], v225 offset:512\n\t"
      "ds_read_b64_tr_b16 v[36:37], v226\n\t"
      "ds_read_b64_tr_b16 v[38:39], v226 offset:512\n\t"
      "ds_read_b64_tr_b16 v[40:41], v227\n\t"
      "ds_read_b64_tr_b16 v[42:43], v227 offset:512\n\t"
      "ds_read_b64_tr_b16 v[44:45], v228\n\t"
      "ds_read_b64_tr_b16 v[46:47], v228 offset:512\n\t"
      "s_add_u32 m0, s21, 0\n\t"
      "s_nop 0\n\t"
      "global_load_lds_dwordx4 v224, s[6:7]\n\t"
      "global_load_lds_dwordx4 v221, s[6:7] offset:2048\n\t"
      "s_add_u32 m0, s21, 4096\n\t"
      "s_nop 0\n\t"
      "global_load_lds_dwordx4 v222, s[6:7]\n\t"
      "s_cbranch_vccnz WR4_%=\n\t"
      "global_load_lds_dwordx4 v223, s[6:7] offset:2048\n\t"
      "WR4_%=:\n\t"
      "s_sub_u32 s23, s23, 1\n\t"
      "s_cselect_b32 s48, -13, 1\n\t"
      "s_cselect_b32 s49, -1, 0\n\t"
      "s_mul_i32 s48, s48, 28672\n\t"
      "s_add_u32 s6, s6, s48\n\t"
      "s_addc_u32 s7, s7, s49\n\t"
      "s_cbranch_vccnz WW3_%=\n\t"
      "s_waitcnt vmcnt(4)\n\t"
      "s_branch WX3_%=\n\t"
      "WW3_%=:\n\t"
      "s_waitcnt vmcnt(3)\n\t"
      "WX3_%=:\n\t"
      "s_waitcnt lgkmcnt(0)\n\t"
      "s_barrier\n\t"
      "ds_read_b64_tr_b16 v[48:49], v225 offset:28672\n\t"
      "ds_read_b64_tr_b16 v[50:51], v225 offset:29184\n\t"
      "ds_read_b64_tr_b16 v[52:53], v226 offset:28672\n\t"
      "ds_read_b64_tr_b16 v[54:55], v226 offset:29184\n\t"
      "ds_read_b64_tr_b16 v[56:57], v227 offset:28672\n\t"
      "ds_read_b64_tr_b16 v[58:59], v227 offset:29184\n\t"
      "ds_read_b64_tr_b16 v[60:61], v228 offset:28672\n\t"
      "ds_read_b64_tr_b16 v[62:63], v228 offset:29184\n\t"
      "s_add_u32 m0, s21, 28672\n\t"
      "s_nop 0\n\t"
      "global_load_lds_dwordx4 v224, s[6:7]\n\t"
      "global_load_lds_dwordx4 v221, s[6:7] offset:2048\n\t"
      "s_add_u32 m0, s21, 32768\n\t"
      "s_nop 0\n\t"
      "global_load_lds_dwordx4 v222, s[6:7]\n\t"
      "s_cbranch_vccnz WR5_%=\n\t"
      "global_load_lds_dwordx4 v223, s[6:7] offset:2048\n\t"
      "WR5_%=:\n\t"
      "s_sub_u32 s23, s23, 1\n\t"
      "s_cselect_b32 s48, -13, 1\n\t"
      "s_cselect_b32 s49, -1, 0\n\t"
      "s_mul_i32 s48, s48, 28672\n\t"
      "s_add_u32 s6, s6, s48\n\t"
      "s_addc_u32 s7, s7, s49\n\t"
      "v_add_u32_e32 v225, 4294909952, v225\n\t"
      "v_add_u32_e32 v226, 4294909952, v226\n\t"
      "v_add_u32_e32 v227, 4294909952, v227\n\t"
      "v_add_u32_e32 v228, 4294909952, v228\n\t"
      "s_cbranch_vccnz WW4_%=\n\t"
      "s_waitcnt vmcnt(4)\n\t"
      "s_branch WX4_%=\n\t"
      "WW4_%=:\n\t"
      "s_waitcnt vmcnt(3)\n\t"
      "WX4_%=:\n\t"
      "s_waitcnt lgkmcnt(0)\n\t"
      "s_barrier\n\t"
      "ds_read_b64_tr_b16 v[64:65], v225\n\t"
      "ds_read_b64_tr_b16 v[66:67], v225 offset:512\n\t"
      "ds_read_b64_tr_b16 v[68:69], v226\n\t"
      "ds_read_b64_tr_b16 v[70:71], v226 offset:512\n\t"
      "ds_read_b64_tr_b16 v[72:73], v227\n\t"
      "ds_read_b64_tr_b16 v[74:75], v227 offset:512\n\t"
      "ds_read_b64_tr_b16 v[76:77], v228\n\t"
      "ds_read_b64_tr_b16 v[78:79], v228 offset:512\n\t"
      "s_add_u32 m0, s21, 57344\n\t"
      "s_nop 0\n\t"
      "global_load_lds_dwordx4 v224, s[6:7]\n\t"
      "global_load_lds_dwordx4 v221, s[6:7] offset:2048\n\t"
      "s_add_u32 m0, s21, 61440\n\t"
      "s_nop 0\n\t"
      "global_load_lds_dwordx4 v222, s[6:7]\n\t"
      "s_cbranch_vccnz WR6_%=\n\t"
      "global_load_lds_dwordx4 v223, s[6:7] offset:2048\n\t"
      "WR6_%=:\n\t"
      "s_sub_u32 s23, s23, 1\n\t"
      "s_cselect_b32 s48, -13, 1\n\t"
      "s_cselect_b32 s49, -1, 0\n\t"
      "s_mul_i32 s48, s48, 28672\n\t"
      "s_add_u32 s6, s6, s48\n\t"
      "s_addc_u32 s7, s7, s49\n\t"
      "s_cbranch_vccnz WW5_%=\n\t"
      "s_waitcnt vmcnt(4)\n\t"
      "s_branch WX5_%=\n\t"
      "WW5_%=:\n\t"
      "s_waitcnt vmcnt(3)\n\t"
      "WX5_%=:\n\t"
      "s_waitcnt lgkmcnt(0)\n\t"
      "s_barrier\n\t"
      "ds_read_b64_tr_b16 v[80:81], v225 offset:28672\n\t"
      "ds_read_b64_tr_b16 v[82:83], v225 offset:29184\n\t"
      "ds_read_b64_tr_b16 v[84:85], v226 offset:28672\n\t"
      "ds_read_b64_tr_b16 v[86:87], v226 offset:29184\n\t"
      "ds_read_b64_tr_b16 v[88:89], v227 offset:28672\n\t"
      "ds_read_b64_tr_b16 v[90:91], v227 offset:29184\n\t"
      "ds_read_b64_tr_b16 v[92:93], v228 offset:28672\n\t"
      "ds_read_b64_tr_b16 v[94:95], v228 offset:29184\n\t"
      "s_add_u32 m0, s21, 86016\n\t"
      "s_nop 0\n\t"
      "global_load_lds_dwordx4 v224, s[6:7]\n\t"
      "global_load_lds_dwordx4 v221, s[6:7] offset:2048\n\t"
      "s_add_u32 m0, s21, 90112\n\t"
      "s_nop 0\n\t"
      "global_load_lds_dwordx4 v222, s[6:7]\n\t"
      "s_cbranch_vccnz WR7_%=\n\t"
      "global_load_lds_dwordx4 v223, s[6:7] offset:2048\n\t"
      "WR7_%=:\n\t"
      "s_sub_u32 s23, s23, 1\n\t"
      "s_cselect_b32 s48, -13, 1\n\t"
      "s_cselect_b32 s49, -1, 0\n\t"
      "s_mul_i32 s48, s48, 28672\n\t"
      "s_add_u32 s6, s6, s48\n\t"
      "s_addc_u32 s7, s7, s49\n\t"
      "v_add_u32_e32 v225, 57344, v225\n\t"
      "v_add_u32_e32 v226, 57344, v226\n\t"
      "v_add_u32_e32 v227, 57344, v227\n\t"
      "v_add_u32_e32 v228, 57344, v228\n\t"
      "s_cbranch_vccnz WW6_%=\n\t"
      "s_waitcnt vmcnt(4)\n\t"
      "s_branch WX6_%=\n\t"
      "WW6_%=:\n\t"
      "s_waitcnt vmcnt(3)\n\t"
      "WX6_%=:\n\t"
      "s_waitcnt lgkmcnt(0)\n\t"
      "s_barrier\n\t"
      "ds_read_b64_tr_b16 v[96:97], v225\n\t"
      "ds_read_b64_tr_b16 v[98:99], v225 offset:512\n\t"
      "ds_read_b64_tr_b16 v[100:101], v226\n\t"
      "ds_read_b64_tr_b16 v[102:103], v226 offset:512\n\t"
      "ds_read_b64_tr_b16 v[104:105], v227\n\t"
      "ds_read_b64_tr_b16 v[106:107], v227 offset:512\n\t"
      "ds_read_b64_tr_b16 v[108:109], v228\n\t"
      "ds_read_b64_tr_b16 v[110:111], v228 offset:512\n\t"
      "s_add_u32 m0, s21, 0\n\t"
      "s_nop 0\n\t"
      "global_load_lds_dwordx4 v224, s[6:7]\n\t"
      "global_load_lds_dwordx4 v221, s[6:7] offset:2048\n\t"
      "s_add_u32 m0, s21, 4096\n\t"
      "s_nop 0\n\t"
      "global_load_lds_dwordx4 v222, s[6:7]\n\t"
      "s_cbranch_vccnz WR8_%=\n\t"
      "global_load_lds_dwordx4 v223, s[6:7] offset:2048\n\t"
      "WR8_%=:\n\t"
      "s_sub_u32 s23, s23, 1\n\t"
      "s_cselect_b32 s48, -13, 1\n\t"
      "s_cselect_b32 s49, -1, 0\n\t"
      "s_mul_i32 s48, s48, 28672\n\t"
      "s_add_u32 s6, s6, s48\n\t"
      "s_addc_u32 s7, s7, s49\n\t"
      "s_cbranch_vccnz WW7_%=\n\t"
      "s_waitcnt vmcnt(4)\n\t"
      "s_branch WX7_%=\n\t"
      "WW7_%=:\n\t"
      "s_waitcnt vmcnt(3)\n\t"
      "WX7_%=:\n\t"
      "s_waitcnt lgkmcnt(0)\n\t"
      "s_barrier\n\t"
      "ds_read_b64_tr_b16 v[112:113], v225 offset:28672\n\t"
      "ds_read_b64_tr_b16 v[114:115], v225 offset:29184\n\t"
      "ds_read_b64_tr_b16 v[116:117], v226 offset:28672\n\t"
      "ds_read_b64_tr_b16 v[118:119], v226 offset:29184\n\t"
      "ds_read_b64_tr_b16 v[120:121], v227 offset:28672\n\t"
      "ds_read_b64_tr_b16 v[122:123], v227 offset:29184\n\t"
      "ds_read_b64_tr_b16 v[124:125], v228 offset:28672\n\t"
      "ds_read_b64_tr_b16 v[126:127], v228 offset:29184\n\t"
      "s_add_u32 m0, s21, 28672\n\t"
      "s_nop 0\n\t"
      "global_load_lds_dwordx4 v224, s[6:7]\n\t"
      "global_load_lds_dwordx4 v221, s[6:7] offset:2048\n\t"
      "s_add_u32 m0, s21, 32768\n\t"
      "s_nop 0\n\t"
      "global_load_lds_dwordx4 v222, s[6:7]\n\t"
      "s_cbranch_vccnz WR9_%=\n\t"
      "global_load_lds_dwordx4 v223, s[6:7] offset:2048\n\t"
      "WR9_%=:\n\t"
      "s_sub_u32 s23, s23, 1\n\t"
      "s_cselect_b32 s48, -13, 1\n\t"
      "s_cselect_b32 s49, -1, 0\n\t"
      "s_mul_i32 s48, s48, 28672\n\t"
      "s_add_u32 s6, s6, s48\n\t"
      "s_addc_u32 s7, s7, s49\n\t"
      "v_add_u32_e32 v225, 4294909952, v225\n\t"
      "v_add_u32_e32 v226, 4294909952, v226\n\t"
      "v_add_u32_e32 v227, 4294909952, v227\n\t"
      "v_add_u32_e32 v228, 4294909952, v228\n\t"
      "s_cbranch_vccnz WW8_%=\n\t"
      "s_waitcnt vmcnt(4)\n\t"
      "s_branch WX8_%=\n\t"
      "WW8_%=:\n\t"
      "s_waitcnt vmcnt(3)\n\t"
      "WX8_%=:\n\t"
      "s_waitcnt lgkmcnt(0)\n\t"
      "s_barrier\n\t"
      "ds_read_b64_tr_b16 v[128:129], v225\n\t"
      "ds_read_b64_tr_b16 v[130:131], v225 offset:512\n\t"
      "ds_read_b64_tr_b16 v[132:133], v226\n\t"
      "ds_read_b64_tr_b16 v[134:135], v226 offset:512\n\t"
      "ds_read_b64_tr_b16 v[136:137], v227\n\t"
      "ds_read_b64_tr_b16 v[138:139], v227 offset:512\n\t"
      "ds_read_b64_tr_b16 v[140:141], v228\n\t"
      "ds_read_b64_tr_b16 v[142:143], v228 offset:512\n\t"
      "s_add_u32 m0, s21, 57344\n\t"
      "s_nop 0\n\t"
      "global_load_lds_dwordx4 v224, s[6:7]\n\t"
      "global_load_lds_dwordx4 v221, s[6:7] offset:2048\n\t"
      "s_add_u32 m0, s21, 61440\n\t"
      "s_nop 0\n\t"
      "global_load_lds_dwordx4 v222, s[6:7]\n\t"
      "s_cbranch_vccnz WR10_%=\n\t"
      "global_load_lds_dwordx4 v223, s[6:7] offset:2048\n\t"
      "WR10_%=:\n\t"
      "s_sub_u32 s23, s23, 1\n\t"
      "s_cselect_b32 s48, -13, 1\n\t"
      "s_cselect_b32 s49, -1, 0\n\t"
      "s_mul_i32 s48, s48, 28672\n\t"
      "s_add_u32 s6, s6, s48\n\t"
      "s_addc_u32 s7, s7, s49\n\t"
      "s_cbranch_vccnz WW9_%=\n\t"
      "s_waitcnt vmcnt(4)\n\t"
      "s_branch WX9_%=\n\t"
      "WW9_%=:\n\t"
      "s_waitcnt vmcnt(3)\n\t"
      "WX9_%=:\n\t"
      "s_waitcnt lgkmcnt(0)\n\t"
      "s_barrier\n\t"
      "ds_read_b64_tr_b16 v[144:145], v225 offset:28672\n\t"
      "ds_read_b64_tr_b16 v[146:147], v225 offset:29184\n\t"
      "ds_read_b64_tr_b16 v[148:149], v226 offset:28672\n\t"
      "ds_read_b64_tr_b16 v[150:151], v226 offset:29184\n\t"
      "ds_read_b64_tr_b16 v[152:153], v227 offset:28672\n\t"
      "ds_read_b64_tr_b16 v[154:155], v227 offset:29184\n\t"
      "ds_read_b64_tr_b16 v[156:157], v228 offset:28672\n\t"
      "ds_read_b64_tr_b16 v[158:159], v228 offset:29184\n\t"
      "s_add_u32 m0, s21, 86016\n\t"
      "s_nop 0\n\t"
      "global_load_lds_dwordx4 v224, s[6:7]\n\t"
      "global_load_lds_dwordx4 v221, s[6:7] offset:2048\n\t"
      "s_add_u32 m0, s21, 90112\n\t"
      "s_nop 0\n\t"
      "global_load_lds_dwordx4 v222, s[6:7]\n\t"
      "s_cbranch_vccnz WR11_%=\n\t"
      "global_load_lds_dwordx4 v223, s[6:7] offset:2048\n\t"
      "WR11_%=:\n\t"
      "s_sub_u32 s23, s23, 1\n\t"
      "s_cselect_b32 s48, -13, 1\n\t"
      "s_cselect_b32 s49, -1, 0\n\t"
      "s_mul_i32 s48, s48, 28672\n\t"
      "s_add_u32 s6, s6, s48\n\t"
      "s_addc_u32 s7, s7, s49\n\t"
      "v_add_u32_e32 v225, 57344, v225\n\t"
      "v_add_u32_e32 v226, 57344, v226\n\t"
      "v_add_u32_e32 v227, 57344, v227\n\t"
      "v_add_u32_e32 v228, 57344, v228\n\t"
      "s_waitcnt lgkmcnt(0)\n\t"
      "s_barrier\n\t"
      "s_add_u32 m0, s21, 0\n\t"
      "s_nop 0\n\t"
      "global_load_lds_dwordx4 v224, s[6:7]\n\t"
      "global_load_lds_dwordx4 v221, s[6:7] offset:2048\n\t"
      "s_add_u32 m0, s21, 4096\n\t"
      "s_nop 0\n\t"
      "global_load_lds_dwordx4 v222, s[6:7]\n\t"
      "s_cbranch_vccnz WR12_%=\n\t"
      "global_load_lds_dwordx4 v223, s[6:7] offset:2048\n\t"
      "WR12_%=:\n\t"
      "s_sub_u32 s23, s23, 1\n\t"
      "s_cselect_b32 s48, -13, 1\n\t"
      "s_cselect_b32 s49, -1, 0\n\t"
      "s_mul_i32 s48, s48, 28672\n\t"
      "s_add_u32 s6, s6, s48\n\t"
      "s_addc_u32 s7, s7, s49\n\t"
      "s_add_u32 m0, s21, 28672\n\t"
      "s_nop 0\n\t"
      "global_load_lds_dwordx4 v224, s[6:7]\n\t"
      "global_load_lds_dwordx4 v221, s[6:7] offset:2048\n\t"
      "s_add_u32 m0, s21, 32768\n\t"
      "s_nop 0\n\t"
      "global_load_lds_dwordx4 v222, s[6:7]\n\t"
      "s_cbranch_vccnz WR13_%=\n\t"
      "global_load_lds_dwordx4 v223, s[6:7] offset:2048\n\t"
      "WR13_%=:\n\t"
      "s_waitcnt vmcnt(0)\n\t"
      "s_mov_b32 s51, 0\n\t"
      "s_branch QE_%=\n\t"
      ".p2align 6\n\t"
      "QL_%=:\n\t"
      "s_sleep 2\n\t"
      "s_add_u32 s51, s51, 1\n\t"
      "s_cmp_lt_u32 s51, 262144\n\t"
      "s_cbranch_scc0 QX_%=\n\t"
      "QE_%=:\n\t"
      "s_mov_b64 exec, 3\n\t"
      "v_mov_b64_e32 v[188:189], 0\n\t"
      "global_atomic_or_x2 v[188:189], v246, v[188:189], s[8:9] sc0 sc1\n\t"
      "s_waitcnt vmcnt(0)\n\t"
      "v_cmp_ne_u64_e32 vcc, s[12:13], v[188:189]\n\t"
      "s_mov_b64 exec, -1\n\t"
      "s_cbranch_vccnz QL_%=\n\t"
      "QX_%=:\n\t"
      "s_mov_b32 s51, 0\n\t"
      "s_branch OE_%=\n\t"
      "OL_%=:\n\t"
      "s_sleep 2\n\t"
      "s_add_u32 s51, s51, 1\n\t"
      "s_cmp_lt_u32 s51, 262144\n\t"
      "s_cbranch_scc0 OX_%=\n\t"
      "OE_%=:\n\t"
      "global_load_dwordx4 v[188:191], v236, s[10:11] sc1\n\t"
      "global_load_dwordx4 v[192:195], v236, s[10:11] offset:1024 sc1\n\t"
      "global_load_dword v198, v247, s[8:9] sc1\n\t"
      "s_waitcnt vmcnt(2)\n\t"
      "v_add3_u32 v196, v188, v189, v190\n\t"
      "v_add_u32_e32 v196, v196, v191\n\t"
      "s_waitcnt vmcnt(1)\n\t"
      "v_add3_u32 v197, v192, v193, v194\n\t"
      "v_add_u32_e32 v197, v197, v195\n\t"
      "s_nop 1\n\t"
      "v_permlane32_swap_b32_e32 v196, v197\n\t"
      "v_add_u32_e32 v196, v196, v197\n\t"
      "v_mov_b32_e32 v197, v196\n\t"
      "s_nop 1\n\t"
      "v_permlane16_swap_b32_e32 v196, v197\n\t"
      "v_add3_u32 v196, v196, v197, s12\n\t"
      "s_waitcnt vmcnt(0)\n\t"
      "v_cmp_ne_u32_e32 vcc, v196, v198\n\t"
      "s_cbranch_vccnz OL_%=\n\t"
      "OX_%=:\n\t"
      "s_mov_b64 vcc, s[46:47]\n\t"
      "ds_write_b64 v240, v[188:189]\n\t"
      "ds_write_b64 v242, v[192:193]\n\t"
      "s_mov_b64 exec, s[44:45]\n\t"
      "ds_write_b64 v241, v[190:191]\n\t"
      "ds_write_b64 v243, v[194:195]\n\t"
      "s_mov_b64 exec, -1\n\t"
      "s_waitcnt lgkmcnt(0)\n\t"
      "s_barrier\n\t"
      "ds_read_b128 v[176:179], v230\n\t"
      "ds_read_b128 v[180:183], v230 offset:64\n\t"
      "ds_read_b128 v[184:187], v230 offset:128\n\t"
      "s_waitcnt lgkmcnt(2)\n\t"
      "v_mfma_f32_16x16x32_bf16 v[160:163], v[176:179], v[0:3], v[160:163]\n\t"
      "v_mfma_f32_16x16x32_bf16 v[164:167], v[176:179], v[4:7], v[164:167]\n\t"
      "v_mfma_f32_16x16x32_bf16 v[168:171], v[176:179], v[8:11], v[168:171]\n\t"
      "s_cbranch_vccnz M0_%=\n\t"
      "v_mfma_f32_16x16x32_bf16 v[172:175], v[176:179], v[12:15], v[172:175]\n\t"
      "M0_%=:\n\t"
      "global_store_dwordx4 v231, v[248:251], s[14:15] sc1\n\t"
      "ds_read_b128 v[176:179], v230 offset:192\n\t"
      "s_waitcnt lgkmcnt(2)\n\t"
      "v_mfma_f32_16x16x32_bf16 v[160:163], v[180:183], v[16:19], v[160:163]\n\t"
      "v_mfma_f32_16x16x32_bf16 v[164:167], v[180:183], v[20:23], v[164:167]\n\t"
      "v_mfma_f32_16x16x32_bf16 v[168:171], v[180:183], v[24:27], v[168:171]\n\t"
      "s_cbranch_vccnz M1_%=\n\t"
      "v_mfma_f32_16x16x32_bf16 v[172:175], v[180:183], v[28:31], v[172:175]\n\t"
      "M1_%=:\n\t"
      "s_mov_b64 exec, s[24:25]\n\t"
      "global_store_dwordx4 v237, v[248:251], s[14:15] sc1\n\t"
      "s_mov_b64 exec, -1\n\t"
      "ds_read_b128 v[180:183], v230 offset:256\n\t"
      "s_waitcnt lgkmcnt(2)\n\t"
      "v_mfma_f32_16x16x32_bf16 v[160:163], v[184:187], v[32:35], v[160:163]\n\t"
      "v_mfma_f32_16x16x32_bf16 v[164:167], v[184:187], v[36:39], v[164:167]\n\t"
      "v_mfma_f32_16x16x32_bf16 v[168:171], v[184:187], v[40:43], v[168:171]\n\t"
      "s_cbranch_vccnz M2_%=\n\t"
      "v_mfma_f32_16x16x32_bf16 v[172:175], v[184:187], v[44:47], v[172:175]\n\t"
      "M2_%=:\n\t"
      "ds_read_b64_tr_b16 v[188:189], v225\n\t"
      "ds_read_b64_tr_b16 v[190:191], v225 offset:512\n\t"
      "s_bitcmp1_b32 s20, 6\n\t"
      "s_cbranch_scc0 SG2_%=\n\t"
      "global_store_dwordx4 v236, v[248:251], s[16:17] sc1\n\t"
      "global_store_dwordx4 v236, v[248:251], s[16:17] offset:1024 sc1\n\t"
      "SG2_%=:\n\t"
      "ds_read_b128 v[184:187], v230 offset:320\n\t"
      "s_waitcnt lgkmcnt(4)\n\t"
      "v_mfma_f32_16x16x32_bf16 v[160:163], v[176:179], v[48:51], v[160:163]\n\t"
      "v_mfma_f32_16x16x32_bf16 v[164:167], v[176:179], v[52:55], v[164:167]\n\t"
      "v_mfma_f32_16x16x32_bf16 v[168:171], v[176:179], v[56:59], v[168:171]\n\t"
      "s_cbranch_vccnz M3_%=\n\t"
      "v_mfma_f32_16x16x32_bf16 v[172:175], v[176:179], v[60:63], v[172:175]\n\t"
      "M3_%=:\n\t"
      "ds_read_b64_tr_b16 v[192:193], v226\n\t"
      "ds_read_b64_tr_b16 v[194:195], v226 offset:512\n\t"
      "ds_read_b128 v[176:179], v230 offset:384\n\t"
      "s_waitcnt lgkmcnt(6)\n\t"
      "v_mfma_f32_16x16x32_bf16 v[160:163], v[180:183], v[64:67], v[160:163]\n\t"
      "v_mfma_f32_16x16x32_bf16 v[164:167], v[180:183], v[68:71], v[164:167]\n\t"
      "v_mfma_f32_16x16x32_bf16 v[168:171], v[180:183], v[72:75], v[168:171]\n\t"
      "s_cbranch_vccnz M4_%=\n\t"
      "v_mfma_f32_16x16x32_bf16 v[172:175], v[180:183], v[76:79], v[172:175]\n\t"
      "M4_%=:\n\t"
      "ds_read_b64_tr_b16 v[196:197], v227\n\t"
      "ds_read_b64_tr_b16 v[198:199], v227 offset:512\n\t"
      "s_mov_b32 exec_lo, 0xffffffff\n\t"
      "s_mov_b32 exec_hi, 0x0\n\t"
      "s_mov_b32 m0, 163328\n\t"
      "s_nop 0\n\t"
      "global_load_lds_dword v238, s[4:5]\n\t"
      "s_mov_b64 exec, -1\n\t"
      "ds_read_b128 v[180:183], v230 offset:448\n\t"
      "s_waitcnt lgkmcnt(6)\n\t"
      "v_mfma_f32_16x16x32_bf16 v[160:163], v[184:187], v[80:83], v[160:163]\n\t"
      "v_mfma_f32_16x16x32_bf16 v[164:167], v[184:187], v[84:87], v[164:167]\n\t"
      "v_mfma_f32_16x16x32_bf16 v[168:171], v[184:187], v[88:91], v[168:171]\n\t"
      "s_cbranch_vccnz M5_%=\n\t"
      "v_mfma_f32_16x16x32_bf16 v[172:175], v[184:187], v[92:95], v[172:175]\n\t"
      "M5_%=:\n\t"
      "ds_read_b64_tr_b16 v[200:201], v228\n\t"
      "ds_read_b64_tr_b16 v[202:203], v228 offset:512\n\t"
      "ds_read_b128 v[184:187], v230 offset:512\n\t"
      "s_waitcnt lgkmcnt(6)\n\t"
      "v_mfma_f32_16x16x32_bf16 v[160:163], v[176:179], v[96:99], v[160:163]\n\t"
      "v_mfma_f32_16x16x32_bf16 v[164:167], v[176:179], v[100:103], v[164:167]\n\t"
      "v_mfma_f32_16x16x32_bf16 v[168:171], v[176:179], v[104:107], v[168:171]\n\t"
      "s_cbranch_vccnz M6_%=\n\t"
      "v_mfma_f32_16x16x32_bf16 v[172:175], v[176:179], v[108:111], v[172:175]\n\t"
      "M6_%=:\n\t"
      "ds_read_b64_tr_b16 v[204:205], v225 offset:28672\n\t"
      "ds_read_b64_tr_b16 v[206:207], v225 offset:29184\n\t"
      "ds_read_b128 v[176:179], v230 offset:576\n\t"
      "s_waitcnt lgkmcnt(6)\n\t"
      "v_mfma_f32_16x16x32_bf16 v[160:163], v[180:183], v[112:115], v[160:163]\n\t"
      "v_mfma_f32_16x16x32_bf16 v[164:167], v[180:183], v[116:119], v[164:167]\n\t"
      "v_mfma_f32_16x16x32_bf16 v[168:171], v[180:183], v[120:123], v[168:171]\n\t"
      "s_cbranch_vccnz M7_%=\n\t"
      "v_mfma_f32_16x16x32_bf16 v[172:175], v[180:183], v[124:127], v[172:175]\n\t"
      "M7_%=:\n\t"
      "ds_read_b64_tr_b16 v[208:209], v226 offset:28672\n\t"
      "ds_read_b64_tr_b16 v[210:211], v226 offset:29184\n\t"
      "ds_read_b128 v[180:183], v230 offset:640\n\t"
      "s_waitcnt lgkmcnt(6)\n\t"
      "v_mfma_f32_16x16x32_bf16 v[160:163], v[184:187], v[128:131], v[160:163]\n\t"
      "v_mfma_f32_16x16x32_bf16 v[164:167], v[184:187], v[132:135], v[164:167]\n\t"
      "v_mfma_f32_16x16x32_bf16 v[168:171], v[184:187], v[136:139], v[168:171]\n\t"
      "s_cbranch_vccnz M8_%=\n\t"
      "v_mfma_f32_16x16x32_bf16 v[172:175], v[184:187], v[140:143], v[172:175]\n\t"
      "M8_%=:\n\t"
      "ds_read_b64_tr_b16 v[212:213], v227 offset:28672\n\t"
      "ds_read_b64_tr_b16 v[214:215], v227 offset:29184\n\t"
      "ds_read_b128 v[184:187], v230 offset:704\n\t"
      "s_waitcnt lgkmcnt(6)\n\t"
      "v_mfma_f32_16x16x32_bf16 v[160:163], v[176:179], v[144:147], v[160:163]\n\t"
      "v_mfma_f32_16x16x32_bf16 v[164:167], v[176:179], v[148:151], v[164:167]\n\t"
      "v_mfma_f32_16x16x32_bf16 v[168:171], v[176:179], v[152:155], v[168:171]\n\t"
      "s_cbranch_vccnz M9_%=\n\t"
      "v_mfma_f32_16x16x32_bf16 v[172:175], v[176:179], v[156:159], v[172:175]\n\t"
      "M9_%=:\n\t"
      "ds_read_b64_tr_b16 v[216:217], v228 offset:28672\n\t"
      "ds_read_b64_tr_b16 v[218:219], v228 offset:29184\n\t"
      "ds_read_b128 v[176:179], v230 offset:768\n\t"
      "s_waitcnt lgkmcnt(6)\n\t"
      "v_mfma_f32_16x16x32_bf16 v[160:163], v[180:183], v[188:191], v[160:163]\n\t"
      "v_add_u32_e32 v225, 4294909952, v225\n\t"
      "v_add_u32_e32 v226, 4294909952, v226\n\t"
      "v_add_u32_e32 v227, 4294909952, v227\n\t"
      "v_add_u32_e32 v228, 4294909952, v228\n\t"
      "ds_read_b64_tr_b16 v[188:189], v225\n\t"
      "ds_read_b64_tr_b16 v[190:191], v225 offset:512\n\t"
      "v_mfma_f32_16x16x32_bf16 v[164:167], v[180:183], v[192:195], v[164:167]\n\t"
      "ds_read_b64_tr_b16 v[192:193], v226\n\t"
      "ds_read_b64_tr_b16 v[194:195], v226 offset:512\n\t"
      "v_mfma_f32_16x16x32_bf16 v[168:171], v[180:183], v[196:199], v[168:171]\n\t"
      "ds_read_b64_tr_b16 v[196:197], v227\n\t"
      "ds_read_b64_tr_b16 v[198:199], v227 offset:512\n\t"
      "s_cbranch_vccnz M10_%=\n\t"
      "v_mfma_f32_16x16x32_bf16 v[172:175], v[180:183], v[200:203], v[172:175]\n\t"
      "M10_%=:\n\t"
      "ds_read_b64_tr_b16 v[200:201], v228\n\t"
      "ds_read_b64_tr_b16 v[202:203], v228 offset:512\n\t"
      "ds_read_b128 v[180:183], v230 offset:832\n\t"
      "s_waitcnt lgkmcnt(12)\n\t"
      "v_mfma_f32_16x16x32_bf16 v[160:163], v[184:187], v[204:207], v[160:163]\n\t"
      "ds_read_b64_tr_b16 v[204:205], v225 offset:28672\n\t"
      "ds_read_b64_tr_b16 v[206:207], v225 offset:29184\n\t"
      "v_mfma_f32_16x16x32_bf16 v[164:167], v[184:187], v[208:211], v[164:167]\n\t"
      "ds_read_b64_tr_b16 v[208:209], v226 offset:28672\n\t"
      "ds_read_b64_tr_b16 v[210:211], v226 offset:29184\n\t"
      "v_mfma_f32_16x16x32_bf16 v[168:171], v[184:187], v[212:215], v[168:171]\n\t"
      "ds_read_b64_tr_b16 v[212:213], v227 offset:28672\n\t"
      "ds_read_b64_tr_b16 v[214:215], v227 offset:29184\n\t"
      "s_waitcnt lgkmcnt(14)\n\t"
      "s_cbranch_vccnz M11_%=\n\t"
      "v_mfma_f32_16x16x32_bf16 v[172:175], v[184:187], v[216:219], v[172:175]\n\t"
      "M11_%=:\n\t"
      "ds_read_b64_tr_b16 v[216:217], v228 offset:28672\n\t"
      "ds_read_b64_tr_b16 v[218:219], v228 offset:29184\n\t"
      "s_waitcnt lgkmcnt(14)\n\t"
      "s_waitcnt lgkmcnt(14)\n\t"
      "v_mfma_f32_16x16x32_bf16 v[160:163], v[176:179], v[188:191], v[160:163]\n\t"
      "s_waitcnt lgkmcnt(13)\n\t"
      "v_mfma_f32_16x16x32_bf16 v[164:167], v[176:179], v[192:195], v[164:167]\n\t"
      "s_waitcnt lgkmcnt(11)\n\t"
      "v_mfma_f32_16x16x32_bf16 v[168:171], v[176:179], v[196:199], v[168:171]\n\t"
      "s_waitcnt lgkmcnt(9)\n\t"
      "s_cbranch_vccnz M12_%=\n\t"
      "v_mfma_f32_16x16x32_bf16 v[172:175], v[176:179], v[200:203], v[172:175]\n\t"
      "M12_%=:\n\t"
      "s_waitcnt lgkmcnt(8)\n\t"
      "s_waitcnt lgkmcnt(6)\n\t"
      "v_mfma_f32_16x16x32_bf16 v[160:163], v[180:183], v[204:207], v[160:163]\n\t"
      "s_waitcnt lgkmcnt(4)\n\t"
      "v_mfma_f32_16x16x32_bf16 v[164:167], v[180:183], v[208:211], v[164:167]\n\t"
      "s_waitcnt lgkmcnt(2)\n\t"
      "v_mfma_f32_16x16x32_bf16 v[168:171], v[180:183], v[212:215], v[168:171]\n\t"
      "s_waitcnt lgkmcnt(0)\n\t"
      "s_cbranch_vccnz M13_%=\n\t"
      "v_mfma_f32_16x16x32_bf16 v[172:175], v[180:183], v[216:219], v[172:175]\n\t"
      "M13_%=:\n\t"
      "s_nop 3\n\t"
      "v_pk_add_f32 v[188:189], v[160:161], v[164:165]\n\t"
      "v_pk_mul_f32 v[192:193], v[160:161], v[160:161]\n\t"
      "v_pk_add_f32 v[190:191], v[162:163], v[166:167]\n\t"
      "v_pk_mul_f32 v[194:195], v[162:163], v[162:163]\n\t"
      "v_pk_fma_f32 v[192:193], v[164:165], v[164:165], v[192:193]\n\t"
      "v_pk_fma_f32 v[194:195], v[166:167], v[166:167], v[194:195]\n\t"
      "v_pk_add_f32 v[188:189], v[188:189], v[168:169]\n\t"
      "v_pk_fma_f32 v[192:193], v[168:169], v[168:169], v[192:193]\n\t"
      "v_pk_add_f32 v[190:191], v[190:191], v[170:171]\n\t"
      "v_pk_fma_f32 v[194:195], v[170:171], v[170:171], v[194:195]\n\t"
      "v_pk_add_f32 v[188:189], v[188:189], v[172:173]\n\t"
      "v_pk_fma_f32 v[192:193], v[172:173], v[172:173], v[192:193]\n\t"
      "v_pk_add_f32 v[190:191], v[190:191], v[174:175]\n\t"
      "v_pk_fma_f32 v[194:195], v[174:175], v[174:175], v[194:195]\n\t"
      "v_add_f32_dpp v196, v188, v188 row_ror:8 row_mask:0xf bank_mask:0x3\n\t"
      "s_nop 1\n\t"
      "v_add_f32_dpp v196, v190, v190 row_ror:8 row_mask:0xf bank_mask:0xc\n\t"
      "v_add_f32_dpp v197, v189, v189 row_ror:8 row_mask:0xf bank_mask:0x3\n\t"
      "s_nop 1\n\t"
      "v_add_f32_dpp v197, v191, v191 row_ror:8 row_mask:0xf bank_mask:0xc\n\t"
      "v_add_f32_dpp v198, v192, v192 row_ror:8 row_mask:0xf bank_mask:0x3\n\t"
      "s_nop 1\n\t"
      "v_add_f32_dpp v198, v194, v194 row_ror:8 row_mask:0xf bank_mask:0xc\n\t"
      "v_add_f32_dpp v199, v193, v193 row_ror:8 row_mask:0xf bank_mask:0x3\n\t"
      "s_nop 1\n\t"
      "v_add_f32_dpp v199, v195, v195 row_ror:8 row_mask:0xf bank_mask:0xc\n\t"
      "v_add_f32_dpp v188, v196, v196 row_shl:4 row_mask:0xf bank_mask:0x5\n\t"
      "s_nop 1\n\t"
      "v_add_f32_dpp v188, v197, v197 row_shr:4 row_mask:0xf bank_mask:0xa\n\t"
      "v_add_f32_dpp v189, v198, v198 row_shl:4 row_mask:0xf bank_mask:0x5\n\t"
      "s_nop 1\n\t"
      "v_add_f32_dpp v189, v199, v199 row_shr:4 row_mask:0xf bank_mask:0xa\n\t"
      "v_add_f32_dpp v188, v188, v188 quad_perm:[1,0,3,2] row_mask:0xf bank_mask:0xf\n\t"
      "s_nop 0\n\t"
      "v_add_f32_dpp v189, v189, v189 quad_perm:[1,0,3,2] row_mask:0xf bank_mask:0xf\n\t"
      "v_add_f32_dpp v188, v188, v188 quad_perm:[2,3,0,1] row_mask:0xf bank_mask:0xf\n\t"
      "s_nop 0\n\t"
      "v_add_f32_dpp v189, v189, v189 quad_perm:[2,3,0,1] row_mask:0xf bank_mask:0xf\n\t"
      "ds_write_b64 v232, v[188:189]\n\t"
      "s_waitcnt vmcnt(1)\n\t"
      "s_waitcnt lgkmcnt(0)\n\t"
      "s_barrier\n\t"
      "ds_read_b128 v[188:191], v233\n\t"
      "s_bitcmp1_b32 s20, 4\n\t"
      "s_cbranch_scc0 HI_%=\n\t"
      "v_mov_b32_e32 v220, s12\n\t"
      "v_mov_b32_e32 v221, s13\n\t"
      "s_mov_b64 exec, 255\n\t"
      "global_store_dwordx2 v239, v[220:221], s[8:9] sc1\n\t"
      "s_mov_b64 exec, -1\n\t"
      "HI_%=:\n\t"
      "s_waitcnt lgkmcnt(0)\n\t"
      "v_pk_add_f32 v[188:189], v[188:189], v[190:191]\n\t"
      "s_nop 1\n\t"
      "v_add_f32_dpp v188, v188, v188 row_ror:4 row_mask:0xf bank_mask:0xf\n\t"
      "v_add_f32_dpp v189, v189, v189 row_ror:4 row_mask:0xf bank_mask:0xf\n\t"
      "s_nop 0\n\t"
      "v_add_f32_dpp v188, v188, v188 row_ror:8 row_mask:0xf bank_mask:0xf\n\t"
      "v_add_f32_dpp v189, v189, v189 row_ror:8 row_mask:0xf bank_mask:0xf\n\t"
      "v_mul_f32_e32 v188, 0x3b124925, v188\n\t"
      "v_mul_f32_e32 v189, 0x3b124925, v189\n\t"
      "v_fma_f32 v189, -v188, v188, v189\n\t"
      "v_max_f32_e32 v189, 0, v189\n\t"
      "v_add_f32_e32 v190, 0x3727c5ac, v189\n\t"
      "v_fmac_f32_e32 v189, 0x3727c5ac, v190\n\t"
      "v_rsq_f32_e32 v191, v189\n\t"
      "s_nop 0\n\t"
      "v_mul_f32_e64 v190, -v188, v191\n\t"
      "v_mov_b32_dpp v192, v191 quad_perm:[0,0,0,0] row_mask:0xf bank_mask:0xf\n\t"
      "s_nop 0\n\t"
      "v_mov_b32_dpp v196, v190 quad_perm:[0,0,0,0] row_mask:0xf bank_mask:0xf\n\t"
      "v_mov_b32_dpp v193, v191 quad_perm:[1,1,1,1] row_mask:0xf bank_mask:0xf\n\t"
      "v_mov_b32_dpp v197, v190 quad_perm:[1,1,1,1] row_mask:0xf bank_mask:0xf\n\t"
      "v_mov_b32_dpp v194, v191 quad_perm:[2,2,2,2] row_mask:0xf bank_mask:0xf\n\t"
      "v_mov_b32_dpp v198, v190 quad_perm:[2,2,2,2] row_mask:0xf bank_mask:0xf\n\t"
      "v_mov_b32_dpp v195, v191 quad_perm:[3,3,3,3] row_mask:0xf bank_mask:0xf\n\t"
      "v_mov_b32_dpp v199, v190 quad_perm:[3,3,3,3] row_mask:0xf bank_mask:0xf\n\t"
      "v_pk_fma_f32 v[160:161], v[160:161], v[192:193], v[196:197]\n\t"
      "v_pk_fma_f32 v[162:163], v[162:163], v[194:195], v[198:199]\n\t"
      "v_pk_fma_f32 v[164:165], v[164:165], v[192:193], v[196:197]\n\t"
      "v_pk_fma_f32 v[166:167], v[166:167], v[194:195], v[198:199]\n\t"
      "v_pk_fma_f32 v[168:169], v[168:169], v[192:193], v[196:197]\n\t"
      "v_pk_fma_f32 v[170:171], v[170:171], v[194:195], v[198:199]\n\t"
      "v_pk_fma_f32 v[172:173], v[172:173], v[192:193], v[196:197]\n\t"
      "v_pk_fma_f32 v[174:175], v[174:175], v[194:195], v[198:199]\n\t"
      "v_cvt_pk_bf16_f32 v212, v160, v161\n\t"
      "v_cvt_pk_bf16_f32 v213, v162, v163\n\t"
      "v_cvt_pk_bf16_f32 v214, v164, v165\n\t"
      "v_cvt_pk_bf16_f32 v215, v166, v167\n\t"
      "v_cvt_pk_bf16_f32 v216, v168, v169\n\t"
      "v_cvt_pk_bf16_f32 v217, v170, v171\n\t"
      "v_cvt_pk_bf16_f32 v218, v172, v173\n\t"
      "v_cvt_pk_bf16_f32 v219, v174, v175\n\t"
      "v_min_u32_e32 v212, -2, v212\n\t"
      "v_min_u32_e32 v213, -2, v213\n\t"
      "v_min_u32_e32 v214, -2, v214\n\t"
      "v_min_u32_e32 v215, -2, v215\n\t"
      "v_min_u32_e32 v216, -2, v216\n\t"
      "v_min_u32_e32 v217, -2, v217\n\t"
      "v_min_u32_e32 v218, -2, v218\n\t"
      "v_min_u32_e32 v219, -2, v219\n\t"
      "v_permlane16_swap_b32_e32 v212, v214\n\t"
      "v_permlane16_swap_b32_e32 v213, v215\n\t"
      "v_permlane16_swap_b32_e32 v216, v218\n\t"
      "v_permlane16_swap_b32_e32 v217, v219\n\t"
      "global_store_dwordx4 v231, v[212:215], s[14:15] sc1\n\t"
      "s_mov_b64 exec, s[24:25]\n\t"
      "global_store_dwordx4 v237, v[216:219], s[14:15] sc1\n\t"
      "s_mov_b64 exec, -1\n\t"
      "v_mov_b32_e32 v240, v188\n\t"
      "v_mul_f32_e32 v241, v189, v191\n\t"
      "v_add_u32_e32 v246, 65536, v234\n\t"
      "v_xor_b32_e32 v242, s12, v188\n\t"
      "v_xor_b32_e32 v243, s13, v241\n\t"
      "s_nop 1\n\t"
      : "+{s[4:19]}"(s), "+{s[20:27]}"(s2), "+{s[28:29]}"(pb1), "+{s[30:31]}"(pb2), "+{s[32:33]}"(pob), "+{s[34:35]}"(px), "+{s36}"(lx), "+{s[38:39]}"(pw), "+{v156}"(vtw), "+{v157}"(vtw2), "+{v158}"(vto), "+{v247}"(vck), "+{v[224:239]}"(v), "+{v[240:243]}"(vo), "+{v246}"(vq)
      :
      : "memory", "vcc", "scc",
        "v0", "v1", "v2", "v3", "v4", "v5", "v6", "v7", "v8", "v9", "v10", "v11", "v12", "v13", "v14", "v15", "v16",
        "v17", "v18", "v19", "v20", "v21", "v22", "v23", "v24", "v25", "v26", "v27", "v28", "v29", "v30", "v31", "v32",
        "v33", "v34", "v35", "v36", "v37", "v38", "v39", "v40", "v41", "v42", "v43", "v44", "v45", "v46", "v47", "v48",
        "v49", "v50", "v51", "v52", "v53", "v54", "v55", "v56", "v57", "v58", "v59", "v60", "v61", "v62", "v63", "v64",
        "v65", "v66", "v67", "v68", "v69", "v70", "v71", "v72", "v73", "v74", "v75", "v76", "v77", "v78", "v79", "v80",
        "v81", "v82", "v83", "v84", "v85", "v86", "v87", "v88", "v89", "v90", "v91", "v92", "v93", "v94", "v95", "v96",
        "v97", "v98", "v99", "v100", "v101", "v102", "v103", "v104", "v105", "v106", "v107", "v108", "v109", "v110",
        "v111", "v112", "v113", "v114", "v115", "v116", "v117", "v118", "v119", "v120", "v121", "v122", "v123", "v124",
        "v125", "v126", "v127", "v128", "v129", "v130", "v131", "v132", "v133", "v134", "v135", "v136", "v137", "v138",
        "v139", "v140", "v141", "v142", "v143", "v144", "v145", "v146", "v147", "v148", "v149", "v150", "v151", "v152",
        "v153", "v154", "v155", "v159", "v160", "v161", "v162", "v163", "v164", "v165", "v166", "v167", "v168", "v169",
        "v170", "v171", "v172", "v173", "v174", "v175", "v176", "v177", "v178", "v179", "v180", "v181", "v182", "v183",
        "v184", "v185", "v186", "v187", "v188", "v189", "v190", "v191", "v192", "v193", "v194", "v195", "v196", "v197",
        "v198", "v199", "v200", "v201", "v202", "v203", "v204", "v205", "v206", "v207", "v208", "v209", "v210", "v211",
        "v212", "v213", "v214", "v215", "v216", "v217", "v218", "v219", "v220", "v221", "v222", "v223", "v248", "v249",
        "v250", "v251", "v252", "v253", "v254", "s40", "s41", "s42", "s43", "s44", "s45", "s46", "s47", "s48", "s49",
        "s50", "s51"
    );
    }
    PIN();
    LANE_BITS(__builtin_amdgcn_mbcnt_hi(~0u, __builtin_amdgcn_mbcnt_lo(~0u, 0u)));
    u32x8 v, s2;
    u32x16 s;
    v[0] = lane * 16;
    u64 kr = (u64)(6 - rot) | ((u64)rot << 32);
    v[1] = lr * 4096 + ((lc ^ swz_tr(lr)) << 4);
    v[2] = wave * G_WV + g * 1024 + q4 * 128 + (((2 * (wave & 3) + (p4 >> 1)) ^ swz_tr(8 * g + q4)) << 4) + (p4 & 1) * 8;
    v[3] = wave * 4096 + (wave & 3) * 1024 + ((lane * 16) ^ (g << 6));
    v[4] = (wave & 3) * 1024 + (((p4 * 16 + (lane >> 2)) * 16) ^ (p4 << 6));
    v[5] = (lane >> 2) * 896 + p4 * 8;
    v[6] = c * 2;
    v[7] = ST_H4 + (blk0 * 4 + ((7 * ks) >> 1) + p4) * 128 + xcd * 16384;
    put(s, 0, (u64)(w2 + st * 128 + (ks * 224 + rot * 32) * 4096));
    put(s, 2, (u64)(ws + WS_G + (blk0 * 56 + 7 * ks) * 1024));
    put(s, 4, (u64)(out + (64 * st + 16 * (wave & 3)) * 896 + blk0 * 32));
    put(s, 6, (u64)(b2 + (64 * st + 16 * (wave & 3)) * 2));
    put(s, 8, (u64)(ws + WS_ST));
    s[12] = bits;
    s[13] = wave * G_WV;
    put(s2, 0, (u64)(ws + WS_HN + (blk0 * 64 + 2 * st + ((wave >> 1) & 1)) * 1024));
    s2[2] = ST_FL + (64 * st + 16 * (wave & 3)) * 16;
    s2[3] = UNI(wave & 1 ? ~0u : 0u);
    put(s2, 4, (u64)(ws + WS_EP));
    asm volatile(
      "s_waitcnt lgkmcnt(0)\n\ts_nop 4\n\t"
      "v_mov_b32_e32 v112, 163312\n\t"
      "ds_read_b128 v[112:115], v112\n\t"
      "v_xor_b32_e32 v248, 64, v241\n\t"
      "v_add_u32_e32 v249, 63488, v241\n\t"
      "v_add_u32_e32 v250, 95232, v248\n\t"
      "v_add_u32_e32 v248, 31744, v248\n\t"
      "v_xor_b32_e32 v251, 32, v242\n\t"
      "v_xor_b32_e32 v252, 64, v242\n\t"
      "v_xor_b32_e32 v253, 96, v242\n\t"
      "s_bitcmp1_b32 s16, 4\n\t"
      "s_cbranch_scc0 GW2_%=\n\t"
      "s_mov_b32 s48, 0xf000f\n\t"
      "s_mov_b32 s49, 0xf000f\n\t"
      "s_mov_b64 exec, s[48:49]\n\t"
      "global_store_dwordx4 v140, v[136:139], s[12:13] sc1\n\t"
      "s_mov_b64 exec, -1\n\t"
      "GW2_%=:\n\t"
      "global_load_ushort v236, v246, s[10:11]\n\t"
      "s_waitcnt lgkmcnt(0)\n\t"
      "v_readfirstlane_b32 s14, v112\n\t"
      "v_readfirstlane_b32 s15, v113\n\t"
      "v_readfirstlane_b32 s26, v114\n\t"
      "v_readfirstlane_b32 s27, v115\n\t"
      "s_add_u32 s52, s6, 0\n\t"
      "s_addc_u32 s53, s7, 0\n\t"
      "s_add_u32 s54, s6, 57344\n\t"
      "s_addc_u32 s55, s7, 0\n\t"
      "s_add_u32 s56, s6, 114688\n\t"
      "s_addc_u32 s57, s7, 0\n\t"
      "s_add_u32 s58, s6, 172032\n\t"
      "s_addc_u32 s59, s7, 0\n\t"
      "s_add_u32 s60, s6, 229376\n\t"
      "s_addc_u32 s61, s7, 0\n\t"
      "s_add_u32 s62, s6, 286720\n\t"
      "s_addc_u32 s63, s7, 0\n\t"
      "s_add_u32 s64, s6, 344064\n\t"
      "s_addc_u32 s65, s7, 0\n\t"
      "s_add_u32 s48, s29, 0\n\t"
      "s_cmp_ge_u32 s48, 7\n\t"
      "s_cselect_b32 s49, 7, 0\n\t"
      "s_sub_u32 s48, s48, s49\n\t"
      "v_lshl_add_u32 v224, s48, 10, v240\n\t"
      "s_add_u32 s48, s29, 1\n\t"
      "s_cmp_ge_u32 s48, 7\n\t"
      "s_cselect_b32 s49, 7, 0\n\t"
      "s_sub_u32 s48, s48, s49\n\t"
      "v_lshl_add_u32 v225, s48, 10, v240\n\t"
      "s_add_u32 s48, s29, 2\n\t"
      "s_cmp_ge_u32 s48, 7\n\t"
      "s_cselect_b32 s49, 7, 0\n\t"
      "s_sub_u32 s48, s48, s49\n\t"
      "v_lshl_add_u32 v226, s48, 10, v240\n\t"
      "s_add_u32 s48, s29, 3\n\t"
      "s_cmp_ge_u32 s48, 7\n\t"
      "s_cselect_b32 s49, 7, 0\n\t"
      "s_sub_u32 s48, s48, s49\n\t"
      "v_lshl_add_u32 v227, s48, 10, v240\n\t"
      "s_add_u32 s48, s29, 4\n\t"
      "s_cmp_ge_u32 s48, 7\n\t"
      "s_cselect_b32 s49, 7, 0\n\t"
      "s_sub_u32 s48, s48, s49\n\t"
      "v_lshl_add_u32 v228, s48, 10, v240\n\t"
      "s_add_u32 s48, s29, 5\n\t"
      "s_cmp_ge_u32 s48, 7\n\t"
      "s_cselect_b32 s49, 7, 0\n\t"
      "s_sub_u32 s48, s48, s49\n\t"
      "v_lshl_add_u32 v229, s48, 10, v240\n\t"
      "s_add_u32 s48, s29, 6\n\t"
      "s_cmp_ge_u32 s48, 7\n\t"
      "s_cselect_b32 s49, 7, 0\n\t"
      "s_sub_u32 s48, s48, s49\n\t"
      "v_lshl_add_u32 v230, s48, 10, v240\n\t"
      "s_sleep 6\n\t"
      "s_add_u32 m0, s17, 0\n\t"
      "s_nop 0\n\t"
      "global_load_lds_dwordx4 v241, s[4:5]\n\t"
      "global_load_lds_dwordx4 v248, s[4:5] offset:1024\n\t"
      "global_load_lds_dwordx4 v249, s[4:5] offset:2048\n\t"
      "global_load_lds_dwordx4 v250, s[4:5] offset:3072\n\t"
      "s_sub_u32 s28, s28, 1\n\t"
      "s_cselect_b32 s48, -6, 1\n\t"
      "s_cselect_b32 s49, -1, 0\n\t"
      "s_mul_i32 s48, s48, 131072\n\t"
      "s_add_u32 s4, s4, s48\n\t"
      "s_addc_u32 s5, s5, s49\n\t"
      "s_waitcnt vmcnt(0)\n\t"
      "s_add_u32 m0, s17, 4096\n\t"
      "s_nop 0\n\t"
      "global_load_lds_dwordx4 v241, s[4:5]\n\t"
      "global_load_lds_dwordx4 v248, s[4:5] offset:1024\n\t"
      "global_load_lds_dwordx4 v249, s[4:5] offset:2048\n\t"
      "global_load_lds_dwordx4 v250, s[4:5] offset:3072\n\t"
      "s_sub_u32 s28, s28, 1\n\t"
      "s_cselect_b32 s48, -6, 1\n\t"
      "s_cselect_b32 s49, -1, 0\n\t"
      "s_mul_i32 s48, s48, 131072\n\t"
      "s_add_u32 s4, s4, s48\n\t"
      "s_addc_u32 s5, s5, s49\n\t"
      "ds_read_b64_tr_b16 v[0:1], v242\n\t"
      "ds_read_b64_tr_b16 v[2:3], v242 offset:512\n\t"
      "ds_read_b64_tr_b16 v[4:5], v251\n\t"
      "ds_read_b64_tr_b16 v[6:7], v251 offset:512\n\t"
      "ds_read_b64_tr_b16 v[8:9], v252\n\t"
      "ds_read_b64_tr_b16 v[10:11], v252 offset:512\n\t"
      "ds_read_b64_tr_b16 v[12:13], v253\n\t"
      "ds_read_b64_tr_b16 v[14:15], v253 offset:512\n\t"
      "s_waitcnt vmcnt(0)\n\t"
      "s_add_u32 m0, s17, 8192\n\t"
      "s_nop 0\n\t"
      "global_load_lds_dwordx4 v241, s[4:5]\n\t"
      "global_load_lds_dwordx4 v248, s[4:5] offset:1024\n\t"
      "global_load_lds_dwordx4 v249, s[4:5] offset:2048\n\t"
      "global_load_lds_dwordx4 v250, s[4:5] offset:3072\n\t"
      "s_sub_u32 s28, s28, 1\n\t"
      "s_cselect_b32 s48, -6, 1\n\t"
      "s_cselect_b32 s49, -1, 0\n\t"
      "s_mul_i32 s48, s48, 131072\n\t"
      "s_add_u32 s4, s4, s48\n\t"
      "s_addc_u32 s5, s5, s49\n\t"
      "ds_read_b64_tr_b16 v[16:17], v242 offset:4096\n\t"
      "ds_read_b64_tr_b16 v[18:19], v242 offset:4608\n\t"
      "ds_read_b64_tr_b16 v[20:21], v251 offset:4096\n\t"
      "ds_read_b64_tr_b16 v[22:23], v251 offset:4608\n\t"
      "ds_read_b64_tr_b16 v[24:25], v252 offset:4096\n\t"
      "ds_read_b64_tr_b16 v[26:27], v252 offset:4608\n\t"
      "ds_read_b64_tr_b16 v[28:29], v253 offset:4096\n\t"
      "ds_read_b64_tr_b16 v[30:31], v253 offset:4608\n\t"
      "v_xor_b32_e32 v220, 1024, v243\n\t"
      "v_xor_b32_e32 v221, 2048, v243\n\t"
      "v_xor_b32_e32 v222, 3072, v243\n\t"
      "v_lshlrev_b32_e32 v236, 16, v236\n\t"
      "v_lshrrev_b32_e32 v112, 4, v240\n\t"
      "v_and_b32_e32 v113, 15, v112\n\t"
      "v_lshrrev_b32_e32 v112, 4, v112\n\t"
      "s_lshr_b32 s48, s17, 10\n\t"
      "s_and_b32 s48, s48, 16\n\t"
      "v_lshlrev_b32_e32 v112, 3, v112\n\t"
      "v_sub_u32_e32 v112, v113, v112\n\t"
      "v_add_u32_e32 v112, s48, v112\n\t"
      "v_mov_b32_e32 v114, 0xffff\n\t"
      "v_mov_b32_e32 v115, 0xffff0000\n\t"
      "v_cmp_eq_u32_e32 vcc, 0, v112\n\t"
      "s_nop 1\n\t"
      "v_cndmask_b32_e32 v216, 0, v114, vcc\n\t"
      "v_cmp_eq_u32_e32 vcc, 1, v112\n\t"
      "s_nop 1\n\t"
      "v_cndmask_b32_e32 v216, v216, v115, vcc\n\t"
      "v_cmp_eq_u32_e32 vcc, 2, v112\n\t"
      "s_nop 1\n\t"
      "v_cndmask_b32_e32 v217, 0, v114, vcc\n\t"
      "v_cmp_eq_u32_e32 vcc, 3, v112\n\t"
      "s_nop 1\n\t"
      "v_cndmask_b32_e32 v217, v217, v115, vcc\n\t"
      "v_cmp_eq_u32_e32 vcc, 4, v112\n\t"
      "s_nop 1\n\t"
      "v_cndmask_b32_e32 v218, 0, v114, vcc\n\t"
      "v_cmp_eq_u32_e32 vcc, 5, v112\n\t"
      "s_nop 1\n\t"
      "v_cndmask_b32_e32 v218, v218, v115, vcc\n\t"
      "v_cmp_eq_u32_e32 vcc, 6, v112\n\t"
      "s_nop 1\n\t"
      "v_cndmask_b32_e32 v219, 0, v114, vcc\n\t"
      "v_cmp_eq_u32_e32 vcc, 7, v112\n\t"
      "s_nop 1\n\t"
      "v_cndmask_b32_e32 v219, v219, v115, vcc\n\t"
      "v_and_b32_e32 v116, 0xf0, v240\n\t"
      "v_add_u32_e32 v116, s22, v116\n\t"
      "v_mov_b64_e32 v[184:185], 0\n\t"
      "v_mov_b64_e32 v[186:187], 0\n\t"
      "v_mov_b64_e32 v[188:189], 0\n\t"
      "v_mov_b64_e32 v[190:191], 0\n\t"
      "v_mov_b64_e32 v[192:193], 0\n\t"
      "v_mov_b64_e32 v[194:195], 0\n\t"
      "v_mov_b64_e32 v[196:197], 0\n\t"
      "v_mov_b64_e32 v[198:199], 0\n\t"
      "v_mov_b64_e32 v[200:201], 0\n\t"
      "v_mov_b64_e32 v[202:203], 0\n\t"
      "v_mov_b64_e32 v[204:205], 0\n\t"
      "v_mov_b64_e32 v[206:207], 0\n\t"
      "v_mov_b64_e32 v[208:209], 0\n\t"
      "v_mov_b64_e32 v[210:211], 0\n\t"
      "s_waitcnt vmcnt(0)\n\t"
      "s_add_u32 m0, s17, 12288\n\t"
      "s_nop 0\n\t"
      "global_load_lds_dwordx4 v241, s[4:5]\n\t"
      "global_load_lds_dwordx4 v248, s[4:5] offset:1024\n\t"
      "global_load_lds_dwordx4 v249, s[4:5] offset:2048\n\t"
      "global_load_lds_dwordx4 v250, s[4:5] offset:3072\n\t"
      "s_sub_u32 s28, s28, 1\n\t"
      "s_cselect_b32 s48, -6, 1\n\t"
      "s_cselect_b32 s49, -1, 0\n\t"
      "s_mul_i32 s48, s48, 131072\n\t"
      "s_add_u32 s4, s4, s48\n\t"
      "s_addc_u32 s5, s5, s49\n\t"
      "ds_read_b64_tr_b16 v[32:33], v242 offset:8192\n\t"
      "ds_read_b64_tr_b16 v[34:35], v242 offset:8704\n\t"
      "ds_read_b64_tr_b16 v[36:37], v251 offset:8192\n\t"
      "ds_read_b64_tr_b16 v[38:39], v251 offset:8704\n\t"
      "ds_read_b64_tr_b16 v[40:41], v252 offset:8192\n\t"
      "ds_read_b64_tr_b16 v[42:43], v252 offset:8704\n\t"
      "ds_read_b64_tr_b16 v[44:45], v253 offset:8192\n\t"
      "ds_read_b64_tr_b16 v[46:47], v253 offset:8704\n\t"
      "s_waitcnt vmcnt(0)\n\t"
      "s_waitcnt lgkmcnt(14)\n\t"
      "s_add_u32 m0, s17, 0\n\t"
      "s_nop 0\n\t"
      "global_load_lds_dwordx4 v241, s[4:5]\n\t"
      "global_load_lds_dwordx4 v248, s[4:5] offset:1024\n\t"
      "global_load_lds_dwordx4 v249, s[4:5] offset:2048\n\t"
      "global_load_lds_dwordx4 v250, s[4:5] offset:3072\n\t"
      "s_sub_u32 s28, s28, 1\n\t"
      "s_cselect_b32 s48, -6, 1\n\t"
      "s_cselect_b32 s49, -1, 0\n\t"
      "s_mul_i32 s48, s48, 131072\n\t"
      "s_add_u32 s4, s4, s48\n\t"
      "s_addc_u32 s5, s5, s49\n\t"
      "ds_read_b64_tr_b16 v[48:49], v242 offset:12288\n\t"
      "ds_read_b64_tr_b16 v[50:51], v242 offset:12800\n\t"
      "ds_read_b64_tr_b16 v[52:53], v251 offset:12288\n\t"
      "ds_read_b64_tr_b16 v[54:55], v251 offset:12800\n\t"
      "ds_read_b64_tr_b16 v[56:57], v252 offset:12288\n\t"
      "ds_read_b64_tr_b16 v[58:59], v252 offset:12800\n\t"
      "ds_read_b64_tr_b16 v[60:61], v253 offset:12288\n\t"
      "ds_read_b64_tr_b16 v[62:63], v253 offset:12800\n\t"
      "s_bitcmp1_b32 s16, 1\n\t"
      "s_cbranch_scc0 HWQ_%=\n\t"
      "global_load_dwordx4 v[112:115], v116, s[12:13] sc1\n\t"
      "HWQ_%=:\n\t"
      "s_waitcnt vmcnt(0)\n\t"
      "s_waitcnt lgkmcnt(14)\n\t"
      "s_add_u32 m0, s17, 4096\n\t"
      "s_nop 0\n\t"
      "global_load_lds_dwordx4 v241, s[4:5]\n\t"
      "global_load_lds_dwordx4 v248, s[4:5] offset:1024\n\t"
      "global_load_lds_dwordx4 v249, s[4:5] offset:2048\n\t"
      "global_load_lds_dwordx4 v250, s[4:5] offset:3072\n\t"
      "s_sub_u32 s28, s28, 1\n\t"
      "s_cselect_b32 s48, -6, 1\n\t"
      "s_cselect_b32 s49, -1, 0\n\t"
      "s_mul_i32 s48, s48, 131072\n\t"
      "s_add_u32 s4, s4, s48\n\t"
      "s_addc_u32 s5, s5, s49\n\t"
      "ds_read_b64_tr_b16 v[64:65], v242\n\t"
      "ds_read_b64_tr_b16 v[66:67], v242 offset:512\n\t"
      "ds_read_b64_tr_b16 v[68:69], v251\n\t"
      "ds_read_b64_tr_b16 v[70:71], v251 offset:512\n\t"
      "ds_read_b64_tr_b16 v[72:73], v252\n\t"
      "ds_read_b64_tr_b16 v[74:75], v252 offset:512\n\t"
      "ds_read_b64_tr_b16 v[76:77], v253\n\t"
      "ds_read_b64_tr_b16 v[78:79], v253 offset:512\n\t"
      "s_waitcnt vmcnt(0)\n\t"
      "s_waitcnt lgkmcnt(14)\n\t"
      "s_add_u32 m0, s17, 8192\n\t"
      "s_nop 0\n\t"
      "global_load_lds_dwordx4 v241, s[4:5]\n\t"
      "global_load_lds_dwordx4 v248, s[4:5] offset:1024\n\t"
      "global_load_lds_dwordx4 v249, s[4:5] offset:2048\n\t"
      "global_load_lds_dwordx4 v250, s[4:5] offset:3072\n\t"
      "ds_read_b64_tr_b16 v[80:81], v242 offset:4096\n\t"
      "ds_read_b64_tr_b16 v[82:83], v242 offset:4608\n\t"
      "ds_read_b64_tr_b16 v[84:85], v251 offset:4096\n\t"
      "ds_read_b64_tr_b16 v[86:87], v251 offset:4608\n\t"
      "ds_read_b64_tr_b16 v[88:89], v252 offset:4096\n\t"
      "ds_read_b64_tr_b16 v[90:91], v252 offset:4608\n\t"
      "ds_read_b64_tr_b16 v[92:93], v253 offset:4096\n\t"
      "ds_read_b64_tr_b16 v[94:95], v253 offset:4608\n\t"
      "s_bitcmp1_b32 s16, 1\n\t"
      "s_cbranch_scc0 HWH_%=\n\t"
      "v_xor_b32_e32 v114, v114, v112\n\t"
      "v_xor_b32_e32 v115, v115, v113\n\t"
      "v_cmp_ne_u64_e32 vcc, s[14:15], v[114:115]\n\t"
      "s_cbranch_vccnz HWR_%=\n\t"
      "HWB_%=:\n\t"
      "s_mov_b32 exec_hi, s23\n\t"
      "s_not_b32 exec_lo, s23\n\t"
      "s_add_u32 s68, s20, 0\n\t"
      "s_addc_u32 s69, s21, 0\n\t"
      "global_load_dwordx4 v[184:187], v240, s[68:69] sc1\n\t"
      "s_add_u32 s68, s20, 65536\n\t"
      "s_addc_u32 s69, s21, 0\n\t"
      "global_load_dwordx4 v[188:191], v240, s[68:69] sc1\n\t"
      "s_add_u32 s68, s20, 131072\n\t"
      "s_addc_u32 s69, s21, 0\n\t"
      "global_load_dwordx4 v[192:195], v240, s[68:69] sc1\n\t"
      "s_add_u32 s68, s20, 196608\n\t"
      "s_addc_u32 s69, s21, 0\n\t"
      "global_load_dwordx4 v[196:199], v240, s[68:69] sc1\n\t"
      "s_add_u32 s68, s20, 262144\n\t"
      "s_addc_u32 s69, s21, 0\n\t"
      "global_load_dwordx4 v[200:203], v240, s[68:69] sc1\n\t"
      "s_add_u32 s68, s20, 327680\n\t"
      "s_addc_u32 s69, s21, 0\n\t"
      "global_load_dwordx4 v[204:207], v240, s[68:69] sc1\n\t"
      "s_add_u32 s68, s20, 393216\n\t"
      "s_addc_u32 s69, s21, 0\n\t"
      "global_load_dwordx4 v[208:211], v240, s[68:69] sc1\n\t"
      "s_mov_b64 exec, -1\n\t"
      "v_add_f32_e32 v236, v236, v112\n\t"
      "v_cvt_pk_bf16_f32 v114, v113, v113\n\t"
      "v_lshlrev_b32_e32 v115, 16, v114\n\t"
      "v_sub_f32_e32 v115, v113, v115\n\t"
      "v_cvt_pk_bf16_f32 v115, v115, v115\n\t"
      "v_mov_b32_e32 v237, v236\n\t"
      "v_mov_b32_e32 v238, v236\n\t"
      "v_mov_b32_e32 v239, v236\n\t"
      "v_and_b32_e32 v128, v216, v115\n\t"
      "v_and_b32_e32 v216, v216, v114\n\t"
      "v_and_b32_e32 v129, v217, v115\n\t"
      "v_and_b32_e32 v217, v217, v114\n\t"
      "v_and_b32_e32 v130, v218, v115\n\t"
      "v_and_b32_e32 v218, v218, v114\n\t"
      "v_and_b32_e32 v131, v219, v115\n\t"
      "v_and_b32_e32 v219, v219, v114\n\t"
      "HWH_%=:\n\t"
      "s_waitcnt vmcnt(0)\n\t"
      "ds_read_b64_tr_b16 v[96:97], v242 offset:8192\n\t"
      "ds_read_b64_tr_b16 v[98:99], v242 offset:8704\n\t"
      "ds_read_b64_tr_b16 v[100:101], v251 offset:8192\n\t"
      "ds_read_b64_tr_b16 v[102:103], v251 offset:8704\n\t"
      "ds_read_b64_tr_b16 v[104:105], v252 offset:8192\n\t"
      "ds_read_b64_tr_b16 v[106:107], v252 offset:8704\n\t"
      "ds_read_b64_tr_b16 v[108:109], v253 offset:8192\n\t"
      "ds_read_b64_tr_b16 v[110:111], v253 offset:8704\n\t"
      "s_mov_b64 exec, 0xf\n\t"
      "v_mov_b64_e32 v[232:233], 0\n\t"
      "global_atomic_or_x2 v[232:233], v247, v[232:233], s[12:13] sc0 sc1\n\t"
      "s_mov_b64 exec, -1\n\t"
      "s_bitcmp1_b32 s16, 1\n\t"
      "s_cbranch_scc0 HWE_%=\n\t"
      "s_waitcnt vmcnt(1)\n\t"
      "v_max3_u32 v234, v184, v185, v186\n\t"
      "v_max3_u32 v234, v234, v187, v188\n\t"
      "v_max3_u32 v234, v234, v189, v190\n\t"
      "v_max3_u32 v234, v234, v191, v192\n\t"
      "v_max3_u32 v234, v234, v193, v194\n\t"
      "v_max3_u32 v234, v234, v195, v196\n\t"
      "v_max3_u32 v234, v234, v197, v198\n\t"
      "v_max3_u32 v234, v234, v199, v200\n\t"
      "v_max3_u32 v234, v234, v201, v202\n\t"
      "v_max3_u32 v234, v234, v203, v204\n\t"
      "v_max3_u32 v234, v234, v205, v206\n\t"
      "v_max3_u32 v234, v234, v207, v208\n\t"
      "v_max3_u32 v234, v234, v209, v210\n\t"
      "v_max_u32_e32 v234, v234, v211\n\t"
      "v_cmp_eq_u32_e32 vcc, -1, v234\n\t"
      "s_cbranch_vccnz HXR_%=\n\t"
      "HXB_%=:\n\t"
      "v_mfma_f32_16x16x32_bf16 v[156:159], v[184:187], v[128:131], v[236:239]\n\t"
      "v_mfma_f32_16x16x32_bf16 v[160:163], v[188:191], v[128:131], v[236:239]\n\t"
      "v_mfma_f32_16x16x32_bf16 v[164:167], v[192:195], v[128:131], v[236:239]\n\t"
      "v_mfma_f32_16x16x32_bf16 v[168:171], v[196:199], v[128:131], v[236:239]\n\t"
      "v_mfma_f32_16x16x32_bf16 v[172:175], v[200:203], v[128:131], v[236:239]\n\t"
      "v_mfma_f32_16x16x32_bf16 v[176:179], v[204:207], v[128:131], v[236:239]\n\t"
      "v_mfma_f32_16x16x32_bf16 v[180:183], v[208:211], v[128:131], v[236:239]\n\t"
      "v_mfma_f32_16x16x32_bf16 v[156:159], v[184:187], v[216:219], v[156:159]\n\t"
      "v_mfma_f32_16x16x32_bf16 v[160:163], v[188:191], v[216:219], v[160:163]\n\t"
      "v_mfma_f32_16x16x32_bf16 v[164:167], v[192:195], v[216:219], v[164:167]\n\t"
      "v_mfma_f32_16x16x32_bf16 v[168:171], v[196:199], v[216:219], v[168:171]\n\t"
      "v_mfma_f32_16x16x32_bf16 v[172:175], v[200:203], v[216:219], v[172:175]\n\t"
      "v_mfma_f32_16x16x32_bf16 v[176:179], v[204:207], v[216:219], v[176:179]\n\t"
      "v_mfma_f32_16x16x32_bf16 v[180:183], v[208:211], v[216:219], v[180:183]\n\t"
      "HWE_%=:\n\t"
      "s_waitcnt lgkmcnt(0)\n\t"
      "s_barrier\n\t"
      "s_mov_b64 exec, 15\n\t"
      "s_waitcnt vmcnt(0)\n\t"
      "v_subrev_u32_e32 v232, s14, v232\n\t"
      "v_cmp_ne_u32_e64 s[48:49], s15, v233\n\t"
      "v_subrev_u32_e32 v232, 0, v232\n\t"
      "v_cmp_lt_u32_e32 vcc, 7, v232\n\t"
      "s_mov_b64 exec, -1\n\t"
      "s_or_b64 vcc, vcc, s[48:49]\n\t"
      "s_cbranch_vccz PRY_%=\n\t"
      "s_mov_b32 s51, 0\n\t"
      "s_branch PRE_%=\n\t"
      "PRL_%=:\n\t"
      "s_sleep 4\n\t"
      "s_add_u32 s51, s51, 1\n\t"
      "s_cmp_lt_u32 s51, 262144\n\t"
      "s_cbranch_scc0 PRX_%=\n\t"
      "PRE_%=:\n\t"
      "s_mov_b64 exec, 15\n\t"
      "v_mov_b64_e32 v[232:233], 0\n\t"
      "global_atomic_or_x2 v[232:233], v247, v[232:233], s[12:13] sc0 sc1\n\t"
      "s_waitcnt vmcnt(0)\n\t"
      "v_subrev_u32_e32 v232, s14, v232\n\t"
      "v_cmp_ne_u32_e64 s[48:49], s15, v233\n\t"
      "v_subrev_u32_e32 v232, 0, v232\n\t"
      "v_cmp_lt_u32_e32 vcc, 7, v232\n\t"
      "s_mov_b64 exec, -1\n\t"
      "s_or_b64 vcc, vcc, s[48:49]\n\t"
      "s_cbranch_vccnz PRL_%=\n\t"
      "PRX_%=:\n\t"
      "PRY_%=:\n\t"
      "global_load_dwordx4 v[128:131], v224, s[52:53] sc1\n\t"
      "global_load_dwordx4 v[132:135], v225, s[52:53] sc1\n\t"
      "global_load_dwordx4 v[136:139], v226, s[52:53] sc1\n\t"
      "global_load_dwordx4 v[140:143], v227, s[52:53] sc1\n\t"
      "global_load_dwordx4 v[144:147], v228, s[52:53] sc1\n\t"
      "global_load_dwordx4 v[148:151], v229, s[52:53] sc1\n\t"
      "global_load_dwordx4 v[152:155], v230, s[52:53] sc1\n\t"
      "s_cmp_ge_u32 s17, 65536\n\t"
      "s_cbranch_scc1 GQ_%=\n\t"
      ".p2align 6\n\t"
      "s_setprio 1\n\t"
      "s_waitcnt vmcnt(6)\n\t"
      "v_max3_u32 v234, v128, v129, v130\n\t"
      "v_max_u32_e32 v234, v234, v131\n\t"
      "v_cmp_eq_u32_e32 vcc, -1, v234\n\t"
      "s_cbranch_vccnz FP0R_%=\n\t"
      "FP0B_%=:\n\t"
      "v_mfma_f32_16x16x32_bf16 v[184:187], v[128:131], v[0:3], v[156:159]\n\t"
      "v_mfma_f32_16x16x32_bf16 v[188:191], v[128:131], v[4:7], 0\n\t"
      "v_mfma_f32_16x16x32_bf16 v[192:195], v[128:131], v[8:11], 0\n\t"
      "v_mfma_f32_16x16x32_bf16 v[196:199], v[128:131], v[12:15], 0\n\t"
      "global_load_dwordx4 v[128:131], v224, s[54:55] sc1\n\t"
      "s_waitcnt vmcnt(6)\n\t"
      "v_max3_u32 v234, v132, v133, v134\n\t"
      "v_max_u32_e32 v234, v234, v135\n\t"
      "v_cmp_eq_u32_e32 vcc, -1, v234\n\t"
      "s_cbranch_vccnz FP1R_%=\n\t"
      "FP1B_%=:\n\t"
      "v_mfma_f32_16x16x32_bf16 v[184:187], v[132:135], v[16:19], v[184:187]\n\t"
      "v_mfma_f32_16x16x32_bf16 v[188:191], v[132:135], v[20:23], v[188:191]\n\t"
      "v_mfma_f32_16x16x32_bf16 v[192:195], v[132:135], v[24:27], v[192:195]\n\t"
      "v_mfma_f32_16x16x32_bf16 v[196:199], v[132:135], v[28:31], v[196:199]\n\t"
      "global_load_dwordx4 v[132:135], v225, s[54:55] sc1\n\t"
      "s_waitcnt vmcnt(6)\n\t"
      "v_max3_u32 v234, v136, v137, v138\n\t"
      "v_max_u32_e32 v234, v234, v139\n\t"
      "v_cmp_eq_u32_e32 vcc, -1, v234\n\t"
      "s_cbranch_vccnz FP2R_%=\n\t"
      "FP2B_%=:\n\t"
      "v_mfma_f32_16x16x32_bf16 v[184:187], v[136:139], v[32:35], v[184:187]\n\t"
      "v_mfma_f32_16x16x32_bf16 v[188:191], v[136:139], v[36:39], v[188:191]\n\t"
      "v_mfma_f32_16x16x32_bf16 v[192:195], v[136:139], v[40:43], v[192:195]\n\t"
      "v_mfma_f32_16x16x32_bf16 v[196:199], v[136:139], v[44:47], v[196:199]\n\t"
      "global_load_dwordx4 v[136:139], v226, s[54:55] sc1\n\t"
      "s_waitcnt vmcnt(6)\n\t"
      "v_max3_u32 v234, v140, v141, v142\n\t"
      "v_max_u32_e32 v234, v234, v143\n\t"
      "v_cmp_eq_u32_e32 vcc, -1, v234\n\t"
      "s_cbranch_vccnz FP3R_%=\n\t"
      "FP3B_%=:\n\t"
      "v_mfma_f32_16x16x32_bf16 v[184:187], v[140:143], v[48:51], v[184:187]\n\t"
      "v_mfma_f32_16x16x32_bf16 v[188:191], v[140:143], v[52:55], v[188:191]\n\t"
      "v_mfma_f32_16x16x32_bf16 v[192:195], v[140:143], v[56:59], v[192:195]\n\t"
      "v_mfma_f32_16x16x32_bf16 v[196:199], v[140:143], v[60:63], v[196:199]\n\t"
      "global_load_dwordx4 v[140:143], v227, s[54:55] sc1\n\t"
      "s_waitcnt vmcnt(6)\n\t"
      "v_max3_u32 v234, v144, v145, v146\n\t"
      "v_max_u32_e32 v234, v234, v147\n\t"
      "v_cmp_eq_u32_e32 vcc, -1, v234\n\t"
      "s_cbranch_vccnz FP4R_%=\n\t"
      "FP4B_%=:\n\t"
      "v_mfma_f32_16x16x32_bf16 v[184:187], v[144:147], v[64:67], v[184:187]\n\t"
      "v_mfma_f32_16x16x32_bf16 v[188:191], v[144:147], v[68:71], v[188:191]\n\t"
      "v_mfma_f32_16x16x32_bf16 v[192:195], v[144:147], v[72:75], v[192:195]\n\t"
      "v_mfma_f32_16x16x32_bf16 v[196:199], v[144:147], v[76:79], v[196:199]\n\t"
      "global_load_dwordx4 v[144:147], v228, s[54:55] sc1\n\t"
      "s_waitcnt vmcnt(6)\n\t"
      "v_max3_u32 v234, v148, v149, v150\n\t"
      "v_max_u32_e32 v234, v234, v151\n\t"
      "v_cmp_eq_u32_e32 vcc, -1, v234\n\t"
      "s_cbranch_vccnz FP5R_%=\n\t"
      "FP5B_%=:\n\t"
      "v_mfma_f32_16x16x32_bf16 v[184:187], v[148:151], v[80:83], v[184:187]\n\t"
      "v_mfma_f32_16x16x32_bf16 v[188:191], v[148:151], v[84:87], v[188:191]\n\t"
      "v_mfma_f32_16x16x32_bf16 v[192:195], v[148:151], v[88:91], v[192:195]\n\t"
      "v_mfma_f32_16x16x32_bf16 v[196:199], v[148:151], v[92:95], v[196:199]\n\t"
      "global_load_dwordx4 v[148:151], v229, s[54:55] sc1\n\t"
      "s_waitcnt vmcnt(6)\n\t"
      "v_max3_u32 v234, v152, v153, v154\n\t"
      "v_max_u32_e32 v234, v234, v155\n\t"
      "v_cmp_eq_u32_e32 vcc, -1, v234\n\t"
      "s_cbranch_vccnz FP6R_%=\n\t"
      "FP6B_%=:\n\t"
      "v_mfma_f32_16x16x32_bf16 v[184:187], v[152:155], v[96:99], v[184:187]\n\t"
      "s_waitcnt vmcnt(5)\n\t"
      "v_cmp_eq_u32_e32 vcc, -1, v128\n\t"
      "v_mfma_f32_16x16x32_bf16 v[188:191], v[152:155], v[100:103], v[188:191]\n\t"
      "v_mfma_f32_16x16x32_bf16 v[192:195], v[152:155], v[104:107], v[192:195]\n\t"
      "s_cbranch_vccnz FP7R_%=\n\t"
      "FP7B_%=:\n\t"
      "v_mfma_f32_16x16x32_bf16 v[196:199], v[152:155], v[108:111], v[196:199]\n\t"
      "global_load_dwordx4 v[152:155], v230, s[54:55] sc1\n\t"
      "s_nop 0\n\t"
      "ds_write_b128 v243, v[184:187]\n\t"
      "s_nop 1\n\t"
      "ds_write_b128 v220, v[188:191]\n\t"
      "ds_write_b128 v221, v[192:195]\n\t"
      "s_nop 0\n\t"
      "ds_write_b128 v222, v[196:199]\n\t"
      "s_waitcnt lgkmcnt(0)\n\t"
      "s_barrier\n\t"
      "ds_read_b128 v[112:115], v244\n\t"
      "ds_read_b128 v[116:119], v244 offset:4096\n\t"
      "ds_read_b128 v[120:123], v244 offset:8192\n\t"
      "ds_read_b128 v[124:127], v244 offset:12288\n\t"
      "ds_read_b128 v[184:187], v244 offset:16384\n\t"
      "ds_read_b128 v[188:191], v244 offset:20480\n\t"
      "ds_read_b128 v[192:195], v244 offset:24576\n\t"
      "ds_read_b128 v[196:199], v244 offset:28672\n\t"
      "v_mfma_f32_16x16x32_bf16 v[200:203], v[128:131], v[0:3], v[160:163]\n\t"
      "s_waitcnt vmcnt(5)\n\t"
      "v_cmp_eq_u32_e32 vcc, -1, v132\n\t"
      "v_mfma_f32_16x16x32_bf16 v[204:207], v[128:131], v[4:7], 0\n\t"
      "s_waitcnt lgkmcnt(6)\n\t"
      "v_add_f32_e32 v112, v112, v116\n\t"
      "v_mfma_f32_16x16x32_bf16 v[208:211], v[128:131], v[8:11], 0\n\t"
      "s_cbranch_vccnz FP8R_%=\n\t"
      "FP8B_%=:\n\t"
      "v_add_f32_e32 v113, v113, v117\n\t"
      "v_add_f32_e32 v114, v114, v118\n\t"
      "v_mfma_f32_16x16x32_bf16 v[212:215], v[128:131], v[12:15], 0\n\t"
      "global_load_dwordx4 v[128:131], v224, s[56:57] sc1\n\t"
      "v_add_f32_e32 v115, v115, v119\n\t"
      "v_mfma_f32_16x16x32_bf16 v[200:203], v[132:135], v[16:19], v[200:203]\n\t"
      "s_waitcnt vmcnt(5)\n\t"
      "v_cmp_eq_u32_e32 vcc, -1, v136\n\t"
      "s_waitcnt lgkmcnt(4)\n\t"
      "v_add_f32_e32 v120, v120, v124\n\t"
      "v_mfma_f32_16x16x32_bf16 v[204:207], v[132:135], v[20:23], v[204:207]\n\t"
      "v_add_f32_e32 v121, v121, v125\n\t"
      "v_add_f32_e32 v122, v122, v126\n\t"
      "v_mfma_f32_16x16x32_bf16 v[208:211], v[132:135], v[24:27], v[208:211]\n\t"
      "s_cbranch_vccnz FP9R_%=\n\t"
      "FP9B_%=:\n\t"
      "v_add_f32_e32 v123, v123, v127\n\t"
      "s_waitcnt lgkmcnt(2)\n\t"
      "v_add_f32_e32 v184, v184, v188\n\t"
      "v_mfma_f32_16x16x32_bf16 v[212:215], v[132:135], v[28:31], v[212:215]\n\t"
      "global_load_dwordx4 v[132:135], v225, s[56:57] sc1\n\t"
      "v_add_f32_e32 v185, v185, v189\n\t"
      "v_mfma_f32_16x16x32_bf16 v[200:203], v[136:139], v[32:35], v[200:203]\n\t"
      "s_waitcnt vmcnt(5)\n\t"
      "v_cmp_eq_u32_e32 vcc, -1, v140\n\t"
      "v_add_f32_e32 v186, v186, v190\n\t"
      "v_mfma_f32_16x16x32_bf16 v[204:207], v[136:139], v[36:39], v[204:207]\n\t"
      "v_add_f32_e32 v187, v187, v191\n\t"
      "v_add_f32_e32 v112, v112, v120\n\t"
      "v_mfma_f32_16x16x32_bf16 v[208:211], v[136:139], v[40:43], v[208:211]\n\t"
      "s_cbranch_vccnz FP10R_%=\n\t"
      "FP10B_%=:\n\t"
      "v_add_f32_e32 v113, v113, v121\n\t"
      "v_add_f32_e32 v114, v114, v122\n\t"
      "v_mfma_f32_16x16x32_bf16 v[212:215], v[136:139], v[44:47], v[212:215]\n\t"
      "global_load_dwordx4 v[136:139], v226, s[56:57] sc1\n\t"
      "v_add_f32_e32 v115, v115, v123\n\t"
      "v_mfma_f32_16x16x32_bf16 v[200:203], v[140:143], v[48:51], v[200:203]\n\t"
      "s_waitcnt vmcnt(5)\n\t"
      "v_cmp_eq_u32_e32 vcc, -1, v144\n\t"
      "s_waitcnt lgkmcnt(0)\n\t"
      "v_add_f32_e32 v192, v192, v196\n\t"
      "v_mfma_f32_16x16x32_bf16 v[204:207], v[140:143], v[52:55], v[204:207]\n\t"
      "v_add_f32_e32 v193, v193, v197\n\t"
      "v_add_f32_e32 v194, v194, v198\n\t"
      "v_mfma_f32_16x16x32_bf16 v[208:211], v[140:143], v[56:59], v[208:211]\n\t"
      "s_cbranch_vccnz FP11R_%=\n\t"
      "FP11B_%=:\n\t"
      "v_add_f32_e32 v195, v195, v199\n\t"
      "v_add_f32_e32 v184, v184, v192\n\t"
      "v_mfma_f32_16x16x32_bf16 v[212:215], v[140:143], v[60:63], v[212:215]\n\t"
      "global_load_dwordx4 v[140:143], v227, s[56:57] sc1\n\t"
      "v_add_f32_e32 v185, v185, v193\n\t"
      "v_mfma_f32_16x16x32_bf16 v[200:203], v[144:147], v[64:67], v[200:203]\n\t"
      "s_waitcnt vmcnt(5)\n\t"
      "v_cmp_eq_u32_e32 vcc, -1, v148\n\t"
      "v_add_f32_e32 v186, v186, v194\n\t"
      "v_mfma_f32_16x16x32_bf16 v[204:207], v[144:147], v[68:71], v[204:207]\n\t"
      "v_add_f32_e32 v187, v187, v195\n\t"
      "v_add_f32_e32 v112, v112, v184\n\t"
      "v_mfma_f32_16x16x32_bf16 v[208:211], v[144:147], v[72:75], v[208:211]\n\t"
      "s_cbranch_vccnz FP12R_%=\n\t"
      "FP12B_%=:\n\t"
      "v_add_f32_e32 v113, v113, v185\n\t"
      "v_add_f32_e32 v114, v114, v186\n\t"
      "v_mfma_f32_16x16x32_bf16 v[212:215], v[144:147], v[76:79], v[212:215]\n\t"
      "global_load_dwordx4 v[144:147], v228, s[56:57] sc1\n\t"
      "v_add_f32_e32 v115, v115, v187\n\t"
      "v_mfma_f32_16x16x32_bf16 v[200:203], v[148:151], v[80:83], v[200:203]\n\t"
      "s_waitcnt vmcnt(5)\n\t"
      "v_cmp_eq_u32_e32 vcc, -1, v152\n\t"
      "v_cvt_pk_bf16_f32 v116, v112, v113\n\t"
      "v_mfma_f32_16x16x32_bf16 v[204:207], v[148:151], v[84:87], v[204:207]\n\t"
      "v_cvt_pk_bf16_f32 v117, v114, v115\n\t"
      "global_store_dwordx2 v245, v[116:117], s[8:9]\n\t"
      "v_mfma_f32_16x16x32_bf16 v[208:211], v[148:151], v[88:91], v[208:211]\n\t"
      "s_cbranch_vccnz FP13R_%=\n\t"
      "FP13B_%=:\n\t"
      "v_mfma_f32_16x16x32_bf16 v[212:215], v[148:151], v[92:95], v[212:215]\n\t"
      "global_load_dwordx4 v[148:151], v229, s[56:57] sc1\n\t"
      "v_mfma_f32_16x16x32_bf16 v[200:203], v[152:155], v[96:99], v[200:203]\n\t"
      "s_waitcnt vmcnt(6)\n\t"
      "v_cmp_eq_u32_e32 vcc, -1, v128\n\t"
      "v_mfma_f32_16x16x32_bf16 v[204:207], v[152:155], v[100:103], v[204:207]\n\t"
      "v_mfma_f32_16x16x32_bf16 v[208:211], v[152:155], v[104:107], v[208:211]\n\t"
      "s_cbranch_vccnz FP14R_%=\n\t"
      "FP14B_%=:\n\t"
      "v_mfma_f32_16x16x32_bf16 v[212:215], v[152:155], v[108:111], v[212:215]\n\t"
      "global_load_dwordx4 v[152:155], v230, s[56:57] sc1\n\t"
      "s_nop 0\n\t"
      "v_cmp_u_f32_e32 vcc, v200, v201\n\t"
      "v_cmp_u_f32_e64 s[48:49], v202, v203\n\t"
      "s_nop 0\n\t"
      "s_or_b64 vcc, vcc, s[48:49]\n\t"
      "s_cbranch_scc1 NP1R_%=\n\t"
      "NP1B_%=:\n\t"
      "ds_write_b128 v243, v[200:203] offset:32768\n\t"
      "ds_write_b128 v220, v[204:207] offset:32768\n\t"
      "ds_write_b128 v221, v[208:211] offset:32768\n\t"
      "ds_write_b128 v222, v[212:215] offset:32768\n\t"
      "s_waitcnt lgkmcnt(0)\n\t"
      "s_barrier\n\t"
      "ds_read_b128 v[112:115], v244 offset:32768\n\t"
      "ds_read_b128 v[116:119], v244 offset:36864\n\t"
      "ds_read_b128 v[120:123], v244 offset:40960\n\t"
      "ds_read_b128 v[124:127], v244 offset:45056\n\t"
      "ds_read_b128 v[200:203], v244 offset:49152\n\t"
      "ds_read_b128 v[204:207], v244 offset:53248\n\t"
      "ds_read_b128 v[208:211], v244 offset:57344\n\t"
      "ds_read_b128 v[212:215], v244 offset:61440\n\t"
      "v_mfma_f32_16x16x32_bf16 v[184:187], v[128:131], v[0:3], v[164:167]\n\t"
      "s_waitcnt vmcnt(6)\n\t"
      "v_cmp_eq_u32_e32 vcc, -1, v132\n\t"
      "v_mfma_f32_16x16x32_bf16 v[188:191], v[128:131], v[4:7], 0\n\t"
      "s_waitcnt lgkmcnt(6)\n\t"
      "v_add_f32_e32 v112, v112, v116\n\t"
      "v_mfma_f32_16x16x32_bf16 v[192:195], v[128:131], v[8:11], 0\n\t"
      "s_cbranch_vccnz FP15R_%=\n\t"
      "FP15B_%=:\n\t"
      "v_add_f32_e32 v113, v113, v117\n\t"
      "v_add_f32_e32 v114, v114, v118\n\t"
      "v_mfma_f32_16x16x32_bf16 v[196:199], v[128:131], v[12:15], 0\n\t"
      "global_load_dwordx4 v[128:131], v224, s[58:59] sc1\n\t"
      "v_add_f32_e32 v115, v115, v119\n\t"
      "v_mfma_f32_16x16x32_bf16 v[184:187], v[132:135], v[16:19], v[184:187]\n\t"
      "s_waitcnt vmcnt(6)\n\t"
      "v_cmp_eq_u32_e32 vcc, -1, v136\n\t"
      "s_waitcnt lgkmcnt(4)\n\t"
      "v_add_f32_e32 v120, v120, v124\n\t"
      "v_mfma_f32_16x16x32_bf16 v[188:191], v[132:135], v[20:23], v[188:191]\n\t"
      "v_add_f32_e32 v121, v121, v125\n\t"
      "v_add_f32_e32 v122, v122, v126\n\t"
      "v_mfma_f32_16x16x32_bf16 v[192:195], v[132:135], v[24:27], v[192:195]\n\t"
      "s_cbranch_vccnz FP16R_%=\n\t"
      "FP16B_%=:\n\t"
      "v_add_f32_e32 v123, v123, v127\n\t"
      "s_waitcnt lgkmcnt(2)\n\t"
      "v_add_f32_e32 v200, v200, v204\n\t"
      "v_mfma_f32_16x16x32_bf16 v[196:199], v[132:135], v[28:31], v[196:199]\n\t"
      "global_load_dwordx4 v[132:135], v225, s[58:59] sc1\n\t"
      "v_add_f32_e32 v201, v201, v205\n\t"
      "v_mfma_f32_16x16x32_bf16 v[184:187], v[136:139], v[32:35], v[184:187]\n\t"
      "s_waitcnt vmcnt(6)\n\t"
      "v_cmp_eq_u32_e32 vcc, -1, v140\n\t"
      "v_add_f32_e32 v202, v202, v206\n\t"
      "v_mfma_f32_16x16x32_bf16 v[188:191], v[136:139], v[36:39], v[188:191]\n\t"
      "v_add_f32_e32 v203, v203, v207\n\t"
      "v_add_f32_e32 v112, v112, v120\n\t"
      "v_mfma_f32_16x16x32_bf16 v[192:195], v[136:139], v[40:43], v[192:195]\n\t"
      "s_cbranch_vccnz FP17R_%=\n\t"
      "FP17B_%=:\n\t"
      "v_add_f32_e32 v113, v113, v121\n\t"
      "v_add_f32_e32 v114, v114, v122\n\t"
      "v_mfma_f32_16x16x32_bf16 v[196:199], v[136:139], v[44:47], v[196:199]\n\t"
      "global_load_dwordx4 v[136:139], v226, s[58:59] sc1\n\t"
      "v_add_f32_e32 v115, v115, v123\n\t"
      "v_mfma_f32_16x16x32_bf16 v[184:187], v[140:143], v[48:51], v[184:187]\n\t"
      "s_waitcnt vmcnt(6)\n\t"
      "v_cmp_eq_u32_e32 vcc, -1, v144\n\t"
      "s_waitcnt lgkmcnt(0)\n\t"
      "v_add_f32_e32 v208, v208, v212\n\t"
      "v_mfma_f32_16x16x32_bf16 v[188:191], v[140:143], v[52:55], v[188:191]\n\t"
      "v_add_f32_e32 v209, v209, v213\n\t"
      "v_add_f32_e32 v210, v210, v214\n\t"
      "v_mfma_f32_16x16x32_bf16 v[192:195], v[140:143], v[56:59], v[192:195]\n\t"
      "s_cbranch_vccnz FP18R_%=\n\t"
      "FP18B_%=:\n\t"
      "v_add_f32_e32 v211, v211, v215\n\t"
      "v_add_f32_e32 v200, v200, v208\n\t"
      "v_mfma_f32_16x16x32_bf16 v[196:199], v[140:143], v[60:63], v[196:199]\n\t"
      "global_load_dwordx4 v[140:143], v227, s[58:59] sc1\n\t"
      "v_add_f32_e32 v201, v201, v209\n\t"
      "v_mfma_f32_16x16x32_bf16 v[184:187], v[144:147], v[64:67], v[184:187]\n\t"
      "s_waitcnt vmcnt(5)\n\t"
      "v_cmp_eq_u32_e32 vcc, -1, v148\n\t"
      "v_add_f32_e32 v202, v202, v210\n\t"
      "v_mfma_f32_16x16x32_bf16 v[188:191], v[144:147], v[68:71], v[188:191]\n\t"
      "v_add_f32_e32 v203, v203, v211\n\t"
      "v_add_f32_e32 v112, v112, v200\n\t"
      "v_mfma_f32_16x16x32_bf16 v[192:195], v[144:147], v[72:75], v[192:195]\n\t"
      "s_cbranch_vccnz FP19R_%=\n\t"
      "FP19B_%=:\n\t"
      "v_add_f32_e32 v113, v113, v201\n\t"
      "v_add_f32_e32 v114, v114, v202\n\t"
      "v_mfma_f32_16x16x32_bf16 v[196:199], v[144:147], v[76:79], v[196:199]\n\t"
      "global_load_dwordx4 v[144:147], v228, s[58:59] sc1\n\t"
      "v_add_f32_e32 v115, v115, v203\n\t"
      "v_mfma_f32_16x16x32_bf16 v[184:187], v[148:151], v[80:83], v[184:187]\n\t"
      "s_waitcnt vmcnt(5)\n\t"
      "v_cmp_eq_u32_e32 vcc, -1, v152\n\t"
      "v_cvt_pk_bf16_f32 v116, v112, v113\n\t"
      "v_mfma_f32_16x16x32_bf16 v[188:191], v[148:151], v[84:87], v[188:191]\n\t"
      "v_cvt_pk_bf16_f32 v117, v114, v115\n\t"
      "global_store_dwordx2 v245, v[116:117], s[8:9] offset:32\n\t"
      "v_mfma_f32_16x16x32_bf16 v[192:195], v[148:151], v[88:91], v[192:195]\n\t"
      "s_cbranch_vccnz FP20R_%=\n\t"
      "FP20B_%=:\n\t"
      "v_mfma_f32_16x16x32_bf16 v[196:199], v[148:151], v[92:95], v[196:199]\n\t"
      "global_load_dwordx4 v[148:151], v229, s[58:59] sc1\n\t"
      "v_mfma_f32_16x16x32_bf16 v[184:187], v[152:155], v[96:99], v[184:187]\n\t"
      "s_waitcnt vmcnt(6)\n\t"
      "v_cmp_eq_u32_e32 vcc, -1, v128\n\t"
      "v_mfma_f32_16x16x32_bf16 v[188:191], v[152:155], v[100:103], v[188:191]\n\t"
      "v_mfma_f32_16x16x32_bf16 v[192:195], v[152:155], v[104:107], v[192:195]\n\t"
      "s_cbranch_vccnz FP21R_%=\n\t"
      "FP21B_%=:\n\t"
      "v_mfma_f32_16x16x32_bf16 v[196:199], v[152:155], v[108:111], v[196:199]\n\t"
      "global_load_dwordx4 v[152:155], v230, s[58:59] sc1\n\t"
      "s_nop 0\n\t"
      "v_cmp_u_f32_e32 vcc, v184, v185\n\t"
      "v_cmp_u_f32_e64 s[48:49], v186, v187\n\t"
      "s_nop 0\n\t"
      "s_or_b64 vcc, vcc, s[48:49]\n\t"
      "s_cbranch_scc1 NP2R_%=\n\t"
      "NP2B_%=:\n\t"
      "ds_write_b128 v243, v[184:187]\n\t"
      "ds_write_b128 v220, v[188:191]\n\t"
      "ds_write_b128 v221, v[192:195]\n\t"
      "ds_write_b128 v222, v[196:199]\n\t"
      "s_waitcnt lgkmcnt(0)\n\t"
      "s_barrier\n\t"
      "ds_read_b128 v[112:115], v244\n\t"
      "ds_read_b128 v[116:119], v244 offset:4096\n\t"
      "ds_read_b128 v[120:123], v244 offset:8192\n\t"
      "ds_read_b128 v[124:127], v244 offset:12288\n\t"
      "ds_read_b128 v[184:187], v244 offset:16384\n\t"
      "ds_read_b128 v[188:191], v244 offset:20480\n\t"
      "ds_read_b128 v[192:195], v244 offset:24576\n\t"
      "ds_read_b128 v[196:199], v244 offset:28672\n\t"
      "v_mfma_f32_16x16x32_bf16 v[200:203], v[128:131], v[0:3], v[168:171]\n\t"
      "s_waitcnt vmcnt(6)\n\t"
      "v_cmp_eq_u32_e32 vcc, -1, v132\n\t"
      "v_mfma_f32_16x16x32_bf16 v[204:207], v[128:131], v[4:7], 0\n\t"
      "s_waitcnt lgkmcnt(6)\n\t"
      "v_add_f32_e32 v112, v112, v116\n\t"
      "v_mfma_f32_16x16x32_bf16 v[208:211], v[128:131], v[8:11], 0\n\t"
      "s_cbranch_vccnz FP22R_%=\n\t"
      "FP22B_%=:\n\t"
      "v_add_f32_e32 v113, v113, v117\n\t"
      "v_add_f32_e32 v114, v114, v118\n\t"
      "v_mfma_f32_16x16x32_bf16 v[212:215], v[128:131], v[12:15], 0\n\t"
      "global_load_dwordx4 v[128:131], v224, s[60:61] sc1\n\t"
      "v_add_f32_e32 v115, v115, v119\n\t"
      "v_mfma_f32_16x16x32_bf16 v[200:203], v[132:135], v[16:19], v[200:203]\n\t"
      "s_waitcnt vmcnt(6)\n\t"
      "v_cmp_eq_u32_e32 vcc, -1, v136\n\t"
      "s_waitcnt lgkmcnt(4)\n\t"
      "v_add_f32_e32 v120, v120, v124\n\t"
      "v_mfma_f32_16x16x32_bf16 v[204:207], v[132:135], v[20:23], v[204:207]\n\t"
      "v_add_f32_e32 v121, v121, v125\n\t"
      "v_add_f32_e32 v122, v122, v126\n\t"
      "v_mfma_f32_16x16x32_bf16 v[208:211], v[132:135], v[24:27], v[208:211]\n\t"
      "s_cbranch_vccnz FP23R_%=\n\t"
      "FP23B_%=:\n\t"
      "v_add_f32_e32 v123, v123, v127\n\t"
      "s_waitcnt lgkmcnt(2)\n\t"
      "v_add_f32_e32 v184, v184, v188\n\t"
      "v_mfma_f32_16x16x32_bf16 v[212:215], v[132:135], v[28:31], v[212:215]\n\t"
      "global_load_dwordx4 v[132:135], v225, s[60:61] sc1\n\t"
      "v_add_f32_e32 v185, v185, v189\n\t"
      "v_mfma_f32_16x16x32_bf16 v[200:203], v[136:139], v[32:35], v[200:203]\n\t"
      "s_waitcnt vmcnt(6)\n\t"
      "v_cmp_eq_u32_e32 vcc, -1, v140\n\t"
      "v_add_f32_e32 v186, v186, v190\n\t"
      "v_mfma_f32_16x16x32_bf16 v[204:207], v[136:139], v[36:39], v[204:207]\n\t"
      "v_add_f32_e32 v187, v187, v191\n\t"
      "v_add_f32_e32 v112, v112, v120\n\t"
      "v_mfma_f32_16x16x32_bf16 v[208:211], v[136:139], v[40:43], v[208:211]\n\t"
      "s_cbranch_vccnz FP24R_%=\n\t"
      "FP24B_%=:\n\t"
      "v_add_f32_e32 v113, v113, v121\n\t"
      "v_add_f32_e32 v114, v114, v122\n\t"
      "v_mfma_f32_16x16x32_bf16 v[212:215], v[136:139], v[44:47], v[212:215]\n\t"
      "global_load_dwordx4 v[136:139], v226, s[60:61] sc1\n\t"
      "v_add_f32_e32 v115, v115, v123\n\t"
      "v_mfma_f32_16x16x32_bf16 v[200:203], v[140:143], v[48:51], v[200:203]\n\t"
      "s_waitcnt vmcnt(6)\n\t"
      "v_cmp_eq_u32_e32 vcc, -1, v144\n\t"
      "s_waitcnt lgkmcnt(0)\n\t"
      "v_add_f32_e32 v192, v192, v196\n\t"
      "v_mfma_f32_16x16x32_bf16 v[204:207], v[140:143], v[52:55], v[204:207]\n\t"
      "v_add_f32_e32 v193, v193, v197\n\t"
      "v_add_f32_e32 v194, v194, v198\n\t"
      "v_mfma_f32_16x16x32_bf16 v[208:211], v[140:143], v[56:59], v[208:211]\n\t"
      "s_cbranch_vccnz FP25R_%=\n\t"
      "FP25B_%=:\n\t"
      "v_add_f32_e32 v195, v195, v199\n\t"
      "v_add_f32_e32 v184, v184, v192\n\t"
      "v_mfma_f32_16x16x32_bf16 v[212:215], v[140:143], v[60:63], v[212:215]\n\t"
      "global_load_dwordx4 v[140:143], v227, s[60:61] sc1\n\t"
      "v_add_f32_e32 v185, v185, v193\n\t"
      "v_mfma_f32_16x16x32_bf16 v[200:203], v[144:147], v[64:67], v[200:203]\n\t"
      "s_waitcnt vmcnt(5)\n\t"
      "v_cmp_eq_u32_e32 vcc, -1, v148\n\t"
      "v_add_f32_e32 v186, v186, v194\n\t"
      "v_mfma_f32_16x16x32_bf16 v[204:207], v[144:147], v[68:71], v[204:207]\n\t"
      "v_add_f32_e32 v187, v187, v195\n\t"
      "v_add_f32_e32 v112, v112, v184\n\t"
      "v_mfma_f32_16x16x32_bf16 v[208:211], v[144:147], v[72:75], v[208:211]\n\t"
      "s_cbranch_vccnz FP26R_%=\n\t"
      "FP26B_%=:\n\t"
      "v_add_f32_e32 v113, v113, v185\n\t"
      "v_add_f32_e32 v114, v114, v186\n\t"
      "v_mfma_f32_16x16x32_bf16 v[212:215], v[144:147], v[76:79], v[212:215]\n\t"
      "global_load_dwordx4 v[144:147], v228, s[60:61] sc1\n\t"
      "v_add_f32_e32 v115, v115, v187\n\t"
      "v_mfma_f32_16x16x32_bf16 v[200:203], v[148:151], v[80:83], v[200:203]\n\t"
      "s_waitcnt vmcnt(5)\n\t"
      "v_cmp_eq_u32_e32 vcc, -1, v152\n\t"
      "v_cvt_pk_bf16_f32 v116, v112, v113\n\t"
      "v_mfma_f32_16x16x32_bf16 v[204:207], v[148:151], v[84:87], v[204:207]\n\t"
      "v_cvt_pk_bf16_f32 v117, v114, v115\n\t"
      "global_store_dwordx2 v245, v[116:117], s[8:9] offset:64\n\t"
      "v_mfma_f32_16x16x32_bf16 v[208:211], v[148:151], v[88:91], v[208:211]\n\t"
      "s_cbranch_vccnz FP27R_%=\n\t"
      "FP27B_%=:\n\t"
      "v_mfma_f32_16x16x32_bf16 v[212:215], v[148:151], v[92:95], v[212:215]\n\t"
      "global_load_dwordx4 v[148:151], v229, s[60:61] sc1\n\t"
      "v_mfma_f32_16x16x32_bf16 v[200:203], v[152:155], v[96:99], v[200:203]\n\t"
      "s_waitcnt vmcnt(6)\n\t"
      "v_cmp_eq_u32_e32 vcc, -1, v128\n\t"
      "v_mfma_f32_16x16x32_bf16 v[204:207], v[152:155], v[100:103], v[204:207]\n\t"
      "v_mfma_f32_16x16x32_bf16 v[208:211], v[152:155], v[104:107], v[208:211]\n\t"
      "s_cbranch_vccnz FP28R_%=\n\t"
      "FP28B_%=:\n\t"
      "v_mfma_f32_16x16x32_bf16 v[212:215], v[152:155], v[108:111], v[212:215]\n\t"
      "global_load_dwordx4 v[152:155], v230, s[60:61] sc1\n\t"
      "s_nop 0\n\t"
      "v_cmp_u_f32_e32 vcc, v200, v201\n\t"
      "v_cmp_u_f32_e64 s[48:49], v202, v203\n\t"
      "s_nop 0\n\t"
      "s_or_b64 vcc, vcc, s[48:49]\n\t"
      "s_cbranch_scc1 NP3R_%=\n\t"
      "NP3B_%=:\n\t"
      "ds_write_b128 v243, v[200:203] offset:32768\n\t"
      "ds_write_b128 v220, v[204:207] offset:32768\n\t"
      "ds_write_b128 v221, v[208:211] offset:32768\n\t"
      "ds_write_b128 v222, v[212:215] offset:32768\n\t"
      "s_waitcnt lgkmcnt(0)\n\t"
      "s_barrier\n\t"
      "ds_read_b128 v[112:115], v244 offset:32768\n\t"
      "ds_read_b128 v[116:119], v244 offset:36864\n\t"
      "ds_read_b128 v[120:123], v244 offset:40960\n\t"
      "ds_read_b128 v[124:127], v244 offset:45056\n\t"
      "ds_read_b128 v[200:203], v244 offset:49152\n\t"
      "ds_read_b128 v[204:207], v244 offset:53248\n\t"
      "ds_read_b128 v[208:211], v244 offset:57344\n\t"
      "ds_read_b128 v[212:215], v244 offset:61440\n\t"
      "v_mfma_f32_16x16x32_bf16 v[184:187], v[128:131], v[0:3], v[172:175]\n\t"
      "s_waitcnt vmcnt(6)\n\t"
      "v_cmp_eq_u32_e32 vcc, -1, v132\n\t"
      "v_mfma_f32_16x16x32_bf16 v[188:191], v[128:131], v[4:7], 0\n\t"
      "s_waitcnt lgkmcnt(6)\n\t"
      "v_add_f32_e32 v112, v112, v116\n\t"
      "v_mfma_f32_16x16x32_bf16 v[192:195], v[128:131], v[8:11], 0\n\t"
      "s_cbranch_vccnz FP29R_%=\n\t"
      "FP29B_%=:\n\t"
      "v_add_f32_e32 v113, v113, v117\n\t"
      "v_add_f32_e32 v114, v114, v118\n\t"
      "v_mfma_f32_16x16x32_bf16 v[196:199], v[128:131], v[12:15], 0\n\t"
      "global_load_dwordx4 v[128:131], v224, s[62:63] sc1\n\t"
      "v_add_f32_e32 v115, v115, v119\n\t"
      "v_mfma_f32_16x16x32_bf16 v[184:187], v[132:135], v[16:19], v[184:187]\n\t"
      "s_waitcnt vmcnt(6)\n\t"
      "v_cmp_eq_u32_e32 vcc, -1, v136\n\t"
      "s_waitcnt lgkmcnt(4)\n\t"
      "v_add_f32_e32 v120, v120, v124\n\t"
      "v_mfma_f32_16x16x32_bf16 v[188:191], v[132:135], v[20:23], v[188:191]\n\t"
      "v_add_f32_e32 v121, v121, v125\n\t"
      "v_add_f32_e32 v122, v122, v126\n\t"
      "v_mfma_f32_16x16x32_bf16 v[192:195], v[132:135], v[24:27], v[192:195]\n\t"
      "s_cbranch_vccnz FP30R_%=\n\t"
      "FP30B_%=:\n\t"
      "v_add_f32_e32 v123, v123, v127\n\t"
      "s_waitcnt lgkmcnt(2)\n\t"
      "v_add_f32_e32 v200, v200, v204\n\t"
      "v_mfma_f32_16x16x32_bf16 v[196:199], v[132:135], v[28:31], v[196:199]\n\t"
      "global_load_dwordx4 v[132:135], v225, s[62:63] sc1\n\t"
      "v_add_f32_e32 v201, v201, v205\n\t"
      "v_mfma_f32_16x16x32_bf16 v[184:187], v[136:139], v[32:35], v[184:187]\n\t"
      "s_waitcnt vmcnt(6)\n\t"
      "v_cmp_eq_u32_e32 vcc, -1, v140\n\t"
      "v_add_f32_e32 v202, v202, v206\n\t"
      "v_mfma_f32_16x16x32_bf16 v[188:191], v[136:139], v[36:39], v[188:191]\n\t"
      "v_add_f32_e32 v203, v203, v207\n\t"
      "v_add_f32_e32 v112, v112, v120\n\t"
      "v_mfma_f32_16x16x32_bf16 v[192:195], v[136:139], v[40:43], v[192:195]\n\t"
      "s_cbranch_vccnz FP31R_%=\n\t"
      "FP31B_%=:\n\t"
      "v_add_f32_e32 v113, v113, v121\n\t"
      "v_add_f32_e32 v114, v114, v122\n\t"
      "v_mfma_f32_16x16x32_bf16 v[196:199], v[136:139], v[44:47], v[196:199]\n\t"
      "global_load_dwordx4 v[136:139], v226, s[62:63] sc1\n\t"
      "v_add_f32_e32 v115, v115, v123\n\t"
      "v_mfma_f32_16x16x32_bf16 v[184:187], v[140:143], v[48:51], v[184:187]\n\t"
      "s_waitcnt vmcnt(6)\n\t"
      "v_cmp_eq_u32_e32 vcc, -1, v144\n\t"
      "s_waitcnt lgkmcnt(0)\n\t"
      "v_add_f32_e32 v208, v208, v212\n\t"
      "v_mfma_f32_16x16x32_bf16 v[188:191], v[140:143], v[52:55], v[188:191]\n\t"
      "v_add_f32_e32 v209, v209, v213\n\t"
      "v_add_f32_e32 v210, v210, v214\n\t"
      "v_mfma_f32_16x16x32_bf16 v[192:195], v[140:143], v[56:59], v[192:195]\n\t"
      "s_cbranch_vccnz FP32R_%=\n\t"
      "FP32B_%=:\n\t"
      "v_add_f32_e32 v211, v211, v215\n\t"
      "v_add_f32_e32 v200, v200, v208\n\t"
      "v_mfma_f32_16x16x32_bf16 v[196:199], v[140:143], v[60:63], v[196:199]\n\t"
      "global_load_dwordx4 v[140:143], v227, s[62:63] sc1\n\t"
      "v_add_f32_e32 v201, v201, v209\n\t"
      "v_mfma_f32_16x16x32_bf16 v[184:187], v[144:147], v[64:67], v[184:187]\n\t"
      "s_waitcnt vmcnt(5)\n\t"
      "v_cmp_eq_u32_e32 vcc, -1, v148\n\t"
      "v_add_f32_e32 v202, v202, v210\n\t"
      "v_mfma_f32_16x16x32_bf16 v[188:191], v[144:147], v[68:71], v[188:191]\n\t"
      "v_add_f32_e32 v203, v203, v211\n\t"
      "v_add_f32_e32 v112, v112, v200\n\t"
      "v_mfma_f32_16x16x32_bf16 v[192:195], v[144:147], v[72:75], v[192:195]\n\t"
      "s_cbranch_vccnz FP33R_%=\n\t"
      "FP33B_%=:\n\t"
      "v_add_f32_e32 v113, v113, v201\n\t"
      "v_add_f32_e32 v114, v114, v202\n\t"
      "v_mfma_f32_16x16x32_bf16 v[196:199], v[144:147], v[76:79], v[196:199]\n\t"
      "global_load_dwordx4 v[144:147], v228, s[62:63] sc1\n\t"
      "v_add_f32_e32 v115, v115, v203\n\t"
      "v_mfma_f32_16x16x32_bf16 v[184:187], v[148:151], v[80:83], v[184:187]\n\t"
      "s_waitcnt vmcnt(5)\n\t"
      "v_cmp_eq_u32_e32 vcc, -1, v152\n\t"
      "v_cvt_pk_bf16_f32 v116, v112, v113\n\t"
      "v_mfma_f32_16x16x32_bf16 v[188:191], v[148:151], v[84:87], v[188:191]\n\t"
      "v_cvt_pk_bf16_f32 v117, v114, v115\n\t"
      "global_store_dwordx2 v245, v[116:117], s[8:9] offset:96\n\t"
      "v_mfma_f32_16x16x32_bf16 v[192:195], v[148:151], v[88:91], v[192:195]\n\t"
      "s_cbranch_vccnz FP34R_%=\n\t"
      "FP34B_%=:\n\t"
      "v_mfma_f32_16x16x32_bf16 v[196:199], v[148:151], v[92:95], v[196:199]\n\t"
      "global_load_dwordx4 v[148:151], v229, s[62:63] sc1\n\t"
      "v_mfma_f32_16x16x32_bf16 v[184:187], v[152:155], v[96:99], v[184:187]\n\t"
      "s_waitcnt vmcnt(6)\n\t"
      "v_cmp_eq_u32_e32 vcc, -1, v128\n\t"
      "v_mfma_f32_16x16x32_bf16 v[188:191], v[152:155], v[100:103], v[188:191]\n\t"
      "v_mfma_f32_16x16x32_bf16 v[192:195], v[152:155], v[104:107], v[192:195]\n\t"
      "s_cbranch_vccnz FP35R_%=\n\t"
      "FP35B_%=:\n\t"
      "v_mfma_f32_16x16x32_bf16 v[196:199], v[152:155], v[108:111], v[196:199]\n\t"
      "global_load_dwordx4 v[152:155], v230, s[62:63] sc1\n\t"
      "s_nop 0\n\t"
      "v_cmp_u_f32_e32 vcc, v184, v185\n\t"
      "v_cmp_u_f32_e64 s[48:49], v186, v187\n\t"
      "s_nop 0\n\t"
      "s_or_b64 vcc, vcc, s[48:49]\n\t"
      "s_cbranch_scc1 NP4R_%=\n\t"
      "NP4B_%=:\n\t"
      "ds_write_b128 v243, v[184:187]\n\t"
      "ds_write_b128 v220, v[188:191]\n\t"
      "ds_write_b128 v221, v[192:195]\n\t"
      "ds_write_b128 v222, v[196:199]\n\t"
      "s_waitcnt lgkmcnt(0)\n\t"
      "s_barrier\n\t"
      "ds_read_b128 v[112:115], v244\n\t"
      "ds_read_b128 v[116:119], v244 offset:4096\n\t"
      "ds_read_b128 v[120:123], v244 offset:8192\n\t"
      "ds_read_b128 v[124:127], v244 offset:12288\n\t"
      "ds_read_b128 v[184:187], v244 offset:16384\n\t"
      "ds_read_b128 v[188:191], v244 offset:20480\n\t"
      "ds_read_b128 v[192:195], v244 offset:24576\n\t"
      "ds_read_b128 v[196:199], v244 offset:28672\n\t"
      "v_mfma_f32_16x16x32_bf16 v[200:203], v[128:131], v[0:3], v[176:179]\n\t"
      "s_waitcnt vmcnt(6)\n\t"
      "v_cmp_eq_u32_e32 vcc, -1, v132\n\t"
      "v_mfma_f32_16x16x32_bf16 v[204:207], v[128:131], v[4:7], 0\n\t"
      "s_waitcnt lgkmcnt(6)\n\t"
      "v_add_f32_e32 v112, v112, v116\n\t"
      "v_mfma_f32_16x16x32_bf16 v[208:211], v[128:131], v[8:11], 0\n\t"
      "s_cbranch_vccnz FP36R_%=\n\t"
      "FP36B_%=:\n\t"
      "v_add_f32_e32 v113, v113, v117\n\t"
      "v_add_f32_e32 v114, v114, v118\n\t"
      "v_mfma_f32_16x16x32_bf16 v[212:215], v[128:131], v[12:15], 0\n\t"
      "global_load_dwordx4 v[128:131], v224, s[64:65] sc1\n\t"
      "v_add_f32_e32 v115, v115, v119\n\t"
      "v_mfma_f32_16x16x32_bf16 v[200:203], v[132:135], v[16:19], v[200:203]\n\t"
      "s_waitcnt vmcnt(6)\n\t"
      "v_cmp_eq_u32_e32 vcc, -1, v136\n\t"
      "s_waitcnt lgkmcnt(4)\n\t"
      "v_add_f32_e32 v120, v120, v124\n\t"
      "v_mfma_f32_16x16x32_bf16 v[204:207], v[132:135], v[20:23], v[204:207]\n\t"
      "v_add_f32_e32 v121, v121, v125\n\t"
      "v_add_f32_e32 v122, v122, v126\n\t"
      "v_mfma_f32_16x16x32_bf16 v[208:211], v[132:135], v[24:27], v[208:211]\n\t"
      "s_cbranch_vccnz FP37R_%=\n\t"
      "FP37B_%=:\n\t"
      "v_add_f32_e32 v123, v123, v127\n\t"
      "s_waitcnt lgkmcnt(2)\n\t"
      "v_add_f32_e32 v184, v184, v188\n\t"
      "v_mfma_f32_16x16x32_bf16 v[212:215], v[132:135], v[28:31], v[212:215]\n\t"
      "global_load_dwordx4 v[132:135], v225, s[64:65] sc1\n\t"
      "v_add_f32_e32 v185, v185, v189\n\t"
      "v_mfma_f32_16x16x32_bf16 v[200:203], v[136:139], v[32:35], v[200:203]\n\t"
      "s_waitcnt vmcnt(6)\n\t"
      "v_cmp_eq_u32_e32 vcc, -1, v140\n\t"
      "v_add_f32_e32 v186, v186, v190\n\t"
      "v_mfma_f32_16x16x32_bf16 v[204:207], v[136:139], v[36:39], v[204:207]\n\t"
      "v_add_f32_e32 v187, v187, v191\n\t"
      "v_add_f32_e32 v112, v112, v120\n\t"
      "v_mfma_f32_16x16x32_bf16 v[208:211], v[136:139], v[40:43], v[208:211]\n\t"
      "s_cbranch_vccnz FP38R_%=\n\t"
      "FP38B_%=:\n\t"
      "v_add_f32_e32 v113, v113, v121\n\t"
      "v_add_f32_e32 v114, v114, v122\n\t"
      "v_mfma_f32_16x16x32_bf16 v[212:215], v[136:139], v[44:47], v[212:215]\n\t"
      "global_load_dwordx4 v[136:139], v226, s[64:65] sc1\n\t"
      "v_add_f32_e32 v115, v115, v123\n\t"
      "v_mfma_f32_16x16x32_bf16 v[200:203], v[140:143], v[48:51], v[200:203]\n\t"
      "s_waitcnt vmcnt(6)\n\t"
      "v_cmp_eq_u32_e32 vcc, -1, v144\n\t"
      "s_waitcnt lgkmcnt(0)\n\t"
      "v_add_f32_e32 v192, v192, v196\n\t"
      "v_mfma_f32_16x16x32_bf16 v[204:207], v[140:143], v[52:55], v[204:207]\n\t"
      "v_add_f32_e32 v193, v193, v197\n\t"
      "v_add_f32_e32 v194, v194, v198\n\t"
      "v_mfma_f32_16x16x32_bf16 v[208:211], v[140:143], v[56:59], v[208:211]\n\t"
      "s_cbranch_vccnz FP39R_%=\n\t"
      "FP39B_%=:\n\t"
      "v_add_f32_e32 v195, v195, v199\n\t"
      "v_add_f32_e32 v184, v184, v192\n\t"
      "v_mfma_f32_16x16x32_bf16 v[212:215], v[140:143], v[60:63], v[212:215]\n\t"
      "global_load_dwordx4 v[140:143], v227, s[64:65] sc1\n\t"
      "v_add_f32_e32 v185, v185, v193\n\t"
      "v_mfma_f32_16x16x32_bf16 v[200:203], v[144:147], v[64:67], v[200:203]\n\t"
      "s_waitcnt vmcnt(5)\n\t"
      "v_cmp_eq_u32_e32 vcc, -1, v148\n\t"
      "v_add_f32_e32 v186, v186, v194\n\t"
      "v_mfma_f32_16x16x32_bf16 v[204:207], v[144:147], v[68:71], v[204:207]\n\t"
      "v_add_f32_e32 v187, v187, v195\n\t"
      "v_add_f32_e32 v112, v112, v184\n\t"
      "v_mfma_f32_16x16x32_bf16 v[208:211], v[144:147], v[72:75], v[208:211]\n\t"
      "s_cbranch_vccnz FP40R_%=\n\t"
      "FP40B_%=:\n\t"
      "v_add_f32_e32 v113, v113, v185\n\t"
      "v_add_f32_e32 v114, v114, v186\n\t"
      "v_mfma_f32_16x16x32_bf16 v[212:215], v[144:147], v[76:79], v[212:215]\n\t"
      "global_load_dwordx4 v[144:147], v228, s[64:65] sc1\n\t"
      "v_add_f32_e32 v115, v115, v187\n\t"
      "v_mfma_f32_16x16x32_bf16 v[200:203], v[148:151], v[80:83], v[200:203]\n\t"
      "s_waitcnt vmcnt(5)\n\t"
      "v_cmp_eq_u32_e32 vcc, -1, v152\n\t"
      "v_cvt_pk_bf16_f32 v116, v112, v113\n\t"
      "v_mfma_f32_16x16x32_bf16 v[204:207], v[148:151], v[84:87], v[204:207]\n\t"
      "v_cvt_pk_bf16_f32 v117, v114, v115\n\t"
      "global_store_dwordx2 v245, v[116:117], s[8:9] offset:128\n\t"
      "v_mfma_f32_16x16x32_bf16 v[208:211], v[148:151], v[88:91], v[208:211]\n\t"
      "s_cbranch_vccnz FP41R_%=\n\t"
      "FP41B_%=:\n\t"
      "v_mfma_f32_16x16x32_bf16 v[212:215], v[148:151], v[92:95], v[212:215]\n\t"
      "global_load_dwordx4 v[148:151], v229, s[64:65] sc1\n\t"
      "v_mfma_f32_16x16x32_bf16 v[200:203], v[152:155], v[96:99], v[200:203]\n\t"
      "s_waitcnt vmcnt(6)\n\t"
      "v_cmp_eq_u32_e32 vcc, -1, v128\n\t"
      "v_mfma_f32_16x16x32_bf16 v[204:207], v[152:155], v[100:103], v[204:207]\n\t"
      "v_mfma_f32_16x16x32_bf16 v[208:211], v[152:155], v[104:107], v[208:211]\n\t"
      "s_cbranch_vccnz FP42R_%=\n\t"
      "FP42B_%=:\n\t"
      "v_mfma_f32_16x16x32_bf16 v[212:215], v[152:155], v[108:111], v[212:215]\n\t"
      "global_load_dwordx4 v[152:155], v230, s[64:65] sc1\n\t"
      "s_nop 0\n\t"
      "v_cmp_u_f32_e32 vcc, v200, v201\n\t"
      "v_cmp_u_f32_e64 s[48:49], v202, v203\n\t"
      "s_nop 0\n\t"
      "s_or_b64 vcc, vcc, s[48:49]\n\t"
      "s_cbranch_scc1 NP5R_%=\n\t"
      "NP5B_%=:\n\t"
      "ds_write_b128 v243, v[200:203] offset:32768\n\t"
      "ds_write_b128 v220, v[204:207] offset:32768\n\t"
      "ds_write_b128 v221, v[208:211] offset:32768\n\t"
      "ds_write_b128 v222, v[212:215] offset:32768\n\t"
      "s_waitcnt lgkmcnt(0)\n\t"
      "s_barrier\n\t"
      "ds_read_b128 v[112:115], v244 offset:32768\n\t"
      "ds_read_b128 v[116:119], v244 offset:36864\n\t"
      "ds_read_b128 v[120:123], v244 offset:40960\n\t"
      "ds_read_b128 v[124:127], v244 offset:45056\n\t"
      "ds_read_b128 v[200:203], v244 offset:49152\n\t"
      "ds_read_b128 v[204:207], v244 offset:53248\n\t"
      "ds_read_b128 v[208:211], v244 offset:57344\n\t"
      "ds_read_b128 v[212:215], v244 offset:61440\n\t"
      "v_mfma_f32_16x16x32_bf16 v[184:187], v[128:131], v[0:3], v[180:183]\n\t"
      "s_waitcnt vmcnt(6)\n\t"
      "v_cmp_eq_u32_e32 vcc, -1, v132\n\t"
      "v_mfma_f32_16x16x32_bf16 v[188:191], v[128:131], v[4:7], 0\n\t"
      "v_mfma_f32_16x16x32_bf16 v[192:195], v[128:131], v[8:11], 0\n\t"
      "s_cbranch_vccnz FP43R_%=\n\t"
      "FP43B_%=:\n\t"
      "v_mfma_f32_16x16x32_bf16 v[196:199], v[128:131], v[12:15], 0\n\t"
      "v_mfma_f32_16x16x32_bf16 v[184:187], v[132:135], v[16:19], v[184:187]\n\t"
      "s_waitcnt vmcnt(5)\n\t"
      "v_cmp_eq_u32_e32 vcc, -1, v136\n\t"
      "s_waitcnt lgkmcnt(6)\n\t"
      "v_add_f32_e32 v112, v112, v116\n\t"
      "v_mfma_f32_16x16x32_bf16 v[188:191], v[132:135], v[20:23], v[188:191]\n\t"
      "v_add_f32_e32 v113, v113, v117\n\t"
      "v_add_f32_e32 v114, v114, v118\n\t"
      "v_mfma_f32_16x16x32_bf16 v[192:195], v[132:135], v[24:27], v[192:195]\n\t"
      "s_cbranch_vccnz FP44R_%=\n\t"
      "FP44B_%=:\n\t"
      "v_add_f32_e32 v115, v115, v119\n\t"
      "s_waitcnt lgkmcnt(4)\n\t"
      "v_add_f32_e32 v120, v120, v124\n\t"
      "v_mfma_f32_16x16x32_bf16 v[196:199], v[132:135], v[28:31], v[196:199]\n\t"
      "v_add_f32_e32 v121, v121, v125\n\t"
      "v_add_f32_e32 v122, v122, v126\n\t"
      "v_mfma_f32_16x16x32_bf16 v[184:187], v[136:139], v[32:35], v[184:187]\n\t"
      "s_waitcnt vmcnt(4)\n\t"
      "v_cmp_eq_u32_e32 vcc, -1, v140\n\t"
      "v_add_f32_e32 v123, v123, v127\n\t"
      "v_mfma_f32_16x16x32_bf16 v[188:191], v[136:139], v[36:39], v[188:191]\n\t"
      "s_waitcnt lgkmcnt(2)\n\t"
      "v_add_f32_e32 v200, v200, v204\n\t"
      "v_add_f32_e32 v201, v201, v205\n\t"
      "v_mfma_f32_16x16x32_bf16 v[192:195], v[136:139], v[40:43], v[192:195]\n\t"
      "s_cbranch_vccnz FP45R_%=\n\t"
      "FP45B_%=:\n\t"
      "v_add_f32_e32 v202, v202, v206\n\t"
      "v_add_f32_e32 v203, v203, v207\n\t"
      "v_mfma_f32_16x16x32_bf16 v[196:199], v[136:139], v[44:47], v[196:199]\n\t"
      "v_add_f32_e32 v112, v112, v120\n\t"
      "v_add_f32_e32 v113, v113, v121\n\t"
      "v_mfma_f32_16x16x32_bf16 v[184:187], v[140:143], v[48:51], v[184:187]\n\t"
      "s_waitcnt vmcnt(3)\n\t"
      "v_cmp_eq_u32_e32 vcc, -1, v144\n\t"
      "v_add_f32_e32 v114, v114, v122\n\t"
      "v_mfma_f32_16x16x32_bf16 v[188:191], v[140:143], v[52:55], v[188:191]\n\t"
      "v_add_f32_e32 v115, v115, v123\n\t"
      "s_waitcnt lgkmcnt(0)\n\t"
      "v_add_f32_e32 v208, v208, v212\n\t"
      "v_mfma_f32_16x16x32_bf16 v[192:195], v[140:143], v[56:59], v[192:195]\n\t"
      "s_cbranch_vccnz FP46R_%=\n\t"
      "FP46B_%=:\n\t"
      "v_add_f32_e32 v209, v209, v213\n\t"
      "v_add_f32_e32 v210, v210, v214\n\t"
      "v_mfma_f32_16x16x32_bf16 v[196:199], v[140:143], v[60:63], v[196:199]\n\t"
      "v_add_f32_e32 v211, v211, v215\n\t"
      "v_add_f32_e32 v200, v200, v208\n\t"
      "v_mfma_f32_16x16x32_bf16 v[184:187], v[144:147], v[64:67], v[184:187]\n\t"
      "s_waitcnt vmcnt(1)\n\t"
      "v_cmp_eq_u32_e32 vcc, -1, v148\n\t"
      "v_add_f32_e32 v201, v201, v209\n\t"
      "v_mfma_f32_16x16x32_bf16 v[188:191], v[144:147], v[68:71], v[188:191]\n\t"
      "v_add_f32_e32 v202, v202, v210\n\t"
      "v_add_f32_e32 v203, v203, v211\n\t"
      "v_mfma_f32_16x16x32_bf16 v[192:195], v[144:147], v[72:75], v[192:195]\n\t"
      "s_cbranch_vccnz FP47R_%=\n\t"
      "FP47B_%=:\n\t"
      "v_add_f32_e32 v112, v112, v200\n\t"
      "v_add_f32_e32 v113, v113, v201\n\t"
      "v_mfma_f32_16x16x32_bf16 v[196:199], v[144:147], v[76:79], v[196:199]\n\t"
      "v_add_f32_e32 v114, v114, v202\n\t"
      "v_add_f32_e32 v115, v115, v203\n\t"
      "v_mfma_f32_16x16x32_bf16 v[184:187], v[148:151], v[80:83], v[184:187]\n\t"
      "s_waitcnt vmcnt(0)\n\t"
      "v_cmp_eq_u32_e32 vcc, -1, v152\n\t"
      "v_cvt_pk_bf16_f32 v116, v112, v113\n\t"
      "v_mfma_f32_16x16x32_bf16 v[188:191], v[148:151], v[84:87], v[188:191]\n\t"
      "v_cvt_pk_bf16_f32 v117, v114, v115\n\t"
      "global_store_dwordx2 v245, v[116:117], s[8:9] offset:160\n\t"
      "v_mfma_f32_16x16x32_bf16 v[192:195], v[148:151], v[88:91], v[192:195]\n\t"
      "s_cbranch_vccnz FP48R_%=\n\t"
      "FP48B_%=:\n\t"
      "v_mfma_f32_16x16x32_bf16 v[196:199], v[148:151], v[92:95], v[196:199]\n\t"
      "v_mfma_f32_16x16x32_bf16 v[184:187], v[152:155], v[96:99], v[184:187]\n\t"
      "v_mfma_f32_16x16x32_bf16 v[188:191], v[152:155], v[100:103], v[188:191]\n\t"
      "v_mfma_f32_16x16x32_bf16 v[192:195], v[152:155], v[104:107], v[192:195]\n\t"
      "v_mfma_f32_16x16x32_bf16 v[196:199], v[152:155], v[108:111], v[196:199]\n\t"
      "s_nop 4\n\t"
      "v_cmp_u_f32_e32 vcc, v184, v185\n\t"
      "v_cmp_u_f32_e64 s[48:49], v186, v187\n\t"
      "s_nop 0\n\t"
      "s_or_b64 vcc, vcc, s[48:49]\n\t"
      "s_cbranch_scc1 NP6R_%=\n\t"
      "NP6B_%=:\n\t"
      "ds_write_b128 v243, v[184:187]\n\t"
      "ds_write_b128 v220, v[188:191]\n\t"
      "ds_write_b128 v221, v[192:195]\n\t"
      "ds_write_b128 v222, v[196:199]\n\t"
      "s_waitcnt lgkmcnt(0)\n\t"
      "s_barrier\n\t"
      "ds_read_b128 v[112:115], v244\n\t"
      "ds_read_b128 v[116:119], v244 offset:4096\n\t"
      "ds_read_b128 v[120:123], v244 offset:8192\n\t"
      "ds_read_b128 v[124:127], v244 offset:12288\n\t"
      "ds_read_b128 v[184:187], v244 offset:16384\n\t"
      "ds_read_b128 v[188:191], v244 offset:20480\n\t"
      "ds_read_b128 v[192:195], v244 offset:24576\n\t"
      "ds_read_b128 v[196:199], v244 offset:28672\n\t"
      "s_waitcnt lgkmcnt(6)\n\t"
      "v_add_f32_e32 v112, v112, v116\n\t"
      "v_add_f32_e32 v113, v113, v117\n\t"
      "v_add_f32_e32 v114, v114, v118\n\t"
      "v_add_f32_e32 v115, v115, v119\n\t"
      "s_waitcnt lgkmcnt(4)\n\t"
      "v_add_f32_e32 v120, v120, v124\n\t"
      "v_add_f32_e32 v121, v121, v125\n\t"
      "v_add_f32_e32 v122, v122, v126\n\t"
      "v_add_f32_e32 v123, v123, v127\n\t"
      "s_waitcnt lgkmcnt(2)\n\t"
      "v_add_f32_e32 v184, v184, v188\n\t"
      "v_add_f32_e32 v185, v185, v189\n\t"
      "v_add_f32_e32 v186, v186, v190\n\t"
      "v_add_f32_e32 v187, v187, v191\n\t"
      "v_add_f32_e32 v112, v112, v120\n\t"
      "v_add_f32_e32 v113, v113, v121\n\t"
      "v_add_f32_e32 v114, v114, v122\n\t"
      "v_add_f32_e32 v115, v115, v123\n\t"
      "s_waitcnt lgkmcnt(0)\n\t"
      "v_add_f32_e32 v192, v192, v196\n\t"
      "v_add_f32_e32 v193, v193, v197\n\t"
      "v_add_f32_e32 v194, v194, v198\n\t"
      "v_add_f32_e32 v195, v195, v199\n\t"
      "v_add_f32_e32 v184, v184, v192\n\t"
      "v_add_f32_e32 v185, v185, v193\n\t"
      "v_add_f32_e32 v186, v186, v194\n\t"
      "v_add_f32_e32 v187, v187, v195\n\t"
      "v_add_f32_e32 v112, v112, v184\n\t"
      "v_add_f32_e32 v113, v113, v185\n\t"
      "v_add_f32_e32 v114, v114, v186\n\t"
      "v_add_f32_e32 v115, v115, v187\n\t"
      "v_cvt_pk_bf16_f32 v116, v112, v113\n\t"
      "v_cvt_pk_bf16_f32 v117, v114, v115\n\t"
      "global_store_dwordx2 v245, v[116:117], s[8:9] offset:192\n\t"
      "GX_%=:\n\t"
      "s_bitcmp1_b32 s16, 5\n\t"
      "s_cbranch_scc0 EP_%=\n\t"
      "v_mov_b32_e32 v0, s26\n\t"
      "v_mov_b32_e32 v1, s27\n\t"
      "v_mov_b32_e32 v2, 0\n\t"
      "s_mov_b64 exec, 1\n\t"
      "global_store_dwordx2 v2, v[0:1], s[24:25] sc1\n\t"
      "s_mov_b64 exec, -1\n\t"
      "EP_%=:\n\t"
      "s_endpgm\n\t"
      ".p2align 6\n\t"
      "GQ_%=:\n\t"
      "s_waitcnt vmcnt(6)\n\t"
      "v_max3_u32 v234, v128, v129, v130\n\t"
      "v_max_u32_e32 v234, v234, v131\n\t"
      "v_cmp_eq_u32_e32 vcc, -1, v234\n\t"
      "s_cbranch_vccnz FQ0R_%=\n\t"
      "FQ0B_%=:\n\t"
      "v_mfma_f32_16x16x32_bf16 v[184:187], v[128:131], v[0:3], 0\n\t"
      "v_mfma_f32_16x16x32_bf16 v[188:191], v[128:131], v[4:7], 0\n\t"
      "v_mfma_f32_16x16x32_bf16 v[192:195], v[128:131], v[8:11], 0\n\t"
      "v_mfma_f32_16x16x32_bf16 v[196:199], v[128:131], v[12:15], 0\n\t"
      "global_load_dwordx4 v[128:131], v224, s[54:55] sc1\n\t"
      "s_waitcnt vmcnt(6)\n\t"
      "v_max3_u32 v234, v132, v133, v134\n\t"
      "v_max_u32_e32 v234, v234, v135\n\t"
      "v_cmp_eq_u32_e32 vcc, -1, v234\n\t"
      "s_cbranch_vccnz FQ1R_%=\n\t"
      "FQ1B_%=:\n\t"
      "v_mfma_f32_16x16x32_bf16 v[184:187], v[132:135], v[16:19], v[184:187]\n\t"
      "v_mfma_f32_16x16x32_bf16 v[188:191], v[132:135], v[20:23], v[188:191]\n\t"
      "v_mfma_f32_16x16x32_bf16 v[192:195], v[132:135], v[24:27], v[192:195]\n\t"
      "v_mfma_f32_16x16x32_bf16 v[196:199], v[132:135], v[28:31], v[196:199]\n\t"
      "global_load_dwordx4 v[132:135], v225, s[54:55] sc1\n\t"
      "s_waitcnt vmcnt(6)\n\t"
      "v_max3_u32 v234, v136, v137, v138\n\t"
      "v_max_u32_e32 v234, v234, v139\n\t"
      "v_cmp_eq_u32_e32 vcc, -1, v234\n\t"
      "s_cbranch_vccnz FQ2R_%=\n\t"
      "FQ2B_%=:\n\t"
      "v_mfma_f32_16x16x32_bf16 v[184:187], v[136:139], v[32:35], v[184:187]\n\t"
      "v_mfma_f32_16x16x32_bf16 v[188:191], v[136:139], v[36:39], v[188:191]\n\t"
      "v_mfma_f32_16x16x32_bf16 v[192:195], v[136:139], v[40:43], v[192:195]\n\t"
      "v_mfma_f32_16x16x32_bf16 v[196:199], v[136:139], v[44:47], v[196:199]\n\t"
      "global_load_dwordx4 v[136:139], v226, s[54:55] sc1\n\t"
      "s_waitcnt vmcnt(6)\n\t"
      "v_max3_u32 v234, v140, v141, v142\n\t"
      "v_max_u32_e32 v234, v234, v143\n\t"
      "v_cmp_eq_u32_e32 vcc, -1, v234\n\t"
      "s_cbranch_vccnz FQ3R_%=\n\t"
      "FQ3B_%=:\n\t"
      "v_mfma_f32_16x16x32_bf16 v[184:187], v[140:143], v[48:51], v[184:187]\n\t"
      "v_mfma_f32_16x16x32_bf16 v[188:191], v[140:143], v[52:55], v[188:191]\n\t"
      "v_mfma_f32_16x16x32_bf16 v[192:195], v[140:143], v[56:59], v[192:195]\n\t"
      "v_mfma_f32_16x16x32_bf16 v[196:199], v[140:143], v[60:63], v[196:199]\n\t"
      "global_load_dwordx4 v[140:143], v227, s[54:55] sc1\n\t"
      "s_waitcnt vmcnt(6)\n\t"
      "v_max3_u32 v234, v144, v145, v146\n\t"
      "v_max_u32_e32 v234, v234, v147\n\t"
      "v_cmp_eq_u32_e32 vcc, -1, v234\n\t"
      "s_cbranch_vccnz FQ4R_%=\n\t"
      "FQ4B_%=:\n\t"
      "v_mfma_f32_16x16x32_bf16 v[184:187], v[144:147], v[64:67], v[184:187]\n\t"
      "v_mfma_f32_16x16x32_bf16 v[188:191], v[144:147], v[68:71], v[188:191]\n\t"
      "v_mfma_f32_16x16x32_bf16 v[192:195], v[144:147], v[72:75], v[192:195]\n\t"
      "v_mfma_f32_16x16x32_bf16 v[196:199], v[144:147], v[76:79], v[196:199]\n\t"
      "global_load_dwordx4 v[144:147], v228, s[54:55] sc1\n\t"
      "s_waitcnt vmcnt(6)\n\t"
      "v_max3_u32 v234, v148, v149, v150\n\t"
      "v_max_u32_e32 v234, v234, v151\n\t"
      "v_cmp_eq_u32_e32 vcc, -1, v234\n\t"
      "s_cbranch_vccnz FQ5R_%=\n\t"
      "FQ5B_%=:\n\t"
      "v_mfma_f32_16x16x32_bf16 v[184:187], v[148:151], v[80:83], v[184:187]\n\t"
      "v_mfma_f32_16x16x32_bf16 v[188:191], v[148:151], v[84:87], v[188:191]\n\t"
      "v_mfma_f32_16x16x32_bf16 v[192:195], v[148:151], v[88:91], v[192:195]\n\t"
      "v_mfma_f32_16x16x32_bf16 v[196:199], v[148:151], v[92:95], v[196:199]\n\t"
      "global_load_dwordx4 v[148:151], v229, s[54:55] sc1\n\t"
      "s_waitcnt vmcnt(6)\n\t"
      "v_max3_u32 v234, v152, v153, v154\n\t"
      "v_max_u32_e32 v234, v234, v155\n\t"
      "v_cmp_eq_u32_e32 vcc, -1, v234\n\t"
      "s_cbranch_vccnz FQ6R_%=\n\t"
      "FQ6B_%=:\n\t"
      "v_mfma_f32_16x16x32_bf16 v[184:187], v[152:155], v[96:99], v[184:187]\n\t"
      "s_waitcnt vmcnt(5)\n\t"
      "v_cmp_eq_u32_e32 vcc, -1, v128\n\t"
      "v_mfma_f32_16x16x32_bf16 v[188:191], v[152:155], v[100:103], v[188:191]\n\t"
      "v_mfma_f32_16x16x32_bf16 v[192:195], v[152:155], v[104:107], v[192:195]\n\t"
      "s_cbranch_vccnz FQ7R_%=\n\t"
      "FQ7B_%=:\n\t"
      "v_mfma_f32_16x16x32_bf16 v[196:199], v[152:155], v[108:111], v[196:199]\n\t"
      "global_load_dwordx4 v[152:155], v230, s[54:55] sc1\n\t"
      "s_nop 0\n\t"
      "ds_write_b128 v243, v[184:187]\n\t"
      "s_nop 1\n\t"
      "ds_write_b128 v220, v[188:191]\n\t"
      "ds_write_b128 v221, v[192:195]\n\t"
      "s_nop 0\n\t"
      "ds_write_b128 v222, v[196:199]\n\t"
      "v_mfma_f32_16x16x32_bf16 v[200:203], v[128:131], v[0:3], 0\n\t"
      "s_waitcnt vmcnt(5)\n\t"
      "v_cmp_eq_u32_e32 vcc, -1, v132\n\t"
      "v_mfma_f32_16x16x32_bf16 v[204:207], v[128:131], v[4:7], 0\n\t"
      "v_mfma_f32_16x16x32_bf16 v[208:211], v[128:131], v[8:11], 0\n\t"
      "s_cbranch_vccnz FQ8R_%=\n\t"
      "FQ8B_%=:\n\t"
      "v_mfma_f32_16x16x32_bf16 v[212:215], v[128:131], v[12:15], 0\n\t"
      "global_load_dwordx4 v[128:131], v224, s[56:57] sc1\n\t"
      "v_mfma_f32_16x16x32_bf16 v[200:203], v[132:135], v[16:19], v[200:203]\n\t"
      "s_waitcnt vmcnt(5)\n\t"
      "v_cmp_eq_u32_e32 vcc, -1, v136\n\t"
      "v_mfma_f32_16x16x32_bf16 v[204:207], v[132:135], v[20:23], v[204:207]\n\t"
      "v_mfma_f32_16x16x32_bf16 v[208:211], v[132:135], v[24:27], v[208:211]\n\t"
      "s_cbranch_vccnz FQ9R_%=\n\t"
      "FQ9B_%=:\n\t"
      "v_mfma_f32_16x16x32_bf16 v[212:215], v[132:135], v[28:31], v[212:215]\n\t"
      "global_load_dwordx4 v[132:135], v225, s[56:57] sc1\n\t"
      "v_mfma_f32_16x16x32_bf16 v[200:203], v[136:139], v[32:35], v[200:203]\n\t"
      "s_waitcnt vmcnt(5)\n\t"
      "v_cmp_eq_u32_e32 vcc, -1, v140\n\t"
      "v_mfma_f32_16x16x32_bf16 v[204:207], v[136:139], v[36:39], v[204:207]\n\t"
      "v_mfma_f32_16x16x32_bf16 v[208:211], v[136:139], v[40:43], v[208:211]\n\t"
      "s_cbranch_vccnz FQ10R_%=\n\t"
      "FQ10B_%=:\n\t"
      "v_mfma_f32_16x16x32_bf16 v[212:215], v[136:139], v[44:47], v[212:215]\n\t"
      "global_load_dwordx4 v[136:139], v226, s[56:57] sc1\n\t"
      "v_mfma_f32_16x16x32_bf16 v[200:203], v[140:143], v[48:51], v[200:203]\n\t"
      "s_waitcnt vmcnt(5)\n\t"
      "v_cmp_eq_u32_e32 vcc, -1, v144\n\t"
      "v_mfma_f32_16x16x32_bf16 v[204:207], v[140:143], v[52:55], v[204:207]\n\t"
      "v_mfma_f32_16x16x32_bf16 v[208:211], v[140:143], v[56:59], v[208:211]\n\t"
      "s_cbranch_vccnz FQ11R_%=\n\t"
      "FQ11B_%=:\n\t"
      "v_mfma_f32_16x16x32_bf16 v[212:215], v[140:143], v[60:63], v[212:215]\n\t"
      "global_load_dwordx4 v[140:143], v227, s[56:57] sc1\n\t"
      "v_mfma_f32_16x16x32_bf16 v[200:203], v[144:147], v[64:67], v[200:203]\n\t"
      "s_waitcnt vmcnt(5)\n\t"
      "v_cmp_eq_u32_e32 vcc, -1, v148\n\t"
      "v_mfma_f32_16x16x32_bf16 v[204:207], v[144:147], v[68:71], v[204:207]\n\t"
      "v_mfma_f32_16x16x32_bf16 v[208:211], v[144:147], v[72:75], v[208:211]\n\t"
      "s_cbranch_vccnz FQ12R_%=\n\t"
      "FQ12B_%=:\n\t"
      "v_mfma_f32_16x16x32_bf16 v[212:215], v[144:147], v[76:79], v[212:215]\n\t"
      "global_load_dwordx4 v[144:147], v228, s[56:57] sc1\n\t"
      "s_waitcnt lgkmcnt(0)\n\t"
      "s_barrier\n\t"
      "s_setprio 2\n\t"
      "v_mfma_f32_16x16x32_bf16 v[200:203], v[148:151], v[80:83], v[200:203]\n\t"
      "s_waitcnt vmcnt(5)\n\t"
      "v_cmp_eq_u32_e32 vcc, -1, v152\n\t"
      "v_mfma_f32_16x16x32_bf16 v[204:207], v[148:151], v[84:87], v[204:207]\n\t"
      "v_mfma_f32_16x16x32_bf16 v[208:211], v[148:151], v[88:91], v[208:211]\n\t"
      "s_cbranch_vccnz FQ13R_%=\n\t"
      "FQ13B_%=:\n\t"
      "v_mfma_f32_16x16x32_bf16 v[212:215], v[148:151], v[92:95], v[212:215]\n\t"
      "global_load_dwordx4 v[148:151], v229, s[56:57] sc1\n\t"
      "v_mfma_f32_16x16x32_bf16 v[200:203], v[152:155], v[96:99], v[200:203]\n\t"
      "s_waitcnt vmcnt(5)\n\t"
      "v_cmp_eq_u32_e32 vcc, -1, v128\n\t"
      "v_mfma_f32_16x16x32_bf16 v[204:207], v[152:155], v[100:103], v[204:207]\n\t"
      "v_mfma_f32_16x16x32_bf16 v[208:211], v[152:155], v[104:107], v[208:211]\n\t"
      "s_cbranch_vccnz FQ14R_%=\n\t"
      "FQ14B_%=:\n\t"
      "v_mfma_f32_16x16x32_bf16 v[212:215], v[152:155], v[108:111], v[212:215]\n\t"
      "global_load_dwordx4 v[152:155], v230, s[56:57] sc1\n\t"
      "s_setprio 0\n\t"
      "v_cmp_u_f32_e32 vcc, v200, v201\n\t"
      "v_cmp_u_f32_e64 s[48:49], v202, v203\n\t"
      "s_nop 0\n\t"
      "s_or_b64 vcc, vcc, s[48:49]\n\t"
      "s_cbranch_scc1 NQ1R_%=\n\t"
      "NQ1B_%=:\n\t"
      "ds_write_b128 v243, v[200:203] offset:32768\n\t"
      "ds_write_b128 v220, v[204:207] offset:32768\n\t"
      "ds_write_b128 v221, v[208:211] offset:32768\n\t"
      "ds_write_b128 v222, v[212:215] offset:32768\n\t"
      "v_mfma_f32_16x16x32_bf16 v[184:187], v[128:131], v[0:3], 0\n\t"
      "s_waitcnt vmcnt(5)\n\t"
      "v_cmp_eq_u32_e32 vcc, -1, v132\n\t"
      "v_mfma_f32_16x16x32_bf16 v[188:191], v[128:131], v[4:7], 0\n\t"
      "v_mfma_f32_16x16x32_bf16 v[192:195], v[128:131], v[8:11], 0\n\t"
      "s_cbranch_vccnz FQ15R_%=\n\t"
      "FQ15B_%=:\n\t"
      "v_mfma_f32_16x16x32_bf16 v[196:199], v[128:131], v[12:15], 0\n\t"
      "global_load_dwordx4 v[128:131], v224, s[58:59] sc1\n\t"
      "v_mfma_f32_16x16x32_bf16 v[184:187], v[132:135], v[16:19], v[184:187]\n\t"
      "s_waitcnt vmcnt(5)\n\t"
      "v_cmp_eq_u32_e32 vcc, -1, v136\n\t"
      "v_mfma_f32_16x16x32_bf16 v[188:191], v[132:135], v[20:23], v[188:191]\n\t"
      "v_mfma_f32_16x16x32_bf16 v[192:195], v[132:135], v[24:27], v[192:195]\n\t"
      "s_cbranch_vccnz FQ16R_%=\n\t"
      "FQ16B_%=:\n\t"
      "v_mfma_f32_16x16x32_bf16 v[196:199], v[132:135], v[28:31], v[196:199]\n\t"
      "global_load_dwordx4 v[132:135], v225, s[58:59] sc1\n\t"
      "v_mfma_f32_16x16x32_bf16 v[184:187], v[136:139], v[32:35], v[184:187]\n\t"
      "s_waitcnt vmcnt(5)\n\t"
      "v_cmp_eq_u32_e32 vcc, -1, v140\n\t"
      "v_mfma_f32_16x16x32_bf16 v[188:191], v[136:139], v[36:39], v[188:191]\n\t"
      "v_mfma_f32_16x16x32_bf16 v[192:195], v[136:139], v[40:43], v[192:195]\n\t"
      "s_cbranch_vccnz FQ17R_%=\n\t"
      "FQ17B_%=:\n\t"
      "v_mfma_f32_16x16x32_bf16 v[196:199], v[136:139], v[44:47], v[196:199]\n\t"
      "global_load_dwordx4 v[136:139], v226, s[58:59] sc1\n\t"
      "v_mfma_f32_16x16x32_bf16 v[184:187], v[140:143], v[48:51], v[184:187]\n\t"
      "s_waitcnt vmcnt(5)\n\t"
      "v_cmp_eq_u32_e32 vcc, -1, v144\n\t"
      "v_mfma_f32_16x16x32_bf16 v[188:191], v[140:143], v[52:55], v[188:191]\n\t"
      "v_mfma_f32_16x16x32_bf16 v[192:195], v[140:143], v[56:59], v[192:195]\n\t"
      "s_cbranch_vccnz FQ18R_%=\n\t"
      "FQ18B_%=:\n\t"
      "v_mfma_f32_16x16x32_bf16 v[196:199], v[140:143], v[60:63], v[196:199]\n\t"
      "global_load_dwordx4 v[140:143], v227, s[58:59] sc1\n\t"
      "v_mfma_f32_16x16x32_bf16 v[184:187], v[144:147], v[64:67], v[184:187]\n\t"
      "s_waitcnt vmcnt(5)\n\t"
      "v_cmp_eq_u32_e32 vcc, -1, v148\n\t"
      "v_mfma_f32_16x16x32_bf16 v[188:191], v[144:147], v[68:71], v[188:191]\n\t"
      "v_mfma_f32_16x16x32_bf16 v[192:195], v[144:147], v[72:75], v[192:195]\n\t"
      "s_cbranch_vccnz FQ19R_%=\n\t"
      "FQ19B_%=:\n\t"
      "v_mfma_f32_16x16x32_bf16 v[196:199], v[144:147], v[76:79], v[196:199]\n\t"
      "global_load_dwordx4 v[144:147], v228, s[58:59] sc1\n\t"
      "s_waitcnt lgkmcnt(0)\n\t"
      "s_barrier\n\t"
      "s_setprio 2\n\t"
      "v_mfma_f32_16x16x32_bf16 v[184:187], v[148:151], v[80:83], v[184:187]\n\t"
      "s_waitcnt vmcnt(5)\n\t"
      "v_cmp_eq_u32_e32 vcc, -1, v152\n\t"
      "v_mfma_f32_16x16x32_bf16 v[188:191], v[148:151], v[84:87], v[188:191]\n\t"
      "v_mfma_f32_16x16x32_bf16 v[192:195], v[148:151], v[88:91], v[192:195]\n\t"
      "s_cbranch_vccnz FQ20R_%=\n\t"
      "FQ20B_%=:\n\t"
      "v_mfma_f32_16x16x32_bf16 v[196:199], v[148:151], v[92:95], v[196:199]\n\t"
      "global_load_dwordx4 v[148:151], v229, s[58:59] sc1\n\t"
      "v_mfma_f32_16x16x32_bf16 v[184:187], v[152:155], v[96:99], v[184:187]\n\t"
      "s_waitcnt vmcnt(5)\n\t"
      "v_cmp_eq_u32_e32 vcc, -1, v128\n\t"
      "v_mfma_f32_16x16x32_bf16 v[188:191], v[152:155], v[100:103], v[188:191]\n\t"
      "v_mfma_f32_16x16x32_bf16 v[192:195], v[152:155], v[104:107], v[192:195]\n\t"
      "s_cbranch_vccnz FQ21R_%=\n\t"
      "FQ21B_%=:\n\t"
      "v_mfma_f32_16x16x32_bf16 v[196:199], v[152:155], v[108:111], v[196:199]\n\t"
      "global_load_dwordx4 v[152:155], v230, s[58:59] sc1\n\t"
      "s_setprio 0\n\t"
      "v_cmp_u_f32_e32 vcc, v184, v185\n\t"
      "v_cmp_u_f32_e64 s[48:49], v186, v187\n\t"
      "s_nop 0\n\t"
      "s_or_b64 vcc, vcc, s[48:49]\n\t"
      "s_cbranch_scc1 NQ2R_%=\n\t"
      "NQ2B_%=:\n\t"
      "ds_write_b128 v243, v[184:187]\n\t"
      "ds_write_b128 v220, v[188:191]\n\t"
      "ds_write_b128 v221, v[192:195]\n\t"
      "ds_write_b128 v222, v[196:199]\n\t"
      "v_mfma_f32_16x16x32_bf16 v[200:203], v[128:131], v[0:3], 0\n\t"
      "s_waitcnt vmcnt(5)\n\t"
      "v_cmp_eq_u32_e32 vcc, -1, v132\n\t"
      "v_mfma_f32_16x16x32_bf16 v[204:207], v[128:131], v[4:7], 0\n\t"
      "v_mfma_f32_16x16x32_bf16 v[208:211], v[128:131], v[8:11], 0\n\t"
      "s_cbranch_vccnz FQ22R_%=\n\t"
      "FQ22B_%=:\n\t"
      "v_mfma_f32_16x16x32_bf16 v[212:215], v[128:131], v[12:15], 0\n\t"
      "global_load_dwordx4 v[128:131], v224, s[60:61] sc1\n\t"
      "v_mfma_f32_16x16x32_bf16 v[200:203], v[132:135], v[16:19], v[200:203]\n\t"
      "s_waitcnt vmcnt(5)\n\t"
      "v_cmp_eq_u32_e32 vcc, -1, v136\n\t"
      "v_mfma_f32_16x16x32_bf16 v[204:207], v[132:135], v[20:23], v[204:207]\n\t"
      "v_mfma_f32_16x16x32_bf16 v[208:211], v[132:135], v[24:27], v[208:211]\n\t"
      "s_cbranch_vccnz FQ23R_%=\n\t"
      "FQ23B_%=:\n\t"
      "v_mfma_f32_16x16x32_bf16 v[212:215], v[132:135], v[28:31], v[212:215]\n\t"
      "global_load_dwordx4 v[132:135], v225, s[60:61] sc1\n\t"
      "v_mfma_f32_16x16x32_bf16 v[200:203], v[136:139], v[32:35], v[200:203]\n\t"
      "s_waitcnt vmcnt(5)\n\t"
      "v_cmp_eq_u32_e32 vcc, -1, v140\n\t"
      "v_mfma_f32_16x16x32_bf16 v[204:207], v[136:139], v[36:39], v[204:207]\n\t"
      "v_mfma_f32_16x16x32_bf16 v[208:211], v[136:139], v[40:43], v[208:211]\n\t"
      "s_cbranch_vccnz FQ24R_%=\n\t"
      "FQ24B_%=:\n\t"
      "v_mfma_f32_16x16x32_bf16 v[212:215], v[136:139], v[44:47], v[212:215]\n\t"
      "global_load_dwordx4 v[136:139], v226, s[60:61] sc1\n\t"
      "v_mfma_f32_16x16x32_bf16 v[200:203], v[140:143], v[48:51], v[200:203]\n\t"
      "s_waitcnt vmcnt(5)\n\t"
      "v_cmp_eq_u32_e32 vcc, -1, v144\n\t"
      "v_mfma_f32_16x16x32_bf16 v[204:207], v[140:143], v[52:55], v[204:207]\n\t"
      "v_mfma_f32_16x16x32_bf16 v[208:211], v[140:143], v[56:59], v[208:211]\n\t"
      "s_cbranch_vccnz FQ25R_%=\n\t"
      "FQ25B_%=:\n\t"
      "v_mfma_f32_16x16x32_bf16 v[212:215], v[140:143], v[60:63], v[212:215]\n\t"
      "global_load_dwordx4 v[140:143], v227, s[60:61] sc1\n\t"
      "v_mfma_f32_16x16x32_bf16 v[200:203], v[144:147], v[64:67], v[200:203]\n\t"
      "s_waitcnt vmcnt(5)\n\t"
      "v_cmp_eq_u32_e32 vcc, -1, v148\n\t"
      "v_mfma_f32_16x16x32_bf16 v[204:207], v[144:147], v[68:71], v[204:207]\n\t"
      "v_mfma_f32_16x16x32_bf16 v[208:211], v[144:147], v[72:75], v[208:211]\n\t"
      "s_cbranch_vccnz FQ26R_%=\n\t"
      "FQ26B_%=:\n\t"
      "v_mfma_f32_16x16x32_bf16 v[212:215], v[144:147], v[76:79], v[212:215]\n\t"
      "global_load_dwordx4 v[144:147], v228, s[60:61] sc1\n\t"
      "s_waitcnt lgkmcnt(0)\n\t"
      "s_barrier\n\t"
      "s_setprio 2\n\t"
      "v_mfma_f32_16x16x32_bf16 v[200:203], v[148:151], v[80:83], v[200:203]\n\t"
      "s_waitcnt vmcnt(5)\n\t"
      "v_cmp_eq_u32_e32 vcc, -1, v152\n\t"
      "v_mfma_f32_16x16x32_bf16 v[204:207], v[148:151], v[84:87], v[204:207]\n\t"
      "v_mfma_f32_16x16x32_bf16 v[208:211], v[148:151], v[88:91], v[208:211]\n\t"
      "s_cbranch_vccnz FQ27R_%=\n\t"
      "FQ27B_%=:\n\t"
      "v_mfma_f32_16x16x32_bf16 v[212:215], v[148:151], v[92:95], v[212:215]\n\t"
      "global_load_dwordx4 v[148:151], v229, s[60:61] sc1\n\t"
      "v_mfma_f32_16x16x32_bf16 v[200:203], v[152:155], v[96:99], v[200:203]\n\t"
      "s_waitcnt vmcnt(5)\n\t"
      "v_cmp_eq_u32_e32 vcc, -1, v128\n\t"
      "v_mfma_f32_16x16x32_bf16 v[204:207], v[152:155], v[100:103], v[204:207]\n\t"
      "v_mfma_f32_16x16x32_bf16 v[208:211], v[152:155], v[104:107], v[208:211]\n\t"
      "s_cbranch_vccnz FQ28R_%=\n\t"
      "FQ28B_%=:\n\t"
      "v_mfma_f32_16x16x32_bf16 v[212:215], v[152:155], v[108:111], v[212:215]\n\t"
      "global_load_dwordx4 v[152:155], v230, s[60:61] sc1\n\t"
      "s_setprio 0\n\t"
      "v_cmp_u_f32_e32 vcc, v200, v201\n\t"
      "v_cmp_u_f32_e64 s[48:49], v202, v203\n\t"
      "s_nop 0\n\t"
      "s_or_b64 vcc, vcc, s[48:49]\n\t"
      "s_cbranch_scc1 NQ3R_%=\n\t"
      "NQ3B_%=:\n\t"
      "ds_write_b128 v243, v[200:203] offset:32768\n\t"
      "ds_write_b128 v220, v[204:207] offset:32768\n\t"
      "ds_write_b128 v221, v[208:211] offset:32768\n\t"
      "ds_write_b128 v222, v[212:215] offset:32768\n\t"
      "v_mfma_f32_16x16x32_bf16 v[184:187], v[128:131], v[0:3], 0\n\t"
      "s_waitcnt vmcnt(5)\n\t"
      "v_cmp_eq_u32_e32 vcc, -1, v132\n\t"
      "v_mfma_f32_16x16x32_bf16 v[188:191], v[128:131], v[4:7], 0\n\t"
      "v_mfma_f32_16x16x32_bf16 v[192:195], v[128:131], v[8:11], 0\n\t"
      "s_cbranch_vccnz FQ29R_%=\n\t"
      "FQ29B_%=:\n\t"
      "v_mfma_f32_16x16x32_bf16 v[196:199], v[128:131], v[12:15], 0\n\t"
      "global_load_dwordx4 v[128:131], v224, s[62:63] sc1\n\t"
      "v_mfma_f32_16x16x32_bf16 v[184:187], v[132:135], v[16:19], v[184:187]\n\t"
      "s_waitcnt vmcnt(5)\n\t"
      "v_cmp_eq_u32_e32 vcc, -1, v136\n\t"
      "v_mfma_f32_16x16x32_bf16 v[188:191], v[132:135], v[20:23], v[188:191]\n\t"
      "v_mfma_f32_16x16x32_bf16 v[192:195], v[132:135], v[24:27], v[192:195]\n\t"
      "s_cbranch_vccnz FQ30R_%=\n\t"
      "FQ30B_%=:\n\t"
      "v_mfma_f32_16x16x32_bf16 v[196:199], v[132:135], v[28:31], v[196:199]\n\t"
      "global_load_dwordx4 v[132:135], v225, s[62:63] sc1\n\t"
      "v_mfma_f32_16x16x32_bf16 v[184:187], v[136:139], v[32:35], v[184:187]\n\t"
      "s_waitcnt vmcnt(5)\n\t"
      "v_cmp_eq_u32_e32 vcc, -1, v140\n\t"
      "v_mfma_f32_16x16x32_bf16 v[188:191], v[136:139], v[36:39], v[188:191]\n\t"
      "v_mfma_f32_16x16x32_bf16 v[192:195], v[136:139], v[40:43], v[192:195]\n\t"
      "s_cbranch_vccnz FQ31R_%=\n\t"
      "FQ31B_%=:\n\t"
      "v_mfma_f32_16x16x32_bf16 v[196:199], v[136:139], v[44:47], v[196:199]\n\t"
      "global_load_dwordx4 v[136:139], v226, s[62:63] sc1\n\t"
      "v_mfma_f32_16x16x32_bf16 v[184:187], v[140:143], v[48:51], v[184:187]\n\t"
      "s_waitcnt vmcnt(5)\n\t"
      "v_cmp_eq_u32_e32 vcc, -1, v144\n\t"
      "v_mfma_f32_16x16x32_bf16 v[188:191], v[140:143], v[52:55], v[188:191]\n\t"
      "v_mfma_f32_16x16x32_bf16 v[192:195], v[140:143], v[56:59], v[192:195]\n\t"
      "s_cbranch_vccnz FQ32R_%=\n\t"
      "FQ32B_%=:\n\t"
      "v_mfma_f32_16x16x32_bf16 v[196:199], v[140:143], v[60:63], v[196:199]\n\t"
      "global_load_dwordx4 v[140:143], v227, s[62:63] sc1\n\t"
      "v_mfma_f32_16x16x32_bf16 v[184:187], v[144:147], v[64:67], v[184:187]\n\t"
      "s_waitcnt vmcnt(5)\n\t"
      "v_cmp_eq_u32_e32 vcc, -1, v148\n\t"
      "v_mfma_f32_16x16x32_bf16 v[188:191], v[144:147], v[68:71], v[188:191]\n\t"
      "v_mfma_f32_16x16x32_bf16 v[192:195], v[144:147], v[72:75], v[192:195]\n\t"
      "s_cbranch_vccnz FQ33R_%=\n\t"
      "FQ33B_%=:\n\t"
      "v_mfma_f32_16x16x32_bf16 v[196:199], v[144:147], v[76:79], v[196:199]\n\t"
      "global_load_dwordx4 v[144:147], v228, s[62:63] sc1\n\t"
      "s_waitcnt lgkmcnt(0)\n\t"
      "s_barrier\n\t"
      "s_setprio 2\n\t"
      "v_mfma_f32_16x16x32_bf16 v[184:187], v[148:151], v[80:83], v[184:187]\n\t"
      "s_waitcnt vmcnt(5)\n\t"
      "v_cmp_eq_u32_e32 vcc, -1, v152\n\t"
      "v_mfma_f32_16x16x32_bf16 v[188:191], v[148:151], v[84:87], v[188:191]\n\t"
      "v_mfma_f32_16x16x32_bf16 v[192:195], v[148:151], v[88:91], v[192:195]\n\t"
      "s_cbranch_vccnz FQ34R_%=\n\t"
      "FQ34B_%=:\n\t"
      "v_mfma_f32_16x16x32_bf16 v[196:199], v[148:151], v[92:95], v[196:199]\n\t"
      "global_load_dwordx4 v[148:151], v229, s[62:63] sc1\n\t"
      "v_mfma_f32_16x16x32_bf16 v[184:187], v[152:155], v[96:99], v[184:187]\n\t"
      "s_waitcnt vmcnt(5)\n\t"
      "v_cmp_eq_u32_e32 vcc, -1, v128\n\t"
      "v_mfma_f32_16x16x32_bf16 v[188:191], v[152:155], v[100:103], v[188:191]\n\t"
      "v_mfma_f32_16x16x32_bf16 v[192:195], v[152:155], v[104:107], v[192:195]\n\t"
      "s_cbranch_vccnz FQ35R_%=\n\t"
      "FQ35B_%=:\n\t"
      "v_mfma_f32_16x16x32_bf16 v[196:199], v[152:155], v[108:111], v[196:199]\n\t"
      "global_load_dwordx4 v[152:155], v230, s[62:63] sc1\n\t"
      "s_setprio 0\n\t"
      "v_cmp_u_f32_e32 vcc, v184, v185\n\t"
      "v_cmp_u_f32_e64 s[48:49], v186, v187\n\t"
      "s_nop 0\n\t"
      "s_or_b64 vcc, vcc, s[48:49]\n\t"
      "s_cbranch_scc1 NQ4R_%=\n\t"
      "NQ4B_%=:\n\t"
      "ds_write_b128 v243, v[184:187]\n\t"
      "ds_write_b128 v220, v[188:191]\n\t"
      "ds_write_b128 v221, v[192:195]\n\t"
      "ds_write_b128 v222, v[196:199]\n\t"
      "v_mfma_f32_16x16x32_bf16 v[200:203], v[128:131], v[0:3], 0\n\t"
      "s_waitcnt vmcnt(5)\n\t"
      "v_cmp_eq_u32_e32 vcc, -1, v132\n\t"
      "v_mfma_f32_16x16x32_bf16 v[204:207], v[128:131], v[4:7], 0\n\t"
      "v_mfma_f32_16x16x32_bf16 v[208:211], v[128:131], v[8:11], 0\n\t"
      "s_cbranch_vccnz FQ36R_%=\n\t"
      "FQ36B_%=:\n\t"
      "v_mfma_f32_16x16x32_bf16 v[212:215], v[128:131], v[12:15], 0\n\t"
      "global_load_dwordx4 v[128:131], v224, s[64:65] sc1\n\t"
      "v_mfma_f32_16x16x32_bf16 v[200:203], v[132:135], v[16:19], v[200:203]\n\t"
      "s_waitcnt vmcnt(5)\n\t"
      "v_cmp_eq_u32_e32 vcc, -1, v136\n\t"
      "v_mfma_f32_16x16x32_bf16 v[204:207], v[132:135], v[20:23], v[204:207]\n\t"
      "v_mfma_f32_16x16x32_bf16 v[208:211], v[132:135], v[24:27], v[208:211]\n\t"
      "s_cbranch_vccnz FQ37R_%=\n\t"
      "FQ37B_%=:\n\t"
      "v_mfma_f32_16x16x32_bf16 v[212:215], v[132:135], v[28:31], v[212:215]\n\t"
      "global_load_dwordx4 v[132:135], v225, s[64:65] sc1\n\t"
      "v_mfma_f32_16x16x32_bf16 v[200:203], v[136:139], v[32:35], v[200:203]\n\t"
      "s_waitcnt vmcnt(5)\n\t"
      "v_cmp_eq_u32_e32 vcc, -1, v140\n\t"
      "v_mfma_f32_16x16x32_bf16 v[204:207], v[136:139], v[36:39], v[204:207]\n\t"
      "v_mfma_f32_16x16x32_bf16 v[208:211], v[136:139], v[40:43], v[208:211]\n\t"
      "s_cbranch_vccnz FQ38R_%=\n\t"
      "FQ38B_%=:\n\t"
      "v_mfma_f32_16x16x32_bf16 v[212:215], v[136:139], v[44:47], v[212:215]\n\t"
      "global_load_dwordx4 v[136:139], v226, s[64:65] sc1\n\t"
      "v_mfma_f32_16x16x32_bf16 v[200:203], v[140:143], v[48:51], v[200:203]\n\t"
      "s_waitcnt vmcnt(5)\n\t"
      "v_cmp_eq_u32_e32 vcc, -1, v144\n\t"
      "v_mfma_f32_16x16x32_bf16 v[204:207], v[140:143], v[52:55], v[204:207]\n\t"
      "v_mfma_f32_16x16x32_bf16 v[208:211], v[140:143], v[56:59], v[208:211]\n\t"
      "s_cbranch_vccnz FQ39R_%=\n\t"
      "FQ39B_%=:\n\t"
      "v_mfma_f32_16x16x32_bf16 v[212:215], v[140:143], v[60:63], v[212:215]\n\t"
      "global_load_dwordx4 v[140:143], v227, s[64:65] sc1\n\t"
      "v_mfma_f32_16x16x32_bf16 v[200:203], v[144:147], v[64:67], v[200:203]\n\t"
      "s_waitcnt vmcnt(5)\n\t"
      "v_cmp_eq_u32_e32 vcc, -1, v148\n\t"
      "v_mfma_f32_16x16x32_bf16 v[204:207], v[144:147], v[68:71], v[204:207]\n\t"
      "v_mfma_f32_16x16x32_bf16 v[208:211], v[144:147], v[72:75], v[208:211]\n\t"
      "s_cbranch_vccnz FQ40R_%=\n\t"
      "FQ40B_%=:\n\t"
      "v_mfma_f32_16x16x32_bf16 v[212:215], v[144:147], v[76:79], v[212:215]\n\t"
      "global_load_dwordx4 v[144:147], v228, s[64:65] sc1\n\t"
      "s_waitcnt lgkmcnt(0)\n\t"
      "s_barrier\n\t"
      "s_setprio 2\n\t"
      "v_mfma_f32_16x16x32_bf16 v[200:203], v[148:151], v[80:83], v[200:203]\n\t"
      "s_waitcnt vmcnt(5)\n\t"
      "v_cmp_eq_u32_e32 vcc, -1, v152\n\t"
      "v_mfma_f32_16x16x32_bf16 v[204:207], v[148:151], v[84:87], v[204:207]\n\t"
      "v_mfma_f32_16x16x32_bf16 v[208:211], v[148:151], v[88:91], v[208:211]\n\t"
      "s_cbranch_vccnz FQ41R_%=\n\t"
      "FQ41B_%=:\n\t"
      "v_mfma_f32_16x16x32_bf16 v[212:215], v[148:151], v[92:95], v[212:215]\n\t"
      "global_load_dwordx4 v[148:151], v229, s[64:65] sc1\n\t"
      "v_mfma_f32_16x16x32_bf16 v[200:203], v[152:155], v[96:99], v[200:203]\n\t"
      "s_waitcnt vmcnt(5)\n\t"
      "v_cmp_eq_u32_e32 vcc, -1, v128\n\t"
      "v_mfma_f32_16x16x32_bf16 v[204:207], v[152:155], v[100:103], v[204:207]\n\t"
      "v_mfma_f32_16x16x32_bf16 v[208:211], v[152:155], v[104:107], v[208:211]\n\t"
      "s_cbranch_vccnz FQ42R_%=\n\t"
      "FQ42B_%=:\n\t"
      "v_mfma_f32_16x16x32_bf16 v[212:215], v[152:155], v[108:111], v[212:215]\n\t"
      "global_load_dwordx4 v[152:155], v230, s[64:65] sc1\n\t"
      "s_setprio 0\n\t"
      "v_cmp_u_f32_e32 vcc, v200, v201\n\t"
      "v_cmp_u_f32_e64 s[48:49], v202, v203\n\t"
      "s_nop 0\n\t"
      "s_or_b64 vcc, vcc, s[48:49]\n\t"
      "s_cbranch_scc1 NQ5R_%=\n\t"
      "NQ5B_%=:\n\t"
      "ds_write_b128 v243, v[200:203] offset:32768\n\t"
      "ds_write_b128 v220, v[204:207] offset:32768\n\t"
      "ds_write_b128 v221, v[208:211] offset:32768\n\t"
      "ds_write_b128 v222, v[212:215] offset:32768\n\t"
      "v_mfma_f32_16x16x32_bf16 v[184:187], v[128:131], v[0:3], 0\n\t"
      "s_waitcnt vmcnt(5)\n\t"
      "v_cmp_eq_u32_e32 vcc, -1, v132\n\t"
      "v_mfma_f32_16x16x32_bf16 v[188:191], v[128:131], v[4:7], 0\n\t"
      "v_mfma_f32_16x16x32_bf16 v[192:195], v[128:131], v[8:11], 0\n\t"
      "s_cbranch_vccnz FQ43R_%=\n\t"
      "FQ43B_%=:\n\t"
      "v_mfma_f32_16x16x32_bf16 v[196:199], v[128:131], v[12:15], 0\n\t"
      "v_mfma_f32_16x16x32_bf16 v[184:187], v[132:135], v[16:19], v[184:187]\n\t"
      "s_waitcnt vmcnt(4)\n\t"
      "v_cmp_eq_u32_e32 vcc, -1, v136\n\t"
      "v_mfma_f32_16x16x32_bf16 v[188:191], v[132:135], v[20:23], v[188:191]\n\t"
      "v_mfma_f32_16x16x32_bf16 v[192:195], v[132:135], v[24:27], v[192:195]\n\t"
      "s_cbranch_vccnz FQ44R_%=\n\t"
      "FQ44B_%=:\n\t"
      "v_mfma_f32_16x16x32_bf16 v[196:199], v[132:135], v[28:31], v[196:199]\n\t"
      "v_mfma_f32_16x16x32_bf16 v[184:187], v[136:139], v[32:35], v[184:187]\n\t"
      "s_waitcnt vmcnt(3)\n\t"
      "v_cmp_eq_u32_e32 vcc, -1, v140\n\t"
      "v_mfma_f32_16x16x32_bf16 v[188:191], v[136:139], v[36:39], v[188:191]\n\t"
      "v_mfma_f32_16x16x32_bf16 v[192:195], v[136:139], v[40:43], v[192:195]\n\t"
      "s_cbranch_vccnz FQ45R_%=\n\t"
      "FQ45B_%=:\n\t"
      "v_mfma_f32_16x16x32_bf16 v[196:199], v[136:139], v[44:47], v[196:199]\n\t"
      "v_mfma_f32_16x16x32_bf16 v[184:187], v[140:143], v[48:51], v[184:187]\n\t"
      "s_waitcnt vmcnt(2)\n\t"
      "v_cmp_eq_u32_e32 vcc, -1, v144\n\t"
      "v_mfma_f32_16x16x32_bf16 v[188:191], v[140:143], v[52:55], v[188:191]\n\t"
      "v_mfma_f32_16x16x32_bf16 v[192:195], v[140:143], v[56:59], v[192:195]\n\t"
      "s_cbranch_vccnz FQ46R_%=\n\t"
      "FQ46B_%=:\n\t"
      "v_mfma_f32_16x16x32_bf16 v[196:199], v[140:143], v[60:63], v[196:199]\n\t"
      "v_mfma_f32_16x16x32_bf16 v[184:187], v[144:147], v[64:67], v[184:187]\n\t"
      "s_waitcnt vmcnt(1)\n\t"
      "v_cmp_eq_u32_e32 vcc, -1, v148\n\t"
      "v_mfma_f32_16x16x32_bf16 v[188:191], v[144:147], v[68:71], v[188:191]\n\t"
      "v_mfma_f32_16x16x32_bf16 v[192:195], v[144:147], v[72:75], v[192:195]\n\t"
      "s_cbranch_vccnz FQ47R_%=\n\t"
      "FQ47B_%=:\n\t"
      "v_mfma_f32_16x16x32_bf16 v[196:199], v[144:147], v[76:79], v[196:199]\n\t"
      "s_waitcnt lgkmcnt(0)\n\t"
      "s_barrier\n\t"
      "s_setprio 2\n\t"
      "v_mfma_f32_16x16x32_bf16 v[184:187], v[148:151], v[80:83], v[184:187]\n\t"
      "s_waitcnt vmcnt(0)\n\t"
      "v_cmp_eq_u32_e32 vcc, -1, v152\n\t"
      "v_mfma_f32_16x16x32_bf16 v[188:191], v[148:151], v[84:87], v[188:191]\n\t"
      "v_mfma_f32_16x16x32_bf16 v[192:195], v[148:151], v[88:91], v[192:195]\n\t"
      "s_cbranch_vccnz FQ48R_%=\n\t"
      "FQ48B_%=:\n\t"
      "v_mfma_f32_16x16x32_bf16 v[196:199], v[148:151], v[92:95], v[196:199]\n\t"
      "v_mfma_f32_16x16x32_bf16 v[184:187], v[152:155], v[96:99], v[184:187]\n\t"
      "v_mfma_f32_16x16x32_bf16 v[188:191], v[152:155], v[100:103], v[188:191]\n\t"
      "v_mfma_f32_16x16x32_bf16 v[192:195], v[152:155], v[104:107], v[192:195]\n\t"
      "v_mfma_f32_16x16x32_bf16 v[196:199], v[152:155], v[108:111], v[196:199]\n\t"
      "s_nop 4\n\t"
      "v_cmp_u_f32_e32 vcc, v184, v185\n\t"
      "v_cmp_u_f32_e64 s[48:49], v186, v187\n\t"
      "s_nop 0\n\t"
      "s_or_b64 vcc, vcc, s[48:49]\n\t"
      "s_cbranch_scc1 NQ6R_%=\n\t"
      "NQ6B_%=:\n\t"
      "ds_write_b128 v243, v[184:187]\n\t"
      "ds_write_b128 v220, v[188:191]\n\t"
      "ds_write_b128 v221, v[192:195]\n\t"
      "ds_write_b128 v222, v[196:199]\n\t"
      "s_waitcnt lgkmcnt(0)\n\t"
      "s_barrier\n\t"
      "s_branch GX_%=\n\t"
      "HWR_%=:\n\t"
      "s_mov_b32 s51, 0\n\t"
      "HWL_%=:\n\t"
      "s_add_u32 s51, s51, 1\n\t"
      "s_cmp_lt_u32 s51, 262144\n\t"
      "s_cbranch_scc0 HWX_%=\n\t"
      "s_sleep 2\n\t"
      "global_load_dwordx4 v[112:115], v116, s[12:13] sc1\n\t"
      "s_waitcnt vmcnt(0)\n\t"
      "v_xor_b32_e32 v114, v114, v112\n\t"
      "v_xor_b32_e32 v115, v115, v113\n\t"
      "v_cmp_ne_u64_e32 vcc, s[14:15], v[114:115]\n\t"
      "s_cbranch_vccnz HWL_%=\n\t"
      "HWX_%=:\n\t"
      "s_branch HWB_%=\n\t"
      "HXR_%=:\n\t"
      "s_mov_b32 s51, 0\n\t"
      "HXL_%=:\n\t"
      "s_add_u32 s51, s51, 1\n\t"
      "s_cmp_lt_u32 s51, 262144\n\t"
      "s_cbranch_scc0 HXX_%=\n\t"
      "s_sleep 2\n\t"
      "s_mov_b32 exec_hi, s23\n\t"
      "s_not_b32 exec_lo, s23\n\t"
      "s_add_u32 s68, s20, 0\n\t"
      "s_addc_u32 s69, s21, 0\n\t"
      "global_load_dwordx4 v[184:187], v240, s[68:69] sc1\n\t"
      "s_add_u32 s68, s20, 65536\n\t"
      "s_addc_u32 s69, s21, 0\n\t"
      "global_load_dwordx4 v[188:191], v240, s[68:69] sc1\n\t"
      "s_add_u32 s68, s20, 131072\n\t"
      "s_addc_u32 s69, s21, 0\n\t"
      "global_load_dwordx4 v[192:195], v240, s[68:69] sc1\n\t"
      "s_add_u32 s68, s20, 196608\n\t"
      "s_addc_u32 s69, s21, 0\n\t"
      "global_load_dwordx4 v[196:199], v240, s[68:69] sc1\n\t"
      "s_add_u32 s68, s20, 262144\n\t"
      "s_addc_u32 s69, s21, 0\n\t"
      "global_load_dwordx4 v[200:203], v240, s[68:69] sc1\n\t"
      "s_add_u32 s68, s20, 327680\n\t"
      "s_addc_u32 s69, s21, 0\n\t"
      "global_load_dwordx4 v[204:207], v240, s[68:69] sc1\n\t"
      "s_add_u32 s68, s20, 393216\n\t"
      "s_addc_u32 s69, s21, 0\n\t"
      "global_load_dwordx4 v[208:211], v240, s[68:69] sc1\n\t"
      "s_mov_b64 exec, -1\n\t"
      "s_waitcnt vmcnt(0)\n\t"
      "v_max3_u32 v234, v184, v185, v186\n\t"
      "v_max3_u32 v234, v234, v187, v188\n\t"
      "v_max3_u32 v234, v234, v189, v190\n\t"
      "v_max3_u32 v234, v234, v191, v192\n\t"
      "v_max3_u32 v234, v234, v193, v194\n\t"
      "v_max3_u32 v234, v234, v195, v196\n\t"
      "v_max3_u32 v234, v234, v197, v198\n\t"
      "v_max3_u32 v234, v234, v199, v200\n\t"
      "v_max3_u32 v234, v234, v201, v202\n\t"
      "v_max3_u32 v234, v234, v203, v204\n\t"
      "v_max3_u32 v234, v234, v205, v206\n\t"
      "v_max3_u32 v234, v234, v207, v208\n\t"
      "v_max3_u32 v234, v234, v209, v210\n\t"
      "v_max_u32_e32 v234, v234, v211\n\t"
      "v_cmp_eq_u32_e32 vcc, -1, v234\n\t"
      "s_cbranch_vccnz HXL_%=\n\t"
      "HXX_%=:\n\t"
      "s_nop 1\n\t"
      "s_branch HXB_%=\n\t"
      "FQ0R_%=:\n\t"
      "s_mov_b32 s51, 0\n\t"
      "FQ0L_%=:\n\t"
      "s_add_u32 s51, s51, 1\n\t"
      "s_cmp_lt_u32 s51, 262144\n\t"
      "s_cbranch_scc0 FQ0X_%=\n\t"
      "s_waitcnt vmcnt(0)\n\t"
      "s_sleep 1\n\t"
      "global_load_dwordx4 v[128:131], v224, s[52:53] sc1\n\t"
      "global_load_dwordx4 v[132:135], v225, s[52:53] sc1\n\t"
      "global_load_dwordx4 v[136:139], v226, s[52:53] sc1\n\t"
      "global_load_dwordx4 v[140:143], v227, s[52:53] sc1\n\t"
      "global_load_dwordx4 v[144:147], v228, s[52:53] sc1\n\t"
      "global_load_dwordx4 v[148:151], v229, s[52:53] sc1\n\t"
      "global_load_dwordx4 v[152:155], v230, s[52:53] sc1\n\t"
      "s_waitcnt vmcnt(6)\n\t"
      "v_max3_u32 v234, v128, v129, v130\n\t"
      "v_max_u32_e32 v234, v234, v131\n\t"
      "v_cmp_eq_u32_e32 vcc, -1, v234\n\t"
      "s_cbranch_vccnz FQ0L_%=\n\t"
      "FQ0X_%=:\n\t"
      "s_branch FQ0B_%=\n\t"
      "FQ1R_%=:\n\t"
      "s_mov_b32 s51, 0\n\t"
      "FQ1L_%=:\n\t"
      "s_add_u32 s51, s51, 1\n\t"
      "s_cmp_lt_u32 s51, 262144\n\t"
      "s_cbranch_scc0 FQ1X_%=\n\t"
      "s_waitcnt vmcnt(0)\n\t"
      "s_sleep 1\n\t"
      "global_load_dwordx4 v[132:135], v225, s[52:53] sc1\n\t"
      "global_load_dwordx4 v[136:139], v226, s[52:53] sc1\n\t"
      "global_load_dwordx4 v[140:143], v227, s[52:53] sc1\n\t"
      "global_load_dwordx4 v[144:147], v228, s[52:53] sc1\n\t"
      "global_load_dwordx4 v[148:151], v229, s[52:53] sc1\n\t"
      "global_load_dwordx4 v[152:155], v230, s[52:53] sc1\n\t"
      "global_load_dwordx4 v[128:131], v224, s[54:55] sc1\n\t"
      "s_waitcnt vmcnt(6)\n\t"
      "v_max3_u32 v234, v132, v133, v134\n\t"
      "v_max_u32_e32 v234, v234, v135\n\t"
      "v_cmp_eq_u32_e32 vcc, -1, v234\n\t"
      "s_cbranch_vccnz FQ1L_%=\n\t"
      "FQ1X_%=:\n\t"
      "s_branch FQ1B_%=\n\t"
      "FQ2R_%=:\n\t"
      "s_mov_b32 s51, 0\n\t"
      "FQ2L_%=:\n\t"
      "s_add_u32 s51, s51, 1\n\t"
      "s_cmp_lt_u32 s51, 262144\n\t"
      "s_cbranch_scc0 FQ2X_%=\n\t"
      "s_waitcnt vmcnt(0)\n\t"
      "s_sleep 1\n\t"
      "global_load_dwordx4 v[136:139], v226, s[52:53] sc1\n\t"
      "global_load_dwordx4 v[140:143], v227, s[52:53] sc1\n\t"
      "global_load_dwordx4 v[144:147], v228, s[52:53] sc1\n\t"
      "global_load_dwordx4 v[148:151], v229, s[52:53] sc1\n\t"
      "global_load_dwordx4 v[152:155], v230, s[52:53] sc1\n\t"
      "global_load_dwordx4 v[128:131], v224, s[54:55] sc1\n\t"
      "global_load_dwordx4 v[132:135], v225, s[54:55] sc1\n\t"
      "s_waitcnt vmcnt(6)\n\t"
      "v_max3_u32 v234, v136, v137, v138\n\t"
      "v_max_u32_e32 v234, v234, v139\n\t"
      "v_cmp_eq_u32_e32 vcc, -1, v234\n\t"
      "s_cbranch_vccnz FQ2L_%=\n\t"
      "FQ2X_%=:\n\t"
      "s_branch FQ2B_%=\n\t"
      "FQ3R_%=:\n\t"
      "s_mov_b32 s51, 0\n\t"
      "FQ3L_%=:\n\t"
      "s_add_u32 s51, s51, 1\n\t"
      "s_cmp_lt_u32 s51, 262144\n\t"
      "s_cbranch_scc0 FQ3X_%=\n\t"
      "s_waitcnt vmcnt(0)\n\t"
      "s_sleep 1\n\t"
      "global_load_dwordx4 v[140:143], v227, s[52:53] sc1\n\t"
      "global_load_dwordx4 v[144:147], v228, s[52:53] sc1\n\t"
      "global_load_dwordx4 v[148:151], v229, s[52:53] sc1\n\t"
      "global_load_dwordx4 v[152:155], v230, s[52:53] sc1\n\t"
      "global_load_dwordx4 v[128:131], v224, s[54:55] sc1\n\t"
      "global_load_dwordx4 v[132:135], v225, s[54:55] sc1\n\t"
      "global_load_dwordx4 v[136:139], v226, s[54:55] sc1\n\t"
      "s_waitcnt vmcnt(6)\n\t"
      "v_max3_u32 v234, v140, v141, v142\n\t"
      "v_max_u32_e32 v234, v234, v143\n\t"
      "v_cmp_eq_u32_e32 vcc, -1, v234\n\t"
      "s_cbranch_vccnz FQ3L_%=\n\t"
      "FQ3X_%=:\n\t"
      "s_branch FQ3B_%=\n\t"
      "FQ4R_%=:\n\t"
      "s_mov_b32 s51, 0\n\t"
      "FQ4L_%=:\n\t"
      "s_add_u32 s51, s51, 1\n\t"
      "s_cmp_lt_u32 s51, 262144\n\t"
      "s_cbranch_scc0 FQ4X_%=\n\t"
      "s_waitcnt vmcnt(0)\n\t"
      "s_sleep 1\n\t"
      "global_load_dwordx4 v[144:147], v228, s[52:53] sc1\n\t"
      "global_load_dwordx4 v[148:151], v229, s[52:53] sc1\n\t"
      "global_load_dwordx4 v[152:155], v230, s[52:53] sc1\n\t"
      "global_load_dwordx4 v[128:131], v224, s[54:55] sc1\n\t"
      "global_load_dwordx4 v[132:135], v225, s[54:55] sc1\n\t"
      "global_load_dwordx4 v[136:139], v226, s[54:55] sc1\n\t"
      "global_load_dwordx4 v[140:143], v227, s[54:55] sc1\n\t"
      "s_waitcnt vmcnt(6)\n\t"
      "v_max3_u32 v234, v144, v145, v146\n\t"
      "v_max_u32_e32 v234, v234, v147\n\t"
      "v_cmp_eq_u32_e32 vcc, -1, v234\n\t"
      "s_cbranch_vccnz FQ4L_%=\n\t"
      "FQ4X_%=:\n\t"
      "s_branch FQ4B_%=\n\t"
      "FQ5R_%=:\n\t"
      "s_mov_b32 s51, 0\n\t"
      "FQ5L_%=:\n\t"
      "s_add_u32 s51, s51, 1\n\t"
      "s_cmp_lt_u32 s51, 262144\n\t"
      "s_cbranch_scc0 FQ5X_%=\n\t"
      "s_waitcnt vmcnt(0)\n\t"
      "s_sleep 1\n\t"
      "global_load_dwordx4 v[148:151], v229, s[52:53] sc1\n\t"
      "global_load_dwordx4 v[152:155], v230, s[52:53] sc1\n\t"
      "global_load_dwordx4 v[128:131], v224, s[54:55] sc1\n\t"
      "global_load_dwordx4 v[132:135], v225, s[54:55] sc1\n\t"
      "global_load_dwordx4 v[136:139], v226, s[54:55] sc1\n\t"
      "global_load_dwordx4 v[140:143], v227, s[54:55] sc1\n\t"
      "global_load_dwordx4 v[144:147], v228, s[54:55] sc1\n\t"
      "s_waitcnt vmcnt(6)\n\t"
      "v_max3_u32 v234, v148, v149, v150\n\t"
      "v_max_u32_e32 v234, v234, v151\n\t"
      "v_cmp_eq_u32_e32 vcc, -1, v234\n\t"
      "s_cbranch_vccnz FQ5L_%=\n\t"
      "FQ5X_%=:\n\t"
      "s_branch FQ5B_%=\n\t"
      "FQ6R_%=:\n\t"
      "s_mov_b32 s51, 0\n\t"
      "FQ6L_%=:\n\t"
      "s_add_u32 s51, s51, 1\n\t"
      "s_cmp_lt_u32 s51, 262144\n\t"
      "s_cbranch_scc0 FQ6X_%=\n\t"
      "s_waitcnt vmcnt(0)\n\t"
      "s_sleep 1\n\t"
      "global_load_dwordx4 v[152:155], v230, s[52:53] sc1\n\t"
      "global_load_dwordx4 v[128:131], v224, s[54:55] sc1\n\t"
      "global_load_dwordx4 v[132:135], v225, s[54:55] sc1\n\t"
      "global_load_dwordx4 v[136:139], v226, s[54:55] sc1\n\t"
      "global_load_dwordx4 v[140:143], v227, s[54:55] sc1\n\t"
      "global_load_dwordx4 v[144:147], v228, s[54:55] sc1\n\t"
      "global_load_dwordx4 v[148:151], v229, s[54:55] sc1\n\t"
      "s_waitcnt vmcnt(6)\n\t"
      "v_max3_u32 v234, v152, v153, v154\n\t"
      "v_max_u32_e32 v234, v234, v155\n\t"
      "v_cmp_eq_u32_e32 vcc, -1, v234\n\t"
      "s_cbranch_vccnz FQ6L_%=\n\t"
      "FQ6X_%=:\n\t"
      "s_branch FQ6B_%=\n\t"
      "FQ7R_%=:\n\t"
      "s_mov_b32 s51, 0\n\t"
      "FQ7L_%=:\n\t"
      "s_add_u32 s51, s51, 1\n\t"
      "s_cmp_lt_u32 s51, 262144\n\t"
      "s_cbranch_scc0 FQ7X_%=\n\t"
      "s_waitcnt vmcnt(0)\n\t"
      "s_sleep 4\n\t"
      "global_load_dwordx4 v[128:131], v224, s[54:55] sc1\n\t"
      "global_load_dwordx4 v[132:135], v225, s[54:55] sc1\n\t"
      "global_load_dwordx4 v[136:139], v226, s[54:55] sc1\n\t"
      "global_load_dwordx4 v[140:143], v227, s[54:55] sc1\n\t"
      "global_load_dwordx4 v[144:147], v228, s[54:55] sc1\n\t"
      "global_load_dwordx4 v[148:151], v229, s[54:55] sc1\n\t"
      "s_waitcnt vmcnt(5)\n\t"
      "v_max3_u32 v234, v128, v129, v130\n\t"
      "v_max_u32_e32 v234, v234, v131\n\t"
      "v_cmp_eq_u32_e32 vcc, -1, v234\n\t"
      "s_cbranch_vccnz FQ7L_%=\n\t"
      "FQ7X_%=:\n\t"
      "s_branch FQ7B_%=\n\t"
      "FQ8R_%=:\n\t"
      "s_mov_b32 s51, 0\n\t"
      "FQ8L_%=:\n\t"
      "s_add_u32 s51, s51, 1\n\t"
      "s_cmp_lt_u32 s51, 262144\n\t"
      "s_cbranch_scc0 FQ8X_%=\n\t"
      "s_waitcnt vmcnt(0)\n\t"
      "s_sleep 4\n\t"
      "global_load_dwordx4 v[132:135], v225, s[54:55] sc1\n\t"
      "global_load_dwordx4 v[136:139], v226, s[54:55] sc1\n\t"
      "global_load_dwordx4 v[140:143], v227, s[54:55] sc1\n\t"
      "global_load_dwordx4 v[144:147], v228, s[54:55] sc1\n\t"
      "global_load_dwordx4 v[148:151], v229, s[54:55] sc1\n\t"
      "global_load_dwordx4 v[152:155], v230, s[54:55] sc1\n\t"
      "s_waitcnt vmcnt(5)\n\t"
      "v_max3_u32 v234, v132, v133, v134\n\t"
      "v_max_u32_e32 v234, v234, v135\n\t"
      "v_cmp_eq_u32_e32 vcc, -1, v234\n\t"
      "s_cbranch_vccnz FQ8L_%=\n\t"
      "FQ8X_%=:\n\t"
      "s_branch FQ8B_%=\n\t"
      "FQ9R_%=:\n\t"
      "s_mov_b32 s51, 0\n\t"
      "FQ9L_%=:\n\t"
      "s_add_u32 s51, s51, 1\n\t"
      "s_cmp_lt_u32 s51, 262144\n\t"
      "s_cbranch_scc0 FQ9X_%=\n\t"
      "s_waitcnt vmcnt(0)\n\t"
      "s_sleep 4\n\t"
      "global_load_dwordx4 v[136:139], v226, s[54:55] sc1\n\t"
      "global_load_dwordx4 v[140:143], v227, s[54:55] sc1\n\t"
      "global_load_dwordx4 v[144:147], v228, s[54:55] sc1\n\t"
      "global_load_dwordx4 v[148:151], v229, s[54:55] sc1\n\t"
      "global_load_dwordx4 v[152:155], v230, s[54:55] sc1\n\t"
      "global_load_dwordx4 v[128:131], v224, s[56:57] sc1\n\t"
      "s_waitcnt vmcnt(5)\n\t"
      "v_max3_u32 v234, v136, v137, v138\n\t"
      "v_max_u32_e32 v234, v234, v139\n\t"
      "v_cmp_eq_u32_e32 vcc, -1, v234\n\t"
      "s_cbranch_vccnz FQ9L_%=\n\t"
      "FQ9X_%=:\n\t"
      "s_branch FQ9B_%=\n\t"
      "FQ10R_%=:\n\t"
      "s_mov_b32 s51, 0\n\t"
      "FQ10L_%=:\n\t"
      "s_add_u32 s51, s51, 1\n\t"
      "s_cmp_lt_u32 s51, 262144\n\t"
      "s_cbranch_scc0 FQ10X_%=\n\t"
      "s_waitcnt vmcnt(0)\n\t"
      "s_sleep 4\n\t"
      "global_load_dwordx4 v[140:143], v227, s[54:55] sc1\n\t"
      "global_load_dwordx4 v[144:147], v228, s[54:55] sc1\n\t"
      "global_load_dwordx4 v[148:151], v229, s[54:55] sc1\n\t"
      "global_load_dwordx4 v[152:155], v230, s[54:55] sc1\n\t"
      "global_load_dwordx4 v[128:131], v224, s[56:57] sc1\n\t"
      "global_load_dwordx4 v[132:135], v225, s[56:57] sc1\n\t"
      "s_waitcnt vmcnt(5)\n\t"
      "v_max3_u32 v234, v140, v141, v142\n\t"
      "v_max_u32_e32 v234, v234, v143\n\t"
      "v_cmp_eq_u32_e32 vcc, -1, v234\n\t"
      "s_cbranch_vccnz FQ10L_%=\n\t"
      "FQ10X_%=:\n\t"
      "s_branch FQ10B_%=\n\t"
      "FQ11R_%=:\n\t"
      "s_mov_b32 s51, 0\n\t"
      "FQ11L_%=:\n\t"
      "s_add_u32 s51, s51, 1\n\t"
      "s_cmp_lt_u32 s51, 262144\n\t"
      "s_cbranch_scc0 FQ11X_%=\n\t"
      "s_waitcnt vmcnt(0)\n\t"
      "s_sleep 4\n\t"
      "global_load_dwordx4 v[144:147], v228, s[54:55] sc1\n\t"
      "global_load_dwordx4 v[148:151], v229, s[54:55] sc1\n\t"
      "global_load_dwordx4 v[152:155], v230, s[54:55] sc1\n\t"
      "global_load_dwordx4 v[128:131], v224, s[56:57] sc1\n\t"
      "global_load_dwordx4 v[132:135], v225, s[56:57] sc1\n\t"
      "global_load_dwordx4 v[136:139], v226, s[56:57] sc1\n\t"
      "s_waitcnt vmcnt(5)\n\t"
      "v_max3_u32 v234, v144, v145, v146\n\t"
      "v_max_u32_e32 v234, v234, v147\n\t"
      "v_cmp_eq_u32_e32 vcc, -1, v234\n\t"
      "s_cbranch_vccnz FQ11L_%=\n\t"
      "FQ11X_%=:\n\t"
      "s_branch FQ11B_%=\n\t"
      "FQ12R_%=:\n\t"
      "s_mov_b32 s51, 0\n\t"
      "FQ12L_%=:\n\t"
      "s_add_u32 s51, s51, 1\n\t"
      "s_cmp_lt_u32 s51, 262144\n\t"
      "s_cbranch_scc0 FQ12X_%=\n\t"
      "s_waitcnt vmcnt(0)\n\t"
      "s_sleep 4\n\t"
      "global_load_dwordx4 v[148:151], v229, s[54:55] sc1\n\t"
      "global_load_dwordx4 v[152:155], v230, s[54:55] sc1\n\t"
      "global_load_dwordx4 v[128:131], v224, s[56:57] sc1\n\t"
      "global_load_dwordx4 v[132:135], v225, s[56:57] sc1\n\t"
      "global_load_dwordx4 v[136:139], v226, s[56:57] sc1\n\t"
      "global_load_dwordx4 v[140:143], v227, s[56:57] sc1\n\t"
      "s_waitcnt vmcnt(5)\n\t"
      "v_max3_u32 v234, v148, v149, v150\n\t"
      "v_max_u32_e32 v234, v234, v151\n\t"
      "v_cmp_eq_u32_e32 vcc, -1, v234\n\t"
      "s_cbranch_vccnz FQ12L_%=\n\t"
      "FQ12X_%=:\n\t"
      "s_branch FQ12B_%=\n\t"
      "FQ13R_%=:\n\t"
      "s_mov_b32 s51, 0\n\t"
      "FQ13L_%=:\n\t"
      "s_add_u32 s51, s51, 1\n\t"
      "s_cmp_lt_u32 s51, 262144\n\t"
      "s_cbranch_scc0 FQ13X_%=\n\t"
      "s_waitcnt vmcnt(0)\n\t"
      "s_sleep 4\n\t"
      "global_load_dwordx4 v[152:155], v230, s[54:55] sc1\n\t"
      "global_load_dwordx4 v[128:131], v224, s[56:57] sc1\n\t"
      "global_load_dwordx4 v[132:135], v225, s[56:57] sc1\n\t"
      "global_load_dwordx4 v[136:139], v226, s[56:57] sc1\n\t"
      "global_load_dwordx4 v[140:143], v227, s[56:57] sc1\n\t"
      "global_load_dwordx4 v[144:147], v228, s[56:57] sc1\n\t"
      "s_waitcnt vmcnt(5)\n\t"
      "v_max3_u32 v234, v152, v153, v154\n\t"
      "v_max_u32_e32 v234, v234, v155\n\t"
      "v_cmp_eq_u32_e32 vcc, -1, v234\n\t"
      "s_cbranch_vccnz FQ13L_%=\n\t"
      "FQ13X_%=:\n\t"
      "s_branch FQ13B_%=\n\t"
      "FQ14R_%=:\n\t"
      "s_mov_b32 s51, 0\n\t"
      "FQ14L_%=:\n\t"
      "s_add_u32 s51, s51, 1\n\t"
      "s_cmp_lt_u32 s51, 262144\n\t"
      "s_cbranch_scc0 FQ14X_%=\n\t"
      "s_waitcnt vmcnt(0)\n\t"
      "s_sleep 4\n\t"
      "global_load_dwordx4 v[128:131], v224, s[56:57] sc1\n\t"
      "global_load_dwordx4 v[132:135], v225, s[56:57] sc1\n\t"
      "global_load_dwordx4 v[136:139], v226, s[56:57] sc1\n\t"
      "global_load_dwordx4 v[140:143], v227, s[56:57] sc1\n\t"
      "global_load_dwordx4 v[144:147], v228, s[56:57] sc1\n\t"
      "global_load_dwordx4 v[148:151], v229, s[56:57] sc1\n\t"
      "s_waitcnt vmcnt(5)\n\t"
      "v_max3_u32 v234, v128, v129, v130\n\t"
      "v_max_u32_e32 v234, v234, v131\n\t"
      "v_cmp_eq_u32_e32 vcc, -1, v234\n\t"
      "s_cbranch_vccnz FQ14L_%=\n\t"
      "FQ14X_%=:\n\t"
      "s_branch FQ14B_%=\n\t"
      "NQ1R_%=:\n\t"
      "s_mov_b32 s51, 0\n\t"
      "NQ1L_%=:\n\t"
      "s_add_u32 s51, s51, 1\n\t"
      "s_cmp_lt_u32 s51, 262144\n\t"
      "s_cbranch_scc0 NQ1X_%=\n\t"
      "s_waitcnt vmcnt(0)\n\t"
      "s_sleep 1\n\t"
      "global_load_dwordx4 v[128:131], v224, s[54:55] sc1\n\t"
      "global_load_dwordx4 v[132:135], v225, s[54:55] sc1\n\t"
      "global_load_dwordx4 v[136:139], v226, s[54:55] sc1\n\t"
      "global_load_dwordx4 v[140:143], v227, s[54:55] sc1\n\t"
      "global_load_dwordx4 v[144:147], v228, s[54:55] sc1\n\t"
      "global_load_dwordx4 v[148:151], v229, s[54:55] sc1\n\t"
      "global_load_dwordx4 v[152:155], v230, s[54:55] sc1\n\t"
      "s_waitcnt vmcnt(0)\n\t"
      "v_max3_u32 v234, v128, v129, v130\n\t"
      "v_max3_u32 v234, v234, v131, v132\n\t"
      "v_max3_u32 v234, v234, v133, v134\n\t"
      "v_max3_u32 v234, v234, v135, v136\n\t"
      "v_max3_u32 v234, v234, v137, v138\n\t"
      "v_max3_u32 v234, v234, v139, v140\n\t"
      "v_max3_u32 v234, v234, v141, v142\n\t"
      "v_max3_u32 v234, v234, v143, v144\n\t"
      "v_max3_u32 v234, v234, v145, v146\n\t"
      "v_max3_u32 v234, v234, v147, v148\n\t"
      "v_max3_u32 v234, v234, v149, v150\n\t"
      "v_max3_u32 v234, v234, v151, v152\n\t"
      "v_max3_u32 v234, v234, v153, v154\n\t"
      "v_max_u32_e32 v234, v234, v155\n\t"
      "v_cmp_eq_u32_e32 vcc, -1, v234\n\t"
      "s_cbranch_vccnz NQ1L_%=\n\t"
      "NQ1X_%=:\n\t"
      "v_mfma_f32_16x16x32_bf16 v[200:203], v[128:131], v[0:3], 0\n\t"
      "v_mfma_f32_16x16x32_bf16 v[204:207], v[128:131], v[4:7], 0\n\t"
      "v_mfma_f32_16x16x32_bf16 v[208:211], v[128:131], v[8:11], 0\n\t"
      "v_mfma_f32_16x16x32_bf16 v[212:215], v[128:131], v[12:15], 0\n\t"
      "v_mfma_f32_16x16x32_bf16 v[200:203], v[132:135], v[16:19], v[200:203]\n\t"
      "v_mfma_f32_16x16x32_bf16 v[204:207], v[132:135], v[20:23], v[204:207]\n\t"
      "v_mfma_f32_16x16x32_bf16 v[208:211], v[132:135], v[24:27], v[208:211]\n\t"
      "v_mfma_f32_16x16x32_bf16 v[212:215], v[132:135], v[28:31], v[212:215]\n\t"
      "v_mfma_f32_16x16x32_bf16 v[200:203], v[136:139], v[32:35], v[200:203]\n\t"
      "v_mfma_f32_16x16x32_bf16 v[204:207], v[136:139], v[36:39], v[204:207]\n\t"
      "v_mfma_f32_16x16x32_bf16 v[208:211], v[136:139], v[40:43], v[208:211]\n\t"
      "v_mfma_f32_16x16x32_bf16 v[212:215], v[136:139], v[44:47], v[212:215]\n\t"
      "v_mfma_f32_16x16x32_bf16 v[200:203], v[140:143], v[48:51], v[200:203]\n\t"
      "v_mfma_f32_16x16x32_bf16 v[204:207], v[140:143], v[52:55], v[204:207]\n\t"
      "v_mfma_f32_16x16x32_bf16 v[208:211], v[140:143], v[56:59], v[208:211]\n\t"
      "v_mfma_f32_16x16x32_bf16 v[212:215], v[140:143], v[60:63], v[212:215]\n\t"
      "v_mfma_f32_16x16x32_bf16 v[200:203], v[144:147], v[64:67], v[200:203]\n\t"
      "v_mfma_f32_16x16x32_bf16 v[204:207], v[144:147], v[68:71], v[204:207]\n\t"
      "v_mfma_f32_16x16x32_bf16 v[208:211], v[144:147], v[72:75], v[208:211]\n\t"
      "v_mfma_f32_16x16x32_bf16 v[212:215], v[144:147], v[76:79], v[212:215]\n\t"
      "v_mfma_f32_16x16x32_bf16 v[200:203], v[148:151], v[80:83], v[200:203]\n\t"
      "v_mfma_f32_16x16x32_bf16 v[204:207], v[148:151], v[84:87], v[204:207]\n\t"
      "v_mfma_f32_16x16x32_bf16 v[208:211], v[148:151], v[88:91], v[208:211]\n\t"
      "v_mfma_f32_16x16x32_bf16 v[212:215], v[148:151], v[92:95], v[212:215]\n\t"
      "v_mfma_f32_16x16x32_bf16 v[200:203], v[152:155], v[96:99], v[200:203]\n\t"
      "v_mfma_f32_16x16x32_bf16 v[204:207], v[152:155], v[100:103], v[204:207]\n\t"
      "v_mfma_f32_16x16x32_bf16 v[208:211], v[152:155], v[104:107], v[208:211]\n\t"
      "v_mfma_f32_16x16x32_bf16 v[212:215], v[152:155], v[108:111], v[212:215]\n\t"
      "global_load_dwordx4 v[128:131], v224, s[56:57] sc1\n\t"
      "global_load_dwordx4 v[132:135], v225, s[56:57] sc1\n\t"
      "global_load_dwordx4 v[136:139], v226, s[56:57] sc1\n\t"
      "global_load_dwordx4 v[140:143], v227, s[56:57] sc1\n\t"
      "global_load_dwordx4 v[144:147], v228, s[56:57] sc1\n\t"
      "global_load_dwordx4 v[148:151], v229, s[56:57] sc1\n\t"
      "global_load_dwordx4 v[152:155], v230, s[56:57] sc1\n\t"
      "s_waitcnt vmcnt(0)\n\t"
      "s_nop 7\n\t"
      "s_branch NQ1B_%=\n\t"
      "FQ15R_%=:\n\t"
      "s_mov_b32 s51, 0\n\t"
      "FQ15L_%=:\n\t"
      "s_add_u32 s51, s51, 1\n\t"
      "s_cmp_lt_u32 s51, 262144\n\t"
      "s_cbranch_scc0 FQ15X_%=\n\t"
      "s_waitcnt vmcnt(0)\n\t"
      "s_sleep 4\n\t"
      "global_load_dwordx4 v[132:135], v225, s[56:57] sc1\n\t"
      "global_load_dwordx4 v[136:139], v226, s[56:57] sc1\n\t"
      "global_load_dwordx4 v[140:143], v227, s[56:57] sc1\n\t"
      "global_load_dwordx4 v[144:147], v228, s[56:57] sc1\n\t"
      "global_load_dwordx4 v[148:151], v229, s[56:57] sc1\n\t"
      "global_load_dwordx4 v[152:155], v230, s[56:57] sc1\n\t"
      "s_waitcnt vmcnt(5)\n\t"
      "v_max3_u32 v234, v132, v133, v134\n\t"
      "v_max_u32_e32 v234, v234, v135\n\t"
      "v_cmp_eq_u32_e32 vcc, -1, v234\n\t"
      "s_cbranch_vccnz FQ15L_%=\n\t"
      "FQ15X_%=:\n\t"
      "s_branch FQ15B_%=\n\t"
      "FQ16R_%=:\n\t"
      "s_mov_b32 s51, 0\n\t"
      "FQ16L_%=:\n\t"
      "s_add_u32 s51, s51, 1\n\t"
      "s_cmp_lt_u32 s51, 262144\n\t"
      "s_cbranch_scc0 FQ16X_%=\n\t"
      "s_waitcnt vmcnt(0)\n\t"
      "s_sleep 4\n\t"
      "global_load_dwordx4 v[136:139], v226, s[56:57] sc1\n\t"
      "global_load_dwordx4 v[140:143], v227, s[56:57] sc1\n\t"
      "global_load_dwordx4 v[144:147], v228, s[56:57] sc1\n\t"
      "global_load_dwordx4 v[148:151], v229, s[56:57] sc1\n\t"
      "global_load_dwordx4 v[152:155], v230, s[56:57] sc1\n\t"
      "global_load_dwordx4 v[128:131], v224, s[58:59] sc1\n\t"
      "s_waitcnt vmcnt(5)\n\t"
      "v_max3_u32 v234, v136, v137, v138\n\t"
      "v_max_u32_e32 v234, v234, v139\n\t"
      "v_cmp_eq_u32_e32 vcc, -1, v234\n\t"
      "s_cbranch_vccnz FQ16L_%=\n\t"
      "FQ16X_%=:\n\t"
      "s_branch FQ16B_%=\n\t"
      "FQ17R_%=:\n\t"
      "s_mov_b32 s51, 0\n\t"
      "FQ17L_%=:\n\t"
      "s_add_u32 s51, s51, 1\n\t"
      "s_cmp_lt_u32 s51, 262144\n\t"
      "s_cbranch_scc0 FQ17X_%=\n\t"
      "s_waitcnt vmcnt(0)\n\t"
      "s_sleep 4\n\t"
      "global_load_dwordx4 v[140:143], v227, s[56:57] sc1\n\t"
      "global_load_dwordx4 v[144:147], v228, s[56:57] sc1\n\t"
      "global_load_dwordx4 v[148:151], v229, s[56:57] sc1\n\t"
      "global_load_dwordx4 v[152:155], v230, s[56:57] sc1\n\t"
      "global_load_dwordx4 v[128:131], v224, s[58:59] sc1\n\t"
      "global_load_dwordx4 v[132:135], v225, s[58:59] sc1\n\t"
      "s_waitcnt vmcnt(5)\n\t"
      "v_max3_u32 v234, v140, v141, v142\n\t"
      "v_max_u32_e32 v234, v234, v143\n\t"
      "v_cmp_eq_u32_e32 vcc, -1, v234\n\t"
      "s_cbranch_vccnz FQ17L_%=\n\t"
      "FQ17X_%=:\n\t"
      "s_branch FQ17B_%=\n\t"
      "FQ18R_%=:\n\t"
      "s_mov_b32 s51, 0\n\t"
      "FQ18L_%=:\n\t"
      "s_add_u32 s51, s51, 1\n\t"
      "s_cmp_lt_u32 s51, 262144\n\t"
      "s_cbranch_scc0 FQ18X_%=\n\t"
      "s_waitcnt vmcnt(0)\n\t"
      "s_sleep 4\n\t"
      "global_load_dwordx4 v[144:147], v228, s[56:57] sc1\n\t"
      "global_load_dwordx4 v[148:151], v229, s[56:57] sc1\n\t"
      "global_load_dwordx4 v[152:155], v230, s[56:57] sc1\n\t"
      "global_load_dwordx4 v[128:131], v224, s[58:59] sc1\n\t"
      "global_load_dwordx4 v[132:135], v225, s[58:59] sc1\n\t"
      "global_load_dwordx4 v[136:139], v226, s[58:59] sc1\n\t"
      "s_waitcnt vmcnt(5)\n\t"
      "v_max3_u32 v234, v144, v145, v146\n\t"
      "v_max_u32_e32 v234, v234, v147\n\t"
      "v_cmp_eq_u32_e32 vcc, -1, v234\n\t"
      "s_cbranch_vccnz FQ18L_%=\n\t"
      "FQ18X_%=:\n\t"
      "s_branch FQ18B_%=\n\t"
      "FQ19R_%=:\n\t"
      "s_mov_b32 s51, 0\n\t"
      "FQ19L_%=:\n\t"
      "s_add_u32 s51, s51, 1\n\t"
      "s_cmp_lt_u32 s51, 262144\n\t"
      "s_cbranch_scc0 FQ19X_%=\n\t"
      "s_waitcnt vmcnt(0)\n\t"
      "s_sleep 4\n\t"
      "global_load_dwordx4 v[148:151], v229, s[56:57] sc1\n\t"
      "global_load_dwordx4 v[152:155], v230, s[56:57] sc1\n\t"
      "global_load_dwordx4 v[128:131], v224, s[58:59] sc1\n\t"
      "global_load_dwordx4 v[132:135], v225, s[58:59] sc1\n\t"
      "global_load_dwordx4 v[136:139], v226, s[58:59] sc1\n\t"
      "global_load_dwordx4 v[140:143], v227, s[58:59] sc1\n\t"
      "s_waitcnt vmcnt(5)\n\t"
      "v_max3_u32 v234, v148, v149, v150\n\t"
      "v_max_u32_e32 v234, v234, v151\n\t"
      "v_cmp_eq_u32_e32 vcc, -1, v234\n\t"
      "s_cbranch_vccnz FQ19L_%=\n\t"
      "FQ19X_%=:\n\t"
      "s_branch FQ19B_%=\n\t"
      "FQ20R_%=:\n\t"
      "s_mov_b32 s51, 0\n\t"
      "FQ20L_%=:\n\t"
      "s_add_u32 s51, s51, 1\n\t"
      "s_cmp_lt_u32 s51, 262144\n\t"
      "s_cbranch_scc0 FQ20X_%=\n\t"
      "s_waitcnt vmcnt(0)\n\t"
      "s_sleep 4\n\t"
      "global_load_dwordx4 v[152:155], v230, s[56:57] sc1\n\t"
      "global_load_dwordx4 v[128:131], v224, s[58:59] sc1\n\t"
      "global_load_dwordx4 v[132:135], v225, s[58:59] sc1\n\t"
      "global_load_dwordx4 v[136:139], v226, s[58:59] sc1\n\t"
      "global_load_dwordx4 v[140:143], v227, s[58:59] sc1\n\t"
      "global_load_dwordx4 v[144:147], v228, s[58:59] sc1\n\t"
      "s_waitcnt vmcnt(5)\n\t"
      "v_max3_u32 v234, v152, v153, v154\n\t"
      "v_max_u32_e32 v234, v234, v155\n\t"
      "v_cmp_eq_u32_e32 vcc, -1, v234\n\t"
      "s_cbranch_vccnz FQ20L_%=\n\t"
      "FQ20X_%=:\n\t"
      "s_branch FQ20B_%=\n\t"
      "FQ21R_%=:\n\t"
      "s_mov_b32 s51, 0\n\t"
      "FQ21L_%=:\n\t"
      "s_add_u32 s51, s51, 1\n\t"
      "s_cmp_lt_u32 s51, 262144\n\t"
      "s_cbranch_scc0 FQ21X_%=\n\t"
      "s_waitcnt vmcnt(0)\n\t"
      "s_sleep 4\n\t"
      "global_load_dwordx4 v[128:131], v224, s[58:59] sc1\n\t"
      "global_load_dwordx4 v[132:135], v225, s[58:59] sc1\n\t"
      "global_load_dwordx4 v[136:139], v226, s[58:59] sc1\n\t"
      "global_load_dwordx4 v[140:143], v227, s[58:59] sc1\n\t"
      "global_load_dwordx4 v[144:147], v228, s[58:59] sc1\n\t"
      "global_load_dwordx4 v[148:151], v229, s[58:59] sc1\n\t"
      "s_waitcnt vmcnt(5)\n\t"
      "v_max3_u32 v234, v128, v129, v130\n\t"
      "v_max_u32_e32 v234, v234, v131\n\t"
      "v_cmp_eq_u32_e32 vcc, -1, v234\n\t"
      "s_cbranch_vccnz FQ21L_%=\n\t"
      "FQ21X_%=:\n\t"
      "s_branch FQ21B_%=\n\t"
      "NQ2R_%=:\n\t"
      "s_mov_b32 s51, 0\n\t"
      "NQ2L_%=:\n\t"
      "s_add_u32 s51, s51, 1\n\t"
      "s_cmp_lt_u32 s51, 262144\n\t"
      "s_cbranch_scc0 NQ2X_%=\n\t"
      "s_waitcnt vmcnt(0)\n\t"
      "s_sleep 1\n\t"
      "global_load_dwordx4 v[128:131], v224, s[56:57] sc1\n\t"
      "global_load_dwordx4 v[132:135], v225, s[56:57] sc1\n\t"
      "global_load_dwordx4 v[136:139], v226, s[56:57] sc1\n\t"
      "global_load_dwordx4 v[140:143], v227, s[56:57] sc1\n\t"
      "global_load_dwordx4 v[144:147], v228, s[56:57] sc1\n\t"
      "global_load_dwordx4 v[148:151], v229, s[56:57] sc1\n\t"
      "global_load_dwordx4 v[152:155], v230, s[56:57] sc1\n\t"
      "s_waitcnt vmcnt(0)\n\t"
      "v_max3_u32 v234, v128, v129, v130\n\t"
      "v_max3_u32 v234, v234, v131, v132\n\t"
      "v_max3_u32 v234, v234, v133, v134\n\t"
      "v_max3_u32 v234, v234, v135, v136\n\t"
      "v_max3_u32 v234, v234, v137, v138\n\t"
      "v_max3_u32 v234, v234, v139, v140\n\t"
      "v_max3_u32 v234, v234, v141, v142\n\t"
      "v_max3_u32 v234, v234, v143, v144\n\t"
      "v_max3_u32 v234, v234, v145, v146\n\t"
      "v_max3_u32 v234, v234, v147, v148\n\t"
      "v_max3_u32 v234, v234, v149, v150\n\t"
      "v_max3_u32 v234, v234, v151, v152\n\t"
      "v_max3_u32 v234, v234, v153, v154\n\t"
      "v_max_u32_e32 v234, v234, v155\n\t"
      "v_cmp_eq_u32_e32 vcc, -1, v234\n\t"
      "s_cbranch_vccnz NQ2L_%=\n\t"
      "NQ2X_%=:\n\t"
      "v_mfma_f32_16x16x32_bf16 v[184:187], v[128:131], v[0:3], 0\n\t"
      "v_mfma_f32_16x16x32_bf16 v[188:191], v[128:131], v[4:7], 0\n\t"
      "v_mfma_f32_16x16x32_bf16 v[192:195], v[128:131], v[8:11], 0\n\t"
      "v_mfma_f32_16x16x32_bf16 v[196:199], v[128:131], v[12:15], 0\n\t"
      "v_mfma_f32_16x16x32_bf16 v[184:187], v[132:135], v[16:19], v[184:187]\n\t"
      "v_mfma_f32_16x16x32_bf16 v[188:191], v[132:135], v[20:23], v[188:191]\n\t"
      "v_mfma_f32_16x16x32_bf16 v[192:195], v[132:135], v[24:27], v[192:195]\n\t"
      "v_mfma_f32_16x16x32_bf16 v[196:199], v[132:135], v[28:31], v[196:199]\n\t"
      "v_mfma_f32_16x16x32_bf16 v[184:187], v[136:139], v[32:35], v[184:187]\n\t"
      "v_mfma_f32_16x16x32_bf16 v[188:191], v[136:139], v[36:39], v[188:191]\n\t"
      "v_mfma_f32_16x16x32_bf16 v[192:195], v[136:139], v[40:43], v[192:195]\n\t"
      "v_mfma_f32_16x16x32_bf16 v[196:199], v[136:139], v[44:47], v[196:199]\n\t"
      "v_mfma_f32_16x16x32_bf16 v[184:187], v[140:143], v[48:51], v[184:187]\n\t"
      "v_mfma_f32_16x16x32_bf16 v[188:191], v[140:143], v[52:55], v[188:191]\n\t"
      "v_mfma_f32_16x16x32_bf16 v[192:195], v[140:143], v[56:59], v[192:195]\n\t"
      "v_mfma_f32_16x16x32_bf16 v[196:199], v[140:143], v[60:63], v[196:199]\n\t"
      "v_mfma_f32_16x16x32_bf16 v[184:187], v[144:147], v[64:67], v[184:187]\n\t"
      "v_mfma_f32_16x16x32_bf16 v[188:191], v[144:147], v[68:71], v[188:191]\n\t"
      "v_mfma_f32_16x16x32_bf16 v[192:195], v[144:147], v[72:75], v[192:195]\n\t"
      "v_mfma_f32_16x16x32_bf16 v[196:199], v[144:147], v[76:79], v[196:199]\n\t"
      "v_mfma_f32_16x16x32_bf16 v[184:187], v[148:151], v[80:83], v[184:187]\n\t"
      "v_mfma_f32_16x16x32_bf16 v[188:191], v[148:151], v[84:87], v[188:191]\n\t"
      "v_mfma_f32_16x16x32_bf16 v[192:195], v[148:151], v[88:91], v[192:195]\n\t"
      "v_mfma_f32_16x16x32_bf16 v[196:199], v[148:151], v[92:95], v[196:199]\n\t"
      "v_mfma_f32_16x16x32_bf16 v[184:187], v[152:155], v[96:99], v[184:187]\n\t"
      "v_mfma_f32_16x16x32_bf16 v[188:191], v[152:155], v[100:103], v[188:191]\n\t"
      "v_mfma_f32_16x16x32_bf16 v[192:195], v[152:155], v[104:107], v[192:195]\n\t"
      "v_mfma_f32_16x16x32_bf16 v[196:199], v[152:155], v[108:111], v[196:199]\n\t"
      "global_load_dwordx4 v[128:131], v224, s[58:59] sc1\n\t"
      "global_load_dwordx4 v[132:135], v225, s[58:59] sc1\n\t"
      "global_load_dwordx4 v[136:139], v226, s[58:59] sc1\n\t"
      "global_load_dwordx4 v[140:143], v227, s[58:59] sc1\n\t"
      "global_load_dwordx4 v[144:147], v228, s[58:59] sc1\n\t"
      "global_load_dwordx4 v[148:151], v229, s[58:59] sc1\n\t"
      "global_load_dwordx4 v[152:155], v230, s[58:59] sc1\n\t"
      "s_waitcnt vmcnt(0)\n\t"
      "s_nop 7\n\t"
      "s_branch NQ2B_%=\n\t"
      "FQ22R_%=:\n\t"
      "s_mov_b32 s51, 0\n\t"
      "FQ22L_%=:\n\t"
      "s_add_u32 s51, s51, 1\n\t"
      "s_cmp_lt_u32 s51, 262144\n\t"
      "s_cbranch_scc0 FQ22X_%=\n\t"
      "s_waitcnt vmcnt(0)\n\t"
      "s_sleep 4\n\t"
      "global_load_dwordx4 v[132:135], v225, s[58:59] sc1\n\t"
      "global_load_dwordx4 v[136:139], v226, s[58:59] sc1\n\t"
      "global_load_dwordx4 v[140:143], v227, s[58:59] sc1\n\t"
      "global_load_dwordx4 v[144:147], v228, s[58:59] sc1\n\t"
      "global_load_dwordx4 v[148:151], v229, s[58:59] sc1\n\t"
      "global_load_dwordx4 v[152:155], v230, s[58:59] sc1\n\t"
      "s_waitcnt vmcnt(5)\n\t"
      "v_max3_u32 v234, v132, v133, v134\n\t"
      "v_max_u32_e32 v234, v234, v135\n\t"
      "v_cmp_eq_u32_e32 vcc, -1, v234\n\t"
      "s_cbranch_vccnz FQ22L_%=\n\t"
      "FQ22X_%=:\n\t"
      "s_branch FQ22B_%=\n\t"
      "FQ23R_%=:\n\t"
      "s_mov_b32 s51, 0\n\t"
      "FQ23L_%=:\n\t"
      "s_add_u32 s51, s51, 1\n\t"
      "s_cmp_lt_u32 s51, 262144\n\t"
      "s_cbranch_scc0 FQ23X_%=\n\t"
      "s_waitcnt vmcnt(0)\n\t"
      "s_sleep 4\n\t"
      "global_load_dwordx4 v[136:139], v226, s[58:59] sc1\n\t"
      "global_load_dwordx4 v[140:143], v227, s[58:59] sc1\n\t"
      "global_load_dwordx4 v[144:147], v228, s[58:59] sc1\n\t"
      "global_load_dwordx4 v[148:151], v229, s[58:59] sc1\n\t"
      "global_load_dwordx4 v[152:155], v230, s[58:59] sc1\n\t"
      "global_load_dwordx4 v[128:131], v224, s[60:61] sc1\n\t"
      "s_waitcnt vmcnt(5)\n\t"
      "v_max3_u32 v234, v136, v137, v138\n\t"
      "v_max_u32_e32 v234, v234, v139\n\t"
      "v_cmp_eq_u32_e32 vcc, -1, v234\n\t"
      "s_cbranch_vccnz FQ23L_%=\n\t"
      "FQ23X_%=:\n\t"
      "s_branch FQ23B_%=\n\t"
      "FQ24R_%=:\n\t"
      "s_mov_b32 s51, 0\n\t"
      "FQ24L_%=:\n\t"
      "s_add_u32 s51, s51, 1\n\t"
      "s_cmp_lt_u32 s51, 262144\n\t"
      "s_cbranch_scc0 FQ24X_%=\n\t"
      "s_waitcnt vmcnt(0)\n\t"
      "s_sleep 4\n\t"
      "global_load_dwordx4 v[140:143], v227, s[58:59] sc1\n\t"
      "global_load_dwordx4 v[144:147], v228, s[58:59] sc1\n\t"
      "global_load_dwordx4 v[148:151], v229, s[58:59] sc1\n\t"
      "global_load_dwordx4 v[152:155], v230, s[58:59] sc1\n\t"
      "global_load_dwordx4 v[128:131], v224, s[60:61] sc1\n\t"
      "global_load_dwordx4 v[132:135], v225, s[60:61] sc1\n\t"
      "s_waitcnt vmcnt(5)\n\t"
      "v_max3_u32 v234, v140, v141, v142\n\t"
      "v_max_u32_e32 v234, v234, v143\n\t"
      "v_cmp_eq_u32_e32 vcc, -1, v234\n\t"
      "s_cbranch_vccnz FQ24L_%=\n\t"
      "FQ24X_%=:\n\t"
      "s_branch FQ24B_%=\n\t"
      "FQ25R_%=:\n\t"
      "s_mov_b32 s51, 0\n\t"
      "FQ25L_%=:\n\t"
      "s_add_u32 s51, s51, 1\n\t"
      "s_cmp_lt_u32 s51, 262144\n\t"
      "s_cbranch_scc0 FQ25X_%=\n\t"
      "s_waitcnt vmcnt(0)\n\t"
      "s_sleep 4\n\t"
      "global_load_dwordx4 v[144:147], v228, s[58:59] sc1\n\t"
      "global_load_dwordx4 v[148:151], v229, s[58:59] sc1\n\t"
      "global_load_dwordx4 v[152:155], v230, s[58:59] sc1\n\t"
      "global_load_dwordx4 v[128:131], v224, s[60:61] sc1\n\t"
      "global_load_dwordx4 v[132:135], v225, s[60:61] sc1\n\t"
      "global_load_dwordx4 v[136:139], v226, s[60:61] sc1\n\t"
      "s_waitcnt vmcnt(5)\n\t"
      "v_max3_u32 v234, v144, v145, v146\n\t"
      "v_max_u32_e32 v234, v234, v147\n\t"
      "v_cmp_eq_u32_e32 vcc, -1, v234\n\t"
      "s_cbranch_vccnz FQ25L_%=\n\t"
      "FQ25X_%=:\n\t"
      "s_branch FQ25B_%=\n\t"
      "FQ26R_%=:\n\t"
      "s_mov_b32 s51, 0\n\t"
      "FQ26L_%=:\n\t"
      "s_add_u32 s51, s51, 1\n\t"
      "s_cmp_lt_u32 s51, 262144\n\t"
      "s_cbranch_scc0 FQ26X_%=\n\t"
      "s_waitcnt vmcnt(0)\n\t"
      "s_sleep 4\n\t"
      "global_load_dwordx4 v[148:151], v229, s[58:59] sc1\n\t"
      "global_load_dwordx4 v[152:155], v230, s[58:59] sc1\n\t"
      "global_load_dwordx4 v[128:131], v224, s[60:61] sc1\n\t"
      "global_load_dwordx4 v[132:135], v225, s[60:61] sc1\n\t"
      "global_load_dwordx4 v[136:139], v226, s[60:61] sc1\n\t"
      "global_load_dwordx4 v[140:143], v227, s[60:61] sc1\n\t"
      "s_waitcnt vmcnt(5)\n\t"
      "v_max3_u32 v234, v148, v149, v150\n\t"
      "v_max_u32_e32 v234, v234, v151\n\t"
      "v_cmp_eq_u32_e32 vcc, -1, v234\n\t"
      "s_cbranch_vccnz FQ26L_%=\n\t"
      "FQ26X_%=:\n\t"
      "s_branch FQ26B_%=\n\t"
      "FQ27R_%=:\n\t"
      "s_mov_b32 s51, 0\n\t"
      "FQ27L_%=:\n\t"
      "s_add_u32 s51, s51, 1\n\t"
      "s_cmp_lt_u32 s51, 262144\n\t"
      "s_cbranch_scc0 FQ27X_%=\n\t"
      "s_waitcnt vmcnt(0)\n\t"
      "s_sleep 4\n\t"
      "global_load_dwordx4 v[152:155], v230, s[58:59] sc1\n\t"
      "global_load_dwordx4 v[128:131], v224, s[60:61] sc1\n\t"
      "global_load_dwordx4 v[132:135], v225, s[60:61] sc1\n\t"
      "global_load_dwordx4 v[136:139], v226, s[60:61] sc1\n\t"
      "global_load_dwordx4 v[140:143], v227, s[60:61] sc1\n\t"
      "global_load_dwordx4 v[144:147], v228, s[60:61] sc1\n\t"
      "s_waitcnt vmcnt(5)\n\t"
      "v_max3_u32 v234, v152, v153, v154\n\t"
      "v_max_u32_e32 v234, v234, v155\n\t"
      "v_cmp_eq_u32_e32 vcc, -1, v234\n\t"
      "s_cbranch_vccnz FQ27L_%=\n\t"
      "FQ27X_%=:\n\t"
      "s_branch FQ27B_%=\n\t"
      "FQ28R_%=:\n\t"
      "s_mov_b32 s51, 0\n\t"
      "FQ28L_%=:\n\t"
      "s_add_u32 s51, s51, 1\n\t"
      "s_cmp_lt_u32 s51, 262144\n\t"
      "s_cbranch_scc0 FQ28X_%=\n\t"
      "s_waitcnt vmcnt(0)\n\t"
      "s_sleep 4\n\t"
      "global_load_dwordx4 v[128:131], v224, s[60:61] sc1\n\t"
      "global_load_dwordx4 v[132:135], v225, s[60:61] sc1\n\t"
      "global_load_dwordx4 v[136:139], v226, s[60:61] sc1\n\t"
      "global_load_dwordx4 v[140:143], v227, s[60:61] sc1\n\t"
      "global_load_dwordx4 v[144:147], v228, s[60:61] sc1\n\t"
      "global_load_dwordx4 v[148:151], v229, s[60:61] sc1\n\t"
      "s_waitcnt vmcnt(5)\n\t"
      "v_max3_u32 v234, v128, v129, v130\n\t"
      "v_max_u32_e32 v234, v234, v131\n\t"
      "v_cmp_eq_u32_e32 vcc, -1, v234\n\t"
      "s_cbranch_vccnz FQ28L_%=\n\t"
      "FQ28X_%=:\n\t"
      "s_branch FQ28B_%=\n\t"
      "NQ3R_%=:\n\t"
      "s_mov_b32 s51, 0\n\t"
      "NQ3L_%=:\n\t"
      "s_add_u32 s51, s51, 1\n\t"
      "s_cmp_lt_u32 s51, 262144\n\t"
      "s_cbranch_scc0 NQ3X_%=\n\t"
      "s_waitcnt vmcnt(0)\n\t"
      "s_sleep 1\n\t"
      "global_load_dwordx4 v[128:131], v224, s[58:59] sc1\n\t"
      "global_load_dwordx4 v[132:135], v225, s[58:59] sc1\n\t"
      "global_load_dwordx4 v[136:139], v226, s[58:59] sc1\n\t"
      "global_load_dwordx4 v[140:143], v227, s[58:59] sc1\n\t"
      "global_load_dwordx4 v[144:147], v228, s[58:59] sc1\n\t"
      "global_load_dwordx4 v[148:151], v229, s[58:59] sc1\n\t"
      "global_load_dwordx4 v[152:155], v230, s[58:59] sc1\n\t"
      "s_waitcnt vmcnt(0)\n\t"
      "v_max3_u32 v234, v128, v129, v130\n\t"
      "v_max3_u32 v234, v234, v131, v132\n\t"
      "v_max3_u32 v234, v234, v133, v134\n\t"
      "v_max3_u32 v234, v234, v135, v136\n\t"
      "v_max3_u32 v234, v234, v137, v138\n\t"
      "v_max3_u32 v234, v234, v139, v140\n\t"
      "v_max3_u32 v234, v234, v141, v142\n\t"
      "v_max3_u32 v234, v234, v143, v144\n\t"
      "v_max3_u32 v234, v234, v145, v146\n\t"
      "v_max3_u32 v234, v234, v147, v148\n\t"
      "v_max3_u32 v234, v234, v149, v150\n\t"
      "v_max3_u32 v234, v234, v151, v152\n\t"
      "v_max3_u32 v234, v234, v153, v154\n\t"
      "v_max_u32_e32 v234, v234, v155\n\t"
      "v_cmp_eq_u32_e32 vcc, -1, v234\n\t"
      "s_cbranch_vccnz NQ3L_%=\n\t"
      "NQ3X_%=:\n\t"
      "v_mfma_f32_16x16x32_bf16 v[200:203], v[128:131], v[0:3], 0\n\t"
      "v_mfma_f32_16x16x32_bf16 v[204:207], v[128:131], v[4:7], 0\n\t"
      "v_mfma_f32_16x16x32_bf16 v[208:211], v[128:131], v[8:11], 0\n\t"
      "v_mfma_f32_16x16x32_bf16 v[212:215], v[128:131], v[12:15], 0\n\t"
      "v_mfma_f32_16x16x32_bf16 v[200:203], v[132:135], v[16:19], v[200:203]\n\t"
      "v_mfma_f32_16x16x32_bf16 v[204:207], v[132:135], v[20:23], v[204:207]\n\t"
      "v_mfma_f32_16x16x32_bf16 v[208:211], v[132:135], v[24:27], v[208:211]\n\t"
      "v_mfma_f32_16x16x32_bf16 v[212:215], v[132:135], v[28:31], v[212:215]\n\t"
      "v_mfma_f32_16x16x32_bf16 v[200:203], v[136:139], v[32:35], v[200:203]\n\t"
      "v_mfma_f32_16x16x32_bf16 v[204:207], v[136:139], v[36:39], v[204:207]\n\t"
      "v_mfma_f32_16x16x32_bf16 v[208:211], v[136:139], v[40:43], v[208:211]\n\t"
      "v_mfma_f32_16x16x32_bf16 v[212:215], v[136:139], v[44:47], v[212:215]\n\t"
      "v_mfma_f32_16x16x32_bf16 v[200:203], v[140:143], v[48:51], v[200:203]\n\t"
      "v_mfma_f32_16x16x32_bf16 v[204:207], v[140:143], v[52:55], v[204:207]\n\t"
      "v_mfma_f32_16x16x32_bf16 v[208:211], v[140:143], v[56:59], v[208:211]\n\t"
      "v_mfma_f32_16x16x32_bf16 v[212:215], v[140:143], v[60:63], v[212:215]\n\t"
      "v_mfma_f32_16x16x32_bf16 v[200:203], v[144:147], v[64:67], v[200:203]\n\t"
      "v_mfma_f32_16x16x32_bf16 v[204:207], v[144:147], v[68:71], v[204:207]\n\t"
      "v_mfma_f32_16x16x32_bf16 v[208:211], v[144:147], v[72:75], v[208:211]\n\t"
      "v_mfma_f32_16x16x32_bf16 v[212:215], v[144:147], v[76:79], v[212:215]\n\t"
      "v_mfma_f32_16x16x32_bf16 v[200:203], v[148:151], v[80:83], v[200:203]\n\t"
      "v_mfma_f32_16x16x32_bf16 v[204:207], v[148:151], v[84:87], v[204:207]\n\t"
      "v_mfma_f32_16x16x32_bf16 v[208:211], v[148:151], v[88:91], v[208:211]\n\t"
      "v_mfma_f32_16x16x32_bf16 v[212:215], v[148:151], v[92:95], v[212:215]\n\t"
      "v_mfma_f32_16x16x32_bf16 v[200:203], v[152:155], v[96:99], v[200:203]\n\t"
      "v_mfma_f32_16x16x32_bf16 v[204:207], v[152:155], v[100:103], v[204:207]\n\t"
      "v_mfma_f32_16x16x32_bf16 v[208:211], v[152:155], v[104:107], v[208:211]\n\t"
      "v_mfma_f32_16x16x32_bf16 v[212:215], v[152:155], v[108:111], v[212:215]\n\t"
      "global_load_dwordx4 v[128:131], v224, s[60:61] sc1\n\t"
      "global_load_dwordx4 v[132:135], v225, s[60:61] sc1\n\t"
      "global_load_dwordx4 v[136:139], v226, s[60:61] sc1\n\t"
      "global_load_dwordx4 v[140:143], v227, s[60:61] sc1\n\t"
      "global_load_dwordx4 v[144:147], v228, s[60:61] sc1\n\t"
      "global_load_dwordx4 v[148:151], v229, s[60:61] sc1\n\t"
      "global_load_dwordx4 v[152:155], v230, s[60:61] sc1\n\t"
      "s_waitcnt vmcnt(0)\n\t"
      "s_nop 7\n\t"
      "s_branch NQ3B_%=\n\t"
      "FQ29R_%=:\n\t"
      "s_mov_b32 s51, 0\n\t"
      "FQ29L_%=:\n\t"
      "s_add_u32 s51, s51, 1\n\t"
      "s_cmp_lt_u32 s51, 262144\n\t"
      "s_cbranch_scc0 FQ29X_%=\n\t"
      "s_waitcnt vmcnt(0)\n\t"
      "s_sleep 4\n\t"
      "global_load_dwordx4 v[132:135], v225, s[60:61] sc1\n\t"
      "global_load_dwordx4 v[136:139], v226, s[60:61] sc1\n\t"
      "global_load_dwordx4 v[140:143], v227, s[60:61] sc1\n\t"
      "global_load_dwordx4 v[144:147], v228, s[60:61] sc1\n\t"
      "global_load_dwordx4 v[148:151], v229, s[60:61] sc1\n\t"
      "global_load_dwordx4 v[152:155], v230, s[60:61] sc1\n\t"
      "s_waitcnt vmcnt(5)\n\t"
      "v_max3_u32 v234, v132, v133, v134\n\t"
      "v_max_u32_e32 v234, v234, v135\n\t"
      "v_cmp_eq_u32_e32 vcc, -1, v234\n\t"
      "s_cbranch_vccnz FQ29L_%=\n\t"
      "FQ29X_%=:\n\t"
      "s_branch FQ29B_%=\n\t"
      "FQ30R_%=:\n\t"
      "s_mov_b32 s51, 0\n\t"
      "FQ30L_%=:\n\t"
      "s_add_u32 s51, s51, 1\n\t"
      "s_cmp_lt_u32 s51, 262144\n\t"
      "s_cbranch_scc0 FQ30X_%=\n\t"
      "s_waitcnt vmcnt(0)\n\t"
      "s_sleep 4\n\t"
      "global_load_dwordx4 v[136:139], v226, s[60:61] sc1\n\t"
      "global_load_dwordx4 v[140:143], v227, s[60:61] sc1\n\t"
      "global_load_dwordx4 v[144:147], v228, s[60:61] sc1\n\t"
      "global_load_dwordx4 v[148:151], v229, s[60:61] sc1\n\t"
      "global_load_dwordx4 v[152:155], v230, s[60:61] sc1\n\t"
      "global_load_dwordx4 v[128:131], v224, s[62:63] sc1\n\t"
      "s_waitcnt vmcnt(5)\n\t"
      "v_max3_u32 v234, v136, v137, v138\n\t"
      "v_max_u32_e32 v234, v234, v139\n\t"
      "v_cmp_eq_u32_e32 vcc, -1, v234\n\t"
      "s_cbranch_vccnz FQ30L_%=\n\t"
      "FQ30X_%=:\n\t"
      "s_branch FQ30B_%=\n\t"
      "FQ31R_%=:\n\t"
      "s_mov_b32 s51, 0\n\t"
      "FQ31L_%=:\n\t"
      "s_add_u32 s51, s51, 1\n\t"
      "s_cmp_lt_u32 s51, 262144\n\t"
      "s_cbranch_scc0 FQ31X_%=\n\t"
      "s_waitcnt vmcnt(0)\n\t"
      "s_sleep 4\n\t"
      "global_load_dwordx4 v[140:143], v227, s[60:61] sc1\n\t"
      "global_load_dwordx4 v[144:147], v228, s[60:61] sc1\n\t"
      "global_load_dwordx4 v[148:151], v229, s[60:61] sc1\n\t"
      "global_load_dwordx4 v[152:155], v230, s[60:61] sc1\n\t"
      "global_load_dwordx4 v[128:131], v224, s[62:63] sc1\n\t"
      "global_load_dwordx4 v[132:135], v225, s[62:63] sc1\n\t"
      "s_waitcnt vmcnt(5)\n\t"
      "v_max3_u32 v234, v140, v141, v142\n\t"
      "v_max_u32_e32 v234, v234, v143\n\t"
      "v_cmp_eq_u32_e32 vcc, -1, v234\n\t"
      "s_cbranch_vccnz FQ31L_%=\n\t"
      "FQ31X_%=:\n\t"
      "s_branch FQ31B_%=\n\t"
      "FQ32R_%=:\n\t"
      "s_mov_b32 s51, 0\n\t"
      "FQ32L_%=:\n\t"
      "s_add_u32 s51, s51, 1\n\t"
      "s_cmp_lt_u32 s51, 262144\n\t"
      "s_cbranch_scc0 FQ32X_%=\n\t"
      "s_waitcnt vmcnt(0)\n\t"
      "s_sleep 4\n\t"
      "global_load_dwordx4 v[144:147], v228, s[60:61] sc1\n\t"
      "global_load_dwordx4 v[148:151], v229, s[60:61] sc1\n\t"
      "global_load_dwordx4 v[152:155], v230, s[60:61] sc1\n\t"
      "global_load_dwordx4 v[128:131], v224, s[62:63] sc1\n\t"
      "global_load_dwordx4 v[132:135], v225, s[62:63] sc1\n\t"
      "global_load_dwordx4 v[136:139], v226, s[62:63] sc1\n\t"
      "s_waitcnt vmcnt(5)\n\t"
      "v_max3_u32 v234, v144, v145, v146\n\t"
      "v_max_u32_e32 v234, v234, v147\n\t"
      "v_cmp_eq_u32_e32 vcc, -1, v234\n\t"
      "s_cbranch_vccnz FQ32L_%=\n\t"
      "FQ32X_%=:\n\t"
      "s_branch FQ32B_%=\n\t"
      "FQ33R_%=:\n\t"
      "s_mov_b32 s51, 0\n\t"
      "FQ33L_%=:\n\t"
      "s_add_u32 s51, s51, 1\n\t"
      "s_cmp_lt_u32 s51, 262144\n\t"
      "s_cbranch_scc0 FQ33X_%=\n\t"
      "s_waitcnt vmcnt(0)\n\t"
      "s_sleep 4\n\t"
      "global_load_dwordx4 v[148:151], v229, s[60:61] sc1\n\t"
      "global_load_dwordx4 v[152:155], v230, s[60:61] sc1\n\t"
      "global_load_dwordx4 v[128:131], v224, s[62:63] sc1\n\t"
      "global_load_dwordx4 v[132:135], v225, s[62:63] sc1\n\t"
      "global_load_dwordx4 v[136:139], v226, s[62:63] sc1\n\t"
      "global_load_dwordx4 v[140:143], v227, s[62:63] sc1\n\t"
      "s_waitcnt vmcnt(5)\n\t"
      "v_max3_u32 v234, v148, v149, v150\n\t"
      "v_max_u32_e32 v234, v234, v151\n\t"
      "v_cmp_eq_u32_e32 vcc, -1, v234\n\t"
      "s_cbranch_vccnz FQ33L_%=\n\t"
      "FQ33X_%=:\n\t"
      "s_branch FQ33B_%=\n\t"
      "FQ34R_%=:\n\t"
      "s_mov_b32 s51, 0\n\t"
      "FQ34L_%=:\n\t"
      "s_add_u32 s51, s51, 1\n\t"
      "s_cmp_lt_u32 s51, 262144\n\t"
      "s_cbranch_scc0 FQ34X_%=\n\t"
      "s_waitcnt vmcnt(0)\n\t"
      "s_sleep 4\n\t"
      "global_load_dwordx4 v[152:155], v230, s[60:61] sc1\n\t"
      "global_load_dwordx4 v[128:131], v224, s[62:63] sc1\n\t"
      "global_load_dwordx4 v[132:135], v225, s[62:63] sc1\n\t"
      "global_load_dwordx4 v[136:139], v226, s[62:63] sc1\n\t"
      "global_load_dwordx4 v[140:143], v227, s[62:63] sc1\n\t"
      "global_load_dwordx4 v[144:147], v228, s[62:63] sc1\n\t"
      "s_waitcnt vmcnt(5)\n\t"
      "v_max3_u32 v234, v152, v153, v154\n\t"
      "v_max_u32_e32 v234, v234, v155\n\t"
      "v_cmp_eq_u32_e32 vcc, -1, v234\n\t"
      "s_cbranch_vccnz FQ34L_%=\n\t"
      "FQ34X_%=:\n\t"
      "s_branch FQ34B_%=\n\t"
      "FQ35R_%=:\n\t"
      "s_mov_b32 s51, 0\n\t"
      "FQ35L_%=:\n\t"
      "s_add_u32 s51, s51, 1\n\t"
      "s_cmp_lt_u32 s51, 262144\n\t"
      "s_cbranch_scc0 FQ35X_%=\n\t"
      "s_waitcnt vmcnt(0)\n\t"
      "s_sleep 4\n\t"
      "global_load_dwordx4 v[128:131], v224, s[62:63] sc1\n\t"
      "global_load_dwordx4 v[132:135], v225, s[62:63] sc1\n\t"
      "global_load_dwordx4 v[136:139], v226, s[62:63] sc1\n\t"
      "global_load_dwordx4 v[140:143], v227, s[62:63] sc1\n\t"
      "global_load_dwordx4 v[144:147], v228, s[62:63] sc1\n\t"
      "global_load_dwordx4 v[148:151], v229, s[62:63] sc1\n\t"
      "s_waitcnt vmcnt(5)\n\t"
      "v_max3_u32 v234, v128, v129, v130\n\t"
      "v_max_u32_e32 v234, v234, v131\n\t"
      "v_cmp_eq_u32_e32 vcc, -1, v234\n\t"
      "s_cbranch_vccnz FQ35L_%=\n\t"
      "FQ35X_%=:\n\t"
      "s_branch FQ35B_%=\n\t"
      "NQ4R_%=:\n\t"
      "s_mov_b32 s51, 0\n\t"
      "NQ4L_%=:\n\t"
      "s_add_u32 s51, s51, 1\n\t"
      "s_cmp_lt_u32 s51, 262144\n\t"
      "s_cbranch_scc0 NQ4X_%=\n\t"
      "s_waitcnt vmcnt(0)\n\t"
      "s_sleep 1\n\t"
      "global_load_dwordx4 v[128:131], v224, s[60:61] sc1\n\t"
      "global_load_dwordx4 v[132:135], v225, s[60:61] sc1\n\t"
      "global_load_dwordx4 v[136:139], v226, s[60:61] sc1\n\t"
      "global_load_dwordx4 v[140:143], v227, s[60:61] sc1\n\t"
      "global_load_dwordx4 v[144:147], v228, s[60:61] sc1\n\t"
      "global_load_dwordx4 v[148:151], v229, s[60:61] sc1\n\t"
      "global_load_dwordx4 v[152:155], v230, s[60:61] sc1\n\t"
      "s_waitcnt vmcnt(0)\n\t"
      "v_max3_u32 v234, v128, v129, v130\n\t"
      "v_max3_u32 v234, v234, v131, v132\n\t"
      "v_max3_u32 v234, v234, v133, v134\n\t"
      "v_max3_u32 v234, v234, v135, v136\n\t"
      "v_max3_u32 v234, v234, v137, v138\n\t"
      "v_max3_u32 v234, v234, v139, v140\n\t"
      "v_max3_u32 v234, v234, v141, v142\n\t"
      "v_max3_u32 v234, v234, v143, v144\n\t"
      "v_max3_u32 v234, v234, v145, v146\n\t"
      "v_max3_u32 v234, v234, v147, v148\n\t"
      "v_max3_u32 v234, v234, v149, v150\n\t"
      "v_max3_u32 v234, v234, v151, v152\n\t"
      "v_max3_u32 v234, v234, v153, v154\n\t"
      "v_max_u32_e32 v234, v234, v155\n\t"
      "v_cmp_eq_u32_e32 vcc, -1, v234\n\t"
      "s_cbranch_vccnz NQ4L_%=\n\t"
      "NQ4X_%=:\n\t"
      "v_mfma_f32_16x16x32_bf16 v[184:187], v[128:131], v[0:3], 0\n\t"
      "v_mfma_f32_16x16x32_bf16 v[188:191], v[128:131], v[4:7], 0\n\t"
      "v_mfma_f32_16x16x32_bf16 v[192:195], v[128:131], v[8:11], 0\n\t"
      "v_mfma_f32_16x16x32_bf16 v[196:199], v[128:131], v[12:15], 0\n\t"
      "v_mfma_f32_16x16x32_bf16 v[184:187], v[132:135], v[16:19], v[184:187]\n\t"
      "v_mfma_f32_16x16x32_bf16 v[188:191], v[132:135], v[20:23], v[188:191]\n\t"
      "v_mfma_f32_16x16x32_bf16 v[192:195], v[132:135], v[24:27], v[192:195]\n\t"
      "v_mfma_f32_16x16x32_bf16 v[196:199], v[132:135], v[28:31], v[196:199]\n\t"
      "v_mfma_f32_16x16x32_bf16 v[184:187], v[136:139], v[32:35], v[184:187]\n\t"
      "v_mfma_f32_16x16x32_bf16 v[188:191], v[136:139], v[36:39], v[188:191]\n\t"
      "v_mfma_f32_16x16x32_bf16 v[192:195], v[136:139], v[40:43], v[192:195]\n\t"
      "v_mfma_f32_16x16x32_bf16 v[196:199], v[136:139], v[44:47], v[196:199]\n\t"
      "v_mfma_f32_16x16x32_bf16 v[184:187], v[140:143], v[48:51], v[184:187]\n\t"
      "v_mfma_f32_16x16x32_bf16 v[188:191], v[140:143], v[52:55], v[188:191]\n\t"
      "v_mfma_f32_16x16x32_bf16 v[192:195], v[140:143], v[56:59], v[192:195]\n\t"
      "v_mfma_f32_16x16x32_bf16 v[196:199], v[140:143], v[60:63], v[196:199]\n\t"
      "v_mfma_f32_16x16x32_bf16 v[184:187], v[144:147], v[64:67], v[184:187]\n\t"
      "v_mfma_f32_16x16x32_bf16 v[188:191], v[144:147], v[68:71], v[188:191]\n\t"
      "v_mfma_f32_16x16x32_bf16 v[192:195], v[144:147], v[72:75], v[192:195]\n\t"
      "v_mfma_f32_16x16x32_bf16 v[196:199], v[144:147], v[76:79], v[196:199]\n\t"
      "v_mfma_f32_16x16x32_bf16 v[184:187], v[148:151], v[80:83], v[184:187]\n\t"
      "v_mfma_f32_16x16x32_bf16 v[188:191], v[148:151], v[84:87], v[188:191]\n\t"
      "v_mfma_f32_16x16x32_bf16 v[192:195], v[148:151], v[88:91], v[192:195]\n\t"
      "v_mfma_f32_16x16x32_bf16 v[196:199], v[148:151], v[92:95], v[196:199]\n\t"
      "v_mfma_f32_16x16x32_bf16 v[184:187], v[152:155], v[96:99], v[184:187]\n\t"
      "v_mfma_f32_16x16x32_bf16 v[188:191], v[152:155], v[100:103], v[188:191]\n\t"
      "v_mfma_f32_16x16x32_bf16 v[192:195], v[152:155], v[104:107], v[192:195]\n\t"
      "v_mfma_f32_16x16x32_bf16 v[196:199], v[152:155], v[108:111], v[196:199]\n\t"
      "global_load_dwordx4 v[128:131], v224, s[62:63] sc1\n\t"
      "global_load_dwordx4 v[132:135], v225, s[62:63] sc1\n\t"
      "global_load_dwordx4 v[136:139], v226, s[62:63] sc1\n\t"
      "global_load_dwordx4 v[140:143], v227, s[62:63] sc1\n\t"
      "global_load_dwordx4 v[144:147], v228, s[62:63] sc1\n\t"
      "global_load_dwordx4 v[148:151], v229, s[62:63] sc1\n\t"
      "global_load_dwordx4 v[152:155], v230, s[62:63] sc1\n\t"
      "s_waitcnt vmcnt(0)\n\t"
      "s_nop 7\n\t"
      "s_branch NQ4B_%=\n\t"
      "FQ36R_%=:\n\t"
      "s_mov_b32 s51, 0\n\t"
      "FQ36L_%=:\n\t"
      "s_add_u32 s51, s51, 1\n\t"
      "s_cmp_lt_u32 s51, 262144\n\t"
      "s_cbranch_scc0 FQ36X_%=\n\t"
      "s_waitcnt vmcnt(0)\n\t"
      "s_sleep 4\n\t"
      "global_load_dwordx4 v[132:135], v225, s[62:63] sc1\n\t"
      "global_load_dwordx4 v[136:139], v226, s[62:63] sc1\n\t"
      "global_load_dwordx4 v[140:143], v227, s[62:63] sc1\n\t"
      "global_load_dwordx4 v[144:147], v228, s[62:63] sc1\n\t"
      "global_load_dwordx4 v[148:151], v229, s[62:63] sc1\n\t"
      "global_load_dwordx4 v[152:155], v230, s[62:63] sc1\n\t"
      "s_waitcnt vmcnt(5)\n\t"
      "v_max3_u32 v234, v132, v133, v134\n\t"
      "v_max_u32_e32 v234, v234, v135\n\t"
      "v_cmp_eq_u32_e32 vcc, -1, v234\n\t"
      "s_cbranch_vccnz FQ36L_%=\n\t"
      "FQ36X_%=:\n\t"
      "s_branch FQ36B_%=\n\t"
      "FQ37R_%=:\n\t"
      "s_mov_b32 s51, 0\n\t"
      "FQ37L_%=:\n\t"
      "s_add_u32 s51, s51, 1\n\t"
      "s_cmp_lt_u32 s51, 262144\n\t"
      "s_cbranch_scc0 FQ37X_%=\n\t"
      "s_waitcnt vmcnt(0)\n\t"
      "s_sleep 4\n\t"
      "global_load_dwordx4 v[136:139], v226, s[62:63] sc1\n\t"
      "global_load_dwordx4 v[140:143], v227, s[62:63] sc1\n\t"
      "global_load_dwordx4 v[144:147], v228, s[62:63] sc1\n\t"
      "global_load_dwordx4 v[148:151], v229, s[62:63] sc1\n\t"
      "global_load_dwordx4 v[152:155], v230, s[62:63] sc1\n\t"
      "global_load_dwordx4 v[128:131], v224, s[64:65] sc1\n\t"
      "s_waitcnt vmcnt(5)\n\t"
      "v_max3_u32 v234, v136, v137, v138\n\t"
      "v_max_u32_e32 v234, v234, v139\n\t"
      "v_cmp_eq_u32_e32 vcc, -1, v234\n\t"
      "s_cbranch_vccnz FQ37L_%=\n\t"
      "FQ37X_%=:\n\t"
      "s_branch FQ37B_%=\n\t"
      "FQ38R_%=:\n\t"
      "s_mov_b32 s51, 0\n\t"
      "FQ38L_%=:\n\t"
      "s_add_u32 s51, s51, 1\n\t"
      "s_cmp_lt_u32 s51, 262144\n\t"
      "s_cbranch_scc0 FQ38X_%=\n\t"
      "s_waitcnt vmcnt(0)\n\t"
      "s_sleep 4\n\t"
      "global_load_dwordx4 v[140:143], v227, s[62:63] sc1\n\t"
      "global_load_dwordx4 v[144:147], v228, s[62:63] sc1\n\t"
      "global_load_dwordx4 v[148:151], v229, s[62:63] sc1\n\t"
      "global_load_dwordx4 v[152:155], v230, s[62:63] sc1\n\t"
      "global_load_dwordx4 v[128:131], v224, s[64:65] sc1\n\t"
      "global_load_dwordx4 v[132:135], v225, s[64:65] sc1\n\t"
      "s_waitcnt vmcnt(5)\n\t"
      "v_max3_u32 v234, v140, v141, v142\n\t"
      "v_max_u32_e32 v234, v234, v143\n\t"
      "v_cmp_eq_u32_e32 vcc, -1, v234\n\t"
      "s_cbranch_vccnz FQ38L_%=\n\t"
      "FQ38X_%=:\n\t"
      "s_branch FQ38B_%=\n\t"
      "FQ39R_%=:\n\t"
      "s_mov_b32 s51, 0\n\t"
      "FQ39L_%=:\n\t"
      "s_add_u32 s51, s51, 1\n\t"
      "s_cmp_lt_u32 s51, 262144\n\t"
      "s_cbranch_scc0 FQ39X_%=\n\t"
      "s_waitcnt vmcnt(0)\n\t"
      "s_sleep 4\n\t"
      "global_load_dwordx4 v[144:147], v228, s[62:63] sc1\n\t"
      "global_load_dwordx4 v[148:151], v229, s[62:63] sc1\n\t"
      "global_load_dwordx4 v[152:155], v230, s[62:63] sc1\n\t"
      "global_load_dwordx4 v[128:131], v224, s[64:65] sc1\n\t"
      "global_load_dwordx4 v[132:135], v225, s[64:65] sc1\n\t"
      "global_load_dwordx4 v[136:139], v226, s[64:65] sc1\n\t"
      "s_waitcnt vmcnt(5)\n\t"
      "v_max3_u32 v234, v144, v145, v146\n\t"
      "v_max_u32_e32 v234, v234, v147\n\t"
      "v_cmp_eq_u32_e32 vcc, -1, v234\n\t"
      "s_cbranch_vccnz FQ39L_%=\n\t"
      "FQ39X_%=:\n\t"
      "s_branch FQ39B_%=\n\t"
      "FQ40R_%=:\n\t"
      "s_mov_b32 s51, 0\n\t"
      "FQ40L_%=:\n\t"
      "s_add_u32 s51, s51, 1\n\t"
      "s_cmp_lt_u32 s51, 262144\n\t"
      "s_cbranch_scc0 FQ40X_%=\n\t"
      "s_waitcnt vmcnt(0)\n\t"
      "s_sleep 4\n\t"
      "global_load_dwordx4 v[148:151], v229, s[62:63] sc1\n\t"
      "global_load_dwordx4 v[152:155], v230, s[62:63] sc1\n\t"
      "global_load_dwordx4 v[128:131], v224, s[64:65] sc1\n\t"
      "global_load_dwordx4 v[132:135], v225, s[64:65] sc1\n\t"
      "global_load_dwordx4 v[136:139], v226, s[64:65] sc1\n\t"
      "global_load_dwordx4 v[140:143], v227, s[64:65] sc1\n\t"
      "s_waitcnt vmcnt(5)\n\t"
      "v_max3_u32 v234, v148, v149, v150\n\t"
      "v_max_u32_e32 v234, v234, v151\n\t"
      "v_cmp_eq_u32_e32 vcc, -1, v234\n\t"
      "s_cbranch_vccnz FQ40L_%=\n\t"
      "FQ40X_%=:\n\t"
      "s_branch FQ40B_%=\n\t"
      "FQ41R_%=:\n\t"
      "s_mov_b32 s51, 0\n\t"
      "FQ41L_%=:\n\t"
      "s_add_u32 s51, s51, 1\n\t"
      "s_cmp_lt_u32 s51, 262144\n\t"
      "s_cbranch_scc0 FQ41X_%=\n\t"
      "s_waitcnt vmcnt(0)\n\t"
      "s_sleep 4\n\t"
      "global_load_dwordx4 v[152:155], v230, s[62:63] sc1\n\t"
      "global_load_dwordx4 v[128:131], v224, s[64:65] sc1\n\t"
      "global_load_dwordx4 v[132:135], v225, s[64:65] sc1\n\t"
      "global_load_dwordx4 v[136:139], v226, s[64:65] sc1\n\t"
      "global_load_dwordx4 v[140:143], v227, s[64:65] sc1\n\t"
      "global_load_dwordx4 v[144:147], v228, s[64:65] sc1\n\t"
      "s_waitcnt vmcnt(5)\n\t"
      "v_max3_u32 v234, v152, v153, v154\n\t"
      "v_max_u32_e32 v234, v234, v155\n\t"
      "v_cmp_eq_u32_e32 vcc, -1, v234\n\t"
      "s_cbranch_vccnz FQ41L_%=\n\t"
      "FQ41X_%=:\n\t"
      "s_branch FQ41B_%=\n\t"
      "FQ42R_%=:\n\t"
      "s_mov_b32 s51, 0\n\t"
      "FQ42L_%=:\n\t"
      "s_add_u32 s51, s51, 1\n\t"
      "s_cmp_lt_u32 s51, 262144\n\t"
      "s_cbranch_scc0 FQ42X_%=\n\t"
      "s_sleep 1\n\t"
      "s_mov_b64 exec, 15\n\t"
      "v_mov_b64_e32 v[232:233], 0\n\t"
      "global_atomic_or_x2 v[232:233], v247, v[232:233], s[12:13] sc0 sc1\n\t"
      "s_waitcnt vmcnt(0)\n\t"
      "v_subrev_u32_e32 v232, s14, v232\n\t"
      "v_cmp_ne_u32_e64 s[48:49], s15, v233\n\t"
      "v_subrev_u32_e32 v232, 6, v232\n\t"
      "v_cmp_lt_u32_e32 vcc, 7, v232\n\t"
      "s_mov_b64 exec, -1\n\t"
      "s_or_b64 vcc, vcc, s[48:49]\n\t"
      "s_cbranch_vccnz FQ42L_%=\n\t"
      "FQ42X_%=:\n\t"
      "FQ42DR_%=:\n\t"
      "s_mov_b32 s51, 0\n\t"
      "FQ42DL_%=:\n\t"
      "s_add_u32 s51, s51, 1\n\t"
      "s_cmp_lt_u32 s51, 262144\n\t"
      "s_cbranch_scc0 FQ42DX_%=\n\t"
      "s_waitcnt vmcnt(0)\n\t"
      "s_sleep 1\n\t"
      "global_load_dwordx4 v[128:131], v224, s[64:65] sc1\n\t"
      "global_load_dwordx4 v[132:135], v225, s[64:65] sc1\n\t"
      "global_load_dwordx4 v[136:139], v226, s[64:65] sc1\n\t"
      "global_load_dwordx4 v[140:143], v227, s[64:65] sc1\n\t"
      "global_load_dwordx4 v[144:147], v228, s[64:65] sc1\n\t"
      "global_load_dwordx4 v[148:151], v229, s[64:65] sc1\n\t"
      "s_waitcnt vmcnt(5)\n\t"
      "v_max3_u32 v234, v128, v129, v130\n\t"
      "v_max_u32_e32 v234, v234, v131\n\t"
      "v_cmp_eq_u32_e32 vcc, -1, v234\n\t"
      "s_cbranch_vccnz FQ42DL_%=\n\t"
      "FQ42DX_%=:\n\t"
      "s_branch FQ42B_%=\n\t"
      "NQ5R_%=:\n\t"
      "s_mov_b32 s51, 0\n\t"
      "NQ5L_%=:\n\t"
      "s_add_u32 s51, s51, 1\n\t"
      "s_cmp_lt_u32 s51, 262144\n\t"
      "s_cbranch_scc0 NQ5X_%=\n\t"
      "s_waitcnt vmcnt(0)\n\t"
      "s_sleep 1\n\t"
      "global_load_dwordx4 v[128:131], v224, s[62:63] sc1\n\t"
      "global_load_dwordx4 v[132:135], v225, s[62:63] sc1\n\t"
      "global_load_dwordx4 v[136:139], v226, s[62:63] sc1\n\t"
      "global_load_dwordx4 v[140:143], v227, s[62:63] sc1\n\t"
      "global_load_dwordx4 v[144:147], v228, s[62:63] sc1\n\t"
      "global_load_dwordx4 v[148:151], v229, s[62:63] sc1\n\t"
      "global_load_dwordx4 v[152:155], v230, s[62:63] sc1\n\t"
      "s_waitcnt vmcnt(0)\n\t"
      "v_max3_u32 v234, v128, v129, v130\n\t"
      "v_max3_u32 v234, v234, v131, v132\n\t"
      "v_max3_u32 v234, v234, v133, v134\n\t"
      "v_max3_u32 v234, v234, v135, v136\n\t"
      "v_max3_u32 v234, v234, v137, v138\n\t"
      "v_max3_u32 v234, v234, v139, v140\n\t"
      "v_max3_u32 v234, v234, v141, v142\n\t"
      "v_max3_u32 v234, v234, v143, v144\n\t"
      "v_max3_u32 v234, v234, v145, v146\n\t"
      "v_max3_u32 v234, v234, v147, v148\n\t"
      "v_max3_u32 v234, v234, v149, v150\n\t"
      "v_max3_u32 v234, v234, v151, v152\n\t"
      "v_max3_u32 v234, v234, v153, v154\n\t"
      "v_max_u32_e32 v234, v234, v155\n\t"
      "v_cmp_eq_u32_e32 vcc, -1, v234\n\t"
      "s_cbranch_vccnz NQ5L_%=\n\t"
      "NQ5X_%=:\n\t"
      "v_mfma_f32_16x16x32_bf16 v[200:203], v[128:131], v[0:3], 0\n\t"
      "v_mfma_f32_16x16x32_bf16 v[204:207], v[128:131], v[4:7], 0\n\t"
      "v_mfma_f32_16x16x32_bf16 v[208:211], v[128:131], v[8:11], 0\n\t"
      "v_mfma_f32_16x16x32_bf16 v[212:215], v[128:131], v[12:15], 0\n\t"
      "v_mfma_f32_16x16x32_bf16 v[200:203], v[132:135], v[16:19], v[200:203]\n\t"
      "v_mfma_f32_16x16x32_bf16 v[204:207], v[132:135], v[20:23], v[204:207]\n\t"
      "v_mfma_f32_16x16x32_bf16 v[208:211], v[132:135], v[24:27], v[208:211]\n\t"
      "v_mfma_f32_16x16x32_bf16 v[212:215], v[132:135], v[28:31], v[212:215]\n\t"
      "v_mfma_f32_16x16x32_bf16 v[200:203], v[136:139], v[32:35], v[200:203]\n\t"
      "v_mfma_f32_16x16x32_bf16 v[204:207], v[136:139], v[36:39], v[204:207]\n\t"
      "v_mfma_f32_16x16x32_bf16 v[208:211], v[136:139], v[40:43], v[208:211]\n\t"
      "v_mfma_f32_16x16x32_bf16 v[212:215], v[136:139], v[44:47], v[212:215]\n\t"
      "v_mfma_f32_16x16x32_bf16 v[200:203], v[140:143], v[48:51], v[200:203]\n\t"
      "v_mfma_f32_16x16x32_bf16 v[204:207], v[140:143], v[52:55], v[204:207]\n\t"
      "v_mfma_f32_16x16x32_bf16 v[208:211], v[140:143], v[56:59], v[208:211]\n\t"
      "v_mfma_f32_16x16x32_bf16 v[212:215], v[140:143], v[60:63], v[212:215]\n\t"
      "v_mfma_f32_16x16x32_bf16 v[200:203], v[144:147], v[64:67], v[200:203]\n\t"
      "v_mfma_f32_16x16x32_bf16 v[204:207], v[144:147], v[68:71], v[204:207]\n\t"
      "v_mfma_f32_16x16x32_bf16 v[208:211], v[144:147], v[72:75], v[208:211]\n\t"
      "v_mfma_f32_16x16x32_bf16 v[212:215], v[144:147], v[76:79], v[212:215]\n\t"
      "v_mfma_f32_16x16x32_bf16 v[200:203], v[148:151], v[80:83], v[200:203]\n\t"
      "v_mfma_f32_16x16x32_bf16 v[204:207], v[148:151], v[84:87], v[204:207]\n\t"
      "v_mfma_f32_16x16x32_bf16 v[208:211], v[148:151], v[88:91], v[208:211]\n\t"
      "v_mfma_f32_16x16x32_bf16 v[212:215], v[148:151], v[92:95], v[212:215]\n\t"
      "v_mfma_f32_16x16x32_bf16 v[200:203], v[152:155], v[96:99], v[200:203]\n\t"
      "v_mfma_f32_16x16x32_bf16 v[204:207], v[152:155], v[100:103], v[204:207]\n\t"
      "v_mfma_f32_16x16x32_bf16 v[208:211], v[152:155], v[104:107], v[208:211]\n\t"
      "v_mfma_f32_16x16x32_bf16 v[212:215], v[152:155], v[108:111], v[212:215]\n\t"
      "global_load_dwordx4 v[128:131], v224, s[64:65] sc1\n\t"
      "global_load_dwordx4 v[132:135], v225, s[64:65] sc1\n\t"
      "global_load_dwordx4 v[136:139], v226, s[64:65] sc1\n\t"
      "global_load_dwordx4 v[140:143], v227, s[64:65] sc1\n\t"
      "global_load_dwordx4 v[144:147], v228, s[64:65] sc1\n\t"
      "global_load_dwordx4 v[148:151], v229, s[64:65] sc1\n\t"
      "global_load_dwordx4 v[152:155], v230, s[64:65] sc1\n\t"
      "s_waitcnt vmcnt(0)\n\t"
      "s_nop 7\n\t"
      "s_branch NQ5B_%=\n\t"
      "FQ43R_%=:\n\t"
      "s_mov_b32 s51, 0\n\t"
      "FQ43L_%=:\n\t"
      "s_add_u32 s51, s51, 1\n\t"
      "s_cmp_lt_u32 s51, 262144\n\t"
      "s_cbranch_scc0 FQ43X_%=\n\t"
      "s_sleep 1\n\t"
      "s_mov_b64 exec, 15\n\t"
      "v_mov_b64_e32 v[232:233], 0\n\t"
      "global_atomic_or_x2 v[232:233], v247, v[232:233], s[12:13] sc0 sc1\n\t"
      "s_waitcnt vmcnt(0)\n\t"
      "v_subrev_u32_e32 v232, s14, v232\n\t"
      "v_cmp_ne_u32_e64 s[48:49], s15, v233\n\t"
      "v_subrev_u32_e32 v232, 6, v232\n\t"
      "v_cmp_lt_u32_e32 vcc, 7, v232\n\t"
      "s_mov_b64 exec, -1\n\t"
      "s_or_b64 vcc, vcc, s[48:49]\n\t"
      "s_cbranch_vccnz FQ43L_%=\n\t"
      "FQ43X_%=:\n\t"
      "FQ43DR_%=:\n\t"
      "s_mov_b32 s51, 0\n\t"
      "FQ43DL_%=:\n\t"
      "s_add_u32 s51, s51, 1\n\t"
      "s_cmp_lt_u32 s51, 262144\n\t"
      "s_cbranch_scc0 FQ43DX_%=\n\t"
      "s_waitcnt vmcnt(0)\n\t"
      "s_sleep 1\n\t"
      "global_load_dwordx4 v[132:135], v225, s[64:65] sc1\n\t"
      "global_load_dwordx4 v[136:139], v226, s[64:65] sc1\n\t"
      "global_load_dwordx4 v[140:143], v227, s[64:65] sc1\n\t"
      "global_load_dwordx4 v[144:147], v228, s[64:65] sc1\n\t"
      "global_load_dwordx4 v[148:151], v229, s[64:65] sc1\n\t"
      "global_load_dwordx4 v[152:155], v230, s[64:65] sc1\n\t"
      "s_waitcnt vmcnt(5)\n\t"
      "v_max3_u32 v234, v132, v133, v134\n\t"
      "v_max_u32_e32 v234, v234, v135\n\t"
      "v_cmp_eq_u32_e32 vcc, -1, v234\n\t"
      "s_cbranch_vccnz FQ43DL_%=\n\t"
      "FQ43DX_%=:\n\t"
      "s_branch FQ43B_%=\n\t"
      "FQ44R_%=:\n\t"
      "s_mov_b32 s51, 0\n\t"
      "FQ44L_%=:\n\t"
      "s_add_u32 s51, s51, 1\n\t"
      "s_cmp_lt_u32 s51, 262144\n\t"
      "s_cbranch_scc0 FQ44X_%=\n\t"
      "s_sleep 1\n\t"
      "s_mov_b64 exec, 15\n\t"
      "v_mov_b64_e32 v[232:233], 0\n\t"
      "global_atomic_or_x2 v[232:233], v247, v[232:233], s[12:13] sc0 sc1\n\t"
      "s_waitcnt vmcnt(0)\n\t"
      "v_subrev_u32_e32 v232, s14, v232\n\t"
      "v_cmp_ne_u32_e64 s[48:49], s15, v233\n\t"
      "v_subrev_u32_e32 v232, 6, v232\n\t"
      "v_cmp_lt_u32_e32 vcc, 7, v232\n\t"
      "s_mov_b64 exec, -1\n\t"
      "s_or_b64 vcc, vcc, s[48:49]\n\t"
      "s_cbranch_vccnz FQ44L_%=\n\t"
      "FQ44X_%=:\n\t"
      "FQ44DR_%=:\n\t"
      "s_mov_b32 s51, 0\n\t"
      "FQ44DL_%=:\n\t"
      "s_add_u32 s51, s51, 1\n\t"
      "s_cmp_lt_u32 s51, 262144\n\t"
      "s_cbranch_scc0 FQ44DX_%=\n\t"
      "s_waitcnt vmcnt(0)\n\t"
      "s_sleep 1\n\t"
      "global_load_dwordx4 v[136:139], v226, s[64:65] sc1\n\t"
      "global_load_dwordx4 v[140:143], v227, s[64:65] sc1\n\t"
      "global_load_dwordx4 v[144:147], v228, s[64:65] sc1\n\t"
      "global_load_dwordx4 v[148:151], v229, s[64:65] sc1\n\t"
      "global_load_dwordx4 v[152:155], v230, s[64:65] sc1\n\t"
      "s_waitcnt vmcnt(4)\n\t"
      "v_max3_u32 v234, v136, v137, v138\n\t"
      "v_max_u32_e32 v234, v234, v139\n\t"
      "v_cmp_eq_u32_e32 vcc, -1, v234\n\t"
      "s_cbranch_vccnz FQ44DL_%=\n\t"
      "FQ44DX_%=:\n\t"
      "s_branch FQ44B_%=\n\t"
      "FQ45R_%=:\n\t"
      "s_mov_b32 s51, 0\n\t"
      "FQ45L_%=:\n\t"
      "s_add_u32 s51, s51, 1\n\t"
      "s_cmp_lt_u32 s51, 262144\n\t"
      "s_cbranch_scc0 FQ45X_%=\n\t"
      "s_sleep 1\n\t"
      "s_mov_b64 exec, 15\n\t"
      "v_mov_b64_e32 v[232:233], 0\n\t"
      "global_atomic_or_x2 v[232:233], v247, v[232:233], s[12:13] sc0 sc1\n\t"
      "s_waitcnt vmcnt(0)\n\t"
      "v_subrev_u32_e32 v232, s14, v232\n\t"
      "v_cmp_ne_u32_e64 s[48:49], s15, v233\n\t"
      "v_subrev_u32_e32 v232, 6, v232\n\t"
      "v_cmp_lt_u32_e32 vcc, 7, v232\n\t"
      "s_mov_b64 exec, -1\n\t"
      "s_or_b64 vcc, vcc, s[48:49]\n\t"
      "s_cbranch_vccnz FQ45L_%=\n\t"
      "FQ45X_%=:\n\t"
      "FQ45DR_%=:\n\t"
      "s_mov_b32 s51, 0\n\t"
      "FQ45DL_%=:\n\t"
      "s_add_u32 s51, s51, 1\n\t"
      "s_cmp_lt_u32 s51, 262144\n\t"
      "s_cbranch_scc0 FQ45DX_%=\n\t"
      "s_waitcnt vmcnt(0)\n\t"
      "s_sleep 1\n\t"
      "global_load_dwordx4 v[140:143], v227, s[64:65] sc1\n\t"
      "global_load_dwordx4 v[144:147], v228, s[64:65] sc1\n\t"
      "global_load_dwordx4 v[148:151], v229, s[64:65] sc1\n\t"
      "global_load_dwordx4 v[152:155], v230, s[64:65] sc1\n\t"
      "s_waitcnt vmcnt(3)\n\t"
      "v_max3_u32 v234, v140, v141, v142\n\t"
      "v_max_u32_e32 v234, v234, v143\n\t"
      "v_cmp_eq_u32_e32 vcc, -1, v234\n\t"
      "s_cbranch_vccnz FQ45DL_%=\n\t"
      "FQ45DX_%=:\n\t"
      "s_branch FQ45B_%=\n\t"
      "FQ46R_%=:\n\t"
      "s_mov_b32 s51, 0\n\t"
      "FQ46L_%=:\n\t"
      "s_add_u32 s51, s51, 1\n\t"
      "s_cmp_lt_u32 s51, 262144\n\t"
      "s_cbranch_scc0 FQ46X_%=\n\t"
      "s_sleep 1\n\t"
      "s_mov_b64 exec, 15\n\t"
      "v_mov_b64_e32 v[232:233], 0\n\t"
      "global_atomic_or_x2 v[232:233], v247, v[232:233], s[12:13] sc0 sc1\n\t"
      "s_waitcnt vmcnt(0)\n\t"
      "v_subrev_u32_e32 v232, s14, v232\n\t"
      "v_cmp_ne_u32_e64 s[48:49], s15, v233\n\t"
      "v_subrev_u32_e32 v232, 6, v232\n\t"
      "v_cmp_lt_u32_e32 vcc, 7, v232\n\t"
      "s_mov_b64 exec, -1\n\t"
      "s_or_b64 vcc, vcc, s[48:49]\n\t"
      "s_cbranch_vccnz FQ46L_%=\n\t"
      "FQ46X_%=:\n\t"
      "FQ46DR_%=:\n\t"
      "s_mov_b32 s51, 0\n\t"
      "FQ46DL_%=:\n\t"
      "s_add_u32 s51, s51, 1\n\t"
      "s_cmp_lt_u32 s51, 262144\n\t"
      "s_cbranch_scc0 FQ46DX_%=\n\t"
      "s_waitcnt vmcnt(0)\n\t"
      "s_sleep 1\n\t"
      "global_load_dwordx4 v[144:147], v228, s[64:65] sc1\n\t"
      "global_load_dwordx4 v[148:151], v229, s[64:65] sc1\n\t"
      "global_load_dwordx4 v[152:155], v230, s[64:65] sc1\n\t"
      "s_waitcnt vmcnt(2)\n\t"
      "v_max3_u32 v234, v144, v145, v146\n\t"
      "v_max_u32_e32 v234, v234, v147\n\t"
      "v_cmp_eq_u32_e32 vcc, -1, v234\n\t"
      "s_cbranch_vccnz FQ46DL_%=\n\t"
      "FQ46DX_%=:\n\t"
      "s_branch FQ46B_%=\n\t"
      "FQ47R_%=:\n\t"
      "s_mov_b32 s51, 0\n\t"
      "FQ47L_%=:\n\t"
      "s_add_u32 s51, s51, 1\n\t"
      "s_cmp_lt_u32 s51, 262144\n\t"
      "s_cbranch_scc0 FQ47X_%=\n\t"
      "s_sleep 1\n\t"
      "s_mov_b64 exec, 15\n\t"
      "v_mov_b64_e32 v[232:233], 0\n\t"
      "global_atomic_or_x2 v[232:233], v247, v[232:233], s[12:13] sc0 sc1\n\t"
      "s_waitcnt vmcnt(0)\n\t"
      "v_subrev_u32_e32 v232, s14, v232\n\t"
      "v_cmp_ne_u32_e64 s[48:49], s15, v233\n\t"
      "v_subrev_u32_e32 v232, 6, v232\n\t"
      "v_cmp_lt_u32_e32 vcc, 7, v232\n\t"
      "s_mov_b64 exec, -1\n\t"
      "s_or_b64 vcc, vcc, s[48:49]\n\t"
      "s_cbranch_vccnz FQ47L_%=\n\t"
      "FQ47X_%=:\n\t"
      "FQ47DR_%=:\n\t"
      "s_mov_b32 s51, 0\n\t"
      "FQ47DL_%=:\n\t"
      "s_add_u32 s51, s51, 1\n\t"
      "s_cmp_lt_u32 s51, 262144\n\t"
      "s_cbranch_scc0 FQ47DX_%=\n\t"
      "s_waitcnt vmcnt(0)\n\t"
      "s_sleep 1\n\t"
      "global_load_dwordx4 v[148:151], v229, s[64:65] sc1\n\t"
      "global_load_dwordx4 v[152:155], v230, s[64:65] sc1\n\t"
      "s_waitcnt vmcnt(1)\n\t"
      "v_max3_u32 v234, v148, v149, v150\n\t"
      "v_max_u32_e32 v234, v234, v151\n\t"
      "v_cmp_eq_u32_e32 vcc, -1, v234\n\t"
      "s_cbranch_vccnz FQ47DL_%=\n\t"
      "FQ47DX_%=:\n\t"
      "s_branch FQ47B_%=\n\t"
      "FQ48R_%=:\n\t"
      "s_mov_b32 s51, 0\n\t"
      "FQ48L_%=:\n\t"
      "s_add_u32 s51, s51, 1\n\t"
      "s_cmp_lt_u32 s51, 262144\n\t"
      "s_cbranch_scc0 FQ48X_%=\n\t"
      "s_sleep 1\n\t"
      "s_mov_b64 exec, 15\n\t"
      "v_mov_b64_e32 v[232:233], 0\n\t"
      "global_atomic_or_x2 v[232:233], v247, v[232:233], s[12:13] sc0 sc1\n\t"
      "s_waitcnt vmcnt(0)\n\t"
      "v_subrev_u32_e32 v232, s14, v232\n\t"
      "v_cmp_ne_u32_e64 s[48:49], s15, v233\n\t"
      "v_subrev_u32_e32 v232, 6, v232\n\t"
      "v_cmp_lt_u32_e32 vcc, 7, v232\n\t"
      "s_mov_b64 exec, -1\n\t"
      "s_or_b64 vcc, vcc, s[48:49]\n\t"
      "s_cbranch_vccnz FQ48L_%=\n\t"
      "FQ48X_%=:\n\t"
      "FQ48DR_%=:\n\t"
      "s_mov_b32 s51, 0\n\t"
      "FQ48DL_%=:\n\t"
      "s_add_u32 s51, s51, 1\n\t"
      "s_cmp_lt_u32 s51, 262144\n\t"
      "s_cbranch_scc0 FQ48DX_%=\n\t"
      "s_waitcnt vmcnt(0)\n\t"
      "s_sleep 1\n\t"
      "global_load_dwordx4 v[152:155], v230, s[64:65] sc1\n\t"
      "s_waitcnt vmcnt(0)\n\t"
      "v_max3_u32 v234, v152, v153, v154\n\t"
      "v_max_u32_e32 v234, v234, v155\n\t"
      "v_cmp_eq_u32_e32 vcc, -1, v234\n\t"
      "s_cbranch_vccnz FQ48DL_%=\n\t"
      "FQ48DX_%=:\n\t"
      "s_branch FQ48B_%=\n\t"
      "NQ6R_%=:\n\t"
      "s_mov_b32 s51, 0\n\t"
      "NQ6L_%=:\n\t"
      "s_add_u32 s51, s51, 1\n\t"
      "s_cmp_lt_u32 s51, 262144\n\t"
      "s_cbranch_scc0 NQ6X_%=\n\t"
      "s_waitcnt vmcnt(0)\n\t"
      "s_sleep 1\n\t"
      "global_load_dwordx4 v[128:131], v224, s[64:65] sc1\n\t"
      "global_load_dwordx4 v[132:135], v225, s[64:65] sc1\n\t"
      "global_load_dwordx4 v[136:139], v226, s[64:65] sc1\n\t"
      "global_load_dwordx4 v[140:143], v227, s[64:65] sc1\n\t"
      "global_load_dwordx4 v[144:147], v228, s[64:65] sc1\n\t"
      "global_load_dwordx4 v[148:151], v229, s[64:65] sc1\n\t"
      "global_load_dwordx4 v[152:155], v230, s[64:65] sc1\n\t"
      "s_waitcnt vmcnt(0)\n\t"
      "v_max3_u32 v234, v128, v129, v130\n\t"
      "v_max3_u32 v234, v234, v131, v132\n\t"
      "v_max3_u32 v234, v234, v133, v134\n\t"
      "v_max3_u32 v234, v234, v135, v136\n\t"
      "v_max3_u32 v234, v234, v137, v138\n\t"
      "v_max3_u32 v234, v234, v139, v140\n\t"
      "v_max3_u32 v234, v234, v141, v142\n\t"
      "v_max3_u32 v234, v234, v143, v144\n\t"
      "v_max3_u32 v234, v234, v145, v146\n\t"
      "v_max3_u32 v234, v234, v147, v148\n\t"
      "v_max3_u32 v234, v234, v149, v150\n\t"
      "v_max3_u32 v234, v234, v151, v152\n\t"
      "v_max3_u32 v234, v234, v153, v154\n\t"
      "v_max_u32_e32 v234, v234, v155\n\t"
      "v_cmp_eq_u32_e32 vcc, -1, v234\n\t"
      "s_cbranch_vccnz NQ6L_%=\n\t"
      "NQ6X_%=:\n\t"
      "v_mfma_f32_16x16x32_bf16 v[184:187], v[128:131], v[0:3], 0\n\t"
      "v_mfma_f32_16x16x32_bf16 v[188:191], v[128:131], v[4:7], 0\n\t"
      "v_mfma_f32_16x16x32_bf16 v[192:195], v[128:131], v[8:11], 0\n\t"
      "v_mfma_f32_16x16x32_bf16 v[196:199], v[128:131], v[12:15], 0\n\t"
      "v_mfma_f32_16x16x32_bf16 v[184:187], v[132:135], v[16:19], v[184:187]\n\t"
      "v_mfma_f32_16x16x32_bf16 v[188:191], v[132:135], v[20:23], v[188:191]\n\t"
      "v_mfma_f32_16x16x32_bf16 v[192:195], v[132:135], v[24:27], v[192:195]\n\t"
      "v_mfma_f32_16x16x32_bf16 v[196:199], v[132:135], v[28:31], v[196:199]\n\t"
      "v_mfma_f32_16x16x32_bf16 v[184:187], v[136:139], v[32:35], v[184:187]\n\t"
      "v_mfma_f32_16x16x32_bf16 v[188:191], v[136:139], v[36:39], v[188:191]\n\t"
      "v_mfma_f32_16x16x32_bf16 v[192:195], v[136:139], v[40:43], v[192:195]\n\t"
      "v_mfma_f32_16x16x32_bf16 v[196:199], v[136:139], v[44:47], v[196:199]\n\t"
      "v_mfma_f32_16x16x32_bf16 v[184:187], v[140:143], v[48:51], v[184:187]\n\t"
      "v_mfma_f32_16x16x32_bf16 v[188:191], v[140:143], v[52:55], v[188:191]\n\t"
      "v_mfma_f32_16x16x32_bf16 v[192:195], v[140:143], v[56:59], v[192:195]\n\t"
      "v_mfma_f32_16x16x32_bf16 v[196:199], v[140:143], v[60:63], v[196:199]\n\t"
      "v_mfma_f32_16x16x32_bf16 v[184:187], v[144:147], v[64:67], v[184:187]\n\t"
      "v_mfma_f32_16x16x32_bf16 v[188:191], v[144:147], v[68:71], v[188:191]\n\t"
      "v_mfma_f32_16x16x32_bf16 v[192:195], v[144:147], v[72:75], v[192:195]\n\t"
      "v_mfma_f32_16x16x32_bf16 v[196:199], v[144:147], v[76:79], v[196:199]\n\t"
      "v_mfma_f32_16x16x32_bf16 v[184:187], v[148:151], v[80:83], v[184:187]\n\t"
      "v_mfma_f32_16x16x32_bf16 v[188:191], v[148:151], v[84:87], v[188:191]\n\t"
      "v_mfma_f32_16x16x32_bf16 v[192:195], v[148:151], v[88:91], v[192:195]\n\t"
      "v_mfma_f32_16x16x32_bf16 v[196:199], v[148:151], v[92:95], v[196:199]\n\t"
      "v_mfma_f32_16x16x32_bf16 v[184:187], v[152:155], v[96:99], v[184:187]\n\t"
      "v_mfma_f32_16x16x32_bf16 v[188:191], v[152:155], v[100:103], v[188:191]\n\t"
      "v_mfma_f32_16x16x32_bf16 v[192:195], v[152:155], v[104:107], v[192:195]\n\t"
      "v_mfma_f32_16x16x32_bf16 v[196:199], v[152:155], v[108:111], v[196:199]\n\t"
      "s_waitcnt vmcnt(0)\n\t"
      "s_nop 7\n\t"
      "s_branch NQ6B_%=\n\t"
      "FP0R_%=:\n\t"
      "s_mov_b32 s51, 0\n\t"
      "FP0L_%=:\n\t"
      "s_add_u32 s51, s51, 1\n\t"
      "s_cmp_lt_u32 s51, 262144\n\t"
      "s_cbranch_scc0 FP0X_%=\n\t"
      "s_waitcnt vmcnt(0)\n\t"
      "s_sleep 1\n\t"
      "global_load_dwordx4 v[128:131], v224, s[52:53] sc1\n\t"
      "global_load_dwordx4 v[132:135], v225, s[52:53] sc1\n\t"
      "global_load_dwordx4 v[136:139], v226, s[52:53] sc1\n\t"
      "global_load_dwordx4 v[140:143], v227, s[52:53] sc1\n\t"
      "global_load_dwordx4 v[144:147], v228, s[52:53] sc1\n\t"
      "global_load_dwordx4 v[148:151], v229, s[52:53] sc1\n\t"
      "global_load_dwordx4 v[152:155], v230, s[52:53] sc1\n\t"
      "s_waitcnt vmcnt(6)\n\t"
      "v_max3_u32 v234, v128, v129, v130\n\t"
      "v_max_u32_e32 v234, v234, v131\n\t"
      "v_cmp_eq_u32_e32 vcc, -1, v234\n\t"
      "s_cbranch_vccnz FP0L_%=\n\t"
      "FP0X_%=:\n\t"
      "s_branch FP0B_%=\n\t"
      "FP1R_%=:\n\t"
      "s_mov_b32 s51, 0\n\t"
      "FP1L_%=:\n\t"
      "s_add_u32 s51, s51, 1\n\t"
      "s_cmp_lt_u32 s51, 262144\n\t"
      "s_cbranch_scc0 FP1X_%=\n\t"
      "s_waitcnt vmcnt(0)\n\t"
      "s_sleep 1\n\t"
      "global_load_dwordx4 v[132:135], v225, s[52:53] sc1\n\t"
      "global_load_dwordx4 v[136:139], v226, s[52:53] sc1\n\t"
      "global_load_dwordx4 v[140:143], v227, s[52:53] sc1\n\t"
      "global_load_dwordx4 v[144:147], v228, s[52:53] sc1\n\t"
      "global_load_dwordx4 v[148:151], v229, s[52:53] sc1\n\t"
      "global_load_dwordx4 v[152:155], v230, s[52:53] sc1\n\t"
      "global_load_dwordx4 v[128:131], v224, s[54:55] sc1\n\t"
      "s_waitcnt vmcnt(6)\n\t"
      "v_max3_u32 v234, v132, v133, v134\n\t"
      "v_max_u32_e32 v234, v234, v135\n\t"
      "v_cmp_eq_u32_e32 vcc, -1, v234\n\t"
      "s_cbranch_vccnz FP1L_%=\n\t"
      "FP1X_%=:\n\t"
      "s_branch FP1B_%=\n\t"
      "FP2R_%=:\n\t"
      "s_mov_b32 s51, 0\n\t"
      "FP2L_%=:\n\t"
      "s_add_u32 s51, s51, 1\n\t"
      "s_cmp_lt_u32 s51, 262144\n\t"
      "s_cbranch_scc0 FP2X_%=\n\t"
      "s_waitcnt vmcnt(0)\n\t"
      "s_sleep 1\n\t"
      "global_load_dwordx4 v[136:139], v226, s[52:53] sc1\n\t"
      "global_load_dwordx4 v[140:143], v227, s[52:53] sc1\n\t"
      "global_load_dwordx4 v[144:147], v228, s[52:53] sc1\n\t"
      "global_load_dwordx4 v[148:151], v229, s[52:53] sc1\n\t"
      "global_load_dwordx4 v[152:155], v230, s[52:53] sc1\n\t"
      "global_load_dwordx4 v[128:131], v224, s[54:55] sc1\n\t"
      "global_load_dwordx4 v[132:135], v225, s[54:55] sc1\n\t"
      "s_waitcnt vmcnt(6)\n\t"
      "v_max3_u32 v234, v136, v137, v138\n\t"
      "v_max_u32_e32 v234, v234, v139\n\t"
      "v_cmp_eq_u32_e32 vcc, -1, v234\n\t"
      "s_cbranch_vccnz FP2L_%=\n\t"
      "FP2X_%=:\n\t"
      "s_branch FP2B_%=\n\t"
      "FP3R_%=:\n\t"
      "s_mov_b32 s51, 0\n\t"
      "FP3L_%=:\n\t"
      "s_add_u32 s51, s51, 1\n\t"
      "s_cmp_lt_u32 s51, 262144\n\t"
      "s_cbranch_scc0 FP3X_%=\n\t"
      "s_waitcnt vmcnt(0)\n\t"
      "s_sleep 1\n\t"
      "global_load_dwordx4 v[140:143], v227, s[52:53] sc1\n\t"
      "global_load_dwordx4 v[144:147], v228, s[52:53] sc1\n\t"
      "global_load_dwordx4 v[148:151], v229, s[52:53] sc1\n\t"
      "global_load_dwordx4 v[152:155], v230, s[52:53] sc1\n\t"
      "global_load_dwordx4 v[128:131], v224, s[54:55] sc1\n\t"
      "global_load_dwordx4 v[132:135], v225, s[54:55] sc1\n\t"
      "global_load_dwordx4 v[136:139], v226, s[54:55] sc1\n\t"
      "s_waitcnt vmcnt(6)\n\t"
      "v_max3_u32 v234, v140, v141, v142\n\t"
      "v_max_u32_e32 v234, v234, v143\n\t"
      "v_cmp_eq_u32_e32 vcc, -1, v234\n\t"
      "s_cbranch_vccnz FP3L_%=\n\t"
      "FP3X_%=:\n\t"
      "s_branch FP3B_%=\n\t"
      "FP4R_%=:\n\t"
      "s_mov_b32 s51, 0\n\t"
      "FP4L_%=:\n\t"
      "s_add_u32 s51, s51, 1\n\t"
      "s_cmp_lt_u32 s51, 262144\n\t"
      "s_cbranch_scc0 FP4X_%=\n\t"
      "s_waitcnt vmcnt(0)\n\t"
      "s_sleep 1\n\t"
      "global_load_dwordx4 v[144:147], v228, s[52:53] sc1\n\t"
      "global_load_dwordx4 v[148:151], v229, s[52:53] sc1\n\t"
      "global_load_dwordx4 v[152:155], v230, s[52:53] sc1\n\t"
      "global_load_dwordx4 v[128:131], v224, s[54:55] sc1\n\t"
      "global_load_dwordx4 v[132:135], v225, s[54:55] sc1\n\t"
      "global_load_dwordx4 v[136:139], v226, s[54:55] sc1\n\t"
      "global_load_dwordx4 v[140:143], v227, s[54:55] sc1\n\t"
      "s_waitcnt vmcnt(6)\n\t"
      "v_max3_u32 v234, v144, v145, v146\n\t"
      "v_max_u32_e32 v234, v234, v147\n\t"
      "v_cmp_eq_u32_e32 vcc, -1, v234\n\t"
      "s_cbranch_vccnz FP4L_%=\n\t"
      "FP4X_%=:\n\t"
      "s_branch FP4B_%=\n\t"
      "FP5R_%=:\n\t"
      "s_mov_b32 s51, 0\n\t"
      "FP5L_%=:\n\t"
      "s_add_u32 s51, s51, 1\n\t"
      "s_cmp_lt_u32 s51, 262144\n\t"
      "s_cbranch_scc0 FP5X_%=\n\t"
      "s_waitcnt vmcnt(0)\n\t"
      "s_sleep 1\n\t"
      "global_load_dwordx4 v[148:151], v229, s[52:53] sc1\n\t"
      "global_load_dwordx4 v[152:155], v230, s[52:53] sc1\n\t"
      "global_load_dwordx4 v[128:131], v224, s[54:55] sc1\n\t"
      "global_load_dwordx4 v[132:135], v225, s[54:55] sc1\n\t"
      "global_load_dwordx4 v[136:139], v226, s[54:55] sc1\n\t"
      "global_load_dwordx4 v[140:143], v227, s[54:55] sc1\n\t"
      "global_load_dwordx4 v[144:147], v228, s[54:55] sc1\n\t"
      "s_waitcnt vmcnt(6)\n\t"
      "v_max3_u32 v234, v148, v149, v150\n\t"
      "v_max_u32_e32 v234, v234, v151\n\t"
      "v_cmp_eq_u32_e32 vcc, -1, v234\n\t"
      "s_cbranch_vccnz FP5L_%=\n\t"
      "FP5X_%=:\n\t"
      "s_branch FP5B_%=\n\t"
      "FP6R_%=:\n\t"
      "s_mov_b32 s51, 0\n\t"
      "FP6L_%=:\n\t"
      "s_add_u32 s51, s51, 1\n\t"
      "s_cmp_lt_u32 s51, 262144\n\t"
      "s_cbranch_scc0 FP6X_%=\n\t"
      "s_waitcnt vmcnt(0)\n\t"
      "s_sleep 1\n\t"
      "global_load_dwordx4 v[152:155], v230, s[52:53] sc1\n\t"
      "global_load_dwordx4 v[128:131], v224, s[54:55] sc1\n\t"
      "global_load_dwordx4 v[132:135], v225, s[54:55] sc1\n\t"
      "global_load_dwordx4 v[136:139], v226, s[54:55] sc1\n\t"
      "global_load_dwordx4 v[140:143], v227, s[54:55] sc1\n\t"
      "global_load_dwordx4 v[144:147], v228, s[54:55] sc1\n\t"
      "global_load_dwordx4 v[148:151], v229, s[54:55] sc1\n\t"
      "s_waitcnt vmcnt(6)\n\t"
      "v_max3_u32 v234, v152, v153, v154\n\t"
      "v_max_u32_e32 v234, v234, v155\n\t"
      "v_cmp_eq_u32_e32 vcc, -1, v234\n\t"
      "s_cbranch_vccnz FP6L_%=\n\t"
      "FP6X_%=:\n\t"
      "s_branch FP6B_%=\n\t"
      "FP7R_%=:\n\t"
      "s_mov_b32 s51, 0\n\t"
      "FP7L_%=:\n\t"
      "s_add_u32 s51, s51, 1\n\t"
      "s_cmp_lt_u32 s51, 262144\n\t"
      "s_cbranch_scc0 FP7X_%=\n\t"
      "s_waitcnt vmcnt(0)\n\t"
      "s_sleep 4\n\t"
      "global_load_dwordx4 v[128:131], v224, s[54:55] sc1\n\t"
      "global_load_dwordx4 v[132:135], v225, s[54:55] sc1\n\t"
      "global_load_dwordx4 v[136:139], v226, s[54:55] sc1\n\t"
      "global_load_dwordx4 v[140:143], v227, s[54:55] sc1\n\t"
      "global_load_dwordx4 v[144:147], v228, s[54:55] sc1\n\t"
      "global_load_dwordx4 v[148:151], v229, s[54:55] sc1\n\t"
      "s_waitcnt vmcnt(5)\n\t"
      "v_max3_u32 v234, v128, v129, v130\n\t"
      "v_max_u32_e32 v234, v234, v131\n\t"
      "v_cmp_eq_u32_e32 vcc, -1, v234\n\t"
      "s_cbranch_vccnz FP7L_%=\n\t"
      "FP7X_%=:\n\t"
      "s_branch FP7B_%=\n\t"
      "FP8R_%=:\n\t"
      "s_mov_b32 s51, 0\n\t"
      "FP8L_%=:\n\t"
      "s_add_u32 s51, s51, 1\n\t"
      "s_cmp_lt_u32 s51, 262144\n\t"
      "s_cbranch_scc0 FP8X_%=\n\t"
      "s_waitcnt vmcnt(0)\n\t"
      "s_sleep 4\n\t"
      "global_load_dwordx4 v[132:135], v225, s[54:55] sc1\n\t"
      "global_load_dwordx4 v[136:139], v226, s[54:55] sc1\n\t"
      "global_load_dwordx4 v[140:143], v227, s[54:55] sc1\n\t"
      "global_load_dwordx4 v[144:147], v228, s[54:55] sc1\n\t"
      "global_load_dwordx4 v[148:151], v229, s[54:55] sc1\n\t"
      "global_load_dwordx4 v[152:155], v230, s[54:55] sc1\n\t"
      "s_waitcnt vmcnt(5)\n\t"
      "v_max3_u32 v234, v132, v133, v134\n\t"
      "v_max_u32_e32 v234, v234, v135\n\t"
      "v_cmp_eq_u32_e32 vcc, -1, v234\n\t"
      "s_cbranch_vccnz FP8L_%=\n\t"
      "FP8X_%=:\n\t"
      "s_branch FP8B_%=\n\t"
      "FP9R_%=:\n\t"
      "s_mov_b32 s51, 0\n\t"
      "FP9L_%=:\n\t"
      "s_add_u32 s51, s51, 1\n\t"
      "s_cmp_lt_u32 s51, 262144\n\t"
      "s_cbranch_scc0 FP9X_%=\n\t"
      "s_waitcnt vmcnt(0)\n\t"
      "s_sleep 4\n\t"
      "global_load_dwordx4 v[136:139], v226, s[54:55] sc1\n\t"
      "global_load_dwordx4 v[140:143], v227, s[54:55] sc1\n\t"
      "global_load_dwordx4 v[144:147], v228, s[54:55] sc1\n\t"
      "global_load_dwordx4 v[148:151], v229, s[54:55] sc1\n\t"
      "global_load_dwordx4 v[152:155], v230, s[54:55] sc1\n\t"
      "global_load_dwordx4 v[128:131], v224, s[56:57] sc1\n\t"
      "s_waitcnt vmcnt(5)\n\t"
      "v_max3_u32 v234, v136, v137, v138\n\t"
      "v_max_u32_e32 v234, v234, v139\n\t"
      "v_cmp_eq_u32_e32 vcc, -1, v234\n\t"
      "s_cbranch_vccnz FP9L_%=\n\t"
      "FP9X_%=:\n\t"
      "s_branch FP9B_%=\n\t"
      "FP10R_%=:\n\t"
      "s_mov_b32 s51, 0\n\t"
      "FP10L_%=:\n\t"
      "s_add_u32 s51, s51, 1\n\t"
      "s_cmp_lt_u32 s51, 262144\n\t"
      "s_cbranch_scc0 FP10X_%=\n\t"
      "s_waitcnt vmcnt(0)\n\t"
      "s_sleep 4\n\t"
      "global_load_dwordx4 v[140:143], v227, s[54:55] sc1\n\t"
      "global_load_dwordx4 v[144:147], v228, s[54:55] sc1\n\t"
      "global_load_dwordx4 v[148:151], v229, s[54:55] sc1\n\t"
      "global_load_dwordx4 v[152:155], v230, s[54:55] sc1\n\t"
      "global_load_dwordx4 v[128:131], v224, s[56:57] sc1\n\t"
      "global_load_dwordx4 v[132:135], v225, s[56:57] sc1\n\t"
      "s_waitcnt vmcnt(5)\n\t"
      "v_max3_u32 v234, v140, v141, v142\n\t"
      "v_max_u32_e32 v234, v234, v143\n\t"
      "v_cmp_eq_u32_e32 vcc, -1, v234\n\t"
      "s_cbranch_vccnz FP10L_%=\n\t"
      "FP10X_%=:\n\t"
      "s_branch FP10B_%=\n\t"
      "FP11R_%=:\n\t"
      "s_mov_b32 s51, 0\n\t"
      "FP11L_%=:\n\t"
      "s_add_u32 s51, s51, 1\n\t"
      "s_cmp_lt_u32 s51, 262144\n\t"
      "s_cbranch_scc0 FP11X_%=\n\t"
      "s_waitcnt vmcnt(0)\n\t"
      "s_sleep 4\n\t"
      "global_load_dwordx4 v[144:147], v228, s[54:55] sc1\n\t"
      "global_load_dwordx4 v[148:151], v229, s[54:55] sc1\n\t"
      "global_load_dwordx4 v[152:155], v230, s[54:55] sc1\n\t"
      "global_load_dwordx4 v[128:131], v224, s[56:57] sc1\n\t"
      "global_load_dwordx4 v[132:135], v225, s[56:57] sc1\n\t"
      "global_load_dwordx4 v[136:139], v226, s[56:57] sc1\n\t"
      "s_waitcnt vmcnt(5)\n\t"
      "v_max3_u32 v234, v144, v145, v146\n\t"
      "v_max_u32_e32 v234, v234, v147\n\t"
      "v_cmp_eq_u32_e32 vcc, -1, v234\n\t"
      "s_cbranch_vccnz FP11L_%=\n\t"
      "FP11X_%=:\n\t"
      "s_branch FP11B_%=\n\t"
      "FP12R_%=:\n\t"
      "s_mov_b32 s51, 0\n\t"
      "FP12L_%=:\n\t"
      "s_add_u32 s51, s51, 1\n\t"
      "s_cmp_lt_u32 s51, 262144\n\t"
      "s_cbranch_scc0 FP12X_%=\n\t"
      "s_waitcnt vmcnt(0)\n\t"
      "s_sleep 4\n\t"
      "global_load_dwordx4 v[148:151], v229, s[54:55] sc1\n\t"
      "global_load_dwordx4 v[152:155], v230, s[54:55] sc1\n\t"
      "global_load_dwordx4 v[128:131], v224, s[56:57] sc1\n\t"
      "global_load_dwordx4 v[132:135], v225, s[56:57] sc1\n\t"
      "global_load_dwordx4 v[136:139], v226, s[56:57] sc1\n\t"
      "global_load_dwordx4 v[140:143], v227, s[56:57] sc1\n\t"
      "s_waitcnt vmcnt(5)\n\t"
      "v_max3_u32 v234, v148, v149, v150\n\t"
      "v_max_u32_e32 v234, v234, v151\n\t"
      "v_cmp_eq_u32_e32 vcc, -1, v234\n\t"
      "s_cbranch_vccnz FP12L_%=\n\t"
      "FP12X_%=:\n\t"
      "s_branch FP12B_%=\n\t"
      "FP13R_%=:\n\t"
      "s_mov_b32 s51, 0\n\t"
      "FP13L_%=:\n\t"
      "s_add_u32 s51, s51, 1\n\t"
      "s_cmp_lt_u32 s51, 262144\n\t"
      "s_cbranch_scc0 FP13X_%=\n\t"
      "s_waitcnt vmcnt(0)\n\t"
      "s_sleep 4\n\t"
      "global_load_dwordx4 v[152:155], v230, s[54:55] sc1\n\t"
      "global_load_dwordx4 v[128:131], v224, s[56:57] sc1\n\t"
      "global_load_dwordx4 v[132:135], v225, s[56:57] sc1\n\t"
      "global_load_dwordx4 v[136:139], v226, s[56:57] sc1\n\t"
      "global_load_dwordx4 v[140:143], v227, s[56:57] sc1\n\t"
      "global_load_dwordx4 v[144:147], v228, s[56:57] sc1\n\t"
      "s_waitcnt vmcnt(5)\n\t"
      "v_max3_u32 v234, v152, v153, v154\n\t"
      "v_max_u32_e32 v234, v234, v155\n\t"
      "v_cmp_eq_u32_e32 vcc, -1, v234\n\t"
      "s_cbranch_vccnz FP13L_%=\n\t"
      "FP13X_%=:\n\t"
      "s_waitcnt vmcnt(0)\n\t"
      "s_branch FP13B_%=\n\t"
      "FP14R_%=:\n\t"
      "s_mov_b32 s51, 0\n\t"
      "FP14L_%=:\n\t"
      "s_add_u32 s51, s51, 1\n\t"
      "s_cmp_lt_u32 s51, 262144\n\t"
      "s_cbranch_scc0 FP14X_%=\n\t"
      "s_waitcnt vmcnt(0)\n\t"
      "s_sleep 4\n\t"
      "global_load_dwordx4 v[128:131], v224, s[56:57] sc1\n\t"
      "global_load_dwordx4 v[132:135], v225, s[56:57] sc1\n\t"
      "global_load_dwordx4 v[136:139], v226, s[56:57] sc1\n\t"
      "global_load_dwordx4 v[140:143], v227, s[56:57] sc1\n\t"
      "global_load_dwordx4 v[144:147], v228, s[56:57] sc1\n\t"
      "global_load_dwordx4 v[148:151], v229, s[56:57] sc1\n\t"
      "s_waitcnt vmcnt(5)\n\t"
      "v_max3_u32 v234, v128, v129, v130\n\t"
      "v_max_u32_e32 v234, v234, v131\n\t"
      "v_cmp_eq_u32_e32 vcc, -1, v234\n\t"
      "s_cbranch_vccnz FP14L_%=\n\t"
      "FP14X_%=:\n\t"
      "s_waitcnt vmcnt(0)\n\t"
      "s_branch FP14B_%=\n\t"
      "NP1R_%=:\n\t"
      "s_mov_b32 s51, 0\n\t"
      "NP1L_%=:\n\t"
      "s_add_u32 s51, s51, 1\n\t"
      "s_cmp_lt_u32 s51, 262144\n\t"
      "s_cbranch_scc0 NP1X_%=\n\t"
      "s_waitcnt vmcnt(0)\n\t"
      "s_sleep 1\n\t"
      "global_load_dwordx4 v[128:131], v224, s[54:55] sc1\n\t"
      "global_load_dwordx4 v[132:135], v225, s[54:55] sc1\n\t"
      "global_load_dwordx4 v[136:139], v226, s[54:55] sc1\n\t"
      "global_load_dwordx4 v[140:143], v227, s[54:55] sc1\n\t"
      "global_load_dwordx4 v[144:147], v228, s[54:55] sc1\n\t"
      "global_load_dwordx4 v[148:151], v229, s[54:55] sc1\n\t"
      "global_load_dwordx4 v[152:155], v230, s[54:55] sc1\n\t"
      "s_waitcnt vmcnt(0)\n\t"
      "v_max3_u32 v234, v128, v129, v130\n\t"
      "v_max3_u32 v234, v234, v131, v132\n\t"
      "v_max3_u32 v234, v234, v133, v134\n\t"
      "v_max3_u32 v234, v234, v135, v136\n\t"
      "v_max3_u32 v234, v234, v137, v138\n\t"
      "v_max3_u32 v234, v234, v139, v140\n\t"
      "v_max3_u32 v234, v234, v141, v142\n\t"
      "v_max3_u32 v234, v234, v143, v144\n\t"
      "v_max3_u32 v234, v234, v145, v146\n\t"
      "v_max3_u32 v234, v234, v147, v148\n\t"
      "v_max3_u32 v234, v234, v149, v150\n\t"
      "v_max3_u32 v234, v234, v151, v152\n\t"
      "v_max3_u32 v234, v234, v153, v154\n\t"
      "v_max_u32_e32 v234, v234, v155\n\t"
      "v_cmp_eq_u32_e32 vcc, -1, v234\n\t"
      "s_cbranch_vccnz NP1L_%=\n\t"
      "NP1X_%=:\n\t"
      "v_mfma_f32_16x16x32_bf16 v[200:203], v[128:131], v[0:3], v[160:163]\n\t"
      "v_mfma_f32_16x16x32_bf16 v[204:207], v[128:131], v[4:7], 0\n\t"
      "v_mfma_f32_16x16x32_bf16 v[208:211], v[128:131], v[8:11], 0\n\t"
      "v_mfma_f32_16x16x32_bf16 v[212:215], v[128:131], v[12:15], 0\n\t"
      "v_mfma_f32_16x16x32_bf16 v[200:203], v[132:135], v[16:19], v[200:203]\n\t"
      "v_mfma_f32_16x16x32_bf16 v[204:207], v[132:135], v[20:23], v[204:207]\n\t"
      "v_mfma_f32_16x16x32_bf16 v[208:211], v[132:135], v[24:27], v[208:211]\n\t"
      "v_mfma_f32_16x16x32_bf16 v[212:215], v[132:135], v[28:31], v[212:215]\n\t"
      "v_mfma_f32_16x16x32_bf16 v[200:203], v[136:139], v[32:35], v[200:203]\n\t"
      "v_mfma_f32_16x16x32_bf16 v[204:207], v[136:139], v[36:39], v[204:207]\n\t"
      "v_mfma_f32_16x16x32_bf16 v[208:211], v[136:139], v[40:43], v[208:211]\n\t"
      "v_mfma_f32_16x16x32_bf16 v[212:215], v[136:139], v[44:47], v[212:215]\n\t"
      "v_mfma_f32_16x16x32_bf16 v[200:203], v[140:143], v[48:51], v[200:203]\n\t"
      "v_mfma_f32_16x16x32_bf16 v[204:207], v[140:143], v[52:55], v[204:207]\n\t"
      "v_mfma_f32_16x16x32_bf16 v[208:211], v[140:143], v[56:59], v[208:211]\n\t"
      "v_mfma_f32_16x16x32_bf16 v[212:215], v[140:143], v[60:63], v[212:215]\n\t"
      "v_mfma_f32_16x16x32_bf16 v[200:203], v[144:147], v[64:67], v[200:203]\n\t"
      "v_mfma_f32_16x16x32_bf16 v[204:207], v[144:147], v[68:71], v[204:207]\n\t"
      "v_mfma_f32_16x16x32_bf16 v[208:211], v[144:147], v[72:75], v[208:211]\n\t"
      "v_mfma_f32_16x16x32_bf16 v[212:215], v[144:147], v[76:79], v[212:215]\n\t"
      "v_mfma_f32_16x16x32_bf16 v[200:203], v[148:151], v[80:83], v[200:203]\n\t"
      "v_mfma_f32_16x16x32_bf16 v[204:207], v[148:151], v[84:87], v[204:207]\n\t"
      "v_mfma_f32_16x16x32_bf16 v[208:211], v[148:151], v[88:91], v[208:211]\n\t"
      "v_mfma_f32_16x16x32_bf16 v[212:215], v[148:151], v[92:95], v[212:215]\n\t"
      "v_mfma_f32_16x16x32_bf16 v[200:203], v[152:155], v[96:99], v[200:203]\n\t"
      "v_mfma_f32_16x16x32_bf16 v[204:207], v[152:155], v[100:103], v[204:207]\n\t"
      "v_mfma_f32_16x16x32_bf16 v[208:211], v[152:155], v[104:107], v[208:211]\n\t"
      "v_mfma_f32_16x16x32_bf16 v[212:215], v[152:155], v[108:111], v[212:215]\n\t"
      "global_load_dwordx4 v[128:131], v224, s[56:57] sc1\n\t"
      "global_load_dwordx4 v[132:135], v225, s[56:57] sc1\n\t"
      "global_load_dwordx4 v[136:139], v226, s[56:57] sc1\n\t"
      "global_load_dwordx4 v[140:143], v227, s[56:57] sc1\n\t"
      "global_load_dwordx4 v[144:147], v228, s[56:57] sc1\n\t"
      "global_load_dwordx4 v[148:151], v229, s[56:57] sc1\n\t"
      "global_load_dwordx4 v[152:155], v230, s[56:57] sc1\n\t"
      "s_waitcnt vmcnt(0)\n\t"
      "s_nop 7\n\t"
      "s_branch NP1B_%=\n\t"
      "FP15R_%=:\n\t"
      "s_mov_b32 s51, 0\n\t"
      "FP15L_%=:\n\t"
      "s_add_u32 s51, s51, 1\n\t"
      "s_cmp_lt_u32 s51, 262144\n\t"
      "s_cbranch_scc0 FP15X_%=\n\t"
      "s_waitcnt vmcnt(0)\n\t"
      "s_sleep 4\n\t"
      "global_load_dwordx4 v[132:135], v225, s[56:57] sc1\n\t"
      "global_load_dwordx4 v[136:139], v226, s[56:57] sc1\n\t"
      "global_load_dwordx4 v[140:143], v227, s[56:57] sc1\n\t"
      "global_load_dwordx4 v[144:147], v228, s[56:57] sc1\n\t"
      "global_load_dwordx4 v[148:151], v229, s[56:57] sc1\n\t"
      "global_load_dwordx4 v[152:155], v230, s[56:57] sc1\n\t"
      "s_waitcnt vmcnt(5)\n\t"
      "v_max3_u32 v234, v132, v133, v134\n\t"
      "v_max_u32_e32 v234, v234, v135\n\t"
      "v_cmp_eq_u32_e32 vcc, -1, v234\n\t"
      "s_cbranch_vccnz FP15L_%=\n\t"
      "FP15X_%=:\n\t"
      "s_waitcnt vmcnt(0)\n\t"
      "s_branch FP15B_%=\n\t"
      "FP16R_%=:\n\t"
      "s_mov_b32 s51, 0\n\t"
      "FP16L_%=:\n\t"
      "s_add_u32 s51, s51, 1\n\t"
      "s_cmp_lt_u32 s51, 262144\n\t"
      "s_cbranch_scc0 FP16X_%=\n\t"
      "s_waitcnt vmcnt(0)\n\t"
      "s_sleep 4\n\t"
      "global_load_dwordx4 v[136:139], v226, s[56:57] sc1\n\t"
      "global_load_dwordx4 v[140:143], v227, s[56:57] sc1\n\t"
      "global_load_dwordx4 v[144:147], v228, s[56:57] sc1\n\t"
      "global_load_dwordx4 v[148:151], v229, s[56:57] sc1\n\t"
      "global_load_dwordx4 v[152:155], v230, s[56:57] sc1\n\t"
      "global_load_dwordx4 v[128:131], v224, s[58:59] sc1\n\t"
      "s_waitcnt vmcnt(5)\n\t"
      "v_max3_u32 v234, v136, v137, v138\n\t"
      "v_max_u32_e32 v234, v234, v139\n\t"
      "v_cmp_eq_u32_e32 vcc, -1, v234\n\t"
      "s_cbranch_vccnz FP16L_%=\n\t"
      "FP16X_%=:\n\t"
      "s_waitcnt vmcnt(0)\n\t"
      "s_branch FP16B_%=\n\t"
      "FP17R_%=:\n\t"
      "s_mov_b32 s51, 0\n\t"
      "FP17L_%=:\n\t"
      "s_add_u32 s51, s51, 1\n\t"
      "s_cmp_lt_u32 s51, 262144\n\t"
      "s_cbranch_scc0 FP17X_%=\n\t"
      "s_waitcnt vmcnt(0)\n\t"
      "s_sleep 4\n\t"
      "global_load_dwordx4 v[140:143], v227, s[56:57] sc1\n\t"
      "global_load_dwordx4 v[144:147], v228, s[56:57] sc1\n\t"
      "global_load_dwordx4 v[148:151], v229, s[56:57] sc1\n\t"
      "global_load_dwordx4 v[152:155], v230, s[56:57] sc1\n\t"
      "global_load_dwordx4 v[128:131], v224, s[58:59] sc1\n\t"
      "global_load_dwordx4 v[132:135], v225, s[58:59] sc1\n\t"
      "s_waitcnt vmcnt(5)\n\t"
      "v_max3_u32 v234, v140, v141, v142\n\t"
      "v_max_u32_e32 v234, v234, v143\n\t"
      "v_cmp_eq_u32_e32 vcc, -1, v234\n\t"
      "s_cbranch_vccnz FP17L_%=\n\t"
      "FP17X_%=:\n\t"
      "s_waitcnt vmcnt(0)\n\t"
      "s_branch FP17B_%=\n\t"
      "FP18R_%=:\n\t"
      "s_mov_b32 s51, 0\n\t"
      "FP18L_%=:\n\t"
      "s_add_u32 s51, s51, 1\n\t"
      "s_cmp_lt_u32 s51, 262144\n\t"
      "s_cbranch_scc0 FP18X_%=\n\t"
      "s_waitcnt vmcnt(0)\n\t"
      "s_sleep 4\n\t"
      "global_load_dwordx4 v[144:147], v228, s[56:57] sc1\n\t"
      "global_load_dwordx4 v[148:151], v229, s[56:57] sc1\n\t"
      "global_load_dwordx4 v[152:155], v230, s[56:57] sc1\n\t"
      "global_load_dwordx4 v[128:131], v224, s[58:59] sc1\n\t"
      "global_load_dwordx4 v[132:135], v225, s[58:59] sc1\n\t"
      "global_load_dwordx4 v[136:139], v226, s[58:59] sc1\n\t"
      "s_waitcnt vmcnt(5)\n\t"
      "v_max3_u32 v234, v144, v145, v146\n\t"
      "v_max_u32_e32 v234, v234, v147\n\t"
      "v_cmp_eq_u32_e32 vcc, -1, v234\n\t"
      "s_cbranch_vccnz FP18L_%=\n\t"
      "FP18X_%=:\n\t"
      "s_waitcnt vmcnt(0)\n\t"
      "s_branch FP18B_%=\n\t"
      "FP19R_%=:\n\t"
      "s_mov_b32 s51, 0\n\t"
      "FP19L_%=:\n\t"
      "s_add_u32 s51, s51, 1\n\t"
      "s_cmp_lt_u32 s51, 262144\n\t"
      "s_cbranch_scc0 FP19X_%=\n\t"
      "s_waitcnt vmcnt(0)\n\t"
      "s_sleep 4\n\t"
      "global_load_dwordx4 v[148:151], v229, s[56:57] sc1\n\t"
      "global_load_dwordx4 v[152:155], v230, s[56:57] sc1\n\t"
      "global_load_dwordx4 v[128:131], v224, s[58:59] sc1\n\t"
      "global_load_dwordx4 v[132:135], v225, s[58:59] sc1\n\t"
      "global_load_dwordx4 v[136:139], v226, s[58:59] sc1\n\t"
      "global_load_dwordx4 v[140:143], v227, s[58:59] sc1\n\t"
      "s_waitcnt vmcnt(5)\n\t"
      "v_max3_u32 v234, v148, v149, v150\n\t"
      "v_max_u32_e32 v234, v234, v151\n\t"
      "v_cmp_eq_u32_e32 vcc, -1, v234\n\t"
      "s_cbranch_vccnz FP19L_%=\n\t"
      "FP19X_%=:\n\t"
      "s_branch FP19B_%=\n\t"
      "FP20R_%=:\n\t"
      "s_mov_b32 s51, 0\n\t"
      "FP20L_%=:\n\t"
      "s_add_u32 s51, s51, 1\n\t"
      "s_cmp_lt_u32 s51, 262144\n\t"
      "s_cbranch_scc0 FP20X_%=\n\t"
      "s_waitcnt vmcnt(0)\n\t"
      "s_sleep 4\n\t"
      "global_load_dwordx4 v[152:155], v230, s[56:57] sc1\n\t"
      "global_load_dwordx4 v[128:131], v224, s[58:59] sc1\n\t"
      "global_load_dwordx4 v[132:135], v225, s[58:59] sc1\n\t"
      "global_load_dwordx4 v[136:139], v226, s[58:59] sc1\n\t"
      "global_load_dwordx4 v[140:143], v227, s[58:59] sc1\n\t"
      "global_load_dwordx4 v[144:147], v228, s[58:59] sc1\n\t"
      "s_waitcnt vmcnt(5)\n\t"
      "v_max3_u32 v234, v152, v153, v154\n\t"
      "v_max_u32_e32 v234, v234, v155\n\t"
      "v_cmp_eq_u32_e32 vcc, -1, v234\n\t"
      "s_cbranch_vccnz FP20L_%=\n\t"
      "FP20X_%=:\n\t"
      "s_waitcnt vmcnt(0)\n\t"
      "s_branch FP20B_%=\n\t"
      "FP21R_%=:\n\t"
      "s_mov_b32 s51, 0\n\t"
      "FP21L_%=:\n\t"
      "s_add_u32 s51, s51, 1\n\t"
      "s_cmp_lt_u32 s51, 262144\n\t"
      "s_cbranch_scc0 FP21X_%=\n\t"
      "s_waitcnt vmcnt(0)\n\t"
      "s_sleep 4\n\t"
      "global_load_dwordx4 v[128:131], v224, s[58:59] sc1\n\t"
      "global_load_dwordx4 v[132:135], v225, s[58:59] sc1\n\t"
      "global_load_dwordx4 v[136:139], v226, s[58:59] sc1\n\t"
      "global_load_dwordx4 v[140:143], v227, s[58:59] sc1\n\t"
      "global_load_dwordx4 v[144:147], v228, s[58:59] sc1\n\t"
      "global_load_dwordx4 v[148:151], v229, s[58:59] sc1\n\t"
      "s_waitcnt vmcnt(5)\n\t"
      "v_max3_u32 v234, v128, v129, v130\n\t"
      "v_max_u32_e32 v234, v234, v131\n\t"
      "v_cmp_eq_u32_e32 vcc, -1, v234\n\t"
      "s_cbranch_vccnz FP21L_%=\n\t"
      "FP21X_%=:\n\t"
      "s_waitcnt vmcnt(0)\n\t"
      "s_branch FP21B_%=\n\t"
      "NP2R_%=:\n\t"
      "s_mov_b32 s51, 0\n\t"
      "NP2L_%=:\n\t"
      "s_add_u32 s51, s51, 1\n\t"
      "s_cmp_lt_u32 s51, 262144\n\t"
      "s_cbranch_scc0 NP2X_%=\n\t"
      "s_waitcnt vmcnt(0)\n\t"
      "s_sleep 1\n\t"
      "global_load_dwordx4 v[128:131], v224, s[56:57] sc1\n\t"
      "global_load_dwordx4 v[132:135], v225, s[56:57] sc1\n\t"
      "global_load_dwordx4 v[136:139], v226, s[56:57] sc1\n\t"
      "global_load_dwordx4 v[140:143], v227, s[56:57] sc1\n\t"
      "global_load_dwordx4 v[144:147], v228, s[56:57] sc1\n\t"
      "global_load_dwordx4 v[148:151], v229, s[56:57] sc1\n\t"
      "global_load_dwordx4 v[152:155], v230, s[56:57] sc1\n\t"
      "s_waitcnt vmcnt(0)\n\t"
      "v_max3_u32 v234, v128, v129, v130\n\t"
      "v_max3_u32 v234, v234, v131, v132\n\t"
      "v_max3_u32 v234, v234, v133, v134\n\t"
      "v_max3_u32 v234, v234, v135, v136\n\t"
      "v_max3_u32 v234, v234, v137, v138\n\t"
      "v_max3_u32 v234, v234, v139, v140\n\t"
      "v_max3_u32 v234, v234, v141, v142\n\t"
      "v_max3_u32 v234, v234, v143, v144\n\t"
      "v_max3_u32 v234, v234, v145, v146\n\t"
      "v_max3_u32 v234, v234, v147, v148\n\t"
      "v_max3_u32 v234, v234, v149, v150\n\t"
      "v_max3_u32 v234, v234, v151, v152\n\t"
      "v_max3_u32 v234, v234, v153, v154\n\t"
      "v_max_u32_e32 v234, v234, v155\n\t"
      "v_cmp_eq_u32_e32 vcc, -1, v234\n\t"
      "s_cbranch_vccnz NP2L_%=\n\t"
      "NP2X_%=:\n\t"
      "v_mfma_f32_16x16x32_bf16 v[184:187], v[128:131], v[0:3], v[164:167]\n\t"
      "v_mfma_f32_16x16x32_bf16 v[188:191], v[128:131], v[4:7], 0\n\t"
      "v_mfma_f32_16x16x32_bf16 v[192:195], v[128:131], v[8:11], 0\n\t"
      "v_mfma_f32_16x16x32_bf16 v[196:199], v[128:131], v[12:15], 0\n\t"
      "v_mfma_f32_16x16x32_bf16 v[184:187], v[132:135], v[16:19], v[184:187]\n\t"
      "v_mfma_f32_16x16x32_bf16 v[188:191], v[132:135], v[20:23], v[188:191]\n\t"
      "v_mfma_f32_16x16x32_bf16 v[192:195], v[132:135], v[24:27], v[192:195]\n\t"
      "v_mfma_f32_16x16x32_bf16 v[196:199], v[132:135], v[28:31], v[196:199]\n\t"
      "v_mfma_f32_16x16x32_bf16 v[184:187], v[136:139], v[32:35], v[184:187]\n\t"
      "v_mfma_f32_16x16x32_bf16 v[188:191], v[136:139], v[36:39], v[188:191]\n\t"
      "v_mfma_f32_16x16x32_bf16 v[192:195], v[136:139], v[40:43], v[192:195]\n\t"
      "v_mfma_f32_16x16x32_bf16 v[196:199], v[136:139], v[44:47], v[196:199]\n\t"
      "v_mfma_f32_16x16x32_bf16 v[184:187], v[140:143], v[48:51], v[184:187]\n\t"
      "v_mfma_f32_16x16x32_bf16 v[188:191], v[140:143], v[52:55], v[188:191]\n\t"
      "v_mfma_f32_16x16x32_bf16 v[192:195], v[140:143], v[56:59], v[192:195]\n\t"
      "v_mfma_f32_16x16x32_bf16 v[196:199], v[140:143], v[60:63], v[196:199]\n\t"
      "v_mfma_f32_16x16x32_bf16 v[184:187], v[144:147], v[64:67], v[184:187]\n\t"
      "v_mfma_f32_16x16x32_bf16 v[188:191], v[144:147], v[68:71], v[188:191]\n\t"
      "v_mfma_f32_16x16x32_bf16 v[192:195], v[144:147], v[72:75], v[192:195]\n\t"
      "v_mfma_f32_16x16x32_bf16 v[196:199], v[144:147], v[76:79], v[196:199]\n\t"
      "v_mfma_f32_16x16x32_bf16 v[184:187], v[148:151], v[80:83], v[184:187]\n\t"
      "v_mfma_f32_16x16x32_bf16 v[188:191], v[148:151], v[84:87], v[188:191]\n\t"
      "v_mfma_f32_16x16x32_bf16 v[192:195], v[148:151], v[88:91], v[192:195]\n\t"
      "v_mfma_f32_16x16x32_bf16 v[196:199], v[148:151], v[92:95], v[196:199]\n\t"
      "v_mfma_f32_16x16x32_bf16 v[184:187], v[152:155], v[96:99], v[184:187]\n\t"
      "v_mfma_f32_16x16x32_bf16 v[188:191], v[152:155], v[100:103], v[188:191]\n\t"
      "v_mfma_f32_16x16x32_bf16 v[192:195], v[152:155], v[104:107], v[192:195]\n\t"
      "v_mfma_f32_16x16x32_bf16 v[196:199], v[152:155], v[108:111], v[196:199]\n\t"
      "global_load_dwordx4 v[128:131], v224, s[58:59] sc1\n\t"
      "global_load_dwordx4 v[132:135], v225, s[58:59] sc1\n\t"
      "global_load_dwordx4 v[136:139], v226, s[58:59] sc1\n\t"
      "global_load_dwordx4 v[140:143], v227, s[58:59] sc1\n\t"
      "global_load_dwordx4 v[144:147], v228, s[58:59] sc1\n\t"
      "global_load_dwordx4 v[148:151], v229, s[58:59] sc1\n\t"
      "global_load_dwordx4 v[152:155], v230, s[58:59] sc1\n\t"
      "s_waitcnt vmcnt(0)\n\t"
      "s_nop 7\n\t"
      "s_branch NP2B_%=\n\t"
      "FP22R_%=:\n\t"
      "s_mov_b32 s51, 0\n\t"
      "FP22L_%=:\n\t"
      "s_add_u32 s51, s51, 1\n\t"
      "s_cmp_lt_u32 s51, 262144\n\t"
      "s_cbranch_scc0 FP22X_%=\n\t"
      "s_waitcnt vmcnt(0)\n\t"
      "s_sleep 4\n\t"
      "global_load_dwordx4 v[132:135], v225, s[58:59] sc1\n\t"
      "global_load_dwordx4 v[136:139], v226, s[58:59] sc1\n\t"
      "global_load_dwordx4 v[140:143], v227, s[58:59] sc1\n\t"
      "global_load_dwordx4 v[144:147], v228, s[58:59] sc1\n\t"
      "global_load_dwordx4 v[148:151], v229, s[58:59] sc1\n\t"
      "global_load_dwordx4 v[152:155], v230, s[58:59] sc1\n\t"
      "s_waitcnt vmcnt(5)\n\t"
      "v_max3_u32 v234, v132, v133, v134\n\t"
      "v_max_u32_e32 v234, v234, v135\n\t"
      "v_cmp_eq_u32_e32 vcc, -1, v234\n\t"
      "s_cbranch_vccnz FP22L_%=\n\t"
      "FP22X_%=:\n\t"
      "s_waitcnt vmcnt(0)\n\t"
      "s_branch FP22B_%=\n\t"
      "FP23R_%=:\n\t"
      "s_mov_b32 s51, 0\n\t"
      "FP23L_%=:\n\t"
      "s_add_u32 s51, s51, 1\n\t"
      "s_cmp_lt_u32 s51, 262144\n\t"
      "s_cbranch_scc0 FP23X_%=\n\t"
      "s_waitcnt vmcnt(0)\n\t"
      "s_sleep 4\n\t"
      "global_load_dwordx4 v[136:139], v226, s[58:59] sc1\n\t"
      "global_load_dwordx4 v[140:143], v227, s[58:59] sc1\n\t"
      "global_load_dwordx4 v[144:147], v228, s[58:59] sc1\n\t"
      "global_load_dwordx4 v[148:151], v229, s[58:59] sc1\n\t"
      "global_load_dwordx4 v[152:155], v230, s[58:59] sc1\n\t"
      "global_load_dwordx4 v[128:131], v224, s[60:61] sc1\n\t"
      "s_waitcnt vmcnt(5)\n\t"
      "v_max3_u32 v234, v136, v137, v138\n\t"
      "v_max_u32_e32 v234, v234, v139\n\t"
      "v_cmp_eq_u32_e32 vcc, -1, v234\n\t"
      "s_cbranch_vccnz FP23L_%=\n\t"
      "FP23X_%=:\n\t"
      "s_waitcnt vmcnt(0)\n\t"
      "s_branch FP23B_%=\n\t"
      "FP24R_%=:\n\t"
      "s_mov_b32 s51, 0\n\t"
      "FP24L_%=:\n\t"
      "s_add_u32 s51, s51, 1\n\t"
      "s_cmp_lt_u32 s51, 262144\n\t"
      "s_cbranch_scc0 FP24X_%=\n\t"
      "s_waitcnt vmcnt(0)\n\t"
      "s_sleep 4\n\t"
      "global_load_dwordx4 v[140:143], v227, s[58:59] sc1\n\t"
      "global_load_dwordx4 v[144:147], v228, s[58:59] sc1\n\t"
      "global_load_dwordx4 v[148:151], v229, s[58:59] sc1\n\t"
      "global_load_dwordx4 v[152:155], v230, s[58:59] sc1\n\t"
      "global_load_dwordx4 v[128:131], v224, s[60:61] sc1\n\t"
      "global_load_dwordx4 v[132:135], v225, s[60:61] sc1\n\t"
      "s_waitcnt vmcnt(5)\n\t"
      "v_max3_u32 v234, v140, v141, v142\n\t"
      "v_max_u32_e32 v234, v234, v143\n\t"
      "v_cmp_eq_u32_e32 vcc, -1, v234\n\t"
      "s_cbranch_vccnz FP24L_%=\n\t"
      "FP24X_%=:\n\t"
      "s_waitcnt vmcnt(0)\n\t"
      "s_branch FP24B_%=\n\t"
      "FP25R_%=:\n\t"
      "s_mov_b32 s51, 0\n\t"
      "FP25L_%=:\n\t"
      "s_add_u32 s51, s51, 1\n\t"
      "s_cmp_lt_u32 s51, 262144\n\t"
      "s_cbranch_scc0 FP25X_%=\n\t"
      "s_waitcnt vmcnt(0)\n\t"
      "s_sleep 4\n\t"
      "global_load_dwordx4 v[144:147], v228, s[58:59] sc1\n\t"
      "global_load_dwordx4 v[148:151], v229, s[58:59] sc1\n\t"
      "global_load_dwordx4 v[152:155], v230, s[58:59] sc1\n\t"
      "global_load_dwordx4 v[128:131], v224, s[60:61] sc1\n\t"
      "global_load_dwordx4 v[132:135], v225, s[60:61] sc1\n\t"
      "global_load_dwordx4 v[136:139], v226, s[60:61] sc1\n\t"
      "s_waitcnt vmcnt(5)\n\t"
      "v_max3_u32 v234, v144, v145, v146\n\t"
      "v_max_u32_e32 v234, v234, v147\n\t"
      "v_cmp_eq_u32_e32 vcc, -1, v234\n\t"
      "s_cbranch_vccnz FP25L_%=\n\t"
      "FP25X_%=:\n\t"
      "s_waitcnt vmcnt(0)\n\t"
      "s_branch FP25B_%=\n\t"
      "FP26R_%=:\n\t"
      "s_mov_b32 s51, 0\n\t"
      "FP26L_%=:\n\t"
      "s_add_u32 s51, s51, 1\n\t"
      "s_cmp_lt_u32 s51, 262144\n\t"
      "s_cbranch_scc0 FP26X_%=\n\t"
      "s_waitcnt vmcnt(0)\n\t"
      "s_sleep 4\n\t"
      "global_load_dwordx4 v[148:151], v229, s[58:59] sc1\n\t"
      "global_load_dwordx4 v[152:155], v230, s[58:59] sc1\n\t"
      "global_load_dwordx4 v[128:131], v224, s[60:61] sc1\n\t"
      "global_load_dwordx4 v[132:135], v225, s[60:61] sc1\n\t"
      "global_load_dwordx4 v[136:139], v226, s[60:61] sc1\n\t"
      "global_load_dwordx4 v[140:143], v227, s[60:61] sc1\n\t"
      "s_waitcnt vmcnt(5)\n\t"
      "v_max3_u32 v234, v148, v149, v150\n\t"
      "v_max_u32_e32 v234, v234, v151\n\t"
      "v_cmp_eq_u32_e32 vcc, -1, v234\n\t"
      "s_cbranch_vccnz FP26L_%=\n\t"
      "FP26X_%=:\n\t"
      "s_branch FP26B_%=\n\t"
      "FP27R_%=:\n\t"
      "s_mov_b32 s51, 0\n\t"
      "FP27L_%=:\n\t"
      "s_add_u32 s51, s51, 1\n\t"
      "s_cmp_lt_u32 s51, 262144\n\t"
      "s_cbranch_scc0 FP27X_%=\n\t"
      "s_waitcnt vmcnt(0)\n\t"
      "s_sleep 4\n\t"
      "global_load_dwordx4 v[152:155], v230, s[58:59] sc1\n\t"
      "global_load_dwordx4 v[128:131], v224, s[60:61] sc1\n\t"
      "global_load_dwordx4 v[132:135], v225, s[60:61] sc1\n\t"
      "global_load_dwordx4 v[136:139], v226, s[60:61] sc1\n\t"
      "global_load_dwordx4 v[140:143], v227, s[60:61] sc1\n\t"
      "global_load_dwordx4 v[144:147], v228, s[60:61] sc1\n\t"
      "s_waitcnt vmcnt(5)\n\t"
      "v_max3_u32 v234, v152, v153, v154\n\t"
      "v_max_u32_e32 v234, v234, v155\n\t"
      "v_cmp_eq_u32_e32 vcc, -1, v234\n\t"
      "s_cbranch_vccnz FP27L_%=\n\t"
      "FP27X_%=:\n\t"
      "s_waitcnt vmcnt(0)\n\t"
      "s_branch FP27B_%=\n\t"
      "FP28R_%=:\n\t"
      "s_mov_b32 s51, 0\n\t"
      "FP28L_%=:\n\t"
      "s_add_u32 s51, s51, 1\n\t"
      "s_cmp_lt_u32 s51, 262144\n\t"
      "s_cbranch_scc0 FP28X_%=\n\t"
      "s_waitcnt vmcnt(0)\n\t"
      "s_sleep 4\n\t"
      "global_load_dwordx4 v[128:131], v224, s[60:61] sc1\n\t"
      "global_load_dwordx4 v[132:135], v225, s[60:61] sc1\n\t"
      "global_load_dwordx4 v[136:139], v226, s[60:61] sc1\n\t"
      "global_load_dwordx4 v[140:143], v227, s[60:61] sc1\n\t"
      "global_load_dwordx4 v[144:147], v228, s[60:61] sc1\n\t"
      "global_load_dwordx4 v[148:151], v229, s[60:61] sc1\n\t"
      "s_waitcnt vmcnt(5)\n\t"
      "v_max3_u32 v234, v128, v129, v130\n\t"
      "v_max_u32_e32 v234, v234, v131\n\t"
      "v_cmp_eq_u32_e32 vcc, -1, v234\n\t"
      "s_cbranch_vccnz FP28L_%=\n\t"
      "FP28X_%=:\n\t"
      "s_waitcnt vmcnt(0)\n\t"
      "s_branch FP28B_%=\n\t"
      "NP3R_%=:\n\t"
      "s_mov_b32 s51, 0\n\t"
      "NP3L_%=:\n\t"
      "s_add_u32 s51, s51, 1\n\t"
      "s_cmp_lt_u32 s51, 262144\n\t"
      "s_cbranch_scc0 NP3X_%=\n\t"
      "s_waitcnt vmcnt(0)\n\t"
      "s_sleep 1\n\t"
      "global_load_dwordx4 v[128:131], v224, s[58:59] sc1\n\t"
      "global_load_dwordx4 v[132:135], v225, s[58:59] sc1\n\t"
      "global_load_dwordx4 v[136:139], v226, s[58:59] sc1\n\t"
      "global_load_dwordx4 v[140:143], v227, s[58:59] sc1\n\t"
      "global_load_dwordx4 v[144:147], v228, s[58:59] sc1\n\t"
      "global_load_dwordx4 v[148:151], v229, s[58:59] sc1\n\t"
      "global_load_dwordx4 v[152:155], v230, s[58:59] sc1\n\t"
      "s_waitcnt vmcnt(0)\n\t"
      "v_max3_u32 v234, v128, v129, v130\n\t"
      "v_max3_u32 v234, v234, v131, v132\n\t"
      "v_max3_u32 v234, v234, v133, v134\n\t"
      "v_max3_u32 v234, v234, v135, v136\n\t"
      "v_max3_u32 v234, v234, v137, v138\n\t"
      "v_max3_u32 v234, v234, v139, v140\n\t"
      "v_max3_u32 v234, v234, v141, v142\n\t"
      "v_max3_u32 v234, v234, v143, v144\n\t"
      "v_max3_u32 v234, v234, v145, v146\n\t"
      "v_max3_u32 v234, v234, v147, v148\n\t"
      "v_max3_u32 v234, v234, v149, v150\n\t"
      "v_max3_u32 v234, v234, v151, v152\n\t"
      "v_max3_u32 v234, v234, v153, v154\n\t"
      "v_max_u32_e32 v234, v234, v155\n\t"
      "v_cmp_eq_u32_e32 vcc, -1, v234\n\t"
      "s_cbranch_vccnz NP3L_%=\n\t"
      "NP3X_%=:\n\t"
      "v_mfma_f32_16x16x32_bf16 v[200:203], v[128:131], v[0:3], v[168:171]\n\t"
      "v_mfma_f32_16x16x32_bf16 v[204:207], v[128:131], v[4:7], 0\n\t"
      "v_mfma_f32_16x16x32_bf16 v[208:211], v[128:131], v[8:11], 0\n\t"
      "v_mfma_f32_16x16x32_bf16 v[212:215], v[128:131], v[12:15], 0\n\t"
      "v_mfma_f32_16x16x32_bf16 v[200:203], v[132:135], v[16:19], v[200:203]\n\t"
      "v_mfma_f32_16x16x32_bf16 v[204:207], v[132:135], v[20:23], v[204:207]\n\t"
      "v_mfma_f32_16x16x32_bf16 v[208:211], v[132:135], v[24:27], v[208:211]\n\t"
      "v_mfma_f32_16x16x32_bf16 v[212:215], v[132:135], v[28:31], v[212:215]\n\t"
      "v_mfma_f32_16x16x32_bf16 v[200:203], v[136:139], v[32:35], v[200:203]\n\t"
      "v_mfma_f32_16x16x32_bf16 v[204:207], v[136:139], v[36:39], v[204:207]\n\t"
      "v_mfma_f32_16x16x32_bf16 v[208:211], v[136:139], v[40:43], v[208:211]\n\t"
      "v_mfma_f32_16x16x32_bf16 v[212:215], v[136:139], v[44:47], v[212:215]\n\t"
      "v_mfma_f32_16x16x32_bf16 v[200:203], v[140:143], v[48:51], v[200:203]\n\t"
      "v_mfma_f32_16x16x32_bf16 v[204:207], v[140:143], v[52:55], v[204:207]\n\t"
      "v_mfma_f32_16x16x32_bf16 v[208:211], v[140:143], v[56:59], v[208:211]\n\t"
      "v_mfma_f32_16x16x32_bf16 v[212:215], v[140:143], v[60:63], v[212:215]\n\t"
      "v_mfma_f32_16x16x32_bf16 v[200:203], v[144:147], v[64:67], v[200:203]\n\t"
      "v_mfma_f32_16x16x32_bf16 v[204:207], v[144:147], v[68:71], v[204:207]\n\t"
      "v_mfma_f32_16x16x32_bf16 v[208:211], v[144:147], v[72:75], v[208:211]\n\t"
      "v_mfma_f32_16x16x32_bf16 v[212:215], v[144:147], v[76:79], v[212:215]\n\t"
      "v_mfma_f32_16x16x32_bf16 v[200:203], v[148:151], v[80:83], v[200:203]\n\t"
      "v_mfma_f32_16x16x32_bf16 v[204:207], v[148:151], v[84:87], v[204:207]\n\t"
      "v_mfma_f32_16x16x32_bf16 v[208:211], v[148:151], v[88:91], v[208:211]\n\t"
      "v_mfma_f32_16x16x32_bf16 v[212:215], v[148:151], v[92:95], v[212:215]\n\t"
      "v_mfma_f32_16x16x32_bf16 v[200:203], v[152:155], v[96:99], v[200:203]\n\t"
      "v_mfma_f32_16x16x32_bf16 v[204:207], v[152:155], v[100:103], v[204:207]\n\t"
      "v_mfma_f32_16x16x32_bf16 v[208:211], v[152:155], v[104:107], v[208:211]\n\t"
      "v_mfma_f32_16x16x32_bf16 v[212:215], v[152:155], v[108:111], v[212:215]\n\t"
      "global_load_dwordx4 v[128:131], v224, s[60:61] sc1\n\t"
      "global_load_dwordx4 v[132:135], v225, s[60:61] sc1\n\t"
      "global_load_dwordx4 v[136:139], v226, s[60:61] sc1\n\t"
      "global_load_dwordx4 v[140:143], v227, s[60:61] sc1\n\t"
      "global_load_dwordx4 v[144:147], v228, s[60:61] sc1\n\t"
      "global_load_dwordx4 v[148:151], v229, s[60:61] sc1\n\t"
      "global_load_dwordx4 v[152:155], v230, s[60:61] sc1\n\t"
      "s_waitcnt vmcnt(0)\n\t"
      "s_nop 7\n\t"
      "s_branch NP3B_%=\n\t"
      "FP29R_%=:\n\t"
      "s_mov_b32 s51, 0\n\t"
      "FP29L_%=:\n\t"
      "s_add_u32 s51, s51, 1\n\t"
      "s_cmp_lt_u32 s51, 262144\n\t"
      "s_cbranch_scc0 FP29X_%=\n\t"
      "s_waitcnt vmcnt(0)\n\t"
      "s_sleep 4\n\t"
      "global_load_dwordx4 v[132:135], v225, s[60:61] sc1\n\t"
      "global_load_dwordx4 v[136:139], v226, s[60:61] sc1\n\t"
      "global_load_dwordx4 v[140:143], v227, s[60:61] sc1\n\t"
      "global_load_dwordx4 v[144:147], v228, s[60:61] sc1\n\t"
      "global_load_dwordx4 v[148:151], v229, s[60:61] sc1\n\t"
      "global_load_dwordx4 v[152:155], v230, s[60:61] sc1\n\t"
      "s_waitcnt vmcnt(5)\n\t"
      "v_max3_u32 v234, v132, v133, v134\n\t"
      "v_max_u32_e32 v234, v234, v135\n\t"
      "v_cmp_eq_u32_e32 vcc, -1, v234\n\t"
      "s_cbranch_vccnz FP29L_%=\n\t"
      "FP29X_%=:\n\t"
      "s_waitcnt vmcnt(0)\n\t"
      "s_branch FP29B_%=\n\t"
      "FP30R_%=:\n\t"
      "s_mov_b32 s51, 0\n\t"
      "FP30L_%=:\n\t"
      "s_add_u32 s51, s51, 1\n\t"
      "s_cmp_lt_u32 s51, 262144\n\t"
      "s_cbranch_scc0 FP30X_%=\n\t"
      "s_waitcnt vmcnt(0)\n\t"
      "s_sleep 4\n\t"
      "global_load_dwordx4 v[136:139], v226, s[60:61] sc1\n\t"
      "global_load_dwordx4 v[140:143], v227, s[60:61] sc1\n\t"
      "global_load_dwordx4 v[144:147], v228, s[60:61] sc1\n\t"
      "global_load_dwordx4 v[148:151], v229, s[60:61] sc1\n\t"
      "global_load_dwordx4 v[152:155], v230, s[60:61] sc1\n\t"
      "global_load_dwordx4 v[128:131], v224, s[62:63] sc1\n\t"
      "s_waitcnt vmcnt(5)\n\t"
      "v_max3_u32 v234, v136, v137, v138\n\t"
      "v_max_u32_e32 v234, v234, v139\n\t"
      "v_cmp_eq_u32_e32 vcc, -1, v234\n\t"
      "s_cbranch_vccnz FP30L_%=\n\t"
      "FP30X_%=:\n\t"
      "s_waitcnt vmcnt(0)\n\t"
      "s_branch FP30B_%=\n\t"
      "FP31R_%=:\n\t"
      "s_mov_b32 s51, 0\n\t"
      "FP31L_%=:\n\t"
      "s_add_u32 s51, s51, 1\n\t"
      "s_cmp_lt_u32 s51, 262144\n\t"
      "s_cbranch_scc0 FP31X_%=\n\t"
      "s_waitcnt vmcnt(0)\n\t"
      "s_sleep 4\n\t"
      "global_load_dwordx4 v[140:143], v227, s[60:61] sc1\n\t"
      "global_load_dwordx4 v[144:147], v228, s[60:61] sc1\n\t"
      "global_load_dwordx4 v[148:151], v229, s[60:61] sc1\n\t"
      "global_load_dwordx4 v[152:155], v230, s[60:61] sc1\n\t"
      "global_load_dwordx4 v[128:131], v224, s[62:63] sc1\n\t"
      "global_load_dwordx4 v[132:135], v225, s[62:63] sc1\n\t"
      "s_waitcnt vmcnt(5)\n\t"
      "v_max3_u32 v234, v140, v141, v142\n\t"
      "v_max_u32_e32 v234, v234, v143\n\t"
      "v_cmp_eq_u32_e32 vcc, -1, v234\n\t"
      "s_cbranch_vccnz FP31L_%=\n\t"
      "FP31X_%=:\n\t"
      "s_waitcnt vmcnt(0)\n\t"
      "s_branch FP31B_%=\n\t"
      "FP32R_%=:\n\t"
      "s_mov_b32 s51, 0\n\t"
      "FP32L_%=:\n\t"
      "s_add_u32 s51, s51, 1\n\t"
      "s_cmp_lt_u32 s51, 262144\n\t"
      "s_cbranch_scc0 FP32X_%=\n\t"
      "s_waitcnt vmcnt(0)\n\t"
      "s_sleep 4\n\t"
      "global_load_dwordx4 v[144:147], v228, s[60:61] sc1\n\t"
      "global_load_dwordx4 v[148:151], v229, s[60:61] sc1\n\t"
      "global_load_dwordx4 v[152:155], v230, s[60:61] sc1\n\t"
      "global_load_dwordx4 v[128:131], v224, s[62:63] sc1\n\t"
      "global_load_dwordx4 v[132:135], v225, s[62:63] sc1\n\t"
      "global_load_dwordx4 v[136:139], v226, s[62:63] sc1\n\t"
      "s_waitcnt vmcnt(5)\n\t"
      "v_max3_u32 v234, v144, v145, v146\n\t"
      "v_max_u32_e32 v234, v234, v147\n\t"
      "v_cmp_eq_u32_e32 vcc, -1, v234\n\t"
      "s_cbranch_vccnz FP32L_%=\n\t"
      "FP32X_%=:\n\t"
      "s_waitcnt vmcnt(0)\n\t"
      "s_branch FP32B_%=\n\t"
      "FP33R_%=:\n\t"
      "s_mov_b32 s51, 0\n\t"
      "FP33L_%=:\n\t"
      "s_add_u32 s51, s51, 1\n\t"
      "s_cmp_lt_u32 s51, 262144\n\t"
      "s_cbranch_scc0 FP33X_%=\n\t"
      "s_waitcnt vmcnt(0)\n\t"
      "s_sleep 4\n\t"
      "global_load_dwordx4 v[148:151], v229, s[60:61] sc1\n\t"
      "global_load_dwordx4 v[152:155], v230, s[60:61] sc1\n\t"
      "global_load_dwordx4 v[128:131], v224, s[62:63] sc1\n\t"
      "global_load_dwordx4 v[132:135], v225, s[62:63] sc1\n\t"
      "global_load_dwordx4 v[136:139], v226, s[62:63] sc1\n\t"
      "global_load_dwordx4 v[140:143], v227, s[62:63] sc1\n\t"
      "s_waitcnt vmcnt(5)\n\t"
      "v_max3_u32 v234, v148, v149, v150\n\t"
      "v_max_u32_e32 v234, v234, v151\n\t"
      "v_cmp_eq_u32_e32 vcc, -1, v234\n\t"
      "s_cbranch_vccnz FP33L_%=\n\t"
      "FP33X_%=:\n\t"
      "s_branch FP33B_%=\n\t"
      "FP34R_%=:\n\t"
      "s_mov_b32 s51, 0\n\t"
      "FP34L_%=:\n\t"
      "s_add_u32 s51, s51, 1\n\t"
      "s_cmp_lt_u32 s51, 262144\n\t"
      "s_cbranch_scc0 FP34X_%=\n\t"
      "s_waitcnt vmcnt(0)\n\t"
      "s_sleep 4\n\t"
      "global_load_dwordx4 v[152:155], v230, s[60:61] sc1\n\t"
      "global_load_dwordx4 v[128:131], v224, s[62:63] sc1\n\t"
      "global_load_dwordx4 v[132:135], v225, s[62:63] sc1\n\t"
      "global_load_dwordx4 v[136:139], v226, s[62:63] sc1\n\t"
      "global_load_dwordx4 v[140:143], v227, s[62:63] sc1\n\t"
      "global_load_dwordx4 v[144:147], v228, s[62:63] sc1\n\t"
      "s_waitcnt vmcnt(5)\n\t"
      "v_max3_u32 v234, v152, v153, v154\n\t"
      "v_max_u32_e32 v234, v234, v155\n\t"
      "v_cmp_eq_u32_e32 vcc, -1, v234\n\t"
      "s_cbranch_vccnz FP34L_%=\n\t"
      "FP34X_%=:\n\t"
      "s_waitcnt vmcnt(0)\n\t"
      "s_branch FP34B_%=\n\t"
      "FP35R_%=:\n\t"
      "s_mov_b32 s51, 0\n\t"
      "FP35L_%=:\n\t"
      "s_add_u32 s51, s51, 1\n\t"
      "s_cmp_lt_u32 s51, 262144\n\t"
      "s_cbranch_scc0 FP35X_%=\n\t"
      "s_waitcnt vmcnt(0)\n\t"
      "s_sleep 4\n\t"
      "global_load_dwordx4 v[128:131], v224, s[62:63] sc1\n\t"
      "global_load_dwordx4 v[132:135], v225, s[62:63] sc1\n\t"
      "global_load_dwordx4 v[136:139], v226, s[62:63] sc1\n\t"
      "global_load_dwordx4 v[140:143], v227, s[62:63] sc1\n\t"
      "global_load_dwordx4 v[144:147], v228, s[62:63] sc1\n\t"
      "global_load_dwordx4 v[148:151], v229, s[62:63] sc1\n\t"
      "s_waitcnt vmcnt(5)\n\t"
      "v_max3_u32 v234, v128, v129, v130\n\t"
      "v_max_u32_e32 v234, v234, v131\n\t"
      "v_cmp_eq_u32_e32 vcc, -1, v234\n\t"
      "s_cbranch_vccnz FP35L_%=\n\t"
      "FP35X_%=:\n\t"
      "s_waitcnt vmcnt(0)\n\t"
      "s_branch FP35B_%=\n\t"
      "NP4R_%=:\n\t"
      "s_mov_b32 s51, 0\n\t"
      "NP4L_%=:\n\t"
      "s_add_u32 s51, s51, 1\n\t"
      "s_cmp_lt_u32 s51, 262144\n\t"
      "s_cbranch_scc0 NP4X_%=\n\t"
      "s_waitcnt vmcnt(0)\n\t"
      "s_sleep 1\n\t"
      "global_load_dwordx4 v[128:131], v224, s[60:61] sc1\n\t"
      "global_load_dwordx4 v[132:135], v225, s[60:61] sc1\n\t"
      "global_load_dwordx4 v[136:139], v226, s[60:61] sc1\n\t"
      "global_load_dwordx4 v[140:143], v227, s[60:61] sc1\n\t"
      "global_load_dwordx4 v[144:147], v228, s[60:61] sc1\n\t"
      "global_load_dwordx4 v[148:151], v229, s[60:61] sc1\n\t"
      "global_load_dwordx4 v[152:155], v230, s[60:61] sc1\n\t"
      "s_waitcnt vmcnt(0)\n\t"
      "v_max3_u32 v234, v128, v129, v130\n\t"
      "v_max3_u32 v234, v234, v131, v132\n\t"
      "v_max3_u32 v234, v234, v133, v134\n\t"
      "v_max3_u32 v234, v234, v135, v136\n\t"
      "v_max3_u32 v234, v234, v137, v138\n\t"
      "v_max3_u32 v234, v234, v139, v140\n\t"
      "v_max3_u32 v234, v234, v141, v142\n\t"
      "v_max3_u32 v234, v234, v143, v144\n\t"
      "v_max3_u32 v234, v234, v145, v146\n\t"
      "v_max3_u32 v234, v234, v147, v148\n\t"
      "v_max3_u32 v234, v234, v149, v150\n\t"
      "v_max3_u32 v234, v234, v151, v152\n\t"
      "v_max3_u32 v234, v234, v153, v154\n\t"
      "v_max_u32_e32 v234, v234, v155\n\t"
      "v_cmp_eq_u32_e32 vcc, -1, v234\n\t"
      "s_cbranch_vccnz NP4L_%=\n\t"
      "NP4X_%=:\n\t"
      "v_mfma_f32_16x16x32_bf16 v[184:187], v[128:131], v[0:3], v[172:175]\n\t"
      "v_mfma_f32_16x16x32_bf16 v[188:191], v[128:131], v[4:7], 0\n\t"
      "v_mfma_f32_16x16x32_bf16 v[192:195], v[128:131], v[8:11], 0\n\t"
      "v_mfma_f32_16x16x32_bf16 v[196:199], v[128:131], v[12:15], 0\n\t"
      "v_mfma_f32_16x16x32_bf16 v[184:187], v[132:135], v[16:19], v[184:187]\n\t"
      "v_mfma_f32_16x16x32_bf16 v[188:191], v[132:135], v[20:23], v[188:191]\n\t"
      "v_mfma_f32_16x16x32_bf16 v[192:195], v[132:135], v[24:27], v[192:195]\n\t"
      "v_mfma_f32_16x16x32_bf16 v[196:199], v[132:135], v[28:31], v[196:199]\n\t"
      "v_mfma_f32_16x16x32_bf16 v[184:187], v[136:139], v[32:35], v[184:187]\n\t"
      "v_mfma_f32_16x16x32_bf16 v[188:191], v[136:139], v[36:39], v[188:191]\n\t"
      "v_mfma_f32_16x16x32_bf16 v[192:195], v[136:139], v[40:43], v[192:195]\n\t"
      "v_mfma_f32_16x16x32_bf16 v[196:199], v[136:139], v[44:47], v[196:199]\n\t"
      "v_mfma_f32_16x16x32_bf16 v[184:187], v[140:143], v[48:51], v[184:187]\n\t"
      "v_mfma_f32_16x16x32_bf16 v[188:191], v[140:143], v[52:55], v[188:191]\n\t"
      "v_mfma_f32_16x16x32_bf16 v[192:195], v[140:143], v[56:59], v[192:195]\n\t"
      "v_mfma_f32_16x16x32_bf16 v[196:199], v[140:143], v[60:63], v[196:199]\n\t"
      "v_mfma_f32_16x16x32_bf16 v[184:187], v[144:147], v[64:67], v[184:187]\n\t"
      "v_mfma_f32_16x16x32_bf16 v[188:191], v[144:147], v[68:71], v[188:191]\n\t"
      "v_mfma_f32_16x16x32_bf16 v[192:195], v[144:147], v[72:75], v[192:195]\n\t"
      "v_mfma_f32_16x16x32_bf16 v[196:199], v[144:147], v[76:79], v[196:199]\n\t"
      "v_mfma_f32_16x16x32_bf16 v[184:187], v[148:151], v[80:83], v[184:187]\n\t"
      "v_mfma_f32_16x16x32_bf16 v[188:191], v[148:151], v[84:87], v[188:191]\n\t"
      "v_mfma_f32_16x16x32_bf16 v[192:195], v[148:151], v[88:91], v[192:195]\n\t"
      "v_mfma_f32_16x16x32_bf16 v[196:199], v[148:151], v[92:95], v[196:199]\n\t"
      "v_mfma_f32_16x16x32_bf16 v[184:187], v[152:155], v[96:99], v[184:187]\n\t"
      "v_mfma_f32_16x16x32_bf16 v[188:191], v[152:155], v[100:103], v[188:191]\n\t"
      "v_mfma_f32_16x16x32_bf16 v[192:195], v[152:155], v[104:107], v[192:195]\n\t"
      "v_mfma_f32_16x16x32_bf16 v[196:199], v[152:155], v[108:111], v[196:199]\n\t"
      "global_load_dwordx4 v[128:131], v224, s[62:63] sc1\n\t"
      "global_load_dwordx4 v[132:135], v225, s[62:63] sc1\n\t"
      "global_load_dwordx4 v[136:139], v226, s[62:63] sc1\n\t"
      "global_load_dwordx4 v[140:143], v227, s[62:63] sc1\n\t"
      "global_load_dwordx4 v[144:147], v228, s[62:63] sc1\n\t"
      "global_load_dwordx4 v[148:151], v229, s[62:63] sc1\n\t"
      "global_load_dwordx4 v[152:155], v230, s[62:63] sc1\n\t"
      "s_waitcnt vmcnt(0)\n\t"
      "s_nop 7\n\t"
      "s_branch NP4B_%=\n\t"
      "FP36R_%=:\n\t"
      "s_mov_b32 s51, 0\n\t"
      "FP36L_%=:\n\t"
      "s_add_u32 s51, s51, 1\n\t"
      "s_cmp_lt_u32 s51, 262144\n\t"
      "s_cbranch_scc0 FP36X_%=\n\t"
      "s_waitcnt vmcnt(0)\n\t"
      "s_sleep 4\n\t"
      "global_load_dwordx4 v[132:135], v225, s[62:63] sc1\n\t"
      "global_load_dwordx4 v[136:139], v226, s[62:63] sc1\n\t"
      "global_load_dwordx4 v[140:143], v227, s[62:63] sc1\n\t"
      "global_load_dwordx4 v[144:147], v228, s[62:63] sc1\n\t"
      "global_load_dwordx4 v[148:151], v229, s[62:63] sc1\n\t"
      "global_load_dwordx4 v[152:155], v230, s[62:63] sc1\n\t"
      "s_waitcnt vmcnt(5)\n\t"
      "v_max3_u32 v234, v132, v133, v134\n\t"
      "v_max_u32_e32 v234, v234, v135\n\t"
      "v_cmp_eq_u32_e32 vcc, -1, v234\n\t"
      "s_cbranch_vccnz FP36L_%=\n\t"
      "FP36X_%=:\n\t"
      "s_waitcnt vmcnt(0)\n\t"
      "s_branch FP36B_%=\n\t"
      "FP37R_%=:\n\t"
      "s_mov_b32 s51, 0\n\t"
      "FP37L_%=:\n\t"
      "s_add_u32 s51, s51, 1\n\t"
      "s_cmp_lt_u32 s51, 262144\n\t"
      "s_cbranch_scc0 FP37X_%=\n\t"
      "s_waitcnt vmcnt(0)\n\t"
      "s_sleep 4\n\t"
      "global_load_dwordx4 v[136:139], v226, s[62:63] sc1\n\t"
      "global_load_dwordx4 v[140:143], v227, s[62:63] sc1\n\t"
      "global_load_dwordx4 v[144:147], v228, s[62:63] sc1\n\t"
      "global_load_dwordx4 v[148:151], v229, s[62:63] sc1\n\t"
      "global_load_dwordx4 v[152:155], v230, s[62:63] sc1\n\t"
      "global_load_dwordx4 v[128:131], v224, s[64:65] sc1\n\t"
      "s_waitcnt vmcnt(5)\n\t"
      "v_max3_u32 v234, v136, v137, v138\n\t"
      "v_max_u32_e32 v234, v234, v139\n\t"
      "v_cmp_eq_u32_e32 vcc, -1, v234\n\t"
      "s_cbranch_vccnz FP37L_%=\n\t"
      "FP37X_%=:\n\t"
      "s_waitcnt vmcnt(0)\n\t"
      "s_branch FP37B_%=\n\t"
      "FP38R_%=:\n\t"
      "s_mov_b32 s51, 0\n\t"
      "FP38L_%=:\n\t"
      "s_add_u32 s51, s51, 1\n\t"
      "s_cmp_lt_u32 s51, 262144\n\t"
      "s_cbranch_scc0 FP38X_%=\n\t"
      "s_waitcnt vmcnt(0)\n\t"
      "s_sleep 4\n\t"
      "global_load_dwordx4 v[140:143], v227, s[62:63] sc1\n\t"
      "global_load_dwordx4 v[144:147], v228, s[62:63] sc1\n\t"
      "global_load_dwordx4 v[148:151], v229, s[62:63] sc1\n\t"
      "global_load_dwordx4 v[152:155], v230, s[62:63] sc1\n\t"
      "global_load_dwordx4 v[128:131], v224, s[64:65] sc1\n\t"
      "global_load_dwordx4 v[132:135], v225, s[64:65] sc1\n\t"
      "s_waitcnt vmcnt(5)\n\t"
      "v_max3_u32 v234, v140, v141, v142\n\t"
      "v_max_u32_e32 v234, v234, v143\n\t"
      "v_cmp_eq_u32_e32 vcc, -1, v234\n\t"
      "s_cbranch_vccnz FP38L_%=\n\t"
      "FP38X_%=:\n\t"
      "s_waitcnt vmcnt(0)\n\t"
      "s_branch FP38B_%=\n\t"
      "FP39R_%=:\n\t"
      "s_mov_b32 s51, 0\n\t"
      "FP39L_%=:\n\t"
      "s_add_u32 s51, s51, 1\n\t"
      "s_cmp_lt_u32 s51, 262144\n\t"
      "s_cbranch_scc0 FP39X_%=\n\t"
      "s_waitcnt vmcnt(0)\n\t"
      "s_sleep 4\n\t"
      "global_load_dwordx4 v[144:147], v228, s[62:63] sc1\n\t"
      "global_load_dwordx4 v[148:151], v229, s[62:63] sc1\n\t"
      "global_load_dwordx4 v[152:155], v230, s[62:63] sc1\n\t"
      "global_load_dwordx4 v[128:131], v224, s[64:65] sc1\n\t"
      "global_load_dwordx4 v[132:135], v225, s[64:65] sc1\n\t"
      "global_load_dwordx4 v[136:139], v226, s[64:65] sc1\n\t"
      "s_waitcnt vmcnt(5)\n\t"
      "v_max3_u32 v234, v144, v145, v146\n\t"
      "v_max_u32_e32 v234, v234, v147\n\t"
      "v_cmp_eq_u32_e32 vcc, -1, v234\n\t"
      "s_cbranch_vccnz FP39L_%=\n\t"
      "FP39X_%=:\n\t"
      "s_waitcnt vmcnt(0)\n\t"
      "s_branch FP39B_%=\n\t"
      "FP40R_%=:\n\t"
      "s_mov_b32 s51, 0\n\t"
      "FP40L_%=:\n\t"
      "s_add_u32 s51, s51, 1\n\t"
      "s_cmp_lt_u32 s51, 262144\n\t"
      "s_cbranch_scc0 FP40X_%=\n\t"
      "s_waitcnt vmcnt(0)\n\t"
      "s_sleep 4\n\t"
      "global_load_dwordx4 v[148:151], v229, s[62:63] sc1\n\t"
      "global_load_dwordx4 v[152:155], v230, s[62:63] sc1\n\t"
      "global_load_dwordx4 v[128:131], v224, s[64:65] sc1\n\t"
      "global_load_dwordx4 v[132:135], v225, s[64:65] sc1\n\t"
      "global_load_dwordx4 v[136:139], v226, s[64:65] sc1\n\t"
      "global_load_dwordx4 v[140:143], v227, s[64:65] sc1\n\t"
      "s_waitcnt vmcnt(5)\n\t"
      "v_max3_u32 v234, v148, v149, v150\n\t"
      "v_max_u32_e32 v234, v234, v151\n\t"
      "v_cmp_eq_u32_e32 vcc, -1, v234\n\t"
      "s_cbranch_vccnz FP40L_%=\n\t"
      "FP40X_%=:\n\t"
      "s_branch FP40B_%=\n\t"
      "FP41R_%=:\n\t"
      "s_mov_b32 s51, 0\n\t"
      "FP41L_%=:\n\t"
      "s_add_u32 s51, s51, 1\n\t"
      "s_cmp_lt_u32 s51, 262144\n\t"
      "s_cbranch_scc0 FP41X_%=\n\t"
      "s_waitcnt vmcnt(0)\n\t"
      "s_sleep 4\n\t"
      "global_load_dwordx4 v[152:155], v230, s[62:63] sc1\n\t"
      "global_load_dwordx4 v[128:131], v224, s[64:65] sc1\n\t"
      "global_load_dwordx4 v[132:135], v225, s[64:65] sc1\n\t"
      "global_load_dwordx4 v[136:139], v226, s[64:65] sc1\n\t"
      "global_load_dwordx4 v[140:143], v227, s[64:65] sc1\n\t"
      "global_load_dwordx4 v[144:147], v228, s[64:65] sc1\n\t"
      "s_waitcnt vmcnt(5)\n\t"
      "v_max3_u32 v234, v152, v153, v154\n\t"
      "v_max_u32_e32 v234, v234, v155\n\t"
      "v_cmp_eq_u32_e32 vcc, -1, v234\n\t"
      "s_cbranch_vccnz FP41L_%=\n\t"
      "FP41X_%=:\n\t"
      "s_waitcnt vmcnt(0)\n\t"
      "s_branch FP41B_%=\n\t"
      "FP42R_%=:\n\t"
      "s_mov_b32 s51, 0\n\t"
      "FP42L_%=:\n\t"
      "s_add_u32 s51, s51, 1\n\t"
      "s_cmp_lt_u32 s51, 262144\n\t"
      "s_cbranch_scc0 FP42X_%=\n\t"
      "s_sleep 1\n\t"
      "s_mov_b64 exec, 15\n\t"
      "v_mov_b64_e32 v[232:233], 0\n\t"
      "global_atomic_or_x2 v[232:233], v247, v[232:233], s[12:13] sc0 sc1\n\t"
      "s_waitcnt vmcnt(0)\n\t"
      "v_subrev_u32_e32 v232, s14, v232\n\t"
      "v_cmp_ne_u32_e64 s[48:49], s15, v233\n\t"
      "v_subrev_u32_e32 v232, 6, v232\n\t"
      "v_cmp_lt_u32_e32 vcc, 7, v232\n\t"
      "s_mov_b64 exec, -1\n\t"
      "s_or_b64 vcc, vcc, s[48:49]\n\t"
      "s_cbranch_vccnz FP42L_%=\n\t"
      "FP42X_%=:\n\t"
      "FP42DR_%=:\n\t"
      "s_mov_b32 s51, 0\n\t"
      "FP42DL_%=:\n\t"
      "s_add_u32 s51, s51, 1\n\t"
      "s_cmp_lt_u32 s51, 262144\n\t"
      "s_cbranch_scc0 FP42DX_%=\n\t"
      "s_waitcnt vmcnt(0)\n\t"
      "s_sleep 1\n\t"
      "global_load_dwordx4 v[128:131], v224, s[64:65] sc1\n\t"
      "global_load_dwordx4 v[132:135], v225, s[64:65] sc1\n\t"
      "global_load_dwordx4 v[136:139], v226, s[64:65] sc1\n\t"
      "global_load_dwordx4 v[140:143], v227, s[64:65] sc1\n\t"
      "global_load_dwordx4 v[144:147], v228, s[64:65] sc1\n\t"
      "global_load_dwordx4 v[148:151], v229, s[64:65] sc1\n\t"
      "s_waitcnt vmcnt(5)\n\t"
      "v_max3_u32 v234, v128, v129, v130\n\t"
      "v_max_u32_e32 v234, v234, v131\n\t"
      "v_cmp_eq_u32_e32 vcc, -1, v234\n\t"
      "s_cbranch_vccnz FP42DL_%=\n\t"
      "FP42DX_%=:\n\t"
      "s_waitcnt vmcnt(0)\n\t"
      "s_branch FP42B_%=\n\t"
      "NP5R_%=:\n\t"
      "s_mov_b32 s51, 0\n\t"
      "NP5L_%=:\n\t"
      "s_add_u32 s51, s51, 1\n\t"
      "s_cmp_lt_u32 s51, 262144\n\t"
      "s_cbranch_scc0 NP5X_%=\n\t"
      "s_waitcnt vmcnt(0)\n\t"
      "s_sleep 1\n\t"
      "global_load_dwordx4 v[128:131], v224, s[62:63] sc1\n\t"
      "global_load_dwordx4 v[132:135], v225, s[62:63] sc1\n\t"
      "global_load_dwordx4 v[136:139], v226, s[62:63] sc1\n\t"
      "global_load_dwordx4 v[140:143], v227, s[62:63] sc1\n\t"
      "global_load_dwordx4 v[144:147], v228, s[62:63] sc1\n\t"
      "global_load_dwordx4 v[148:151], v229, s[62:63] sc1\n\t"
      "global_load_dwordx4 v[152:155], v230, s[62:63] sc1\n\t"
      "s_waitcnt vmcnt(0)\n\t"
      "v_max3_u32 v234, v128, v129, v130\n\t"
      "v_max3_u32 v234, v234, v131, v132\n\t"
      "v_max3_u32 v234, v234, v133, v134\n\t"
      "v_max3_u32 v234, v234, v135, v136\n\t"
      "v_max3_u32 v234, v234, v137, v138\n\t"
      "v_max3_u32 v234, v234, v139, v140\n\t"
      "v_max3_u32 v234, v234, v141, v142\n\t"
      "v_max3_u32 v234, v234, v143, v144\n\t"
      "v_max3_u32 v234, v234, v145, v146\n\t"
      "v_max3_u32 v234, v234, v147, v148\n\t"
      "v_max3_u32 v234, v234, v149, v150\n\t"
      "v_max3_u32 v234, v234, v151, v152\n\t"
      "v_max3_u32 v234, v234, v153, v154\n\t"
      "v_max_u32_e32 v234, v234, v155\n\t"
      "v_cmp_eq_u32_e32 vcc, -1, v234\n\t"
      "s_cbranch_vccnz NP5L_%=\n\t"
      "NP5X_%=:\n\t"
      "v_mfma_f32_16x16x32_bf16 v[200:203], v[128:131], v[0:3], v[176:179]\n\t"
      "v_mfma_f32_16x16x32_bf16 v[204:207], v[128:131], v[4:7], 0\n\t"
      "v_mfma_f32_16x16x32_bf16 v[208:211], v[128:131], v[8:11], 0\n\t"
      "v_mfma_f32_16x16x32_bf16 v[212:215], v[128:131], v[12:15], 0\n\t"
      "v_mfma_f32_16x16x32_bf16 v[200:203], v[132:135], v[16:19], v[200:203]\n\t"
      "v_mfma_f32_16x16x32_bf16 v[204:207], v[132:135], v[20:23], v[204:207]\n\t"
      "v_mfma_f32_16x16x32_bf16 v[208:211], v[132:135], v[24:27], v[208:211]\n\t"
      "v_mfma_f32_16x16x32_bf16 v[212:215], v[132:135], v[28:31], v[212:215]\n\t"
      "v_mfma_f32_16x16x32_bf16 v[200:203], v[136:139], v[32:35], v[200:203]\n\t"
      "v_mfma_f32_16x16x32_bf16 v[204:207], v[136:139], v[36:39], v[204:207]\n\t"
      "v_mfma_f32_16x16x32_bf16 v[208:211], v[136:139], v[40:43], v[208:211]\n\t"
      "v_mfma_f32_16x16x32_bf16 v[212:215], v[136:139], v[44:47], v[212:215]\n\t"
      "v_mfma_f32_16x16x32_bf16 v[200:203], v[140:143], v[48:51], v[200:203]\n\t"
      "v_mfma_f32_16x16x32_bf16 v[204:207], v[140:143], v[52:55], v[204:207]\n\t"
      "v_mfma_f32_16x16x32_bf16 v[208:211], v[140:143], v[56:59], v[208:211]\n\t"
      "v_mfma_f32_16x16x32_bf16 v[212:215], v[140:143], v[60:63], v[212:215]\n\t"
      "v_mfma_f32_16x16x32_bf16 v[200:203], v[144:147], v[64:67], v[200:203]\n\t"
      "v_mfma_f32_16x16x32_bf16 v[204:207], v[144:147], v[68:71], v[204:207]\n\t"
      "v_mfma_f32_16x16x32_bf16 v[208:211], v[144:147], v[72:75], v[208:211]\n\t"
      "v_mfma_f32_16x16x32_bf16 v[212:215], v[144:147], v[76:79], v[212:215]\n\t"
      "v_mfma_f32_16x16x32_bf16 v[200:203], v[148:151], v[80:83], v[200:203]\n\t"
      "v_mfma_f32_16x16x32_bf16 v[204:207], v[148:151], v[84:87], v[204:207]\n\t"
      "v_mfma_f32_16x16x32_bf16 v[208:211], v[148:151], v[88:91], v[208:211]\n\t"
      "v_mfma_f32_16x16x32_bf16 v[212:215], v[148:151], v[92:95], v[212:215]\n\t"
      "v_mfma_f32_16x16x32_bf16 v[200:203], v[152:155], v[96:99], v[200:203]\n\t"
      "v_mfma_f32_16x16x32_bf16 v[204:207], v[152:155], v[100:103], v[204:207]\n\t"
      "v_mfma_f32_16x16x32_bf16 v[208:211], v[152:155], v[104:107], v[208:211]\n\t"
      "v_mfma_f32_16x16x32_bf16 v[212:215], v[152:155], v[108:111], v[212:215]\n\t"
      "global_load_dwordx4 v[128:131], v224, s[64:65] sc1\n\t"
      "global_load_dwordx4 v[132:135], v225, s[64:65] sc1\n\t"
      "global_load_dwordx4 v[136:139], v226, s[64:65] sc1\n\t"
      "global_load_dwordx4 v[140:143], v227, s[64:65] sc1\n\t"
      "global_load_dwordx4 v[144:147], v228, s[64:65] sc1\n\t"
      "global_load_dwordx4 v[148:151], v229, s[64:65] sc1\n\t"
      "global_load_dwordx4 v[152:155], v230, s[64:65] sc1\n\t"
      "s_waitcnt vmcnt(0)\n\t"
      "s_nop 7\n\t"
      "s_branch NP5B_%=\n\t"
      "FP43R_%=:\n\t"
      "s_mov_b32 s51, 0\n\t"
      "FP43L_%=:\n\t"
      "s_add_u32 s51, s51, 1\n\t"
      "s_cmp_lt_u32 s51, 262144\n\t"
      "s_cbranch_scc0 FP43X_%=\n\t"
      "s_sleep 1\n\t"
      "s_mov_b64 exec, 15\n\t"
      "v_mov_b64_e32 v[232:233], 0\n\t"
      "global_atomic_or_x2 v[232:233], v247, v[232:233], s[12:13] sc0 sc1\n\t"
      "s_waitcnt vmcnt(0)\n\t"
      "v_subrev_u32_e32 v232, s14, v232\n\t"
      "v_cmp_ne_u32_e64 s[48:49], s15, v233\n\t"
      "v_subrev_u32_e32 v232, 6, v232\n\t"
      "v_cmp_lt_u32_e32 vcc, 7, v232\n\t"
      "s_mov_b64 exec, -1\n\t"
      "s_or_b64 vcc, vcc, s[48:49]\n\t"
      "s_cbranch_vccnz FP43L_%=\n\t"
      "FP43X_%=:\n\t"
      "FP43DR_%=:\n\t"
      "s_mov_b32 s51, 0\n\t"
      "FP43DL_%=:\n\t"
      "s_add_u32 s51, s51, 1\n\t"
      "s_cmp_lt_u32 s51, 262144\n\t"
      "s_cbranch_scc0 FP43DX_%=\n\t"
      "s_waitcnt vmcnt(0)\n\t"
      "s_sleep 1\n\t"
      "global_load_dwordx4 v[132:135], v225, s[64:65] sc1\n\t"
      "global_load_dwordx4 v[136:139], v226, s[64:65] sc1\n\t"
      "global_load_dwordx4 v[140:143], v227, s[64:65] sc1\n\t"
      "global_load_dwordx4 v[144:147], v228, s[64:65] sc1\n\t"
      "global_load_dwordx4 v[148:151], v229, s[64:65] sc1\n\t"
      "global_load_dwordx4 v[152:155], v230, s[64:65] sc1\n\t"
      "s_waitcnt vmcnt(5)\n\t"
      "v_max3_u32 v234, v132, v133, v134\n\t"
      "v_max_u32_e32 v234, v234, v135\n\t"
      "v_cmp_eq_u32_e32 vcc, -1, v234\n\t"
      "s_cbranch_vccnz FP43DL_%=\n\t"
      "FP43DX_%=:\n\t"
      "s_waitcnt vmcnt(0)\n\t"
      "s_branch FP43B_%=\n\t"
      "FP44R_%=:\n\t"
      "s_mov_b32 s51, 0\n\t"
      "FP44L_%=:\n\t"
      "s_add_u32 s51, s51, 1\n\t"
      "s_cmp_lt_u32 s51, 262144\n\t"
      "s_cbranch_scc0 FP44X_%=\n\t"
      "s_sleep 1\n\t"
      "s_mov_b64 exec, 15\n\t"
      "v_mov_b64_e32 v[232:233], 0\n\t"
      "global_atomic_or_x2 v[232:233], v247, v[232:233], s[12:13] sc0 sc1\n\t"
      "s_waitcnt vmcnt(0)\n\t"
      "v_subrev_u32_e32 v232, s14, v232\n\t"
      "v_cmp_ne_u32_e64 s[48:49], s15, v233\n\t"
      "v_subrev_u32_e32 v232, 6, v232\n\t"
      "v_cmp_lt_u32_e32 vcc, 7, v232\n\t"
      "s_mov_b64 exec, -1\n\t"
      "s_or_b64 vcc, vcc, s[48:49]\n\t"
      "s_cbranch_vccnz FP44L_%=\n\t"
      "FP44X_%=:\n\t"
      "FP44DR_%=:\n\t"
      "s_mov_b32 s51, 0\n\t"
      "FP44DL_%=:\n\t"
      "s_add_u32 s51, s51, 1\n\t"
      "s_cmp_lt_u32 s51, 262144\n\t"
      "s_cbranch_scc0 FP44DX_%=\n\t"
      "s_waitcnt vmcnt(0)\n\t"
      "s_sleep 1\n\t"
      "global_load_dwordx4 v[136:139], v226, s[64:65] sc1\n\t"
      "global_load_dwordx4 v[140:143], v227, s[64:65] sc1\n\t"
      "global_load_dwordx4 v[144:147], v228, s[64:65] sc1\n\t"
      "global_load_dwordx4 v[148:151], v229, s[64:65] sc1\n\t"
      "global_load_dwordx4 v[152:155], v230, s[64:65] sc1\n\t"
      "s_waitcnt vmcnt(4)\n\t"
      "v_max3_u32 v234, v136, v137, v138\n\t"
      "v_max_u32_e32 v234, v234, v139\n\t"
      "v_cmp_eq_u32_e32 vcc, -1, v234\n\t"
      "s_cbranch_vccnz FP44DL_%=\n\t"
      "FP44DX_%=:\n\t"
      "s_waitcnt vmcnt(0)\n\t"
      "s_branch FP44B_%=\n\t"
      "FP45R_%=:\n\t"
      "s_mov_b32 s51, 0\n\t"
      "FP45L_%=:\n\t"
      "s_add_u32 s51, s51, 1\n\t"
      "s_cmp_lt_u32 s51, 262144\n\t"
      "s_cbranch_scc0 FP45X_%=\n\t"
      "s_sleep 1\n\t"
      "s_mov_b64 exec, 15\n\t"
      "v_mov_b64_e32 v[232:233], 0\n\t"
      "global_atomic_or_x2 v[232:233], v247, v[232:233], s[12:13] sc0 sc1\n\t"
      "s_waitcnt vmcnt(0)\n\t"
      "v_subrev_u32_e32 v232, s14, v232\n\t"
      "v_cmp_ne_u32_e64 s[48:49], s15, v233\n\t"
      "v_subrev_u32_e32 v232, 6, v232\n\t"
      "v_cmp_lt_u32_e32 vcc, 7, v232\n\t"
      "s_mov_b64 exec, -1\n\t"
      "s_or_b64 vcc, vcc, s[48:49]\n\t"
      "s_cbranch_vccnz FP45L_%=\n\t"
      "FP45X_%=:\n\t"
      "FP45DR_%=:\n\t"
      "s_mov_b32 s51, 0\n\t"
      "FP45DL_%=:\n\t"
      "s_add_u32 s51, s51, 1\n\t"
      "s_cmp_lt_u32 s51, 262144\n\t"
      "s_cbranch_scc0 FP45DX_%=\n\t"
      "s_waitcnt vmcnt(0)\n\t"
      "s_sleep 1\n\t"
      "global_load_dwordx4 v[140:143], v227, s[64:65] sc1\n\t"
      "global_load_dwordx4 v[144:147], v228, s[64:65] sc1\n\t"
      "global_load_dwordx4 v[148:151], v229, s[64:65] sc1\n\t"
      "global_load_dwordx4 v[152:155], v230, s[64:65] sc1\n\t"
      "s_waitcnt vmcnt(3)\n\t"
      "v_max3_u32 v234, v140, v141, v142\n\t"
      "v_max_u32_e32 v234, v234, v143\n\t"
      "v_cmp_eq_u32_e32 vcc, -1, v234\n\t"
      "s_cbranch_vccnz FP45DL_%=\n\t"
      "FP45DX_%=:\n\t"
      "s_waitcnt vmcnt(0)\n\t"
      "s_branch FP45B_%=\n\t"
      "FP46R_%=:\n\t"
      "s_mov_b32 s51, 0\n\t"
      "FP46L_%=:\n\t"
      "s_add_u32 s51, s51, 1\n\t"
      "s_cmp_lt_u32 s51, 262144\n\t"
      "s_cbranch_scc0 FP46X_%=\n\t"
      "s_sleep 1\n\t"
      "s_mov_b64 exec, 15\n\t"
      "v_mov_b64_e32 v[232:233], 0\n\t"
      "global_atomic_or_x2 v[232:233], v247, v[232:233], s[12:13] sc0 sc1\n\t"
      "s_waitcnt vmcnt(0)\n\t"
      "v_subrev_u32_e32 v232, s14, v232\n\t"
      "v_cmp_ne_u32_e64 s[48:49], s15, v233\n\t"
      "v_subrev_u32_e32 v232, 6, v232\n\t"
      "v_cmp_lt_u32_e32 vcc, 7, v232\n\t"
      "s_mov_b64 exec, -1\n\t"
      "s_or_b64 vcc, vcc, s[48:49]\n\t"
      "s_cbranch_vccnz FP46L_%=\n\t"
      "FP46X_%=:\n\t"
      "FP46DR_%=:\n\t"
      "s_mov_b32 s51, 0\n\t"
      "FP46DL_%=:\n\t"
      "s_add_u32 s51, s51, 1\n\t"
      "s_cmp_lt_u32 s51, 262144\n\t"
      "s_cbranch_scc0 FP46DX_%=\n\t"
      "s_waitcnt vmcnt(0)\n\t"
      "s_sleep 1\n\t"
      "global_load_dwordx4 v[144:147], v228, s[64:65] sc1\n\t"
      "global_load_dwordx4 v[148:151], v229, s[64:65] sc1\n\t"
      "global_load_dwordx4 v[152:155], v230, s[64:65] sc1\n\t"
      "s_waitcnt vmcnt(2)\n\t"
      "v_max3_u32 v234, v144, v145, v146\n\t"
      "v_max_u32_e32 v234, v234, v147\n\t"
      "v_cmp_eq_u32_e32 vcc, -1, v234\n\t"
      "s_cbranch_vccnz FP46DL_%=\n\t"
      "FP46DX_%=:\n\t"
      "s_waitcnt vmcnt(0)\n\t"
      "s_branch FP46B_%=\n\t"
      "FP47R_%=:\n\t"
      "s_mov_b32 s51, 0\n\t"
      "FP47L_%=:\n\t"
      "s_add_u32 s51, s51, 1\n\t"
      "s_cmp_lt_u32 s51, 262144\n\t"
      "s_cbranch_scc0 FP47X_%=\n\t"
      "s_sleep 1\n\t"
      "s_mov_b64 exec, 15\n\t"
      "v_mov_b64_e32 v[232:233], 0\n\t"
      "global_atomic_or_x2 v[232:233], v247, v[232:233], s[12:13] sc0 sc1\n\t"
      "s_waitcnt vmcnt(0)\n\t"
      "v_subrev_u32_e32 v232, s14, v232\n\t"
      "v_cmp_ne_u32_e64 s[48:49], s15, v233\n\t"
      "v_subrev_u32_e32 v232, 6, v232\n\t"
      "v_cmp_lt_u32_e32 vcc, 7, v232\n\t"
      "s_mov_b64 exec, -1\n\t"
      "s_or_b64 vcc, vcc, s[48:49]\n\t"
      "s_cbranch_vccnz FP47L_%=\n\t"
      "FP47X_%=:\n\t"
      "FP47DR_%=:\n\t"
      "s_mov_b32 s51, 0\n\t"
      "FP47DL_%=:\n\t"
      "s_add_u32 s51, s51, 1\n\t"
      "s_cmp_lt_u32 s51, 262144\n\t"
      "s_cbranch_scc0 FP47DX_%=\n\t"
      "s_waitcnt vmcnt(0)\n\t"
      "s_sleep 1\n\t"
      "global_load_dwordx4 v[148:151], v229, s[64:65] sc1\n\t"
      "global_load_dwordx4 v[152:155], v230, s[64:65] sc1\n\t"
      "s_waitcnt vmcnt(1)\n\t"
      "v_max3_u32 v234, v148, v149, v150\n\t"
      "v_max_u32_e32 v234, v234, v151\n\t"
      "v_cmp_eq_u32_e32 vcc, -1, v234\n\t"
      "s_cbranch_vccnz FP47DL_%=\n\t"
      "FP47DX_%=:\n\t"
      "s_branch FP47B_%=\n\t"
      "FP48R_%=:\n\t"
      "s_mov_b32 s51, 0\n\t"
      "FP48L_%=:\n\t"
      "s_add_u32 s51, s51, 1\n\t"
      "s_cmp_lt_u32 s51, 262144\n\t"
      "s_cbranch_scc0 FP48X_%=\n\t"
      "s_sleep 1\n\t"
      "s_mov_b64 exec, 15\n\t"
      "v_mov_b64_e32 v[232:233], 0\n\t"
      "global_atomic_or_x2 v[232:233], v247, v[232:233], s[12:13] sc0 sc1\n\t"
      "s_waitcnt vmcnt(0)\n\t"
      "v_subrev_u32_e32 v232, s14, v232\n\t"
      "v_cmp_ne_u32_e64 s[48:49], s15, v233\n\t"
      "v_subrev_u32_e32 v232, 6, v232\n\t"
      "v_cmp_lt_u32_e32 vcc, 7, v232\n\t"
      "s_mov_b64 exec, -1\n\t"
      "s_or_b64 vcc, vcc, s[48:49]\n\t"
      "s_cbranch_vccnz FP48L_%=\n\t"
      "FP48X_%=:\n\t"
      "FP48DR_%=:\n\t"
      "s_mov_b32 s51, 0\n\t"
      "FP48DL_%=:\n\t"
      "s_add_u32 s51, s51, 1\n\t"
      "s_cmp_lt_u32 s51, 262144\n\t"
      "s_cbranch_scc0 FP48DX_%=\n\t"
      "s_waitcnt vmcnt(0)\n\t"
      "s_sleep 1\n\t"
      "global_load_dwordx4 v[152:155], v230, s[64:65] sc1\n\t"
      "s_waitcnt vmcnt(0)\n\t"
      "v_max3_u32 v234, v152, v153, v154\n\t"
      "v_max_u32_e32 v234, v234, v155\n\t"
      "v_cmp_eq_u32_e32 vcc, -1, v234\n\t"
      "s_cbranch_vccnz FP48DL_%=\n\t"
      "FP48DX_%=:\n\t"
      "s_waitcnt vmcnt(0)\n\t"
      "s_branch FP48B_%=\n\t"
      "NP6R_%=:\n\t"
      "s_mov_b32 s51, 0\n\t"
      "NP6L_%=:\n\t"
      "s_add_u32 s51, s51, 1\n\t"
      "s_cmp_lt_u32 s51, 262144\n\t"
      "s_cbranch_scc0 NP6X_%=\n\t"
      "s_waitcnt vmcnt(0)\n\t"
      "s_sleep 1\n\t"
      "global_load_dwordx4 v[128:131], v224, s[64:65] sc1\n\t"
      "global_load_dwordx4 v[132:135], v225, s[64:65] sc1\n\t"
      "global_load_dwordx4 v[136:139], v226, s[64:65] sc1\n\t"
      "global_load_dwordx4 v[140:143], v227, s[64:65] sc1\n\t"
      "global_load_dwordx4 v[144:147], v228, s[64:65] sc1\n\t"
      "global_load_dwordx4 v[148:151], v229, s[64:65] sc1\n\t"
      "global_load_dwordx4 v[152:155], v230, s[64:65] sc1\n\t"
      "s_waitcnt vmcnt(0)\n\t"
      "v_max3_u32 v234, v128, v129, v130\n\t"
      "v_max3_u32 v234, v234, v131, v132\n\t"
      "v_max3_u32 v234, v234, v133, v134\n\t"
      "v_max3_u32 v234, v234, v135, v136\n\t"
      "v_max3_u32 v234, v234, v137, v138\n\t"
      "v_max3_u32 v234, v234, v139, v140\n\t"
      "v_max3_u32 v234, v234, v141, v142\n\t"
      "v_max3_u32 v234, v234, v143, v144\n\t"
      "v_max3_u32 v234, v234, v145, v146\n\t"
      "v_max3_u32 v234, v234, v147, v148\n\t"
      "v_max3_u32 v234, v234, v149, v150\n\t"
      "v_max3_u32 v234, v234, v151, v152\n\t"
      "v_max3_u32 v234, v234, v153, v154\n\t"
      "v_max_u32_e32 v234, v234, v155\n\t"
      "v_cmp_eq_u32_e32 vcc, -1, v234\n\t"
      "s_cbranch_vccnz NP6L_%=\n\t"
      "NP6X_%=:\n\t"
      "v_mfma_f32_16x16x32_bf16 v[184:187], v[128:131], v[0:3], v[180:183]\n\t"
      "v_mfma_f32_16x16x32_bf16 v[188:191], v[128:131], v[4:7], 0\n\t"
      "v_mfma_f32_16x16x32_bf16 v[192:195], v[128:131], v[8:11], 0\n\t"
      "v_mfma_f32_16x16x32_bf16 v[196:199], v[128:131], v[12:15], 0\n\t"
      "v_mfma_f32_16x16x32_bf16 v[184:187], v[132:135], v[16:19], v[184:187]\n\t"
      "v_mfma_f32_16x16x32_bf16 v[188:191], v[132:135], v[20:23], v[188:191]\n\t"
      "v_mfma_f32_16x16x32_bf16 v[192:195], v[132:135], v[24:27], v[192:195]\n\t"
      "v_mfma_f32_16x16x32_bf16 v[196:199], v[132:135], v[28:31], v[196:199]\n\t"
      "v_mfma_f32_16x16x32_bf16 v[184:187], v[136:139], v[32:35], v[184:187]\n\t"
      "v_mfma_f32_16x16x32_bf16 v[188:191], v[136:139], v[36:39], v[188:191]\n\t"
      "v_mfma_f32_16x16x32_bf16 v[192:195], v[136:139], v[40:43], v[192:195]\n\t"
      "v_mfma_f32_16x16x32_bf16 v[196:199], v[136:139], v[44:47], v[196:199]\n\t"
      "v_mfma_f32_16x16x32_bf16 v[184:187], v[140:143], v[48:51], v[184:187]\n\t"
      "v_mfma_f32_16x16x32_bf16 v[188:191], v[140:143], v[52:55], v[188:191]\n\t"
      "v_mfma_f32_16x16x32_bf16 v[192:195], v[140:143], v[56:59], v[192:195]\n\t"
      "v_mfma_f32_16x16x32_bf16 v[196:199], v[140:143], v[60:63], v[196:199]\n\t"
      "v_mfma_f32_16x16x32_bf16 v[184:187], v[144:147], v[64:67], v[184:187]\n\t"
      "v_mfma_f32_16x16x32_bf16 v[188:191], v[144:147], v[68:71], v[188:191]\n\t"
      "v_mfma_f32_16x16x32_bf16 v[192:195], v[144:147], v[72:75], v[192:195]\n\t"
      "v_mfma_f32_16x16x32_bf16 v[196:199], v[144:147], v[76:79], v[196:199]\n\t"
      "v_mfma_f32_16x16x32_bf16 v[184:187], v[148:151], v[80:83], v[184:187]\n\t"
      "v_mfma_f32_16x16x32_bf16 v[188:191], v[148:151], v[84:87], v[188:191]\n\t"
      "v_mfma_f32_16x16x32_bf16 v[192:195], v[148:151], v[88:91], v[192:195]\n\t"
      "v_mfma_f32_16x16x32_bf16 v[196:199], v[148:151], v[92:95], v[196:199]\n\t"
      "v_mfma_f32_16x16x32_bf16 v[184:187], v[152:155], v[96:99], v[184:187]\n\t"
      "v_mfma_f32_16x16x32_bf16 v[188:191], v[152:155], v[100:103], v[188:191]\n\t"
      "v_mfma_f32_16x16x32_bf16 v[192:195], v[152:155], v[104:107], v[192:195]\n\t"
      "v_mfma_f32_16x16x32_bf16 v[196:199], v[152:155], v[108:111], v[196:199]\n\t"
      "s_waitcnt vmcnt(0)\n\t"
      "s_nop 7\n\t"
      "s_branch NP6B_%=\n\t"
      : "+{s[4:19]}"(s), "+{s[20:27]}"(s2), "+{s[28:29]}"(kr), "+{v[240:247]}"(v), "+{v[136:139]}"(vo), "+{v140}"(vq)
      :
      : "memory", "vcc", "scc",
        "v0", "v1", "v2", "v3", "v4", "v5", "v6", "v7", "v8", "v9", "v10", "v11", "v12", "v13", "v14", "v15", "v16",
        "v17", "v18", "v19", "v20", "v21", "v22", "v23", "v24", "v25", "v26", "v27", "v28", "v29", "v30", "v31", "v32",
        "v33", "v34", "v35", "v36", "v37", "v38", "v39", "v40", "v41", "v42", "v43", "v44", "v45", "v46", "v47", "v48",
        "v49", "v50", "v51", "v52", "v53", "v54", "v55", "v56", "v57", "v58", "v59", "v60", "v61", "v62", "v63", "v64",
        "v65", "v66", "v67", "v68", "v69", "v70", "v71", "v72", "v73", "v74", "v75", "v76", "v77", "v78", "v79", "v80",
        "v81", "v82", "v83", "v84", "v85", "v86", "v87", "v88", "v89", "v90", "v91", "v92", "v93", "v94", "v95", "v96",
        "v97", "v98", "v99", "v100", "v101", "v102", "v103", "v104", "v105", "v106", "v107", "v108", "v109", "v110",
        "v111", "v112", "v113", "v114", "v115", "v116", "v117", "v118", "v119", "v120", "v121", "v122", "v123", "v124",
        "v125", "v126", "v127", "v128", "v129", "v130", "v131", "v132", "v133", "v134", "v135", "v141", "v142", "v143",
        "v144", "v145", "v146", "v147", "v148", "v149", "v150", "v151", "v152", "v153", "v154", "v155", "v156", "v157",
        "v158", "v159", "v160", "v161", "v162", "v163", "v164", "v165", "v166", "v167", "v168", "v169", "v170", "v171",
        "v172", "v173", "v174", "v175", "v176", "v177", "v178", "v179", "v180", "v181", "v182", "v183", "v184", "v185",
        "v186", "v187", "v188", "v189", "v190", "v191", "v192", "v193", "v194", "v195", "v196", "v197", "v198", "v199",
        "v200", "v201", "v202", "v203", "v204", "v205", "v206", "v207", "v208", "v209", "v210", "v211", "v212", "v213",
        "v214", "v215", "v216", "v217", "v218", "v219", "v220", "v221", "v222", "v223", "v224", "v225", "v226", "v227",
        "v228", "v229", "v230", "v231", "v232", "v233", "v234", "v235", "v236", "v237", "v238", "v239", "v248", "v249",
        "v250", "v251", "v252", "v253", "s44", "s45", "s46", "s47", "s48", "s49", "s50", "s51", "s52", "s53", "s54",
        "s55", "s56", "s57", "s58", "s59", "s60", "s61", "s62", "s63", "s64", "s65", "s66", "s67", "s68", "s69", "s70"
    );
    __builtin_amdgcn_endpgm();
  }
}

extern "C" void kernel_launch(void* const* d_in, const int* in_sizes, int n_in, void* d_out, int out_size,
                              void* d_ws, size_t ws_size, hipStream_t stream) {
  (void)hipFuncSetAttribute((const void*)fused_block, hipFuncAttributeMaxDynamicSharedMemorySize, LDS_BYTES);
  fused_block<<<256, 512, LDS_BYTES, stream>>>((const char*)d_in[0], (const char*)d_in[1], (const char*)d_in[2],
                                               (const char*)d_in[4], (const char*)d_in[5], (const char*)d_in[6],
                                               (const char*)d_in[7], (char*)d_out, (char*)d_ws);
}
```

```cpp
#include <hip/hip_runtime.h>
#include <stdint.h>

#define WS_O 9437184
#define WS_HN 2097152
#define WS_G 6291456
#define WS_ST 8388608
#define ST_FL 65536
#define ST_H3 262144
#define ST_H4 393216
#define ST_H2 32768
#define ST_CK 524288
#define ST_PW 114688
#define LDS_PR 163296
#define WS_EP 8519680
#define A_UNIT 5120
#define A_HALO 40960
#define A_SLOT 43008
#define A_CS 131072
#define A_ST 132096
#define A_EX 142336
#define A_FL 154624
#define B_UNIT 7168
#define B_SLOT 57344
#define B_X 114688
#define B_OP 928
#define B_PS 131072
#define G_WV 16384
#define A_W0 49152
#define A_WD 40960
#define G_RED 0
#define G_RED2 32768
#define LDS_KEY 163312
#define LDS_SINK 163328
#define LDS_BYTES 163840
#define XO 3
#define KROT 14
#define KMUL 1
#define HP4 1
#define W2E 1
#define R4 0
#define SP3 1

typedef unsigned short u16;
typedef unsigned long long u64;
typedef __attribute__((ext_vector_type(4))) unsigned int u32x4;
typedef __attribute__((ext_vector_type(8))) unsigned int u32x8;
typedef __attribute__((ext_vector_type(16))) unsigned int u32x16;
#define UNI(x) (unsigned)__builtin_amdgcn_readfirstlane((int)(x))
#define PIN() __builtin_amdgcn_sched_barrier(0)

__device__ __forceinline__ int swz_tr(int row) { return (((row >> 1) & 1) | (((row >> 3) & 1) << 1)) << 1; }
__device__ __forceinline__ int swz_row(int row) { return (row >> 1) & 7; }
__device__ __forceinline__ int unit(int blk, int slice, int par) { return (((blk < 14 ? blk : blk - 14) * 8 + slice) * 2 + ((blk < 14 ? 0 : 1) ^ par)) * 8192; }
template <class T>
__device__ __forceinline__ void put(T& s, int i, u64 p) { s[i] = (unsigned)p; s[i + 1] = (unsigned)(p >> 32); }
template <class T>
__device__ __forceinline__ void put2(T& s, int i, unsigned lo, unsigned hi) { s[i] = UNI(lo); s[i + 1] = UNI(hi); }

__global__ __launch_bounds__(512, 2) void fused_block(const char* __restrict__ x, const char* __restrict__ wqkv,
                                                   const char* __restrict__ wo, const char* __restrict__ w1,
                                                   const char* __restrict__ b1, const char* __restrict__ w2,
                                                   const char* __restrict__ b2, char* __restrict__ out,
                                                   char* __restrict__ ws0) {
  u32x8 f;
  asm volatile(
      "s_load_dwordx4 s[4:7], s[0:1], 0x0\n\t"
      "v_readfirstlane_b32 s3, v0\n\t"
      "s_getpc_b64 s[40:41]\n\t"
      "s_lshr_b32 s12, s2, 3\n\t"
      "s_or_b32 s13, s3, s12\n\t"
      "s_and_b32 s13, s13, 0x1d0\n\t"
      "s_cmp_lg_u32 s13, 16\n\t"
      "s_cbranch_scc1 ET2_%=\n\t"
      "s_and_b32 s12, s12, 7\n\t"
      "s_mov_b32 m0, 163328\n\t"
      "v_lshl_add_u32 v3, s12, 6, v0\n\t"
      "v_min_u32_e32 v3, 504, v3\n\t"
      "v_lshlrev_b32_e32 v3, 7, v3\n\t"
      "global_load_lds_dword v3, s[40:41]\n\t"
      "ET2_%=:\n\t"
      "v_lshrrev_b32_e32 v1, 3, v0\n\t"
      "v_and_b32_e32 v2, 7, v0\n\t"
      "s_and_b32 s17, s2, 7\n\t"
      "s_lshr_b32 s18, s2, 3\n\t"
      "s_lshr_b32 s3, s3, 6\n\t"
      "s_cmpk_gt_u32 s2, 0x7f\n\t"
      "s_cbranch_scc1 EB_%=\n\t"
      "v_and_b32_e32 v3, 2, v1\n\t"
      "v_lshrrev_b32_e32 v4, 1, v1\n\t"
      "s_lshr_b32 s19, s18, 3\n\t"
      "s_lshl1_add_u32 s19, s17, s19\n\t"
      "v_and_or_b32 v3, v4, 4, v3\n\t"
      "v_xor_b32_e32 v3, v3, v2\n\t"
      "v_min_u32_e32 v3, 6, v3\n\t"
      "v_mul_u32_u24_e32 v5, 2688, v1\n\t"
      "v_lshl_add_u32 v3, v3, 4, v5\n\t"
      "v_add_u32_e32 v195, 2048, v3\n\t"
      "v_add_u32_e32 v196, 1920, v3\n\t"
      "v_add_u32_e32 v197, 5888, v3\n\t"
      "s_mul_i32 s12, s19, 114688\n\t"
      "v_and_b32_e32 v5, 0x38, v1\n\t"
      "v_add_u32_e32 v5, v5, v1\n\t"
      "v_mul_u32_u24_e32 v5, 896, v5\n\t"
      "s_and_b32 s13, s18, 7\n\t"
      "s_mul_i32 s13, s13, 112\n\t"
      "v_and_b32_e32 v6, 3, v4\n\t"
      "v_xor_b32_e32 v6, v6, v2\n\t"
      "v_lshl_add_u32 v5, v6, 4, v5\n\t"
      "s_sub_u32 s12, s12, 15360\n\t"
      "s_ashr_i32 s14, s12, 31\n\t"
      "s_sub_u32 s13, s13, 4096\n\t"
      "v_add_u32_e32 v192, 15360, v5\n\t"
      "v_xor_b32_e32 v5, 64, v5\n\t"
      "v_add_u32_e32 v193, 21504, v5\n\t"
      "s_mul_i32 s9, s3, 5120\n\t"
      "s_waitcnt lgkmcnt(0)\n\t"
      "s_add_u32 s4, s4, s12\n\t"
      "s_addc_u32 s5, s5, s14\n\t"
      "s_add_u32 s6, s6, s13\n\t"
      "s_addc_u32 s7, s7, -1\n\t"
      "s_add_u32 m0, s9, 0\n\t"
      "s_nop 0\n\t"
      "global_load_lds_dwordx4 v195, s[6:7] offset:2048\n\t"
      "global_load_lds_dwordx4 v192, s[4:5]\n\t"
      "global_load_lds_dwordx4 v193, s[4:5] offset:1024\n\t"
      "global_load_lds_dwordx4 v196, s[6:7] offset:3072\n\t"
      "s_add_u32 m0, s9, 4096\n\t"
      "s_nop 0\n\t"
      "global_load_lds_dwordx4 v197, s[6:7]\n\t"
      "s_sub_u32 s3, s3, 4\n\t"
      "s_cbranch_scc1 EX_%=\n\t"
      "s_cmp_eq_u32 s19, 0\n\t"
      "s_cselect_b32 s13, 16, 0\n\t"
      "s_lshl2_add_u32 s13, s3, s13\n\t"
      "v_and_or_b32 v3, v1, 3, s13\n\t"
      "v_bfe_u32 v4, v3, 1, 3\n\t"
      "v_mul_u32_u24_e32 v3, 896, v3\n\t"
      "s_cmp_eq_u32 s3, 0\n\t"
      "s_cselect_b32 s10, 0xffffff00, -1\n\t"
      "s_mov_b32 s11, 0\n\t"
      "v_xor_b32_e32 v4, v4, v2\n\t"
      "v_lshl_add_u32 v3, v4, 4, v3\n\t"
      "v_add_u32_e32 v194, 1024, v3\n\t"
      "s_lshl_b32 s12, s3, 9\n\t"
      "s_add_u32 s12, s12, 40960\n\t"
      "s_add_u32 m0, s12, 0\n\t"
      "s_mov_b64 exec, s[10:11]\n\t"
      "global_load_lds_dwordx4 v194, s[4:5]\n\t"
      "s_mov_b64 exec, -1\n\t"
      "s_sub_u32 s40, s4, 57344\n\t"
      "s_subb_u32 s41, s5, 0\n\t"
      "s_sub_u32 s42, s6, 86016\n\t"
      "s_subb_u32 s43, s7, 0\n\t"
      "s_add_u32 s40, s40, 128\n\t"
      "s_addc_u32 s41, s41, 0\n\t"
      "s_add_u32 s42, s42, 172032\n\t"
      "s_addc_u32 s43, s43, 0\n\t"
      "s_add_u32 m0, s9, 22528\n\t"
      "s_nop 0\n\t"
      "global_load_lds_dwordx4 v195, s[42:43] offset:2048\n\t"
      "global_load_lds_dwordx4 v192, s[40:41]\n\t"
      "global_load_lds_dwordx4 v193, s[40:41] offset:1024\n\t"
      "global_load_lds_dwordx4 v196, s[42:43] offset:3072\n\t"
      "s_add_u32 m0, s9, 26624\n\t"
      "s_nop 0\n\t"
      "global_load_lds_dwordx4 v197, s[42:43]\n\t"
      "s_branch EX_%=\n\t"
      "EB_%=:\n\t"
      "v_and_b32_e32 v3, 63, v0\n\t"
      "v_lshlrev_b32_e32 v3, 4, v3\n\t"
      "s_sub_u32 s18, s18, 16\n\t"
      "s_min_u32 s16, s3, 6\n\t"
      "s_lshl4_add_u32 s15, s17, s18\n\t"
      "s_mul_i32 s15, s15, 14336\n\t"
      "s_lshl_b32 s12, s16, 7\n\t"
      "s_add_u32 s15, s15, s12\n\t"
      "v_mul_u32_u24_e32 v6, 56, v3\n\t"
      "v_add_u32_e32 v6, s15, v6\n\t"
      "s_add_u32 s19, s17, 3\n\t"
      "s_and_b32 s19, s19, 7\n\t"
      "s_lshl4_add_u32 s19, s19, s18\n\t"
      "s_lshl3_add_u32 s12, s19, s3\n\t"
      "s_lshl_b32 s12, s12, 11\n\t"
      "s_mul_i32 s19, s19, 14336\n\t"
      "s_lshl_b32 s16, s16, 11\n\t"
      "s_add_u32 s19, s19, s16\n\t"
      "s_add_u32 s8, s16, 114688\n\t"
      "v_add_u32_e32 v1, s19, v3\n\t"
      "s_lshl4_add_u32 s16, s3, s18\n\t"
      "s_mul_i32 s13, s16, 6784\n\t"
      "s_add_u32 s13, s13, 344064\n\t"
      "s_mul_i32 s14, s16, 2688\n\t"
      "s_add_u32 s14, s14, 0\n\t"
      "v_min_u32_e32 v4, 832, v3\n\t"
      "v_lshl_add_u32 v4, v4, 3, s13\n\t"
      "v_min_u32_e32 v4, 1204096, v4\n\t"
      "v_min_u32_e32 v5, 320, v3\n\t"
      "v_lshl_add_u32 v5, v5, 3, s14\n\t"
      "s_mov_b32 m0, 163328\n\t"
      "s_waitcnt lgkmcnt(0)\n\t"
      "s_mov_b64 exec, 0xffff\n\t"
      "global_load_lds_dword v6, s[4:5]\n\t"
      "s_mov_b64 exec, -1\n\t"
      "global_load_lds_dword v5, s[6:7]\n\t"
      "s_mov_b32 m0, s8\n\t"
      "s_nop 0\n\t"
      "global_load_lds_dwordx4 v1, s[4:5]\n\t"
      "global_load_lds_dwordx4 v1, s[4:5] offset:1024\n\t"
      "s_mov_b32 m0, 163328\n\t"
      "s_barrier\n\t"
      "global_load_lds_dword v4, s[6:7]\n\t"
      "EX_%=:\n\t"
      : "={v[192:199]}"(f)
      : "{s[0:1]}"(__builtin_amdgcn_kernarg_segment_ptr()), "{s2}"(blockIdx.x), "{v0}"(threadIdx.x)
      : "memory", "scc", "s3", "s4", "s5", "s6", "s7", "s8", "s9", "s10", "s11", "s12", "s13", "s14", "s15", "s16", "s17", "s18", "s19", "v1", "v2",
        "v3", "v4", "v5", "v6", "s40", "s41", "s42", "s43", "s48", "s49", "s50"
      );
  PIN();
  char* const ws = ws0 + (-(unsigned)(uintptr_t)ws0 & ((2u << 20) - 1));
  const int wave = __builtin_amdgcn_readfirstlane(threadIdx.x >> 6);
  const int xcd = blockIdx.x & 7, idx = blockIdx.x >> 3, side = xcd >> 2, xp = xcd & 3;
#define LANE_BITS(l) \
  const int lane = (l), g = lane >> 4, c = lane & 15, q4 = c >> 2, p4 = lane & 3, lr = lane >> 3, lc = lane & 7

  if (__builtin_expect(idx < 16, 1)) {
    const int rt = 2 * xcd + (idx >> 3), hp = idx & 7, hh = (wave >> 1) & 1, half = wave & 1;
    const unsigned sta = A_ST + half * 1024;
    if (wave < 4) {
      LANE_BITS(threadIdx.x & 63);
      u32x16 v;
      u32x8 s;
      v[0] = 4 * half * A_UNIT + c * 128 + ((g ^ swz_row(c)) << 4);
      v[1] = A_HALO + c * 128 + ((g ^ swz_row(c)) << 4);
#pragma unroll
      for (int j = 0; j < 2; ++j) {
        const int by = 56 * hh + 32 * j + 8 * p4;
        v[2 + j] = g * A_UNIT + 2048 + q4 * 128 + (((by >> 4) ^ swz_tr(8 * g + q4)) << 4) + (by & 8);
      }
      const unsigned csa = A_CS + hh * 384, exa = A_EX + hh * 3 * 2048;
      v[4] = csa + g * 16;
      v[5] = csa + c * 4;
      v[6] = v[4] + 64 * half;
      v[7] = v[5] + 64 * half;
      v[8] = A_FL + (wave ^ 1) * 8;
      v[9] = sta + g * 16;
      v[10] = sta + c * 4;
      const int key = 4 * g + (c & 1);
      v[11] = sta + 384 + (key <= c ? 128 : 64) + key * 4;
      unsigned vk1 = sta + 384 + (key + 2 <= c ? 128 : 64) + key * 4 + 8;
      v[12] = exa + half * 2048 + lane * 16;
      v[13] = exa + half * 2048 + lane * 8;
      v[14] = exa + lane * 16 + 8 * half;
      v[15] = lane * 16;
      put(s, 0, (u64)(ws + WS_O + ((8 * rt + 4 * half) * 16 + 2 * hp + hh) * 1024));
      s[2] = half;
      s[3] = UNI(rt == 0 && half == 0 ? 0xff800000u : 0u);
      s[4] = exa + 1024 + 512 * half;
      s[5] = A_FL + 32 + 4 * half;
      put(s, 6, (u64)(ws + WS_ST));
      unsigned hint = ST_H2 + (((rt * 8 + hp) * 2 + half) * 2 + hh) * 64;
      unsigned chk = ST_CK + ((rt * 2 + half) * 16 + 2 * hp + hh) * 256;
    asm volatile(
      "s_waitcnt lgkmcnt(0)\n\ts_nop 4\n\t"
      "v_xor_b32_e32 v248, 64, v224\n\t"
      "v_xor_b32_e32 v249, 64, v225\n\t"
      "s_mov_b32 s36, 0xaaaaaaaa\n\t"
      "s_mov_b32 s37, 0xaaaaaaaa\n\t"
      "s_mov_b32 s38, 0xfff0ffff\n\t"
      "s_mov_b32 s39, 0xf000ff00\n\t"
      "s_mov_b32 s40, 0xffc0fffc\n\t"
      "s_mov_b32 s41, 0xc000fc00\n\t"
      "s_mov_b32 s42, 0x0\n\t"
      "s_mov_b32 s43, 0xffff0000\n\t"
      "s_mov_b32 s44, 0x10001\n\t"
      "s_mov_b32 s45, 0x10001\n\t"
      "v_mov_b32_e32 v216, 0xffff\n\t"
      "v_mov_b32_e32 v217, 0xffff0000\n\t"
      "v_cndmask_b32_e64 v216, v216, v217, s[36:37]\n\t"
      "v_cndmask_b32_e64 v240, v216, 0, s[38:39]\n\t"
      "v_cndmask_b32_e64 v241, v216, 0, s[40:41]\n\t"
      "v_cndmask_b32_e64 v242, 0, v216, s[38:39]\n\t"
      "v_cndmask_b32_e64 v243, 0, v216, s[40:41]\n\t"
      "v_mov_b32_e32 v244, s7\n\t"
      "v_mov_b32_e32 v245, s7\n\t"
      "v_mov_b32_e32 v246, s7\n\t"
      "v_mov_b32_e32 v247, s7\n\t"
      "v_mov_b64_e32 v[0:1], 0\n\t"
      "v_mov_b64_e32 v[2:3], 0\n\t"
      "v_mov_b64_e32 v[4:5], 0\n\t"
      "v_mov_b64_e32 v[6:7], 0\n\t"
      "v_mov_b64_e32 v[8:9], 0\n\t"
      "v_mov_b64_e32 v[10:11], 0\n\t"
      "v_mov_b64_e32 v[12:13], 0\n\t"
      "v_mov_b64_e32 v[14:15], 0\n\t"
      "v_mov_b64_e32 v[16:17], 0\n\t"
      "v_mov_b64_e32 v[18:19], 0\n\t"
      "v_mov_b64_e32 v[20:21], 0\n\t"
      "v_mov_b64_e32 v[22:23], 0\n\t"
      "v_mov_b64_e32 v[24:25], 0\n\t"
      "v_mov_b64_e32 v[26:27], 0\n\t"
      "v_mov_b64_e32 v[28:29], 0\n\t"
      "v_mov_b64_e32 v[30:31], 0\n\t"
      "v_mov_b64_e32 v[32:33], 0\n\t"
      "v_mov_b64_e32 v[34:35], 0\n\t"
      "v_mov_b64_e32 v[36:37], 0\n\t"
      "v_mov_b64_e32 v[38:39], 0\n\t"
      "v_mov_b64_e32 v[40:41], 0\n\t"
      "v_mov_b64_e32 v[42:43], 0\n\t"
      "v_mov_b64_e32 v[44:45], 0\n\t"
      "v_mov_b64_e32 v[46:47], 0\n\t"
      "v_mov_b64_e32 v[48:49], 0\n\t"
      "v_mov_b64_e32 v[50:51], 0\n\t"
      "v_mov_b64_e32 v[52:53], 0\n\t"
      "v_mov_b64_e32 v[54:55], 0\n\t"
      "v_mov_b64_e32 v[56:57], 0\n\t"
      "v_mov_b64_e32 v[58:59], 0\n\t"
      "v_mov_b64_e32 v[60:61], 0\n\t"
      "v_mov_b64_e32 v[62:63], 0\n\t"
      "v_mov_b64_e32 v[64:65], 0\n\t"
      "v_mov_b64_e32 v[66:67], 0\n\t"
      "v_mov_b64_e32 v[68:69], 0\n\t"
      "v_mov_b64_e32 v[70:71], 0\n\t"
      "v_mov_b64_e32 v[72:73], 0\n\t"
      "v_mov_b64_e32 v[74:75], 0\n\t"
      "v_mov_b64_e32 v[76:77], 0\n\t"
      "v_mov_b64_e32 v[78:79], 0\n\t"
      "v_mov_b64_e32 v[80:81], 0\n\t"
      "v_mov_b64_e32 v[82:83], 0\n\t"
      "v_mov_b64_e32 v[84:85], 0\n\t"
      "v_mov_b64_e32 v[86:87], 0\n\t"
      "v_mov_b64_e32 v[88:89], 0\n\t"
      "v_mov_b64_e32 v[90:91], 0\n\t"
      "v_mov_b64_e32 v[92:93], 0\n\t"
      "v_mov_b64_e32 v[94:95], 0\n\t"
      "v_mov_b64_e32 v[96:97], 0\n\t"
      "v_mov_b64_e32 v[98:99], 0\n\t"
      "v_mov_b64_e32 v[100:101], 0\n\t"
      "v_mov_b64_e32 v[102:103], 0\n\t"
      "v_mov_b64_e32 v[104:105], 0\n\t"
      "v_mov_b64_e32 v[106:107], 0\n\t"
      "s_mov_b32 s47, 6\n\t"
      "s_cmp_eq_u32 s6, 0\n\t"
      "s_cbranch_scc0 LOOP1_%=\n\t"
      "s_waitcnt vmcnt(0)\n\t"
      "s_barrier\n\t"
      "ds_read_b64_tr_b16 v[108:109], v226\n\t"
      "ds_read_b64_tr_b16 v[110:111], v226 offset:512\n\t"
      "ds_read_b128 v[132:135], v224\n\t"
      "ds_read_b128 v[136:139], v224 offset:5120\n\t"
      "ds_read_b128 v[140:143], v224 offset:10240\n\t"
      "ds_read_b128 v[144:147], v224 offset:15360\n\t"
      "ds_read_b64_tr_b16 v[112:113], v227\n\t"
      "ds_read_b64_tr_b16 v[114:115], v227 offset:512\n\t"
      "ds_read_b128 v[148:151], v225\n\t"
      "ds_read_b64_tr_b16 v[116:117], v226 offset:1024\n\t"
      "ds_read_b64_tr_b16 v[118:119], v226 offset:1536\n\t"
      "ds_read_b64_tr_b16 v[120:121], v227 offset:1024\n\t"
      "ds_read_b64_tr_b16 v[122:123], v227 offset:1536\n\t"
      "ds_read_b64_tr_b16 v[124:125], v226 offset:2048\n\t"
      "ds_read_b64_tr_b16 v[126:127], v226 offset:2560\n\t"
      "ds_read_b64_tr_b16 v[128:129], v227 offset:2048\n\t"
      "ds_read_b64_tr_b16 v[130:131], v227 offset:2560\n\t"
      "s_branch EVEN0_%=\n\t"
      "s_waitcnt lgkmcnt(0)\n\t"
      ".p2align 6\n\t"
      "ODD0_%=:\n\t"
      "s_waitcnt lgkmcnt(0)\n\t"
      "v_mfma_f32_16x16x32_bf16 v[0:3], v[152:155], v[176:179], v[0:3]\n\t"
      "v_add_u32_e32 v224, s46, v224\n\t"
      "v_add_u32_e32 v226, s46, v226\n\t"
      "v_mfma_f32_16x16x32_bf16 v[24:27], v[152:155], v[180:183], v[24:27]\n\t"
      "v_add_u32_e32 v227, s46, v227\n\t"
      "v_add_u32_e32 v225, s46, v225\n\t"
      "v_mfma_f32_16x16x32_bf16 v[48:51], v[152:155], v[184:187], v[48:51]\n\t"
      "s_barrier\n\t"
      "ds_read_b64_tr_b16 v[108:109], v226\n\t"
      "v_mfma_f32_16x16x32_bf16 v[72:75], v[152:155], v[188:191], v[72:75]\n\t"
      "ds_read_b64_tr_b16 v[110:111], v226 offset:512\n\t"
      "v_mfma_f32_16x16x32_bf16 v[4:7], v[156:159], v[176:179], v[4:7]\n\t"
      "ds_read_b128 v[132:135], v224\n\t"
      "v_mfma_f32_16x16x32_bf16 v[28:31], v[156:159], v[180:183], v[28:31]\n\t"
      "ds_read_b128 v[136:139], v224 offset:5120\n\t"
      "v_mfma_f32_16x16x32_bf16 v[52:55], v[156:159], v[184:187], v[52:55]\n\t"
      "ds_read_b128 v[140:143], v224 offset:10240\n\t"
      "v_mfma_f32_16x16x32_bf16 v[76:79], v[156:159], v[188:191], v[76:79]\n\t"
      "ds_read_b128 v[144:147], v224 offset:15360\n\t"
      "v_mfma_f32_16x16x32_bf16 v[96:99], v[152:155], v[192:195], v[96:99]\n\t"
      "v_mfma_f32_16x16x32_bf16 v[8:11], v[160:163], v[176:179], v[8:11]\n\t"
      "ds_read_b64_tr_b16 v[112:113], v227\n\t"
      "v_mfma_f32_16x16x32_bf16 v[32:35], v[160:163], v[180:183], v[32:35]\n\t"
      "ds_read_b64_tr_b16 v[114:115], v227 offset:512\n\t"
      "v_mfma_f32_16x16x32_bf16 v[56:59], v[160:163], v[184:187], v[56:59]\n\t"
      "ds_read_b128 v[148:151], v225\n\t"
      "v_mfma_f32_16x16x32_bf16 v[80:83], v[160:163], v[188:191], v[80:83]\n\t"
      "ds_read_b64_tr_b16 v[116:117], v226 offset:1024\n\t"
      "v_mfma_f32_16x16x32_bf16 v[12:15], v[164:167], v[176:179], v[12:15]\n\t"
      "ds_read_b64_tr_b16 v[118:119], v226 offset:1536\n\t"
      "v_mfma_f32_16x16x32_bf16 v[36:39], v[164:167], v[180:183], v[36:39]\n\t"
      "ds_read_b64_tr_b16 v[120:121], v227 offset:1024\n\t"
      "v_mfma_f32_16x16x32_bf16 v[60:63], v[164:167], v[184:187], v[60:63]\n\t"
      "v_mfma_f32_16x16x32_bf16 v[84:87], v[164:167], v[188:191], v[84:87]\n\t"
      "ds_read_b64_tr_b16 v[122:123], v227 offset:1536\n\t"
      "v_mfma_f32_16x16x32_bf16 v[100:103], v[160:163], v[192:195], v[100:103]\n\t"
      "ds_read_b64_tr_b16 v[124:125], v226 offset:2048\n\t"
      "v_mfma_f32_16x16x32_bf16 v[16:19], v[176:179], v[168:171], v[16:19]\n\t"
      "ds_read_b64_tr_b16 v[126:127], v226 offset:2560\n\t"
      "v_mfma_f32_16x16x32_bf16 v[40:43], v[180:183], v[168:171], v[40:43]\n\t"
      "ds_read_b64_tr_b16 v[128:129], v227 offset:2048\n\t"
      "v_mfma_f32_16x16x32_bf16 v[64:67], v[184:187], v[168:171], v[64:67]\n\t"
      "ds_read_b64_tr_b16 v[130:131], v227 offset:2560\n\t"
      "v_mfma_f32_16x16x32_bf16 v[88:91], v[188:191], v[168:171], v[88:91]\n\t"
      "v_mfma_f32_16x16x32_bf16 v[20:23], v[176:179], v[172:175], v[20:23]\n\t"
      "v_mfma_f32_16x16x32_bf16 v[44:47], v[180:183], v[172:175], v[44:47]\n\t"
      "v_add_u32_e32 v248, s46, v248\n\t"
      "v_add_u32_e32 v249, s46, v249\n\t"
      "v_mfma_f32_16x16x32_bf16 v[68:71], v[184:187], v[172:175], v[68:71]\n\t"
      "v_mfma_f32_16x16x32_bf16 v[92:95], v[188:191], v[172:175], v[92:95]\n\t"
      "v_mfma_f32_16x16x32_bf16 v[104:107], v[192:195], v[168:171], v[104:107]\n\t"
      "EVEN0_%=:\n\t"
      "s_waitcnt lgkmcnt(0)\n\t"
      "v_mfma_f32_16x16x32_bf16 v[0:3], v[108:111], v[132:135], v[0:3]\n\t"
      "ds_read_b64_tr_b16 v[152:153], v226 offset:20480\n\t"
      "v_mfma_f32_16x16x32_bf16 v[24:27], v[108:111], v[136:139], v[24:27]\n\t"
      "ds_read_b64_tr_b16 v[154:155], v226 offset:20992\n\t"
      "v_mfma_f32_16x16x32_bf16 v[48:51], v[108:111], v[140:143], v[48:51]\n\t"
      "ds_read_b128 v[176:179], v248\n\t"
      "v_mfma_f32_16x16x32_bf16 v[72:75], v[108:111], v[144:147], v[72:75]\n\t"
      "ds_read_b128 v[180:183], v248 offset:5120\n\t"
      "v_mfma_f32_16x16x32_bf16 v[4:7], v[112:115], v[132:135], v[4:7]\n\t"
      "v_mfma_f32_16x16x32_bf16 v[28:31], v[112:115], v[136:139], v[28:31]\n\t"
      "ds_read_b128 v[184:187], v248 offset:10240\n\t"
      "v_mfma_f32_16x16x32_bf16 v[52:55], v[112:115], v[140:143], v[52:55]\n\t"
      "ds_read_b128 v[188:191], v248 offset:15360\n\t"
      "v_mfma_f32_16x16x32_bf16 v[76:79], v[112:115], v[144:147], v[76:79]\n\t"
      "ds_read_b64_tr_b16 v[156:157], v227 offset:20480\n\t"
      "v_mfma_f32_16x16x32_bf16 v[96:99], v[108:111], v[148:151], v[96:99]\n\t"
      "v_mfma_f32_16x16x32_bf16 v[8:11], v[116:119], v[132:135], v[8:11]\n\t"
      "ds_read_b64_tr_b16 v[158:159], v227 offset:20992\n\t"
      "v_mfma_f32_16x16x32_bf16 v[32:35], v[116:119], v[136:139], v[32:35]\n\t"
      "ds_read_b128 v[192:195], v249\n\t"
      "v_mfma_f32_16x16x32_bf16 v[56:59], v[116:119], v[140:143], v[56:59]\n\t"
      "ds_read_b64_tr_b16 v[160:161], v226 offset:21504\n\t"
      "v_mfma_f32_16x16x32_bf16 v[80:83], v[116:119], v[144:147], v[80:83]\n\t"
      "ds_read_b64_tr_b16 v[162:163], v226 offset:22016\n\t"
      "v_mfma_f32_16x16x32_bf16 v[12:15], v[120:123], v[132:135], v[12:15]\n\t"
      "v_mfma_f32_16x16x32_bf16 v[36:39], v[120:123], v[136:139], v[36:39]\n\t"
      "ds_read_b64_tr_b16 v[164:165], v227 offset:21504\n\t"
      "v_mfma_f32_16x16x32_bf16 v[60:63], v[120:123], v[140:143], v[60:63]\n\t"
      "ds_read_b64_tr_b16 v[166:167], v227 offset:22016\n\t"
      "v_mfma_f32_16x16x32_bf16 v[84:87], v[120:123], v[144:147], v[84:87]\n\t"
      "ds_read_b64_tr_b16 v[168:169], v226 offset:22528\n\t"
      "v_mfma_f32_16x16x32_bf16 v[100:103], v[116:119], v[148:151], v[100:103]\n\t"
      "v_mfma_f32_16x16x32_bf16 v[16:19], v[132:135], v[124:127], v[16:19]\n\t"
      "ds_read_b64_tr_b16 v[170:171], v226 offset:23040\n\t"
      "v_mfma_f32_16x16x32_bf16 v[40:43], v[136:139], v[124:127], v[40:43]\n\t"
      "ds_read_b64_tr_b16 v[172:173], v227 offset:22528\n\t"
      "v_mfma_f32_16x16x32_bf16 v[64:67], v[140:143], v[124:127], v[64:67]\n\t"
      "ds_read_b64_tr_b16 v[174:175], v227 offset:23040\n\t"
      "s_cmp_eq_u32 s47, 4\n\t"
      "s_cselect_b32 s46, -2, 1\n\t"
      "v_mfma_f32_16x16x32_bf16 v[88:91], v[144:147], v[124:127], v[88:91]\n\t"
      "s_cmp_eq_u32 s47, 1\n\t"
      "s_cselect_b32 s46, -2, s46\n\t"
      "v_mfma_f32_16x16x32_bf16 v[20:23], v[132:135], v[128:131], v[20:23]\n\t"
      "s_mul_i32 s46, s46, 43008\n\t"
      "v_mfma_f32_16x16x32_bf16 v[44:47], v[136:139], v[128:131], v[44:47]\n\t"
      "v_mfma_f32_16x16x32_bf16 v[68:71], v[140:143], v[128:131], v[68:71]\n\t"
      "v_mfma_f32_16x16x32_bf16 v[92:95], v[144:147], v[128:131], v[92:95]\n\t"
      "s_sub_u32 s47, s47, 1\n\t"
      "v_mfma_f32_16x16x32_bf16 v[104:107], v[148:151], v[124:127], v[104:107]\n\t"
      "s_cbranch_scc0 ODD0_%=\n\t"
      "s_waitcnt lgkmcnt(0)\n\t"
      "v_mfma_f32_16x16x32_bf16 v[96:99], v[152:155], v[192:195], v[96:99]\n\t"
      "v_mov_b32_e32 v212, s9\n\t"
      "ds_read_b32 v212, v212\n\t"
      "ds_read2_b32 v[128:129], v234 offset0:32 offset1:32\n\t"
      "v_mfma_f32_16x16x32_bf16 v[100:103], v[160:163], v[192:195], v[100:103]\n\t"
      "ds_read2_b32 v[130:131], v234 offset0:48 offset1:48\n\t"
      "v_mfma_f32_16x16x32_bf16 v[104:107], v[192:195], v[168:171], v[104:107]\n\t"
      "ds_read2_b32 v[132:133], v234 offset0:64 offset1:64\n\t"
      "ds_read2_b32 v[134:135], v234 offset0:80 offset1:80\n\t"
      "v_mfma_f32_16x16x32_bf16 v[80:83], v[160:163], v[188:191], v[80:83]\n\t"
      "ds_read2_b32 v[248:249], v234 offset0:0 offset1:0\n\t"
      "v_mfma_f32_16x16x32_bf16 v[84:87], v[164:167], v[188:191], v[84:87]\n\t"
      "ds_read_b128 v[196:199], v233\n\t"
      "ds_read_b128 v[136:139], v233 offset:128\n\t"
      "v_mfma_f32_16x16x32_bf16 v[88:91], v[188:191], v[168:171], v[88:91]\n\t"
      "ds_read_b128 v[140:143], v233 offset:192\n\t"
      "v_mfma_f32_16x16x32_bf16 v[92:95], v[188:191], v[172:175], v[92:95]\n\t"
      "ds_read_b128 v[144:147], v233 offset:256\n\t"
      "ds_read_b128 v[148:151], v233 offset:320\n\t"
      "v_mfma_f32_16x16x32_bf16 v[56:59], v[160:163], v[184:187], v[56:59]\n\t"
      "s_mov_b64 exec, s[44:45]\n\t"
      "ds_write_b128 v230, v[96:99]\n\t"
      "ds_write_b128 v230, v[100:103] offset:128\n\t"
      "s_mov_b64 exec, 0xffff\n\t"
      "ds_write_b32 v231, v104 offset:256\n\t"
      "v_xor_b32_e32 v227, 8, v232\n\t"
      "ds_write_b32 v227, v227\n\t"
      "s_mov_b64 exec, -1\n\t"
      "v_mfma_f32_16x16x32_bf16 v[60:63], v[164:167], v[184:187], v[60:63]\n\t"
      "v_mfma_f32_16x16x32_bf16 v[64:67], v[184:187], v[168:171], v[64:67]\n\t"
      "v_mfma_f32_16x16x32_bf16 v[68:71], v[184:187], v[172:175], v[68:71]\n\t"
      "s_waitcnt lgkmcnt(4)\n\t"
      "v_mfma_f32_16x16x32_bf16 v[32:35], v[160:163], v[180:183], v[32:35]\n\t"
      "ds_read_b32 v226, v232\n\t"
      "ds_read_b128 v[116:119], v228 offset:128\n\t"
      "v_mfma_f32_16x16x32_bf16 v[36:39], v[164:167], v[180:183], v[36:39]\n\t"
      "ds_read_b128 v[120:123], v228 offset:192\n\t"
      "ds_read2_b32 v[250:251], v229 offset0:64 offset1:64\n\t"
      "v_mfma_f32_16x16x32_bf16 v[40:43], v[180:183], v[168:171], v[40:43]\n\t"
      "ds_read2_b32 v[252:253], v229 offset0:80 offset1:80\n\t"
      "ds_read_b128 v[124:127], v230 offset:128\n\t"
      "v_mfma_f32_16x16x32_bf16 v[44:47], v[180:183], v[172:175], v[44:47]\n\t"
      "ds_read2_b32 v[224:225], v231 offset0:64 offset1:64\n\t"
      "ds_read_b128 v[108:111], v228\n\t"
      "v_mfma_f32_16x16x32_bf16 v[8:11], v[160:163], v[176:179], v[8:11]\n\t"
      "ds_read_b128 v[112:115], v228 offset:64\n\t"
      "ds_read2_b32 v[204:205], v235 offset0:0 offset1:16\n\t"
      "v_mfma_f32_16x16x32_bf16 v[12:15], v[164:167], v[176:179], v[12:15]\n\t"
      "ds_read2_b32 v[206:207], v235 offset0:32 offset1:48\n\t"
      "ds_read2_b32 v[208:209], v254 offset0:0 offset1:16\n\t"
      "v_mfma_f32_16x16x32_bf16 v[16:19], v[176:179], v[168:171], v[16:19]\n\t"
      "ds_read2_b32 v[210:211], v254 offset0:32 offset1:48\n\t"
      "ds_read2_b32 v[200:201], v234 offset0:192 offset1:208\n\t"
      "v_mfma_f32_16x16x32_bf16 v[20:23], v[176:179], v[172:175], v[20:23]\n\t"
      "ds_read2_b32 v[202:203], v234 offset0:224 offset1:240\n\t"
      "LOOPX_%=:\n\t"
      "s_waitcnt lgkmcnt(14)\n\t"
      "v_min_u32_e32 v226, v226, v212\n\t"
      "v_cmp_eq_u32_e32 vcc, 0, v226\n\t"
      "s_cbranch_vccnz XAR_%=\n\t"
      "XAB_%=:\n\t"
      "s_waitcnt lgkmcnt(12)\n\t"
      "v_pk_fma_f32 v[80:81], v[134:135], v[116:117], v[80:81]\n\t"
      "v_pk_fma_f32 v[82:83], v[134:135], v[118:119], v[82:83]\n\t"
      "v_pk_fma_f32 v[84:85], v[134:135], v[120:121], v[84:85]\n\t"
      "v_pk_fma_f32 v[86:87], v[134:135], v[122:123], v[86:87]\n\t"
      "v_mfma_f32_16x16x32_bf16 v[72:75], v[152:155], v[188:191], v[72:75]\n\t"
      "v_cvt_pk_bf16_f32 v80, v80, v81\n\t"
      "v_cvt_pk_bf16_f32 v81, v82, v83\n\t"
      "v_cvt_pk_bf16_f32 v82, v84, v85\n\t"
      "v_cvt_pk_bf16_f32 v83, v86, v87\n\t"
      "v_mfma_f32_16x16x32_bf16 v[76:79], v[156:159], v[188:191], v[76:79]\n\t"
      "ds_write_b128 v236, v[80:83] offset:2048\n\t"
      "s_waitcnt lgkmcnt(11)\n\t"
      "v_pk_fma_f32 v[88:89], v[148:149], v[250:251], v[88:89]\n\t"
      "v_pk_fma_f32 v[90:91], v[150:151], v[250:251], v[90:91]\n\t"
      "v_pk_fma_f32 v[92:93], v[148:149], v[252:253], v[92:93]\n\t"
      "v_pk_fma_f32 v[94:95], v[150:151], v[252:253], v[94:95]\n\t"
      "v_mfma_f32_16x16x32_bf16 v[48:51], v[152:155], v[184:187], v[48:51]\n\t"
      "v_cvt_pk_bf16_f32 v216, v88, v89\n\t"
      "v_cvt_pk_bf16_f32 v217, v90, v91\n\t"
      "v_cvt_pk_bf16_f32 v218, v92, v93\n\t"
      "v_cvt_pk_bf16_f32 v219, v94, v95\n\t"
      "v_mfma_f32_16x16x32_bf16 v[52:55], v[156:159], v[184:187], v[52:55]\n\t"
      "ds_write_b64 v237, v[216:217] offset:3072\n\t"
      "ds_write_b64 v237, v[218:219] offset:3584\n\t"
      "s_waitcnt lgkmcnt(11)\n\t"
      "v_pk_fma_f32 v[100:101], v[248:249], v[124:125], v[100:101]\n\t"
      "v_pk_fma_f32 v[104:105], v[196:197], v[224:225], v[104:105]\n\t"
      "v_pk_fma_f32 v[102:103], v[248:249], v[126:127], v[102:103]\n\t"
      "v_pk_fma_f32 v[106:107], v[198:199], v[224:225], v[106:107]\n\t"
      "v_mfma_f32_16x16x32_bf16 v[24:27], v[152:155], v[180:183], v[24:27]\n\t"
      "v_cvt_pk_bf16_f32 v220, v100, v101\n\t"
      "v_cvt_pk_bf16_f32 v221, v102, v103\n\t"
      "v_cvt_pk_bf16_f32 v222, v104, v105\n\t"
      "v_cvt_pk_bf16_f32 v223, v106, v107\n\t"
      "v_mfma_f32_16x16x32_bf16 v[28:31], v[156:159], v[180:183], v[28:31]\n\t"
      "ds_write_b64 v238, v[220:221]\n\t"
      "v_lshrrev_b32_e32 v96, 1, v239\n\t"
      "v_add_u32_e32 v96, s8, v96\n\t"
      "ds_write_b64 v96, v[222:223]\n\t"
      "v_xor_b32_e32 v227, 8, v232\n\t"
      "ds_write_b32 v227, v227 offset:4\n\t"
      "v_pk_fma_f32 v[56:57], v[132:133], v[116:117], v[56:57]\n\t"
      "v_pk_fma_f32 v[58:59], v[132:133], v[118:119], v[58:59]\n\t"
      "v_pk_fma_f32 v[60:61], v[132:133], v[120:121], v[60:61]\n\t"
      "v_pk_fma_f32 v[62:63], v[132:133], v[122:123], v[62:63]\n\t"
      "v_mfma_f32_16x16x32_bf16 v[0:3], v[152:155], v[176:179], v[0:3]\n\t"
      "v_cvt_pk_bf16_f32 v56, v56, v57\n\t"
      "v_cvt_pk_bf16_f32 v57, v58, v59\n\t"
      "v_cvt_pk_bf16_f32 v58, v60, v61\n\t"
      "v_cvt_pk_bf16_f32 v59, v62, v63\n\t"
      "v_mfma_f32_16x16x32_bf16 v[4:7], v[156:159], v[176:179], v[4:7]\n\t"
      "v_mov_b32_e32 v164, v216\n\t"
      "v_mov_b32_e32 v165, v217\n\t"
      "v_mov_b32_e32 v174, v218\n\t"
      "v_mov_b32_e32 v175, v219\n\t"
      "s_waitcnt lgkmcnt(12)\n\t"
      "v_pk_fma_f32 v[72:73], v[134:135], v[108:109], v[72:73]\n\t"
      "v_pk_fma_f32 v[74:75], v[134:135], v[110:111], v[74:75]\n\t"
      "v_pk_fma_f32 v[76:77], v[134:135], v[112:113], v[76:77]\n\t"
      "v_pk_fma_f32 v[78:79], v[134:135], v[114:115], v[78:79]\n\t"
      "v_cvt_pk_bf16_f32 v72, v72, v73\n\t"
      "v_cvt_pk_bf16_f32 v73, v74, v75\n\t"
      "v_cvt_pk_bf16_f32 v74, v76, v77\n\t"
      "v_cvt_pk_bf16_f32 v75, v78, v79\n\t"
      "v_cndmask_b32_e64 v74, v74, 0, s[42:43]\n\t"
      "v_cndmask_b32_e64 v75, v75, 0, s[42:43]\n\t"
      "s_nop 1\n\t"
      "v_mfma_f32_16x16x32_bf16 v[76:79], v[56:59], v[72:75], 0\n\t"
      "v_mfma_f32_16x16x32_bf16 v[84:87], v[80:83], v[72:75], 0\n\t"
      "v_pk_fma_f32 v[32:33], v[130:131], v[116:117], v[32:33]\n\t"
      "v_pk_fma_f32 v[34:35], v[130:131], v[118:119], v[34:35]\n\t"
      "v_pk_fma_f32 v[36:37], v[130:131], v[120:121], v[36:37]\n\t"
      "v_pk_fma_f32 v[38:39], v[130:131], v[122:123], v[38:39]\n\t"
      "v_cvt_pk_bf16_f32 v32, v32, v33\n\t"
      "v_cvt_pk_bf16_f32 v33, v34, v35\n\t"
      "v_cvt_pk_bf16_f32 v34, v36, v37\n\t"
      "v_cvt_pk_bf16_f32 v35, v38, v39\n\t"
      "v_pk_fma_f32 v[48:49], v[132:133], v[108:109], v[48:49]\n\t"
      "v_pk_fma_f32 v[50:51], v[132:133], v[110:111], v[50:51]\n\t"
      "v_pk_fma_f32 v[52:53], v[132:133], v[112:113], v[52:53]\n\t"
      "v_pk_fma_f32 v[54:55], v[132:133], v[114:115], v[54:55]\n\t"
      "v_cvt_pk_bf16_f32 v48, v48, v49\n\t"
      "v_cvt_pk_bf16_f32 v49, v50, v51\n\t"
      "v_cvt_pk_bf16_f32 v50, v52, v53\n\t"
      "v_cvt_pk_bf16_f32 v51, v54, v55\n\t"
      "v_cndmask_b32_e64 v50, v50, 0, s[42:43]\n\t"
      "v_cndmask_b32_e64 v51, v51, 0, s[42:43]\n\t"
      "v_pk_fma_f32 v[64:65], v[144:145], v[250:251], v[64:65]\n\t"
      "v_pk_fma_f32 v[66:67], v[146:147], v[250:251], v[66:67]\n\t"
      "v_pk_fma_f32 v[68:69], v[144:145], v[252:253], v[68:69]\n\t"
      "v_pk_fma_f32 v[70:71], v[146:147], v[252:253], v[70:71]\n\t"
      "v_cvt_pk_bf16_f32 v162, v64, v65\n\t"
      "v_cvt_pk_bf16_f32 v163, v66, v67\n\t"
      "v_cvt_pk_bf16_f32 v172, v68, v69\n\t"
      "v_cvt_pk_bf16_f32 v173, v70, v71\n\t"
      "ds_read_b32 v226, v232 offset:4\n\t"
      "ds_read_b128 v[152:155], v236\n\t"
      "ds_read_b64 v[156:157], v237 offset:1024\n\t"
      "ds_read_b64 v[166:167], v237 offset:1536\n\t"
      "v_mfma_f32_16x16x32_bf16 v[52:55], v[32:35], v[48:51], 0\n\t"
      "v_mfma_f32_16x16x32_bf16 v[60:63], v[56:59], v[48:51], 0\n\t"
      "v_pk_fma_f32 v[8:9], v[128:129], v[116:117], v[8:9]\n\t"
      "v_pk_fma_f32 v[10:11], v[128:129], v[118:119], v[10:11]\n\t"
      "v_pk_fma_f32 v[12:13], v[128:129], v[120:121], v[12:13]\n\t"
      "v_pk_fma_f32 v[14:15], v[128:129], v[122:123], v[14:15]\n\t"
      "v_cvt_pk_bf16_f32 v8, v8, v9\n\t"
      "v_cvt_pk_bf16_f32 v9, v10, v11\n\t"
      "v_cvt_pk_bf16_f32 v10, v12, v13\n\t"
      "v_cvt_pk_bf16_f32 v11, v14, v15\n\t"
      "v_pk_fma_f32 v[24:25], v[130:131], v[108:109], v[24:25]\n\t"
      "v_pk_fma_f32 v[26:27], v[130:131], v[110:111], v[26:27]\n\t"
      "v_pk_fma_f32 v[28:29], v[130:131], v[112:113], v[28:29]\n\t"
      "v_pk_fma_f32 v[30:31], v[130:131], v[114:115], v[30:31]\n\t"
      "v_cvt_pk_bf16_f32 v24, v24, v25\n\t"
      "v_cvt_pk_bf16_f32 v25, v26, v27\n\t"
      "v_cvt_pk_bf16_f32 v26, v28, v29\n\t"
      "v_cvt_pk_bf16_f32 v27, v30, v31\n\t"
      "v_cndmask_b32_e64 v26, v26, 0, s[42:43]\n\t"
      "v_cndmask_b32_e64 v27, v27, 0, s[42:43]\n\t"
      "v_pk_fma_f32 v[40:41], v[140:141], v[250:251], v[40:41]\n\t"
      "v_pk_fma_f32 v[42:43], v[142:143], v[250:251], v[42:43]\n\t"
      "v_pk_fma_f32 v[44:45], v[140:141], v[252:253], v[44:45]\n\t"
      "v_pk_fma_f32 v[46:47], v[142:143], v[252:253], v[46:47]\n\t"
      "v_cvt_pk_bf16_f32 v160, v40, v41\n\t"
      "v_cvt_pk_bf16_f32 v161, v42, v43\n\t"
      "v_cvt_pk_bf16_f32 v170, v44, v45\n\t"
      "v_cvt_pk_bf16_f32 v171, v46, v47\n\t"
      "v_mfma_f32_16x16x32_bf16 v[28:31], v[8:11], v[24:27], 0\n\t"
      "v_mfma_f32_16x16x32_bf16 v[36:39], v[32:35], v[24:27], 0\n\t"
      "v_pk_fma_f32 v[0:1], v[128:129], v[108:109], v[0:1]\n\t"
      "v_pk_fma_f32 v[2:3], v[128:129], v[110:111], v[2:3]\n\t"
      "v_pk_fma_f32 v[4:5], v[128:129], v[112:113], v[4:5]\n\t"
      "v_pk_fma_f32 v[6:7], v[128:129], v[114:115], v[6:7]\n\t"
      "v_cvt_pk_bf16_f32 v0, v0, v1\n\t"
      "v_cvt_pk_bf16_f32 v1, v2, v3\n\t"
      "v_cvt_pk_bf16_f32 v2, v4, v5\n\t"
      "v_cvt_pk_bf16_f32 v3, v6, v7\n\t"
      "v_cndmask_b32_e64 v2, v2, 0, s[42:43]\n\t"
      "v_cndmask_b32_e64 v3, v3, 0, s[42:43]\n\t"
      "v_pk_fma_f32 v[16:17], v[136:137], v[250:251], v[16:17]\n\t"
      "v_pk_fma_f32 v[18:19], v[138:139], v[250:251], v[18:19]\n\t"
      "v_pk_fma_f32 v[20:21], v[136:137], v[252:253], v[20:21]\n\t"
      "v_pk_fma_f32 v[22:23], v[138:139], v[252:253], v[22:23]\n\t"
      "v_cvt_pk_bf16_f32 v158, v16, v17\n\t"
      "v_cvt_pk_bf16_f32 v159, v18, v19\n\t"
      "v_cvt_pk_bf16_f32 v168, v20, v21\n\t"
      "v_cvt_pk_bf16_f32 v169, v22, v23\n\t"
      "s_waitcnt lgkmcnt(3)\n\t"
      "v_cmp_eq_u32_e32 vcc, 0, v226\n\t"
      "s_cbranch_vccnz XBR_%=\n\t"
      "XBB_%=:\n\t"
      "s_waitcnt lgkmcnt(2)\n\t"
      "v_mfma_f32_16x16x32_bf16 v[4:7], v[152:155], v[0:3], v[244:247]\n\t"
      "v_mfma_f32_16x16x32_bf16 v[12:15], v[8:11], v[0:3], 0\n\t"
      "v_mov_b32_e32 v252, 163312\n\t"
      "ds_read_b64 v[250:251], v252\n\t"
      "v_cndmask_b32_e64 v76, v76, v77, s[36:37]\n\t"
      "v_cndmask_b32_e64 v84, v84, v85, s[36:37]\n\t"
      "v_cndmask_b32_e64 v78, v78, v79, s[36:37]\n\t"
      "v_cndmask_b32_e64 v86, v86, v87, s[36:37]\n\t"
      "v_cndmask_b32_e64 v179, v76, v84, s[38:39]\n\t"
      "v_cndmask_b32_e64 v183, v78, v86, s[40:41]\n\t"
      "v_cndmask_b32_e64 v52, v52, v53, s[36:37]\n\t"
      "v_cndmask_b32_e64 v60, v60, v61, s[36:37]\n\t"
      "v_cndmask_b32_e64 v54, v54, v55, s[36:37]\n\t"
      "v_cndmask_b32_e64 v62, v62, v63, s[36:37]\n\t"
      "v_cndmask_b32_e64 v178, v52, v60, s[38:39]\n\t"
      "v_cndmask_b32_e64 v182, v54, v62, s[40:41]\n\t"
      "v_pk_mul_f32 v[178:179], v[178:179], v[206:207]\n\t"
      "v_pk_mul_f32 v[182:183], v[182:183], v[210:211]\n\t"
      "v_max_f32_e32 v219, v179, v183\n\t"
      "v_max_f32_e32 v218, v178, v182\n\t"
      "v_cndmask_b32_e64 v28, v28, v29, s[36:37]\n\t"
      "v_cndmask_b32_e64 v36, v36, v37, s[36:37]\n\t"
      "v_cndmask_b32_e64 v30, v30, v31, s[36:37]\n\t"
      "v_cndmask_b32_e64 v38, v38, v39, s[36:37]\n\t"
      "v_cndmask_b32_e64 v177, v28, v36, s[38:39]\n\t"
      "v_cndmask_b32_e64 v181, v30, v38, s[40:41]\n\t"
      "v_cndmask_b32_e64 v4, v4, v5, s[36:37]\n\t"
      "v_cndmask_b32_e64 v12, v12, v13, s[36:37]\n\t"
      "v_cndmask_b32_e64 v6, v6, v7, s[36:37]\n\t"
      "v_cndmask_b32_e64 v14, v14, v15, s[36:37]\n\t"
      "v_cndmask_b32_e64 v176, v4, v12, s[38:39]\n\t"
      "v_cndmask_b32_e64 v180, v6, v14, s[40:41]\n\t"
      "v_pk_mul_f32 v[176:177], v[176:177], v[204:205]\n\t"
      "v_pk_mul_f32 v[180:181], v[180:181], v[208:209]\n\t"
      "v_max_f32_e32 v217, v177, v181\n\t"
      "v_max_f32_e32 v216, v176, v180\n\t"
      "v_permlane16_swap_b32_e32 v218, v219\n\t"
      "s_nop 0\n\t"
      "v_permlane16_swap_b32_e32 v216, v217\n\t"
      "v_max_f32_e32 v218, v218, v219\n\t"
      "v_max_f32_e32 v216, v216, v217\n\t"
      "v_mov_b32_e32 v219, v218\n\t"
      "v_mov_b32_e32 v217, v216\n\t"
      "s_nop 0\n\t"
      "v_permlane32_swap_b32_e32 v218, v219\n\t"
      "v_permlane32_swap_b32_e32 v216, v217\n\t"
      "v_max_f32_e32 v218, v218, v219\n\t"
      "v_max_f32_e32 v216, v216, v217\n\t"
      "v_mov_b32_e32 v219, v218\n\t"
      "v_mov_b32_e32 v217, v216\n\t"
      "s_nop 0\n\t"
      "v_permlane16_swap_b32_e32 v218, v219\n\t"
      "v_permlane16_swap_b32_e32 v216, v217\n\t"
      "v_pk_add_f32 v[178:179], v[178:179], v[218:219] neg_lo:[0,1] neg_hi:[0,1]\n\t"
      "v_pk_add_f32 v[182:183], v[182:183], v[218:219] neg_lo:[0,1] neg_hi:[0,1]\n\t"
      "v_pk_add_f32 v[176:177], v[176:177], v[216:217] neg_lo:[0,1] neg_hi:[0,1]\n\t"
      "v_pk_add_f32 v[180:181], v[180:181], v[216:217] neg_lo:[0,1] neg_hi:[0,1]\n\t"
      "v_pk_mul_f32 v[178:179], v[178:179], v[202:203]\n\t"
      "v_pk_mul_f32 v[182:183], v[182:183], v[202:203]\n\t"
      "v_pk_mul_f32 v[176:177], v[176:177], v[200:201]\n\t"
      "v_pk_mul_f32 v[180:181], v[180:181], v[200:201]\n\t"
      "v_exp_f32_e32 v179, v179\n\t"
      "v_exp_f32_e32 v183, v183\n\t"
      "v_exp_f32_e32 v178, v178\n\t"
      "v_exp_f32_e32 v182, v182\n\t"
      "v_exp_f32_e32 v177, v177\n\t"
      "v_exp_f32_e32 v181, v181\n\t"
      "v_exp_f32_e32 v176, v176\n\t"
      "v_exp_f32_e32 v180, v180\n\t"
      "s_waitcnt lgkmcnt(0)\n\t"
      "v_mov_b32_e32 v253, s12\n\t"
      "s_mov_b64 exec, 1\n\t"
      "global_store_dwordx2 v253, v[250:251], s[10:11] sc1\n\t"
      "s_mov_b64 exec, -1\n\t"
      "v_pk_add_f32 v[218:219], v[178:179], v[182:183]\n\t"
      "v_pk_mul_f32 v[178:179], v[178:179], v[206:207]\n\t"
      "v_pk_mul_f32 v[182:183], v[182:183], v[210:211]\n\t"
      "v_cvt_pk_bf16_f32 v179, v179, v179\n\t"
      "v_cvt_pk_bf16_f32 v183, v183, v183\n\t"
      "v_and_b32_e32 v72, v240, v179\n\t"
      "v_and_b32_e32 v73, v241, v183\n\t"
      "v_and_b32_e32 v74, v242, v179\n\t"
      "v_and_b32_e32 v75, v243, v183\n\t"
      "v_cvt_pk_bf16_f32 v178, v178, v178\n\t"
      "v_cvt_pk_bf16_f32 v182, v182, v182\n\t"
      "v_and_b32_e32 v48, v240, v178\n\t"
      "v_and_b32_e32 v49, v241, v182\n\t"
      "v_and_b32_e32 v50, v242, v178\n\t"
      "v_and_b32_e32 v51, v243, v182\n\t"
      "v_mfma_f32_16x16x32_bf16 v[88:91], v[162:165], v[72:75], 0\n\t"
      "v_mfma_f32_16x16x32_bf16 v[92:95], v[172:175], v[72:75], 0\n\t"
      "v_pk_add_f32 v[216:217], v[176:177], v[180:181]\n\t"
      "v_pk_mul_f32 v[176:177], v[176:177], v[204:205]\n\t"
      "v_pk_mul_f32 v[180:181], v[180:181], v[208:209]\n\t"
      "v_cvt_pk_bf16_f32 v177, v177, v177\n\t"
      "v_cvt_pk_bf16_f32 v181, v181, v181\n\t"
      "v_and_b32_e32 v24, v240, v177\n\t"
      "v_and_b32_e32 v25, v241, v181\n\t"
      "v_and_b32_e32 v26, v242, v177\n\t"
      "v_and_b32_e32 v27, v243, v181\n\t"
      "v_mfma_f32_16x16x32_bf16 v[64:67], v[160:163], v[48:51], 0\n\t"
      "v_mfma_f32_16x16x32_bf16 v[68:71], v[170:173], v[48:51], 0\n\t"
      "v_cvt_pk_bf16_f32 v176, v176, v176\n\t"
      "v_cvt_pk_bf16_f32 v180, v180, v180\n\t"
      "v_and_b32_e32 v0, v240, v176\n\t"
      "v_and_b32_e32 v1, v241, v180\n\t"
      "v_and_b32_e32 v2, v242, v176\n\t"
      "v_and_b32_e32 v3, v243, v180\n\t"
      "v_mfma_f32_16x16x32_bf16 v[40:43], v[158:161], v[24:27], 0\n\t"
      "v_mfma_f32_16x16x32_bf16 v[44:47], v[168:171], v[24:27], 0\n\t"
      "s_add_u32 s48, s4, 49152\n\t"
      "s_addc_u32 s49, s5, 0\n\t"
      "v_mfma_f32_16x16x32_bf16 v[16:19], v[156:159], v[0:3], 0\n\t"
      "v_mfma_f32_16x16x32_bf16 v[20:23], v[166:169], v[0:3], 0\n\t"
      "v_permlane16_swap_b32_e32 v218, v219\n\t"
      "v_permlane16_swap_b32_e32 v216, v217\n\t"
      "v_add_f32_e32 v218, v218, v219\n\t"
      "v_add_f32_e32 v216, v216, v217\n\t"
      "v_mov_b32_e32 v219, v218\n\t"
      "v_mov_b32_e32 v217, v216\n\t"
      "s_nop 0\n\t"
      "v_permlane32_swap_b32_e32 v218, v219\n\t"
      "v_permlane32_swap_b32_e32 v216, v217\n\t"
      "v_add_f32_e32 v218, v218, v219\n\t"
      "v_add_f32_e32 v216, v216, v217\n\t"
      "v_mov_b32_e32 v219, v218\n\t"
      "v_mov_b32_e32 v217, v216\n\t"
      "s_nop 0\n\t"
      "v_permlane16_swap_b32_e32 v218, v219\n\t"
      "v_permlane16_swap_b32_e32 v216, v217\n\t"
      "v_rcp_f32_e32 v190, v219\n\t"
      "v_rcp_f32_e32 v188, v218\n\t"
      "v_rcp_f32_e32 v186, v217\n\t"
      "v_rcp_f32_e32 v184, v216\n\t"
      "v_mov_b32_e32 v191, v190\n\t"
      "v_mov_b32_e32 v189, v188\n\t"
      "v_mov_b32_e32 v187, v186\n\t"
      "v_mov_b32_e32 v185, v184\n\t"
      "v_lshrrev_b32_e32 v252, 2, v239\n\t"
      "v_add_u32_e32 v252, s13, v252\n\t"
      "v_pk_mul_f32 v[88:89], v[190:191], v[88:89]\n\t"
      "v_pk_mul_f32 v[90:91], v[190:191], v[90:91]\n\t"
      "v_pk_mul_f32 v[92:93], v[190:191], v[92:93]\n\t"
      "v_pk_mul_f32 v[94:95], v[190:191], v[94:95]\n\t"
      "v_cvt_pk_bf16_f32 v80, v88, v89\n\t"
      "v_cvt_pk_bf16_f32 v81, v90, v91\n\t"
      "v_cvt_pk_bf16_f32 v82, v92, v93\n\t"
      "v_cvt_pk_bf16_f32 v83, v94, v95\n\t"
      "global_store_dwordx4 v239, v[80:83], s[48:49] sc1\n\t"
      "s_sub_u32 s48, s48, 16384\n\t"
      "s_subb_u32 s49, s49, 0\n\t"
      "v_add3_u32 v219, v80, v81, v82\n\t"
      "v_add_u32_e32 v219, v219, v83\n\t"
      "v_pk_mul_f32 v[64:65], v[188:189], v[64:65]\n\t"
      "v_pk_mul_f32 v[66:67], v[188:189], v[66:67]\n\t"
      "v_pk_mul_f32 v[68:69], v[188:189], v[68:69]\n\t"
      "v_pk_mul_f32 v[70:71], v[188:189], v[70:71]\n\t"
      "v_cvt_pk_bf16_f32 v56, v64, v65\n\t"
      "v_cvt_pk_bf16_f32 v57, v66, v67\n\t"
      "v_cvt_pk_bf16_f32 v58, v68, v69\n\t"
      "v_cvt_pk_bf16_f32 v59, v70, v71\n\t"
      "global_store_dwordx4 v239, v[56:59], s[48:49] sc1\n\t"
      "s_sub_u32 s48, s48, 16384\n\t"
      "s_subb_u32 s49, s49, 0\n\t"
      "v_add3_u32 v218, v56, v57, v58\n\t"
      "v_add_u32_e32 v218, v218, v59\n\t"
      "v_pk_mul_f32 v[40:41], v[186:187], v[40:41]\n\t"
      "v_pk_mul_f32 v[42:43], v[186:187], v[42:43]\n\t"
      "v_pk_mul_f32 v[44:45], v[186:187], v[44:45]\n\t"
      "v_pk_mul_f32 v[46:47], v[186:187], v[46:47]\n\t"
      "v_cvt_pk_bf16_f32 v32, v40, v41\n\t"
      "v_cvt_pk_bf16_f32 v33, v42, v43\n\t"
      "v_cvt_pk_bf16_f32 v34, v44, v45\n\t"
      "v_cvt_pk_bf16_f32 v35, v46, v47\n\t"
      "global_store_dwordx4 v239, v[32:35], s[48:49] sc1\n\t"
      "s_sub_u32 s48, s48, 16384\n\t"
      "s_subb_u32 s49, s49, 0\n\t"
      "v_add3_u32 v217, v32, v33, v34\n\t"
      "v_add_u32_e32 v217, v217, v35\n\t"
      "s_nop 1\n\t"
      "v_permlane32_swap_b32_e32 v217, v219\n\t"
      "v_add_u32_e32 v217, v217, v219\n\t"
      "v_pk_mul_f32 v[16:17], v[184:185], v[16:17]\n\t"
      "v_pk_mul_f32 v[18:19], v[184:185], v[18:19]\n\t"
      "v_pk_mul_f32 v[20:21], v[184:185], v[20:21]\n\t"
      "v_pk_mul_f32 v[22:23], v[184:185], v[22:23]\n\t"
      "v_cvt_pk_bf16_f32 v8, v16, v17\n\t"
      "v_cvt_pk_bf16_f32 v9, v18, v19\n\t"
      "v_cvt_pk_bf16_f32 v10, v20, v21\n\t"
      "v_cvt_pk_bf16_f32 v11, v22, v23\n\t"
      "global_store_dwordx4 v239, v[8:11], s[48:49] sc1\n\t"
      "v_add3_u32 v216, v8, v9, v10\n\t"
      "v_add_u32_e32 v216, v216, v11\n\t"
      "s_nop 1\n\t"
      "v_permlane32_swap_b32_e32 v216, v218\n\t"
      "v_add_u32_e32 v216, v216, v218\n\t"
      "s_nop 1\n\t"
      "v_permlane16_swap_b32_e32 v216, v217\n\t"
      "v_add3_u32 v216, v216, v217, v250\n\t"
      "global_store_dword v252, v216, s[10:11] sc1\n\t"
      "s_barrier\n\t"
      "s_nop 1\n\t"
      "s_branch CEND_%=\n\t"
      "LOOP1_%=:\n\t"
      "s_waitcnt vmcnt(0)\n\t"
      "s_barrier\n\t"
      "ds_read_b64_tr_b16 v[108:109], v226\n\t"
      "ds_read_b64_tr_b16 v[110:111], v226 offset:512\n\t"
      "ds_read_b128 v[132:135], v224\n\t"
      "ds_read_b128 v[136:139], v224 offset:5120\n\t"
      "ds_read_b128 v[140:143], v224 offset:10240\n\t"
      "ds_read_b128 v[144:147], v224 offset:15360\n\t"
      "ds_read_b64_tr_b16 v[112:113], v227\n\t"
      "ds_read_b64_tr_b16 v[114:115], v227 offset:512\n\t"
      "ds_read_b128 v[148:151], v225\n\t"
      "ds_read_b64_tr_b16 v[116:117], v226 offset:1024\n\t"
      "ds_read_b64_tr_b16 v[118:119], v226 offset:1536\n\t"
      "ds_read_b64_tr_b16 v[120:121], v227 offset:1024\n\t"
      "ds_read_b64_tr_b16 v[122:123], v227 offset:1536\n\t"
      "ds_read_b64_tr_b16 v[124:125], v226 offset:2048\n\t"
      "ds_read_b64_tr_b16 v[126:127], v226 offset:2560\n\t"
      "ds_read_b64_tr_b16 v[128:129], v227 offset:2048\n\t"
      "ds_read_b64_tr_b16 v[130:131], v227 offset:2560\n\t"
      "s_branch EVEN1_%=\n\t"
      "s_waitcnt lgkmcnt(0)\n\t"
      ".p2align 6\n\t"
      "ODD1_%=:\n\t"
      "s_waitcnt lgkmcnt(0)\n\t"
      "v_mfma_f32_16x16x32_bf16 v[0:3], v[152:155], v[176:179], v[0:3]\n\t"
      "v_add_u32_e32 v224, s46, v224\n\t"
      "v_add_u32_e32 v226, s46, v226\n\t"
      "v_mfma_f32_16x16x32_bf16 v[24:27], v[152:155], v[180:183], v[24:27]\n\t"
      "v_add_u32_e32 v227, s46, v227\n\t"
      "v_add_u32_e32 v225, s46, v225\n\t"
      "v_mfma_f32_16x16x32_bf16 v[48:51], v[152:155], v[184:187], v[48:51]\n\t"
      "s_barrier\n\t"
      "ds_read_b64_tr_b16 v[108:109], v226\n\t"
      "v_mfma_f32_16x16x32_bf16 v[72:75], v[152:155], v[188:191], v[72:75]\n\t"
      "ds_read_b64_tr_b16 v[110:111], v226 offset:512\n\t"
      "v_mfma_f32_16x16x32_bf16 v[4:7], v[156:159], v[176:179], v[4:7]\n\t"
      "ds_read_b128 v[132:135], v224\n\t"
      "v_mfma_f32_16x16x32_bf16 v[28:31], v[156:159], v[180:183], v[28:31]\n\t"
      "ds_read_b128 v[136:139], v224 offset:5120\n\t"
      "v_mfma_f32_16x16x32_bf16 v[52:55], v[156:159], v[184:187], v[52:55]\n\t"
      "ds_read_b128 v[140:143], v224 offset:10240\n\t"
      "v_mfma_f32_16x16x32_bf16 v[76:79], v[156:159], v[188:191], v[76:79]\n\t"
      "ds_read_b128 v[144:147], v224 offset:15360\n\t"
      "v_mfma_f32_16x16x32_bf16 v[96:99], v[156:159], v[192:195], v[96:99]\n\t"
      "v_mfma_f32_16x16x32_bf16 v[8:11], v[160:163], v[176:179], v[8:11]\n\t"
      "ds_read_b64_tr_b16 v[112:113], v227\n\t"
      "v_mfma_f32_16x16x32_bf16 v[32:35], v[160:163], v[180:183], v[32:35]\n\t"
      "ds_read_b64_tr_b16 v[114:115], v227 offset:512\n\t"
      "v_mfma_f32_16x16x32_bf16 v[56:59], v[160:163], v[184:187], v[56:59]\n\t"
      "ds_read_b128 v[148:151], v225\n\t"
      "v_mfma_f32_16x16x32_bf16 v[80:83], v[160:163], v[188:191], v[80:83]\n\t"
      "ds_read_b64_tr_b16 v[116:117], v226 offset:1024\n\t"
      "v_mfma_f32_16x16x32_bf16 v[12:15], v[164:167], v[176:179], v[12:15]\n\t"
      "ds_read_b64_tr_b16 v[118:119], v226 offset:1536\n\t"
      "v_mfma_f32_16x16x32_bf16 v[36:39], v[164:167], v[180:183], v[36:39]\n\t"
      "ds_read_b64_tr_b16 v[120:121], v227 offset:1024\n\t"
      "v_mfma_f32_16x16x32_bf16 v[60:63], v[164:167], v[184:187], v[60:63]\n\t"
      "v_mfma_f32_16x16x32_bf16 v[84:87], v[164:167], v[188:191], v[84:87]\n\t"
      "ds_read_b64_tr_b16 v[122:123], v227 offset:1536\n\t"
      "v_mfma_f32_16x16x32_bf16 v[100:103], v[164:167], v[192:195], v[100:103]\n\t"
      "ds_read_b64_tr_b16 v[124:125], v226 offset:2048\n\t"
      "v_mfma_f32_16x16x32_bf16 v[16:19], v[176:179], v[168:171], v[16:19]\n\t"
      "ds_read_b64_tr_b16 v[126:127], v226 offset:2560\n\t"
      "v_mfma_f32_16x16x32_bf16 v[40:43], v[180:183], v[168:171], v[40:43]\n\t"
      "ds_read_b64_tr_b16 v[128:129], v227 offset:2048\n\t"
      "v_mfma_f32_16x16x32_bf16 v[64:67], v[184:187], v[168:171], v[64:67]\n\t"
      "ds_read_b64_tr_b16 v[130:131], v227 offset:2560\n\t"
      "v_mfma_f32_16x16x32_bf16 v[88:91], v[188:191], v[168:171], v[88:91]\n\t"
      "v_mfma_f32_16x16x32_bf16 v[20:23], v[176:179], v[172:175], v[20:23]\n\t"
      "v_mfma_f32_16x16x32_bf16 v[44:47], v[180:183], v[172:175], v[44:47]\n\t"
      "v_add_u32_e32 v248, s46, v248\n\t"
      "v_add_u32_e32 v249, s46, v249\n\t"
      "v_mfma_f32_16x16x32_bf16 v[68:71], v[184:187], v[172:175], v[68:71]\n\t"
      "v_mfma_f32_16x16x32_bf16 v[92:95], v[188:191], v[172:175], v[92:95]\n\t"
      "v_mfma_f32_16x16x32_bf16 v[104:107], v[192:195], v[172:175], v[104:107]\n\t"
      "EVEN1_%=:\n\t"
      "s_waitcnt lgkmcnt(0)\n\t"
      "v_mfma_f32_16x16x32_bf16 v[0:3], v[108:111], v[132:135], v[0:3]\n\t"
      "ds_read_b64_tr_b16 v[152:153], v226 offset:20480\n\t"
      "v_mfma_f32_16x16x32_bf16 v[24:27], v[108:111], v[136:139], v[24:27]\n\t"
      "ds_read_b64_tr_b16 v[154:155], v226 offset:20992\n\t"
      "v_mfma_f32_16x16x32_bf16 v[48:51], v[108:111], v[140:143], v[48:51]\n\t"
      "ds_read_b128 v[176:179], v248\n\t"
      "v_mfma_f32_16x16x32_bf16 v[72:75], v[108:111], v[144:147], v[72:75]\n\t"
      "ds_read_b128 v[180:183], v248 offset:5120\n\t"
      "v_mfma_f32_16x16x32_bf16 v[4:7], v[112:115], v[132:135], v[4:7]\n\t"
      "v_mfma_f32_16x16x32_bf16 v[28:31], v[112:115], v[136:139], v[28:31]\n\t"
      "ds_read_b128 v[184:187], v248 offset:10240\n\t"
      "v_mfma_f32_16x16x32_bf16 v[52:55], v[112:115], v[140:143], v[52:55]\n\t"
      "ds_read_b128 v[188:191], v248 offset:15360\n\t"
      "v_mfma_f32_16x16x32_bf16 v[76:79], v[112:115], v[144:147], v[76:79]\n\t"
      "ds_read_b64_tr_b16 v[156:157], v227 offset:20480\n\t"
      "v_mfma_f32_16x16x32_bf16 v[96:99], v[112:115], v[148:151], v[96:99]\n\t"
      "v_mfma_f32_16x16x32_bf16 v[8:11], v[116:119], v[132:135], v[8:11]\n\t"
      "ds_read_b64_tr_b16 v[158:159], v227 offset:20992\n\t"
      "v_mfma_f32_16x16x32_bf16 v[32:35], v[116:119], v[136:139], v[32:35]\n\t"
      "ds_read_b128 v[192:195], v249\n\t"
      "v_mfma_f32_16x16x32_bf16 v[56:59], v[116:119], v[140:143], v[56:59]\n\t"
      "ds_read_b64_tr_b16 v[160:161], v226 offset:21504\n\t"
      "v_mfma_f32_16x16x32_bf16 v[80:83], v[116:119], v[144:147], v[80:83]\n\t"
      "ds_read_b64_tr_b16 v[162:163], v226 offset:22016\n\t"
      "v_mfma_f32_16x16x32_bf16 v[12:15], v[120:123], v[132:135], v[12:15]\n\t"
      "v_mfma_f32_16x16x32_bf16 v[36:39], v[120:123], v[136:139], v[36:39]\n\t"
      "ds_read_b64_tr_b16 v[164:165], v227 offset:21504\n\t"
      "v_mfma_f32_16x16x32_bf16 v[60:63], v[120:123], v[140:143], v[60:63]\n\t"
      "ds_read_b64_tr_b16 v[166:167], v227 offset:22016\n\t"
      "v_mfma_f32_16x16x32_bf16 v[84:87], v[120:123], v[144:147], v[84:87]\n\t"
      "ds_read_b64_tr_b16 v[168:169], v226 offset:22528\n\t"
      "v_mfma_f32_16x16x32_bf16 v[100:103], v[120:123], v[148:151], v[100:103]\n\t"
      "v_mfma_f32_16x16x32_bf16 v[16:19], v[132:135], v[124:127], v[16:19]\n\t"
      "ds_read_b64_tr_b16 v[170:171], v226 offset:23040\n\t"
      "v_mfma_f32_16x16x32_bf16 v[40:43], v[136:139], v[124:127], v[40:43]\n\t"
      "ds_read_b64_tr_b16 v[172:173], v227 offset:22528\n\t"
      "v_mfma_f32_16x16x32_bf16 v[64:67], v[140:143], v[124:127], v[64:67]\n\t"
      "ds_read_b64_tr_b16 v[174:175], v227 offset:23040\n\t"
      "s_cmp_eq_u32 s47, 4\n\t"
      "s_cselect_b32 s46, -2, 1\n\t"
      "v_mfma_f32_16x16x32_bf16 v[88:91], v[144:147], v[124:127], v[88:91]\n\t"
      "s_cmp_eq_u32 s47, 1\n\t"
      "s_cselect_b32 s46, -2, s46\n\t"
      "v_mfma_f32_16x16x32_bf16 v[20:23], v[132:135], v[128:131], v[20:23]\n\t"
      "s_mul_i32 s46, s46, 43008\n\t"
      "v_mfma_f32_16x16x32_bf16 v[44:47], v[136:139], v[128:131], v[44:47]\n\t"
      "v_mfma_f32_16x16x32_bf16 v[68:71], v[140:143], v[128:131], v[68:71]\n\t"
      "v_mfma_f32_16x16x32_bf16 v[92:95], v[144:147], v[128:131], v[92:95]\n\t"
      "s_sub_u32 s47, s47, 1\n\t"
      "v_mfma_f32_16x16x32_bf16 v[104:107], v[148:151], v[128:131], v[104:107]\n\t"
      "s_cbranch_scc0 ODD1_%=\n\t"
      "s_waitcnt lgkmcnt(0)\n\t"
      "v_mfma_f32_16x16x32_bf16 v[96:99], v[156:159], v[192:195], v[96:99]\n\t"
      "v_mov_b32_e32 v212, s9\n\t"
      "ds_read_b32 v212, v212\n\t"
      "ds_read2_b32 v[128:129], v234 offset0:32 offset1:32\n\t"
      "v_mfma_f32_16x16x32_bf16 v[100:103], v[164:167], v[192:195], v[100:103]\n\t"
      "ds_read2_b32 v[130:131], v234 offset0:48 offset1:48\n\t"
      "v_mfma_f32_16x16x32_bf16 v[104:107], v[192:195], v[172:175], v[104:107]\n\t"
      "ds_read2_b32 v[132:133], v234 offset0:64 offset1:64\n\t"
      "ds_read2_b32 v[134:135], v234 offset0:80 offset1:80\n\t"
      "v_mfma_f32_16x16x32_bf16 v[80:83], v[160:163], v[188:191], v[80:83]\n\t"
      "ds_read2_b32 v[248:249], v234 offset0:0 offset1:0\n\t"
      "v_mfma_f32_16x16x32_bf16 v[84:87], v[164:167], v[188:191], v[84:87]\n\t"
      "ds_read_b128 v[196:199], v233\n\t"
      "ds_read_b128 v[136:139], v233 offset:128\n\t"
      "v_mfma_f32_16x16x32_bf16 v[88:91], v[188:191], v[168:171], v[88:91]\n\t"
      "ds_read_b128 v[140:143], v233 offset:192\n\t"
      "v_mfma_f32_16x16x32_bf16 v[92:95], v[188:191], v[172:175], v[92:95]\n\t"
      "ds_read_b128 v[144:147], v233 offset:256\n\t"
      "ds_read_b128 v[148:151], v233 offset:320\n\t"
      "v_mfma_f32_16x16x32_bf16 v[56:59], v[160:163], v[184:187], v[56:59]\n\t"
      "s_mov_b64 exec, s[44:45]\n\t"
      "ds_write_b128 v230, v[96:99]\n\t"
      "ds_write_b128 v230, v[100:103] offset:128\n\t"
      "s_mov_b64 exec, 0xffff\n\t"
      "ds_write_b32 v231, v104 offset:256\n\t"
      "v_xor_b32_e32 v227, 8, v232\n\t"
      "ds_write_b32 v227, v227\n\t"
      "s_mov_b64 exec, -1\n\t"
      "v_mfma_f32_16x16x32_bf16 v[60:63], v[164:167], v[184:187], v[60:63]\n\t"
      "v_mfma_f32_16x16x32_bf16 v[64:67], v[184:187], v[168:171], v[64:67]\n\t"
      "v_mfma_f32_16x16x32_bf16 v[68:71], v[184:187], v[172:175], v[68:71]\n\t"
      "s_waitcnt lgkmcnt(4)\n\t"
      "v_mfma_f32_16x16x32_bf16 v[32:35], v[160:163], v[180:183], v[32:35]\n\t"
      "ds_read_b32 v226, v232\n\t"
      "ds_read_b128 v[116:119], v228 offset:128\n\t"
      "v_mfma_f32_16x16x32_bf16 v[36:39], v[164:167], v[180:183], v[36:39]\n\t"
      "ds_read_b128 v[120:123], v228 offset:192\n\t"
      "ds_read2_b32 v[250:251], v229 offset0:64 offset1:64\n\t"
      "v_mfma_f32_16x16x32_bf16 v[40:43], v[180:183], v[168:171], v[40:43]\n\t"
      "ds_read2_b32 v[252:253], v229 offset0:80 offset1:80\n\t"
      "ds_read_b128 v[124:127], v230 offset:128\n\t"
      "v_mfma_f32_16x16x32_bf16 v[44:47], v[180:183], v[172:175], v[44:47]\n\t"
      "ds_read2_b32 v[224:225], v231 offset0:64 offset1:64\n\t"
      "ds_read_b128 v[108:111], v228\n\t"
      "v_mfma_f32_16x16x32_bf16 v[8:11], v[160:163], v[176:179], v[8:11]\n\t"
      "ds_read_b128 v[112:115], v228 offset:64\n\t"
      "ds_read2_b32 v[204:205], v235 offset0:0 offset1:16\n\t"
      "v_mfma_f32_16x16x32_bf16 v[12:15], v[164:167], v[176:179], v[12:15]\n\t"
      "ds_read2_b32 v[206:207], v235 offset0:32 offset1:48\n\t"
      "ds_read2_b32 v[208:209], v254 offset0:0 offset1:16\n\t"
      "v_mfma_f32_16x16x32_bf16 v[16:19], v[176:179], v[168:171], v[16:19]\n\t"
      "ds_read2_b32 v[210:211], v254 offset0:32 offset1:48\n\t"
      "ds_read2_b32 v[200:201], v234 offset0:192 offset1:208\n\t"
      "v_mfma_f32_16x16x32_bf16 v[20:23], v[176:179], v[172:175], v[20:23]\n\t"
      "ds_read2_b32 v[202:203], v234 offset0:224 offset1:240\n\t"
      "s_branch LOOPX_%=\n\t"
      "XAR_%=:\n\t"
      "s_mov_b32 s51, 0\n\t"
      "XAL_%=:\n\t"
      "s_add_u32 s51, s51, 1\n\t"
      "s_cmp_lt_u32 s51, 262144\n\t"
      "s_cbranch_scc0 XAX_%=\n\t"
      "s_waitcnt lgkmcnt(0)\n\t"
      "v_mov_b32_e32 v212, s9\n\t"
      "ds_read_b32 v212, v212\n\t"
      "ds_read2_b32 v[128:129], v234 offset0:32 offset1:32\n\t"
      "ds_read2_b32 v[130:131], v234 offset0:48 offset1:48\n\t"
      "ds_read2_b32 v[132:133], v234 offset0:64 offset1:64\n\t"
      "ds_read2_b32 v[134:135], v234 offset0:80 offset1:80\n\t"
      "ds_read2_b32 v[248:249], v234 offset0:0 offset1:0\n\t"
      "ds_read_b128 v[196:199], v233\n\t"
      "ds_read_b128 v[136:139], v233 offset:128\n\t"
      "ds_read_b128 v[140:143], v233 offset:192\n\t"
      "ds_read_b128 v[144:147], v233 offset:256\n\t"
      "ds_read_b128 v[148:151], v233 offset:320\n\t"
      "s_waitcnt lgkmcnt(0)\n\t"
      "ds_read_b32 v226, v232\n\t"
      "ds_read_b128 v[116:119], v228 offset:128\n\t"
      "ds_read_b128 v[120:123], v228 offset:192\n\t"
      "ds_read2_b32 v[250:251], v229 offset0:64 offset1:64\n\t"
      "ds_read2_b32 v[252:253], v229 offset0:80 offset1:80\n\t"
      "ds_read_b128 v[124:127], v230 offset:128\n\t"
      "ds_read2_b32 v[224:225], v231 offset0:64 offset1:64\n\t"
      "ds_read_b128 v[108:111], v228\n\t"
      "ds_read_b128 v[112:115], v228 offset:64\n\t"
      "ds_read2_b32 v[204:205], v235 offset0:0 offset1:16\n\t"
      "ds_read2_b32 v[206:207], v235 offset0:32 offset1:48\n\t"
      "ds_read2_b32 v[208:209], v254 offset0:0 offset1:16\n\t"
      "ds_read2_b32 v[210:211], v254 offset0:32 offset1:48\n\t"
      "ds_read2_b32 v[200:201], v234 offset0:192 offset1:208\n\t"
      "ds_read2_b32 v[202:203], v234 offset0:224 offset1:240\n\t"
      "s_waitcnt lgkmcnt(0)\n\t"
      "v_min_u32_e32 v226, v226, v212\n\t"
      "v_cmp_eq_u32_e32 vcc, 0, v226\n\t"
      "s_cbranch_vccnz XAL_%=\n\t"
      "XAX_%=:\n\t"
      "s_branch XAB_%=\n\t"
      "XBR_%=:\n\t"
      "s_mov_b32 s51, 0\n\t"
      "XBL_%=:\n\t"
      "s_add_u32 s51, s51, 1\n\t"
      "s_cmp_lt_u32 s51, 262144\n\t"
      "s_cbranch_scc0 XBX_%=\n\t"
      "s_waitcnt lgkmcnt(0)\n\t"
      "ds_read_b32 v226, v232 offset:4\n\t"
      "ds_read_b128 v[152:155], v236\n\t"
      "ds_read_b64 v[156:157], v237 offset:1024\n\t"
      "ds_read_b64 v[166:167], v237 offset:1536\n\t"
      "s_waitcnt lgkmcnt(0)\n\t"
      "v_cmp_eq_u32_e32 vcc, 0, v226\n\t"
      "s_cbranch_vccnz XBL_%=\n\t"
      "XBX_%=:\n\t"
      "s_branch XBB_%=\n\t"
      "CEND_%=:\n\t"
      : "+{s[4:11]}"(s), "+{s12}"(hint), "+{s13}"(chk), "+{v[224:239]}"(v), "+{v254}"(vk1)
      :
      : "memory", "vcc", "scc",
        "v0", "v1", "v2", "v3", "v4", "v5", "v6", "v7", "v8", "v9", "v10", "v11", "v12", "v13", "v14", "v15", "v16",
        "v17", "v18", "v19", "v20", "v21", "v22", "v23", "v24", "v25", "v26", "v27", "v28", "v29", "v30", "v31", "v32",
        "v33", "v34", "v35", "v36", "v37", "v38", "v39", "v40", "v41", "v42", "v43", "v44", "v45", "v46", "v47", "v48",
        "v49", "v50", "v51", "v52", "v53", "v54", "v55", "v56", "v57", "v58", "v59", "v60", "v61", "v62", "v63", "v64",
        "v65", "v66", "v67", "v68", "v69", "v70", "v71", "v72", "v73", "v74", "v75", "v76", "v77", "v78", "v79", "v80",
        "v81", "v82", "v83", "v84", "v85", "v86", "v87", "v88", "v89", "v90", "v91", "v92", "v93", "v94", "v95", "v96",
        "v97", "v98", "v99", "v100", "v101", "v102", "v103", "v104", "v105", "v106", "v107", "v108", "v109", "v110",
        "v111", "v112", "v113", "v114", "v115", "v116", "v117", "v118", "v119", "v120", "v121", "v122", "v123", "v124",
        "v125", "v126", "v127", "v128", "v129", "v130", "v131", "v132", "v133", "v134", "v135", "v136", "v137", "v138",
        "v139", "v140", "v141", "v142", "v143", "v144", "v145", "v146", "v147", "v148", "v149", "v150", "v151", "v152",
        "v153", "v154", "v155", "v156", "v157", "v158", "v159", "v160", "v161", "v162", "v163", "v164", "v165", "v166",
        "v167", "v168", "v169", "v170", "v171", "v172", "v173", "v174", "v175", "v176", "v177", "v178", "v179", "v180",
        "v181", "v182", "v183", "v184", "v185", "v186", "v187", "v188", "v189", "v190", "v191", "v192", "v193", "v194",
        "v195", "v196", "v197", "v198", "v199", "v200", "v201", "v202", "v203", "v204", "v205", "v206", "v207", "v208",
        "v209", "v210", "v211", "v212", "v213", "v214", "v215", "v216", "v217", "v218", "v219", "v220", "v221", "v222",
        "v223", "v240", "v241", "v242", "v243", "v244", "v245", "v246", "v247", "v248", "v249", "v250", "v251", "v252",
        "v253", "s36", "s37", "s38", "s39", "s40", "s41", "s42", "s43", "s44", "s45", "s46", "s47", "s48", "s49", "s50",
        "s51", "s52", "s53", "s54", "s55"
    );
    } else {
      LANE_BITS(threadIdx.x & 63);
      u32x8 v;
      u32x16 s;
      v[0] = ((8 * rt + 4 * half + g) * 16 + c) * 16;
      v[1] = (max(8 * rt + ((g & 1) ? 0 : 4 * half) - 1, 0) * 16 + c) * 16;
      v[2] = sta + 128 + lane * 4;
      v[3] = sta + ((g & 1) ? 0 : 64) + c * 4;
      v[4] = lr * A_SLOT + A_HALO + lc * 16;
      v[6] = (side * 1024 + (idx * 4 + wave - 4) * 8 + lr) * 3584 + (7 * xp + min(lc, 6)) * 128;
      v[7] = v[6] + (1 - 2 * side) * 1024 * 3584;
      u64 tw = (u64)w1;
      const int n = idx * 4 + wave - 4;
      u64 pf = (u64)(n < 14 ? b1 + xp * 896 + n * 64 : n < 30 ? b2 + xp * 1024 + (n - 14) * 64 : n < 38 ? w1 + (n - 30) * 1000000
                : n < 46 ? w2 + (n - 38) * 1000000 : n < 48 ? out + (n - 46) * 1800000 : ws + (n & 3) * (4 << 20) + (4 << 20) - 4096);
      s[4] = UNI((blockIdx.x >> 1) == 60 && wave == 4 ? (xcd ? WS_G : WS_HN) + 0x1c0000 - WS_ST : 0);
      v[5] = lane * (8192 + 640);
      put(s, 0, (u64)(x + (rt * 128 * 896 + 128) - (16 * 896 + 1024)));
      put(s, 2, (u64)(wqkv + (hp * 112 + 64 * 2688) - 4096));
      s[5] = wave * A_UNIT;
      put2(s, 6, wave > 4 ? 0xffffffffu : 0xffffff00u, 0u);
      s[8] = A_HALO + (wave - 4) * 512;
      s[9] = A_FL + 32 + 4 * half;
      put(s, 10, (u64)(ws + WS_ST));
    asm volatile(
      "s_waitcnt lgkmcnt(0)\n\ts_nop 4\n\t"
      "s_load_dwordx2 s[18:19], s[14:15], 0x20000\n\t"
      "s_sub_u32 s40, s4, 57344\n\t"
      "s_subb_u32 s41, s5, 0\n\t"
      "s_sub_u32 s42, s6, 86016\n\t"
      "s_subb_u32 s43, s7, 0\n\t"
      "s_setprio 2\n\t"
      "v_mov_b32_e32 v0, 0x3f803f80\n\t"
      "v_mov_b32_e32 v1, 0x3f803f80\n\t"
      "v_mov_b32_e32 v2, 0x3f803f80\n\t"
      "v_mov_b32_e32 v3, 0x3f803f80\n\t"
      "s_mov_b64 exec, 0xffffff\n\t"
      "ds_write_b128 v204, v[0:3]\n\t"
      "s_mov_b64 exec, -1\n\t"
      "v_mov_b32_e32 v32, 154624\n\t"
      "v_mov_b32_e32 v33, s13\n\t"
      "v_mov_b32_e32 v36, 0\n\t"
      "v_mov_b32_e32 v37, 0\n\t"
      "v_mov_b32_e32 v38, 0\n\t"
      "v_mov_b32_e32 v39, 0\n\t"
      "ds_write_b128 v32, v[36:39]\n\t"
      "ds_write_b128 v32, v[36:39] offset:16\n\t"
      "ds_write_b128 v32, v[36:39] offset:32\n\t"
      "s_add_u32 m0, s9, 43008\n\t"
      "s_nop 0\n\t"
      "global_load_lds_dwordx4 v195, s[6:7] offset:2048\n\t"
      "global_load_lds_dwordx4 v192, s[4:5]\n\t"
      "global_load_lds_dwordx4 v193, s[4:5] offset:1024\n\t"
      "global_load_lds_dwordx4 v196, s[6:7] offset:3072\n\t"
      "s_add_u32 m0, s9, 47104\n\t"
      "s_nop 0\n\t"
      "global_load_lds_dwordx4 v197, s[6:7]\n\t"
      "s_add_u32 m0, s12, 43008\n\t"
      "s_mov_b64 exec, s[10:11]\n\t"
      "global_load_lds_dwordx4 v194, s[4:5]\n\t"
      "s_mov_b64 exec, -1\n\t"
      "s_waitcnt lgkmcnt(0)\n\t"
      "s_add_u32 s18, s18, 1\n\t"
      "s_addc_u32 s19, s19, 0\n\t"
      "s_mul_i32 s48, s18, 0x9e3779b9\n\t"
      "s_mul_hi_u32 s17, s18, 0x7f4a7c15\n\t"
      "s_add_u32 s17, s17, s48\n\t"
      "s_mul_i32 s48, s19, 0x7f4a7c15\n\t"
      "s_add_u32 s17, s17, s48\n\t"
      "s_mul_i32 s16, s18, 0x7f4a7c15\n\t"
      "s_add_u32 s16, s16, 0xd192ed03\n\t"
      "s_addc_u32 s17, s17, 0xd1b54a32\n\t"
      "v_mov_b32_e32 v12, s16\n\t"
      "v_mov_b32_e32 v13, s17\n\t"
      "v_mov_b32_e32 v14, s18\n\t"
      "v_mov_b32_e32 v15, s19\n\t"
      "v_mov_b32_e32 v16, 163312\n\t"
      "s_mov_b64 exec, 1\n\t"
      "ds_write_b128 v16, v[12:15]\n\t"
      "s_mov_b64 exec, -1\n\t"
      "s_waitcnt vmcnt(11) lgkmcnt(0)\n\t"
      "s_barrier\n\t"
      "s_add_u32 s4, s4, 128\n\t"
      "s_addc_u32 s5, s5, 0\n\t"
      "s_add_u32 s6, s6, 172032\n\t"
      "s_addc_u32 s7, s7, 0\n\t"
      "s_add_u32 s40, s40, 128\n\t"
      "s_addc_u32 s41, s41, 0\n\t"
      "s_add_u32 s42, s42, 172032\n\t"
      "s_addc_u32 s43, s43, 0\n\t"
      "s_add_u32 m0, s9, 65536\n\t"
      "s_nop 0\n\t"
      "global_load_lds_dwordx4 v195, s[42:43] offset:2048\n\t"
      "global_load_lds_dwordx4 v192, s[40:41]\n\t"
      "global_load_lds_dwordx4 v193, s[40:41] offset:1024\n\t"
      "global_load_lds_dwordx4 v196, s[42:43] offset:3072\n\t"
      "s_add_u32 m0, s9, 69632\n\t"
      "s_nop 0\n\t"
      "global_load_lds_dwordx4 v197, s[42:43]\n\t"
      "s_add_u32 m0, s9, 86016\n\t"
      "s_nop 0\n\t"
      "global_load_lds_dwordx4 v195, s[6:7] offset:2048\n\t"
      "global_load_lds_dwordx4 v192, s[4:5]\n\t"
      "global_load_lds_dwordx4 v193, s[4:5] offset:1024\n\t"
      "global_load_lds_dwordx4 v196, s[6:7] offset:3072\n\t"
      "s_add_u32 m0, s9, 90112\n\t"
      "s_nop 0\n\t"
      "global_load_lds_dwordx4 v197, s[6:7]\n\t"
      "s_add_u32 m0, s12, 86016\n\t"
      "s_mov_b64 exec, s[10:11]\n\t"
      "global_load_lds_dwordx4 v194, s[4:5]\n\t"
      "s_mov_b64 exec, -1\n\t"
      "s_waitcnt vmcnt(11)\n\t"
      "s_barrier\n\t"
      "s_add_u32 s4, s4, 128\n\t"
      "s_addc_u32 s5, s5, 0\n\t"
      "s_add_u32 s6, s6, 172032\n\t"
      "s_addc_u32 s7, s7, 0\n\t"
      "s_add_u32 s40, s40, 128\n\t"
      "s_addc_u32 s41, s41, 0\n\t"
      "s_add_u32 s42, s42, 172032\n\t"
      "s_addc_u32 s43, s43, 0\n\t"
      "s_add_u32 m0, s9, 4294946816\n\t"
      "s_nop 0\n\t"
      "global_load_lds_dwordx4 v195, s[42:43] offset:2048\n\t"
      "global_load_lds_dwordx4 v192, s[40:41]\n\t"
      "global_load_lds_dwordx4 v193, s[40:41] offset:1024\n\t"
      "global_load_lds_dwordx4 v196, s[42:43] offset:3072\n\t"
      "s_add_u32 m0, s9, 4294950912\n\t"
      "s_nop 0\n\t"
      "global_load_lds_dwordx4 v197, s[42:43]\n\t"
      "s_add_u32 m0, s9, 0\n\t"
      "s_nop 0\n\t"
      "global_load_lds_dwordx4 v195, s[6:7] offset:2048\n\t"
      "global_load_lds_dwordx4 v192, s[4:5]\n\t"
      "global_load_lds_dwordx4 v193, s[4:5] offset:1024\n\t"
      "global_load_lds_dwordx4 v196, s[6:7] offset:3072\n\t"
      "s_add_u32 m0, s9, 4096\n\t"
      "s_nop 0\n\t"
      "global_load_lds_dwordx4 v197, s[6:7]\n\t"
      "s_add_u32 m0, s12, 0\n\t"
      "s_mov_b64 exec, s[10:11]\n\t"
      "global_load_lds_dwordx4 v194, s[4:5]\n\t"
      "s_mov_b64 exec, -1\n\t"
      "s_cmp_eq_u32 s8, 0\n\t"
      "s_cbranch_scc1 PRt_%=\n\t"
      "s_add_u32 s46, s14, s8\n\t"
      "s_addc_u32 s47, s15, -1\n\t"
      "s_load_dword s50, s[46:47], 0x1f80\n\t"
      "PRt_%=:\n\t"
      "s_waitcnt vmcnt(11)\n\t"
      "s_barrier\n\t"
      "s_add_u32 s4, s4, 128\n\t"
      "s_addc_u32 s5, s5, 0\n\t"
      "s_add_u32 s6, s6, 172032\n\t"
      "s_addc_u32 s7, s7, 0\n\t"
      "s_add_u32 s40, s40, 128\n\t"
      "s_addc_u32 s41, s41, 0\n\t"
      "s_add_u32 s42, s42, 172032\n\t"
      "s_addc_u32 s43, s43, 0\n\t"
      "s_add_u32 m0, s9, 22528\n\t"
      "s_nop 0\n\t"
      "global_load_lds_dwordx4 v195, s[42:43] offset:2048\n\t"
      "global_load_lds_dwordx4 v192, s[40:41]\n\t"
      "global_load_lds_dwordx4 v193, s[40:41] offset:1024\n\t"
      "global_load_lds_dwordx4 v196, s[42:43] offset:3072\n\t"
      "s_add_u32 m0, s9, 26624\n\t"
      "s_nop 0\n\t"
      "global_load_lds_dwordx4 v197, s[42:43]\n\t"
      "s_add_u32 m0, s9, 43008\n\t"
      "s_nop 0\n\t"
      "global_load_lds_dwordx4 v195, s[6:7] offset:2048\n\t"
      "global_load_lds_dwordx4 v192, s[4:5]\n\t"
      "global_load_lds_dwordx4 v193, s[4:5] offset:1024\n\t"
      "global_load_lds_dwordx4 v196, s[6:7] offset:3072\n\t"
      "s_add_u32 m0, s9, 47104\n\t"
      "s_nop 0\n\t"
      "global_load_lds_dwordx4 v197, s[6:7]\n\t"
      "s_add_u32 m0, s12, 43008\n\t"
      "s_mov_b64 exec, s[10:11]\n\t"
      "global_load_lds_dwordx4 v194, s[4:5]\n\t"
      "s_mov_b64 exec, -1\n\t"
      "s_load_dword s50, s[22:23], 0x0\n\t"
      "s_waitcnt vmcnt(11)\n\t"
      "s_barrier\n\t"
      "s_add_u32 s4, s4, 128\n\t"
      "s_addc_u32 s5, s5, 0\n\t"
      "s_add_u32 s6, s6, 172032\n\t"
      "s_addc_u32 s7, s7, 0\n\t"
      "s_add_u32 s40, s40, 128\n\t"
      "s_addc_u32 s41, s41, 0\n\t"
      "s_add_u32 s42, s42, 172032\n\t"
      "s_addc_u32 s43, s43, 0\n\t"
      "s_add_u32 m0, s9, 65536\n\t"
      "s_nop 0\n\t"
      "global_load_lds_dwordx4 v195, s[42:43] offset:2048\n\t"
      "global_load_lds_dwordx4 v192, s[40:41]\n\t"
      "global_load_lds_dwordx4 v193, s[40:41] offset:1024\n\t"
      "global_load_lds_dwordx4 v196, s[42:43] offset:3072\n\t"
      "s_add_u32 m0, s9, 69632\n\t"
      "s_nop 0\n\t"
      "global_load_lds_dwordx4 v197, s[42:43]\n\t"
      "s_add_u32 m0, s9, 86016\n\t"
      "s_nop 0\n\t"
      "global_load_lds_dwordx4 v195, s[6:7] offset:2048\n\t"
      "global_load_lds_dwordx4 v192, s[4:5]\n\t"
      "global_load_lds_dwordx4 v193, s[4:5] offset:1024\n\t"
      "global_load_lds_dwordx4 v196, s[6:7] offset:3072\n\t"
      "s_add_u32 m0, s9, 90112\n\t"
      "s_nop 0\n\t"
      "global_load_lds_dwordx4 v197, s[6:7]\n\t"
      "s_add_u32 m0, s12, 86016\n\t"
      "s_mov_b64 exec, s[10:11]\n\t"
      "global_load_lds_dwordx4 v194, s[4:5]\n\t"
      "s_mov_b64 exec, -1\n\t"
      "s_waitcnt vmcnt(11)\n\t"
      "s_barrier\n\t"
      "s_add_u32 s4, s4, 128\n\t"
      "s_addc_u32 s5, s5, 0\n\t"
      "s_add_u32 s6, s6, 172032\n\t"
      "s_addc_u32 s7, s7, 0\n\t"
      "s_add_u32 s40, s40, 128\n\t"
      "s_addc_u32 s41, s41, 0\n\t"
      "s_add_u32 s42, s42, 172032\n\t"
      "s_addc_u32 s43, s43, 0\n\t"
      "s_add_u32 m0, s9, 4294946816\n\t"
      "s_nop 0\n\t"
      "global_load_lds_dwordx4 v195, s[42:43] offset:2048\n\t"
      "global_load_lds_dwordx4 v192, s[40:41]\n\t"
      "global_load_lds_dwordx4 v193, s[40:41] offset:1024\n\t"
      "global_load_lds_dwordx4 v196, s[42:43] offset:3072\n\t"
      "s_add_u32 m0, s9, 4294950912\n\t"
      "s_nop 0\n\t"
      "global_load_lds_dwordx4 v197, s[42:43]\n\t"
      "s_add_u32 m0, s9, 0\n\t"
      "s_nop 0\n\t"
      "global_load_lds_dwordx4 v195, s[6:7] offset:2048\n\t"
      "global_load_lds_dwordx4 v192, s[4:5]\n\t"
      "global_load_lds_dwordx4 v193, s[4:5] offset:1024\n\t"
      "global_load_lds_dwordx4 v196, s[6:7] offset:3072\n\t"
      "s_add_u32 m0, s9, 4096\n\t"
      "s_nop 0\n\t"
      "global_load_lds_dwordx4 v197, s[6:7]\n\t"
      "s_add_u32 m0, s12, 0\n\t"
      "s_mov_b64 exec, s[10:11]\n\t"
      "global_load_lds_dwordx4 v194, s[4:5]\n\t"
      "s_mov_b64 exec, -1\n\t"
      "global_load_dwordx4 v[4:7], v200, s[14:15] sc1\n\t"
      "global_load_dwordx4 v[8:11], v201, s[14:15] sc1\n\t"
      "s_cmp_eq_u32 s8, 0\n\t"
      "s_cbranch_scc1 PRu_%=\n\t"
      "s_add_u32 s46, s14, s8\n\t"
      "s_addc_u32 s47, s15, -1\n\t"
      "s_mov_b64 exec, 15\n\t"
      "global_store_dword v205, v0, s[46:47] sc1\n\t"
      "s_mov_b64 exec, -1\n\t"
      "PRu_%=:\n\t"
      "s_mov_b32 m0, 163328\n\t"
      "s_add_u32 s44, s20, 0\n\t"
      "s_addc_u32 s45, s21, 0\n\t"
      "global_load_lds_dword v206, s[44:45]\n\t"
      "s_waitcnt vmcnt(14)\n\t"
      "s_barrier\n\t"
      "s_mov_b32 m0, 163328\n\t"
      "s_add_u32 s44, s20, 1835008\n\t"
      "s_addc_u32 s45, s21, 0\n\t"
      "global_load_lds_dword v206, s[44:45]\n\t"
      "s_waitcnt vmcnt(4)\n\t"
      "s_barrier\n\t"
      "s_waitcnt vmcnt(2)\n\t"
      "v_xor_b32_e32 v6, v6, v4\n\t"
      "v_xor_b32_e32 v7, v7, v5\n\t"
      "v_xor_b32_e32 v10, v10, v8\n\t"
      "v_xor_b32_e32 v11, v11, v9\n\t"
      "v_cmp_ne_u64_e64 s[48:49], s[16:17], v[6:7]\n\t"
      "v_cmp_ne_u64_e32 vcc, s[16:17], v[10:11]\n\t"
      "s_nop 0\n\t"
      "s_or_b64 vcc, vcc, s[48:49]\n\t"
      "s_cbranch_scc1 GRR_%=\n\t"
      "GRB_%=:\n\t"
      "v_mul_f32_e32 v6, 0x3e8b97f6, v5\n\t"
      "ds_write2_b32 v202, v4, v5 offset0:0 offset1:96\n\t"
      "ds_write2_b32 v203, v8, v9 offset0:0 offset1:96\n\t"
      "ds_write_b32 v202, v6 offset:640\n\t"
      "s_mov_b64 exec, 1\n\t"
      "ds_write_b32 v33, v33\n\t"
      "s_mov_b64 exec, -1\n\t"
      "s_setprio 0\n\t"
      "s_mov_b32 m0, 163328\n\t"
      "s_add_u32 s44, s20, 0\n\t"
      "s_addc_u32 s45, s21, 0\n\t"
      "global_load_lds_dword v207, s[44:45]\n\t"
      "s_cmp_eq_u32 s8, 0\n\t"
      "s_cbranch_scc0 PRB_%=\n\t"
      "PRC_%=:\n\t"
      "s_mov_b32 m0, 163328\n\t"
      "s_add_u32 s44, s20, 1835008\n\t"
      "s_addc_u32 s45, s21, 0\n\t"
      "global_load_lds_dword v207, s[44:45]\n\t"
      "s_waitcnt lgkmcnt(0)\n\t"
      "s_branch HEND_%=\n\t"
      "GRR_%=:\n\t"
      "s_mov_b32 s51, 0\n\t"
      "GRL_%=:\n\t"
      "s_add_u32 s51, s51, 1\n\t"
      "s_cmp_lt_u32 s51, 262144\n\t"
      "s_cbranch_scc0 GRX_%=\n\t"
      "s_sleep 2\n\t"
      "global_load_dwordx4 v[4:7], v200, s[14:15] sc1\n\t"
      "global_load_dwordx4 v[8:11], v201, s[14:15] sc1\n\t"
      "s_waitcnt vmcnt(0)\n\t"
      "v_xor_b32_e32 v6, v6, v4\n\t"
      "v_xor_b32_e32 v7, v7, v5\n\t"
      "v_xor_b32_e32 v10, v10, v8\n\t"
      "v_xor_b32_e32 v11, v11, v9\n\t"
      "v_cmp_ne_u64_e64 s[48:49], s[16:17], v[6:7]\n\t"
      "v_cmp_ne_u64_e32 vcc, s[16:17], v[10:11]\n\t"
      "s_nop 0\n\t"
      "s_or_b64 vcc, vcc, s[48:49]\n\t"
      "s_cbranch_scc1 GRL_%=\n\t"
      "GRX_%=:\n\t"
      "s_branch GRB_%=\n\t"
      "PRB_%=:\n\t"
      "s_add_u32 s44, s14, s8\n\t"
      "s_addc_u32 s45, s15, -1\n\t"
      "s_mov_b64 s[46:47], -1\n\t"
      "s_mov_b32 s48, 0\n\t"
      ".p2align 6\n\t"
      "PRL_%=:\n\t"
      "s_waitcnt lgkmcnt(0)\n\t"
      "s_memtime s[40:41]\n\t"
      "s_waitcnt lgkmcnt(0)\n\t"
      "s_load_dword s50, s[44:45], s48\n\t"
      "s_waitcnt lgkmcnt(0)\n\t"
      "s_memtime s[42:43]\n\t"
      "s_add_u32 s48, s48, 8832\n\t"
      "s_waitcnt lgkmcnt(0)\n\t"
      "s_sub_u32 s42, s42, s40\n\t"
      "s_min_u32 s42, s46, s42\n\t"
      "s_mov_b32 s46, s47\n\t"
      "s_mov_b32 s47, s42\n\t"
      "s_cmp_lt_u32 s48, 35328\n\t"
      "s_cbranch_scc1 PRL_%=\n\t"
      "s_cmp_lt_u32 s47, s46\n\t"
      "s_addc_u32 s42, s16, 0\n\t"
      "s_cmp_lt_i32 s8, -2097152\n\t"
      "s_cselect_b32 s43, 0, 8\n\t"
      "s_add_u32 s43, s43, 114688\n\t"
      "v_mbcnt_lo_u32_b32 v2, -1, 0\n\t"
      "v_mov_b32_e32 v0, s42\n\t"
      "v_mov_b32_e32 v1, s17\n\t"
      "v_lshl_add_u32 v2, v2, 7, s43\n\t"
      "s_mov_b64 exec, 0xff\n\t"
      "global_store_dwordx2 v2, v[0:1], s[14:15] sc1\n\t"
      "s_mov_b64 exec, -1\n\t"
      "s_branch PRC_%=\n\t"
      "HEND_%=:\n\t"
      : "+{s[4:19]}"(s), "+{s[20:21]}"(tw), "+{s[22:23]}"(pf), "+{v[192:199]}"(f), "+{v[200:207]}"(v)
      :
      : "memory", "vcc", "scc",
        "v0", "v1", "v2", "v3", "v4", "v5", "v6", "v7", "v8", "v9", "v10", "v11", "v12", "v13", "v14", "v15", "v16",
        "v17", "v18", "v19", "v20", "v21", "v22", "v23", "v24", "v25", "v26", "v27", "v28", "v29", "v30", "v31", "v32",
        "v33", "v34", "v35", "v36", "v37", "v38", "v39", "s40", "s41", "s42", "s43", "s44", "s45", "s46", "s47", "s48",
        "s49", "s50", "s51"
    );
    }
    PIN();
    LANE_BITS(__builtin_amdgcn_mbcnt_hi(~0u, __builtin_amdgcn_mbcnt_lo(~0u, 0u)));
    const bool has3 = idx < 14;
    const int ft = has3 ? 7 * xp + (idx >> 1) : 0, blk0 = 7 * (2 * side + (idx & 1));
    u32x8 v;
    u32x16 s;
    v[0] = lane * 16;
    const int rot = 4 * (idx & 1), kg = 2 * ft + ((wave >> 1) & 1);
    u64 kr = (u64)(7 - rot) | ((u64)rot << 32);
    v[1] = has3 ? lr * 3584 + ((lc ^ swz_tr(lr)) << 4) : SP3 ? (min(lane, 55) + 96 * (idx & 1)) * 4096 + 384 + xp * 1024 : (lr + 8 * side) * 4096 + lc * 128 + xp * 1024;
    v[2] = wave * G_WV + g * 1024 + q4 * 128 + (((2 * (wave & 3) + (p4 >> 1)) ^ swz_tr(8 * g + q4)) << 4) + (p4 & 1) * 8;
    v[3] = wave * 4096 + (wave & 3) * 1024 + ((lane * 16) ^ ((g & 1) << 7));
    const int gg = 2 * (lane >> 5) + (lane & 1);
    v[4] = (wave & 3) * 1024 + (((gg * 16 + ((lane >> 1) & 15)) * 16) ^ ((gg & 1) << 7));
    v[5] = (wave & 1) * 512 + lane * 8;
    v[6] = g * 8;
    v[7] = ST_H3 + (wave * 16 + c) * 128 + xcd * 16384;
    put(s, 0, has3 ? (u64)(w1 + ft * 128 + (wave * 256 + rot * 32) * 3584) : (u64)(w2 + (wave * 224 + (idx & 1) * 16) * 4096));
    put(s, 2, (u64)(ws + WS_HN + unit(blk0, wave, 0)));
    put(s, 4, (u64)(ws + WS_G + unit(blk0, kg / 7, 0) + kg % 7 * 1024));
    put(s, 6, (u64)(b1 + (64 * ft + 16 * (wave & 3)) * 2));
    put(s, 8, (u64)(ws + WS_ST));
    u64 hint = (u64)(ST_H4 + (blk0 * 4 + ft) * 128) | ((u64)(ST_FL + (4 * ft + 2 * side + (idx & 1)) * 256) << 32);
    s[12] = UNI((has3 ? 4 : 0) |  (wave == ((xcd + XO) & 7) ? 16 : 0));
    s[13] = wave * G_WV;
    asm volatile(
      "s_waitcnt lgkmcnt(0)\n\ts_nop 4\n\t"
      "s_bitcmp0_b32 s16, 2\n\t"
      "s_cbranch_scc1 ASP_%=\n\t"
      "v_mov_b32_e32 v216, 163312\n\t"
      "ds_read_b128 v[216:219], v216\n\t"
      "v_xor_b32_e32 v248, 64, v241\n\t"
      "v_add_u32_e32 v249, 55296, v241\n\t"
      "v_add_u32_e32 v250, 82944, v248\n\t"
      "v_add_u32_e32 v248, 27648, v248\n\t"
      "v_xor_b32_e32 v251, 32, v242\n\t"
      "v_xor_b32_e32 v252, 64, v242\n\t"
      "v_xor_b32_e32 v253, 96, v242\n\t"
      "s_cmp_ge_u32 s17, 65536\n\t"
      "s_cbranch_scc0 BOH_%=\n\t"
      "s_barrier\n\t"
      "BOH_%=:\n\t"
      "s_add_u32 m0, s17, 0\n\t"
      "s_nop 0\n\t"
      "global_load_lds_dwordx4 v241, s[4:5]\n\t"
      "global_load_lds_dwordx4 v248, s[4:5] offset:1024\n\t"
      "global_load_lds_dwordx4 v249, s[4:5] offset:2048\n\t"
      "global_load_lds_dwordx4 v250, s[4:5] offset:3072\n\t"
      "s_sub_u32 s20, s20, 1\n\t"
      "s_cselect_b32 s48, -7, 1\n\t"
      "s_cselect_b32 s49, -1, 0\n\t"
      "s_mul_i32 s48, s48, 114688\n\t"
      "s_add_u32 s4, s4, s48\n\t"
      "s_addc_u32 s5, s5, s49\n\t"
      "global_load_dwordx2 v[238:239], v246, s[10:11]\n\t"
      "s_waitcnt lgkmcnt(0)\n\t"
      "v_readfirstlane_b32 s14, v216\n\t"
      "v_readfirstlane_b32 s15, v217\n\t"
      "v_readfirstlane_b32 s46, v218\n\t"
      "v_readfirstlane_b32 s47, v219\n\t"
      "s_add_u32 s52, s6, 0\n\t"
      "s_addc_u32 s53, s7, 0\n\t"
      "s_add_u32 s54, s6, 131072\n\t"
      "s_addc_u32 s55, s7, 0\n\t"
      "s_add_u32 s56, s6, 262144\n\t"
      "s_addc_u32 s57, s7, 0\n\t"
      "s_add_u32 s58, s6, 393216\n\t"
      "s_addc_u32 s59, s7, 0\n\t"
      "s_add_u32 s60, s6, 524288\n\t"
      "s_addc_u32 s61, s7, 0\n\t"
      "s_add_u32 s62, s6, 655360\n\t"
      "s_addc_u32 s63, s7, 0\n\t"
      "s_add_u32 s64, s6, 786432\n\t"
      "s_addc_u32 s65, s7, 0\n\t"
      "s_add_u32 s48, s21, 0\n\t"
      "s_cmp_ge_u32 s48, 8\n\t"
      "s_cselect_b32 s49, 8, 0\n\t"
      "s_sub_u32 s48, s48, s49\n\t"
      "v_lshl_add_u32 v176, s48, 10, v240\n\t"
      "s_add_u32 s48, s21, 1\n\t"
      "s_cmp_ge_u32 s48, 8\n\t"
      "s_cselect_b32 s49, 8, 0\n\t"
      "s_sub_u32 s48, s48, s49\n\t"
      "v_lshl_add_u32 v177, s48, 10, v240\n\t"
      "s_add_u32 s48, s21, 2\n\t"
      "s_cmp_ge_u32 s48, 8\n\t"
      "s_cselect_b32 s49, 8, 0\n\t"
      "s_sub_u32 s48, s48, s49\n\t"
      "v_lshl_add_u32 v178, s48, 10, v240\n\t"
      "s_add_u32 s48, s21, 3\n\t"
      "s_cmp_ge_u32 s48, 8\n\t"
      "s_cselect_b32 s49, 8, 0\n\t"
      "s_sub_u32 s48, s48, s49\n\t"
      "v_lshl_add_u32 v179, s48, 10, v240\n\t"
      "s_add_u32 s48, s21, 4\n\t"
      "s_cmp_ge_u32 s48, 8\n\t"
      "s_cselect_b32 s49, 8, 0\n\t"
      "s_sub_u32 s48, s48, s49\n\t"
      "v_lshl_add_u32 v180, s48, 10, v240\n\t"
      "s_add_u32 s48, s21, 5\n\t"
      "s_cmp_ge_u32 s48, 8\n\t"
      "s_cselect_b32 s49, 8, 0\n\t"
      "s_sub_u32 s48, s48, s49\n\t"
      "v_lshl_add_u32 v181, s48, 10, v240\n\t"
      "s_add_u32 s48, s21, 6\n\t"
      "s_cmp_ge_u32 s48, 8\n\t"
      "s_cselect_b32 s49, 8, 0\n\t"
      "s_sub_u32 s48, s48, s49\n\t"
      "v_lshl_add_u32 v182, s48, 10, v240\n\t"
      "s_add_u32 s48, s21, 7\n\t"
      "s_cmp_ge_u32 s48, 8\n\t"
      "s_cselect_b32 s49, 8, 0\n\t"
      "s_sub_u32 s48, s48, s49\n\t"
      "v_lshl_add_u32 v183, s48, 10, v240\n\t"
      "s_add_u32 m0, s17, 4096\n\t"
      "s_nop 0\n\t"
      "global_load_lds_dwordx4 v241, s[4:5]\n\t"
      "global_load_lds_dwordx4 v248, s[4:5] offset:1024\n\t"
      "global_load_lds_dwordx4 v249, s[4:5] offset:2048\n\t"
      "global_load_lds_dwordx4 v250, s[4:5] offset:3072\n\t"
      "s_sub_u32 s20, s20, 1\n\t"
      "s_cselect_b32 s48, -7, 1\n\t"
      "s_cselect_b32 s49, -1, 0\n\t"
      "s_mul_i32 s48, s48, 114688\n\t"
      "s_add_u32 s4, s4, s48\n\t"
      "s_addc_u32 s5, s5, s49\n\t"
      "s_waitcnt vmcnt(5)\n\t"
      "s_add_u32 m0, s17, 8192\n\t"
      "s_nop 0\n\t"
      "global_load_lds_dwordx4 v241, s[4:5]\n\t"
      "global_load_lds_dwordx4 v248, s[4:5] offset:1024\n\t"
      "global_load_lds_dwordx4 v249, s[4:5] offset:2048\n\t"
      "global_load_lds_dwordx4 v250, s[4:5] offset:3072\n\t"
      "s_sub_u32 s20, s20, 1\n\t"
      "s_cselect_b32 s48, -7, 1\n\t"
      "s_cselect_b32 s49, -1, 0\n\t"
      "s_mul_i32 s48, s48, 114688\n\t"
      "s_add_u32 s4, s4, s48\n\t"
      "s_addc_u32 s5, s5, s49\n\t"
      "ds_read_b64_tr_b16 v[0:1], v242\n\t"
      "ds_read_b64_tr_b16 v[2:3], v242 offset:512\n\t"
      "ds_read_b64_tr_b16 v[4:5], v251\n\t"
      "ds_read_b64_tr_b16 v[6:7], v251 offset:512\n\t"
      "ds_read_b64_tr_b16 v[8:9], v252\n\t"
      "ds_read_b64_tr_b16 v[10:11], v252 offset:512\n\t"
      "ds_read_b64_tr_b16 v[12:13], v253\n\t"
      "ds_read_b64_tr_b16 v[14:15], v253 offset:512\n\t"
      "s_waitcnt vmcnt(4)\n\t"
      "s_add_u32 m0, s17, 12288\n\t"
      "s_nop 0\n\t"
      "global_load_lds_dwordx4 v241, s[4:5]\n\t"
      "global_load_lds_dwordx4 v248, s[4:5] offset:1024\n\t"
      "global_load_lds_dwordx4 v249, s[4:5] offset:2048\n\t"
      "global_load_lds_dwordx4 v250, s[4:5] offset:3072\n\t"
      "s_sub_u32 s20, s20, 1\n\t"
      "s_cselect_b32 s48, -7, 1\n\t"
      "s_cselect_b32 s49, -1, 0\n\t"
      "s_mul_i32 s48, s48, 114688\n\t"
      "s_add_u32 s4, s4, s48\n\t"
      "s_addc_u32 s5, s5, s49\n\t"
      "ds_read_b64_tr_b16 v[16:17], v242 offset:4096\n\t"
      "ds_read_b64_tr_b16 v[18:19], v242 offset:4608\n\t"
      "ds_read_b64_tr_b16 v[20:21], v251 offset:4096\n\t"
      "ds_read_b64_tr_b16 v[22:23], v251 offset:4608\n\t"
      "ds_read_b64_tr_b16 v[24:25], v252 offset:4096\n\t"
      "ds_read_b64_tr_b16 v[26:27], v252 offset:4608\n\t"
      "ds_read_b64_tr_b16 v[28:29], v253 offset:4096\n\t"
      "ds_read_b64_tr_b16 v[30:31], v253 offset:4608\n\t"
      "v_xor_b32_e32 v164, 1024, v243\n\t"
      "v_xor_b32_e32 v165, 2048, v243\n\t"
      "v_xor_b32_e32 v166, 3072, v243\n\t"
      "v_lshlrev_b32_e32 v236, 16, v238\n\t"
      "v_and_b32_e32 v237, 0xffff0000, v238\n\t"
      "v_lshlrev_b32_e32 v238, 16, v239\n\t"
      "v_and_b32_e32 v239, 0xffff0000, v239\n\t"
      "v_mov_b32_e32 v235, 0xbdd2d3e8\n\t"
      "s_waitcnt vmcnt(4)\n\t"
      "s_waitcnt lgkmcnt(8)\n\t"
      "s_add_u32 m0, s17, 0\n\t"
      "s_nop 0\n\t"
      "global_load_lds_dwordx4 v241, s[4:5]\n\t"
      "global_load_lds_dwordx4 v248, s[4:5] offset:1024\n\t"
      "global_load_lds_dwordx4 v249, s[4:5] offset:2048\n\t"
      "global_load_lds_dwordx4 v250, s[4:5] offset:3072\n\t"
      "s_sub_u32 s20, s20, 1\n\t"
      "s_cselect_b32 s48, -7, 1\n\t"
      "s_cselect_b32 s49, -1, 0\n\t"
      "s_mul_i32 s48, s48, 114688\n\t"
      "s_add_u32 s4, s4, s48\n\t"
      "s_addc_u32 s5, s5, s49\n\t"
      "ds_read_b64_tr_b16 v[32:33], v242 offset:8192\n\t"
      "ds_read_b64_tr_b16 v[34:35], v242 offset:8704\n\t"
      "ds_read_b64_tr_b16 v[36:37], v251 offset:8192\n\t"
      "ds_read_b64_tr_b16 v[38:39], v251 offset:8704\n\t"
      "ds_read_b64_tr_b16 v[40:41], v252 offset:8192\n\t"
      "ds_read_b64_tr_b16 v[42:43], v252 offset:8704\n\t"
      "ds_read_b64_tr_b16 v[44:45], v253 offset:8192\n\t"
      "ds_read_b64_tr_b16 v[46:47], v253 offset:8704\n\t"
      "s_waitcnt vmcnt(4)\n\t"
      "s_waitcnt lgkmcnt(8)\n\t"
      "s_add_u32 m0, s17, 4096\n\t"
      "s_nop 0\n\t"
      "global_load_lds_dwordx4 v241, s[4:5]\n\t"
      "global_load_lds_dwordx4 v248, s[4:5] offset:1024\n\t"
      "global_load_lds_dwordx4 v249, s[4:5] offset:2048\n\t"
      "global_load_lds_dwordx4 v250, s[4:5] offset:3072\n\t"
      "s_sub_u32 s20, s20, 1\n\t"
      "s_cselect_b32 s48, -7, 1\n\t"
      "s_cselect_b32 s49, -1, 0\n\t"
      "s_mul_i32 s48, s48, 114688\n\t"
      "s_add_u32 s4, s4, s48\n\t"
      "s_addc_u32 s5, s5, s49\n\t"
      "ds_read_b64_tr_b16 v[48:49], v242 offset:12288\n\t"
      "ds_read_b64_tr_b16 v[50:51], v242 offset:12800\n\t"
      "ds_read_b64_tr_b16 v[52:53], v251 offset:12288\n\t"
      "ds_read_b64_tr_b16 v[54:55], v251 offset:12800\n\t"
      "ds_read_b64_tr_b16 v[56:57], v252 offset:12288\n\t"
      "ds_read_b64_tr_b16 v[58:59], v252 offset:12800\n\t"
      "ds_read_b64_tr_b16 v[60:61], v253 offset:12288\n\t"
      "ds_read_b64_tr_b16 v[62:63], v253 offset:12800\n\t"
      "s_waitcnt vmcnt(4)\n\t"
      "s_waitcnt lgkmcnt(8)\n\t"
      "s_add_u32 m0, s17, 8192\n\t"
      "s_nop 0\n\t"
      "global_load_lds_dwordx4 v241, s[4:5]\n\t"
      "global_load_lds_dwordx4 v248, s[4:5] offset:1024\n\t"
      "global_load_lds_dwordx4 v249, s[4:5] offset:2048\n\t"
      "global_load_lds_dwordx4 v250, s[4:5] offset:3072\n\t"
      "s_sub_u32 s20, s20, 1\n\t"
      "s_cselect_b32 s48, -7, 1\n\t"
      "s_cselect_b32 s49, -1, 0\n\t"
      "s_mul_i32 s48, s48, 114688\n\t"
      "s_add_u32 s4, s4, s48\n\t"
      "s_addc_u32 s5, s5, s49\n\t"
      "ds_read_b64_tr_b16 v[64:65], v242\n\t"
      "ds_read_b64_tr_b16 v[66:67], v242 offset:512\n\t"
      "ds_read_b64_tr_b16 v[68:69], v251\n\t"
      "ds_read_b64_tr_b16 v[70:71], v251 offset:512\n\t"
      "ds_read_b64_tr_b16 v[72:73], v252\n\t"
      "ds_read_b64_tr_b16 v[74:75], v252 offset:512\n\t"
      "ds_read_b64_tr_b16 v[76:77], v253\n\t"
      "ds_read_b64_tr_b16 v[78:79], v253 offset:512\n\t"
      "s_waitcnt vmcnt(4)\n\t"
      "s_waitcnt lgkmcnt(8)\n\t"
      "s_add_u32 m0, s17, 12288\n\t"
      "s_nop 0\n\t"
      "global_load_lds_dwordx4 v241, s[4:5]\n\t"
      "global_load_lds_dwordx4 v248, s[4:5] offset:1024\n\t"
      "global_load_lds_dwordx4 v249, s[4:5] offset:2048\n\t"
      "global_load_lds_dwordx4 v250, s[4:5] offset:3072\n\t"
      "ds_read_b64_tr_b16 v[80:81], v242 offset:4096\n\t"
      "ds_read_b64_tr_b16 v[82:83], v242 offset:4608\n\t"
      "ds_read_b64_tr_b16 v[84:85], v251 offset:4096\n\t"
      "ds_read_b64_tr_b16 v[86:87], v251 offset:4608\n\t"
      "ds_read_b64_tr_b16 v[88:89], v252 offset:4096\n\t"
      "ds_read_b64_tr_b16 v[90:91], v252 offset:4608\n\t"
      "ds_read_b64_tr_b16 v[92:93], v253 offset:4096\n\t"
      "ds_read_b64_tr_b16 v[94:95], v253 offset:4608\n\t"
      "s_waitcnt vmcnt(4)\n\t"
      "ds_read_b64_tr_b16 v[96:97], v242 offset:8192\n\t"
      "ds_read_b64_tr_b16 v[98:99], v242 offset:8704\n\t"
      "ds_read_b64_tr_b16 v[100:101], v251 offset:8192\n\t"
      "ds_read_b64_tr_b16 v[102:103], v251 offset:8704\n\t"
      "ds_read_b64_tr_b16 v[104:105], v252 offset:8192\n\t"
      "ds_read_b64_tr_b16 v[106:107], v252 offset:8704\n\t"
      "ds_read_b64_tr_b16 v[108:109], v253 offset:8192\n\t"
      "ds_read_b64_tr_b16 v[110:111], v253 offset:8704\n\t"
      "s_waitcnt vmcnt(0)\n\t"
      "ds_read_b64_tr_b16 v[112:113], v242 offset:12288\n\t"
      "ds_read_b64_tr_b16 v[114:115], v242 offset:12800\n\t"
      "ds_read_b64_tr_b16 v[116:117], v251 offset:12288\n\t"
      "ds_read_b64_tr_b16 v[118:119], v251 offset:12800\n\t"
      "ds_read_b64_tr_b16 v[120:121], v252 offset:12288\n\t"
      "ds_read_b64_tr_b16 v[122:123], v252 offset:12800\n\t"
      "ds_read_b64_tr_b16 v[124:125], v253 offset:12288\n\t"
      "ds_read_b64_tr_b16 v[126:127], v253 offset:12800\n\t"
      "s_waitcnt lgkmcnt(0)\n\t"
      "s_barrier\n\t"
      "s_mov_b32 s51, 0\n\t"
      "s_branch PRE_%=\n\t"
      "PRL_%=:\n\t"
      "s_sleep 4\n\t"
      "s_add_u32 s51, s51, 1\n\t"
      "s_cmp_lt_u32 s51, 262144\n\t"
      "s_cbranch_scc0 PRX_%=\n\t"
      "PRE_%=:\n\t"
      "s_mov_b64 exec, 65535\n\t"
      "s_bitcmp1_b32 s16, 4\n\t"
      "s_cbranch_scc1 PM_%=\n\t"
      "global_load_dwordx2 v[232:233], v247, s[12:13] sc1\n\t"
      "s_waitcnt vmcnt(0)\n\t"
      "PM_%=:\n\t"
      "s_cbranch_scc0 PN_%=\n\t"
      "v_mov_b64_e32 v[232:233], 0\n\t"
      "global_atomic_or_x2 v[232:233], v247, v[232:233], s[12:13] sc0 sc1\n\t"
      "s_waitcnt vmcnt(0)\n\t"
      "PN_%=:\n\t"
      "v_subrev_u32_e32 v232, s14, v232\n\t"
      "v_cmp_ne_u32_e64 s[48:49], s15, v233\n\t"
      "v_subrev_u32_e32 v232, 0, v232\n\t"
      "v_cmp_lt_u32_e32 vcc, 7, v232\n\t"
      "s_mov_b64 exec, -1\n\t"
      "s_or_b64 vcc, vcc, s[48:49]\n\t"
      "s_cbranch_vccnz PRL_%=\n\t"
      "PRX_%=:\n\t"
      "PRY_%=:\n\t"
      "v_readfirstlane_b32 s48, v232\n\t"
      "s_nop 0\n\t"
      "s_lshl_b32 s49, s48, 13\n\t"
      "s_and_b32 s49, s49, 8192\n\t"
      "s_xor_b32 s52, s52, s49\n\t"
      "global_load_dwordx4 v[128:131], v176, s[52:53] sc1\n\t"
      "global_load_dwordx4 v[132:135], v177, s[52:53] sc1\n\t"
      "global_load_dwordx4 v[136:139], v178, s[52:53] sc1\n\t"
      "global_load_dwordx4 v[140:143], v179, s[52:53] sc1\n\t"
      "global_load_dwordx4 v[144:147], v180, s[52:53] sc1\n\t"
      "global_load_dwordx4 v[148:151], v181, s[52:53] sc1\n\t"
      "global_load_dwordx4 v[152:155], v182, s[52:53] sc1\n\t"
      "global_load_dwordx4 v[156:159], v183, s[52:53] sc1\n\t"
      "s_xor_b32 s54, s54, s49\n\t"
      "s_xor_b32 s56, s56, s49\n\t"
      "s_xor_b32 s58, s58, s49\n\t"
      "s_xor_b32 s60, s60, s49\n\t"
      "s_xor_b32 s62, s62, s49\n\t"
      "s_xor_b32 s64, s64, s49\n\t"
      "s_lshl_b32 s48, s48, 12\n\t"
      "s_and_b32 s48, s48, 8192\n\t"
      "s_xor_b32 s8, s8, s48\n\t"
      "s_cmp_ge_u32 s17, 65536\n\t"
      "s_cbranch_scc1 GQ_%=\n\t"
      ".p2align 6\n\t"
      "s_setprio 1\n\t"
      "v_mov_b32_e32 v232, s14\n\t"
      "v_mov_b32_e32 v233, s15\n\t"
      "v_lshl_add_u32 v247, v240, 10, s22\n\t"
      "s_waitcnt vmcnt(7)\n\t"
      "v_max3_u32 v234, v128, v129, v130\n\t"
      "v_max_u32_e32 v234, v234, v131\n\t"
      "v_cmp_eq_u32_e32 vcc, -1, v234\n\t"
      "s_cbranch_vccnz FP0R_%=\n\t"
      "FP0B_%=:\n\t"
      "v_mfma_f32_16x16x32_bf16 v[184:187], v[0:3], v[128:131], 0\n\t"
      "v_mfma_f32_16x16x32_bf16 v[188:191], v[4:7], v[128:131], 0\n\t"
      "v_mfma_f32_16x16x32_bf16 v[192:195], v[8:11], v[128:131], 0\n\t"
      "v_mfma_f32_16x16x32_bf16 v[196:199], v[12:15], v[128:131], 0\n\t"
      "global_load_dwordx4 v[128:131], v176, s[54:55] sc1\n\t"
      "s_waitcnt vmcnt(7)\n\t"
      "v_max3_u32 v234, v132, v133, v134\n\t"
      "v_max_u32_e32 v234, v234, v135\n\t"
      "v_cmp_eq_u32_e32 vcc, -1, v234\n\t"
      "s_cbranch_vccnz FP1R_%=\n\t"
      "FP1B_%=:\n\t"
      "v_mfma_f32_16x16x32_bf16 v[184:187], v[16:19], v[132:135], v[184:187]\n\t"
      "v_mfma_f32_16x16x32_bf16 v[188:191], v[20:23], v[132:135], v[188:191]\n\t"
      "v_mfma_f32_16x16x32_bf16 v[192:195], v[24:27], v[132:135], v[192:195]\n\t"
      "v_mfma_f32_16x16x32_bf16 v[196:199], v[28:31], v[132:135], v[196:199]\n\t"
      "global_load_dwordx4 v[132:135], v177, s[54:55] sc1\n\t"
      "s_waitcnt vmcnt(7)\n\t"
      "v_max3_u32 v234, v136, v137, v138\n\t"
      "v_max_u32_e32 v234, v234, v139\n\t"
      "v_cmp_eq_u32_e32 vcc, -1, v234\n\t"
      "s_cbranch_vccnz FP2R_%=\n\t"
      "FP2B_%=:\n\t"
      "v_mfma_f32_16x16x32_bf16 v[184:187], v[32:35], v[136:139], v[184:187]\n\t"
      "v_mfma_f32_16x16x32_bf16 v[188:191], v[36:39], v[136:139], v[188:191]\n\t"
      "v_mfma_f32_16x16x32_bf16 v[192:195], v[40:43], v[136:139], v[192:195]\n\t"
      "v_mfma_f32_16x16x32_bf16 v[196:199], v[44:47], v[136:139], v[196:199]\n\t"
      "global_load_dwordx4 v[136:139], v178, s[54:55] sc1\n\t"
      "s_waitcnt vmcnt(7)\n\t"
      "v_max3_u32 v234, v140, v141, v142\n\t"
      "v_max_u32_e32 v234, v234, v143\n\t"
      "v_cmp_eq_u32_e32 vcc, -1, v234\n\t"
      "s_cbranch_vccnz FP3R_%=\n\t"
      "FP3B_%=:\n\t"
      "v_mfma_f32_16x16x32_bf16 v[184:187], v[48:51], v[140:143], v[184:187]\n\t"
      "v_mfma_f32_16x16x32_bf16 v[188:191], v[52:55], v[140:143], v[188:191]\n\t"
      "v_mfma_f32_16x16x32_bf16 v[192:195], v[56:59], v[140:143], v[192:195]\n\t"
      "v_mfma_f32_16x16x32_bf16 v[196:199], v[60:63], v[140:143], v[196:199]\n\t"
      "global_load_dwordx4 v[140:143], v179, s[54:55] sc1\n\t"
      "s_waitcnt vmcnt(7)\n\t"
      "v_max3_u32 v234, v144, v145, v146\n\t"
      "v_max_u32_e32 v234, v234, v147\n\t"
      "v_cmp_eq_u32_e32 vcc, -1, v234\n\t"
      "s_cbranch_vccnz FP4R_%=\n\t"
      "FP4B_%=:\n\t"
      "v_mfma_f32_16x16x32_bf16 v[184:187], v[64:67], v[144:147], v[184:187]\n\t"
      "v_mfma_f32_16x16x32_bf16 v[188:191], v[68:71], v[144:147], v[188:191]\n\t"
      "v_mfma_f32_16x16x32_bf16 v[192:195], v[72:75], v[144:147], v[192:195]\n\t"
      "v_mfma_f32_16x16x32_bf16 v[196:199], v[76:79], v[144:147], v[196:199]\n\t"
      "global_load_dwordx4 v[144:147], v180, s[54:55] sc1\n\t"
      "s_waitcnt vmcnt(7)\n\t"
      "v_max3_u32 v234, v148, v149, v150\n\t"
      "v_max_u32_e32 v234, v234, v151\n\t"
      "v_cmp_eq_u32_e32 vcc, -1, v234\n\t"
      "s_cbranch_vccnz FP5R_%=\n\t"
      "FP5B_%=:\n\t"
      "v_mfma_f32_16x16x32_bf16 v[184:187], v[80:83], v[148:151], v[184:187]\n\t"
      "v_mfma_f32_16x16x32_bf16 v[188:191], v[84:87], v[148:151], v[188:191]\n\t"
      "v_mfma_f32_16x16x32_bf16 v[192:195], v[88:91], v[148:151], v[192:195]\n\t"
      "v_mfma_f32_16x16x32_bf16 v[196:199], v[92:95], v[148:151], v[196:199]\n\t"
      "global_load_dwordx4 v[148:151], v181, s[54:55] sc1\n\t"
      "s_waitcnt vmcnt(7)\n\t"
      "v_max3_u32 v234, v152, v153, v154\n\t"
      "v_max_u32_e32 v234, v234, v155\n\t"
      "v_cmp_eq_u32_e32 vcc, -1, v234\n\t"
      "s_cbranch_vccnz FP6R_%=\n\t"
      "FP6B_%=:\n\t"
      "v_mfma_f32_16x16x32_bf16 v[184:187], v[96:99], v[152:155], v[184:187]\n\t"
      "v_mfma_f32_16x16x32_bf16 v[188:191], v[100:103], v[152:155], v[188:191]\n\t"
      "v_mfma_f32_16x16x32_bf16 v[192:195], v[104:107], v[152:155], v[192:195]\n\t"
      "v_mfma_f32_16x16x32_bf16 v[196:199], v[108:111], v[152:155], v[196:199]\n\t"
      "global_load_dwordx4 v[152:155], v182, s[54:55] sc1\n\t"
      "s_waitcnt vmcnt(7)\n\t"
      "v_max3_u32 v234, v156, v157, v158\n\t"
      "v_max_u32_e32 v234, v234, v159\n\t"
      "v_cmp_eq_u32_e32 vcc, -1, v234\n\t"
      "s_cbranch_vccnz FP7R_%=\n\t"
      "FP7B_%=:\n\t"
      "v_mfma_f32_16x16x32_bf16 v[184:187], v[112:115], v[156:159], v[184:187]\n\t"
      "v_mfma_f32_16x16x32_bf16 v[188:191], v[116:119], v[156:159], v[188:191]\n\t"
      "v_mfma_f32_16x16x32_bf16 v[192:195], v[120:123], v[156:159], v[192:195]\n\t"
      "v_mfma_f32_16x16x32_bf16 v[196:199], v[124:127], v[156:159], v[196:199]\n\t"
      "global_load_dwordx4 v[156:159], v183, s[54:55] sc1\n\t"
      "s_nop 3\n\t"
      "ds_write_b128 v243, v[184:187]\n\t"
      "ds_write_b128 v164, v[188:191]\n\t"
      "ds_write_b128 v165, v[192:195]\n\t"
      "ds_write_b128 v166, v[196:199]\n\t"
      "s_waitcnt lgkmcnt(0)\n\t"
      "s_barrier\n\t"
      "ds_read_b128 v[216:219], v244\n\t"
      "ds_read_b128 v[220:223], v244 offset:4096\n\t"
      "ds_read_b128 v[224:227], v244 offset:8192\n\t"
      "ds_read_b128 v[228:231], v244 offset:12288\n\t"
      "ds_read_b128 v[184:187], v244 offset:16384\n\t"
      "ds_read_b128 v[188:191], v244 offset:20480\n\t"
      "ds_read_b128 v[192:195], v244 offset:24576\n\t"
      "ds_read_b128 v[196:199], v244 offset:28672\n\t"
      "s_waitcnt lgkmcnt(6)\n\t"
      "v_add_f32_e32 v216, v216, v220\n\t"
      "v_add_f32_e32 v217, v217, v221\n\t"
      "v_add_f32_e32 v218, v218, v222\n\t"
      "v_add_f32_e32 v219, v219, v223\n\t"
      "s_waitcnt lgkmcnt(4)\n\t"
      "v_add_f32_e32 v224, v224, v228\n\t"
      "v_add_f32_e32 v225, v225, v229\n\t"
      "v_add_f32_e32 v226, v226, v230\n\t"
      "v_add_f32_e32 v227, v227, v231\n\t"
      "s_waitcnt lgkmcnt(2)\n\t"
      "v_add_f32_e32 v184, v184, v188\n\t"
      "v_add_f32_e32 v185, v185, v189\n\t"
      "v_add_f32_e32 v186, v186, v190\n\t"
      "v_add_f32_e32 v187, v187, v191\n\t"
      "v_add_f32_e32 v216, v216, v224\n\t"
      "v_add_f32_e32 v217, v217, v225\n\t"
      "v_add_f32_e32 v218, v218, v226\n\t"
      "v_add_f32_e32 v219, v219, v227\n\t"
      "s_waitcnt lgkmcnt(0)\n\t"
      "v_add_f32_e32 v192, v192, v196\n\t"
      "s_waitcnt vmcnt(7)\n\t"
      "v_mfma_f32_16x16x32_bf16 v[200:203], v[0:3], v[128:131], 0\n\t"
      "v_add_f32_e32 v193, v193, v197\n\t"
      "v_add_f32_e32 v194, v194, v198\n\t"
      "v_mfma_f32_16x16x32_bf16 v[204:207], v[4:7], v[128:131], 0\n\t"
      "v_add_f32_e32 v195, v195, v199\n\t"
      "v_add_f32_e32 v184, v184, v192\n\t"
      "v_mfma_f32_16x16x32_bf16 v[208:211], v[8:11], v[128:131], 0\n\t"
      "v_add_f32_e32 v185, v185, v193\n\t"
      "v_add_f32_e32 v186, v186, v194\n\t"
      "v_mfma_f32_16x16x32_bf16 v[212:215], v[12:15], v[128:131], 0\n\t"
      "global_load_dwordx4 v[128:131], v176, s[56:57] sc1\n\t"
      "v_add_f32_e32 v187, v187, v195\n\t"
      "s_waitcnt vmcnt(7)\n\t"
      "v_mfma_f32_16x16x32_bf16 v[200:203], v[16:19], v[132:135], v[200:203]\n\t"
      "v_add_f32_e32 v216, v216, v184\n\t"
      "v_add_f32_e32 v217, v217, v185\n\t"
      "v_mfma_f32_16x16x32_bf16 v[204:207], v[20:23], v[132:135], v[204:207]\n\t"
      "v_add_f32_e32 v218, v218, v186\n\t"
      "v_add_f32_e32 v219, v219, v187\n\t"
      "v_mfma_f32_16x16x32_bf16 v[208:211], v[24:27], v[132:135], v[208:211]\n\t"
      "v_mul_f32_e32 v220, v216, v216\n\t"
      "v_mul_f32_e32 v221, v217, v217\n\t"
      "v_mfma_f32_16x16x32_bf16 v[212:215], v[28:31], v[132:135], v[212:215]\n\t"
      "global_load_dwordx4 v[132:135], v177, s[56:57] sc1\n\t"
      "v_mul_f32_e32 v222, v218, v218\n\t"
      "s_waitcnt vmcnt(7)\n\t"
      "v_mfma_f32_16x16x32_bf16 v[200:203], v[32:35], v[136:139], v[200:203]\n\t"
      "v_mul_f32_e32 v223, v219, v219\n\t"
      "v_fmaak_f32 v220, v235, v220, 0xc0135761\n\t"
      "v_mfma_f32_16x16x32_bf16 v[204:207], v[36:39], v[136:139], v[204:207]\n\t"
      "v_fmaak_f32 v221, v235, v221, 0xc0135761\n\t"
      "v_fmaak_f32 v222, v235, v222, 0xc0135761\n\t"
      "v_mfma_f32_16x16x32_bf16 v[208:211], v[40:43], v[136:139], v[208:211]\n\t"
      "v_fmaak_f32 v223, v235, v223, 0xc0135761\n\t"
      "v_mul_f32_e32 v220, v216, v220\n\t"
      "v_mfma_f32_16x16x32_bf16 v[212:215], v[44:47], v[136:139], v[212:215]\n\t"
      "global_load_dwordx4 v[136:139], v178, s[56:57] sc1\n\t"
      "v_mul_f32_e32 v221, v217, v221\n\t"
      "s_waitcnt vmcnt(7)\n\t"
      "v_mfma_f32_16x16x32_bf16 v[200:203], v[48:51], v[140:143], v[200:203]\n\t"
      "v_mul_f32_e32 v222, v218, v222\n\t"
      "v_mul_f32_e32 v223, v219, v223\n\t"
      "v_mfma_f32_16x16x32_bf16 v[204:207], v[52:55], v[140:143], v[204:207]\n\t"
      "v_exp_f32_e32 v220, v220\n\t"
      "v_mfma_f32_16x16x32_bf16 v[208:211], v[56:59], v[140:143], v[208:211]\n\t"
      "v_exp_f32_e32 v221, v221\n\t"
      "v_mfma_f32_16x16x32_bf16 v[212:215], v[60:63], v[140:143], v[212:215]\n\t"
      "global_load_dwordx4 v[140:143], v179, s[56:57] sc1\n\t"
      "s_waitcnt vmcnt(7)\n\t"
      "v_mfma_f32_16x16x32_bf16 v[200:203], v[64:67], v[144:147], v[200:203]\n\t"
      "v_exp_f32_e32 v222, v222\n\t"
      "v_mfma_f32_16x16x32_bf16 v[204:207], v[68:71], v[144:147], v[204:207]\n\t"
      "v_exp_f32_e32 v223, v223\n\t"
      "v_mfma_f32_16x16x32_bf16 v[208:211], v[72:75], v[144:147], v[208:211]\n\t"
      "v_add_f32_e32 v220, 1.0, v220\n\t"
      "v_mfma_f32_16x16x32_bf16 v[212:215], v[76:79], v[144:147], v[212:215]\n\t"
      "global_load_dwordx4 v[144:147], v180, s[56:57] sc1\n\t"
      "v_add_f32_e32 v221, 1.0, v221\n\t"
      "s_waitcnt vmcnt(7)\n\t"
      "v_mfma_f32_16x16x32_bf16 v[200:203], v[80:83], v[148:151], v[200:203]\n\t"
      "v_add_f32_e32 v222, 1.0, v222\n\t"
      "v_add_f32_e32 v223, 1.0, v223\n\t"
      "v_mfma_f32_16x16x32_bf16 v[204:207], v[84:87], v[148:151], v[204:207]\n\t"
      "v_rcp_f32_e32 v220, v220\n\t"
      "v_mfma_f32_16x16x32_bf16 v[208:211], v[88:91], v[148:151], v[208:211]\n\t"
      "v_rcp_f32_e32 v221, v221\n\t"
      "v_mfma_f32_16x16x32_bf16 v[212:215], v[92:95], v[148:151], v[212:215]\n\t"
      "global_load_dwordx4 v[148:151], v181, s[56:57] sc1\n\t"
      "s_waitcnt vmcnt(7)\n\t"
      "v_mfma_f32_16x16x32_bf16 v[200:203], v[96:99], v[152:155], v[200:203]\n\t"
      "v_rcp_f32_e32 v222, v222\n\t"
      "v_mfma_f32_16x16x32_bf16 v[204:207], v[100:103], v[152:155], v[204:207]\n\t"
      "v_rcp_f32_e32 v223, v223\n\t"
      "v_mfma_f32_16x16x32_bf16 v[208:211], v[104:107], v[152:155], v[208:211]\n\t"
      "v_mul_f32_e32 v216, v216, v220\n\t"
      "v_mfma_f32_16x16x32_bf16 v[212:215], v[108:111], v[152:155], v[212:215]\n\t"
      "global_load_dwordx4 v[152:155], v182, s[56:57] sc1\n\t"
      "v_mul_f32_e32 v217, v217, v221\n\t"
      "s_waitcnt vmcnt(7)\n\t"
      "v_mfma_f32_16x16x32_bf16 v[200:203], v[112:115], v[156:159], v[200:203]\n\t"
      "v_mul_f32_e32 v218, v218, v222\n\t"
      "v_mul_f32_e32 v219, v219, v223\n\t"
      "v_mfma_f32_16x16x32_bf16 v[204:207], v[116:119], v[156:159], v[204:207]\n\t"
      "v_cvt_pk_bf16_f32 v220, v216, v217\n\t"
      "v_cvt_pk_bf16_f32 v221, v218, v219\n\t"
      "v_mfma_f32_16x16x32_bf16 v[208:211], v[120:123], v[156:159], v[208:211]\n\t"
      "v_min_u32_e32 v220, -2, v220\n\t"
      "v_min_u32_e32 v221, -2, v221\n\t"
      "v_mfma_f32_16x16x32_bf16 v[212:215], v[124:127], v[156:159], v[212:215]\n\t"
      "global_load_dwordx4 v[156:159], v183, s[56:57] sc1\n\t"
      "global_store_dwordx2 v245, v[220:221], s[8:9] sc1\n\t"
      "s_add_u32 s8, s8, 131072\n\t"
      "s_addc_u32 s9, s9, 0\n\t"
      "v_cmp_u_f32_e32 vcc, v200, v200\n\t"
      "s_cbranch_vccnz NP1R_%=\n\t"
      "NP1B_%=:\n\t"
      "ds_write_b128 v243, v[200:203] offset:32768\n\t"
      "ds_write_b128 v164, v[204:207] offset:32768\n\t"
      "ds_write_b128 v165, v[208:211] offset:32768\n\t"
      "ds_write_b128 v166, v[212:215] offset:32768\n\t"
      "s_waitcnt lgkmcnt(0)\n\t"
      "s_barrier\n\t"
      "ds_read_b128 v[216:219], v244 offset:32768\n\t"
      "ds_read_b128 v[220:223], v244 offset:36864\n\t"
      "ds_read_b128 v[224:227], v244 offset:40960\n\t"
      "ds_read_b128 v[228:231], v244 offset:45056\n\t"
      "ds_read_b128 v[200:203], v244 offset:49152\n\t"
      "ds_read_b128 v[204:207], v244 offset:53248\n\t"
      "ds_read_b128 v[208:211], v244 offset:57344\n\t"
      "ds_read_b128 v[212:215], v244 offset:61440\n\t"
      "s_waitcnt lgkmcnt(6)\n\t"
      "v_add_f32_e32 v216, v216, v220\n\t"
      "v_add_f32_e32 v217, v217, v221\n\t"
      "v_add_f32_e32 v218, v218, v222\n\t"
      "v_add_f32_e32 v219, v219, v223\n\t"
      "s_waitcnt lgkmcnt(4)\n\t"
      "v_add_f32_e32 v224, v224, v228\n\t"
      "v_add_f32_e32 v225, v225, v229\n\t"
      "v_add_f32_e32 v226, v226, v230\n\t"
      "v_add_f32_e32 v227, v227, v231\n\t"
      "s_waitcnt lgkmcnt(2)\n\t"
      "v_add_f32_e32 v200, v200, v204\n\t"
      "v_add_f32_e32 v201, v201, v205\n\t"
      "v_add_f32_e32 v202, v202, v206\n\t"
      "v_add_f32_e32 v203, v203, v207\n\t"
      "v_add_f32_e32 v216, v216, v224\n\t"
      "v_add_f32_e32 v217, v217, v225\n\t"
      "v_add_f32_e32 v218, v218, v226\n\t"
      "v_add_f32_e32 v219, v219, v227\n\t"
      "s_waitcnt lgkmcnt(0)\n\t"
      "v_add_f32_e32 v208, v208, v212\n\t"
      "s_waitcnt vmcnt(8)\n\t"
      "v_mfma_f32_16x16x32_bf16 v[184:187], v[0:3], v[128:131], 0\n\t"
      "v_add_f32_e32 v209, v209, v213\n\t"
      "v_add_f32_e32 v210, v210, v214\n\t"
      "s_cmp_eq_u32 s17, 0\n\t"
      "s_cbranch_scc0 HI0_%=\n\t"
      "s_mov_b64 exec, 0xff\n\t"
      "global_store_dwordx2 v247, v[232:233], s[12:13] sc1\n\t"
      "s_mov_b64 exec, -1\n\t"
      "s_nop 0\n\t"
      "v_add_u32_e32 v232, 6, v232\n\t"
      "HI0_%=:\n\t"
      "v_mfma_f32_16x16x32_bf16 v[188:191], v[4:7], v[128:131], 0\n\t"
      "v_add_f32_e32 v211, v211, v215\n\t"
      "v_add_f32_e32 v200, v200, v208\n\t"
      "v_mfma_f32_16x16x32_bf16 v[192:195], v[8:11], v[128:131], 0\n\t"
      "v_add_f32_e32 v201, v201, v209\n\t"
      "v_add_f32_e32 v202, v202, v210\n\t"
      "v_mfma_f32_16x16x32_bf16 v[196:199], v[12:15], v[128:131], 0\n\t"
      "global_load_dwordx4 v[128:131], v176, s[58:59] sc1\n\t"
      "v_add_f32_e32 v203, v203, v211\n\t"
      "s_waitcnt vmcnt(8)\n\t"
      "v_mfma_f32_16x16x32_bf16 v[184:187], v[16:19], v[132:135], v[184:187]\n\t"
      "v_add_f32_e32 v216, v216, v200\n\t"
      "v_add_f32_e32 v217, v217, v201\n\t"
      "v_mfma_f32_16x16x32_bf16 v[188:191], v[20:23], v[132:135], v[188:191]\n\t"
      "v_add_f32_e32 v218, v218, v202\n\t"
      "v_add_f32_e32 v219, v219, v203\n\t"
      "v_mfma_f32_16x16x32_bf16 v[192:195], v[24:27], v[132:135], v[192:195]\n\t"
      "v_mul_f32_e32 v220, v216, v216\n\t"
      "v_mul_f32_e32 v221, v217, v217\n\t"
      "v_mfma_f32_16x16x32_bf16 v[196:199], v[28:31], v[132:135], v[196:199]\n\t"
      "global_load_dwordx4 v[132:135], v177, s[58:59] sc1\n\t"
      "v_mul_f32_e32 v222, v218, v218\n\t"
      "s_waitcnt vmcnt(8)\n\t"
      "v_mfma_f32_16x16x32_bf16 v[184:187], v[32:35], v[136:139], v[184:187]\n\t"
      "v_mul_f32_e32 v223, v219, v219\n\t"
      "v_fmaak_f32 v220, v235, v220, 0xc0135761\n\t"
      "v_mfma_f32_16x16x32_bf16 v[188:191], v[36:39], v[136:139], v[188:191]\n\t"
      "v_fmaak_f32 v221, v235, v221, 0xc0135761\n\t"
      "v_fmaak_f32 v222, v235, v222, 0xc0135761\n\t"
      "v_mfma_f32_16x16x32_bf16 v[192:195], v[40:43], v[136:139], v[192:195]\n\t"
      "v_fmaak_f32 v223, v235, v223, 0xc0135761\n\t"
      "v_mul_f32_e32 v220, v216, v220\n\t"
      "v_mfma_f32_16x16x32_bf16 v[196:199], v[44:47], v[136:139], v[196:199]\n\t"
      "global_load_dwordx4 v[136:139], v178, s[58:59] sc1\n\t"
      "v_mul_f32_e32 v221, v217, v221\n\t"
      "s_waitcnt vmcnt(8)\n\t"
      "v_mfma_f32_16x16x32_bf16 v[184:187], v[48:51], v[140:143], v[184:187]\n\t"
      "v_mul_f32_e32 v222, v218, v222\n\t"
      "v_mul_f32_e32 v223, v219, v223\n\t"
      "v_mfma_f32_16x16x32_bf16 v[188:191], v[52:55], v[140:143], v[188:191]\n\t"
      "v_exp_f32_e32 v220, v220\n\t"
      "v_mfma_f32_16x16x32_bf16 v[192:195], v[56:59], v[140:143], v[192:195]\n\t"
      "v_exp_f32_e32 v221, v221\n\t"
      "v_mfma_f32_16x16x32_bf16 v[196:199], v[60:63], v[140:143], v[196:199]\n\t"
      "global_load_dwordx4 v[140:143], v179, s[58:59] sc1\n\t"
      "s_waitcnt vmcnt(8)\n\t"
      "v_mfma_f32_16x16x32_bf16 v[184:187], v[64:67], v[144:147], v[184:187]\n\t"
      "v_exp_f32_e32 v222, v222\n\t"
      "v_mfma_f32_16x16x32_bf16 v[188:191], v[68:71], v[144:147], v[188:191]\n\t"
      "v_exp_f32_e32 v223, v223\n\t"
      "v_mfma_f32_16x16x32_bf16 v[192:195], v[72:75], v[144:147], v[192:195]\n\t"
      "v_add_f32_e32 v220, 1.0, v220\n\t"
      "v_mfma_f32_16x16x32_bf16 v[196:199], v[76:79], v[144:147], v[196:199]\n\t"
      "global_load_dwordx4 v[144:147], v180, s[58:59] sc1\n\t"
      "v_add_f32_e32 v221, 1.0, v221\n\t"
      "s_waitcnt vmcnt(8)\n\t"
      "v_mfma_f32_16x16x32_bf16 v[184:187], v[80:83], v[148:151], v[184:187]\n\t"
      "v_add_f32_e32 v222, 1.0, v222\n\t"
      "v_add_f32_e32 v223, 1.0, v223\n\t"
      "v_mfma_f32_16x16x32_bf16 v[188:191], v[84:87], v[148:151], v[188:191]\n\t"
      "v_rcp_f32_e32 v220, v220\n\t"
      "v_mfma_f32_16x16x32_bf16 v[192:195], v[88:91], v[148:151], v[192:195]\n\t"
      "v_rcp_f32_e32 v221, v221\n\t"
      "v_mfma_f32_16x16x32_bf16 v[196:199], v[92:95], v[148:151], v[196:199]\n\t"
      "global_load_dwordx4 v[148:151], v181, s[58:59] sc1\n\t"
      "s_waitcnt vmcnt(8)\n\t"
      "v_mfma_f32_16x16x32_bf16 v[184:187], v[96:99], v[152:155], v[184:187]\n\t"
      "v_rcp_f32_e32 v222, v222\n\t"
      "v_mfma_f32_16x16x32_bf16 v[188:191], v[100:103], v[152:155], v[188:191]\n\t"
      "v_rcp_f32_e32 v223, v223\n\t"
      "v_mfma_f32_16x16x32_bf16 v[192:195], v[104:107], v[152:155], v[192:195]\n\t"
      "v_mul_f32_e32 v216, v216, v220\n\t"
      "v_mfma_f32_16x16x32_bf16 v[196:199], v[108:111], v[152:155], v[196:199]\n\t"
      "global_load_dwordx4 v[152:155], v182, s[58:59] sc1\n\t"
      "v_mul_f32_e32 v217, v217, v221\n\t"
      "s_waitcnt vmcnt(8)\n\t"
      "v_mfma_f32_16x16x32_bf16 v[184:187], v[112:115], v[156:159], v[184:187]\n\t"
      "v_mul_f32_e32 v218, v218, v222\n\t"
      "v_mul_f32_e32 v219, v219, v223\n\t"
      "v_mfma_f32_16x16x32_bf16 v[188:191], v[116:119], v[156:159], v[188:191]\n\t"
      "v_cvt_pk_bf16_f32 v220, v216, v217\n\t"
      "v_cvt_pk_bf16_f32 v221, v218, v219\n\t"
      "v_mfma_f32_16x16x32_bf16 v[192:195], v[120:123], v[156:159], v[192:195]\n\t"
      "v_min_u32_e32 v220, -2, v220\n\t"
      "v_min_u32_e32 v221, -2, v221\n\t"
      "v_mfma_f32_16x16x32_bf16 v[196:199], v[124:127], v[156:159], v[196:199]\n\t"
      "global_load_dwordx4 v[156:159], v183, s[58:59] sc1\n\t"
      "global_store_dwordx2 v245, v[220:221], s[8:9] sc1\n\t"
      "s_add_u32 s8, s8, 131072\n\t"
      "s_addc_u32 s9, s9, 0\n\t"
      "v_cmp_u_f32_e32 vcc, v184, v184\n\t"
      "s_cbranch_vccnz NP2R_%=\n\t"
      "NP2B_%=:\n\t"
      "ds_write_b128 v243, v[184:187]\n\t"
      "ds_write_b128 v164, v[188:191]\n\t"
      "ds_write_b128 v165, v[192:195]\n\t"
      "ds_write_b128 v166, v[196:199]\n\t"
      "s_waitcnt lgkmcnt(0)\n\t"
      "s_barrier\n\t"
      "ds_read_b128 v[216:219], v244\n\t"
      "ds_read_b128 v[220:223], v244 offset:4096\n\t"
      "ds_read_b128 v[224:227], v244 offset:8192\n\t"
      "ds_read_b128 v[228:231], v244 offset:12288\n\t"
      "ds_read_b128 v[184:187], v244 offset:16384\n\t"
      "ds_read_b128 v[188:191], v244 offset:20480\n\t"
      "ds_read_b128 v[192:195], v244 offset:24576\n\t"
      "ds_read_b128 v[196:199], v244 offset:28672\n\t"
      "s_waitcnt lgkmcnt(6)\n\t"
      "v_add_f32_e32 v216, v216, v220\n\t"
      "v_add_f32_e32 v217, v217, v221\n\t"
      "v_add_f32_e32 v218, v218, v222\n\t"
      "v_add_f32_e32 v219, v219, v223\n\t"
      "s_waitcnt lgkmcnt(4)\n\t"
      "v_add_f32_e32 v224, v224, v228\n\t"
      "v_add_f32_e32 v225, v225, v229\n\t"
      "v_add_f32_e32 v226, v226, v230\n\t"
      "v_add_f32_e32 v227, v227, v231\n\t"
      "s_waitcnt lgkmcnt(2)\n\t"
      "v_add_f32_e32 v184, v184, v188\n\t"
      "v_add_f32_e32 v185, v185, v189\n\t"
      "v_add_f32_e32 v186, v186, v190\n\t"
      "v_add_f32_e32 v187, v187, v191\n\t"
      "v_add_f32_e32 v216, v216, v224\n\t"
      "v_add_f32_e32 v217, v217, v225\n\t"
      "v_add_f32_e32 v218, v218, v226\n\t"
      "v_add_f32_e32 v219, v219, v227\n\t"
      "s_waitcnt lgkmcnt(0)\n\t"
      "v_add_f32_e32 v192, v192, v196\n\t"
      "s_waitcnt vmcnt(8)\n\t"
      "v_mfma_f32_16x16x32_bf16 v[200:203], v[0:3], v[128:131], 0\n\t"
      "v_add_f32_e32 v193, v193, v197\n\t"
      "v_add_f32_e32 v194, v194, v198\n\t"
      "v_mfma_f32_16x16x32_bf16 v[204:207], v[4:7], v[128:131], 0\n\t"
      "v_add_f32_e32 v195, v195, v199\n\t"
      "v_add_f32_e32 v184, v184, v192\n\t"
      "v_mfma_f32_16x16x32_bf16 v[208:211], v[8:11], v[128:131], 0\n\t"
      "v_add_f32_e32 v185, v185, v193\n\t"
      "v_add_f32_e32 v186, v186, v194\n\t"
      "v_mfma_f32_16x16x32_bf16 v[212:215], v[12:15], v[128:131], 0\n\t"
      "global_load_dwordx4 v[128:131], v176, s[60:61] sc1\n\t"
      "v_add_f32_e32 v187, v187, v195\n\t"
      "s_waitcnt vmcnt(8)\n\t"
      "v_mfma_f32_16x16x32_bf16 v[200:203], v[16:19], v[132:135], v[200:203]\n\t"
      "v_add_f32_e32 v216, v216, v184\n\t"
      "v_add_f32_e32 v217, v217, v185\n\t"
      "v_mfma_f32_16x16x32_bf16 v[204:207], v[20:23], v[132:135], v[204:207]\n\t"
      "v_add_f32_e32 v218, v218, v186\n\t"
      "v_add_f32_e32 v219, v219, v187\n\t"
      "v_mfma_f32_16x16x32_bf16 v[208:211], v[24:27], v[132:135], v[208:211]\n\t"
      "v_mul_f32_e32 v220, v216, v216\n\t"
      "v_mul_f32_e32 v221, v217, v217\n\t"
      "v_mfma_f32_16x16x32_bf16 v[212:215], v[28:31], v[132:135], v[212:215]\n\t"
      "global_load_dwordx4 v[132:135], v177, s[60:61] sc1\n\t"
      "v_mul_f32_e32 v222, v218, v218\n\t"
      "s_waitcnt vmcnt(8)\n\t"
      "v_mfma_f32_16x16x32_bf16 v[200:203], v[32:35], v[136:139], v[200:203]\n\t"
      "v_mul_f32_e32 v223, v219, v219\n\t"
      "v_fmaak_f32 v220, v235, v220, 0xc0135761\n\t"
      "v_mfma_f32_16x16x32_bf16 v[204:207], v[36:39], v[136:139], v[204:207]\n\t"
      "v_fmaak_f32 v221, v235, v221, 0xc0135761\n\t"
      "v_fmaak_f32 v222, v235, v222, 0xc0135761\n\t"
      "v_mfma_f32_16x16x32_bf16 v[208:211], v[40:43], v[136:139], v[208:211]\n\t"
      "v_fmaak_f32 v223, v235, v223, 0xc0135761\n\t"
      "v_mul_f32_e32 v220, v216, v220\n\t"
      "v_mfma_f32_16x16x32_bf16 v[212:215], v[44:47], v[136:139], v[212:215]\n\t"
      "global_load_dwordx4 v[136:139], v178, s[60:61] sc1\n\t"
      "v_mul_f32_e32 v221, v217, v221\n\t"
      "s_waitcnt vmcnt(8)\n\t"
      "v_mfma_f32_16x16x32_bf16 v[200:203], v[48:51], v[140:143], v[200:203]\n\t"
      "v_mul_f32_e32 v222, v218, v222\n\t"
      "v_mul_f32_e32 v223, v219, v223\n\t"
      "v_mfma_f32_16x16x32_bf16 v[204:207], v[52:55], v[140:143], v[204:207]\n\t"
      "v_exp_f32_e32 v220, v220\n\t"
      "v_mfma_f32_16x16x32_bf16 v[208:211], v[56:59], v[140:143], v[208:211]\n\t"
      "v_exp_f32_e32 v221, v221\n\t"
      "v_mfma_f32_16x16x32_bf16 v[212:215], v[60:63], v[140:143], v[212:215]\n\t"
      "global_load_dwordx4 v[140:143], v179, s[60:61] sc1\n\t"
      "s_waitcnt vmcnt(8)\n\t"
      "v_mfma_f32_16x16x32_bf16 v[200:203], v[64:67], v[144:147], v[200:203]\n\t"
      "v_exp_f32_e32 v222, v222\n\t"
      "v_mfma_f32_16x16x32_bf16 v[204:207], v[68:71], v[144:147], v[204:207]\n\t"
      "v_exp_f32_e32 v223, v223\n\t"
      "v_mfma_f32_16x16x32_bf16 v[208:211], v[72:75], v[144:147], v[208:211]\n\t"
      "v_add_f32_e32 v220, 1.0, v220\n\t"
      "v_mfma_f32_16x16x32_bf16 v[212:215], v[76:79], v[144:147], v[212:215]\n\t"
      "global_load_dwordx4 v[144:147], v180, s[60:61] sc1\n\t"
      "v_add_f32_e32 v221, 1.0, v221\n\t"
      "s_waitcnt vmcnt(8)\n\t"
      "v_mfma_f32_16x16x32_bf16 v[200:203], v[80:83], v[148:151], v[200:203]\n\t"
      "v_add_f32_e32 v222, 1.0, v222\n\t"
      "v_add_f32_e32 v223, 1.0, v223\n\t"
      "v_mfma_f32_16x16x32_bf16 v[204:207], v[84:87], v[148:151], v[204:207]\n\t"
      "v_rcp_f32_e32 v220, v220\n\t"
      "v_mfma_f32_16x16x32_bf16 v[208:211], v[88:91], v[148:151], v[208:211]\n\t"
      "v_rcp_f32_e32 v221, v221\n\t"
      "v_mfma_f32_16x16x32_bf16 v[212:215], v[92:95], v[148:151], v[212:215]\n\t"
      "global_load_dwordx4 v[148:151], v181, s[60:61] sc1\n\t"
      "s_waitcnt vmcnt(8)\n\t"
      "v_mfma_f32_16x16x32_bf16 v[200:203], v[96:99], v[152:155], v[200:203]\n\t"
      "v_rcp_f32_e32 v222, v222\n\t"
      "v_mfma_f32_16x16x32_bf16 v[204:207], v[100:103], v[152:155], v[204:207]\n\t"
      "v_rcp_f32_e32 v223, v223\n\t"
      "v_mfma_f32_16x16x32_bf16 v[208:211], v[104:107], v[152:155], v[208:211]\n\t"
      "v_mul_f32_e32 v216, v216, v220\n\t"
      "v_mfma_f32_16x16x32_bf16 v[212:215], v[108:111], v[152:155], v[212:215]\n\t"
      "global_load_dwordx4 v[152:155], v182, s[60:61] sc1\n\t"
      "v_mul_f32_e32 v217, v217, v221\n\t"
      "s_waitcnt vmcnt(8)\n\t"
      "v_mfma_f32_16x16x32_bf16 v[200:203], v[112:115], v[156:159], v[200:203]\n\t"
      "v_mul_f32_e32 v218, v218, v222\n\t"
      "v_mul_f32_e32 v219, v219, v223\n\t"
      "v_mfma_f32_16x16x32_bf16 v[204:207], v[116:119], v[156:159], v[204:207]\n\t"
      "v_cvt_pk_bf16_f32 v220, v216, v217\n\t"
      "v_cvt_pk_bf16_f32 v221, v218, v219\n\t"
      "v_mfma_f32_16x16x32_bf16 v[208:211], v[120:123], v[156:159], v[208:211]\n\t"
      "v_min_u32_e32 v220, -2, v220\n\t"
      "v_min_u32_e32 v221, -2, v221\n\t"
      "v_mfma_f32_16x16x32_bf16 v[212:215], v[124:127], v[156:159], v[212:215]\n\t"
      "global_load_dwordx4 v[156:159], v183, s[60:61] sc1\n\t"
      "global_store_dwordx2 v245, v[220:221], s[8:9] sc1\n\t"
      "s_add_u32 s8, s8, 131072\n\t"
      "s_addc_u32 s9, s9, 0\n\t"
      "v_cmp_u_f32_e32 vcc, v200, v200\n\t"
      "s_cbranch_vccnz NP3R_%=\n\t"
      "NP3B_%=:\n\t"
      "ds_write_b128 v243, v[200:203] offset:32768\n\t"
      "ds_write_b128 v164, v[204:207] offset:32768\n\t"
      "ds_write_b128 v165, v[208:211] offset:32768\n\t"
      "ds_write_b128 v166, v[212:215] offset:32768\n\t"
      "s_waitcnt lgkmcnt(0)\n\t"
      "s_barrier\n\t"
      "ds_read_b128 v[216:219], v244 offset:32768\n\t"
      "ds_read_b128 v[220:223], v244 offset:36864\n\t"
      "ds_read_b128 v[224:227], v244 offset:40960\n\t"
      "ds_read_b128 v[228:231], v244 offset:45056\n\t"
      "ds_read_b128 v[200:203], v244 offset:49152\n\t"
      "ds_read_b128 v[204:207], v244 offset:53248\n\t"
      "ds_read_b128 v[208:211], v244 offset:57344\n\t"
      "ds_read_b128 v[212:215], v244 offset:61440\n\t"
      "s_waitcnt lgkmcnt(6)\n\t"
      "v_add_f32_e32 v216, v216, v220\n\t"
      "v_add_f32_e32 v217, v217, v221\n\t"
      "v_add_f32_e32 v218, v218, v222\n\t"
      "v_add_f32_e32 v219, v219, v223\n\t"
      "s_waitcnt lgkmcnt(4)\n\t"
      "v_add_f32_e32 v224, v224, v228\n\t"
      "v_add_f32_e32 v225, v225, v229\n\t"
      "v_add_f32_e32 v226, v226, v230\n\t"
      "v_add_f32_e32 v227, v227, v231\n\t"
      "s_waitcnt lgkmcnt(2)\n\t"
      "v_add_f32_e32 v200, v200, v204\n\t"
      "v_add_f32_e32 v201, v201, v205\n\t"
      "v_add_f32_e32 v202, v202, v206\n\t"
      "v_add_f32_e32 v203, v203, v207\n\t"
      "v_add_f32_e32 v216, v216, v224\n\t"
      "v_add_f32_e32 v217, v217, v225\n\t"
      "v_add_f32_e32 v218, v218, v226\n\t"
      "v_add_f32_e32 v219, v219, v227\n\t"
      "s_waitcnt lgkmcnt(0)\n\t"
      "v_add_f32_e32 v208, v208, v212\n\t"
      "s_waitcnt vmcnt(8)\n\t"
      "v_mfma_f32_16x16x32_bf16 v[184:187], v[0:3], v[128:131], 0\n\t"
      "v_add_f32_e32 v209, v209, v213\n\t"
      "v_add_f32_e32 v210, v210, v214\n\t"
      "v_mfma_f32_16x16x32_bf16 v[188:191], v[4:7], v[128:131], 0\n\t"
      "v_add_f32_e32 v211, v211, v215\n\t"
      "v_add_f32_e32 v200, v200, v208\n\t"
      "v_mfma_f32_16x16x32_bf16 v[192:195], v[8:11], v[128:131], 0\n\t"
      "v_add_f32_e32 v201, v201, v209\n\t"
      "v_add_f32_e32 v202, v202, v210\n\t"
      "v_mfma_f32_16x16x32_bf16 v[196:199], v[12:15], v[128:131], 0\n\t"
      "global_load_dwordx4 v[128:131], v176, s[62:63] sc1\n\t"
      "v_add_f32_e32 v203, v203, v211\n\t"
      "s_waitcnt vmcnt(8)\n\t"
      "v_mfma_f32_16x16x32_bf16 v[184:187], v[16:19], v[132:135], v[184:187]\n\t"
      "v_add_f32_e32 v216, v216, v200\n\t"
      "v_add_f32_e32 v217, v217, v201\n\t"
      "v_mfma_f32_16x16x32_bf16 v[188:191], v[20:23], v[132:135], v[188:191]\n\t"
      "v_add_f32_e32 v218, v218, v202\n\t"
      "v_add_f32_e32 v219, v219, v203\n\t"
      "v_mfma_f32_16x16x32_bf16 v[192:195], v[24:27], v[132:135], v[192:195]\n\t"
      "v_mul_f32_e32 v220, v216, v216\n\t"
      "v_mul_f32_e32 v221, v217, v217\n\t"
      "v_mfma_f32_16x16x32_bf16 v[196:199], v[28:31], v[132:135], v[196:199]\n\t"
      "global_load_dwordx4 v[132:135], v177, s[62:63] sc1\n\t"
      "v_mul_f32_e32 v222, v218, v218\n\t"
      "s_waitcnt vmcnt(8)\n\t"
      "v_mfma_f32_16x16x32_bf16 v[184:187], v[32:35], v[136:139], v[184:187]\n\t"
      "v_mul_f32_e32 v223, v219, v219\n\t"
      "v_fmaak_f32 v220, v235, v220, 0xc0135761\n\t"
      "v_mfma_f32_16x16x32_bf16 v[188:191], v[36:39], v[136:139], v[188:191]\n\t"
      "v_fmaak_f32 v221, v235, v221, 0xc0135761\n\t"
      "v_fmaak_f32 v222, v235, v222, 0xc0135761\n\t"
      "v_mfma_f32_16x16x32_bf16 v[192:195], v[40:43], v[136:139], v[192:195]\n\t"
      "v_fmaak_f32 v223, v235, v223, 0xc0135761\n\t"
      "v_mul_f32_e32 v220, v216, v220\n\t"
      "v_mfma_f32_16x16x32_bf16 v[196:199], v[44:47], v[136:139], v[196:199]\n\t"
      "global_load_dwordx4 v[136:139], v178, s[62:63] sc1\n\t"
      "v_mul_f32_e32 v221, v217, v221\n\t"
      "s_waitcnt vmcnt(8)\n\t"
      "v_mfma_f32_16x16x32_bf16 v[184:187], v[48:51], v[140:143], v[184:187]\n\t"
      "v_mul_f32_e32 v222, v218, v222\n\t"
      "v_mul_f32_e32 v223, v219, v223\n\t"
      "v_mfma_f32_16x16x32_bf16 v[188:191], v[52:55], v[140:143], v[188:191]\n\t"
      "v_exp_f32_e32 v220, v220\n\t"
      "v_mfma_f32_16x16x32_bf16 v[192:195], v[56:59], v[140:143], v[192:195]\n\t"
      "v_exp_f32_e32 v221, v221\n\t"
      "v_mfma_f32_16x16x32_bf16 v[196:199], v[60:63], v[140:143], v[196:199]\n\t"
      "global_load_dwordx4 v[140:143], v179, s[62:63] sc1\n\t"
      "s_waitcnt vmcnt(8)\n\t"
      "v_mfma_f32_16x16x32_bf16 v[184:187], v[64:67], v[144:147], v[184:187]\n\t"
      "v_exp_f32_e32 v222, v222\n\t"
      "v_mfma_f32_16x16x32_bf16 v[188:191], v[68:71], v[144:147], v[188:191]\n\t"
      "v_exp_f32_e32 v223, v223\n\t"
      "v_mfma_f32_16x16x32_bf16 v[192:195], v[72:75], v[144:147], v[192:195]\n\t"
      "v_add_f32_e32 v220, 1.0, v220\n\t"
      "v_mfma_f32_16x16x32_bf16 v[196:199], v[76:79], v[144:147], v[196:199]\n\t"
      "global_load_dwordx4 v[144:147], v180, s[62:63] sc1\n\t"
      "v_add_f32_e32 v221, 1.0, v221\n\t"
      "s_waitcnt vmcnt(8)\n\t"
      "v_mfma_f32_16x16x32_bf16 v[184:187], v[80:83], v[148:151], v[184:187]\n\t"
      "v_add_f32_e32 v222, 1.0, v222\n\t"
      "v_add_f32_e32 v223, 1.0, v223\n\t"
      "v_mfma_f32_16x16x32_bf16 v[188:191], v[84:87], v[148:151], v[188:191]\n\t"
      "v_rcp_f32_e32 v220, v220\n\t"
      "v_mfma_f32_16x16x32_bf16 v[192:195], v[88:91], v[148:151], v[192:195]\n\t"
      "v_rcp_f32_e32 v221, v221\n\t"
      "v_mfma_f32_16x16x32_bf16 v[196:199], v[92:95], v[148:151], v[196:199]\n\t"
      "global_load_dwordx4 v[148:151], v181, s[62:63] sc1\n\t"
      "s_waitcnt vmcnt(8)\n\t"
      "v_mfma_f32_16x16x32_bf16 v[184:187], v[96:99], v[152:155], v[184:187]\n\t"
      "v_rcp_f32_e32 v222, v222\n\t"
      "v_mfma_f32_16x16x32_bf16 v[188:191], v[100:103], v[152:155], v[188:191]\n\t"
      "v_rcp_f32_e32 v223, v223\n\t"
      "v_mfma_f32_16x16x32_bf16 v[192:195], v[104:107], v[152:155], v[192:195]\n\t"
      "v_mul_f32_e32 v216, v216, v220\n\t"
      "v_mfma_f32_16x16x32_bf16 v[196:199], v[108:111], v[152:155], v[196:199]\n\t"
      "global_load_dwordx4 v[152:155], v182, s[62:63] sc1\n\t"
      "v_mul_f32_e32 v217, v217, v221\n\t"
      "s_waitcnt vmcnt(8)\n\t"
      "v_mfma_f32_16x16x32_bf16 v[184:187], v[112:115], v[156:159], v[184:187]\n\t"
      "v_mul_f32_e32 v218, v218, v222\n\t"
      "v_mul_f32_e32 v219, v219, v223\n\t"
      "v_mfma_f32_16x16x32_bf16 v[188:191], v[116:119], v[156:159], v[188:191]\n\t"
      "v_cvt_pk_bf16_f32 v220, v216, v217\n\t"
      "v_cvt_pk_bf16_f32 v221, v218, v219\n\t"
      "v_mfma_f32_16x16x32_bf16 v[192:195], v[120:123], v[156:159], v[192:195]\n\t"
      "v_min_u32_e32 v220, -2, v220\n\t"
      "v_min_u32_e32 v221, -2, v221\n\t"
      "v_mfma_f32_16x16x32_bf16 v[196:199], v[124:127], v[156:159], v[196:199]\n\t"
      "global_load_dwordx4 v[156:159], v183, s[62:63] sc1\n\t"
      "global_store_dwordx2 v245, v[220:221], s[8:9] sc1\n\t"
      "s_add_u32 s8, s8, 131072\n\t"
      "s_addc_u32 s9, s9, 0\n\t"
      "v_cmp_u_f32_e32 vcc, v184, v184\n\t"
      "s_cbranch_vccnz NP4R_%=\n\t"
      "NP4B_%=:\n\t"
      "ds_write_b128 v243, v[184:187]\n\t"
      "ds_write_b128 v164, v[188:191]\n\t"
      "ds_write_b128 v165, v[192:195]\n\t"
      "ds_write_b128 v166, v[196:199]\n\t"
      "s_waitcnt lgkmcnt(0)\n\t"
      "s_barrier\n\t"
      "ds_read_b128 v[216:219], v244\n\t"
      "ds_read_b128 v[220:223], v244 offset:4096\n\t"
      "ds_read_b128 v[224:227], v244 offset:8192\n\t"
      "ds_read_b128 v[228:231], v244 offset:12288\n\t"
      "ds_read_b128 v[184:187], v244 offset:16384\n\t"
      "ds_read_b128 v[188:191], v244 offset:20480\n\t"
      "ds_read_b128 v[192:195], v244 offset:24576\n\t"
      "ds_read_b128 v[196:199], v244 offset:28672\n\t"
      "s_waitcnt lgkmcnt(6)\n\t"
      "v_add_f32_e32 v216, v216, v220\n\t"
      "v_add_f32_e32 v217, v217, v221\n\t"
      "v_add_f32_e32 v218, v218, v222\n\t"
      "v_add_f32_e32 v219, v219, v223\n\t"
      "s_waitcnt lgkmcnt(4)\n\t"
      "v_add_f32_e32 v224, v224, v228\n\t"
      "v_add_f32_e32 v225, v225, v229\n\t"
      "v_add_f32_e32 v226, v226, v230\n\t"
      "v_add_f32_e32 v227, v227, v231\n\t"
      "s_waitcnt lgkmcnt(2)\n\t"
      "v_add_f32_e32 v184, v184, v188\n\t"
      "v_add_f32_e32 v185, v185, v189\n\t"
      "v_add_f32_e32 v186, v186, v190\n\t"
      "v_add_f32_e32 v187, v187, v191\n\t"
      "v_add_f32_e32 v216, v216, v224\n\t"
      "v_add_f32_e32 v217, v217, v225\n\t"
      "v_add_f32_e32 v218, v218, v226\n\t"
      "v_add_f32_e32 v219, v219, v227\n\t"
      "s_waitcnt lgkmcnt(0)\n\t"
      "v_add_f32_e32 v192, v192, v196\n\t"
      "s_waitcnt vmcnt(8)\n\t"
      "v_mfma_f32_16x16x32_bf16 v[200:203], v[0:3], v[128:131], 0\n\t"
      "v_add_f32_e32 v193, v193, v197\n\t"
      "v_add_f32_e32 v194, v194, v198\n\t"
      "v_mfma_f32_16x16x32_bf16 v[204:207], v[4:7], v[128:131], 0\n\t"
      "v_add_f32_e32 v195, v195, v199\n\t"
      "v_add_f32_e32 v184, v184, v192\n\t"
      "v_mfma_f32_16x16x32_bf16 v[208:211], v[8:11], v[128:131], 0\n\t"
      "v_add_f32_e32 v185, v185, v193\n\t"
      "v_add_f32_e32 v186, v186, v194\n\t"
      "v_mfma_f32_16x16x32_bf16 v[212:215], v[12:15], v[128:131], 0\n\t"
      "global_load_dwordx4 v[128:131], v176, s[64:65] sc1\n\t"
      "v_add_f32_e32 v187, v187, v195\n\t"
      "s_waitcnt vmcnt(8)\n\t"
      "v_mfma_f32_16x16x32_bf16 v[200:203], v[16:19], v[132:135], v[200:203]\n\t"
      "v_add_f32_e32 v216, v216, v184\n\t"
      "v_add_f32_e32 v217, v217, v185\n\t"
      "v_mfma_f32_16x16x32_bf16 v[204:207], v[20:23], v[132:135], v[204:207]\n\t"
      "v_add_f32_e32 v218, v218, v186\n\t"
      "v_add_f32_e32 v219, v219, v187\n\t"
      "v_mfma_f32_16x16x32_bf16 v[208:211], v[24:27], v[132:135], v[208:211]\n\t"
      "v_mul_f32_e32 v220, v216, v216\n\t"
      "v_mul_f32_e32 v221, v217, v217\n\t"
      "v_mfma_f32_16x16x32_bf16 v[212:215], v[28:31], v[132:135], v[212:215]\n\t"
      "global_load_dwordx4 v[132:135], v177, s[64:65] sc1\n\t"
      "v_mul_f32_e32 v222, v218, v218\n\t"
      "s_waitcnt vmcnt(8)\n\t"
      "v_mfma_f32_16x16x32_bf16 v[200:203], v[32:35], v[136:139], v[200:203]\n\t"
      "v_mul_f32_e32 v223, v219, v219\n\t"
      "v_fmaak_f32 v220, v235, v220, 0xc0135761\n\t"
      "v_mfma_f32_16x16x32_bf16 v[204:207], v[36:39], v[136:139], v[204:207]\n\t"
      "v_fmaak_f32 v221, v235, v221, 0xc0135761\n\t"
      "v_fmaak_f32 v222, v235, v222, 0xc0135761\n\t"
      "v_mfma_f32_16x16x32_bf16 v[208:211], v[40:43], v[136:139], v[208:211]\n\t"
      "v_fmaak_f32 v223, v235, v223, 0xc0135761\n\t"
      "v_mul_f32_e32 v220, v216, v220\n\t"
      "v_mfma_f32_16x16x32_bf16 v[212:215], v[44:47], v[136:139], v[212:215]\n\t"
      "global_load_dwordx4 v[136:139], v178, s[64:65] sc1\n\t"
      "v_mul_f32_e32 v221, v217, v221\n\t"
      "s_waitcnt vmcnt(8)\n\t"
      "v_mfma_f32_16x16x32_bf16 v[200:203], v[48:51], v[140:143], v[200:203]\n\t"
      "v_mul_f32_e32 v222, v218, v222\n\t"
      "v_mul_f32_e32 v223, v219, v223\n\t"
      "v_mfma_f32_16x16x32_bf16 v[204:207], v[52:55], v[140:143], v[204:207]\n\t"
      "v_exp_f32_e32 v220, v220\n\t"
      "v_mfma_f32_16x16x32_bf16 v[208:211], v[56:59], v[140:143], v[208:211]\n\t"
      "v_exp_f32_e32 v221, v221\n\t"
      "v_mfma_f32_16x16x32_bf16 v[212:215], v[60:63], v[140:143], v[212:215]\n\t"
      "global_load_dwordx4 v[140:143], v179, s[64:65] sc1\n\t"
      "s_waitcnt vmcnt(8)\n\t"
      "v_mfma_f32_16x16x32_bf16 v[200:203], v[64:67], v[144:147], v[200:203]\n\t"
      "v_exp_f32_e32 v222, v222\n\t"
      "v_mfma_f32_16x16x32_bf16 v[204:207], v[68:71], v[144:147], v[204:207]\n\t"
      "v_exp_f32_e32 v223, v223\n\t"
      "v_mfma_f32_16x16x32_bf16 v[208:211], v[72:75], v[144:147], v[208:211]\n\t"
      "v_add_f32_e32 v220, 1.0, v220\n\t"
      "v_mfma_f32_16x16x32_bf16 v[212:215], v[76:79], v[144:147], v[212:215]\n\t"
      "global_load_dwordx4 v[144:147], v180, s[64:65] sc1\n\t"
      "v_add_f32_e32 v221, 1.0, v221\n\t"
      "s_waitcnt vmcnt(8)\n\t"
      "v_mfma_f32_16x16x32_bf16 v[200:203], v[80:83], v[148:151], v[200:203]\n\t"
      "v_add_f32_e32 v222, 1.0, v222\n\t"
      "v_add_f32_e32 v223, 1.0, v223\n\t"
      "v_mfma_f32_16x16x32_bf16 v[204:207], v[84:87], v[148:151], v[204:207]\n\t"
      "v_rcp_f32_e32 v220, v220\n\t"
      "v_mfma_f32_16x16x32_bf16 v[208:211], v[88:91], v[148:151], v[208:211]\n\t"
      "v_rcp_f32_e32 v221, v221\n\t"
      "v_mfma_f32_16x16x32_bf16 v[212:215], v[92:95], v[148:151], v[212:215]\n\t"
      "global_load_dwordx4 v[148:151], v181, s[64:65] sc1\n\t"
      "s_waitcnt vmcnt(8)\n\t"
      "v_mfma_f32_16x16x32_bf16 v[200:203], v[96:99], v[152:155], v[200:203]\n\t"
      "v_rcp_f32_e32 v222, v222\n\t"
      "v_mfma_f32_16x16x32_bf16 v[204:207], v[100:103], v[152:155], v[204:207]\n\t"
      "v_rcp_f32_e32 v223, v223\n\t"
      "v_mfma_f32_16x16x32_bf16 v[208:211], v[104:107], v[152:155], v[208:211]\n\t"
      "v_mul_f32_e32 v216, v216, v220\n\t"
      "v_mfma_f32_16x16x32_bf16 v[212:215], v[108:111], v[152:155], v[212:215]\n\t"
      "global_load_dwordx4 v[152:155], v182, s[64:65] sc1\n\t"
      "v_mul_f32_e32 v217, v217, v221\n\t"
      "s_waitcnt vmcnt(8)\n\t"
      "v_mfma_f32_16x16x32_bf16 v[200:203], v[112:115], v[156:159], v[200:203]\n\t"
      "v_mul_f32_e32 v218, v218, v222\n\t"
      "v_mul_f32_e32 v219, v219, v223\n\t"
      "v_mfma_f32_16x16x32_bf16 v[204:207], v[116:119], v[156:159], v[204:207]\n\t"
      "v_cvt_pk_bf16_f32 v220, v216, v217\n\t"
      "v_cvt_pk_bf16_f32 v221, v218, v219\n\t"
      "v_mfma_f32_16x16x32_bf16 v[208:211], v[120:123], v[156:159], v[208:211]\n\t"
      "v_min_u32_e32 v220, -2, v220\n\t"
      "v_min_u32_e32 v221, -2, v221\n\t"
      "v_mfma_f32_16x16x32_bf16 v[212:215], v[124:127], v[156:159], v[212:215]\n\t"
      "global_load_dwordx4 v[156:159], v183, s[64:65] sc1\n\t"
      "global_store_dwordx2 v245, v[220:221], s[8:9] sc1\n\t"
      "s_add_u32 s8, s8, 131072\n\t"
      "s_addc_u32 s9, s9, 0\n\t"
      "v_cmp_u_f32_e32 vcc, v200, v200\n\t"
      "s_cbranch_vccnz NP5R_%=\n\t"
      "NP5B_%=:\n\t"
      "ds_write_b128 v243, v[200:203] offset:32768\n\t"
      "ds_write_b128 v164, v[204:207] offset:32768\n\t"
      "ds_write_b128 v165, v[208:211] offset:32768\n\t"
      "ds_write_b128 v166, v[212:215] offset:32768\n\t"
      "s_waitcnt lgkmcnt(0)\n\t"
      "s_barrier\n\t"
      "ds_read_b128 v[216:219], v244 offset:32768\n\t"
      "ds_read_b128 v[220:223], v244 offset:36864\n\t"
      "ds_read_b128 v[224:227], v244 offset:40960\n\t"
      "ds_read_b128 v[228:231], v244 offset:45056\n\t"
      "ds_read_b128 v[200:203], v244 offset:49152\n\t"
      "ds_read_b128 v[204:207], v244 offset:53248\n\t"
      "ds_read_b128 v[208:211], v244 offset:57344\n\t"
      "ds_read_b128 v[212:215], v244 offset:61440\n\t"
      "s_waitcnt lgkmcnt(6)\n\t"
      "v_add_f32_e32 v216, v216, v220\n\t"
      "v_add_f32_e32 v217, v217, v221\n\t"
      "v_add_f32_e32 v218, v218, v222\n\t"
      "v_add_f32_e32 v219, v219, v223\n\t"
      "s_waitcnt lgkmcnt(4)\n\t"
      "v_add_f32_e32 v224, v224, v228\n\t"
      "v_add_f32_e32 v225, v225, v229\n\t"
      "s_waitcnt vmcnt(8)\n\t"
      "v_mfma_f32_16x16x32_bf16 v[184:187], v[0:3], v[128:131], 0\n\t"
      "v_add_f32_e32 v226, v226, v230\n\t"
      "v_add_f32_e32 v227, v227, v231\n\t"
      "v_mfma_f32_16x16x32_bf16 v[188:191], v[4:7], v[128:131], 0\n\t"
      "s_waitcnt lgkmcnt(2)\n\t"
      "v_add_f32_e32 v200, v200, v204\n\t"
      "v_add_f32_e32 v201, v201, v205\n\t"
      "v_mfma_f32_16x16x32_bf16 v[192:195], v[8:11], v[128:131], 0\n\t"
      "v_add_f32_e32 v202, v202, v206\n\t"
      "v_add_f32_e32 v203, v203, v207\n\t"
      "v_mfma_f32_16x16x32_bf16 v[196:199], v[12:15], v[128:131], 0\n\t"
      "v_add_f32_e32 v216, v216, v224\n\t"
      "v_add_f32_e32 v217, v217, v225\n\t"
      "s_waitcnt vmcnt(7)\n\t"
      "v_mfma_f32_16x16x32_bf16 v[184:187], v[16:19], v[132:135], v[184:187]\n\t"
      "v_add_f32_e32 v218, v218, v226\n\t"
      "v_add_f32_e32 v219, v219, v227\n\t"
      "v_mfma_f32_16x16x32_bf16 v[188:191], v[20:23], v[132:135], v[188:191]\n\t"
      "s_waitcnt lgkmcnt(0)\n\t"
      "v_add_f32_e32 v208, v208, v212\n\t"
      "v_add_f32_e32 v209, v209, v213\n\t"
      "v_mfma_f32_16x16x32_bf16 v[192:195], v[24:27], v[132:135], v[192:195]\n\t"
      "v_add_f32_e32 v210, v210, v214\n\t"
      "v_add_f32_e32 v211, v211, v215\n\t"
      "v_mfma_f32_16x16x32_bf16 v[196:199], v[28:31], v[132:135], v[196:199]\n\t"
      "v_add_f32_e32 v200, v200, v208\n\t"
      "v_add_f32_e32 v201, v201, v209\n\t"
      "s_waitcnt vmcnt(6)\n\t"
      "v_mfma_f32_16x16x32_bf16 v[184:187], v[32:35], v[136:139], v[184:187]\n\t"
      "v_add_f32_e32 v202, v202, v210\n\t"
      "v_add_f32_e32 v203, v203, v211\n\t"
      "v_mfma_f32_16x16x32_bf16 v[188:191], v[36:39], v[136:139], v[188:191]\n\t"
      "v_add_f32_e32 v216, v216, v200\n\t"
      "v_add_f32_e32 v217, v217, v201\n\t"
      "v_mfma_f32_16x16x32_bf16 v[192:195], v[40:43], v[136:139], v[192:195]\n\t"
      "v_add_f32_e32 v218, v218, v202\n\t"
      "v_add_f32_e32 v219, v219, v203\n\t"
      "v_mfma_f32_16x16x32_bf16 v[196:199], v[44:47], v[136:139], v[196:199]\n\t"
      "v_mul_f32_e32 v220, v216, v216\n\t"
      "v_mul_f32_e32 v221, v217, v217\n\t"
      "s_waitcnt vmcnt(5)\n\t"
      "v_mfma_f32_16x16x32_bf16 v[184:187], v[48:51], v[140:143], v[184:187]\n\t"
      "v_mul_f32_e32 v222, v218, v218\n\t"
      "v_mul_f32_e32 v223, v219, v219\n\t"
      "v_mfma_f32_16x16x32_bf16 v[188:191], v[52:55], v[140:143], v[188:191]\n\t"
      "v_fmaak_f32 v220, v235, v220, 0xc0135761\n\t"
      "v_fmaak_f32 v221, v235, v221, 0xc0135761\n\t"
      "v_mfma_f32_16x16x32_bf16 v[192:195], v[56:59], v[140:143], v[192:195]\n\t"
      "v_fmaak_f32 v222, v235, v222, 0xc0135761\n\t"
      "v_fmaak_f32 v223, v235, v223, 0xc0135761\n\t"
      "v_mfma_f32_16x16x32_bf16 v[196:199], v[60:63], v[140:143], v[196:199]\n\t"
      "v_mul_f32_e32 v220, v216, v220\n\t"
      "v_mul_f32_e32 v221, v217, v221\n\t"
      "s_waitcnt vmcnt(4)\n\t"
      "v_mfma_f32_16x16x32_bf16 v[184:187], v[64:67], v[144:147], v[184:187]\n\t"
      "v_mul_f32_e32 v222, v218, v222\n\t"
      "v_mul_f32_e32 v223, v219, v223\n\t"
      "v_mfma_f32_16x16x32_bf16 v[188:191], v[68:71], v[144:147], v[188:191]\n\t"
      "v_exp_f32_e32 v220, v220\n\t"
      "v_mfma_f32_16x16x32_bf16 v[192:195], v[72:75], v[144:147], v[192:195]\n\t"
      "v_exp_f32_e32 v221, v221\n\t"
      "v_mfma_f32_16x16x32_bf16 v[196:199], v[76:79], v[144:147], v[196:199]\n\t"
      "v_exp_f32_e32 v222, v222\n\t"
      "s_waitcnt vmcnt(3)\n\t"
      "v_mfma_f32_16x16x32_bf16 v[184:187], v[80:83], v[148:151], v[184:187]\n\t"
      "v_exp_f32_e32 v223, v223\n\t"
      "v_mfma_f32_16x16x32_bf16 v[188:191], v[84:87], v[148:151], v[188:191]\n\t"
      "v_add_f32_e32 v220, 1.0, v220\n\t"
      "v_add_f32_e32 v221, 1.0, v221\n\t"
      "v_mfma_f32_16x16x32_bf16 v[192:195], v[88:91], v[148:151], v[192:195]\n\t"
      "v_add_f32_e32 v222, 1.0, v222\n\t"
      "v_add_f32_e32 v223, 1.0, v223\n\t"
      "v_mfma_f32_16x16x32_bf16 v[196:199], v[92:95], v[148:151], v[196:199]\n\t"
      "v_rcp_f32_e32 v220, v220\n\t"
      "s_waitcnt vmcnt(2)\n\t"
      "v_mfma_f32_16x16x32_bf16 v[184:187], v[96:99], v[152:155], v[184:187]\n\t"
      "v_rcp_f32_e32 v221, v221\n\t"
      "v_mfma_f32_16x16x32_bf16 v[188:191], v[100:103], v[152:155], v[188:191]\n\t"
      "v_rcp_f32_e32 v222, v222\n\t"
      "v_mfma_f32_16x16x32_bf16 v[192:195], v[104:107], v[152:155], v[192:195]\n\t"
      "v_rcp_f32_e32 v223, v223\n\t"
      "v_mfma_f32_16x16x32_bf16 v[196:199], v[108:111], v[152:155], v[196:199]\n\t"
      "v_mul_f32_e32 v216, v216, v220\n\t"
      "s_waitcnt vmcnt(1)\n\t"
      "v_mfma_f32_16x16x32_bf16 v[184:187], v[112:115], v[156:159], v[184:187]\n\t"
      "v_mul_f32_e32 v217, v217, v221\n\t"
      "v_mul_f32_e32 v218, v218, v222\n\t"
      "v_mfma_f32_16x16x32_bf16 v[188:191], v[116:119], v[156:159], v[188:191]\n\t"
      "v_mul_f32_e32 v219, v219, v223\n\t"
      "v_cvt_pk_bf16_f32 v220, v216, v217\n\t"
      "v_mfma_f32_16x16x32_bf16 v[192:195], v[120:123], v[156:159], v[192:195]\n\t"
      "v_cvt_pk_bf16_f32 v221, v218, v219\n\t"
      "v_min_u32_e32 v220, -2, v220\n\t"
      "v_mfma_f32_16x16x32_bf16 v[196:199], v[124:127], v[156:159], v[196:199]\n\t"
      "v_min_u32_e32 v221, -2, v221\n\t"
      "global_store_dwordx2 v245, v[220:221], s[8:9] sc1\n\t"
      "s_add_u32 s8, s8, 131072\n\t"
      "s_addc_u32 s9, s9, 0\n\t"
      "v_cmp_u_f32_e32 vcc, v184, v184\n\t"
      "s_cbranch_vccnz NP6R_%=\n\t"
      "NP6B_%=:\n\t"
      "ds_write_b128 v243, v[184:187]\n\t"
      "ds_write_b128 v164, v[188:191]\n\t"
      "ds_write_b128 v165, v[192:195]\n\t"
      "ds_write_b128 v166, v[196:199]\n\t"
      "s_waitcnt lgkmcnt(0)\n\t"
      "s_barrier\n\t"
      "ds_read_b128 v[216:219], v244\n\t"
      "ds_read_b128 v[220:223], v244 offset:4096\n\t"
      "ds_read_b128 v[224:227], v244 offset:8192\n\t"
      "ds_read_b128 v[228:231], v244 offset:12288\n\t"
      "ds_read_b128 v[184:187], v244 offset:16384\n\t"
      "ds_read_b128 v[188:191], v244 offset:20480\n\t"
      "ds_read_b128 v[192:195], v244 offset:24576\n\t"
      "ds_read_b128 v[196:199], v244 offset:28672\n\t"
      "s_waitcnt lgkmcnt(6)\n\t"
      "v_add_f32_e32 v216, v216, v220\n\t"
      "v_add_f32_e32 v217, v217, v221\n\t"
      "v_add_f32_e32 v218, v218, v222\n\t"
      "v_add_f32_e32 v219, v219, v223\n\t"
      "s_waitcnt lgkmcnt(4)\n\t"
      "v_add_f32_e32 v224, v224, v228\n\t"
      "v_add_f32_e32 v225, v225, v229\n\t"
      "v_add_f32_e32 v226, v226, v230\n\t"
      "v_add_f32_e32 v227, v227, v231\n\t"
      "s_waitcnt lgkmcnt(2)\n\t"
      "v_add_f32_e32 v184, v184, v188\n\t"
      "v_add_f32_e32 v185, v185, v189\n\t"
      "v_add_f32_e32 v186, v186, v190\n\t"
      "v_add_f32_e32 v187, v187, v191\n\t"
      "v_add_f32_e32 v216, v216, v224\n\t"
      "v_add_f32_e32 v217, v217, v225\n\t"
      "v_add_f32_e32 v218, v218, v226\n\t"
      "v_add_f32_e32 v219, v219, v227\n\t"
      "s_waitcnt lgkmcnt(0)\n\t"
      "v_add_f32_e32 v192, v192, v196\n\t"
      "v_add_f32_e32 v193, v193, v197\n\t"
      "v_add_f32_e32 v194, v194, v198\n\t"
      "v_add_f32_e32 v195, v195, v199\n\t"
      "v_add_f32_e32 v184, v184, v192\n\t"
      "v_add_f32_e32 v185, v185, v193\n\t"
      "v_add_f32_e32 v186, v186, v194\n\t"
      "v_add_f32_e32 v187, v187, v195\n\t"
      "v_add_f32_e32 v216, v216, v184\n\t"
      "v_add_f32_e32 v217, v217, v185\n\t"
      "v_add_f32_e32 v218, v218, v186\n\t"
      "v_add_f32_e32 v219, v219, v187\n\t"
      "v_mul_f32_e32 v220, v216, v216\n\t"
      "v_mul_f32_e32 v221, v217, v217\n\t"
      "v_mul_f32_e32 v222, v218, v218\n\t"
      "v_mul_f32_e32 v223, v219, v219\n\t"
      "v_fmaak_f32 v220, v235, v220, 0xc0135761\n\t"
      "v_fmaak_f32 v221, v235, v221, 0xc0135761\n\t"
      "v_fmaak_f32 v222, v235, v222, 0xc0135761\n\t"
      "v_fmaak_f32 v223, v235, v223, 0xc0135761\n\t"
      "v_mul_f32_e32 v220, v216, v220\n\t"
      "v_mul_f32_e32 v221, v217, v221\n\t"
      "v_mul_f32_e32 v222, v218, v222\n\t"
      "v_mul_f32_e32 v223, v219, v223\n\t"
      "v_exp_f32_e32 v220, v220\n\t"
      "v_exp_f32_e32 v221, v221\n\t"
      "v_exp_f32_e32 v222, v222\n\t"
      "v_exp_f32_e32 v223, v223\n\t"
      "v_add_f32_e32 v220, 1.0, v220\n\t"
      "v_add_f32_e32 v221, 1.0, v221\n\t"
      "v_add_f32_e32 v222, 1.0, v222\n\t"
      "v_add_f32_e32 v223, 1.0, v223\n\t"
      "v_rcp_f32_e32 v220, v220\n\t"
      "v_rcp_f32_e32 v221, v221\n\t"
      "v_rcp_f32_e32 v222, v222\n\t"
      "v_rcp_f32_e32 v223, v223\n\t"
      "v_mul_f32_e32 v216, v216, v220\n\t"
      "v_mul_f32_e32 v217, v217, v221\n\t"
      "v_mul_f32_e32 v218, v218, v222\n\t"
      "v_mul_f32_e32 v219, v219, v223\n\t"
      "v_cvt_pk_bf16_f32 v220, v216, v217\n\t"
      "v_cvt_pk_bf16_f32 v221, v218, v219\n\t"
      "v_min_u32_e32 v220, -2, v220\n\t"
      "v_min_u32_e32 v221, -2, v221\n\t"
      "s_cmp_eq_u32 s17, 0\n\t"
      "s_cbranch_scc0 HI6_%=\n\t"
      "s_mov_b64 exec, 0xff\n\t"
      "global_store_dwordx2 v247, v[232:233], s[12:13] sc1\n\t"
      "s_mov_b64 exec, -1\n\t"
      "HI6_%=:\n\t"
      "global_store_dwordx2 v245, v[220:221], s[8:9] sc1\n\t"
      "GX_%=:\n\t"
      "AEND_%=:\n\t"
      "s_endpgm\n\t"
      ".p2align 6\n\t"
      "GQ_%=:\n\t"
      "s_waitcnt vmcnt(7)\n\t"
      "v_max3_u32 v234, v128, v129, v130\n\t"
      "v_max_u32_e32 v234, v234, v131\n\t"
      "v_cmp_eq_u32_e32 vcc, -1, v234\n\t"
      "s_cbranch_vccnz FQ0R_%=\n\t"
      "FQ0B_%=:\n\t"
      "v_mfma_f32_16x16x32_bf16 v[184:187], v[0:3], v[128:131], v[236:239]\n\t"
      "v_mfma_f32_16x16x32_bf16 v[188:191], v[4:7], v[128:131], 0\n\t"
      "v_mfma_f32_16x16x32_bf16 v[192:195], v[8:11], v[128:131], 0\n\t"
      "v_mfma_f32_16x16x32_bf16 v[196:199], v[12:15], v[128:131], 0\n\t"
      "global_load_dwordx4 v[128:131], v176, s[54:55] sc1\n\t"
      "s_waitcnt vmcnt(7)\n\t"
      "v_max3_u32 v234, v132, v133, v134\n\t"
      "v_max_u32_e32 v234, v234, v135\n\t"
      "v_cmp_eq_u32_e32 vcc, -1, v234\n\t"
      "s_cbranch_vccnz FQ1R_%=\n\t"
      "FQ1B_%=:\n\t"
      "v_mfma_f32_16x16x32_bf16 v[184:187], v[16:19], v[132:135], v[184:187]\n\t"
      "v_mfma_f32_16x16x32_bf16 v[188:191], v[20:23], v[132:135], v[188:191]\n\t"
      "v_mfma_f32_16x16x32_bf16 v[192:195], v[24:27], v[132:135], v[192:195]\n\t"
      "v_mfma_f32_16x16x32_bf16 v[196:199], v[28:31], v[132:135], v[196:199]\n\t"
      "global_load_dwordx4 v[132:135], v177, s[54:55] sc1\n\t"
      "s_waitcnt vmcnt(7)\n\t"
      "v_max3_u32 v234, v136, v137, v138\n\t"
      "v_max_u32_e32 v234, v234, v139\n\t"
      "v_cmp_eq_u32_e32 vcc, -1, v234\n\t"
      "s_cbranch_vccnz FQ2R_%=\n\t"
      "FQ2B_%=:\n\t"
      "v_mfma_f32_16x16x32_bf16 v[184:187], v[32:35], v[136:139], v[184:187]\n\t"
      "v_mfma_f32_16x16x32_bf16 v[188:191], v[36:39], v[136:139], v[188:191]\n\t"
      "v_mfma_f32_16x16x32_bf16 v[192:195], v[40:43], v[136:139], v[192:195]\n\t"
      "v_mfma_f32_16x16x32_bf16 v[196:199], v[44:47], v[136:139], v[196:199]\n\t"
      "global_load_dwordx4 v[136:139], v178, s[54:55] sc1\n\t"
      "s_waitcnt vmcnt(7)\n\t"
      "v_max3_u32 v234, v140, v141, v142\n\t"
      "v_max_u32_e32 v234, v234, v143\n\t"
      "v_cmp_eq_u32_e32 vcc, -1, v234\n\t"
      "s_cbranch_vccnz FQ3R_%=\n\t"
      "FQ3B_%=:\n\t"
      "v_mfma_f32_16x16x32_bf16 v[184:187], v[48:51], v[140:143], v[184:187]\n\t"
      "v_mfma_f32_16x16x32_bf16 v[188:191], v[52:55], v[140:143], v[188:191]\n\t"
      "v_mfma_f32_16x16x32_bf16 v[192:195], v[56:59], v[140:143], v[192:195]\n\t"
      "v_mfma_f32_16x16x32_bf16 v[196:199], v[60:63], v[140:143], v[196:199]\n\t"
      "global_load_dwordx4 v[140:143], v179, s[54:55] sc1\n\t"
      "s_waitcnt vmcnt(7)\n\t"
      "v_max3_u32 v234, v144, v145, v146\n\t"
      "v_max_u32_e32 v234, v234, v147\n\t"
      "v_cmp_eq_u32_e32 vcc, -1, v234\n\t"
      "s_cbranch_vccnz FQ4R_%=\n\t"
      "FQ4B_%=:\n\t"
      "v_mfma_f32_16x16x32_bf16 v[184:187], v[64:67], v[144:147], v[184:187]\n\t"
      "v_mfma_f32_16x16x32_bf16 v[188:191], v[68:71], v[144:147], v[188:191]\n\t"
      "v_mfma_f32_16x16x32_bf16 v[192:195], v[72:75], v[144:147], v[192:195]\n\t"
      "v_mfma_f32_16x16x32_bf16 v[196:199], v[76:79], v[144:147], v[196:199]\n\t"
      "global_load_dwordx4 v[144:147], v180, s[54:55] sc1\n\t"
      "s_waitcnt vmcnt(7)\n\t"
      "v_max3_u32 v234, v148, v149, v150\n\t"
      "v_max_u32_e32 v234, v234, v151\n\t"
      "v_cmp_eq_u32_e32 vcc, -1, v234\n\t"
      "s_cbranch_vccnz FQ5R_%=\n\t"
      "FQ5B_%=:\n\t"
      "v_mfma_f32_16x16x32_bf16 v[184:187], v[80:83], v[148:151], v[184:187]\n\t"
      "v_mfma_f32_16x16x32_bf16 v[188:191], v[84:87], v[148:151], v[188:191]\n\t"
      "v_mfma_f32_16x16x32_bf16 v[192:195], v[88:91], v[148:151], v[192:195]\n\t"
      "v_mfma_f32_16x16x32_bf16 v[196:199], v[92:95], v[148:151], v[196:199]\n\t"
      "global_load_dwordx4 v[148:151], v181, s[54:55] sc1\n\t"
      "s_waitcnt vmcnt(7)\n\t"
      "v_max3_u32 v234, v152, v153, v154\n\t"
      "v_max_u32_e32 v234, v234, v155\n\t"
      "v_cmp_eq_u32_e32 vcc, -1, v234\n\t"
      "s_cbranch_vccnz FQ6R_%=\n\t"
      "FQ6B_%=:\n\t"
      "v_mfma_f32_16x16x32_bf16 v[184:187], v[96:99], v[152:155], v[184:187]\n\t"
      "v_mfma_f32_16x16x32_bf16 v[188:191], v[100:103], v[152:155], v[188:191]\n\t"
      "v_mfma_f32_16x16x32_bf16 v[192:195], v[104:107], v[152:155], v[192:195]\n\t"
      "v_mfma_f32_16x16x32_bf16 v[196:199], v[108:111], v[152:155], v[196:199]\n\t"
      "global_load_dwordx4 v[152:155], v182, s[54:55] sc1\n\t"
      "s_waitcnt vmcnt(7)\n\t"
      "v_max3_u32 v234, v156, v157, v158\n\t"
      "v_max_u32_e32 v234, v234, v159\n\t"
      "v_cmp_eq_u32_e32 vcc, -1, v234\n\t"
      "s_cbranch_vccnz FQ7R_%=\n\t"
      "FQ7B_%=:\n\t"
      "v_mfma_f32_16x16x32_bf16 v[184:187], v[112:115], v[156:159], v[184:187]\n\t"
      "v_mfma_f32_16x16x32_bf16 v[188:191], v[116:119], v[156:159], v[188:191]\n\t"
      "v_mfma_f32_16x16x32_bf16 v[192:195], v[120:123], v[156:159], v[192:195]\n\t"
      "v_mfma_f32_16x16x32_bf16 v[196:199], v[124:127], v[156:159], v[196:199]\n\t"
      "global_load_dwordx4 v[156:159], v183, s[54:55] sc1\n\t"
      "s_nop 3\n\t"
      "ds_write_b128 v243, v[184:187]\n\t"
      "ds_write_b128 v164, v[188:191]\n\t"
      "ds_write_b128 v165, v[192:195]\n\t"
      "ds_write_b128 v166, v[196:199]\n\t"
      "s_waitcnt vmcnt(7)\n\t"
      "v_mfma_f32_16x16x32_bf16 v[200:203], v[0:3], v[128:131], v[236:239]\n\t"
      "v_mfma_f32_16x16x32_bf16 v[204:207], v[4:7], v[128:131], 0\n\t"
      "v_mfma_f32_16x16x32_bf16 v[208:211], v[8:11], v[128:131], 0\n\t"
      "v_mfma_f32_16x16x32_bf16 v[212:215], v[12:15], v[128:131], 0\n\t"
      "global_load_dwordx4 v[128:131], v176, s[56:57] sc1\n\t"
      "s_waitcnt vmcnt(7)\n\t"
      "v_mfma_f32_16x16x32_bf16 v[200:203], v[16:19], v[132:135], v[200:203]\n\t"
      "v_mfma_f32_16x16x32_bf16 v[204:207], v[20:23], v[132:135], v[204:207]\n\t"
      "v_mfma_f32_16x16x32_bf16 v[208:211], v[24:27], v[132:135], v[208:211]\n\t"
      "v_mfma_f32_16x16x32_bf16 v[212:215], v[28:31], v[132:135], v[212:215]\n\t"
      "global_load_dwordx4 v[132:135], v177, s[56:57] sc1\n\t"
      "s_waitcnt vmcnt(7)\n\t"
      "v_mfma_f32_16x16x32_bf16 v[200:203], v[32:35], v[136:139], v[200:203]\n\t"
      "v_mfma_f32_16x16x32_bf16 v[204:207], v[36:39], v[136:139], v[204:207]\n\t"
      "v_mfma_f32_16x16x32_bf16 v[208:211], v[40:43], v[136:139], v[208:211]\n\t"
      "v_mfma_f32_16x16x32_bf16 v[212:215], v[44:47], v[136:139], v[212:215]\n\t"
      "global_load_dwordx4 v[136:139], v178, s[56:57] sc1\n\t"
      "s_waitcnt vmcnt(7)\n\t"
      "v_mfma_f32_16x16x32_bf16 v[200:203], v[48:51], v[140:143], v[200:203]\n\t"
      "v_mfma_f32_16x16x32_bf16 v[204:207], v[52:55], v[140:143], v[204:207]\n\t"
      "v_mfma_f32_16x16x32_bf16 v[208:211], v[56:59], v[140:143], v[208:211]\n\t"
      "v_mfma_f32_16x16x32_bf16 v[212:215], v[60:63], v[140:143], v[212:215]\n\t"
      "global_load_dwordx4 v[140:143], v179, s[56:57] sc1\n\t"
      "s_waitcnt vmcnt(7)\n\t"
      "v_mfma_f32_16x16x32_bf16 v[200:203], v[64:67], v[144:147], v[200:203]\n\t"
      "v_mfma_f32_16x16x32_bf16 v[204:207], v[68:71], v[144:147], v[204:207]\n\t"
      "v_mfma_f32_16x16x32_bf16 v[208:211], v[72:75], v[144:147], v[208:211]\n\t"
      "v_mfma_f32_16x16x32_bf16 v[212:215], v[76:79], v[144:147], v[212:215]\n\t"
      "global_load_dwordx4 v[144:147], v180, s[56:57] sc1\n\t"
      "s_waitcnt vmcnt(7)\n\t"
      "v_mfma_f32_16x16x32_bf16 v[200:203], v[80:83], v[148:151], v[200:203]\n\t"
      "v_mfma_f32_16x16x32_bf16 v[204:207], v[84:87], v[148:151], v[204:207]\n\t"
      "v_mfma_f32_16x16x32_bf16 v[208:211], v[88:91], v[148:151], v[208:211]\n\t"
      "v_mfma_f32_16x16x32_bf16 v[212:215], v[92:95], v[148:151], v[212:215]\n\t"
      "global_load_dwordx4 v[148:151], v181, s[56:57] sc1\n\t"
      "s_waitcnt lgkmcnt(0)\n\t"
      "s_barrier\n\t"
      "s_setprio 2\n\t"
      "s_waitcnt vmcnt(7)\n\t"
      "v_mfma_f32_16x16x32_bf16 v[200:203], v[96:99], v[152:155], v[200:203]\n\t"
      "v_mfma_f32_16x16x32_bf16 v[204:207], v[100:103], v[152:155], v[204:207]\n\t"
      "v_mfma_f32_16x16x32_bf16 v[208:211], v[104:107], v[152:155], v[208:211]\n\t"
      "v_mfma_f32_16x16x32_bf16 v[212:215], v[108:111], v[152:155], v[212:215]\n\t"
      "global_load_dwordx4 v[152:155], v182, s[56:57] sc1\n\t"
      "s_waitcnt vmcnt(7)\n\t"
      "v_mfma_f32_16x16x32_bf16 v[200:203], v[112:115], v[156:159], v[200:203]\n\t"
      "v_mfma_f32_16x16x32_bf16 v[204:207], v[116:119], v[156:159], v[204:207]\n\t"
      "v_mfma_f32_16x16x32_bf16 v[208:211], v[120:123], v[156:159], v[208:211]\n\t"
      "v_mfma_f32_16x16x32_bf16 v[212:215], v[124:127], v[156:159], v[212:215]\n\t"
      "global_load_dwordx4 v[156:159], v183, s[56:57] sc1\n\t"
      "s_setprio 0\n\t"
      "s_nop 2\n\t"
      "v_cmp_u_f32_e32 vcc, v200, v200\n\t"
      "s_cbranch_vccnz NQ1R_%=\n\t"
      "NQ1B_%=:\n\t"
      "ds_write_b128 v243, v[200:203] offset:32768\n\t"
      "ds_write_b128 v164, v[204:207] offset:32768\n\t"
      "ds_write_b128 v165, v[208:211] offset:32768\n\t"
      "ds_write_b128 v166, v[212:215] offset:32768\n\t"
      "s_waitcnt vmcnt(7)\n\t"
      "v_mfma_f32_16x16x32_bf16 v[184:187], v[0:3], v[128:131], v[236:239]\n\t"
      "v_mfma_f32_16x16x32_bf16 v[188:191], v[4:7], v[128:131], 0\n\t"
      "v_mfma_f32_16x16x32_bf16 v[192:195], v[8:11], v[128:131], 0\n\t"
      "v_mfma_f32_16x16x32_bf16 v[196:199], v[12:15], v[128:131], 0\n\t"
      "global_load_dwordx4 v[128:131], v176, s[58:59] sc1\n\t"
      "s_waitcnt vmcnt(7)\n\t"
      "v_mfma_f32_16x16x32_bf16 v[184:187], v[16:19], v[132:135], v[184:187]\n\t"
      "v_mfma_f32_16x16x32_bf16 v[188:191], v[20:23], v[132:135], v[188:191]\n\t"
      "v_mfma_f32_16x16x32_bf16 v[192:195], v[24:27], v[132:135], v[192:195]\n\t"
      "v_mfma_f32_16x16x32_bf16 v[196:199], v[28:31], v[132:135], v[196:199]\n\t"
      "global_load_dwordx4 v[132:135], v177, s[58:59] sc1\n\t"
      "s_waitcnt vmcnt(7)\n\t"
      "v_mfma_f32_16x16x32_bf16 v[184:187], v[32:35], v[136:139], v[184:187]\n\t"
      "v_mfma_f32_16x16x32_bf16 v[188:191], v[36:39], v[136:139], v[188:191]\n\t"
      "v_mfma_f32_16x16x32_bf16 v[192:195], v[40:43], v[136:139], v[192:195]\n\t"
      "v_mfma_f32_16x16x32_bf16 v[196:199], v[44:47], v[136:139], v[196:199]\n\t"
      "global_load_dwordx4 v[136:139], v178, s[58:59] sc1\n\t"
      "s_waitcnt vmcnt(7)\n\t"
      "v_mfma_f32_16x16x32_bf16 v[184:187], v[48:51], v[140:143], v[184:187]\n\t"
      "v_mfma_f32_16x16x32_bf16 v[188:191], v[52:55], v[140:143], v[188:191]\n\t"
      "v_mfma_f32_16x16x32_bf16 v[192:195], v[56:59], v[140:143], v[192:195]\n\t"
      "v_mfma_f32_16x16x32_bf16 v[196:199], v[60:63], v[140:143], v[196:199]\n\t"
      "global_load_dwordx4 v[140:143], v179, s[58:59] sc1\n\t"
      "s_waitcnt vmcnt(7)\n\t"
      "v_mfma_f32_16x16x32_bf16 v[184:187], v[64:67], v[144:147], v[184:187]\n\t"
      "v_mfma_f32_16x16x32_bf16 v[188:191], v[68:71], v[144:147], v[188:191]\n\t"
      "v_mfma_f32_16x16x32_bf16 v[192:195], v[72:75], v[144:147], v[192:195]\n\t"
      "v_mfma_f32_16x16x32_bf16 v[196:199], v[76:79], v[144:147], v[196:199]\n\t"
      "global_load_dwordx4 v[144:147], v180, s[58:59] sc1\n\t"
      "s_waitcnt vmcnt(7)\n\t"
      "v_mfma_f32_16x16x32_bf16 v[184:187], v[80:83], v[148:151], v[184:187]\n\t"
      "v_mfma_f32_16x16x32_bf16 v[188:191], v[84:87], v[148:151], v[188:191]\n\t"
      "v_mfma_f32_16x16x32_bf16 v[192:195], v[88:91], v[148:151], v[192:195]\n\t"
      "v_mfma_f32_16x16x32_bf16 v[196:199], v[92:95], v[148:151], v[196:199]\n\t"
      "global_load_dwordx4 v[148:151], v181, s[58:59] sc1\n\t"
      "s_waitcnt lgkmcnt(0)\n\t"
      "s_barrier\n\t"
      "s_setprio 2\n\t"
      "s_waitcnt vmcnt(7)\n\t"
      "v_mfma_f32_16x16x32_bf16 v[184:187], v[96:99], v[152:155], v[184:187]\n\t"
      "v_mfma_f32_16x16x32_bf16 v[188:191], v[100:103], v[152:155], v[188:191]\n\t"
      "v_mfma_f32_16x16x32_bf16 v[192:195], v[104:107], v[152:155], v[192:195]\n\t"
      "v_mfma_f32_16x16x32_bf16 v[196:199], v[108:111], v[152:155], v[196:199]\n\t"
      "global_load_dwordx4 v[152:155], v182, s[58:59] sc1\n\t"
      "s_waitcnt vmcnt(7)\n\t"
      "v_mfma_f32_16x16x32_bf16 v[184:187], v[112:115], v[156:159], v[184:187]\n\t"
      "v_mfma_f32_16x16x32_bf16 v[188:191], v[116:119], v[156:159], v[188:191]\n\t"
      "v_mfma_f32_16x16x32_bf16 v[192:195], v[120:123], v[156:159], v[192:195]\n\t"
      "v_mfma_f32_16x16x32_bf16 v[196:199], v[124:127], v[156:159], v[196:199]\n\t"
      "global_load_dwordx4 v[156:159], v183, s[58:59] sc1\n\t"
      "s_setprio 0\n\t"
      "s_nop 2\n\t"
      "v_cmp_u_f32_e32 vcc, v184, v184\n\t"
      "s_cbranch_vccnz NQ2R_%=\n\t"
      "NQ2B_%=:\n\t"
      "ds_write_b128 v243, v[184:187]\n\t"
      "ds_write_b128 v164, v[188:191]\n\t"
      "ds_write_b128 v165, v[192:195]\n\t"
      "ds_write_b128 v166, v[196:199]\n\t"
      "s_waitcnt vmcnt(7)\n\t"
      "v_mfma_f32_16x16x32_bf16 v[200:203], v[0:3], v[128:131], v[236:239]\n\t"
      "v_mfma_f32_16x16x32_bf16 v[204:207], v[4:7], v[128:131], 0\n\t"
      "v_mfma_f32_16x16x32_bf16 v[208:211], v[8:11], v[128:131], 0\n\t"
      "v_mfma_f32_16x16x32_bf16 v[212:215], v[12:15], v[128:131], 0\n\t"
      "global_load_dwordx4 v[128:131], v176, s[60:61] sc1\n\t"
      "s_waitcnt vmcnt(7)\n\t"
      "v_mfma_f32_16x16x32_bf16 v[200:203], v[16:19], v[132:135], v[200:203]\n\t"
      "v_mfma_f32_16x16x32_bf16 v[204:207], v[20:23], v[132:135], v[204:207]\n\t"
      "v_mfma_f32_16x16x32_bf16 v[208:211], v[24:27], v[132:135], v[208:211]\n\t"
      "v_mfma_f32_16x16x32_bf16 v[212:215], v[28:31], v[132:135], v[212:215]\n\t"
      "global_load_dwordx4 v[132:135], v177, s[60:61] sc1\n\t"
      "s_waitcnt vmcnt(7)\n\t"
      "v_mfma_f32_16x16x32_bf16 v[200:203], v[32:35], v[136:139], v[200:203]\n\t"
      "v_mfma_f32_16x16x32_bf16 v[204:207], v[36:39], v[136:139], v[204:207]\n\t"
      "v_mfma_f32_16x16x32_bf16 v[208:211], v[40:43], v[136:139], v[208:211]\n\t"
      "v_mfma_f32_16x16x32_bf16 v[212:215], v[44:47], v[136:139], v[212:215]\n\t"
      "global_load_dwordx4 v[136:139], v178, s[60:61] sc1\n\t"
      "s_waitcnt vmcnt(7)\n\t"
      "v_mfma_f32_16x16x32_bf16 v[200:203], v[48:51], v[140:143], v[200:203]\n\t"
      "v_mfma_f32_16x16x32_bf16 v[204:207], v[52:55], v[140:143], v[204:207]\n\t"
      "v_mfma_f32_16x16x32_bf16 v[208:211], v[56:59], v[140:143], v[208:211]\n\t"
      "v_mfma_f32_16x16x32_bf16 v[212:215], v[60:63], v[140:143], v[212:215]\n\t"
      "global_load_dwordx4 v[140:143], v179, s[60:61] sc1\n\t"
      "s_waitcnt vmcnt(7)\n\t"
      "v_mfma_f32_16x16x32_bf16 v[200:203], v[64:67], v[144:147], v[200:203]\n\t"
      "v_mfma_f32_16x16x32_bf16 v[204:207], v[68:71], v[144:147], v[204:207]\n\t"
      "v_mfma_f32_16x16x32_bf16 v[208:211], v[72:75], v[144:147], v[208:211]\n\t"
      "v_mfma_f32_16x16x32_bf16 v[212:215], v[76:79], v[144:147], v[212:215]\n\t"
      "global_load_dwordx4 v[144:147], v180, s[60:61] sc1\n\t"
      "s_waitcnt vmcnt(7)\n\t"
      "v_mfma_f32_16x16x32_bf16 v[200:203], v[80:83], v[148:151], v[200:203]\n\t"
      "v_mfma_f32_16x16x32_bf16 v[204:207], v[84:87], v[148:151], v[204:207]\n\t"
      "v_mfma_f32_16x16x32_bf16 v[208:211], v[88:91], v[148:151], v[208:211]\n\t"
      "v_mfma_f32_16x16x32_bf16 v[212:215], v[92:95], v[148:151], v[212:215]\n\t"
      "global_load_dwordx4 v[148:151], v181, s[60:61] sc1\n\t"
      "s_waitcnt lgkmcnt(0)\n\t"
      "s_barrier\n\t"
      "s_setprio 2\n\t"
      "s_waitcnt vmcnt(7)\n\t"
      "v_mfma_f32_16x16x32_bf16 v[200:203], v[96:99], v[152:155], v[200:203]\n\t"
      "v_mfma_f32_16x16x32_bf16 v[204:207], v[100:103], v[152:155], v[204:207]\n\t"
      "v_mfma_f32_16x16x32_bf16 v[208:211], v[104:107], v[152:155], v[208:211]\n\t"
      "v_mfma_f32_16x16x32_bf16 v[212:215], v[108:111], v[152:155], v[212:215]\n\t"
      "global_load_dwordx4 v[152:155], v182, s[60:61] sc1\n\t"
      "s_waitcnt vmcnt(7)\n\t"
      "v_mfma_f32_16x16x32_bf16 v[200:203], v[112:115], v[156:159], v[200:203]\n\t"
      "v_mfma_f32_16x16x32_bf16 v[204:207], v[116:119], v[156:159], v[204:207]\n\t"
      "v_mfma_f32_16x16x32_bf16 v[208:211], v[120:123], v[156:159], v[208:211]\n\t"
      "v_mfma_f32_16x16x32_bf16 v[212:215], v[124:127], v[156:159], v[212:215]\n\t"
      "global_load_dwordx4 v[156:159], v183, s[60:61] sc1\n\t"
      "s_setprio 0\n\t"
      "s_nop 2\n\t"
      "v_cmp_u_f32_e32 vcc, v200, v200\n\t"
      "s_cbranch_vccnz NQ3R_%=\n\t"
      "NQ3B_%=:\n\t"
      "ds_write_b128 v243, v[200:203] offset:32768\n\t"
      "ds_write_b128 v164, v[204:207] offset:32768\n\t"
      "ds_write_b128 v165, v[208:211] offset:32768\n\t"
      "ds_write_b128 v166, v[212:215] offset:32768\n\t"
      "s_waitcnt vmcnt(7)\n\t"
      "v_mfma_f32_16x16x32_bf16 v[184:187], v[0:3], v[128:131], v[236:239]\n\t"
      "v_mfma_f32_16x16x32_bf16 v[188:191], v[4:7], v[128:131], 0\n\t"
      "v_mfma_f32_16x16x32_bf16 v[192:195], v[8:11], v[128:131], 0\n\t"
      "v_mfma_f32_16x16x32_bf16 v[196:199], v[12:15], v[128:131], 0\n\t"
      "global_load_dwordx4 v[128:131], v176, s[62:63] sc1\n\t"
      "s_waitcnt vmcnt(7)\n\t"
      "v_mfma_f32_16x16x32_bf16 v[184:187], v[16:19], v[132:135], v[184:187]\n\t"
      "v_mfma_f32_16x16x32_bf16 v[188:191], v[20:23], v[132:135], v[188:191]\n\t"
      "v_mfma_f32_16x16x32_bf16 v[192:195], v[24:27], v[132:135], v[192:195]\n\t"
      "v_mfma_f32_16x16x32_bf16 v[196:199], v[28:31], v[132:135], v[196:199]\n\t"
      "global_load_dwordx4 v[132:135], v177, s[62:63] sc1\n\t"
      "s_waitcnt vmcnt(7)\n\t"
      "v_mfma_f32_16x16x32_bf16 v[184:187], v[32:35], v[136:139], v[184:187]\n\t"
      "v_mfma_f32_16x16x32_bf16 v[188:191], v[36:39], v[136:139], v[188:191]\n\t"
      "v_mfma_f32_16x16x32_bf16 v[192:195], v[40:43], v[136:139], v[192:195]\n\t"
      "v_mfma_f32_16x16x32_bf16 v[196:199], v[44:47], v[136:139], v[196:199]\n\t"
      "global_load_dwordx4 v[136:139], v178, s[62:63] sc1\n\t"
      "s_waitcnt vmcnt(7)\n\t"
      "v_mfma_f32_16x16x32_bf16 v[184:187], v[48:51], v[140:143], v[184:187]\n\t"
      "v_mfma_f32_16x16x32_bf16 v[188:191], v[52:55], v[140:143], v[188:191]\n\t"
      "v_mfma_f32_16x16x32_bf16 v[192:195], v[56:59], v[140:143], v[192:195]\n\t"
      "v_mfma_f32_16x16x32_bf16 v[196:199], v[60:63], v[140:143], v[196:199]\n\t"
      "global_load_dwordx4 v[140:143], v179, s[62:63] sc1\n\t"
      "s_waitcnt vmcnt(7)\n\t"
      "v_mfma_f32_16x16x32_bf16 v[184:187], v[64:67], v[144:147], v[184:187]\n\t"
      "v_mfma_f32_16x16x32_bf16 v[188:191], v[68:71], v[144:147], v[188:191]\n\t"
      "v_mfma_f32_16x16x32_bf16 v[192:195], v[72:75], v[144:147], v[192:195]\n\t"
      "v_mfma_f32_16x16x32_bf16 v[196:199], v[76:79], v[144:147], v[196:199]\n\t"
      "global_load_dwordx4 v[144:147], v180, s[62:63] sc1\n\t"
      "s_waitcnt vmcnt(7)\n\t"
      "v_mfma_f32_16x16x32_bf16 v[184:187], v[80:83], v[148:151], v[184:187]\n\t"
      "v_mfma_f32_16x16x32_bf16 v[188:191], v[84:87], v[148:151], v[188:191]\n\t"
      "v_mfma_f32_16x16x32_bf16 v[192:195], v[88:91], v[148:151], v[192:195]\n\t"
      "v_mfma_f32_16x16x32_bf16 v[196:199], v[92:95], v[148:151], v[196:199]\n\t"
      "global_load_dwordx4 v[148:151], v181, s[62:63] sc1\n\t"
      "s_waitcnt lgkmcnt(0)\n\t"
      "s_barrier\n\t"
      "s_setprio 2\n\t"
      "s_waitcnt vmcnt(7)\n\t"
      "v_mfma_f32_16x16x32_bf16 v[184:187], v[96:99], v[152:155], v[184:187]\n\t"
      "v_mfma_f32_16x16x32_bf16 v[188:191], v[100:103], v[152:155], v[188:191]\n\t"
      "v_mfma_f32_16x16x32_bf16 v[192:195], v[104:107], v[152:155], v[192:195]\n\t"
      "v_mfma_f32_16x16x32_bf16 v[196:199], v[108:111], v[152:155], v[196:199]\n\t"
      "global_load_dwordx4 v[152:155], v182, s[62:63] sc1\n\t"
      "s_waitcnt vmcnt(7)\n\t"
      "v_mfma_f32_16x16x32_bf16 v[184:187], v[112:115], v[156:159], v[184:187]\n\t"
      "v_mfma_f32_16x16x32_bf16 v[188:191], v[116:119], v[156:159], v[188:191]\n\t"
      "v_mfma_f32_16x16x32_bf16 v[192:195], v[120:123], v[156:159], v[192:195]\n\t"
      "v_mfma_f32_16x16x32_bf16 v[196:199], v[124:127], v[156:159], v[196:199]\n\t"
      "global_load_dwordx4 v[156:159], v183, s[62:63] sc1\n\t"
      "s_setprio 0\n\t"
      "s_nop 2\n\t"
      "v_cmp_u_f32_e32 vcc, v184, v184\n\t"
      "s_cbranch_vccnz NQ4R_%=\n\t"
      "NQ4B_%=:\n\t"
      "ds_write_b128 v243, v[184:187]\n\t"
      "ds_write_b128 v164, v[188:191]\n\t"
      "ds_write_b128 v165, v[192:195]\n\t"
      "ds_write_b128 v166, v[196:199]\n\t"
      "s_waitcnt vmcnt(7)\n\t"
      "v_mfma_f32_16x16x32_bf16 v[200:203], v[0:3], v[128:131], v[236:239]\n\t"
      "v_mfma_f32_16x16x32_bf16 v[204:207], v[4:7], v[128:131], 0\n\t"
      "v_mfma_f32_16x16x32_bf16 v[208:211], v[8:11], v[128:131], 0\n\t"
      "v_mfma_f32_16x16x32_bf16 v[212:215], v[12:15], v[128:131], 0\n\t"
      "global_load_dwordx4 v[128:131], v176, s[64:65] sc1\n\t"
      "s_waitcnt vmcnt(7)\n\t"
      "v_mfma_f32_16x16x32_bf16 v[200:203], v[16:19], v[132:135], v[200:203]\n\t"
      "v_mfma_f32_16x16x32_bf16 v[204:207], v[20:23], v[132:135], v[204:207]\n\t"
      "v_mfma_f32_16x16x32_bf16 v[208:211], v[24:27], v[132:135], v[208:211]\n\t"
      "v_mfma_f32_16x16x32_bf16 v[212:215], v[28:31], v[132:135], v[212:215]\n\t"
      "global_load_dwordx4 v[132:135], v177, s[64:65] sc1\n\t"
      "s_waitcnt vmcnt(7)\n\t"
      "v_mfma_f32_16x16x32_bf16 v[200:203], v[32:35], v[136:139], v[200:203]\n\t"
      "v_mfma_f32_16x16x32_bf16 v[204:207], v[36:39], v[136:139], v[204:207]\n\t"
      "v_mfma_f32_16x16x32_bf16 v[208:211], v[40:43], v[136:139], v[208:211]\n\t"
      "v_mfma_f32_16x16x32_bf16 v[212:215], v[44:47], v[136:139], v[212:215]\n\t"
      "global_load_dwordx4 v[136:139], v178, s[64:65] sc1\n\t"
      "s_waitcnt vmcnt(7)\n\t"
      "v_mfma_f32_16x16x32_bf16 v[200:203], v[48:51], v[140:143], v[200:203]\n\t"
      "v_mfma_f32_16x16x32_bf16 v[204:207], v[52:55], v[140:143], v[204:207]\n\t"
      "v_mfma_f32_16x16x32_bf16 v[208:211], v[56:59], v[140:143], v[208:211]\n\t"
      "v_mfma_f32_16x16x32_bf16 v[212:215], v[60:63], v[140:143], v[212:215]\n\t"
      "global_load_dwordx4 v[140:143], v179, s[64:65] sc1\n\t"
      "s_waitcnt vmcnt(7)\n\t"
      "v_mfma_f32_16x16x32_bf16 v[200:203], v[64:67], v[144:147], v[200:203]\n\t"
      "v_mfma_f32_16x16x32_bf16 v[204:207], v[68:71], v[144:147], v[204:207]\n\t"
      "v_mfma_f32_16x16x32_bf16 v[208:211], v[72:75], v[144:147], v[208:211]\n\t"
      "v_mfma_f32_16x16x32_bf16 v[212:215], v[76:79], v[144:147], v[212:215]\n\t"
      "global_load_dwordx4 v[144:147], v180, s[64:65] sc1\n\t"
      "s_waitcnt vmcnt(7)\n\t"
      "v_mfma_f32_16x16x32_bf16 v[200:203], v[80:83], v[148:151], v[200:203]\n\t"
      "v_mfma_f32_16x16x32_bf16 v[204:207], v[84:87], v[148:151], v[204:207]\n\t"
      "v_mfma_f32_16x16x32_bf16 v[208:211], v[88:91], v[148:151], v[208:211]\n\t"
      "v_mfma_f32_16x16x32_bf16 v[212:215], v[92:95], v[148:151], v[212:215]\n\t"
      "global_load_dwordx4 v[148:151], v181, s[64:65] sc1\n\t"
      "s_waitcnt lgkmcnt(0)\n\t"
      "s_barrier\n\t"
      "s_setprio 2\n\t"
      "s_waitcnt vmcnt(7)\n\t"
      "v_mfma_f32_16x16x32_bf16 v[200:203], v[96:99], v[152:155], v[200:203]\n\t"
      "v_mfma_f32_16x16x32_bf16 v[204:207], v[100:103], v[152:155], v[204:207]\n\t"
      "v_mfma_f32_16x16x32_bf16 v[208:211], v[104:107], v[152:155], v[208:211]\n\t"
      "v_mfma_f32_16x16x32_bf16 v[212:215], v[108:111], v[152:155], v[212:215]\n\t"
      "global_load_dwordx4 v[152:155], v182, s[64:65] sc1\n\t"
      "s_waitcnt vmcnt(7)\n\t"
      "v_mfma_f32_16x16x32_bf16 v[200:203], v[112:115], v[156:159], v[200:203]\n\t"
      "v_mfma_f32_16x16x32_bf16 v[204:207], v[116:119], v[156:159], v[204:207]\n\t"
      "v_mfma_f32_16x16x32_bf16 v[208:211], v[120:123], v[156:159], v[208:211]\n\t"
      "v_mfma_f32_16x16x32_bf16 v[212:215], v[124:127], v[156:159], v[212:215]\n\t"
      "global_load_dwordx4 v[156:159], v183, s[64:65] sc1\n\t"
      "s_setprio 0\n\t"
      "s_nop 2\n\t"
      "v_cmp_u_f32_e32 vcc, v200, v200\n\t"
      "s_cbranch_vccnz NQ5R_%=\n\t"
      "NQ5B_%=:\n\t"
      "ds_write_b128 v243, v[200:203] offset:32768\n\t"
      "ds_write_b128 v164, v[204:207] offset:32768\n\t"
      "ds_write_b128 v165, v[208:211] offset:32768\n\t"
      "ds_write_b128 v166, v[212:215] offset:32768\n\t"
      "s_waitcnt vmcnt(7)\n\t"
      "v_mfma_f32_16x16x32_bf16 v[184:187], v[0:3], v[128:131], v[236:239]\n\t"
      "v_mfma_f32_16x16x32_bf16 v[188:191], v[4:7], v[128:131], 0\n\t"
      "v_mfma_f32_16x16x32_bf16 v[192:195], v[8:11], v[128:131], 0\n\t"
      "v_mfma_f32_16x16x32_bf16 v[196:199], v[12:15], v[128:131], 0\n\t"
      "s_waitcnt vmcnt(6)\n\t"
      "v_mfma_f32_16x16x32_bf16 v[184:187], v[16:19], v[132:135], v[184:187]\n\t"
      "v_mfma_f32_16x16x32_bf16 v[188:191], v[20:23], v[132:135], v[188:191]\n\t"
      "v_mfma_f32_16x16x32_bf16 v[192:195], v[24:27], v[132:135], v[192:195]\n\t"
      "v_mfma_f32_16x16x32_bf16 v[196:199], v[28:31], v[132:135], v[196:199]\n\t"
      "s_waitcnt vmcnt(5)\n\t"
      "v_mfma_f32_16x16x32_bf16 v[184:187], v[32:35], v[136:139], v[184:187]\n\t"
      "v_mfma_f32_16x16x32_bf16 v[188:191], v[36:39], v[136:139], v[188:191]\n\t"
      "v_mfma_f32_16x16x32_bf16 v[192:195], v[40:43], v[136:139], v[192:195]\n\t"
      "v_mfma_f32_16x16x32_bf16 v[196:199], v[44:47], v[136:139], v[196:199]\n\t"
      "s_waitcnt vmcnt(4)\n\t"
      "v_mfma_f32_16x16x32_bf16 v[184:187], v[48:51], v[140:143], v[184:187]\n\t"
      "v_mfma_f32_16x16x32_bf16 v[188:191], v[52:55], v[140:143], v[188:191]\n\t"
      "v_mfma_f32_16x16x32_bf16 v[192:195], v[56:59], v[140:143], v[192:195]\n\t"
      "v_mfma_f32_16x16x32_bf16 v[196:199], v[60:63], v[140:143], v[196:199]\n\t"
      "s_waitcnt vmcnt(3)\n\t"
      "v_mfma_f32_16x16x32_bf16 v[184:187], v[64:67], v[144:147], v[184:187]\n\t"
      "v_mfma_f32_16x16x32_bf16 v[188:191], v[68:71], v[144:147], v[188:191]\n\t"
      "v_mfma_f32_16x16x32_bf16 v[192:195], v[72:75], v[144:147], v[192:195]\n\t"
      "v_mfma_f32_16x16x32_bf16 v[196:199], v[76:79], v[144:147], v[196:199]\n\t"
      "s_waitcnt vmcnt(2)\n\t"
      "v_mfma_f32_16x16x32_bf16 v[184:187], v[80:83], v[148:151], v[184:187]\n\t"
      "v_mfma_f32_16x16x32_bf16 v[188:191], v[84:87], v[148:151], v[188:191]\n\t"
      "v_mfma_f32_16x16x32_bf16 v[192:195], v[88:91], v[148:151], v[192:195]\n\t"
      "v_mfma_f32_16x16x32_bf16 v[196:199], v[92:95], v[148:151], v[196:199]\n\t"
      "s_waitcnt lgkmcnt(0)\n\t"
      "s_barrier\n\t"
      "s_setprio 2\n\t"
      "s_waitcnt vmcnt(1)\n\t"
      "v_mfma_f32_16x16x32_bf16 v[184:187], v[96:99], v[152:155], v[184:187]\n\t"
      "v_mfma_f32_16x16x32_bf16 v[188:191], v[100:103], v[152:155], v[188:191]\n\t"
      "v_mfma_f32_16x16x32_bf16 v[192:195], v[104:107], v[152:155], v[192:195]\n\t"
      "v_mfma_f32_16x16x32_bf16 v[196:199], v[108:111], v[152:155], v[196:199]\n\t"
      "s_waitcnt vmcnt(0)\n\t"
      "v_mfma_f32_16x16x32_bf16 v[184:187], v[112:115], v[156:159], v[184:187]\n\t"
      "v_mfma_f32_16x16x32_bf16 v[188:191], v[116:119], v[156:159], v[188:191]\n\t"
      "v_mfma_f32_16x16x32_bf16 v[192:195], v[120:123], v[156:159], v[192:195]\n\t"
      "v_mfma_f32_16x16x32_bf16 v[196:199], v[124:127], v[156:159], v[196:199]\n\t"
      "s_nop 4\n\t"
      "v_cmp_u_f32_e32 vcc, v184, v184\n\t"
      "s_cbranch_vccnz NQ6R_%=\n\t"
      "NQ6B_%=:\n\t"
      "ds_write_b128 v243, v[184:187]\n\t"
      "ds_write_b128 v164, v[188:191]\n\t"
      "ds_write_b128 v165, v[192:195]\n\t"
      "ds_write_b128 v166, v[196:199]\n\t"
      "s_waitcnt lgkmcnt(0)\n\t"
      "s_barrier\n\t"
      "s_branch GX_%=\n\t"
      "ASP_%=:\n\t"
      "s_mov_b32 m0, 163328\n\t"
      "s_cmp_ge_u32 s17, 65536\n\t"
      "s_cbranch_scc0 BOT_%=\n\t"
      "s_barrier\n\t"
      "BOT_%=:\n\t"
      "global_load_lds_dword v241, s[4:5]\n\t"
      "s_add_u32 s4, s4, 229376\n\t"
      "s_addc_u32 s5, s5, 0\n\t"
      "global_load_lds_dword v241, s[4:5]\n\t"
      "s_add_u32 s4, s4, 229376\n\t"
      "s_addc_u32 s5, s5, 0\n\t"
      "s_branch AEND_%=\n\t"
      "FQ0R_%=:\n\t"
      "s_mov_b32 s51, 0\n\t"
      "FQ0L_%=:\n\t"
      "s_add_u32 s51, s51, 1\n\t"
      "s_cmp_lt_u32 s51, 262144\n\t"
      "s_cbranch_scc0 FQ0X_%=\n\t"
      "s_waitcnt vmcnt(0)\n\t"
      "s_sleep 1\n\t"
      "global_load_dwordx4 v[128:131], v176, s[52:53] sc1\n\t"
      "global_load_dwordx4 v[132:135], v177, s[52:53] sc1\n\t"
      "global_load_dwordx4 v[136:139], v178, s[52:53] sc1\n\t"
      "global_load_dwordx4 v[140:143], v179, s[52:53] sc1\n\t"
      "global_load_dwordx4 v[144:147], v180, s[52:53] sc1\n\t"
      "global_load_dwordx4 v[148:151], v181, s[52:53] sc1\n\t"
      "global_load_dwordx4 v[152:155], v182, s[52:53] sc1\n\t"
      "global_load_dwordx4 v[156:159], v183, s[52:53] sc1\n\t"
      "s_waitcnt vmcnt(7)\n\t"
      "v_max3_u32 v234, v128, v129, v130\n\t"
      "v_max_u32_e32 v234, v234, v131\n\t"
      "v_cmp_eq_u32_e32 vcc, -1, v234\n\t"
      "s_cbranch_vccnz FQ0L_%=\n\t"
      "FQ0X_%=:\n\t"
      "s_branch FQ0B_%=\n\t"
      "FQ1R_%=:\n\t"
      "s_mov_b32 s51, 0\n\t"
      "FQ1L_%=:\n\t"
      "s_add_u32 s51, s51, 1\n\t"
      "s_cmp_lt_u32 s51, 262144\n\t"
      "s_cbranch_scc0 FQ1X_%=\n\t"
      "s_waitcnt vmcnt(0)\n\t"
      "s_sleep 1\n\t"
      "global_load_dwordx4 v[132:135], v177, s[52:53] sc1\n\t"
      "global_load_dwordx4 v[136:139], v178, s[52:53] sc1\n\t"
      "global_load_dwordx4 v[140:143], v179, s[52:53] sc1\n\t"
      "global_load_dwordx4 v[144:147], v180, s[52:53] sc1\n\t"
      "global_load_dwordx4 v[148:151], v181, s[52:53] sc1\n\t"
      "global_load_dwordx4 v[152:155], v182, s[52:53] sc1\n\t"
      "global_load_dwordx4 v[156:159], v183, s[52:53] sc1\n\t"
      "global_load_dwordx4 v[128:131], v176, s[54:55] sc1\n\t"
      "s_waitcnt vmcnt(7)\n\t"
      "v_max3_u32 v234, v132, v133, v134\n\t"
      "v_max_u32_e32 v234, v234, v135\n\t"
      "v_cmp_eq_u32_e32 vcc, -1, v234\n\t"
      "s_cbranch_vccnz FQ1L_%=\n\t"
      "FQ1X_%=:\n\t"
      "s_branch FQ1B_%=\n\t"
      "FQ2R_%=:\n\t"
      "s_mov_b32 s51, 0\n\t"
      "FQ2L_%=:\n\t"
      "s_add_u32 s51, s51, 1\n\t"
      "s_cmp_lt_u32 s51, 262144\n\t"
      "s_cbranch_scc0 FQ2X_%=\n\t"
      "s_waitcnt vmcnt(0)\n\t"
      "s_sleep 1\n\t"
      "global_load_dwordx4 v[136:139], v178, s[52:53] sc1\n\t"
      "global_load_dwordx4 v[140:143], v179, s[52:53] sc1\n\t"
      "global_load_dwordx4 v[144:147], v180, s[52:53] sc1\n\t"
      "global_load_dwordx4 v[148:151], v181, s[52:53] sc1\n\t"
      "global_load_dwordx4 v[152:155], v182, s[52:53] sc1\n\t"
      "global_load_dwordx4 v[156:159], v183, s[52:53] sc1\n\t"
      "global_load_dwordx4 v[128:131], v176, s[54:55] sc1\n\t"
      "global_load_dwordx4 v[132:135], v177, s[54:55] sc1\n\t"
      "s_waitcnt vmcnt(7)\n\t"
      "v_max3_u32 v234, v136, v137, v138\n\t"
      "v_max_u32_e32 v234, v234, v139\n\t"
      "v_cmp_eq_u32_e32 vcc, -1, v234\n\t"
      "s_cbranch_vccnz FQ2L_%=\n\t"
      "FQ2X_%=:\n\t"
      "s_branch FQ2B_%=\n\t"
      "FQ3R_%=:\n\t"
      "s_mov_b32 s51, 0\n\t"
      "FQ3L_%=:\n\t"
      "s_add_u32 s51, s51, 1\n\t"
      "s_cmp_lt_u32 s51, 262144\n\t"
      "s_cbranch_scc0 FQ3X_%=\n\t"
      "s_waitcnt vmcnt(0)\n\t"
      "s_sleep 1\n\t"
      "global_load_dwordx4 v[140:143], v179, s[52:53] sc1\n\t"
      "global_load_dwordx4 v[144:147], v180, s[52:53] sc1\n\t"
      "global_load_dwordx4 v[148:151], v181, s[52:53] sc1\n\t"
      "global_load_dwordx4 v[152:155], v182, s[52:53] sc1\n\t"
      "global_load_dwordx4 v[156:159], v183, s[52:53] sc1\n\t"
      "global_load_dwordx4 v[128:131], v176, s[54:55] sc1\n\t"
      "global_load_dwordx4 v[132:135], v177, s[54:55] sc1\n\t"
      "global_load_dwordx4 v[136:139], v178, s[54:55] sc1\n\t"
      "s_waitcnt vmcnt(7)\n\t"
      "v_max3_u32 v234, v140, v141, v142\n\t"
      "v_max_u32_e32 v234, v234, v143\n\t"
      "v_cmp_eq_u32_e32 vcc, -1, v234\n\t"
      "s_cbranch_vccnz FQ3L_%=\n\t"
      "FQ3X_%=:\n\t"
      "s_branch FQ3B_%=\n\t"
      "FQ4R_%=:\n\t"
      "s_mov_b32 s51, 0\n\t"
      "FQ4L_%=:\n\t"
      "s_add_u32 s51, s51, 1\n\t"
      "s_cmp_lt_u32 s51, 262144\n\t"
      "s_cbranch_scc0 FQ4X_%=\n\t"
      "s_waitcnt vmcnt(0)\n\t"
      "s_sleep 1\n\t"
      "global_load_dwordx4 v[144:147], v180, s[52:53] sc1\n\t"
      "global_load_dwordx4 v[148:151], v181, s[52:53] sc1\n\t"
      "global_load_dwordx4 v[152:155], v182, s[52:53] sc1\n\t"
      "global_load_dwordx4 v[156:159], v183, s[52:53] sc1\n\t"
      "global_load_dwordx4 v[128:131], v176, s[54:55] sc1\n\t"
      "global_load_dwordx4 v[132:135], v177, s[54:55] sc1\n\t"
      "global_load_dwordx4 v[136:139], v178, s[54:55] sc1\n\t"
      "global_load_dwordx4 v[140:143], v179, s[54:55] sc1\n\t"
      "s_waitcnt vmcnt(7)\n\t"
      "v_max3_u32 v234, v144, v145, v146\n\t"
      "v_max_u32_e32 v234, v234, v147\n\t"
      "v_cmp_eq_u32_e32 vcc, -1, v234\n\t"
      "s_cbranch_vccnz FQ4L_%=\n\t"
      "FQ4X_%=:\n\t"
      "s_branch FQ4B_%=\n\t"
      "FQ5R_%=:\n\t"
      "s_mov_b32 s51, 0\n\t"
      "FQ5L_%=:\n\t"
      "s_add_u32 s51, s51, 1\n\t"
      "s_cmp_lt_u32 s51, 262144\n\t"
      "s_cbranch_scc0 FQ5X_%=\n\t"
      "s_waitcnt vmcnt(0)\n\t"
      "s_sleep 1\n\t"
      "global_load_dwordx4 v[148:151], v181, s[52:53] sc1\n\t"
      "global_load_dwordx4 v[152:155], v182, s[52:53] sc1\n\t"
      "global_load_dwordx4 v[156:159], v183, s[52:53] sc1\n\t"
      "global_load_dwordx4 v[128:131], v176, s[54:55] sc1\n\t"
      "global_load_dwordx4 v[132:135], v177, s[54:55] sc1\n\t"
      "global_load_dwordx4 v[136:139], v178, s[54:55] sc1\n\t"
      "global_load_dwordx4 v[140:143], v179, s[54:55] sc1\n\t"
      "global_load_dwordx4 v[144:147], v180, s[54:55] sc1\n\t"
      "s_waitcnt vmcnt(7)\n\t"
      "v_max3_u32 v234, v148, v149, v150\n\t"
      "v_max_u32_e32 v234, v234, v151\n\t"
      "v_cmp_eq_u32_e32 vcc, -1, v234\n\t"
      "s_cbranch_vccnz FQ5L_%=\n\t"
      "FQ5X_%=:\n\t"
      "s_branch FQ5B_%=\n\t"
      "FQ6R_%=:\n\t"
      "s_mov_b32 s51, 0\n\t"
      "FQ6L_%=:\n\t"
      "s_add_u32 s51, s51, 1\n\t"
      "s_cmp_lt_u32 s51, 262144\n\t"
      "s_cbranch_scc0 FQ6X_%=\n\t"
      "s_waitcnt vmcnt(0)\n\t"
      "s_sleep 1\n\t"
      "global_load_dwordx4 v[152:155], v182, s[52:53] sc1\n\t"
      "global_load_dwordx4 v[156:159], v183, s[52:53] sc1\n\t"
      "global_load_dwordx4 v[128:131], v176, s[54:55] sc1\n\t"
      "global_load_dwordx4 v[132:135], v177, s[54:55] sc1\n\t"
      "global_load_dwordx4 v[136:139], v178, s[54:55] sc1\n\t"
      "global_load_dwordx4 v[140:143], v179, s[54:55] sc1\n\t"
      "global_load_dwordx4 v[144:147], v180, s[54:55] sc1\n\t"
      "global_load_dwordx4 v[148:151], v181, s[54:55] sc1\n\t"
      "s_waitcnt vmcnt(7)\n\t"
      "v_max3_u32 v234, v152, v153, v154\n\t"
      "v_max_u32_e32 v234, v234, v155\n\t"
      "v_cmp_eq_u32_e32 vcc, -1, v234\n\t"
      "s_cbranch_vccnz FQ6L_%=\n\t"
      "FQ6X_%=:\n\t"
      "s_branch FQ6B_%=\n\t"
      "FQ7R_%=:\n\t"
      "s_mov_b32 s51, 0\n\t"
      "FQ7L_%=:\n\t"
      "s_add_u32 s51, s51, 1\n\t"
      "s_cmp_lt_u32 s51, 262144\n\t"
      "s_cbranch_scc0 FQ7X_%=\n\t"
      "s_waitcnt vmcnt(0)\n\t"
      "s_sleep 1\n\t"
      "global_load_dwordx4 v[156:159], v183, s[52:53] sc1\n\t"
      "global_load_dwordx4 v[128:131], v176, s[54:55] sc1\n\t"
      "global_load_dwordx4 v[132:135], v177, s[54:55] sc1\n\t"
      "global_load_dwordx4 v[136:139], v178, s[54:55] sc1\n\t"
      "global_load_dwordx4 v[140:143], v179, s[54:55] sc1\n\t"
      "global_load_dwordx4 v[144:147], v180, s[54:55] sc1\n\t"
      "global_load_dwordx4 v[148:151], v181, s[54:55] sc1\n\t"
      "global_load_dwordx4 v[152:155], v182, s[54:55] sc1\n\t"
      "s_waitcnt vmcnt(7)\n\t"
      "v_max3_u32 v234, v156, v157, v158\n\t"
      "v_max_u32_e32 v234, v234, v159\n\t"
      "v_cmp_eq_u32_e32 vcc, -1, v234\n\t"
      "s_cbranch_vccnz FQ7L_%=\n\t"
      "FQ7X_%=:\n\t"
      "s_branch FQ7B_%=\n\t"
      "NQ1R_%=:\n\t"
      "s_mov_b32 s51, 0\n\t"
      "NQ1L_%=:\n\t"
      "s_add_u32 s51, s51, 1\n\t"
      "s_cmp_lt_u32 s51, 262144\n\t"
      "s_cbranch_scc0 NQ1X_%=\n\t"
      "s_waitcnt vmcnt(0)\n\t"
      "s_sleep 1\n\t"
      "global_load_dwordx4 v[128:131], v176, s[54:55] sc1\n\t"
      "global_load_dwordx4 v[132:135], v177, s[54:55] sc1\n\t"
      "global_load_dwordx4 v[136:139], v178, s[54:55] sc1\n\t"
      "global_load_dwordx4 v[140:143], v179, s[54:55] sc1\n\t"
      "global_load_dwordx4 v[144:147], v180, s[54:55] sc1\n\t"
      "global_load_dwordx4 v[148:151], v181, s[54:55] sc1\n\t"
      "global_load_dwordx4 v[152:155], v182, s[54:55] sc1\n\t"
      "global_load_dwordx4 v[156:159], v183, s[54:55] sc1\n\t"
      "s_waitcnt vmcnt(0)\n\t"
      "v_max3_u32 v234, v128, v129, v130\n\t"
      "v_max3_u32 v234, v234, v131, v132\n\t"
      "v_max3_u32 v234, v234, v133, v134\n\t"
      "v_max3_u32 v234, v234, v135, v136\n\t"
      "v_max3_u32 v234, v234, v137, v138\n\t"
      "v_max3_u32 v234, v234, v139, v140\n\t"
      "v_max3_u32 v234, v234, v141, v142\n\t"
      "v_max3_u32 v234, v234, v143, v144\n\t"
      "v_max3_u32 v234, v234, v145, v146\n\t"
      "v_max3_u32 v234, v234, v147, v148\n\t"
      "v_max3_u32 v234, v234, v149, v150\n\t"
      "v_max3_u32 v234, v234, v151, v152\n\t"
      "v_max3_u32 v234, v234, v153, v154\n\t"
      "v_max3_u32 v234, v234, v155, v156\n\t"
      "v_max3_u32 v234, v234, v157, v158\n\t"
      "v_max_u32_e32 v234, v234, v159\n\t"
      "v_cmp_eq_u32_e32 vcc, -1, v234\n\t"
      "s_cbranch_vccnz NQ1L_%=\n\t"
      "NQ1X_%=:\n\t"
      "v_mfma_f32_16x16x32_bf16 v[200:203], v[0:3], v[128:131], v[236:239]\n\t"
      "v_mfma_f32_16x16x32_bf16 v[204:207], v[4:7], v[128:131], 0\n\t"
      "v_mfma_f32_16x16x32_bf16 v[208:211], v[8:11], v[128:131], 0\n\t"
      "v_mfma_f32_16x16x32_bf16 v[212:215], v[12:15], v[128:131], 0\n\t"
      "v_mfma_f32_16x16x32_bf16 v[200:203], v[16:19], v[132:135], v[200:203]\n\t"
      "v_mfma_f32_16x16x32_bf16 v[204:207], v[20:23], v[132:135], v[204:207]\n\t"
      "v_mfma_f32_16x16x32_bf16 v[208:211], v[24:27], v[132:135], v[208:211]\n\t"
      "v_mfma_f32_16x16x32_bf16 v[212:215], v[28:31], v[132:135], v[212:215]\n\t"
      "v_mfma_f32_16x16x32_bf16 v[200:203], v[32:35], v[136:139], v[200:203]\n\t"
      "v_mfma_f32_16x16x32_bf16 v[204:207], v[36:39], v[136:139], v[204:207]\n\t"
      "v_mfma_f32_16x16x32_bf16 v[208:211], v[40:43], v[136:139], v[208:211]\n\t"
      "v_mfma_f32_16x16x32_bf16 v[212:215], v[44:47], v[136:139], v[212:215]\n\t"
      "v_mfma_f32_16x16x32_bf16 v[200:203], v[48:51], v[140:143], v[200:203]\n\t"
      "v_mfma_f32_16x16x32_bf16 v[204:207], v[52:55], v[140:143], v[204:207]\n\t"
      "v_mfma_f32_16x16x32_bf16 v[208:211], v[56:59], v[140:143], v[208:211]\n\t"
      "v_mfma_f32_16x16x32_bf16 v[212:215], v[60:63], v[140:143], v[212:215]\n\t"
      "v_mfma_f32_16x16x32_bf16 v[200:203], v[64:67], v[144:147], v[200:203]\n\t"
      "v_mfma_f32_16x16x32_bf16 v[204:207], v[68:71], v[144:147], v[204:207]\n\t"
      "v_mfma_f32_16x16x32_bf16 v[208:211], v[72:75], v[144:147], v[208:211]\n\t"
      "v_mfma_f32_16x16x32_bf16 v[212:215], v[76:79], v[144:147], v[212:215]\n\t"
      "v_mfma_f32_16x16x32_bf16 v[200:203], v[80:83], v[148:151], v[200:203]\n\t"
      "v_mfma_f32_16x16x32_bf16 v[204:207], v[84:87], v[148:151], v[204:207]\n\t"
      "v_mfma_f32_16x16x32_bf16 v[208:211], v[88:91], v[148:151], v[208:211]\n\t"
      "v_mfma_f32_16x16x32_bf16 v[212:215], v[92:95], v[148:151], v[212:215]\n\t"
      "v_mfma_f32_16x16x32_bf16 v[200:203], v[96:99], v[152:155], v[200:203]\n\t"
      "v_mfma_f32_16x16x32_bf16 v[204:207], v[100:103], v[152:155], v[204:207]\n\t"
      "v_mfma_f32_16x16x32_bf16 v[208:211], v[104:107], v[152:155], v[208:211]\n\t"
      "v_mfma_f32_16x16x32_bf16 v[212:215], v[108:111], v[152:155], v[212:215]\n\t"
      "v_mfma_f32_16x16x32_bf16 v[200:203], v[112:115], v[156:159], v[200:203]\n\t"
      "v_mfma_f32_16x16x32_bf16 v[204:207], v[116:119], v[156:159], v[204:207]\n\t"
      "v_mfma_f32_16x16x32_bf16 v[208:211], v[120:123], v[156:159], v[208:211]\n\t"
      "v_mfma_f32_16x16x32_bf16 v[212:215], v[124:127], v[156:159], v[212:215]\n\t"
      "global_load_dwordx4 v[128:131], v176, s[56:57] sc1\n\t"
      "global_load_dwordx4 v[132:135], v177, s[56:57] sc1\n\t"
      "global_load_dwordx4 v[136:139], v178, s[56:57] sc1\n\t"
      "global_load_dwordx4 v[140:143], v179, s[56:57] sc1\n\t"
      "global_load_dwordx4 v[144:147], v180, s[56:57] sc1\n\t"
      "global_load_dwordx4 v[148:151], v181, s[56:57] sc1\n\t"
      "global_load_dwordx4 v[152:155], v182, s[56:57] sc1\n\t"
      "global_load_dwordx4 v[156:159], v183, s[56:57] sc1\n\t"
      "s_waitcnt vmcnt(0)\n\t"
      "s_nop 7\n\t"
      "s_branch NQ1B_%=\n\t"
      "NQ2R_%=:\n\t"
      "s_mov_b32 s51, 0\n\t"
      "NQ2L_%=:\n\t"
      "s_add_u32 s51, s51, 1\n\t"
      "s_cmp_lt_u32 s51, 262144\n\t"
      "s_cbranch_scc0 NQ2X_%=\n\t"
      "s_waitcnt vmcnt(0)\n\t"
      "s_sleep 1\n\t"
      "global_load_dwordx4 v[128:131], v176, s[56:57] sc1\n\t"
      "global_load_dwordx4 v[132:135], v177, s[56:57] sc1\n\t"
      "global_load_dwordx4 v[136:139], v178, s[56:57] sc1\n\t"
      "global_load_dwordx4 v[140:143], v179, s[56:57] sc1\n\t"
      "global_load_dwordx4 v[144:147], v180, s[56:57] sc1\n\t"
      "global_load_dwordx4 v[148:151], v181, s[56:57] sc1\n\t"
      "global_load_dwordx4 v[152:155], v182, s[56:57] sc1\n\t"
      "global_load_dwordx4 v[156:159], v183, s[56:57] sc1\n\t"
      "s_waitcnt vmcnt(0)\n\t"
      "v_max3_u32 v234, v128, v129, v130\n\t"
      "v_max3_u32 v234, v234, v131, v132\n\t"
      "v_max3_u32 v234, v234, v133, v134\n\t"
      "v_max3_u32 v234, v234, v135, v136\n\t"
      "v_max3_u32 v234, v234, v137, v138\n\t"
      "v_max3_u32 v234, v234, v139, v140\n\t"
      "v_max3_u32 v234, v234, v141, v142\n\t"
      "v_max3_u32 v234, v234, v143, v144\n\t"
      "v_max3_u32 v234, v234, v145, v146\n\t"
      "v_max3_u32 v234, v234, v147, v148\n\t"
      "v_max3_u32 v234, v234, v149, v150\n\t"
      "v_max3_u32 v234, v234, v151, v152\n\t"
      "v_max3_u32 v234, v234, v153, v154\n\t"
      "v_max3_u32 v234, v234, v155, v156\n\t"
      "v_max3_u32 v234, v234, v157, v158\n\t"
      "v_max_u32_e32 v234, v234, v159\n\t"
      "v_cmp_eq_u32_e32 vcc, -1, v234\n\t"
      "s_cbranch_vccnz NQ2L_%=\n\t"
      "NQ2X_%=:\n\t"
      "v_mfma_f32_16x16x32_bf16 v[184:187], v[0:3], v[128:131], v[236:239]\n\t"
      "v_mfma_f32_16x16x32_bf16 v[188:191], v[4:7], v[128:131], 0\n\t"
      "v_mfma_f32_16x16x32_bf16 v[192:195], v[8:11], v[128:131], 0\n\t"
      "v_mfma_f32_16x16x32_bf16 v[196:199], v[12:15], v[128:131], 0\n\t"
      "v_mfma_f32_16x16x32_bf16 v[184:187], v[16:19], v[132:135], v[184:187]\n\t"
      "v_mfma_f32_16x16x32_bf16 v[188:191], v[20:23], v[132:135], v[188:191]\n\t"
      "v_mfma_f32_16x16x32_bf16 v[192:195], v[24:27], v[132:135], v[192:195]\n\t"
      "v_mfma_f32_16x16x32_bf16 v[196:199], v[28:31], v[132:135], v[196:199]\n\t"
      "v_mfma_f32_16x16x32_bf16 v[184:187], v[32:35], v[136:139], v[184:187]\n\t"
      "v_mfma_f32_16x16x32_bf16 v[188:191], v[36:39], v[136:139], v[188:191]\n\t"
      "v_mfma_f32_16x16x32_bf16 v[192:195], v[40:43], v[136:139], v[192:195]\n\t"
      "v_mfma_f32_16x16x32_bf16 v[196:199], v[44:47], v[136:139], v[196:199]\n\t"
      "v_mfma_f32_16x16x32_bf16 v[184:187], v[48:51], v[140:143], v[184:187]\n\t"
      "v_mfma_f32_16x16x32_bf16 v[188:191], v[52:55], v[140:143], v[188:191]\n\t"
      "v_mfma_f32_16x16x32_bf16 v[192:195], v[56:59], v[140:143], v[192:195]\n\t"
      "v_mfma_f32_16x16x32_bf16 v[196:199], v[60:63], v[140:143], v[196:199]\n\t"
      "v_mfma_f32_16x16x32_bf16 v[184:187], v[64:67], v[144:147], v[184:187]\n\t"
      "v_mfma_f32_16x16x32_bf16 v[188:191], v[68:71], v[144:147], v[188:191]\n\t"
      "v_mfma_f32_16x16x32_bf16 v[192:195], v[72:75], v[144:147], v[192:195]\n\t"
      "v_mfma_f32_16x16x32_bf16 v[196:199], v[76:79], v[144:147], v[196:199]\n\t"
      "v_mfma_f32_16x16x32_bf16 v[184:187], v[80:83], v[148:151], v[184:187]\n\t"
      "v_mfma_f32_16x16x32_bf16 v[188:191], v[84:87], v[148:151], v[188:191]\n\t"
      "v_mfma_f32_16x16x32_bf16 v[192:195], v[88:91], v[148:151], v[192:195]\n\t"
      "v_mfma_f32_16x16x32_bf16 v[196:199], v[92:95], v[148:151], v[196:199]\n\t"
      "v_mfma_f32_16x16x32_bf16 v[184:187], v[96:99], v[152:155], v[184:187]\n\t"
      "v_mfma_f32_16x16x32_bf16 v[188:191], v[100:103], v[152:155], v[188:191]\n\t"
      "v_mfma_f32_16x16x32_bf16 v[192:195], v[104:107], v[152:155], v[192:195]\n\t"
      "v_mfma_f32_16x16x32_bf16 v[196:199], v[108:111], v[152:155], v[196:199]\n\t"
      "v_mfma_f32_16x16x32_bf16 v[184:187], v[112:115], v[156:159], v[184:187]\n\t"
      "v_mfma_f32_16x16x32_bf16 v[188:191], v[116:119], v[156:159], v[188:191]\n\t"
      "v_mfma_f32_16x16x32_bf16 v[192:195], v[120:123], v[156:159], v[192:195]\n\t"
      "v_mfma_f32_16x16x32_bf16 v[196:199], v[124:127], v[156:159], v[196:199]\n\t"
      "global_load_dwordx4 v[128:131], v176, s[58:59] sc1\n\t"
      "global_load_dwordx4 v[132:135], v177, s[58:59] sc1\n\t"
      "global_load_dwordx4 v[136:139], v178, s[58:59] sc1\n\t"
      "global_load_dwordx4 v[140:143], v179, s[58:59] sc1\n\t"
      "global_load_dwordx4 v[144:147], v180, s[58:59] sc1\n\t"
      "global_load_dwordx4 v[148:151], v181, s[58:59] sc1\n\t"
      "global_load_dwordx4 v[152:155], v182, s[58:59] sc1\n\t"
      "global_load_dwordx4 v[156:159], v183, s[58:59] sc1\n\t"
      "s_waitcnt vmcnt(0)\n\t"
      "s_nop 7\n\t"
      "s_branch NQ2B_%=\n\t"
      "NQ3R_%=:\n\t"
      "s_mov_b32 s51, 0\n\t"
      "NQ3L_%=:\n\t"
      "s_add_u32 s51, s51, 1\n\t"
      "s_cmp_lt_u32 s51, 262144\n\t"
      "s_cbranch_scc0 NQ3X_%=\n\t"
      "s_waitcnt vmcnt(0)\n\t"
      "s_sleep 1\n\t"
      "global_load_dwordx4 v[128:131], v176, s[58:59] sc1\n\t"
      "global_load_dwordx4 v[132:135], v177, s[58:59] sc1\n\t"
      "global_load_dwordx4 v[136:139], v178, s[58:59] sc1\n\t"
      "global_load_dwordx4 v[140:143], v179, s[58:59] sc1\n\t"
      "global_load_dwordx4 v[144:147], v180, s[58:59] sc1\n\t"
      "global_load_dwordx4 v[148:151], v181, s[58:59] sc1\n\t"
      "global_load_dwordx4 v[152:155], v182, s[58:59] sc1\n\t"
      "global_load_dwordx4 v[156:159], v183, s[58:59] sc1\n\t"
      "s_waitcnt vmcnt(0)\n\t"
      "v_max3_u32 v234, v128, v129, v130\n\t"
      "v_max3_u32 v234, v234, v131, v132\n\t"
      "v_max3_u32 v234, v234, v133, v134\n\t"
      "v_max3_u32 v234, v234, v135, v136\n\t"
      "v_max3_u32 v234, v234, v137, v138\n\t"
      "v_max3_u32 v234, v234, v139, v140\n\t"
      "v_max3_u32 v234, v234, v141, v142\n\t"
      "v_max3_u32 v234, v234, v143, v144\n\t"
      "v_max3_u32 v234, v234, v145, v146\n\t"
      "v_max3_u32 v234, v234, v147, v148\n\t"
      "v_max3_u32 v234, v234, v149, v150\n\t"
      "v_max3_u32 v234, v234, v151, v152\n\t"
      "v_max3_u32 v234, v234, v153, v154\n\t"
      "v_max3_u32 v234, v234, v155, v156\n\t"
      "v_max3_u32 v234, v234, v157, v158\n\t"
      "v_max_u32_e32 v234, v234, v159\n\t"
      "v_cmp_eq_u32_e32 vcc, -1, v234\n\t"
      "s_cbranch_vccnz NQ3L_%=\n\t"
      "NQ3X_%=:\n\t"
      "v_mfma_f32_16x16x32_bf16 v[200:203], v[0:3], v[128:131], v[236:239]\n\t"
      "v_mfma_f32_16x16x32_bf16 v[204:207], v[4:7], v[128:131], 0\n\t"
      "v_mfma_f32_16x16x32_bf16 v[208:211], v[8:11], v[128:131], 0\n\t"
      "v_mfma_f32_16x16x32_bf16 v[212:215], v[12:15], v[128:131], 0\n\t"
      "v_mfma_f32_16x16x32_bf16 v[200:203], v[16:19], v[132:135], v[200:203]\n\t"
      "v_mfma_f32_16x16x32_bf16 v[204:207], v[20:23], v[132:135], v[204:207]\n\t"
      "v_mfma_f32_16x16x32_bf16 v[208:211], v[24:27], v[132:135], v[208:211]\n\t"
      "v_mfma_f32_16x16x32_bf16 v[212:215], v[28:31], v[132:135], v[212:215]\n\t"
      "v_mfma_f32_16x16x32_bf16 v[200:203], v[32:35], v[136:139], v[200:203]\n\t"
      "v_mfma_f32_16x16x32_bf16 v[204:207], v[36:39], v[136:139], v[204:207]\n\t"
      "v_mfma_f32_16x16x32_bf16 v[208:211], v[40:43], v[136:139], v[208:211]\n\t"
      "v_mfma_f32_16x16x32_bf16 v[212:215], v[44:47], v[136:139], v[212:215]\n\t"
      "v_mfma_f32_16x16x32_bf16 v[200:203], v[48:51], v[140:143], v[200:203]\n\t"
      "v_mfma_f32_16x16x32_bf16 v[204:207], v[52:55], v[140:143], v[204:207]\n\t"
      "v_mfma_f32_16x16x32_bf16 v[208:211], v[56:59], v[140:143], v[208:211]\n\t"
      "v_mfma_f32_16x16x32_bf16 v[212:215], v[60:63], v[140:143], v[212:215]\n\t"
      "v_mfma_f32_16x16x32_bf16 v[200:203], v[64:67], v[144:147], v[200:203]\n\t"
      "v_mfma_f32_16x16x32_bf16 v[204:207], v[68:71], v[144:147], v[204:207]\n\t"
      "v_mfma_f32_16x16x32_bf16 v[208:211], v[72:75], v[144:147], v[208:211]\n\t"
      "v_mfma_f32_16x16x32_bf16 v[212:215], v[76:79], v[144:147], v[212:215]\n\t"
      "v_mfma_f32_16x16x32_bf16 v[200:203], v[80:83], v[148:151], v[200:203]\n\t"
      "v_mfma_f32_16x16x32_bf16 v[204:207], v[84:87], v[148:151], v[204:207]\n\t"
      "v_mfma_f32_16x16x32_bf16 v[208:211], v[88:91], v[148:151], v[208:211]\n\t"
      "v_mfma_f32_16x16x32_bf16 v[212:215], v[92:95], v[148:151], v[212:215]\n\t"
      "v_mfma_f32_16x16x32_bf16 v[200:203], v[96:99], v[152:155], v[200:203]\n\t"
      "v_mfma_f32_16x16x32_bf16 v[204:207], v[100:103], v[152:155], v[204:207]\n\t"
      "v_mfma_f32_16x16x32_bf16 v[208:211], v[104:107], v[152:155], v[208:211]\n\t"
      "v_mfma_f32_16x16x32_bf16 v[212:215], v[108:111], v[152:155], v[212:215]\n\t"
      "v_mfma_f32_16x16x32_bf16 v[200:203], v[112:115], v[156:159], v[200:203]\n\t"
      "v_mfma_f32_16x16x32_bf16 v[204:207], v[116:119], v[156:159], v[204:207]\n\t"
      "v_mfma_f32_16x16x32_bf16 v[208:211], v[120:123], v[156:159], v[208:211]\n\t"
      "v_mfma_f32_16x16x32_bf16 v[212:215], v[124:127], v[156:159], v[212:215]\n\t"
      "global_load_dwordx4 v[128:131], v176, s[60:61] sc1\n\t"
      "global_load_dwordx4 v[132:135], v177, s[60:61] sc1\n\t"
      "global_load_dwordx4 v[136:139], v178, s[60:61] sc1\n\t"
      "global_load_dwordx4 v[140:143], v179, s[60:61] sc1\n\t"
      "global_load_dwordx4 v[144:147], v180, s[60:61] sc1\n\t"
      "global_load_dwordx4 v[148:151], v181, s[60:61] sc1\n\t"
      "global_load_dwordx4 v[152:155], v182, s[60:61] sc1\n\t"
      "global_load_dwordx4 v[156:159], v183, s[60:61] sc1\n\t"
      "s_waitcnt vmcnt(0)\n\t"
      "s_nop 7\n\t"
      "s_branch NQ3B_%=\n\t"
      "NQ4R_%=:\n\t"
      "s_mov_b32 s51, 0\n\t"
      "NQ4L_%=:\n\t"
      "s_add_u32 s51, s51, 1\n\t"
      "s_cmp_lt_u32 s51, 262144\n\t"
      "s_cbranch_scc0 NQ4X_%=\n\t"
      "s_waitcnt vmcnt(0)\n\t"
      "s_sleep 1\n\t"
      "global_load_dwordx4 v[128:131], v176, s[60:61] sc1\n\t"
      "global_load_dwordx4 v[132:135], v177, s[60:61] sc1\n\t"
      "global_load_dwordx4 v[136:139], v178, s[60:61] sc1\n\t"
      "global_load_dwordx4 v[140:143], v179, s[60:61] sc1\n\t"
      "global_load_dwordx4 v[144:147], v180, s[60:61] sc1\n\t"
      "global_load_dwordx4 v[148:151], v181, s[60:61] sc1\n\t"
      "global_load_dwordx4 v[152:155], v182, s[60:61] sc1\n\t"
      "global_load_dwordx4 v[156:159], v183, s[60:61] sc1\n\t"
      "s_waitcnt vmcnt(0)\n\t"
      "v_max3_u32 v234, v128, v129, v130\n\t"
      "v_max3_u32 v234, v234, v131, v132\n\t"
      "v_max3_u32 v234, v234, v133, v134\n\t"
      "v_max3_u32 v234, v234, v135, v136\n\t"
      "v_max3_u32 v234, v234, v137, v138\n\t"
      "v_max3_u32 v234, v234, v139, v140\n\t"
      "v_max3_u32 v234, v234, v141, v142\n\t"
      "v_max3_u32 v234, v234, v143, v144\n\t"
      "v_max3_u32 v234, v234, v145, v146\n\t"
      "v_max3_u32 v234, v234, v147, v148\n\t"
      "v_max3_u32 v234, v234, v149, v150\n\t"
      "v_max3_u32 v234, v234, v151, v152\n\t"
      "v_max3_u32 v234, v234, v153, v154\n\t"
      "v_max3_u32 v234, v234, v155, v156\n\t"
      "v_max3_u32 v234, v234, v157, v158\n\t"
      "v_max_u32_e32 v234, v234, v159\n\t"
      "v_cmp_eq_u32_e32 vcc, -1, v234\n\t"
      "s_cbranch_vccnz NQ4L_%=\n\t"
      "NQ4X_%=:\n\t"
      "v_mfma_f32_16x16x32_bf16 v[184:187], v[0:3], v[128:131], v[236:239]\n\t"
      "v_mfma_f32_16x16x32_bf16 v[188:191], v[4:7], v[128:131], 0\n\t"
      "v_mfma_f32_16x16x32_bf16 v[192:195], v[8:11], v[128:131], 0\n\t"
      "v_mfma_f32_16x16x32_bf16 v[196:199], v[12:15], v[128:131], 0\n\t"
      "v_mfma_f32_16x16x32_bf16 v[184:187], v[16:19], v[132:135], v[184:187]\n\t"
      "v_mfma_f32_16x16x32_bf16 v[188:191], v[20:23], v[132:135], v[188:191]\n\t"
      "v_mfma_f32_16x16x32_bf16 v[192:195], v[24:27], v[132:135], v[192:195]\n\t"
      "v_mfma_f32_16x16x32_bf16 v[196:199], v[28:31], v[132:135], v[196:199]\n\t"
      "v_mfma_f32_16x16x32_bf16 v[184:187], v[32:35], v[136:139], v[184:187]\n\t"
      "v_mfma_f32_16x16x32_bf16 v[188:191], v[36:39], v[136:139], v[188:191]\n\t"
      "v_mfma_f32_16x16x32_bf16 v[192:195], v[40:43], v[136:139], v[192:195]\n\t"
      "v_mfma_f32_16x16x32_bf16 v[196:199], v[44:47], v[136:139], v[196:199]\n\t"
      "v_mfma_f32_16x16x32_bf16 v[184:187], v[48:51], v[140:143], v[184:187]\n\t"
      "v_mfma_f32_16x16x32_bf16 v[188:191], v[52:55], v[140:143], v[188:191]\n\t"
      "v_mfma_f32_16x16x32_bf16 v[192:195], v[56:59], v[140:143], v[192:195]\n\t"
      "v_mfma_f32_16x16x32_bf16 v[196:199], v[60:63], v[140:143], v[196:199]\n\t"
      "v_mfma_f32_16x16x32_bf16 v[184:187], v[64:67], v[144:147], v[184:187]\n\t"
      "v_mfma_f32_16x16x32_bf16 v[188:191], v[68:71], v[144:147], v[188:191]\n\t"
      "v_mfma_f32_16x16x32_bf16 v[192:195], v[72:75], v[144:147], v[192:195]\n\t"
      "v_mfma_f32_16x16x32_bf16 v[196:199], v[76:79], v[144:147], v[196:199]\n\t"
      "v_mfma_f32_16x16x32_bf16 v[184:187], v[80:83], v[148:151], v[184:187]\n\t"
      "v_mfma_f32_16x16x32_bf16 v[188:191], v[84:87], v[148:151], v[188:191]\n\t"
      "v_mfma_f32_16x16x32_bf16 v[192:195], v[88:91], v[148:151], v[192:195]\n\t"
      "v_mfma_f32_16x16x32_bf16 v[196:199], v[92:95], v[148:151], v[196:199]\n\t"
      "v_mfma_f32_16x16x32_bf16 v[184:187], v[96:99], v[152:155], v[184:187]\n\t"
      "v_mfma_f32_16x16x32_bf16 v[188:191], v[100:103], v[152:155], v[188:191]\n\t"
      "v_mfma_f32_16x16x32_bf16 v[192:195], v[104:107], v[152:155], v[192:195]\n\t"
      "v_mfma_f32_16x16x32_bf16 v[196:199], v[108:111], v[152:155], v[196:199]\n\t"
      "v_mfma_f32_16x16x32_bf16 v[184:187], v[112:115], v[156:159], v[184:187]\n\t"
      "v_mfma_f32_16x16x32_bf16 v[188:191], v[116:119], v[156:159], v[188:191]\n\t"
      "v_mfma_f32_16x16x32_bf16 v[192:195], v[120:123], v[156:159], v[192:195]\n\t"
      "v_mfma_f32_16x16x32_bf16 v[196:199], v[124:127], v[156:159], v[196:199]\n\t"
      "global_load_dwordx4 v[128:131], v176, s[62:63] sc1\n\t"
      "global_load_dwordx4 v[132:135], v177, s[62:63] sc1\n\t"
      "global_load_dwordx4 v[136:139], v178, s[62:63] sc1\n\t"
      "global_load_dwordx4 v[140:143], v179, s[62:63] sc1\n\t"
      "global_load_dwordx4 v[144:147], v180, s[62:63] sc1\n\t"
      "global_load_dwordx4 v[148:151], v181, s[62:63] sc1\n\t"
      "global_load_dwordx4 v[152:155], v182, s[62:63] sc1\n\t"
      "global_load_dwordx4 v[156:159], v183, s[62:63] sc1\n\t"
      "s_waitcnt vmcnt(0)\n\t"
      "s_nop 7\n\t"
      "s_branch NQ4B_%=\n\t"
      "NQ5R_%=:\n\t"
      "s_mov_b32 s51, 0\n\t"
      "NQ5L_%=:\n\t"
      "s_add_u32 s51, s51, 1\n\t"
      "s_cmp_lt_u32 s51, 262144\n\t"
      "s_cbranch_scc0 NQ5X_%=\n\t"
      "s_waitcnt vmcnt(0)\n\t"
      "s_sleep 1\n\t"
      "global_load_dwordx4 v[128:131], v176, s[62:63] sc1\n\t"
      "global_load_dwordx4 v[132:135], v177, s[62:63] sc1\n\t"
      "global_load_dwordx4 v[136:139], v178, s[62:63] sc1\n\t"
      "global_load_dwordx4 v[140:143], v179, s[62:63] sc1\n\t"
      "global_load_dwordx4 v[144:147], v180, s[62:63] sc1\n\t"
      "global_load_dwordx4 v[148:151], v181, s[62:63] sc1\n\t"
      "global_load_dwordx4 v[152:155], v182, s[62:63] sc1\n\t"
      "global_load_dwordx4 v[156:159], v183, s[62:63] sc1\n\t"
      "s_waitcnt vmcnt(0)\n\t"
      "v_max3_u32 v234, v128, v129, v130\n\t"
      "v_max3_u32 v234, v234, v131, v132\n\t"
      "v_max3_u32 v234, v234, v133, v134\n\t"
      "v_max3_u32 v234, v234, v135, v136\n\t"
      "v_max3_u32 v234, v234, v137, v138\n\t"
      "v_max3_u32 v234, v234, v139, v140\n\t"
      "v_max3_u32 v234, v234, v141, v142\n\t"
      "v_max3_u32 v234, v234, v143, v144\n\t"
      "v_max3_u32 v234, v234, v145, v146\n\t"
      "v_max3_u32 v234, v234, v147, v148\n\t"
      "v_max3_u32 v234, v234, v149, v150\n\t"
      "v_max3_u32 v234, v234, v151, v152\n\t"
      "v_max3_u32 v234, v234, v153, v154\n\t"
      "v_max3_u32 v234, v234, v155, v156\n\t"
      "v_max3_u32 v234, v234, v157, v158\n\t"
      "v_max_u32_e32 v234, v234, v159\n\t"
      "v_cmp_eq_u32_e32 vcc, -1, v234\n\t"
      "s_cbranch_vccnz NQ5L_%=\n\t"
      "NQ5X_%=:\n\t"
      "v_mfma_f32_16x16x32_bf16 v[200:203], v[0:3], v[128:131], v[236:239]\n\t"
      "v_mfma_f32_16x16x32_bf16 v[204:207], v[4:7], v[128:131], 0\n\t"
      "v_mfma_f32_16x16x32_bf16 v[208:211], v[8:11], v[128:131], 0\n\t"
      "v_mfma_f32_16x16x32_bf16 v[212:215], v[12:15], v[128:131], 0\n\t"
      "v_mfma_f32_16x16x32_bf16 v[200:203], v[16:19], v[132:135], v[200:203]\n\t"
      "v_mfma_f32_16x16x32_bf16 v[204:207], v[20:23], v[132:135], v[204:207]\n\t"
      "v_mfma_f32_16x16x32_bf16 v[208:211], v[24:27], v[132:135], v[208:211]\n\t"
      "v_mfma_f32_16x16x32_bf16 v[212:215], v[28:31], v[132:135], v[212:215]\n\t"
      "v_mfma_f32_16x16x32_bf16 v[200:203], v[32:35], v[136:139], v[200:203]\n\t"
      "v_mfma_f32_16x16x32_bf16 v[204:207], v[36:39], v[136:139], v[204:207]\n\t"
      "v_mfma_f32_16x16x32_bf16 v[208:211], v[40:43], v[136:139], v[208:211]\n\t"
      "v_mfma_f32_16x16x32_bf16 v[212:215], v[44:47], v[136:139], v[212:215]\n\t"
      "v_mfma_f32_16x16x32_bf16 v[200:203], v[48:51], v[140:143], v[200:203]\n\t"
      "v_mfma_f32_16x16x32_bf16 v[204:207], v[52:55], v[140:143], v[204:207]\n\t"
      "v_mfma_f32_16x16x32_bf16 v[208:211], v[56:59], v[140:143], v[208:211]\n\t"
      "v_mfma_f32_16x16x32_bf16 v[212:215], v[60:63], v[140:143], v[212:215]\n\t"
      "v_mfma_f32_16x16x32_bf16 v[200:203], v[64:67], v[144:147], v[200:203]\n\t"
      "v_mfma_f32_16x16x32_bf16 v[204:207], v[68:71], v[144:147], v[204:207]\n\t"
      "v_mfma_f32_16x16x32_bf16 v[208:211], v[72:75], v[144:147], v[208:211]\n\t"
      "v_mfma_f32_16x16x32_bf16 v[212:215], v[76:79], v[144:147], v[212:215]\n\t"
      "v_mfma_f32_16x16x32_bf16 v[200:203], v[80:83], v[148:151], v[200:203]\n\t"
      "v_mfma_f32_16x16x32_bf16 v[204:207], v[84:87], v[148:151], v[204:207]\n\t"
      "v_mfma_f32_16x16x32_bf16 v[208:211], v[88:91], v[148:151], v[208:211]\n\t"
      "v_mfma_f32_16x16x32_bf16 v[212:215], v[92:95], v[148:151], v[212:215]\n\t"
      "v_mfma_f32_16x16x32_bf16 v[200:203], v[96:99], v[152:155], v[200:203]\n\t"
      "v_mfma_f32_16x16x32_bf16 v[204:207], v[100:103], v[152:155], v[204:207]\n\t"
      "v_mfma_f32_16x16x32_bf16 v[208:211], v[104:107], v[152:155], v[208:211]\n\t"
      "v_mfma_f32_16x16x32_bf16 v[212:215], v[108:111], v[152:155], v[212:215]\n\t"
      "v_mfma_f32_16x16x32_bf16 v[200:203], v[112:115], v[156:159], v[200:203]\n\t"
      "v_mfma_f32_16x16x32_bf16 v[204:207], v[116:119], v[156:159], v[204:207]\n\t"
      "v_mfma_f32_16x16x32_bf16 v[208:211], v[120:123], v[156:159], v[208:211]\n\t"
      "v_mfma_f32_16x16x32_bf16 v[212:215], v[124:127], v[156:159], v[212:215]\n\t"
      "global_load_dwordx4 v[128:131], v176, s[64:65] sc1\n\t"
      "global_load_dwordx4 v[132:135], v177, s[64:65] sc1\n\t"
      "global_load_dwordx4 v[136:139], v178, s[64:65] sc1\n\t"
      "global_load_dwordx4 v[140:143], v179, s[64:65] sc1\n\t"
      "global_load_dwordx4 v[144:147], v180, s[64:65] sc1\n\t"
      "global_load_dwordx4 v[148:151], v181, s[64:65] sc1\n\t"
      "global_load_dwordx4 v[152:155], v182, s[64:65] sc1\n\t"
      "global_load_dwordx4 v[156:159], v183, s[64:65] sc1\n\t"
      "s_waitcnt vmcnt(0)\n\t"
      "s_nop 7\n\t"
      "s_branch NQ5B_%=\n\t"
      "NQ6R_%=:\n\t"
      "s_mov_b32 s51, 0\n\t"
      "NQ6L_%=:\n\t"
      "s_add_u32 s51, s51, 1\n\t"
      "s_cmp_lt_u32 s51, 262144\n\t"
      "s_cbranch_scc0 NQ6X_%=\n\t"
      "s_waitcnt vmcnt(0)\n\t"
      "s_sleep 1\n\t"
      "global_load_dwordx4 v[128:131], v176, s[64:65] sc1\n\t"
      "global_load_dwordx4 v[132:135], v177, s[64:65] sc1\n\t"
      "global_load_dwordx4 v[136:139], v178, s[64:65] sc1\n\t"
      "global_load_dwordx4 v[140:143], v179, s[64:65] sc1\n\t"
      "global_load_dwordx4 v[144:147], v180, s[64:65] sc1\n\t"
      "global_load_dwordx4 v[148:151], v181, s[64:65] sc1\n\t"
      "global_load_dwordx4 v[152:155], v182, s[64:65] sc1\n\t"
      "global_load_dwordx4 v[156:159], v183, s[64:65] sc1\n\t"
      "s_waitcnt vmcnt(0)\n\t"
      "v_max3_u32 v234, v128, v129, v130\n\t"
      "v_max3_u32 v234, v234, v131, v132\n\t"
      "v_max3_u32 v234, v234, v133, v134\n\t"
      "v_max3_u32 v234, v234, v135, v136\n\t"
      "v_max3_u32 v234, v234, v137, v138\n\t"
      "v_max3_u32 v234, v234, v139, v140\n\t"
      "v_max3_u32 v234, v234, v141, v142\n\t"
      "v_max3_u32 v234, v234, v143, v144\n\t"
      "v_max3_u32 v234, v234, v145, v146\n\t"
      "v_max3_u32 v234, v234, v147, v148\n\t"
      "v_max3_u32 v234, v234, v149, v150\n\t"
      "v_max3_u32 v234, v234, v151, v152\n\t"
      "v_max3_u32 v234, v234, v153, v154\n\t"
      "v_max3_u32 v234, v234, v155, v156\n\t"
      "v_max3_u32 v234, v234, v157, v158\n\t"
      "v_max_u32_e32 v234, v234, v159\n\t"
      "v_cmp_eq_u32_e32 vcc, -1, v234\n\t"
      "s_cbranch_vccnz NQ6L_%=\n\t"
      "NQ6X_%=:\n\t"
      "v_mfma_f32_16x16x32_bf16 v[184:187], v[0:3], v[128:131], v[236:239]\n\t"
      "v_mfma_f32_16x16x32_bf16 v[188:191], v[4:7], v[128:131], 0\n\t"
      "v_mfma_f32_16x16x32_bf16 v[192:195], v[8:11], v[128:131], 0\n\t"
      "v_mfma_f32_16x16x32_bf16 v[196:199], v[12:15], v[128:131], 0\n\t"
      "v_mfma_f32_16x16x32_bf16 v[184:187], v[16:19], v[132:135], v[184:187]\n\t"
      "v_mfma_f32_16x16x32_bf16 v[188:191], v[20:23], v[132:135], v[188:191]\n\t"
      "v_mfma_f32_16x16x32_bf16 v[192:195], v[24:27], v[132:135], v[192:195]\n\t"
      "v_mfma_f32_16x16x32_bf16 v[196:199], v[28:31], v[132:135], v[196:199]\n\t"
      "v_mfma_f32_16x16x32_bf16 v[184:187], v[32:35], v[136:139], v[184:187]\n\t"
      "v_mfma_f32_16x16x32_bf16 v[188:191], v[36:39], v[136:139], v[188:191]\n\t"
      "v_mfma_f32_16x16x32_bf16 v[192:195], v[40:43], v[136:139], v[192:195]\n\t"
      "v_mfma_f32_16x16x32_bf16 v[196:199], v[44:47], v[136:139], v[196:199]\n\t"
      "v_mfma_f32_16x16x32_bf16 v[184:187], v[48:51], v[140:143], v[184:187]\n\t"
      "v_mfma_f32_16x16x32_bf16 v[188:191], v[52:55], v[140:143], v[188:191]\n\t"
      "v_mfma_f32_16x16x32_bf16 v[192:195], v[56:59], v[140:143], v[192:195]\n\t"
      "v_mfma_f32_16x16x32_bf16 v[196:199], v[60:63], v[140:143], v[196:199]\n\t"
      "v_mfma_f32_16x16x32_bf16 v[184:187], v[64:67], v[144:147], v[184:187]\n\t"
      "v_mfma_f32_16x16x32_bf16 v[188:191], v[68:71], v[144:147], v[188:191]\n\t"
      "v_mfma_f32_16x16x32_bf16 v[192:195], v[72:75], v[144:147], v[192:195]\n\t"
      "v_mfma_f32_16x16x32_bf16 v[196:199], v[76:79], v[144:147], v[196:199]\n\t"
      "v_mfma_f32_16x16x32_bf16 v[184:187], v[80:83], v[148:151], v[184:187]\n\t"
      "v_mfma_f32_16x16x32_bf16 v[188:191], v[84:87], v[148:151], v[188:191]\n\t"
      "v_mfma_f32_16x16x32_bf16 v[192:195], v[88:91], v[148:151], v[192:195]\n\t"
      "v_mfma_f32_16x16x32_bf16 v[196:199], v[92:95], v[148:151], v[196:199]\n\t"
      "v_mfma_f32_16x16x32_bf16 v[184:187], v[96:99], v[152:155], v[184:187]\n\t"
      "v_mfma_f32_16x16x32_bf16 v[188:191], v[100:103], v[152:155], v[188:191]\n\t"
      "v_mfma_f32_16x16x32_bf16 v[192:195], v[104:107], v[152:155], v[192:195]\n\t"
      "v_mfma_f32_16x16x32_bf16 v[196:199], v[108:111], v[152:155], v[196:199]\n\t"
      "v_mfma_f32_16x16x32_bf16 v[184:187], v[112:115], v[156:159], v[184:187]\n\t"
      "v_mfma_f32_16x16x32_bf16 v[188:191], v[116:119], v[156:159], v[188:191]\n\t"
      "v_mfma_f32_16x16x32_bf16 v[192:195], v[120:123], v[156:159], v[192:195]\n\t"
      "v_mfma_f32_16x16x32_bf16 v[196:199], v[124:127], v[156:159], v[196:199]\n\t"
      "s_waitcnt vmcnt(0)\n\t"
      "s_nop 7\n\t"
      "s_branch NQ6B_%=\n\t"
      "FP0R_%=:\n\t"
      "s_mov_b32 s51, 0\n\t"
      "FP0L_%=:\n\t"
      "s_add_u32 s51, s51, 1\n\t"
      "s_cmp_lt_u32 s51, 262144\n\t"
      "s_cbranch_scc0 FP0X_%=\n\t"
      "s_waitcnt vmcnt(0)\n\t"
      "s_sleep 1\n\t"
      "global_load_dwordx4 v[128:131], v176, s[52:53] sc1\n\t"
      "global_load_dwordx4 v[132:135], v177, s[52:53] sc1\n\t"
      "global_load_dwordx4 v[136:139], v178, s[52:53] sc1\n\t"
      "global_load_dwordx4 v[140:143], v179, s[52:53] sc1\n\t"
      "global_load_dwordx4 v[144:147], v180, s[52:53] sc1\n\t"
      "global_load_dwordx4 v[148:151], v181, s[52:53] sc1\n\t"
      "global_load_dwordx4 v[152:155], v182, s[52:53] sc1\n\t"
      "global_load_dwordx4 v[156:159], v183, s[52:53] sc1\n\t"
      "s_waitcnt vmcnt(7)\n\t"
      "v_max3_u32 v234, v128, v129, v130\n\t"
      "v_max_u32_e32 v234, v234, v131\n\t"
      "v_cmp_eq_u32_e32 vcc, -1, v234\n\t"
      "s_cbranch_vccnz FP0L_%=\n\t"
      "FP0X_%=:\n\t"
      "s_branch FP0B_%=\n\t"
      "FP1R_%=:\n\t"
      "s_mov_b32 s51, 0\n\t"
      "FP1L_%=:\n\t"
      "s_add_u32 s51, s51, 1\n\t"
      "s_cmp_lt_u32 s51, 262144\n\t"
      "s_cbranch_scc0 FP1X_%=\n\t"
      "s_waitcnt vmcnt(0)\n\t"
      "s_sleep 1\n\t"
      "global_load_dwordx4 v[132:135], v177, s[52:53] sc1\n\t"
      "global_load_dwordx4 v[136:139], v178, s[52:53] sc1\n\t"
      "global_load_dwordx4 v[140:143], v179, s[52:53] sc1\n\t"
      "global_load_dwordx4 v[144:147], v180, s[52:53] sc1\n\t"
      "global_load_dwordx4 v[148:151], v181, s[52:53] sc1\n\t"
      "global_load_dwordx4 v[152:155], v182, s[52:53] sc1\n\t"
      "global_load_dwordx4 v[156:159], v183, s[52:53] sc1\n\t"
      "global_load_dwordx4 v[128:131], v176, s[54:55] sc1\n\t"
      "s_waitcnt vmcnt(7)\n\t"
      "v_max3_u32 v234, v132, v133, v134\n\t"
      "v_max_u32_e32 v234, v234, v135\n\t"
      "v_cmp_eq_u32_e32 vcc, -1, v234\n\t"
      "s_cbranch_vccnz FP1L_%=\n\t"
      "FP1X_%=:\n\t"
      "s_branch FP1B_%=\n\t"
      "FP2R_%=:\n\t"
      "s_mov_b32 s51, 0\n\t"
      "FP2L_%=:\n\t"
      "s_add_u32 s51, s51, 1\n\t"
      "s_cmp_lt_u32 s51, 262144\n\t"
      "s_cbranch_scc0 FP2X_%=\n\t"
      "s_waitcnt vmcnt(0)\n\t"
      "s_sleep 1\n\t"
      "global_load_dwordx4 v[136:139], v178, s[52:53] sc1\n\t"
      "global_load_dwordx4 v[140:143], v179, s[52:53] sc1\n\t"
      "global_load_dwordx4 v[144:147], v180, s[52:53] sc1\n\t"
      "global_load_dwordx4 v[148:151], v181, s[52:53] sc1\n\t"
      "global_load_dwordx4 v[152:155], v182, s[52:53] sc1\n\t"
      "global_load_dwordx4 v[156:159], v183, s[52:53] sc1\n\t"
      "global_load_dwordx4 v[128:131], v176, s[54:55] sc1\n\t"
      "global_load_dwordx4 v[132:135], v177, s[54:55] sc1\n\t"
      "s_waitcnt vmcnt(7)\n\t"
      "v_max3_u32 v234, v136, v137, v138\n\t"
      "v_max_u32_e32 v234, v234, v139\n\t"
      "v_cmp_eq_u32_e32 vcc, -1, v234\n\t"
      "s_cbranch_vccnz FP2L_%=\n\t"
      "FP2X_%=:\n\t"
      "s_branch FP2B_%=\n\t"
      "FP3R_%=:\n\t"
      "s_mov_b32 s51, 0\n\t"
      "FP3L_%=:\n\t"
      "s_add_u32 s51, s51, 1\n\t"
      "s_cmp_lt_u32 s51, 262144\n\t"
      "s_cbranch_scc0 FP3X_%=\n\t"
      "s_waitcnt vmcnt(0)\n\t"
      "s_sleep 1\n\t"
      "global_load_dwordx4 v[140:143], v179, s[52:53] sc1\n\t"
      "global_load_dwordx4 v[144:147], v180, s[52:53] sc1\n\t"
      "global_load_dwordx4 v[148:151], v181, s[52:53] sc1\n\t"
      "global_load_dwordx4 v[152:155], v182, s[52:53] sc1\n\t"
      "global_load_dwordx4 v[156:159], v183, s[52:53] sc1\n\t"
      "global_load_dwordx4 v[128:131], v176, s[54:55] sc1\n\t"
      "global_load_dwordx4 v[132:135], v177, s[54:55] sc1\n\t"
      "global_load_dwordx4 v[136:139], v178, s[54:55] sc1\n\t"
      "s_waitcnt vmcnt(7)\n\t"
      "v_max3_u32 v234, v140, v141, v142\n\t"
      "v_max_u32_e32 v234, v234, v143\n\t"
      "v_cmp_eq_u32_e32 vcc, -1, v234\n\t"
      "s_cbranch_vccnz FP3L_%=\n\t"
      "FP3X_%=:\n\t"
      "s_branch FP3B_%=\n\t"
      "FP4R_%=:\n\t"
      "s_mov_b32 s51, 0\n\t"
      "FP4L_%=:\n\t"
      "s_add_u32 s51, s51, 1\n\t"
      "s_cmp_lt_u32 s51, 262144\n\t"
      "s_cbranch_scc0 FP4X_%=\n\t"
      "s_waitcnt vmcnt(0)\n\t"
      "s_sleep 1\n\t"
      "global_load_dwordx4 v[144:147], v180, s[52:53] sc1\n\t"
      "global_load_dwordx4 v[148:151], v181, s[52:53] sc1\n\t"
      "global_load_dwordx4 v[152:155], v182, s[52:53] sc1\n\t"
      "global_load_dwordx4 v[156:159], v183, s[52:53] sc1\n\t"
      "global_load_dwordx4 v[128:131], v176, s[54:55] sc1\n\t"
      "global_load_dwordx4 v[132:135], v177, s[54:55] sc1\n\t"
      "global_load_dwordx4 v[136:139], v178, s[54:55] sc1\n\t"
      "global_load_dwordx4 v[140:143], v179, s[54:55] sc1\n\t"
      "s_waitcnt vmcnt(7)\n\t"
      "v_max3_u32 v234, v144, v145, v146\n\t"
      "v_max_u32_e32 v234, v234, v147\n\t"
      "v_cmp_eq_u32_e32 vcc, -1, v234\n\t"
      "s_cbranch_vccnz FP4L_%=\n\t"
      "FP4X_%=:\n\t"
      "s_branch FP4B_%=\n\t"
      "FP5R_%=:\n\t"
      "s_mov_b32 s51, 0\n\t"
      "FP5L_%=:\n\t"
      "s_add_u32 s51, s51, 1\n\t"
      "s_cmp_lt_u32 s51, 262144\n\t"
      "s_cbranch_scc0 FP5X_%=\n\t"
      "s_waitcnt vmcnt(0)\n\t"
      "s_sleep 1\n\t"
      "global_load_dwordx4 v[148:151], v181, s[52:53] sc1\n\t"
      "global_load_dwordx4 v[152:155], v182, s[52:53] sc1\n\t"
      "global_load_dwordx4 v[156:159], v183, s[52:53] sc1\n\t"
      "global_load_dwordx4 v[128:131], v176, s[54:55] sc1\n\t"
      "global_load_dwordx4 v[132:135], v177, s[54:55] sc1\n\t"
      "global_load_dwordx4 v[136:139], v178, s[54:55] sc1\n\t"
      "global_load_dwordx4 v[140:143], v179, s[54:55] sc1\n\t"
      "global_load_dwordx4 v[144:147], v180, s[54:55] sc1\n\t"
      "s_waitcnt vmcnt(7)\n\t"
      "v_max3_u32 v234, v148, v149, v150\n\t"
      "v_max_u32_e32 v234, v234, v151\n\t"
      "v_cmp_eq_u32_e32 vcc, -1, v234\n\t"
      "s_cbranch_vccnz FP5L_%=\n\t"
      "FP5X_%=:\n\t"
      "s_branch FP5B_%=\n\t"
      "FP6R_%=:\n\t"
      "s_mov_b32 s51, 0\n\t"
      "FP6L_%=:\n\t"
      "s_add_u32 s51, s51, 1\n\t"
      "s_cmp_lt_u32 s51, 262144\n\t"
      "s_cbranch_scc0 FP6X_%=\n\t"
      "s_waitcnt vmcnt(0)\n\t"
      "s_sleep 1\n\t"
      "global_load_dwordx4 v[152:155], v182, s[52:53] sc1\n\t"
      "global_load_dwordx4 v[156:159], v183, s[52:53] sc1\n\t"
      "global_load_dwordx4 v[128:131], v176, s[54:55] sc1\n\t"
      "global_load_dwordx4 v[132:135], v177, s[54:55] sc1\n\t"
      "global_load_dwordx4 v[136:139], v178, s[54:55] sc1\n\t"
      "global_load_dwordx4 v[140:143], v179, s[54:55] sc1\n\t"
      "global_load_dwordx4 v[144:147], v180, s[54:55] sc1\n\t"
      "global_load_dwordx4 v[148:151], v181, s[54:55] sc1\n\t"
      "s_waitcnt vmcnt(7)\n\t"
      "v_max3_u32 v234, v152, v153, v154\n\t"
      "v_max_u32_e32 v234, v234, v155\n\t"
      "v_cmp_eq_u32_e32 vcc, -1, v234\n\t"
      "s_cbranch_vccnz FP6L_%=\n\t"
      "FP6X_%=:\n\t"
      "s_branch FP6B_%=\n\t"
      "FP7R_%=:\n\t"
      "s_mov_b32 s51, 0\n\t"
      "FP7L_%=:\n\t"
      "s_add_u32 s51, s51, 1\n\t"
      "s_cmp_lt_u32 s51, 262144\n\t"
      "s_cbranch_scc0 FP7X_%=\n\t"
      "s_waitcnt vmcnt(0)\n\t"
      "s_sleep 1\n\t"
      "global_load_dwordx4 v[156:159], v183, s[52:53] sc1\n\t"
      "global_load_dwordx4 v[128:131], v176, s[54:55] sc1\n\t"
      "global_load_dwordx4 v[132:135], v177, s[54:55] sc1\n\t"
      "global_load_dwordx4 v[136:139], v178, s[54:55] sc1\n\t"
      "global_load_dwordx4 v[140:143], v179, s[54:55] sc1\n\t"
      "global_load_dwordx4 v[144:147], v180, s[54:55] sc1\n\t"
      "global_load_dwordx4 v[148:151], v181, s[54:55] sc1\n\t"
      "global_load_dwordx4 v[152:155], v182, s[54:55] sc1\n\t"
      "s_waitcnt vmcnt(7)\n\t"
      "v_max3_u32 v234, v156, v157, v158\n\t"
      "v_max_u32_e32 v234, v234, v159\n\t"
      "v_cmp_eq_u32_e32 vcc, -1, v234\n\t"
      "s_cbranch_vccnz FP7L_%=\n\t"
      "FP7X_%=:\n\t"
      "s_branch FP7B_%=\n\t"
      "NP1R_%=:\n\t"
      "s_mov_b32 s51, 0\n\t"
      "NP1L_%=:\n\t"
      "s_add_u32 s51, s51, 1\n\t"
      "s_cmp_lt_u32 s51, 262144\n\t"
      "s_cbranch_scc0 NP1X_%=\n\t"
      "s_waitcnt vmcnt(0)\n\t"
      "s_sleep 1\n\t"
      "global_load_dwordx4 v[128:131], v176, s[54:55] sc1\n\t"
      "global_load_dwordx4 v[132:135], v177, s[54:55] sc1\n\t"
      "global_load_dwordx4 v[136:139], v178, s[54:55] sc1\n\t"
      "global_load_dwordx4 v[140:143], v179, s[54:55] sc1\n\t"
      "global_load_dwordx4 v[144:147], v180, s[54:55] sc1\n\t"
      "global_load_dwordx4 v[148:151], v181, s[54:55] sc1\n\t"
      "global_load_dwordx4 v[152:155], v182, s[54:55] sc1\n\t"
      "global_load_dwordx4 v[156:159], v183, s[54:55] sc1\n\t"
      "s_waitcnt vmcnt(0)\n\t"
      "v_max3_u32 v234, v128, v129, v130\n\t"
      "v_max3_u32 v234, v234, v131, v132\n\t"
      "v_max3_u32 v234, v234, v133, v134\n\t"
      "v_max3_u32 v234, v234, v135, v136\n\t"
      "v_max3_u32 v234, v234, v137, v138\n\t"
      "v_max3_u32 v234, v234, v139, v140\n\t"
      "v_max3_u32 v234, v234, v141, v142\n\t"
      "v_max3_u32 v234, v234, v143, v144\n\t"
      "v_max3_u32 v234, v234, v145, v146\n\t"
      "v_max3_u32 v234, v234, v147, v148\n\t"
      "v_max3_u32 v234, v234, v149, v150\n\t"
      "v_max3_u32 v234, v234, v151, v152\n\t"
      "v_max3_u32 v234, v234, v153, v154\n\t"
      "v_max3_u32 v234, v234, v155, v156\n\t"
      "v_max3_u32 v234, v234, v157, v158\n\t"
      "v_max_u32_e32 v234, v234, v159\n\t"
      "v_cmp_eq_u32_e32 vcc, -1, v234\n\t"
      "s_cbranch_vccnz NP1L_%=\n\t"
      "NP1X_%=:\n\t"
      "v_mfma_f32_16x16x32_bf16 v[200:203], v[0:3], v[128:131], 0\n\t"
      "v_mfma_f32_16x16x32_bf16 v[204:207], v[4:7], v[128:131], 0\n\t"
      "v_mfma_f32_16x16x32_bf16 v[208:211], v[8:11], v[128:131], 0\n\t"
      "v_mfma_f32_16x16x32_bf16 v[212:215], v[12:15], v[128:131], 0\n\t"
      "v_mfma_f32_16x16x32_bf16 v[200:203], v[16:19], v[132:135], v[200:203]\n\t"
      "v_mfma_f32_16x16x32_bf16 v[204:207], v[20:23], v[132:135], v[204:207]\n\t"
      "v_mfma_f32_16x16x32_bf16 v[208:211], v[24:27], v[132:135], v[208:211]\n\t"
      "v_mfma_f32_16x16x32_bf16 v[212:215], v[28:31], v[132:135], v[212:215]\n\t"
      "v_mfma_f32_16x16x32_bf16 v[200:203], v[32:35], v[136:139], v[200:203]\n\t"
      "v_mfma_f32_16x16x32_bf16 v[204:207], v[36:39], v[136:139], v[204:207]\n\t"
      "v_mfma_f32_16x16x32_bf16 v[208:211], v[40:43], v[136:139], v[208:211]\n\t"
      "v_mfma_f32_16x16x32_bf16 v[212:215], v[44:47], v[136:139], v[212:215]\n\t"
      "v_mfma_f32_16x16x32_bf16 v[200:203], v[48:51], v[140:143], v[200:203]\n\t"
      "v_mfma_f32_16x16x32_bf16 v[204:207], v[52:55], v[140:143], v[204:207]\n\t"
      "v_mfma_f32_16x16x32_bf16 v[208:211], v[56:59], v[140:143], v[208:211]\n\t"
      "v_mfma_f32_16x16x32_bf16 v[212:215], v[60:63], v[140:143], v[212:215]\n\t"
      "v_mfma_f32_16x16x32_bf16 v[200:203], v[64:67], v[144:147], v[200:203]\n\t"
      "v_mfma_f32_16x16x32_bf16 v[204:207], v[68:71], v[144:147], v[204:207]\n\t"
      "v_mfma_f32_16x16x32_bf16 v[208:211], v[72:75], v[144:147], v[208:211]\n\t"
      "v_mfma_f32_16x16x32_bf16 v[212:215], v[76:79], v[144:147], v[212:215]\n\t"
      "v_mfma_f32_16x16x32_bf16 v[200:203], v[80:83], v[148:151], v[200:203]\n\t"
      "v_mfma_f32_16x16x32_bf16 v[204:207], v[84:87], v[148:151], v[204:207]\n\t"
      "v_mfma_f32_16x16x32_bf16 v[208:211], v[88:91], v[148:151], v[208:211]\n\t"
      "v_mfma_f32_16x16x32_bf16 v[212:215], v[92:95], v[148:151], v[212:215]\n\t"
      "v_mfma_f32_16x16x32_bf16 v[200:203], v[96:99], v[152:155], v[200:203]\n\t"
      "v_mfma_f32_16x16x32_bf16 v[204:207], v[100:103], v[152:155], v[204:207]\n\t"
      "v_mfma_f32_16x16x32_bf16 v[208:211], v[104:107], v[152:155], v[208:211]\n\t"
      "v_mfma_f32_16x16x32_bf16 v[212:215], v[108:111], v[152:155], v[212:215]\n\t"
      "v_mfma_f32_16x16x32_bf16 v[200:203], v[112:115], v[156:159], v[200:203]\n\t"
      "v_mfma_f32_16x16x32_bf16 v[204:207], v[116:119], v[156:159], v[204:207]\n\t"
      "v_mfma_f32_16x16x32_bf16 v[208:211], v[120:123], v[156:159], v[208:211]\n\t"
      "v_mfma_f32_16x16x32_bf16 v[212:215], v[124:127], v[156:159], v[212:215]\n\t"
      "global_load_dwordx4 v[128:131], v176, s[56:57] sc1\n\t"
      "global_load_dwordx4 v[132:135], v177, s[56:57] sc1\n\t"
      "global_load_dwordx4 v[136:139], v178, s[56:57] sc1\n\t"
      "global_load_dwordx4 v[140:143], v179, s[56:57] sc1\n\t"
      "global_load_dwordx4 v[144:147], v180, s[56:57] sc1\n\t"
      "global_load_dwordx4 v[148:151], v181, s[56:57] sc1\n\t"
      "global_load_dwordx4 v[152:155], v182, s[56:57] sc1\n\t"
      "global_load_dwordx4 v[156:159], v183, s[56:57] sc1\n\t"
      "s_waitcnt vmcnt(0)\n\t"
      "s_nop 7\n\t"
      "s_branch NP1B_%=\n\t"
      "NP2R_%=:\n\t"
      "s_mov_b32 s51, 0\n\t"
      "NP2L_%=:\n\t"
      "s_add_u32 s51, s51, 1\n\t"
      "s_cmp_lt_u32 s51, 262144\n\t"
      "s_cbranch_scc0 NP2X_%=\n\t"
      "s_waitcnt vmcnt(0)\n\t"
      "s_sleep 1\n\t"
      "global_load_dwordx4 v[128:131], v176, s[56:57] sc1\n\t"
      "global_load_dwordx4 v[132:135], v177, s[56:57] sc1\n\t"
      "global_load_dwordx4 v[136:139], v178, s[56:57] sc1\n\t"
      "global_load_dwordx4 v[140:143], v179, s[56:57] sc1\n\t"
      "global_load_dwordx4 v[144:147], v180, s[56:57] sc1\n\t"
      "global_load_dwordx4 v[148:151], v181, s[56:57] sc1\n\t"
      "global_load_dwordx4 v[152:155], v182, s[56:57] sc1\n\t"
      "global_load_dwordx4 v[156:159], v183, s[56:57] sc1\n\t"
      "s_waitcnt vmcnt(0)\n\t"
      "v_max3_u32 v234, v128, v129, v130\n\t"
      "v_max3_u32 v234, v234, v131, v132\n\t"
      "v_max3_u32 v234, v234, v133, v134\n\t"
      "v_max3_u32 v234, v234, v135, v136\n\t"
      "v_max3_u32 v234, v234, v137, v138\n\t"
      "v_max3_u32 v234, v234, v139, v140\n\t"
      "v_max3_u32 v234, v234, v141, v142\n\t"
      "v_max3_u32 v234, v234, v143, v144\n\t"
      "v_max3_u32 v234, v234, v145, v146\n\t"
      "v_max3_u32 v234, v234, v147, v148\n\t"
      "v_max3_u32 v234, v234, v149, v150\n\t"
      "v_max3_u32 v234, v234, v151, v152\n\t"
      "v_max3_u32 v234, v234, v153, v154\n\t"
      "v_max3_u32 v234, v234, v155, v156\n\t"
      "v_max3_u32 v234, v234, v157, v158\n\t"
      "v_max_u32_e32 v234, v234, v159\n\t"
      "v_cmp_eq_u32_e32 vcc, -1, v234\n\t"
      "s_cbranch_vccnz NP2L_%=\n\t"
      "NP2X_%=:\n\t"
      "v_mfma_f32_16x16x32_bf16 v[184:187], v[0:3], v[128:131], 0\n\t"
      "v_mfma_f32_16x16x32_bf16 v[188:191], v[4:7], v[128:131], 0\n\t"
      "v_mfma_f32_16x16x32_bf16 v[192:195], v[8:11], v[128:131], 0\n\t"
      "v_mfma_f32_16x16x32_bf16 v[196:199], v[12:15], v[128:131], 0\n\t"
      "v_mfma_f32_16x16x32_bf16 v[184:187], v[16:19], v[132:135], v[184:187]\n\t"
      "v_mfma_f32_16x16x32_bf16 v[188:191], v[20:23], v[132:135], v[188:191]\n\t"
      "v_mfma_f32_16x16x32_bf16 v[192:195], v[24:27], v[132:135], v[192:195]\n\t"
      "v_mfma_f32_16x16x32_bf16 v[196:199], v[28:31], v[132:135], v[196:199]\n\t"
      "v_mfma_f32_16x16x32_bf16 v[184:187], v[32:35], v[136:139], v[184:187]\n\t"
      "v_mfma_f32_16x16x32_bf16 v[188:191], v[36:39], v[136:139], v[188:191]\n\t"
      "v_mfma_f32_16x16x32_bf16 v[192:195], v[40:43], v[136:139], v[192:195]\n\t"
      "v_mfma_f32_16x16x32_bf16 v[196:199], v[44:47], v[136:139], v[196:199]\n\t"
      "v_mfma_f32_16x16x32_bf16 v[184:187], v[48:51], v[140:143], v[184:187]\n\t"
      "v_mfma_f32_16x16x32_bf16 v[188:191], v[52:55], v[140:143], v[188:191]\n\t"
      "v_mfma_f32_16x16x32_bf16 v[192:195], v[56:59], v[140:143], v[192:195]\n\t"
      "v_mfma_f32_16x16x32_bf16 v[196:199], v[60:63], v[140:143], v[196:199]\n\t"
      "v_mfma_f32_16x16x32_bf16 v[184:187], v[64:67], v[144:147], v[184:187]\n\t"
      "v_mfma_f32_16x16x32_bf16 v[188:191], v[68:71], v[144:147], v[188:191]\n\t"
      "v_mfma_f32_16x16x32_bf16 v[192:195], v[72:75], v[144:147], v[192:195]\n\t"
      "v_mfma_f32_16x16x32_bf16 v[196:199], v[76:79], v[144:147], v[196:199]\n\t"
      "v_mfma_f32_16x16x32_bf16 v[184:187], v[80:83], v[148:151], v[184:187]\n\t"
      "v_mfma_f32_16x16x32_bf16 v[188:191], v[84:87], v[148:151], v[188:191]\n\t"
      "v_mfma_f32_16x16x32_bf16 v[192:195], v[88:91], v[148:151], v[192:195]\n\t"
      "v_mfma_f32_16x16x32_bf16 v[196:199], v[92:95], v[148:151], v[196:199]\n\t"
      "v_mfma_f32_16x16x32_bf16 v[184:187], v[96:99], v[152:155], v[184:187]\n\t"
      "v_mfma_f32_16x16x32_bf16 v[188:191], v[100:103], v[152:155], v[188:191]\n\t"
      "v_mfma_f32_16x16x32_bf16 v[192:195], v[104:107], v[152:155], v[192:195]\n\t"
      "v_mfma_f32_16x16x32_bf16 v[196:199], v[108:111], v[152:155], v[196:199]\n\t"
      "v_mfma_f32_16x16x32_bf16 v[184:187], v[112:115], v[156:159], v[184:187]\n\t"
      "v_mfma_f32_16x16x32_bf16 v[188:191], v[116:119], v[156:159], v[188:191]\n\t"
      "v_mfma_f32_16x16x32_bf16 v[192:195], v[120:123], v[156:159], v[192:195]\n\t"
      "v_mfma_f32_16x16x32_bf16 v[196:199], v[124:127], v[156:159], v[196:199]\n\t"
      "global_load_dwordx4 v[128:131], v176, s[58:59] sc1\n\t"
      "global_load_dwordx4 v[132:135], v177, s[58:59] sc1\n\t"
      "global_load_dwordx4 v[136:139], v178, s[58:59] sc1\n\t"
      "global_load_dwordx4 v[140:143], v179, s[58:59] sc1\n\t"
      "global_load_dwordx4 v[144:147], v180, s[58:59] sc1\n\t"
      "global_load_dwordx4 v[148:151], v181, s[58:59] sc1\n\t"
      "global_load_dwordx4 v[152:155], v182, s[58:59] sc1\n\t"
      "global_load_dwordx4 v[156:159], v183, s[58:59] sc1\n\t"
      "s_waitcnt vmcnt(0)\n\t"
      "s_nop 7\n\t"
      "s_branch NP2B_%=\n\t"
      "NP3R_%=:\n\t"
      "s_mov_b32 s51, 0\n\t"
      "NP3L_%=:\n\t"
      "s_add_u32 s51, s51, 1\n\t"
      "s_cmp_lt_u32 s51, 262144\n\t"
      "s_cbranch_scc0 NP3X_%=\n\t"
      "s_waitcnt vmcnt(0)\n\t"
      "s_sleep 1\n\t"
      "global_load_dwordx4 v[128:131], v176, s[58:59] sc1\n\t"
      "global_load_dwordx4 v[132:135], v177, s[58:59] sc1\n\t"
      "global_load_dwordx4 v[136:139], v178, s[58:59] sc1\n\t"
      "global_load_dwordx4 v[140:143], v179, s[58:59] sc1\n\t"
      "global_load_dwordx4 v[144:147], v180, s[58:59] sc1\n\t"
      "global_load_dwordx4 v[148:151], v181, s[58:59] sc1\n\t"
      "global_load_dwordx4 v[152:155], v182, s[58:59] sc1\n\t"
      "global_load_dwordx4 v[156:159], v183, s[58:59] sc1\n\t"
      "s_waitcnt vmcnt(0)\n\t"
      "v_max3_u32 v234, v128, v129, v130\n\t"
      "v_max3_u32 v234, v234, v131, v132\n\t"
      "v_max3_u32 v234, v234, v133, v134\n\t"
      "v_max3_u32 v234, v234, v135, v136\n\t"
      "v_max3_u32 v234, v234, v137, v138\n\t"
      "v_max3_u32 v234, v234, v139, v140\n\t"
      "v_max3_u32 v234, v234, v141, v142\n\t"
      "v_max3_u32 v234, v234, v143, v144\n\t"
      "v_max3_u32 v234, v234, v145, v146\n\t"
      "v_max3_u32 v234, v234, v147, v148\n\t"
      "v_max3_u32 v234, v234, v149, v150\n\t"
      "v_max3_u32 v234, v234, v151, v152\n\t"
      "v_max3_u32 v234, v234, v153, v154\n\t"
      "v_max3_u32 v234, v234, v155, v156\n\t"
      "v_max3_u32 v234, v234, v157, v158\n\t"
      "v_max_u32_e32 v234, v234, v159\n\t"
      "v_cmp_eq_u32_e32 vcc, -1, v234\n\t"
      "s_cbranch_vccnz NP3L_%=\n\t"
      "NP3X_%=:\n\t"
      "v_mfma_f32_16x16x32_bf16 v[200:203], v[0:3], v[128:131], 0\n\t"
      "v_mfma_f32_16x16x32_bf16 v[204:207], v[4:7], v[128:131], 0\n\t"
      "v_mfma_f32_16x16x32_bf16 v[208:211], v[8:11], v[128:131], 0\n\t"
      "v_mfma_f32_16x16x32_bf16 v[212:215], v[12:15], v[128:131], 0\n\t"
      "v_mfma_f32_16x16x32_bf16 v[200:203], v[16:19], v[132:135], v[200:203]\n\t"
      "v_mfma_f32_16x16x32_bf16 v[204:207], v[20:23], v[132:135], v[204:207]\n\t"
      "v_mfma_f32_16x16x32_bf16 v[208:211], v[24:27], v[132:135], v[208:211]\n\t"
      "v_mfma_f32_16x16x32_bf16 v[212:215], v[28:31], v[132:135], v[212:215]\n\t"
      "v_mfma_f32_16x16x32_bf16 v[200:203], v[32:35], v[136:139], v[200:203]\n\t"
      "v_mfma_f32_16x16x32_bf16 v[204:207], v[36:39], v[136:139], v[204:207]\n\t"
      "v_mfma_f32_16x16x32_bf16 v[208:211], v[40:43], v[136:139], v[208:211]\n\t"
      "v_mfma_f32_16x16x32_bf16 v[212:215], v[44:47], v[136:139], v[212:215]\n\t"
      "v_mfma_f32_16x16x32_bf16 v[200:203], v[48:51], v[140:143], v[200:203]\n\t"
      "v_mfma_f32_16x16x32_bf16 v[204:207], v[52:55], v[140:143], v[204:207]\n\t"
      "v_mfma_f32_16x16x32_bf16 v[208:211], v[56:59], v[140:143], v[208:211]\n\t"
      "v_mfma_f32_16x16x32_bf16 v[212:215], v[60:63], v[140:143], v[212:215]\n\t"
      "v_mfma_f32_16x16x32_bf16 v[200:203], v[64:67], v[144:147], v[200:203]\n\t"
      "v_mfma_f32_16x16x32_bf16 v[204:207], v[68:71], v[144:147], v[204:207]\n\t"
      "v_mfma_f32_16x16x32_bf16 v[208:211], v[72:75], v[144:147], v[208:211]\n\t"
      "v_mfma_f32_16x16x32_bf16 v[212:215], v[76:79], v[144:147], v[212:215]\n\t"
      "v_mfma_f32_16x16x32_bf16 v[200:203], v[80:83], v[148:151], v[200:203]\n\t"
      "v_mfma_f32_16x16x32_bf16 v[204:207], v[84:87], v[148:151], v[204:207]\n\t"
      "v_mfma_f32_16x16x32_bf16 v[208:211], v[88:91], v[148:151], v[208:211]\n\t"
      "v_mfma_f32_16x16x32_bf16 v[212:215], v[92:95], v[148:151], v[212:215]\n\t"
      "v_mfma_f32_16x16x32_bf16 v[200:203], v[96:99], v[152:155], v[200:203]\n\t"
      "v_mfma_f32_16x16x32_bf16 v[204:207], v[100:103], v[152:155], v[204:207]\n\t"
      "v_mfma_f32_16x16x32_bf16 v[208:211], v[104:107], v[152:155], v[208:211]\n\t"
      "v_mfma_f32_16x16x32_bf16 v[212:215], v[108:111], v[152:155], v[212:215]\n\t"
      "v_mfma_f32_16x16x32_bf16 v[200:203], v[112:115], v[156:159], v[200:203]\n\t"
      "v_mfma_f32_16x16x32_bf16 v[204:207], v[116:119], v[156:159], v[204:207]\n\t"
      "v_mfma_f32_16x16x32_bf16 v[208:211], v[120:123], v[156:159], v[208:211]\n\t"
      "v_mfma_f32_16x16x32_bf16 v[212:215], v[124:127], v[156:159], v[212:215]\n\t"
      "global_load_dwordx4 v[128:131], v176, s[60:61] sc1\n\t"
      "global_load_dwordx4 v[132:135], v177, s[60:61] sc1\n\t"
      "global_load_dwordx4 v[136:139], v178, s[60:61] sc1\n\t"
      "global_load_dwordx4 v[140:143], v179, s[60:61] sc1\n\t"
      "global_load_dwordx4 v[144:147], v180, s[60:61] sc1\n\t"
      "global_load_dwordx4 v[148:151], v181, s[60:61] sc1\n\t"
      "global_load_dwordx4 v[152:155], v182, s[60:61] sc1\n\t"
      "global_load_dwordx4 v[156:159], v183, s[60:61] sc1\n\t"
      "s_waitcnt vmcnt(0)\n\t"
      "s_nop 7\n\t"
      "s_branch NP3B_%=\n\t"
      "NP4R_%=:\n\t"
      "s_mov_b32 s51, 0\n\t"
      "NP4L_%=:\n\t"
      "s_add_u32 s51, s51, 1\n\t"
      "s_cmp_lt_u32 s51, 262144\n\t"
      "s_cbranch_scc0 NP4X_%=\n\t"
      "s_waitcnt vmcnt(0)\n\t"
      "s_sleep 1\n\t"
      "global_load_dwordx4 v[128:131], v176, s[60:61] sc1\n\t"
      "global_load_dwordx4 v[132:135], v177, s[60:61] sc1\n\t"
      "global_load_dwordx4 v[136:139], v178, s[60:61] sc1\n\t"
      "global_load_dwordx4 v[140:143], v179, s[60:61] sc1\n\t"
      "global_load_dwordx4 v[144:147], v180, s[60:61] sc1\n\t"
      "global_load_dwordx4 v[148:151], v181, s[60:61] sc1\n\t"
      "global_load_dwordx4 v[152:155], v182, s[60:61] sc1\n\t"
      "global_load_dwordx4 v[156:159], v183, s[60:61] sc1\n\t"
      "s_waitcnt vmcnt(0)\n\t"
      "v_max3_u32 v234, v128, v129, v130\n\t"
      "v_max3_u32 v234, v234, v131, v132\n\t"
      "v_max3_u32 v234, v234, v133, v134\n\t"
      "v_max3_u32 v234, v234, v135, v136\n\t"
      "v_max3_u32 v234, v234, v137, v138\n\t"
      "v_max3_u32 v234, v234, v139, v140\n\t"
      "v_max3_u32 v234, v234, v141, v142\n\t"
      "v_max3_u32 v234, v234, v143, v144\n\t"
      "v_max3_u32 v234, v234, v145, v146\n\t"
      "v_max3_u32 v234, v234, v147, v148\n\t"
      "v_max3_u32 v234, v234, v149, v150\n\t"
      "v_max3_u32 v234, v234, v151, v152\n\t"
      "v_max3_u32 v234, v234, v153, v154\n\t"
      "v_max3_u32 v234, v234, v155, v156\n\t"
      "v_max3_u32 v234, v234, v157, v158\n\t"
      "v_max_u32_e32 v234, v234, v159\n\t"
      "v_cmp_eq_u32_e32 vcc, -1, v234\n\t"
      "s_cbranch_vccnz NP4L_%=\n\t"
      "NP4X_%=:\n\t"
      "v_mfma_f32_16x16x32_bf16 v[184:187], v[0:3], v[128:131], 0\n\t"
      "v_mfma_f32_16x16x32_bf16 v[188:191], v[4:7], v[128:131], 0\n\t"
      "v_mfma_f32_16x16x32_bf16 v[192:195], v[8:11], v[128:131], 0\n\t"
      "v_mfma_f32_16x16x32_bf16 v[196:199], v[12:15], v[128:131], 0\n\t"
      "v_mfma_f32_16x16x32_bf16 v[184:187], v[16:19], v[132:135], v[184:187]\n\t"
      "v_mfma_f32_16x16x32_bf16 v[188:191], v[20:23], v[132:135], v[188:191]\n\t"
      "v_mfma_f32_16x16x32_bf16 v[192:195], v[24:27], v[132:135], v[192:195]\n\t"
      "v_mfma_f32_16x16x32_bf16 v[196:199], v[28:31], v[132:135], v[196:199]\n\t"
      "v_mfma_f32_16x16x32_bf16 v[184:187], v[32:35], v[136:139], v[184:187]\n\t"
      "v_mfma_f32_16x16x32_bf16 v[188:191], v[36:39], v[136:139], v[188:191]\n\t"
      "v_mfma_f32_16x16x32_bf16 v[192:195], v[40:43], v[136:139], v[192:195]\n\t"
      "v_mfma_f32_16x16x32_bf16 v[196:199], v[44:47], v[136:139], v[196:199]\n\t"
      "v_mfma_f32_16x16x32_bf16 v[184:187], v[48:51], v[140:143], v[184:187]\n\t"
      "v_mfma_f32_16x16x32_bf16 v[188:191], v[52:55], v[140:143], v[188:191]\n\t"
      "v_mfma_f32_16x16x32_bf16 v[192:195], v[56:59], v[140:143], v[192:195]\n\t"
      "v_mfma_f32_16x16x32_bf16 v[196:199], v[60:63], v[140:143], v[196:199]\n\t"
      "v_mfma_f32_16x16x32_bf16 v[184:187], v[64:67], v[144:147], v[184:187]\n\t"
      "v_mfma_f32_16x16x32_bf16 v[188:191], v[68:71], v[144:147], v[188:191]\n\t"
      "v_mfma_f32_16x16x32_bf16 v[192:195], v[72:75], v[144:147], v[192:195]\n\t"
      "v_mfma_f32_16x16x32_bf16 v[196:199], v[76:79], v[144:147], v[196:199]\n\t"
      "v_mfma_f32_16x16x32_bf16 v[184:187], v[80:83], v[148:151], v[184:187]\n\t"
      "v_mfma_f32_16x16x32_bf16 v[188:191], v[84:87], v[148:151], v[188:191]\n\t"
      "v_mfma_f32_16x16x32_bf16 v[192:195], v[88:91], v[148:151], v[192:195]\n\t"
      "v_mfma_f32_16x16x32_bf16 v[196:199], v[92:95], v[148:151], v[196:199]\n\t"
      "v_mfma_f32_16x16x32_bf16 v[184:187], v[96:99], v[152:155], v[184:187]\n\t"
      "v_mfma_f32_16x16x32_bf16 v[188:191], v[100:103], v[152:155], v[188:191]\n\t"
      "v_mfma_f32_16x16x32_bf16 v[192:195], v[104:107], v[152:155], v[192:195]\n\t"
      "v_mfma_f32_16x16x32_bf16 v[196:199], v[108:111], v[152:155], v[196:199]\n\t"
      "v_mfma_f32_16x16x32_bf16 v[184:187], v[112:115], v[156:159], v[184:187]\n\t"
      "v_mfma_f32_16x16x32_bf16 v[188:191], v[116:119], v[156:159], v[188:191]\n\t"
      "v_mfma_f32_16x16x32_bf16 v[192:195], v[120:123], v[156:159], v[192:195]\n\t"
      "v_mfma_f32_16x16x32_bf16 v[196:199], v[124:127], v[156:159], v[196:199]\n\t"
      "global_load_dwordx4 v[128:131], v176, s[62:63] sc1\n\t"
      "global_load_dwordx4 v[132:135], v177, s[62:63] sc1\n\t"
      "global_load_dwordx4 v[136:139], v178, s[62:63] sc1\n\t"
      "global_load_dwordx4 v[140:143], v179, s[62:63] sc1\n\t"
      "global_load_dwordx4 v[144:147], v180, s[62:63] sc1\n\t"
      "global_load_dwordx4 v[148:151], v181, s[62:63] sc1\n\t"
      "global_load_dwordx4 v[152:155], v182, s[62:63] sc1\n\t"
      "global_load_dwordx4 v[156:159], v183, s[62:63] sc1\n\t"
      "s_waitcnt vmcnt(0)\n\t"
      "s_nop 7\n\t"
      "s_branch NP4B_%=\n\t"
      "NP5R_%=:\n\t"
      "s_mov_b32 s51, 0\n\t"
      "NP5L_%=:\n\t"
      "s_add_u32 s51, s51, 1\n\t"
      "s_cmp_lt_u32 s51, 262144\n\t"
      "s_cbranch_scc0 NP5X_%=\n\t"
      "s_waitcnt vmcnt(0)\n\t"
      "s_sleep 1\n\t"
      "global_load_dwordx4 v[128:131], v176, s[62:63] sc1\n\t"
      "global_load_dwordx4 v[132:135], v177, s[62:63] sc1\n\t"
      "global_load_dwordx4 v[136:139], v178, s[62:63] sc1\n\t"
      "global_load_dwordx4 v[140:143], v179, s[62:63] sc1\n\t"
      "global_load_dwordx4 v[144:147], v180, s[62:63] sc1\n\t"
      "global_load_dwordx4 v[148:151], v181, s[62:63] sc1\n\t"
      "global_load_dwordx4 v[152:155], v182, s[62:63] sc1\n\t"
      "global_load_dwordx4 v[156:159], v183, s[62:63] sc1\n\t"
      "s_waitcnt vmcnt(0)\n\t"
      "v_max3_u32 v234, v128, v129, v130\n\t"
      "v_max3_u32 v234, v234, v131, v132\n\t"
      "v_max3_u32 v234, v234, v133, v134\n\t"
      "v_max3_u32 v234, v234, v135, v136\n\t"
      "v_max3_u32 v234, v234, v137, v138\n\t"
      "v_max3_u32 v234, v234, v139, v140\n\t"
      "v_max3_u32 v234, v234, v141, v142\n\t"
      "v_max3_u32 v234, v234, v143, v144\n\t"
      "v_max3_u32 v234, v234, v145, v146\n\t"
      "v_max3_u32 v234, v234, v147, v148\n\t"
      "v_max3_u32 v234, v234, v149, v150\n\t"
      "v_max3_u32 v234, v234, v151, v152\n\t"
      "v_max3_u32 v234, v234, v153, v154\n\t"
      "v_max3_u32 v234, v234, v155, v156\n\t"
      "v_max3_u32 v234, v234, v157, v158\n\t"
      "v_max_u32_e32 v234, v234, v159\n\t"
      "v_cmp_eq_u32_e32 vcc, -1, v234\n\t"
      "s_cbranch_vccnz NP5L_%=\n\t"
      "NP5X_%=:\n\t"
      "v_mfma_f32_16x16x32_bf16 v[200:203], v[0:3], v[128:131], 0\n\t"
      "v_mfma_f32_16x16x32_bf16 v[204:207], v[4:7], v[128:131], 0\n\t"
      "v_mfma_f32_16x16x32_bf16 v[208:211], v[8:11], v[128:131], 0\n\t"
      "v_mfma_f32_16x16x32_bf16 v[212:215], v[12:15], v[128:131], 0\n\t"
      "v_mfma_f32_16x16x32_bf16 v[200:203], v[16:19], v[132:135], v[200:203]\n\t"
      "v_mfma_f32_16x16x32_bf16 v[204:207], v[20:23], v[132:135], v[204:207]\n\t"
      "v_mfma_f32_16x16x32_bf16 v[208:211], v[24:27], v[132:135], v[208:211]\n\t"
      "v_mfma_f32_16x16x32_bf16 v[212:215], v[28:31], v[132:135], v[212:215]\n\t"
      "v_mfma_f32_16x16x32_bf16 v[200:203], v[32:35], v[136:139], v[200:203]\n\t"
      "v_mfma_f32_16x16x32_bf16 v[204:207], v[36:39], v[136:139], v[204:207]\n\t"
      "v_mfma_f32_16x16x32_bf16 v[208:211], v[40:43], v[136:139], v[208:211]\n\t"
      "v_mfma_f32_16x16x32_bf16 v[212:215], v[44:47], v[136:139], v[212:215]\n\t"
      "v_mfma_f32_16x16x32_bf16 v[200:203], v[48:51], v[140:143], v[200:203]\n\t"
      "v_mfma_f32_16x16x32_bf16 v[204:207], v[52:55], v[140:143], v[204:207]\n\t"
      "v_mfma_f32_16x16x32_bf16 v[208:211], v[56:59], v[140:143], v[208:211]\n\t"
      "v_mfma_f32_16x16x32_bf16 v[212:215], v[60:63], v[140:143], v[212:215]\n\t"
      "v_mfma_f32_16x16x32_bf16 v[200:203], v[64:67], v[144:147], v[200:203]\n\t"
      "v_mfma_f32_16x16x32_bf16 v[204:207], v[68:71], v[144:147], v[204:207]\n\t"
      "v_mfma_f32_16x16x32_bf16 v[208:211], v[72:75], v[144:147], v[208:211]\n\t"
      "v_mfma_f32_16x16x32_bf16 v[212:215], v[76:79], v[144:147], v[212:215]\n\t"
      "v_mfma_f32_16x16x32_bf16 v[200:203], v[80:83], v[148:151], v[200:203]\n\t"
      "v_mfma_f32_16x16x32_bf16 v[204:207], v[84:87], v[148:151], v[204:207]\n\t"
      "v_mfma_f32_16x16x32_bf16 v[208:211], v[88:91], v[148:151], v[208:211]\n\t"
      "v_mfma_f32_16x16x32_bf16 v[212:215], v[92:95], v[148:151], v[212:215]\n\t"
      "v_mfma_f32_16x16x32_bf16 v[200:203], v[96:99], v[152:155], v[200:203]\n\t"
      "v_mfma_f32_16x16x32_bf16 v[204:207], v[100:103], v[152:155], v[204:207]\n\t"
      "v_mfma_f32_16x16x32_bf16 v[208:211], v[104:107], v[152:155], v[208:211]\n\t"
      "v_mfma_f32_16x16x32_bf16 v[212:215], v[108:111], v[152:155], v[212:215]\n\t"
      "v_mfma_f32_16x16x32_bf16 v[200:203], v[112:115], v[156:159], v[200:203]\n\t"
      "v_mfma_f32_16x16x32_bf16 v[204:207], v[116:119], v[156:159], v[204:207]\n\t"
      "v_mfma_f32_16x16x32_bf16 v[208:211], v[120:123], v[156:159], v[208:211]\n\t"
      "v_mfma_f32_16x16x32_bf16 v[212:215], v[124:127], v[156:159], v[212:215]\n\t"
      "global_load_dwordx4 v[128:131], v176, s[64:65] sc1\n\t"
      "global_load_dwordx4 v[132:135], v177, s[64:65] sc1\n\t"
      "global_load_dwordx4 v[136:139], v178, s[64:65] sc1\n\t"
      "global_load_dwordx4 v[140:143], v179, s[64:65] sc1\n\t"
      "global_load_dwordx4 v[144:147], v180, s[64:65] sc1\n\t"
      "global_load_dwordx4 v[148:151], v181, s[64:65] sc1\n\t"
      "global_load_dwordx4 v[152:155], v182, s[64:65] sc1\n\t"
      "global_load_dwordx4 v[156:159], v183, s[64:65] sc1\n\t"
      "s_waitcnt vmcnt(0)\n\t"
      "s_nop 7\n\t"
      "s_branch NP5B_%=\n\t"
      "NP6R_%=:\n\t"
      "s_mov_b32 s51, 0\n\t"
      "NP6L_%=:\n\t"
      "s_add_u32 s51, s51, 1\n\t"
      "s_cmp_lt_u32 s51, 262144\n\t"
      "s_cbranch_scc0 NP6X_%=\n\t"
      "s_waitcnt vmcnt(0)\n\t"
      "s_sleep 1\n\t"
      "global_load_dwordx4 v[128:131], v176, s[64:65] sc1\n\t"
      "global_load_dwordx4 v[132:135], v177, s[64:65] sc1\n\t"
      "global_load_dwordx4 v[136:139], v178, s[64:65] sc1\n\t"
      "global_load_dwordx4 v[140:143], v179, s[64:65] sc1\n\t"
      "global_load_dwordx4 v[144:147], v180, s[64:65] sc1\n\t"
      "global_load_dwordx4 v[148:151], v181, s[64:65] sc1\n\t"
      "global_load_dwordx4 v[152:155], v182, s[64:65] sc1\n\t"
      "global_load_dwordx4 v[156:159], v183, s[64:65] sc1\n\t"
      "s_waitcnt vmcnt(0)\n\t"
      "v_max3_u32 v234, v128, v129, v130\n\t"
      "v_max3_u32 v234, v234, v131, v132\n\t"
      "v_max3_u32 v234, v234, v133, v134\n\t"
      "v_max3_u32 v234, v234, v135, v136\n\t"
      "v_max3_u32 v234, v234, v137, v138\n\t"
      "v_max3_u32 v234, v234, v139, v140\n\t"
      "v_max3_u32 v234, v234, v141, v142\n\t"
      "v_max3_u32 v234, v234, v143, v144\n\t"
      "v_max3_u32 v234, v234, v145, v146\n\t"
      "v_max3_u32 v234, v234, v147, v148\n\t"
      "v_max3_u32 v234, v234, v149, v150\n\t"
      "v_max3_u32 v234, v234, v151, v152\n\t"
      "v_max3_u32 v234, v234, v153, v154\n\t"
      "v_max3_u32 v234, v234, v155, v156\n\t"
      "v_max3_u32 v234, v234, v157, v158\n\t"
      "v_max_u32_e32 v234, v234, v159\n\t"
      "v_cmp_eq_u32_e32 vcc, -1, v234\n\t"
      "s_cbranch_vccnz NP6L_%=\n\t"
      "NP6X_%=:\n\t"
      "v_mfma_f32_16x16x32_bf16 v[184:187], v[0:3], v[128:131], 0\n\t"
      "v_mfma_f32_16x16x32_bf16 v[188:191], v[4:7], v[128:131], 0\n\t"
      "v_mfma_f32_16x16x32_bf16 v[192:195], v[8:11], v[128:131], 0\n\t"
      "v_mfma_f32_16x16x32_bf16 v[196:199], v[12:15], v[128:131], 0\n\t"
      "v_mfma_f32_16x16x32_bf16 v[184:187], v[16:19], v[132:135], v[184:187]\n\t"
      "v_mfma_f32_16x16x32_bf16 v[188:191], v[20:23], v[132:135], v[188:191]\n\t"
      "v_mfma_f32_16x16x32_bf16 v[192:195], v[24:27], v[132:135], v[192:195]\n\t"
      "v_mfma_f32_16x16x32_bf16 v[196:199], v[28:31], v[132:135], v[196:199]\n\t"
      "v_mfma_f32_16x16x32_bf16 v[184:187], v[32:35], v[136:139], v[184:187]\n\t"
      "v_mfma_f32_16x16x32_bf16 v[188:191], v[36:39], v[136:139], v[188:191]\n\t"
      "v_mfma_f32_16x16x32_bf16 v[192:195], v[40:43], v[136:139], v[192:195]\n\t"
      "v_mfma_f32_16x16x32_bf16 v[196:199], v[44:47], v[136:139], v[196:199]\n\t"
      "v_mfma_f32_16x16x32_bf16 v[184:187], v[48:51], v[140:143], v[184:187]\n\t"
      "v_mfma_f32_16x16x32_bf16 v[188:191], v[52:55], v[140:143], v[188:191]\n\t"
      "v_mfma_f32_16x16x32_bf16 v[192:195], v[56:59], v[140:143], v[192:195]\n\t"
      "v_mfma_f32_16x16x32_bf16 v[196:199], v[60:63], v[140:143], v[196:199]\n\t"
      "v_mfma_f32_16x16x32_bf16 v[184:187], v[64:67], v[144:147], v[184:187]\n\t"
      "v_mfma_f32_16x16x32_bf16 v[188:191], v[68:71], v[144:147], v[188:191]\n\t"
      "v_mfma_f32_16x16x32_bf16 v[192:195], v[72:75], v[144:147], v[192:195]\n\t"
      "v_mfma_f32_16x16x32_bf16 v[196:199], v[76:79], v[144:147], v[196:199]\n\t"
      "v_mfma_f32_16x16x32_bf16 v[184:187], v[80:83], v[148:151], v[184:187]\n\t"
      "v_mfma_f32_16x16x32_bf16 v[188:191], v[84:87], v[148:151], v[188:191]\n\t"
      "v_mfma_f32_16x16x32_bf16 v[192:195], v[88:91], v[148:151], v[192:195]\n\t"
      "v_mfma_f32_16x16x32_bf16 v[196:199], v[92:95], v[148:151], v[196:199]\n\t"
      "v_mfma_f32_16x16x32_bf16 v[184:187], v[96:99], v[152:155], v[184:187]\n\t"
      "v_mfma_f32_16x16x32_bf16 v[188:191], v[100:103], v[152:155], v[188:191]\n\t"
      "v_mfma_f32_16x16x32_bf16 v[192:195], v[104:107], v[152:155], v[192:195]\n\t"
      "v_mfma_f32_16x16x32_bf16 v[196:199], v[108:111], v[152:155], v[196:199]\n\t"
      "v_mfma_f32_16x16x32_bf16 v[184:187], v[112:115], v[156:159], v[184:187]\n\t"
      "v_mfma_f32_16x16x32_bf16 v[188:191], v[116:119], v[156:159], v[188:191]\n\t"
      "v_mfma_f32_16x16x32_bf16 v[192:195], v[120:123], v[156:159], v[192:195]\n\t"
      "v_mfma_f32_16x16x32_bf16 v[196:199], v[124:127], v[156:159], v[196:199]\n\t"
      "s_waitcnt vmcnt(0)\n\t"
      "s_nop 7\n\t"
      "s_branch NP6B_%=\n\t"
      : "+{s[4:19]}"(s), "+{s[20:21]}"(kr), "+{s[22:23]}"(hint), "+{v[240:247]}"(v)
      :
      : "memory", "vcc", "scc",
        "v0", "v1", "v2", "v3", "v4", "v5", "v6", "v7", "v8", "v9", "v10", "v11", "v12", "v13", "v14", "v15", "v16",
        "v17", "v18", "v19", "v20", "v21", "v22", "v23", "v24", "v25", "v26", "v27", "v28", "v29", "v30", "v31", "v32",
        "v33", "v34", "v35", "v36", "v37", "v38", "v39", "v40", "v41", "v42", "v43", "v44", "v45", "v46", "v47", "v48",
        "v49", "v50", "v51", "v52", "v53", "v54", "v55", "v56", "v57", "v58", "v59", "v60", "v61", "v62", "v63", "v64",
        "v65", "v66", "v67", "v68", "v69", "v70", "v71", "v72", "v73", "v74", "v75", "v76", "v77", "v78", "v79", "v80",
        "v81", "v82", "v83", "v84", "v85", "v86", "v87", "v88", "v89", "v90", "v91", "v92", "v93", "v94", "v95", "v96",
        "v97", "v98", "v99", "v100", "v101", "v102", "v103", "v104", "v105", "v106", "v107", "v108", "v109", "v110",
        "v111", "v112", "v113", "v114", "v115", "v116", "v117", "v118", "v119", "v120", "v121", "v122", "v123", "v124",
        "v125", "v126", "v127", "v128", "v129", "v130", "v131", "v132", "v133", "v134", "v135", "v136", "v137", "v138",
        "v139", "v140", "v141", "v142", "v143", "v144", "v145", "v146", "v147", "v148", "v149", "v150", "v151", "v152",
        "v153", "v154", "v155", "v156", "v157", "v158", "v159", "v160", "v161", "v162", "v163", "v164", "v165", "v166",
        "v167", "v168", "v169", "v170", "v171", "v172", "v173", "v174", "v175", "v176", "v177", "v178", "v179", "v180",
        "v181", "v182", "v183", "v184", "v185", "v186", "v187", "v188", "v189", "v190", "v191", "v192", "v193", "v194",
        "v195", "v196", "v197", "v198", "v199", "v200", "v201", "v202", "v203", "v204", "v205", "v206", "v207", "v208",
        "v209", "v210", "v211", "v212", "v213", "v214", "v215", "v216", "v217", "v218", "v219", "v220", "v221", "v222",
        "v223", "v224", "v225", "v226", "v227", "v228", "v229", "v230", "v231", "v232", "v233", "v234", "v235", "v236",
        "v237", "v238", "v239", "v248", "v249", "v250", "v251", "v252", "v253", "s44", "s45", "s46", "s47", "s48",
        "s49", "s50", "s51", "s52", "s53", "s54", "s55", "s56", "s57", "s58", "s59", "s60", "s61", "s62", "s63", "s64",
        "s65", "s66", "s67", "s68", "s69", "s70"
    );
    __builtin_amdgcn_endpgm();
  } else {
    const int ib = idx - 16, t = 16 * ((xcd + XO) & 7) + ib;
    const int st = 8 * xp + (ib >> 1), blk0 = 7 * (2 * side + (ib & 1));
    const int rot = R4 == 0 ? 4 * (ib & 1) + 2 * side - (ib & side & 1) : R4 == 1 ? 3 + 2 * side : R4 == 2 ? 3 : (3 * (ib & 1) + (ib >> 1) + 2 * side) % 6, rn = (rot + W2E) % 7;
    const unsigned bits = UNI((wave >= 4 ? 1 : 0) | ((wave >= 4) != HP4 ? 2 : 0) | (wave == 0 ? 16 : 0) |
                              (t == 0 && wave == 0 ? 32 : 0) | (wave < 7 && t < 112 ? 64 : 0));
    u32x4 vo;
    unsigned vq, par = ST_PW + xcd * 128;
    {
      LANE_BITS(threadIdx.x & 63);
      const int cb0 = wave < 4 ? 4 * wave : 4 + 3 * wave;
      const int k0 = (ib + 2 * xcd) % KROT * KMUL;
      u32x16 v, s;
      u32x8 s2;
      v[0] = (8 * (wave & 3) + lr) * 896 + (wave >> 2) * 128 + ((lc ^ swz_tr(8 * wave + lr)) << 4) + 4096;
#pragma unroll
      for (int b = 0; b < 4; ++b) {
        const int cb = min(cb0 + b, 27);
        v[1 + b] = g * B_UNIT + (cb >> 2) * 1024 + q4 * 128 + (((2 * (cb & 3) + (p4 >> 1)) ^ swz_tr(8 * g + q4)) << 4) + (p4 & 1) * 8;
        const int by = 112 * wave + 56 * (b >> 1) + 32 * (b & 1) + 8 * g - 64 * k0;
        vo[b] = B_X + c * B_OP + (by < 0 ? by + 896 : by);
      }
      v[5] = B_X + (4 * g + q4) * 896 + cb0 * 32 + p4 * 8;
      v[6] = B_X + c * B_OP + 16 * g;
      v[7] = unit(cb0, t >> 4, 0) + (g & 1) * 131072 + (t & 15) * 512 + (g >> 1) * 256 + c * 16;
      v[13] = v[7] - unit(cb0, 0, 0) + unit(cb0 + 2, 0, 0);
      v[8] = B_PS + (4 * g + q4) * 64 + wave * 8;
      v[9] = B_PS + (4 * g + p4) * 64 + q4 * 16;
      v[10] = (t * 16 + 4 * g + p4) * 16;
      v[11] = lane * 16 + ((2 * (t >> 2) + 1) % 7 ? 1024 : 16384 - 6144);
      v[12] = lane * 16;
      v[14] = (wave * 224 + 96 + 64 * (side ^ (lane >> 5)) + ib * 4 + (lr & 3)) * 4096 + lc * 128 + xp * 1024;
      v[15] = ST_H3 + t * 128 + lc * 16384;
      put(s, 0, (u64)w2);
      put(s, 2, (u64)(wo + k0 * 32 * 896 - 4096));
      put(s, 4, (u64)(ws + WS_ST));
      put(s, 6, (u64)(ws + WS_O + (t * 16 + 2 * wave) * 1024));
      put(s, 10, (u64)(ws + WS_HN));
      put(s, 12, (u64)(ws + WS_G + unit(7 * (t & 3), 2 * (t >> 2) / 7, 0) + wave * 131072 + 2 * (t >> 2) % 7 * 1024));
      put(s, 14, (u64)(w2 + st * 128 + (wave * 224 + rot * 32) * 4096));
      unsigned vn = lr * 4096 + ((lc ^ swz_tr(lr)) << 4), lw = wave * G_WV;
      vq = ST_H2 + ((((t >> 3) * 8 + wave) * 2 + ((t >> 2) & 1)) * 2 + (lane & 1)) * 64;
      unsigned vck = ST_CK + (((t >> 2) * 16 + 2 * wave + (g >> 1)) * 4 + (t & 3)) * 64 + c * 4;
      s2[0] = bits;
      s2[1] = (wave & 3) * B_UNIT + (wave >> 2) * 1024;
      s2[2] = UNI(wave < 4 ? ~0u : 0u);
      s2[3] = 13 - k0;
      put2(s2, 4, wave < 4 ? ~0u : 0xffffu, wave < 4 ? ~0u : 0xffffu);
    asm volatile(
      "s_waitcnt lgkmcnt(0)\n\ts_nop 4\n\t"
      "s_load_dwordx2 s[26:27], s[8:9], 0x20000\n\t"
      "v_mov_b32_e32 v248, -1\n\t"
      "v_mov_b32_e32 v249, -1\n\t"
      "v_mov_b32_e32 v250, -1\n\t"
      "v_mov_b32_e32 v251, -1\n\t"
      "v_add_u32_e32 v221, 4294965504, v224\n\t"
      "v_add_u32_e32 v222, 512, v224\n\t"
      "v_add_u32_e32 v223, 4294966016, v224\n\t"
      "s_bitcmp1_b32 s20, 0\n\t"
      "s_cselect_b64 s[46:47], -1, 0\n\t"
      "s_mov_b64 vcc, s[46:47]\n\t"
      "s_mov_b32 s44, 0xffffffff\n\t"
      "s_mov_b32 s45, 0xffff\n\t"
      "s_waitcnt vmcnt(1)\n\t"
      "s_barrier\n\t"
      "ds_read_b64_tr_b16 v[204:205], v229\n\t"
      "ds_read_b64_tr_b16 v[206:207], v229 offset:32\n\t"
      "ds_read_b64_tr_b16 v[208:209], v229 offset:64\n\t"
      "ds_read_b64_tr_b16 v[210:211], v229 offset:96\n\t"
      "s_waitcnt lgkmcnt(0)\n\t"
      "s_add_u32 s26, s26, 1\n\t"
      "s_addc_u32 s27, s27, 0\n\t"
      "s_mul_i32 s48, s26, 0x9e3779b9\n\t"
      "s_mul_hi_u32 s13, s26, 0x7f4a7c15\n\t"
      "s_add_u32 s13, s13, s48\n\t"
      "s_mul_i32 s48, s27, 0x7f4a7c15\n\t"
      "s_add_u32 s13, s13, s48\n\t"
      "s_mul_i32 s12, s26, 0x7f4a7c15\n\t"
      "s_add_u32 s12, s12, 0xd192ed03\n\t"
      "s_addc_u32 s13, s13, 0xd1b54a32\n\t"
      "v_mov_b32_e32 v188, s12\n\t"
      "v_mov_b32_e32 v189, s13\n\t"
      "v_mov_b32_e32 v190, s26\n\t"
      "v_mov_b32_e32 v191, s27\n\t"
      "v_mov_b32_e32 v192, 163312\n\t"
      "s_mov_b64 exec, 1\n\t"
      "ds_write_b128 v192, v[188:191]\n\t"
      "s_mov_b64 exec, -1\n\t"
      "v_and_b32_e32 v210, s22, v210\n\t"
      "v_and_b32_e32 v211, s22, v211\n\t"
      "v_lshlrev_b32_e32 v160, 16, v204\n\t"
      "v_and_b32_e32 v161, 0xffff0000, v204\n\t"
      "v_lshlrev_b32_e32 v162, 16, v205\n\t"
      "v_and_b32_e32 v163, 0xffff0000, v205\n\t"
      "v_lshlrev_b32_e32 v164, 16, v206\n\t"
      "v_and_b32_e32 v165, 0xffff0000, v206\n\t"
      "v_lshlrev_b32_e32 v166, 16, v207\n\t"
      "v_and_b32_e32 v167, 0xffff0000, v207\n\t"
      "v_lshlrev_b32_e32 v168, 16, v208\n\t"
      "v_and_b32_e32 v169, 0xffff0000, v208\n\t"
      "v_lshlrev_b32_e32 v170, 16, v209\n\t"
      "v_and_b32_e32 v171, 0xffff0000, v209\n\t"
      "v_lshlrev_b32_e32 v172, 16, v210\n\t"
      "v_and_b32_e32 v173, 0xffff0000, v210\n\t"
      "v_lshlrev_b32_e32 v174, 16, v211\n\t"
      "v_and_b32_e32 v175, 0xffff0000, v211\n\t"
      "v_pk_add_f32 v[188:189], v[160:161], v[164:165]\n\t"
      "v_pk_mul_f32 v[192:193], v[160:161], v[160:161]\n\t"
      "v_pk_add_f32 v[190:191], v[162:163], v[166:167]\n\t"
      "v_pk_mul_f32 v[194:195], v[162:163], v[162:163]\n\t"
      "v_pk_fma_f32 v[192:193], v[164:165], v[164:165], v[192:193]\n\t"
      "v_pk_fma_f32 v[194:195], v[166:167], v[166:167], v[194:195]\n\t"
      "v_pk_add_f32 v[188:189], v[188:189], v[168:169]\n\t"
      "v_pk_fma_f32 v[192:193], v[168:169], v[168:169], v[192:193]\n\t"
      "v_pk_add_f32 v[190:191], v[190:191], v[170:171]\n\t"
      "v_pk_fma_f32 v[194:195], v[170:171], v[170:171], v[194:195]\n\t"
      "v_pk_add_f32 v[188:189], v[188:189], v[172:173]\n\t"
      "v_pk_fma_f32 v[192:193], v[172:173], v[172:173], v[192:193]\n\t"
      "v_pk_add_f32 v[190:191], v[190:191], v[174:175]\n\t"
      "v_pk_fma_f32 v[194:195], v[174:175], v[174:175], v[194:195]\n\t"
      "v_add_f32_dpp v196, v188, v188 row_ror:8 row_mask:0xf bank_mask:0x3\n\t"
      "s_nop 1\n\t"
      "v_add_f32_dpp v196, v190, v190 row_ror:8 row_mask:0xf bank_mask:0xc\n\t"
      "v_add_f32_dpp v197, v189, v189 row_ror:8 row_mask:0xf bank_mask:0x3\n\t"
      "s_nop 1\n\t"
      "v_add_f32_dpp v197, v191, v191 row_ror:8 row_mask:0xf bank_mask:0xc\n\t"
      "v_add_f32_dpp v198, v192, v192 row_ror:8 row_mask:0xf bank_mask:0x3\n\t"
      "s_nop 1\n\t"
      "v_add_f32_dpp v198, v194, v194 row_ror:8 row_mask:0xf bank_mask:0xc\n\t"
      "v_add_f32_dpp v199, v193, v193 row_ror:8 row_mask:0xf bank_mask:0x3\n\t"
      "s_nop 1\n\t"
      "v_add_f32_dpp v199, v195, v195 row_ror:8 row_mask:0xf bank_mask:0xc\n\t"
      "v_add_f32_dpp v188, v196, v196 row_shl:4 row_mask:0xf bank_mask:0x5\n\t"
      "s_nop 1\n\t"
      "v_add_f32_dpp v188, v197, v197 row_shr:4 row_mask:0xf bank_mask:0xa\n\t"
      "v_add_f32_dpp v189, v198, v198 row_shl:4 row_mask:0xf bank_mask:0x5\n\t"
      "s_nop 1\n\t"
      "v_add_f32_dpp v189, v199, v199 row_shr:4 row_mask:0xf bank_mask:0xa\n\t"
      "v_add_f32_dpp v188, v188, v188 quad_perm:[1,0,3,2] row_mask:0xf bank_mask:0xf\n\t"
      "s_nop 0\n\t"
      "v_add_f32_dpp v189, v189, v189 quad_perm:[1,0,3,2] row_mask:0xf bank_mask:0xf\n\t"
      "v_add_f32_dpp v188, v188, v188 quad_perm:[2,3,0,1] row_mask:0xf bank_mask:0xf\n\t"
      "s_nop 0\n\t"
      "v_add_f32_dpp v189, v189, v189 quad_perm:[2,3,0,1] row_mask:0xf bank_mask:0xf\n\t"
      "ds_write_b64 v232, v[188:189]\n\t"
      "s_add_u32 m0, s21, 0\n\t"
      "s_nop 0\n\t"
      "global_load_lds_dwordx4 v224, s[6:7]\n\t"
      "global_load_lds_dwordx4 v221, s[6:7] offset:2048\n\t"
      "s_add_u32 m0, s21, 4096\n\t"
      "s_nop 0\n\t"
      "global_load_lds_dwordx4 v222, s[6:7]\n\t"
      "s_cbranch_vccnz WR0_%=\n\t"
      "global_load_lds_dwordx4 v223, s[6:7] offset:2048\n\t"
      "WR0_%=:\n\t"
      "s_sub_u32 s23, s23, 1\n\t"
      "s_cselect_b32 s48, -13, 1\n\t"
      "s_cselect_b32 s49, -1, 0\n\t"
      "s_mul_i32 s48, s48, 28672\n\t"
      "s_add_u32 s6, s6, s48\n\t"
      "s_addc_u32 s7, s7, s49\n\t"
      "s_add_u32 m0, s21, 28672\n\t"
      "s_nop 0\n\t"
      "global_load_lds_dwordx4 v224, s[6:7]\n\t"
      "global_load_lds_dwordx4 v221, s[6:7] offset:2048\n\t"
      "s_add_u32 m0, s21, 32768\n\t"
      "s_nop 0\n\t"
      "global_load_lds_dwordx4 v222, s[6:7]\n\t"
      "s_cbranch_vccnz WR1_%=\n\t"
      "global_load_lds_dwordx4 v223, s[6:7] offset:2048\n\t"
      "WR1_%=:\n\t"
      "s_sub_u32 s23, s23, 1\n\t"
      "s_cselect_b32 s48, -13, 1\n\t"
      "s_cselect_b32 s49, -1, 0\n\t"
      "s_mul_i32 s48, s48, 28672\n\t"
      "s_add_u32 s6, s6, s48\n\t"
      "s_addc_u32 s7, s7, s49\n\t"
      "s_waitcnt lgkmcnt(0)\n\t"
      "s_barrier\n\t"
      "ds_read_b128 v[188:191], v233\n\t"
      "s_waitcnt lgkmcnt(0)\n\t"
      "v_pk_add_f32 v[188:189], v[188:189], v[190:191]\n\t"
      "s_nop 1\n\t"
      "v_add_f32_dpp v188, v188, v188 row_ror:4 row_mask:0xf bank_mask:0xf\n\t"
      "v_add_f32_dpp v189, v189, v189 row_ror:4 row_mask:0xf bank_mask:0xf\n\t"
      "s_nop 0\n\t"
      "v_add_f32_dpp v188, v188, v188 row_ror:8 row_mask:0xf bank_mask:0xf\n\t"
      "v_add_f32_dpp v189, v189, v189 row_ror:8 row_mask:0xf bank_mask:0xf\n\t"
      "v_mul_f32_e32 v188, 0x3b124925, v188\n\t"
      "v_mul_f32_e32 v189, 0x3b124925, v189\n\t"
      "v_fma_f32 v189, -v188, v188, v189\n\t"
      "v_max_f32_e32 v189, 0, v189\n\t"
      "s_bitcmp1_b32 s20, 4\n\t"
      "s_cbranch_scc0 GW_%=\n\t"
      "v_add_f32_e32 v189, 0x3727c5ac, v189\n\t"
      "v_rsq_f32_e32 v189, v189\n\t"
      "v_xor_b32_e32 v188, 0x80000000, v188\n\t"
      "v_xor_b32_e32 v190, s12, v188\n\t"
      "v_xor_b32_e32 v191, s13, v189\n\t"
      "s_mov_b32 s48, 0xf000f\n\t"
      "s_mov_b32 s49, 0xf000f\n\t"
      "s_mov_b64 exec, s[48:49]\n\t"
      "global_store_dwordx4 v234, v[188:191], s[8:9] sc1\n\t"
      "s_mov_b64 exec, -1\n\t"
      "GW_%=:\n\t"
      "global_store_dwordx4 v231, v[248:251], s[14:15] sc1\n\t"
      "s_mov_b64 exec, s[24:25]\n\t"
      "global_store_dwordx4 v237, v[248:251], s[14:15] sc1\n\t"
      "s_mov_b64 exec, -1\n\t"
      "s_cbranch_vccnz WW0_%=\n\t"
      "s_waitcnt vmcnt(6)\n\t"
      "s_branch WX0_%=\n\t"
      "WW0_%=:\n\t"
      "s_waitcnt vmcnt(5)\n\t"
      "WX0_%=:\n\t"
      "s_waitcnt lgkmcnt(0)\n\t"
      "s_barrier\n\t"
      "ds_read_b64_tr_b16 v[0:1], v225\n\t"
      "ds_read_b64_tr_b16 v[2:3], v225 offset:512\n\t"
      "ds_read_b64_tr_b16 v[4:5], v226\n\t"
      "ds_read_b64_tr_b16 v[6:7], v226 offset:512\n\t"
      "ds_read_b64_tr_b16 v[8:9], v227\n\t"
      "ds_read_b64_tr_b16 v[10:11], v227 offset:512\n\t"
      "ds_read_b64_tr_b16 v[12:13], v228\n\t"
      "ds_read_b64_tr_b16 v[14:15], v228 offset:512\n\t"
      "s_add_u32 m0, s21, 57344\n\t"
      "s_nop 0\n\t"
      "global_load_lds_dwordx4 v224, s[6:7]\n\t"
      "global_load_lds_dwordx4 v221, s[6:7] offset:2048\n\t"
      "s_add_u32 m0, s21, 61440\n\t"
      "s_nop 0\n\t"
      "global_load_lds_dwordx4 v222, s[6:7]\n\t"
      "s_cbranch_vccnz WR2_%=\n\t"
      "global_load_lds_dwordx4 v223, s[6:7] offset:2048\n\t"
      "WR2_%=:\n\t"
      "s_sub_u32 s23, s23, 1\n\t"
      "s_cselect_b32 s48, -13, 1\n\t"
      "s_cselect_b32 s49, -1, 0\n\t"
      "s_mul_i32 s48, s48, 28672\n\t"
      "s_add_u32 s6, s6, s48\n\t"
      "s_addc_u32 s7, s7, s49\n\t"
      "s_cbranch_vccnz WW1_%=\n\t"
      "s_waitcnt vmcnt(6)\n\t"
      "s_branch WX1_%=\n\t"
      "WW1_%=:\n\t"
      "s_waitcnt vmcnt(5)\n\t"
      "WX1_%=:\n\t"
      "s_waitcnt lgkmcnt(0)\n\t"
      "s_barrier\n\t"
      "ds_read_b64_tr_b16 v[16:17], v225 offset:28672\n\t"
      "ds_read_b64_tr_b16 v[18:19], v225 offset:29184\n\t"
      "ds_read_b64_tr_b16 v[20:21], v226 offset:28672\n\t"
      "ds_read_b64_tr_b16 v[22:23], v226 offset:29184\n\t"
      "ds_read_b64_tr_b16 v[24:25], v227 offset:28672\n\t"
      "ds_read_b64_tr_b16 v[26:27], v227 offset:29184\n\t"
      "ds_read_b64_tr_b16 v[28:29], v228 offset:28672\n\t"
      "ds_read_b64_tr_b16 v[30:31], v228 offset:29184\n\t"
      "s_add_u32 m0, s21, 86016\n\t"
      "s_nop 0\n\t"
      "global_load_lds_dwordx4 v224, s[6:7]\n\t"
      "global_load_lds_dwordx4 v221, s[6:7] offset:2048\n\t"
      "s_add_u32 m0, s21, 90112\n\t"
      "s_nop 0\n\t"
      "global_load_lds_dwordx4 v222, s[6:7]\n\t"
      "s_cbranch_vccnz WR3_%=\n\t"
      "global_load_lds_dwordx4 v223, s[6:7] offset:2048\n\t"
      "WR3_%=:\n\t"
      "s_sub_u32 s23, s23, 1\n\t"
      "s_cselect_b32 s48, -13, 1\n\t"
      "s_cselect_b32 s49, -1, 0\n\t"
      "s_mul_i32 s48, s48, 28672\n\t"
      "s_add_u32 s6, s6, s48\n\t"
      "s_addc_u32 s7, s7, s49\n\t"
      "v_add_u32_e32 v225, 57344, v225\n\t"
      "v_add_u32_e32 v226, 57344, v226\n\t"
      "v_add_u32_e32 v227, 57344, v227\n\t"
      "v_add_u32_e32 v228, 57344, v228\n\t"
      "s_cbranch_vccnz WW2_%=\n\t"
      "s_waitcnt vmcnt(4)\n\t"
      "s_branch WX2_%=\n\t"
      "WW2_%=:\n\t"
      "s_waitcnt vmcnt(3)\n\t"
      "WX2_%=:\n\t"
      "s_waitcnt lgkmcnt(0)\n\t"
      "s_barrier\n\t"
      "ds_read_b64_tr_b16 v[32:33], v225\n\t"
      "ds_read_b64_tr_b16 v[34:35], v225 offset:512\n\t"
      "ds_read_b64_tr_b16 v[36:37], v226\n\t"
      "ds_read_b64_tr_b16 v[38:39], v226 offset:512\n\t"
      "ds_read_b64_tr_b16 v[40:41], v227\n\t"
      "ds_read_b64_tr_b16 v[42:43], v227 offset:512\n\t"
      "ds_read_b64_tr_b16 v[44:45], v228\n\t"
      "ds_read_b64_tr_b16 v[46:47], v228 offset:512\n\t"
      "s_add_u32 m0, s21, 0\n\t"
      "s_nop 0\n\t"
      "global_load_lds_dwordx4 v224, s[6:7]\n\t"
      "global_load_lds_dwordx4 v221, s[6:7] offset:2048\n\t"
      "s_add_u32 m0, s21, 4096\n\t"
      "s_nop 0\n\t"
      "global_load_lds_dwordx4 v222, s[6:7]\n\t"
      "s_cbranch_vccnz WR4_%=\n\t"
      "global_load_lds_dwordx4 v223, s[6:7] offset:2048\n\t"
      "WR4_%=:\n\t"
      "s_sub_u32 s23, s23, 1\n\t"
      "s_cselect_b32 s48, -13, 1\n\t"
      "s_cselect_b32 s49, -1, 0\n\t"
      "s_mul_i32 s48, s48, 28672\n\t"
      "s_add_u32 s6, s6, s48\n\t"
      "s_addc_u32 s7, s7, s49\n\t"
      "s_cbranch_vccnz WW3_%=\n\t"
      "s_waitcnt vmcnt(4)\n\t"
      "s_branch WX3_%=\n\t"
      "WW3_%=:\n\t"
      "s_waitcnt vmcnt(3)\n\t"
      "WX3_%=:\n\t"
      "s_waitcnt lgkmcnt(0)\n\t"
      "s_barrier\n\t"
      "ds_read_b64_tr_b16 v[48:49], v225 offset:28672\n\t"
      "ds_read_b64_tr_b16 v[50:51], v225 offset:29184\n\t"
      "ds_read_b64_tr_b16 v[52:53], v226 offset:28672\n\t"
      "ds_read_b64_tr_b16 v[54:55], v226 offset:29184\n\t"
      "ds_read_b64_tr_b16 v[56:57], v227 offset:28672\n\t"
      "ds_read_b64_tr_b16 v[58:59], v227 offset:29184\n\t"
      "ds_read_b64_tr_b16 v[60:61], v228 offset:28672\n\t"
      "ds_read_b64_tr_b16 v[62:63], v228 offset:29184\n\t"
      "s_add_u32 m0, s21, 28672\n\t"
      "s_nop 0\n\t"
      "global_load_lds_dwordx4 v224, s[6:7]\n\t"
      "global_load_lds_dwordx4 v221, s[6:7] offset:2048\n\t"
      "s_add_u32 m0, s21, 32768\n\t"
      "s_nop 0\n\t"
      "global_load_lds_dwordx4 v222, s[6:7]\n\t"
      "s_cbranch_vccnz WR5_%=\n\t"
      "global_load_lds_dwordx4 v223, s[6:7] offset:2048\n\t"
      "WR5_%=:\n\t"
      "s_sub_u32 s23, s23, 1\n\t"
      "s_cselect_b32 s48, -13, 1\n\t"
      "s_cselect_b32 s49, -1, 0\n\t"
      "s_mul_i32 s48, s48, 28672\n\t"
      "s_add_u32 s6, s6, s48\n\t"
      "s_addc_u32 s7, s7, s49\n\t"
      "v_add_u32_e32 v225, 4294909952, v225\n\t"
      "v_add_u32_e32 v226, 4294909952, v226\n\t"
      "v_add_u32_e32 v227, 4294909952, v227\n\t"
      "v_add_u32_e32 v228, 4294909952, v228\n\t"
      "s_cbranch_vccnz WW4_%=\n\t"
      "s_waitcnt vmcnt(4)\n\t"
      "s_branch WX4_%=\n\t"
      "WW4_%=:\n\t"
      "s_waitcnt vmcnt(3)\n\t"
      "WX4_%=:\n\t"
      "s_waitcnt lgkmcnt(0)\n\t"
      "s_barrier\n\t"
      "ds_read_b64_tr_b16 v[64:65], v225\n\t"
      "ds_read_b64_tr_b16 v[66:67], v225 offset:512\n\t"
      "ds_read_b64_tr_b16 v[68:69], v226\n\t"
      "ds_read_b64_tr_b16 v[70:71], v226 offset:512\n\t"
      "ds_read_b64_tr_b16 v[72:73], v227\n\t"
      "ds_read_b64_tr_b16 v[74:75], v227 offset:512\n\t"
      "ds_read_b64_tr_b16 v[76:77], v228\n\t"
      "ds_read_b64_tr_b16 v[78:79], v228 offset:512\n\t"
      "s_add_u32 m0, s21, 57344\n\t"
      "s_nop 0\n\t"
      "global_load_lds_dwordx4 v224, s[6:7]\n\t"
      "global_load_lds_dwordx4 v221, s[6:7] offset:2048\n\t"
      "s_add_u32 m0, s21, 61440\n\t"
      "s_nop 0\n\t"
      "global_load_lds_dwordx4 v222, s[6:7]\n\t"
      "s_cbranch_vccnz WR6_%=\n\t"
      "global_load_lds_dwordx4 v223, s[6:7] offset:2048\n\t"
      "WR6_%=:\n\t"
      "s_sub_u32 s23, s23, 1\n\t"
      "s_cselect_b32 s48, -13, 1\n\t"
      "s_cselect_b32 s49, -1, 0\n\t"
      "s_mul_i32 s48, s48, 28672\n\t"
      "s_add_u32 s6, s6, s48\n\t"
      "s_addc_u32 s7, s7, s49\n\t"
      "s_cbranch_vccnz WW5_%=\n\t"
      "s_waitcnt vmcnt(4)\n\t"
      "s_branch WX5_%=\n\t"
      "WW5_%=:\n\t"
      "s_waitcnt vmcnt(3)\n\t"
      "WX5_%=:\n\t"
      "s_waitcnt lgkmcnt(0)\n\t"
      "s_barrier\n\t"
      "ds_read_b64_tr_b16 v[80:81], v225 offset:28672\n\t"
      "ds_read_b64_tr_b16 v[82:83], v225 offset:29184\n\t"
      "ds_read_b64_tr_b16 v[84:85], v226 offset:28672\n\t"
      "ds_read_b64_tr_b16 v[86:87], v226 offset:29184\n\t"
      "ds_read_b64_tr_b16 v[88:89], v227 offset:28672\n\t"
      "ds_read_b64_tr_b16 v[90:91], v227 offset:29184\n\t"
      "ds_read_b64_tr_b16 v[92:93], v228 offset:28672\n\t"
      "ds_read_b64_tr_b16 v[94:95], v228 offset:29184\n\t"
      "s_add_u32 m0, s21, 86016\n\t"
      "s_nop 0\n\t"
      "global_load_lds_dwordx4 v224, s[6:7]\n\t"
      "global_load_lds_dwordx4 v221, s[6:7] offset:2048\n\t"
      "s_add_u32 m0, s21, 90112\n\t"
      "s_nop 0\n\t"
      "global_load_lds_dwordx4 v222, s[6:7]\n\t"
      "s_cbranch_vccnz WR7_%=\n\t"
      "global_load_lds_dwordx4 v223, s[6:7] offset:2048\n\t"
      "WR7_%=:\n\t"
      "s_sub_u32 s23, s23, 1\n\t"
      "s_cselect_b32 s48, -13, 1\n\t"
      "s_cselect_b32 s49, -1, 0\n\t"
      "s_mul_i32 s48, s48, 28672\n\t"
      "s_add_u32 s6, s6, s48\n\t"
      "s_addc_u32 s7, s7, s49\n\t"
      "v_add_u32_e32 v225, 57344, v225\n\t"
      "v_add_u32_e32 v226, 57344, v226\n\t"
      "v_add_u32_e32 v227, 57344, v227\n\t"
      "v_add_u32_e32 v228, 57344, v228\n\t"
      "s_cbranch_vccnz WW6_%=\n\t"
      "s_waitcnt vmcnt(4)\n\t"
      "s_branch WX6_%=\n\t"
      "WW6_%=:\n\t"
      "s_waitcnt vmcnt(3)\n\t"
      "WX6_%=:\n\t"
      "s_waitcnt lgkmcnt(0)\n\t"
      "s_barrier\n\t"
      "ds_read_b64_tr_b16 v[96:97], v225\n\t"
      "ds_read_b64_tr_b16 v[98:99], v225 offset:512\n\t"
      "ds_read_b64_tr_b16 v[100:101], v226\n\t"
      "ds_read_b64_tr_b16 v[102:103], v226 offset:512\n\t"
      "ds_read_b64_tr_b16 v[104:105], v227\n\t"
      "ds_read_b64_tr_b16 v[106:107], v227 offset:512\n\t"
      "ds_read_b64_tr_b16 v[108:109], v228\n\t"
      "ds_read_b64_tr_b16 v[110:111], v228 offset:512\n\t"
      "s_add_u32 m0, s21, 0\n\t"
      "s_nop 0\n\t"
      "global_load_lds_dwordx4 v224, s[6:7]\n\t"
      "global_load_lds_dwordx4 v221, s[6:7] offset:2048\n\t"
      "s_add_u32 m0, s21, 4096\n\t"
      "s_nop 0\n\t"
      "global_load_lds_dwordx4 v222, s[6:7]\n\t"
      "s_cbranch_vccnz WR8_%=\n\t"
      "global_load_lds_dwordx4 v223, s[6:7] offset:2048\n\t"
      "WR8_%=:\n\t"
      "s_sub_u32 s23, s23, 1\n\t"
      "s_cselect_b32 s48, -13, 1\n\t"
      "s_cselect_b32 s49, -1, 0\n\t"
      "s_mul_i32 s48, s48, 28672\n\t"
      "s_add_u32 s6, s6, s48\n\t"
      "s_addc_u32 s7, s7, s49\n\t"
      "s_cbranch_vccnz WW7_%=\n\t"
      "s_waitcnt vmcnt(4)\n\t"
      "s_branch WX7_%=\n\t"
      "WW7_%=:\n\t"
      "s_waitcnt vmcnt(3)\n\t"
      "WX7_%=:\n\t"
      "s_waitcnt lgkmcnt(0)\n\t"
      "s_barrier\n\t"
      "ds_read_b64_tr_b16 v[112:113], v225 offset:28672\n\t"
      "ds_read_b64_tr_b16 v[114:115], v225 offset:29184\n\t"
      "ds_read_b64_tr_b16 v[116:117], v226 offset:28672\n\t"
      "ds_read_b64_tr_b16 v[118:119], v226 offset:29184\n\t"
      "ds_read_b64_tr_b16 v[120:121], v227 offset:28672\n\t"
      "ds_read_b64_tr_b16 v[122:123], v227 offset:29184\n\t"
      "ds_read_b64_tr_b16 v[124:125], v228 offset:28672\n\t"
      "ds_read_b64_tr_b16 v[126:127], v228 offset:29184\n\t"
      "s_add_u32 m0, s21, 28672\n\t"
      "s_nop 0\n\t"
      "global_load_lds_dwordx4 v224, s[6:7]\n\t"
      "global_load_lds_dwordx4 v221, s[6:7] offset:2048\n\t"
      "s_add_u32 m0, s21, 32768\n\t"
      "s_nop 0\n\t"
      "global_load_lds_dwordx4 v222, s[6:7]\n\t"
      "s_cbranch_vccnz WR9_%=\n\t"
      "global_load_lds_dwordx4 v223, s[6:7] offset:2048\n\t"
      "WR9_%=:\n\t"
      "s_sub_u32 s23, s23, 1\n\t"
      "s_cselect_b32 s48, -13, 1\n\t"
      "s_cselect_b32 s49, -1, 0\n\t"
      "s_mul_i32 s48, s48, 28672\n\t"
      "s_add_u32 s6, s6, s48\n\t"
      "s_addc_u32 s7, s7, s49\n\t"
      "v_add_u32_e32 v225, 4294909952, v225\n\t"
      "v_add_u32_e32 v226, 4294909952, v226\n\t"
      "v_add_u32_e32 v227, 4294909952, v227\n\t"
      "v_add_u32_e32 v228, 4294909952, v228\n\t"
      "s_cbranch_vccnz WW8_%=\n\t"
      "s_waitcnt vmcnt(4)\n\t"
      "s_branch WX8_%=\n\t"
      "WW8_%=:\n\t"
      "s_waitcnt vmcnt(3)\n\t"
      "WX8_%=:\n\t"
      "s_waitcnt lgkmcnt(0)\n\t"
      "s_barrier\n\t"
      "ds_read_b64_tr_b16 v[128:129], v225\n\t"
      "ds_read_b64_tr_b16 v[130:131], v225 offset:512\n\t"
      "ds_read_b64_tr_b16 v[132:133], v226\n\t"
      "ds_read_b64_tr_b16 v[134:135], v226 offset:512\n\t"
      "ds_read_b64_tr_b16 v[136:137], v227\n\t"
      "ds_read_b64_tr_b16 v[138:139], v227 offset:512\n\t"
      "ds_read_b64_tr_b16 v[140:141], v228\n\t"
      "ds_read_b64_tr_b16 v[142:143], v228 offset:512\n\t"
      "s_add_u32 m0, s21, 57344\n\t"
      "s_nop 0\n\t"
      "global_load_lds_dwordx4 v224, s[6:7]\n\t"
      "global_load_lds_dwordx4 v221, s[6:7] offset:2048\n\t"
      "s_add_u32 m0, s21, 61440\n\t"
      "s_nop 0\n\t"
      "global_load_lds_dwordx4 v222, s[6:7]\n\t"
      "s_cbranch_vccnz WR10_%=\n\t"
      "global_load_lds_dwordx4 v223, s[6:7] offset:2048\n\t"
      "WR10_%=:\n\t"
      "s_sub_u32 s23, s23, 1\n\t"
      "s_cselect_b32 s48, -13, 1\n\t"
      "s_cselect_b32 s49, -1, 0\n\t"
      "s_mul_i32 s48, s48, 28672\n\t"
      "s_add_u32 s6, s6, s48\n\t"
      "s_addc_u32 s7, s7, s49\n\t"
      "s_cbranch_vccnz WW9_%=\n\t"
      "s_waitcnt vmcnt(4)\n\t"
      "s_branch WX9_%=\n\t"
      "WW9_%=:\n\t"
      "s_waitcnt vmcnt(3)\n\t"
      "WX9_%=:\n\t"
      "s_waitcnt lgkmcnt(0)\n\t"
      "s_barrier\n\t"
      "ds_read_b64_tr_b16 v[144:145], v225 offset:28672\n\t"
      "ds_read_b64_tr_b16 v[146:147], v225 offset:29184\n\t"
      "ds_read_b64_tr_b16 v[148:149], v226 offset:28672\n\t"
      "ds_read_b64_tr_b16 v[150:151], v226 offset:29184\n\t"
      "ds_read_b64_tr_b16 v[152:153], v227 offset:28672\n\t"
      "ds_read_b64_tr_b16 v[154:155], v227 offset:29184\n\t"
      "ds_read_b64_tr_b16 v[156:157], v228 offset:28672\n\t"
      "ds_read_b64_tr_b16 v[158:159], v228 offset:29184\n\t"
      "s_add_u32 m0, s21, 86016\n\t"
      "s_nop 0\n\t"
      "global_load_lds_dwordx4 v224, s[6:7]\n\t"
      "global_load_lds_dwordx4 v221, s[6:7] offset:2048\n\t"
      "s_add_u32 m0, s21, 90112\n\t"
      "s_nop 0\n\t"
      "global_load_lds_dwordx4 v222, s[6:7]\n\t"
      "s_cbranch_vccnz WR11_%=\n\t"
      "global_load_lds_dwordx4 v223, s[6:7] offset:2048\n\t"
      "WR11_%=:\n\t"
      "s_sub_u32 s23, s23, 1\n\t"
      "s_cselect_b32 s48, -13, 1\n\t"
      "s_cselect_b32 s49, -1, 0\n\t"
      "s_mul_i32 s48, s48, 28672\n\t"
      "s_add_u32 s6, s6, s48\n\t"
      "s_addc_u32 s7, s7, s49\n\t"
      "v_add_u32_e32 v225, 57344, v225\n\t"
      "v_add_u32_e32 v226, 57344, v226\n\t"
      "v_add_u32_e32 v227, 57344, v227\n\t"
      "v_add_u32_e32 v228, 57344, v228\n\t"
      "s_waitcnt lgkmcnt(0)\n\t"
      "s_barrier\n\t"
      "s_add_u32 m0, s21, 0\n\t"
      "s_nop 0\n\t"
      "global_load_lds_dwordx4 v224, s[6:7]\n\t"
      "global_load_lds_dwordx4 v221, s[6:7] offset:2048\n\t"
      "s_add_u32 m0, s21, 4096\n\t"
      "s_nop 0\n\t"
      "global_load_lds_dwordx4 v222, s[6:7]\n\t"
      "s_cbranch_vccnz WR12_%=\n\t"
      "global_load_lds_dwordx4 v223, s[6:7] offset:2048\n\t"
      "WR12_%=:\n\t"
      "s_sub_u32 s23, s23, 1\n\t"
      "s_cselect_b32 s48, -13, 1\n\t"
      "s_cselect_b32 s49, -1, 0\n\t"
      "s_mul_i32 s48, s48, 28672\n\t"
      "s_add_u32 s6, s6, s48\n\t"
      "s_addc_u32 s7, s7, s49\n\t"
      "s_add_u32 m0, s21, 28672\n\t"
      "s_nop 0\n\t"
      "global_load_lds_dwordx4 v224, s[6:7]\n\t"
      "global_load_lds_dwordx4 v221, s[6:7] offset:2048\n\t"
      "s_add_u32 m0, s21, 32768\n\t"
      "s_nop 0\n\t"
      "global_load_lds_dwordx4 v222, s[6:7]\n\t"
      "s_cbranch_vccnz WR13_%=\n\t"
      "global_load_lds_dwordx4 v223, s[6:7] offset:2048\n\t"
      "WR13_%=:\n\t"
      "s_waitcnt vmcnt(0)\n\t"
      "s_mov_b32 s51, 0\n\t"
      "s_branch QE_%=\n\t"
      ".p2align 6\n\t"
      "QL_%=:\n\t"
      "s_sleep 2\n\t"
      "s_add_u32 s51, s51, 1\n\t"
      "s_cmp_lt_u32 s51, 262144\n\t"
      "s_cbranch_scc0 QX_%=\n\t"
      "QE_%=:\n\t"
      "s_mov_b64 exec, 3\n\t"
      "v_mov_b64_e32 v[188:189], 0\n\t"
      "global_atomic_or_x2 v[188:189], v246, v[188:189], s[8:9] sc0 sc1\n\t"
      "s_waitcnt vmcnt(0)\n\t"
      "v_cmp_ne_u64_e32 vcc, s[12:13], v[188:189]\n\t"
      "s_mov_b64 exec, -1\n\t"
      "s_cbranch_vccnz QL_%=\n\t"
      "QX_%=:\n\t"
      "s_mov_b32 s51, 0\n\t"
      "s_branch OE_%=\n\t"
      "OL_%=:\n\t"
      "s_sleep 2\n\t"
      "s_add_u32 s51, s51, 1\n\t"
      "s_cmp_lt_u32 s51, 262144\n\t"
      "s_cbranch_scc0 OX_%=\n\t"
      "OE_%=:\n\t"
      "v_mov_b32_e32 v252, s28\n\t"
      "global_load_dwordx4 v[220:223], v252, s[8:9] sc1\n\t"
      "global_load_dwordx4 v[188:191], v236, s[10:11] sc1\n\t"
      "global_load_dwordx4 v[192:195], v236, s[10:11] offset:1024 sc1\n\t"
      "global_load_dword v198, v247, s[8:9] sc1\n\t"
      "s_waitcnt vmcnt(2)\n\t"
      "v_add3_u32 v196, v188, v189, v190\n\t"
      "v_add_u32_e32 v196, v196, v191\n\t"
      "s_waitcnt vmcnt(1)\n\t"
      "v_add3_u32 v197, v192, v193, v194\n\t"
      "v_add_u32_e32 v197, v197, v195\n\t"
      "s_nop 1\n\t"
      "v_permlane32_swap_b32_e32 v196, v197\n\t"
      "v_add_u32_e32 v196, v196, v197\n\t"
      "v_mov_b32_e32 v197, v196\n\t"
      "s_nop 1\n\t"
      "v_permlane16_swap_b32_e32 v196, v197\n\t"
      "v_add3_u32 v196, v196, v197, s12\n\t"
      "s_waitcnt vmcnt(0)\n\t"
      "v_cmp_ne_u32_e32 vcc, v196, v198\n\t"
      "s_cbranch_vccnz OL_%=\n\t"
      "OX_%=:\n\t"
      "s_mov_b64 vcc, s[46:47]\n\t"
      "ds_write_b64 v240, v[188:189]\n\t"
      "ds_write_b64 v242, v[192:193]\n\t"
      "s_mov_b64 exec, s[44:45]\n\t"
      "ds_write_b64 v241, v[190:191]\n\t"
      "ds_write_b64 v243, v[194:195]\n\t"
      "s_mov_b64 exec, -1\n\t"
      "v_subrev_u32_e32 v222, s12, v222\n\t"
      "v_subrev_u32_e32 v253, s12, v220\n\t"
      "v_xor_b32_e32 v223, v223, v221\n\t"
      "v_xor_b32_e32 v254, s13, v221\n\t"
      "v_or_b32_e32 v223, v223, v254\n\t"
      "v_or_b32_e32 v254, v222, v253\n\t"
      "v_and_or_b32 v223, v254, -2, v223\n\t"
      "v_cmp_ne_u32_e32 vcc, 0, v223\n\t"
      "s_cbranch_vccnz PWR_%=\n\t"
      "PWB_%=:\n\t"
      "v_lshl_add_u32 v220, v222, 1, v220\n\t"
      "v_lshl_add_u32 v222, v222, 1, v253\n\t"
      "v_lshlrev_b32_e32 v253, 13, v253\n\t"
      "v_readfirstlane_b32 s28, v222\n\t"
      "v_xor_b32_e32 v231, v231, v253\n\t"
      "v_xor_b32_e32 v237, v237, v253\n\t"
      "s_mov_b64 vcc, s[46:47]\n\t"
      "s_waitcnt lgkmcnt(0)\n\t"
      "s_barrier\n\t"
      "ds_read_b128 v[176:179], v230\n\t"
      "ds_read_b128 v[180:183], v230 offset:64\n\t"
      "ds_read_b128 v[184:187], v230 offset:128\n\t"
      "s_waitcnt lgkmcnt(2)\n\t"
      "v_mfma_f32_16x16x32_bf16 v[160:163], v[176:179], v[0:3], v[160:163]\n\t"
      "v_mfma_f32_16x16x32_bf16 v[164:167], v[176:179], v[4:7], v[164:167]\n\t"
      "v_mfma_f32_16x16x32_bf16 v[168:171], v[176:179], v[8:11], v[168:171]\n\t"
      "s_cbranch_vccnz M0_%=\n\t"
      "v_mfma_f32_16x16x32_bf16 v[172:175], v[176:179], v[12:15], v[172:175]\n\t"
      "M0_%=:\n\t"
      "s_bitcmp1_b32 s20, 6\n\t"
      "s_cbranch_scc0 SG2_%=\n\t"
      "global_store_dwordx4 v236, v[248:251], s[16:17] sc1\n\t"
      "global_store_dwordx4 v235, v[248:251], s[16:17] sc1\n\t"
      "SG2_%=:\n\t"
      "ds_read_b128 v[176:179], v230 offset:192\n\t"
      "s_waitcnt lgkmcnt(2)\n\t"
      "v_mfma_f32_16x16x32_bf16 v[160:163], v[180:183], v[16:19], v[160:163]\n\t"
      "v_mfma_f32_16x16x32_bf16 v[164:167], v[180:183], v[20:23], v[164:167]\n\t"
      "v_mfma_f32_16x16x32_bf16 v[168:171], v[180:183], v[24:27], v[168:171]\n\t"
      "s_cbranch_vccnz M1_%=\n\t"
      "v_mfma_f32_16x16x32_bf16 v[172:175], v[180:183], v[28:31], v[172:175]\n\t"
      "M1_%=:\n\t"
      "ds_read_b128 v[180:183], v230 offset:256\n\t"
      "s_waitcnt lgkmcnt(2)\n\t"
      "v_mfma_f32_16x16x32_bf16 v[160:163], v[184:187], v[32:35], v[160:163]\n\t"
      "v_mfma_f32_16x16x32_bf16 v[164:167], v[184:187], v[36:39], v[164:167]\n\t"
      "v_mfma_f32_16x16x32_bf16 v[168:171], v[184:187], v[40:43], v[168:171]\n\t"
      "s_cbranch_vccnz M2_%=\n\t"
      "v_mfma_f32_16x16x32_bf16 v[172:175], v[184:187], v[44:47], v[172:175]\n\t"
      "M2_%=:\n\t"
      "ds_read_b64_tr_b16 v[188:189], v225\n\t"
      "ds_read_b64_tr_b16 v[190:191], v225 offset:512\n\t"
      "s_mov_b32 exec_lo, 0xffffffff\n\t"
      "s_mov_b32 exec_hi, 0x0\n\t"
      "s_mov_b32 m0, 163328\n\t"
      "s_nop 0\n\t"
      "global_load_lds_dword v238, s[4:5]\n\t"
      "s_mov_b64 exec, -1\n\t"
      "ds_read_b128 v[184:187], v230 offset:320\n\t"
      "s_waitcnt lgkmcnt(4)\n\t"
      "v_mfma_f32_16x16x32_bf16 v[160:163], v[176:179], v[48:51], v[160:163]\n\t"
      "v_mfma_f32_16x16x32_bf16 v[164:167], v[176:179], v[52:55], v[164:167]\n\t"
      "v_mfma_f32_16x16x32_bf16 v[168:171], v[176:179], v[56:59], v[168:171]\n\t"
      "s_cbranch_vccnz M3_%=\n\t"
      "v_mfma_f32_16x16x32_bf16 v[172:175], v[176:179], v[60:63], v[172:175]\n\t"
      "M3_%=:\n\t"
      "ds_read_b64_tr_b16 v[192:193], v226\n\t"
      "ds_read_b64_tr_b16 v[194:195], v226 offset:512\n\t"
      "ds_read_b128 v[176:179], v230 offset:384\n\t"
      "s_waitcnt lgkmcnt(6)\n\t"
      "v_mfma_f32_16x16x32_bf16 v[160:163], v[180:183], v[64:67], v[160:163]\n\t"
      "v_mfma_f32_16x16x32_bf16 v[164:167], v[180:183], v[68:71], v[164:167]\n\t"
      "v_mfma_f32_16x16x32_bf16 v[168:171], v[180:183], v[72:75], v[168:171]\n\t"
      "s_cbranch_vccnz M4_%=\n\t"
      "v_mfma_f32_16x16x32_bf16 v[172:175], v[180:183], v[76:79], v[172:175]\n\t"
      "M4_%=:\n\t"
      "ds_read_b64_tr_b16 v[196:197], v227\n\t"
      "ds_read_b64_tr_b16 v[198:199], v227 offset:512\n\t"
      "ds_read_b128 v[180:183], v230 offset:448\n\t"
      "s_waitcnt lgkmcnt(6)\n\t"
      "v_mfma_f32_16x16x32_bf16 v[160:163], v[184:187], v[80:83], v[160:163]\n\t"
      "v_mfma_f32_16x16x32_bf16 v[164:167], v[184:187], v[84:87], v[164:167]\n\t"
      "v_mfma_f32_16x16x32_bf16 v[168:171], v[184:187], v[88:91], v[168:171]\n\t"
      "s_cbranch_vccnz M5_%=\n\t"
      "v_mfma_f32_16x16x32_bf16 v[172:175], v[184:187], v[92:95], v[172:175]\n\t"
      "M5_%=:\n\t"
      "ds_read_b64_tr_b16 v[200:201], v228\n\t"
      "ds_read_b64_tr_b16 v[202:203], v228 offset:512\n\t"
      "ds_read_b128 v[184:187], v230 offset:512\n\t"
      "s_waitcnt lgkmcnt(6)\n\t"
      "v_mfma_f32_16x16x32_bf16 v[160:163], v[176:179], v[96:99], v[160:163]\n\t"
      "v_mfma_f32_16x16x32_bf16 v[164:167], v[176:179], v[100:103], v[164:167]\n\t"
      "v_mfma_f32_16x16x32_bf16 v[168:171], v[176:179], v[104:107], v[168:171]\n\t"
      "s_cbranch_vccnz M6_%=\n\t"
      "v_mfma_f32_16x16x32_bf16 v[172:175], v[176:179], v[108:111], v[172:175]\n\t"
      "M6_%=:\n\t"
      "ds_read_b64_tr_b16 v[204:205], v225 offset:28672\n\t"
      "ds_read_b64_tr_b16 v[206:207], v225 offset:29184\n\t"
      "ds_read_b128 v[176:179], v230 offset:576\n\t"
      "s_waitcnt lgkmcnt(6)\n\t"
      "v_mfma_f32_16x16x32_bf16 v[160:163], v[180:183], v[112:115], v[160:163]\n\t"
      "v_mfma_f32_16x16x32_bf16 v[164:167], v[180:183], v[116:119], v[164:167]\n\t"
      "v_mfma_f32_16x16x32_bf16 v[168:171], v[180:183], v[120:123], v[168:171]\n\t"
      "s_cbranch_vccnz M7_%=\n\t"
      "v_mfma_f32_16x16x32_bf16 v[172:175], v[180:183], v[124:127], v[172:175]\n\t"
      "M7_%=:\n\t"
      "ds_read_b64_tr_b16 v[208:209], v226 offset:28672\n\t"
      "ds_read_b64_tr_b16 v[210:211], v226 offset:29184\n\t"
      "ds_read_b128 v[180:183], v230 offset:640\n\t"
      "s_waitcnt lgkmcnt(6)\n\t"
      "v_mfma_f32_16x16x32_bf16 v[160:163], v[184:187], v[128:131], v[160:163]\n\t"
      "v_mfma_f32_16x16x32_bf16 v[164:167], v[184:187], v[132:135], v[164:167]\n\t"
      "v_mfma_f32_16x16x32_bf16 v[168:171], v[184:187], v[136:139], v[168:171]\n\t"
      "s_cbranch_vccnz M8_%=\n\t"
      "v_mfma_f32_16x16x32_bf16 v[172:175], v[184:187], v[140:143], v[172:175]\n\t"
      "M8_%=:\n\t"
      "ds_read_b64_tr_b16 v[212:213], v227 offset:28672\n\t"
      "ds_read_b64_tr_b16 v[214:215], v227 offset:29184\n\t"
      "ds_read_b128 v[184:187], v230 offset:704\n\t"
      "s_waitcnt lgkmcnt(6)\n\t"
      "v_mfma_f32_16x16x32_bf16 v[160:163], v[176:179], v[144:147], v[160:163]\n\t"
      "v_mfma_f32_16x16x32_bf16 v[164:167], v[176:179], v[148:151], v[164:167]\n\t"
      "v_mfma_f32_16x16x32_bf16 v[168:171], v[176:179], v[152:155], v[168:171]\n\t"
      "s_cbranch_vccnz M9_%=\n\t"
      "v_mfma_f32_16x16x32_bf16 v[172:175], v[176:179], v[156:159], v[172:175]\n\t"
      "M9_%=:\n\t"
      "ds_read_b64_tr_b16 v[216:217], v228 offset:28672\n\t"
      "ds_read_b64_tr_b16 v[218:219], v228 offset:29184\n\t"
      "ds_read_b128 v[176:179], v230 offset:768\n\t"
      "s_waitcnt lgkmcnt(6)\n\t"
      "v_mfma_f32_16x16x32_bf16 v[160:163], v[180:183], v[188:191], v[160:163]\n\t"
      "v_add_u32_e32 v225, 4294909952, v225\n\t"
      "v_add_u32_e32 v226, 4294909952, v226\n\t"
      "v_add_u32_e32 v227, 4294909952, v227\n\t"
      "v_add_u32_e32 v228, 4294909952, v228\n\t"
      "ds_read_b64_tr_b16 v[188:189], v225\n\t"
      "ds_read_b64_tr_b16 v[190:191], v225 offset:512\n\t"
      "v_mfma_f32_16x16x32_bf16 v[164:167], v[180:183], v[192:195], v[164:167]\n\t"
      "ds_read_b64_tr_b16 v[192:193], v226\n\t"
      "ds_read_b64_tr_b16 v[194:195], v226 offset:512\n\t"
      "v_mfma_f32_16x16x32_bf16 v[168:171], v[180:183], v[196:199], v[168:171]\n\t"
      "ds_read_b64_tr_b16 v[196:197], v227\n\t"
      "ds_read_b64_tr_b16 v[198:199], v227 offset:512\n\t"
      "s_cbranch_vccnz M10_%=\n\t"
      "v_mfma_f32_16x16x32_bf16 v[172:175], v[180:183], v[200:203], v[172:175]\n\t"
      "M10_%=:\n\t"
      "ds_read_b64_tr_b16 v[200:201], v228\n\t"
      "ds_read_b64_tr_b16 v[202:203], v228 offset:512\n\t"
      "ds_read_b128 v[180:183], v230 offset:832\n\t"
      "s_waitcnt lgkmcnt(12)\n\t"
      "v_mfma_f32_16x16x32_bf16 v[160:163], v[184:187], v[204:207], v[160:163]\n\t"
      "ds_read_b64_tr_b16 v[204:205], v225 offset:28672\n\t"
      "ds_read_b64_tr_b16 v[206:207], v225 offset:29184\n\t"
      "v_mfma_f32_16x16x32_bf16 v[164:167], v[184:187], v[208:211], v[164:167]\n\t"
      "ds_read_b64_tr_b16 v[208:209], v226 offset:28672\n\t"
      "ds_read_b64_tr_b16 v[210:211], v226 offset:29184\n\t"
      "v_mfma_f32_16x16x32_bf16 v[168:171], v[184:187], v[212:215], v[168:171]\n\t"
      "ds_read_b64_tr_b16 v[212:213], v227 offset:28672\n\t"
      "ds_read_b64_tr_b16 v[214:215], v227 offset:29184\n\t"
      "s_waitcnt lgkmcnt(14)\n\t"
      "s_cbranch_vccnz M11_%=\n\t"
      "v_mfma_f32_16x16x32_bf16 v[172:175], v[184:187], v[216:219], v[172:175]\n\t"
      "M11_%=:\n\t"
      "ds_read_b64_tr_b16 v[216:217], v228 offset:28672\n\t"
      "ds_read_b64_tr_b16 v[218:219], v228 offset:29184\n\t"
      "s_waitcnt lgkmcnt(14)\n\t"
      "s_waitcnt lgkmcnt(14)\n\t"
      "v_mfma_f32_16x16x32_bf16 v[160:163], v[176:179], v[188:191], v[160:163]\n\t"
      "s_waitcnt lgkmcnt(13)\n\t"
      "v_mfma_f32_16x16x32_bf16 v[164:167], v[176:179], v[192:195], v[164:167]\n\t"
      "s_waitcnt lgkmcnt(11)\n\t"
      "v_mfma_f32_16x16x32_bf16 v[168:171], v[176:179], v[196:199], v[168:171]\n\t"
      "s_waitcnt lgkmcnt(9)\n\t"
      "s_cbranch_vccnz M12_%=\n\t"
      "v_mfma_f32_16x16x32_bf16 v[172:175], v[176:179], v[200:203], v[172:175]\n\t"
      "M12_%=:\n\t"
      "s_waitcnt lgkmcnt(8)\n\t"
      "s_waitcnt lgkmcnt(6)\n\t"
      "v_mfma_f32_16x16x32_bf16 v[160:163], v[180:183], v[204:207], v[160:163]\n\t"
      "s_waitcnt lgkmcnt(4)\n\t"
      "v_mfma_f32_16x16x32_bf16 v[164:167], v[180:183], v[208:211], v[164:167]\n\t"
      "s_waitcnt lgkmcnt(2)\n\t"
      "v_mfma_f32_16x16x32_bf16 v[168:171], v[180:183], v[212:215], v[168:171]\n\t"
      "s_waitcnt lgkmcnt(0)\n\t"
      "s_cbranch_vccnz M13_%=\n\t"
      "v_mfma_f32_16x16x32_bf16 v[172:175], v[180:183], v[216:219], v[172:175]\n\t"
      "M13_%=:\n\t"
      "s_nop 3\n\t"
      "v_pk_add_f32 v[188:189], v[160:161], v[164:165]\n\t"
      "v_pk_mul_f32 v[192:193], v[160:161], v[160:161]\n\t"
      "v_pk_add_f32 v[190:191], v[162:163], v[166:167]\n\t"
      "v_pk_mul_f32 v[194:195], v[162:163], v[162:163]\n\t"
      "v_pk_fma_f32 v[192:193], v[164:165], v[164:165], v[192:193]\n\t"
      "v_pk_fma_f32 v[194:195], v[166:167], v[166:167], v[194:195]\n\t"
      "v_pk_add_f32 v[188:189], v[188:189], v[168:169]\n\t"
      "v_pk_fma_f32 v[192:193], v[168:169], v[168:169], v[192:193]\n\t"
      "v_pk_add_f32 v[190:191], v[190:191], v[170:171]\n\t"
      "v_pk_fma_f32 v[194:195], v[170:171], v[170:171], v[194:195]\n\t"
      "v_pk_add_f32 v[188:189], v[188:189], v[172:173]\n\t"
      "v_pk_fma_f32 v[192:193], v[172:173], v[172:173], v[192:193]\n\t"
      "v_pk_add_f32 v[190:191], v[190:191], v[174:175]\n\t"
      "v_pk_fma_f32 v[194:195], v[174:175], v[174:175], v[194:195]\n\t"
      "v_add_f32_dpp v196, v188, v188 row_ror:8 row_mask:0xf bank_mask:0x3\n\t"
      "s_nop 1\n\t"
      "v_add_f32_dpp v196, v190, v190 row_ror:8 row_mask:0xf bank_mask:0xc\n\t"
      "v_add_f32_dpp v197, v189, v189 row_ror:8 row_mask:0xf bank_mask:0x3\n\t"
      "s_nop 1\n\t"
      "v_add_f32_dpp v197, v191, v191 row_ror:8 row_mask:0xf bank_mask:0xc\n\t"
      "v_add_f32_dpp v198, v192, v192 row_ror:8 row_mask:0xf bank_mask:0x3\n\t"
      "s_nop 1\n\t"
      "v_add_f32_dpp v198, v194, v194 row_ror:8 row_mask:0xf bank_mask:0xc\n\t"
      "v_add_f32_dpp v199, v193, v193 row_ror:8 row_mask:0xf bank_mask:0x3\n\t"
      "s_nop 1\n\t"
      "v_add_f32_dpp v199, v195, v195 row_ror:8 row_mask:0xf bank_mask:0xc\n\t"
      "v_add_f32_dpp v188, v196, v196 row_shl:4 row_mask:0xf bank_mask:0x5\n\t"
      "s_nop 1\n\t"
      "v_add_f32_dpp v188, v197, v197 row_shr:4 row_mask:0xf bank_mask:0xa\n\t"
      "v_add_f32_dpp v189, v198, v198 row_shl:4 row_mask:0xf bank_mask:0x5\n\t"
      "s_nop 1\n\t"
      "v_add_f32_dpp v189, v199, v199 row_shr:4 row_mask:0xf bank_mask:0xa\n\t"
      "v_add_f32_dpp v188, v188, v188 quad_perm:[1,0,3,2] row_mask:0xf bank_mask:0xf\n\t"
      "s_nop 0\n\t"
      "v_add_f32_dpp v189, v189, v189 quad_perm:[1,0,3,2] row_mask:0xf bank_mask:0xf\n\t"
      "v_add_f32_dpp v188, v188, v188 quad_perm:[2,3,0,1] row_mask:0xf bank_mask:0xf\n\t"
      "s_nop 0\n\t"
      "v_add_f32_dpp v189, v189, v189 quad_perm:[2,3,0,1] row_mask:0xf bank_mask:0xf\n\t"
      "ds_write_b64 v232, v[188:189]\n\t"
      "v_xor_b32_e32 v1, 64, v245\n\t"
      "v_add_u32_e32 v2, 63488, v245\n\t"
      "v_add_u32_e32 v3, 95232, v1\n\t"
      "v_add_u32_e32 v1, 31744, v1\n\t"
      "s_waitcnt vmcnt(1)\n\t"
      "s_waitcnt lgkmcnt(0)\n\t"
      "s_barrier\n\t"
      "ds_read_b128 v[188:191], v233\n\t"
      "s_bitcmp1_b32 s20, 4\n\t"
      "s_cbranch_scc0 HI_%=\n\t"
      "s_mov_b64 exec, 255\n\t"
      "global_store_dwordx2 v239, v[220:221], s[8:9] sc1\n\t"
      "s_mov_b64 exec, -1\n\t"
      "HI_%=:\n\t"
      "s_waitcnt lgkmcnt(0)\n\t"
      "v_pk_add_f32 v[188:189], v[188:189], v[190:191]\n\t"
      "s_nop 1\n\t"
      "v_add_f32_dpp v188, v188, v188 row_ror:4 row_mask:0xf bank_mask:0xf\n\t"
      "v_add_f32_dpp v189, v189, v189 row_ror:4 row_mask:0xf bank_mask:0xf\n\t"
      "s_nop 0\n\t"
      "v_add_f32_dpp v188, v188, v188 row_ror:8 row_mask:0xf bank_mask:0xf\n\t"
      "v_add_f32_dpp v189, v189, v189 row_ror:8 row_mask:0xf bank_mask:0xf\n\t"
      "v_mul_f32_e32 v188, 0x3b124925, v188\n\t"
      "v_mul_f32_e32 v189, 0x3b124925, v189\n\t"
      "v_fma_f32 v189, -v188, v188, v189\n\t"
      "v_max_f32_e32 v189, 0, v189\n\t"
      "v_add_f32_e32 v190, 0x3727c5ac, v189\n\t"
      "v_fmac_f32_e32 v189, 0x3727c5ac, v190\n\t"
      "v_rsq_f32_e32 v191, v189\n\t"
      "s_nop 0\n\t"
      "v_mul_f32_e64 v190, -v188, v191\n\t"
      "v_mov_b32_dpp v192, v191 quad_perm:[0,0,0,0] row_mask:0xf bank_mask:0xf\n\t"
      "s_nop 0\n\t"
      "v_mov_b32_dpp v196, v190 quad_perm:[0,0,0,0] row_mask:0xf bank_mask:0xf\n\t"
      "v_mov_b32_dpp v193, v191 quad_perm:[1,1,1,1] row_mask:0xf bank_mask:0xf\n\t"
      "v_mov_b32_dpp v197, v190 quad_perm:[1,1,1,1] row_mask:0xf bank_mask:0xf\n\t"
      "v_mov_b32_dpp v194, v191 quad_perm:[2,2,2,2] row_mask:0xf bank_mask:0xf\n\t"
      "v_mov_b32_dpp v198, v190 quad_perm:[2,2,2,2] row_mask:0xf bank_mask:0xf\n\t"
      "v_mov_b32_dpp v195, v191 quad_perm:[3,3,3,3] row_mask:0xf bank_mask:0xf\n\t"
      "v_mov_b32_dpp v199, v190 quad_perm:[3,3,3,3] row_mask:0xf bank_mask:0xf\n\t"
      "v_pk_fma_f32 v[160:161], v[160:161], v[192:193], v[196:197]\n\t"
      "v_pk_fma_f32 v[162:163], v[162:163], v[194:195], v[198:199]\n\t"
      "v_pk_fma_f32 v[164:165], v[164:165], v[192:193], v[196:197]\n\t"
      "v_pk_fma_f32 v[166:167], v[166:167], v[194:195], v[198:199]\n\t"
      "v_pk_fma_f32 v[168:169], v[168:169], v[192:193], v[196:197]\n\t"
      "v_pk_fma_f32 v[170:171], v[170:171], v[194:195], v[198:199]\n\t"
      "v_pk_fma_f32 v[172:173], v[172:173], v[192:193], v[196:197]\n\t"
      "v_pk_fma_f32 v[174:175], v[174:175], v[194:195], v[198:199]\n\t"
      "v_cvt_pk_bf16_f32 v212, v160, v161\n\t"
      "v_cvt_pk_bf16_f32 v213, v162, v163\n\t"
      "v_cvt_pk_bf16_f32 v214, v164, v165\n\t"
      "v_cvt_pk_bf16_f32 v215, v166, v167\n\t"
      "v_cvt_pk_bf16_f32 v216, v168, v169\n\t"
      "v_cvt_pk_bf16_f32 v217, v170, v171\n\t"
      "v_cvt_pk_bf16_f32 v218, v172, v173\n\t"
      "v_cvt_pk_bf16_f32 v219, v174, v175\n\t"
      "v_min_u32_e32 v212, -2, v212\n\t"
      "v_min_u32_e32 v213, -2, v213\n\t"
      "v_min_u32_e32 v214, -2, v214\n\t"
      "v_min_u32_e32 v215, -2, v215\n\t"
      "v_min_u32_e32 v216, -2, v216\n\t"
      "v_min_u32_e32 v217, -2, v217\n\t"
      "v_min_u32_e32 v218, -2, v218\n\t"
      "v_min_u32_e32 v219, -2, v219\n\t"
      "v_permlane16_swap_b32_e32 v212, v214\n\t"
      "v_permlane16_swap_b32_e32 v213, v215\n\t"
      "v_permlane16_swap_b32_e32 v216, v218\n\t"
      "v_permlane16_swap_b32_e32 v217, v219\n\t"
      "global_store_dwordx4 v231, v[212:215], s[14:15] sc1\n\t"
      "s_mov_b64 exec, s[24:25]\n\t"
      "global_store_dwordx4 v237, v[216:219], s[14:15] sc1\n\t"
      "s_mov_b64 exec, -1\n\t"
      "s_add_u32 m0, s29, 0\n\t"
      "s_nop 0\n\t"
      "global_load_lds_dwordx4 v245, s[18:19]\n\t"
      "global_load_lds_dwordx4 v1, s[18:19] offset:1024\n\t"
      "global_load_lds_dwordx4 v2, s[18:19] offset:2048\n\t"
      "global_load_lds_dwordx4 v3, s[18:19] offset:3072\n\t"
      "s_add_u32 s18, s18, 131072\n\t"
      "s_addc_u32 s19, s19, 0\n\t"
      "v_mov_b32_e32 v240, v188\n\t"
      "v_mul_f32_e32 v241, v189, v191\n\t"
      "v_add_u32_e32 v246, 65536, v234\n\t"
      "v_xor_b32_e32 v242, s12, v188\n\t"
      "v_xor_b32_e32 v243, s13, v241\n\t"
      "s_nop 1\n\t"
      "s_branch BFX_%=\n\t"
      "PWR_%=:\n\t"
      "s_mov_b32 s51, 0\n\t"
      "PWL_%=:\n\t"
      "s_add_u32 s51, s51, 1\n\t"
      "s_cmp_lt_u32 s51, 262144\n\t"
      "s_cbranch_scc0 PWX_%=\n\t"
      "s_sleep 1\n\t"
      "v_mov_b32_e32 v252, s28\n\t"
      "global_load_dwordx4 v[220:223], v252, s[8:9] sc1\n\t"
      "s_waitcnt vmcnt(0)\n\t"
      "v_subrev_u32_e32 v222, s12, v222\n\t"
      "v_subrev_u32_e32 v253, s12, v220\n\t"
      "v_xor_b32_e32 v223, v223, v221\n\t"
      "v_xor_b32_e32 v254, s13, v221\n\t"
      "v_or_b32_e32 v223, v223, v254\n\t"
      "v_or_b32_e32 v254, v222, v253\n\t"
      "v_and_or_b32 v223, v254, -2, v223\n\t"
      "v_cmp_ne_u32_e32 vcc, 0, v223\n\t"
      "s_cbranch_vccnz PWL_%=\n\t"
      "PWX_%=:\n\t"
      "v_and_b32_e32 v222, 1, v222\n\t"
      "v_and_b32_e32 v253, 1, v253\n\t"
      "s_branch PWB_%=\n\t"
      "BFX_%=:\n\t"
      : "+{s[4:19]}"(s), "+{s[20:27]}"(s2), "+{s28}"(par), "+{s29}"(lw), "+{v245}"(vn), "+{v247}"(vck), "+{v[224:239]}"(v), "+{v[240:243]}"(vo), "+{v246}"(vq)
      :
      : "memory", "vcc", "scc",
        "v0", "v1", "v2", "v3", "v4", "v5", "v6", "v7", "v8", "v9", "v10", "v11", "v12", "v13", "v14", "v15", "v16",
        "v17", "v18", "v19", "v20", "v21", "v22", "v23", "v24", "v25", "v26", "v27", "v28", "v29", "v30", "v31", "v32",
        "v33", "v34", "v35", "v36", "v37", "v38", "v39", "v40", "v41", "v42", "v43", "v44", "v45", "v46", "v47", "v48",
        "v49", "v50", "v51", "v52", "v53", "v54", "v55", "v56", "v57", "v58", "v59", "v60", "v61", "v62", "v63", "v64",
        "v65", "v66", "v67", "v68", "v69", "v70", "v71", "v72", "v73", "v74", "v75", "v76", "v77", "v78", "v79", "v80",
        "v81", "v82", "v83", "v84", "v85", "v86", "v87", "v88", "v89", "v90", "v91", "v92", "v93", "v94", "v95", "v96",
        "v97", "v98", "v99", "v100", "v101", "v102", "v103", "v104", "v105", "v106", "v107", "v108", "v109", "v110",
        "v111", "v112", "v113", "v114", "v115", "v116", "v117", "v118", "v119", "v120", "v121", "v122", "v123", "v124",
        "v125", "v126", "v127", "v128", "v129", "v130", "v131", "v132", "v133", "v134", "v135", "v136", "v137", "v138",
        "v139", "v140", "v141", "v142", "v143", "v144", "v145", "v146", "v147", "v148", "v149", "v150", "v151", "v152",
        "v153", "v154", "v155", "v156", "v157", "v158", "v159", "v160", "v161", "v162", "v163", "v164", "v165", "v166",
        "v167", "v168", "v169", "v170", "v171", "v172", "v173", "v174", "v175", "v176", "v177", "v178", "v179", "v180",
        "v181", "v182", "v183", "v184", "v185", "v186", "v187", "v188", "v189", "v190", "v191", "v192", "v193", "v194",
        "v195", "v196", "v197", "v198", "v199", "v200", "v201", "v202", "v203", "v204", "v205", "v206", "v207", "v208",
        "v209", "v210", "v211", "v212", "v213", "v214", "v215", "v216", "v217", "v218", "v219", "v220", "v221", "v222",
        "v223", "v248", "v249", "v250", "v251", "v252", "v253", "v254", "s40", "s41", "s42", "s43", "s44", "s45", "s46",
        "s47", "s48", "s49", "s50", "s51"
    );
    }
    PIN();
    LANE_BITS(__builtin_amdgcn_mbcnt_hi(~0u, __builtin_amdgcn_mbcnt_lo(~0u, 0u)));
    u32x8 v, s2;
    u32x16 s;
    v[0] = lane * 16;
    u64 kr = (u64)(6 - rn) | ((u64)rot << 32);
    v[1] = lr * 4096 + ((lc ^ swz_tr(lr)) << 4);
    v[2] = wave * G_WV + g * 1024 + q4 * 128 + (((2 * (wave & 3) + (p4 >> 1)) ^ swz_tr(8 * g + q4)) << 4) + (p4 & 1) * 8;
    v[3] = wave * 4096 + (wave & 3) * 1024 + ((lane * 16) ^ (g << 6));
    v[4] = (wave & 3) * 1024 + (((p4 * 16 + (lane >> 2)) * 16) ^ (p4 << 6));
    v[5] = (lane >> 2) * 896 + p4 * 8;
    v[6] = c * 2;
    v[7] = ST_H4 + (blk0 * 4 + ((7 * wave) >> 1) + p4) * 128 + xcd * 16384;
    put(s, 0, (u64)(w2 + st * 128 + (wave * 224 + rn * 32) * 4096));
    put(s, 2, (u64)(ws + WS_G + unit(blk0, wave, par >> 1)));
    put(s, 4, (u64)(out + (64 * st + 16 * (wave & 3)) * 896 + blk0 * 32));
    put(s, 6, (u64)(b2 + (64 * st + 16 * (wave & 3)) * 2));
    put(s, 8, (u64)(ws + WS_ST));
    s[12] = bits;
    s[13] = wave * G_WV;
    put(s2, 0, (u64)(ws + WS_HN + unit(blk0, st >> 2, par & 1) + (2 * (st & 3) + ((wave >> 1) & 1)) * 1024));
    s2[2] = ST_FL + (64 * st + 16 * (wave & 3)) * 16;
    s2[3] = UNI(wave & 1 ? ~0u : 0u);
    put(s2, 4, (u64)(ws + WS_EP));
    asm volatile(
      "s_waitcnt lgkmcnt(0)\n\ts_nop 4\n\t"
      "v_mov_b32_e32 v112, 163312\n\t"
      "ds_read_b128 v[112:115], v112\n\t"
      "v_xor_b32_e32 v248, 64, v241\n\t"
      "v_add_u32_e32 v249, 63488, v241\n\t"
      "v_add_u32_e32 v250, 95232, v248\n\t"
      "v_add_u32_e32 v248, 31744, v248\n\t"
      "v_xor_b32_e32 v251, 32, v242\n\t"
      "v_xor_b32_e32 v252, 64, v242\n\t"
      "v_xor_b32_e32 v253, 96, v242\n\t"
      "s_bitcmp1_b32 s16, 4\n\t"
      "s_cbranch_scc0 GW2_%=\n\t"
      "s_mov_b32 s48, 0xf000f\n\t"
      "s_mov_b32 s49, 0xf000f\n\t"
      "s_mov_b64 exec, s[48:49]\n\t"
      "global_store_dwordx4 v140, v[136:139], s[12:13] sc1\n\t"
      "s_mov_b64 exec, -1\n\t"
      "GW2_%=:\n\t"
      "global_load_ushort v236, v246, s[10:11]\n\t"
      "s_waitcnt lgkmcnt(0)\n\t"
      "v_readfirstlane_b32 s14, v112\n\t"
      "v_readfirstlane_b32 s15, v113\n\t"
      "v_readfirstlane_b32 s26, v114\n\t"
      "v_readfirstlane_b32 s27, v115\n\t"
      "s_add_u32 s52, s6, 0\n\t"
      "s_addc_u32 s53, s7, 0\n\t"
      "s_add_u32 s54, s6, 131072\n\t"
      "s_addc_u32 s55, s7, 0\n\t"
      "s_add_u32 s56, s6, 262144\n\t"
      "s_addc_u32 s57, s7, 0\n\t"
      "s_add_u32 s58, s6, 393216\n\t"
      "s_addc_u32 s59, s7, 0\n\t"
      "s_add_u32 s60, s6, 524288\n\t"
      "s_addc_u32 s61, s7, 0\n\t"
      "s_add_u32 s62, s6, 655360\n\t"
      "s_addc_u32 s63, s7, 0\n\t"
      "s_add_u32 s64, s6, 786432\n\t"
      "s_addc_u32 s65, s7, 0\n\t"
      "s_add_u32 s48, s29, 0\n\t"
      "s_cmp_ge_u32 s48, 7\n\t"
      "s_cselect_b32 s49, 7, 0\n\t"
      "s_sub_u32 s48, s48, s49\n\t"
      "v_lshl_add_u32 v224, s48, 10, v240\n\t"
      "s_add_u32 s48, s29, 1\n\t"
      "s_cmp_ge_u32 s48, 7\n\t"
      "s_cselect_b32 s49, 7, 0\n\t"
      "s_sub_u32 s48, s48, s49\n\t"
      "v_lshl_add_u32 v225, s48, 10, v240\n\t"
      "s_add_u32 s48, s29, 2\n\t"
      "s_cmp_ge_u32 s48, 7\n\t"
      "s_cselect_b32 s49, 7, 0\n\t"
      "s_sub_u32 s48, s48, s49\n\t"
      "v_lshl_add_u32 v226, s48, 10, v240\n\t"
      "s_add_u32 s48, s29, 3\n\t"
      "s_cmp_ge_u32 s48, 7\n\t"
      "s_cselect_b32 s49, 7, 0\n\t"
      "s_sub_u32 s48, s48, s49\n\t"
      "v_lshl_add_u32 v227, s48, 10, v240\n\t"
      "s_add_u32 s48, s29, 4\n\t"
      "s_cmp_ge_u32 s48, 7\n\t"
      "s_cselect_b32 s49, 7, 0\n\t"
      "s_sub_u32 s48, s48, s49\n\t"
      "v_lshl_add_u32 v228, s48, 10, v240\n\t"
      "s_add_u32 s48, s29, 5\n\t"
      "s_cmp_ge_u32 s48, 7\n\t"
      "s_cselect_b32 s49, 7, 0\n\t"
      "s_sub_u32 s48, s48, s49\n\t"
      "v_lshl_add_u32 v229, s48, 10, v240\n\t"
      "s_add_u32 s48, s29, 6\n\t"
      "s_cmp_ge_u32 s48, 7\n\t"
      "s_cselect_b32 s49, 7, 0\n\t"
      "s_sub_u32 s48, s48, s49\n\t"
      "v_lshl_add_u32 v230, s48, 10, v240\n\t"
      "v_xor_b32_e32 v220, 1024, v243\n\t"
      "v_xor_b32_e32 v221, 2048, v243\n\t"
      "v_xor_b32_e32 v222, 3072, v243\n\t"
      "v_lshrrev_b32_e32 v112, 4, v240\n\t"
      "v_and_b32_e32 v113, 15, v112\n\t"
      "v_lshrrev_b32_e32 v112, 4, v112\n\t"
      "s_lshr_b32 s48, s17, 10\n\t"
      "s_and_b32 s48, s48, 16\n\t"
      "v_lshlrev_b32_e32 v112, 3, v112\n\t"
      "v_sub_u32_e32 v112, v113, v112\n\t"
      "v_add_u32_e32 v112, s48, v112\n\t"
      "v_mov_b32_e32 v114, 0xffff\n\t"
      "v_mov_b32_e32 v115, 0xffff0000\n\t"
      "v_cmp_eq_u32_e32 vcc, 0, v112\n\t"
      "s_nop 1\n\t"
      "v_cndmask_b32_e32 v216, 0, v114, vcc\n\t"
      "v_cmp_eq_u32_e32 vcc, 1, v112\n\t"
      "s_nop 1\n\t"
      "v_cndmask_b32_e32 v216, v216, v115, vcc\n\t"
      "v_cmp_eq_u32_e32 vcc, 2, v112\n\t"
      "s_nop 1\n\t"
      "v_cndmask_b32_e32 v217, 0, v114, vcc\n\t"
      "v_cmp_eq_u32_e32 vcc, 3, v112\n\t"
      "s_nop 1\n\t"
      "v_cndmask_b32_e32 v217, v217, v115, vcc\n\t"
      "v_cmp_eq_u32_e32 vcc, 4, v112\n\t"
      "s_nop 1\n\t"
      "v_cndmask_b32_e32 v218, 0, v114, vcc\n\t"
      "v_cmp_eq_u32_e32 vcc, 5, v112\n\t"
      "s_nop 1\n\t"
      "v_cndmask_b32_e32 v218, v218, v115, vcc\n\t"
      "v_cmp_eq_u32_e32 vcc, 6, v112\n\t"
      "s_nop 1\n\t"
      "v_cndmask_b32_e32 v219, 0, v114, vcc\n\t"
      "v_cmp_eq_u32_e32 vcc, 7, v112\n\t"
      "s_nop 1\n\t"
      "v_cndmask_b32_e32 v219, v219, v115, vcc\n\t"
      "v_and_b32_e32 v116, 0xf0, v240\n\t"
      "v_add_u32_e32 v116, s22, v116\n\t"
      "v_mov_b64_e32 v[184:185], 0\n\t"
      "v_mov_b64_e32 v[186:187], 0\n\t"
      "v_mov_b64_e32 v[188:189], 0\n\t"
      "v_mov_b64_e32 v[190:191], 0\n\t"
      "v_mov_b64_e32 v[192:193], 0\n\t"
      "v_mov_b64_e32 v[194:195], 0\n\t"
      "v_mov_b64_e32 v[196:197], 0\n\t"
      "v_mov_b64_e32 v[198:199], 0\n\t"
      "v_mov_b64_e32 v[200:201], 0\n\t"
      "v_mov_b64_e32 v[202:203], 0\n\t"
      "v_mov_b64_e32 v[204:205], 0\n\t"
      "v_mov_b64_e32 v[206:207], 0\n\t"
      "v_mov_b64_e32 v[208:209], 0\n\t"
      "v_mov_b64_e32 v[210:211], 0\n\t"
      "s_waitcnt vmcnt(1)\n\t"
      "s_add_u32 m0, s17, 4096\n\t"
      "s_nop 0\n\t"
      "global_load_lds_dwordx4 v241, s[4:5]\n\t"
      "global_load_lds_dwordx4 v248, s[4:5] offset:1024\n\t"
      "global_load_lds_dwordx4 v249, s[4:5] offset:2048\n\t"
      "global_load_lds_dwordx4 v250, s[4:5] offset:3072\n\t"
      "s_sub_u32 s28, s28, 1\n\t"
      "s_cselect_b32 s48, -6, 1\n\t"
      "s_cselect_b32 s49, -1, 0\n\t"
      "s_mul_i32 s48, s48, 131072\n\t"
      "s_add_u32 s4, s4, s48\n\t"
      "s_addc_u32 s5, s5, s49\n\t"
      "ds_read_b64_tr_b16 v[0:1], v242\n\t"
      "ds_read_b64_tr_b16 v[2:3], v242 offset:512\n\t"
      "ds_read_b64_tr_b16 v[4:5], v251\n\t"
      "ds_read_b64_tr_b16 v[6:7], v251 offset:512\n\t"
      "ds_read_b64_tr_b16 v[8:9], v252\n\t"
      "ds_read_b64_tr_b16 v[10:11], v252 offset:512\n\t"
      "ds_read_b64_tr_b16 v[12:13], v253\n\t"
      "ds_read_b64_tr_b16 v[14:15], v253 offset:512\n\t"
      "s_waitcnt vmcnt(0)\n\t"
      "s_add_u32 m0, s17, 8192\n\t"
      "s_nop 0\n\t"
      "global_load_lds_dwordx4 v241, s[4:5]\n\t"
      "global_load_lds_dwordx4 v248, s[4:5] offset:1024\n\t"
      "global_load_lds_dwordx4 v249, s[4:5] offset:2048\n\t"
      "global_load_lds_dwordx4 v250, s[4:5] offset:3072\n\t"
      "s_sub_u32 s28, s28, 1\n\t"
      "s_cselect_b32 s48, -6, 1\n\t"
      "s_cselect_b32 s49, -1, 0\n\t"
      "s_mul_i32 s48, s48, 131072\n\t"
      "s_add_u32 s4, s4, s48\n\t"
      "s_addc_u32 s5, s5, s49\n\t"
      "ds_read_b64_tr_b16 v[16:17], v242 offset:4096\n\t"
      "ds_read_b64_tr_b16 v[18:19], v242 offset:4608\n\t"
      "ds_read_b64_tr_b16 v[20:21], v251 offset:4096\n\t"
      "ds_read_b64_tr_b16 v[22:23], v251 offset:4608\n\t"
      "ds_read_b64_tr_b16 v[24:25], v252 offset:4096\n\t"
      "ds_read_b64_tr_b16 v[26:27], v252 offset:4608\n\t"
      "ds_read_b64_tr_b16 v[28:29], v253 offset:4096\n\t"
      "ds_read_b64_tr_b16 v[30:31], v253 offset:4608\n\t"
      "s_waitcnt vmcnt(0)\n\t"
      "s_add_u32 m0, s17, 12288\n\t"
      "s_nop 0\n\t"
      "global_load_lds_dwordx4 v241, s[4:5]\n\t"
      "global_load_lds_dwordx4 v248, s[4:5] offset:1024\n\t"
      "global_load_lds_dwordx4 v249, s[4:5] offset:2048\n\t"
      "global_load_lds_dwordx4 v250, s[4:5] offset:3072\n\t"
      "s_sub_u32 s28, s28, 1\n\t"
      "s_cselect_b32 s48, -6, 1\n\t"
      "s_cselect_b32 s49, -1, 0\n\t"
      "s_mul_i32 s48, s48, 131072\n\t"
      "s_add_u32 s4, s4, s48\n\t"
      "s_addc_u32 s5, s5, s49\n\t"
      "ds_read_b64_tr_b16 v[32:33], v242 offset:8192\n\t"
      "ds_read_b64_tr_b16 v[34:35], v242 offset:8704\n\t"
      "ds_read_b64_tr_b16 v[36:37], v251 offset:8192\n\t"
      "ds_read_b64_tr_b16 v[38:39], v251 offset:8704\n\t"
      "ds_read_b64_tr_b16 v[40:41], v252 offset:8192\n\t"
      "ds_read_b64_tr_b16 v[42:43], v252 offset:8704\n\t"
      "ds_read_b64_tr_b16 v[44:45], v253 offset:8192\n\t"
      "ds_read_b64_tr_b16 v[46:47], v253 offset:8704\n\t"
      "s_waitcnt vmcnt(0)\n\t"
      "s_waitcnt lgkmcnt(14)\n\t"
      "s_add_u32 m0, s17, 0\n\t"
      "s_nop 0\n\t"
      "global_load_lds_dwordx4 v241, s[4:5]\n\t"
      "global_load_lds_dwordx4 v248, s[4:5] offset:1024\n\t"
      "global_load_lds_dwordx4 v249, s[4:5] offset:2048\n\t"
      "global_load_lds_dwordx4 v250, s[4:5] offset:3072\n\t"
      "s_sub_u32 s28, s28, 1\n\t"
      "s_cselect_b32 s48, -6, 1\n\t"
      "s_cselect_b32 s49, -1, 0\n\t"
      "s_mul_i32 s48, s48, 131072\n\t"
      "s_add_u32 s4, s4, s48\n\t"
      "s_addc_u32 s5, s5, s49\n\t"
      "ds_read_b64_tr_b16 v[48:49], v242 offset:12288\n\t"
      "ds_read_b64_tr_b16 v[50:51], v242 offset:12800\n\t"
      "ds_read_b64_tr_b16 v[52:53], v251 offset:12288\n\t"
      "ds_read_b64_tr_b16 v[54:55], v251 offset:12800\n\t"
      "ds_read_b64_tr_b16 v[56:57], v252 offset:12288\n\t"
      "ds_read_b64_tr_b16 v[58:59], v252 offset:12800\n\t"
      "ds_read_b64_tr_b16 v[60:61], v253 offset:12288\n\t"
      "ds_read_b64_tr_b16 v[62:63], v253 offset:12800\n\t"
      "s_bitcmp1_b32 s16, 1\n\t"
      "s_cbranch_scc0 HWQ_%=\n\t"
      "global_load_dwordx4 v[112:115], v116, s[12:13] sc1\n\t"
      "HWQ_%=:\n\t"
      "s_waitcnt vmcnt(0)\n\t"
      "s_waitcnt lgkmcnt(14)\n\t"
      "s_add_u32 m0, s17, 4096\n\t"
      "s_nop 0\n\t"
      "global_load_lds_dwordx4 v241, s[4:5]\n\t"
      "global_load_lds_dwordx4 v248, s[4:5] offset:1024\n\t"
      "global_load_lds_dwordx4 v249, s[4:5] offset:2048\n\t"
      "global_load_lds_dwordx4 v250, s[4:5] offset:3072\n\t"
      "s_sub_u32 s28, s28, 1\n\t"
      "s_cselect_b32 s48, -6, 1\n\t"
      "s_cselect_b32 s49, -1, 0\n\t"
      "s_mul_i32 s48, s48, 131072\n\t"
      "s_add_u32 s4, s4, s48\n\t"
      "s_addc_u32 s5, s5, s49\n\t"
      "ds_read_b64_tr_b16 v[64:65], v242\n\t"
      "ds_read_b64_tr_b16 v[66:67], v242 offset:512\n\t"
      "ds_read_b64_tr_b16 v[68:69], v251\n\t"
      "ds_read_b64_tr_b16 v[70:71], v251 offset:512\n\t"
      "ds_read_b64_tr_b16 v[72:73], v252\n\t"
      "ds_read_b64_tr_b16 v[74:75], v252 offset:512\n\t"
      "ds_read_b64_tr_b16 v[76:77], v253\n\t"
      "ds_read_b64_tr_b16 v[78:79], v253 offset:512\n\t"
      "s_waitcnt vmcnt(0)\n\t"
      "s_waitcnt lgkmcnt(14)\n\t"
      "s_add_u32 m0, s17, 8192\n\t"
      "s_nop 0\n\t"
      "global_load_lds_dwordx4 v241, s[4:5]\n\t"
      "global_load_lds_dwordx4 v248, s[4:5] offset:1024\n\t"
      "global_load_lds_dwordx4 v249, s[4:5] offset:2048\n\t"
      "global_load_lds_dwordx4 v250, s[4:5] offset:3072\n\t"
      "ds_read_b64_tr_b16 v[80:81], v242 offset:4096\n\t"
      "ds_read_b64_tr_b16 v[82:83], v242 offset:4608\n\t"
      "ds_read_b64_tr_b16 v[84:85], v251 offset:4096\n\t"
      "ds_read_b64_tr_b16 v[86:87], v251 offset:4608\n\t"
      "ds_read_b64_tr_b16 v[88:89], v252 offset:4096\n\t"
      "ds_read_b64_tr_b16 v[90:91], v252 offset:4608\n\t"
      "ds_read_b64_tr_b16 v[92:93], v253 offset:4096\n\t"
      "ds_read_b64_tr_b16 v[94:95], v253 offset:4608\n\t"
      "s_bitcmp1_b32 s16, 1\n\t"
      "s_cbranch_scc0 HWH_%=\n\t"
      "v_xor_b32_e32 v114, v114, v112\n\t"
      "v_xor_b32_e32 v115, v115, v113\n\t"
      "v_cmp_ne_u64_e32 vcc, s[14:15], v[114:115]\n\t"
      "s_cbranch_vccnz HWR_%=\n\t"
      "HWB_%=:\n\t"
      "s_mov_b32 exec_hi, s23\n\t"
      "s_not_b32 exec_lo, s23\n\t"
      "s_add_u32 s68, s20, 0\n\t"
      "s_addc_u32 s69, s21, 0\n\t"
      "global_load_dwordx4 v[184:187], v240, s[68:69] sc1\n\t"
      "s_add_u32 s68, s20, 131072\n\t"
      "s_addc_u32 s69, s21, 0\n\t"
      "global_load_dwordx4 v[188:191], v240, s[68:69] sc1\n\t"
      "s_add_u32 s68, s20, 262144\n\t"
      "s_addc_u32 s69, s21, 0\n\t"
      "global_load_dwordx4 v[192:195], v240, s[68:69] sc1\n\t"
      "s_add_u32 s68, s20, 393216\n\t"
      "s_addc_u32 s69, s21, 0\n\t"
      "global_load_dwordx4 v[196:199], v240, s[68:69] sc1\n\t"
      "s_add_u32 s68, s20, 524288\n\t"
      "s_addc_u32 s69, s21, 0\n\t"
      "global_load_dwordx4 v[200:203], v240, s[68:69] sc1\n\t"
      "s_add_u32 s68, s20, 655360\n\t"
      "s_addc_u32 s69, s21, 0\n\t"
      "global_load_dwordx4 v[204:207], v240, s[68:69] sc1\n\t"
      "s_add_u32 s68, s20, 786432\n\t"
      "s_addc_u32 s69, s21, 0\n\t"
      "global_load_dwordx4 v[208:211], v240, s[68:69] sc1\n\t"
      "s_mov_b64 exec, -1\n\t"
      "v_lshlrev_b32_e32 v236, 16, v236\n\t"
      "v_add_f32_e32 v236, v236, v112\n\t"
      "v_cvt_pk_bf16_f32 v114, v113, v113\n\t"
      "v_lshlrev_b32_e32 v115, 16, v114\n\t"
      "v_sub_f32_e32 v115, v113, v115\n\t"
      "v_cvt_pk_bf16_f32 v115, v115, v115\n\t"
      "v_mov_b32_e32 v237, v236\n\t"
      "v_mov_b32_e32 v238, v236\n\t"
      "v_mov_b32_e32 v239, v236\n\t"
      "v_and_b32_e32 v120, v216, v115\n\t"
      "v_and_b32_e32 v216, v216, v114\n\t"
      "v_and_b32_e32 v121, v217, v115\n\t"
      "v_and_b32_e32 v217, v217, v114\n\t"
      "v_and_b32_e32 v122, v218, v115\n\t"
      "v_and_b32_e32 v218, v218, v114\n\t"
      "v_and_b32_e32 v123, v219, v115\n\t"
      "v_and_b32_e32 v219, v219, v114\n\t"
      "HWH_%=:\n\t"
      "s_waitcnt vmcnt(0)\n\t"
      "ds_read_b64_tr_b16 v[96:97], v242 offset:8192\n\t"
      "ds_read_b64_tr_b16 v[98:99], v242 offset:8704\n\t"
      "ds_read_b64_tr_b16 v[100:101], v251 offset:8192\n\t"
      "ds_read_b64_tr_b16 v[102:103], v251 offset:8704\n\t"
      "ds_read_b64_tr_b16 v[104:105], v252 offset:8192\n\t"
      "ds_read_b64_tr_b16 v[106:107], v252 offset:8704\n\t"
      "ds_read_b64_tr_b16 v[108:109], v253 offset:8192\n\t"
      "ds_read_b64_tr_b16 v[110:111], v253 offset:8704\n\t"
      "s_mov_b64 exec, 15\n\t"
      "v_mov_b64_e32 v[232:233], 0\n\t"
      "global_atomic_or_x2 v[232:233], v247, v[232:233], s[12:13] sc0 sc1\n\t"
      "s_mov_b64 exec, -1\n\t"
      "s_bitcmp1_b32 s16, 1\n\t"
      "s_cbranch_scc0 HWE_%=\n\t"
      "s_waitcnt vmcnt(1)\n\t"
      "v_max3_u32 v234, v184, v185, v186\n\t"
      "v_max3_u32 v234, v234, v187, v188\n\t"
      "v_max3_u32 v234, v234, v189, v190\n\t"
      "v_max3_u32 v234, v234, v191, v192\n\t"
      "v_max3_u32 v234, v234, v193, v194\n\t"
      "v_max3_u32 v234, v234, v195, v196\n\t"
      "v_max3_u32 v234, v234, v197, v198\n\t"
      "v_max3_u32 v234, v234, v199, v200\n\t"
      "v_max3_u32 v234, v234, v201, v202\n\t"
      "v_max3_u32 v234, v234, v203, v204\n\t"
      "v_max3_u32 v234, v234, v205, v206\n\t"
      "v_max3_u32 v234, v234, v207, v208\n\t"
      "v_max3_u32 v234, v234, v209, v210\n\t"
      "v_max_u32_e32 v234, v234, v211\n\t"
      "v_cmp_eq_u32_e32 vcc, -1, v234\n\t"
      "s_cbranch_vccnz HXR_%=\n\t"
      "HXB_%=:\n\t"
      "v_mfma_f32_16x16x32_bf16 v[156:159], v[184:187], v[120:123], v[236:239]\n\t"
      "v_mfma_f32_16x16x32_bf16 v[160:163], v[188:191], v[120:123], v[236:239]\n\t"
      "v_mfma_f32_16x16x32_bf16 v[164:167], v[192:195], v[120:123], v[236:239]\n\t"
      "v_mfma_f32_16x16x32_bf16 v[168:171], v[196:199], v[120:123], v[236:239]\n\t"
      "v_mfma_f32_16x16x32_bf16 v[172:175], v[200:203], v[120:123], v[236:239]\n\t"
      "v_mfma_f32_16x16x32_bf16 v[176:179], v[204:207], v[120:123], v[236:239]\n\t"
      "v_mfma_f32_16x16x32_bf16 v[180:183], v[208:211], v[120:123], v[236:239]\n\t"
      "v_mfma_f32_16x16x32_bf16 v[156:159], v[184:187], v[216:219], v[156:159]\n\t"
      "v_mfma_f32_16x16x32_bf16 v[160:163], v[188:191], v[216:219], v[160:163]\n\t"
      "v_mfma_f32_16x16x32_bf16 v[164:167], v[192:195], v[216:219], v[164:167]\n\t"
      "v_mfma_f32_16x16x32_bf16 v[168:171], v[196:199], v[216:219], v[168:171]\n\t"
      "v_mfma_f32_16x16x32_bf16 v[172:175], v[200:203], v[216:219], v[172:175]\n\t"
      "v_mfma_f32_16x16x32_bf16 v[176:179], v[204:207], v[216:219], v[176:179]\n\t"
      "v_mfma_f32_16x16x32_bf16 v[180:183], v[208:211], v[216:219], v[180:183]\n\t"
      "HWE_%=:\n\t"
      "s_waitcnt lgkmcnt(0)\n\t"
      "s_barrier\n\t"
      "s_mov_b64 exec, 15\n\t"
      "s_waitcnt vmcnt(0)\n\t"
      "v_subrev_u32_e32 v232, s14, v232\n\t"
      "v_cmp_ne_u32_e64 s[48:49], s15, v233\n\t"
      "v_subrev_u32_e32 v232, 0, v232\n\t"
      "v_cmp_lt_u32_e32 vcc, 7, v232\n\t"
      "s_mov_b64 exec, -1\n\t"
      "s_or_b64 vcc, vcc, s[48:49]\n\t"
      "s_cbranch_vccz PRY_%=\n\t"
      "s_mov_b32 s51, 0\n\t"
      "s_branch PRE_%=\n\t"
      "PRL_%=:\n\t"
      "s_sleep 4\n\t"
      "s_add_u32 s51, s51, 1\n\t"
      "s_cmp_lt_u32 s51, 262144\n\t"
      "s_cbranch_scc0 PRX_%=\n\t"
      "PRE_%=:\n\t"
      "s_mov_b64 exec, 15\n\t"
      "v_mov_b64_e32 v[232:233], 0\n\t"
      "global_atomic_or_x2 v[232:233], v247, v[232:233], s[12:13] sc0 sc1\n\t"
      "s_waitcnt vmcnt(0)\n\t"
      "v_subrev_u32_e32 v232, s14, v232\n\t"
      "v_cmp_ne_u32_e64 s[48:49], s15, v233\n\t"
      "v_subrev_u32_e32 v232, 0, v232\n\t"
      "v_cmp_lt_u32_e32 vcc, 7, v232\n\t"
      "s_mov_b64 exec, -1\n\t"
      "s_or_b64 vcc, vcc, s[48:49]\n\t"
      "s_cbranch_vccnz PRL_%=\n\t"
      "PRX_%=:\n\t"
      "PRY_%=:\n\t"
      "global_load_dwordx4 v[128:131], v224, s[52:53] sc1\n\t"
      "global_load_dwordx4 v[132:135], v225, s[52:53] sc1\n\t"
      "global_load_dwordx4 v[136:139], v226, s[52:53] sc1\n\t"
      "global_load_dwordx4 v[140:143], v227, s[52:53] sc1\n\t"
      "global_load_dwordx4 v[144:147], v228, s[52:53] sc1\n\t"
      "global_load_dwordx4 v[148:151], v229, s[52:53] sc1\n\t"
      "global_load_dwordx4 v[152:155], v230, s[52:53] sc1\n\t"
      "s_cmp_ge_u32 s17, 65536\n\t"
      "s_cbranch_scc1 GQ_%=\n\t"
      ".p2align 6\n\t"
      "s_setprio 1\n\t"
      "s_waitcnt vmcnt(6)\n\t"
      "v_max3_u32 v234, v128, v129, v130\n\t"
      "v_max_u32_e32 v234, v234, v131\n\t"
      "v_cmp_eq_u32_e32 vcc, -1, v234\n\t"
      "s_cbranch_vccnz FP0R_%=\n\t"
      "FP0B_%=:\n\t"
      "v_mfma_f32_16x16x32_bf16 v[184:187], v[128:131], v[0:3], v[156:159]\n\t"
      "v_mfma_f32_16x16x32_bf16 v[188:191], v[128:131], v[4:7], 0\n\t"
      "v_mfma_f32_16x16x32_bf16 v[192:195], v[128:131], v[8:11], 0\n\t"
      "v_mfma_f32_16x16x32_bf16 v[196:199], v[128:131], v[12:15], 0\n\t"
      "global_load_dwordx4 v[128:131], v224, s[54:55] sc1\n\t"
      "s_waitcnt vmcnt(6)\n\t"
      "v_max3_u32 v234, v132, v133, v134\n\t"
      "v_max_u32_e32 v234, v234, v135\n\t"
      "v_cmp_eq_u32_e32 vcc, -1, v234\n\t"
      "s_cbranch_vccnz FP1R_%=\n\t"
      "FP1B_%=:\n\t"
      "v_mfma_f32_16x16x32_bf16 v[184:187], v[132:135], v[16:19], v[184:187]\n\t"
      "v_mfma_f32_16x16x32_bf16 v[188:191], v[132:135], v[20:23], v[188:191]\n\t"
      "v_mfma_f32_16x16x32_bf16 v[192:195], v[132:135], v[24:27], v[192:195]\n\t"
      "v_mfma_f32_16x16x32_bf16 v[196:199], v[132:135], v[28:31], v[196:199]\n\t"
      "global_load_dwordx4 v[132:135], v225, s[54:55] sc1\n\t"
      "s_waitcnt vmcnt(6)\n\t"
      "v_max3_u32 v234, v136, v137, v138\n\t"
      "v_max_u32_e32 v234, v234, v139\n\t"
      "v_cmp_eq_u32_e32 vcc, -1, v234\n\t"
      "s_cbranch_vccnz FP2R_%=\n\t"
      "FP2B_%=:\n\t"
      "v_mfma_f32_16x16x32_bf16 v[184:187], v[136:139], v[32:35], v[184:187]\n\t"
      "v_mfma_f32_16x16x32_bf16 v[188:191], v[136:139], v[36:39], v[188:191]\n\t"
      "v_mfma_f32_16x16x32_bf16 v[192:195], v[136:139], v[40:43], v[192:195]\n\t"
      "v_mfma_f32_16x16x32_bf16 v[196:199], v[136:139], v[44:47], v[196:199]\n\t"
      "global_load_dwordx4 v[136:139], v226, s[54:55] sc1\n\t"
      "s_waitcnt vmcnt(6)\n\t"
      "v_max3_u32 v234, v140, v141, v142\n\t"
      "v_max_u32_e32 v234, v234, v143\n\t"
      "v_cmp_eq_u32_e32 vcc, -1, v234\n\t"
      "s_cbranch_vccnz FP3R_%=\n\t"
      "FP3B_%=:\n\t"
      "v_mfma_f32_16x16x32_bf16 v[184:187], v[140:143], v[48:51], v[184:187]\n\t"
      "v_mfma_f32_16x16x32_bf16 v[188:191], v[140:143], v[52:55], v[188:191]\n\t"
      "v_mfma_f32_16x16x32_bf16 v[192:195], v[140:143], v[56:59], v[192:195]\n\t"
      "v_mfma_f32_16x16x32_bf16 v[196:199], v[140:143], v[60:63], v[196:199]\n\t"
      "global_load_dwordx4 v[140:143], v227, s[54:55] sc1\n\t"
      "s_waitcnt vmcnt(6)\n\t"
      "v_max3_u32 v234, v144, v145, v146\n\t"
      "v_max_u32_e32 v234, v234, v147\n\t"
      "v_cmp_eq_u32_e32 vcc, -1, v234\n\t"
      "s_cbranch_vccnz FP4R_%=\n\t"
      "FP4B_%=:\n\t"
      "v_mfma_f32_16x16x32_bf16 v[184:187], v[144:147], v[64:67], v[184:187]\n\t"
      "v_mfma_f32_16x16x32_bf16 v[188:191], v[144:147], v[68:71], v[188:191]\n\t"
      "v_mfma_f32_16x16x32_bf16 v[192:195], v[144:147], v[72:75], v[192:195]\n\t"
      "v_mfma_f32_16x16x32_bf16 v[196:199], v[144:147], v[76:79], v[196:199]\n\t"
      "global_load_dwordx4 v[144:147], v228, s[54:55] sc1\n\t"
      "s_waitcnt vmcnt(6)\n\t"
      "v_max3_u32 v234, v148, v149, v150\n\t"
      "v_max_u32_e32 v234, v234, v151\n\t"
      "v_cmp_eq_u32_e32 vcc, -1, v234\n\t"
      "s_cbranch_vccnz FP5R_%=\n\t"
      "FP5B_%=:\n\t"
      "v_mfma_f32_16x16x32_bf16 v[184:187], v[148:151], v[80:83], v[184:187]\n\t"
      "v_mfma_f32_16x16x32_bf16 v[188:191], v[148:151], v[84:87], v[188:191]\n\t"
      "v_mfma_f32_16x16x32_bf16 v[192:195], v[148:151], v[88:91], v[192:195]\n\t"
      "v_mfma_f32_16x16x32_bf16 v[196:199], v[148:151], v[92:95], v[196:199]\n\t"
      "global_load_dwordx4 v[148:151], v229, s[54:55] sc1\n\t"
      "s_waitcnt vmcnt(6)\n\t"
      "v_max3_u32 v234, v152, v153, v154\n\t"
      "v_max_u32_e32 v234, v234, v155\n\t"
      "v_cmp_eq_u32_e32 vcc, -1, v234\n\t"
      "s_cbranch_vccnz FP6R_%=\n\t"
      "FP6B_%=:\n\t"
      "v_mfma_f32_16x16x32_bf16 v[184:187], v[152:155], v[96:99], v[184:187]\n\t"
      "s_waitcnt vmcnt(5)\n\t"
      "v_cmp_eq_u32_e32 vcc, -1, v128\n\t"
      "v_mfma_f32_16x16x32_bf16 v[188:191], v[152:155], v[100:103], v[188:191]\n\t"
      "v_mfma_f32_16x16x32_bf16 v[192:195], v[152:155], v[104:107], v[192:195]\n\t"
      "s_cbranch_vccnz FP7R_%=\n\t"
      "FP7B_%=:\n\t"
      "v_mfma_f32_16x16x32_bf16 v[196:199], v[152:155], v[108:111], v[196:199]\n\t"
      "global_load_dwordx4 v[152:155], v230, s[54:55] sc1\n\t"
      "s_nop 0\n\t"
      "ds_write_b128 v243, v[184:187]\n\t"
      "s_nop 1\n\t"
      "ds_write_b128 v220, v[188:191]\n\t"
      "ds_write_b128 v221, v[192:195]\n\t"
      "s_nop 0\n\t"
      "ds_write_b128 v222, v[196:199]\n\t"
      "s_waitcnt lgkmcnt(0)\n\t"
      "s_barrier\n\t"
      "ds_read_b128 v[112:115], v244\n\t"
      "ds_read_b128 v[116:119], v244 offset:4096\n\t"
      "ds_read_b128 v[120:123], v244 offset:8192\n\t"
      "ds_read_b128 v[124:127], v244 offset:12288\n\t"
      "ds_read_b128 v[184:187], v244 offset:16384\n\t"
      "ds_read_b128 v[188:191], v244 offset:20480\n\t"
      "ds_read_b128 v[192:195], v244 offset:24576\n\t"
      "ds_read_b128 v[196:199], v244 offset:28672\n\t"
      "v_mfma_f32_16x16x32_bf16 v[200:203], v[128:131], v[0:3], v[160:163]\n\t"
      "s_waitcnt vmcnt(5)\n\t"
      "v_cmp_eq_u32_e32 vcc, -1, v132\n\t"
      "v_mfma_f32_16x16x32_bf16 v[204:207], v[128:131], v[4:7], 0\n\t"
      "s_waitcnt lgkmcnt(6)\n\t"
      "v_add_f32_e32 v112, v112, v116\n\t"
      "v_mfma_f32_16x16x32_bf16 v[208:211], v[128:131], v[8:11], 0\n\t"
      "s_cbranch_vccnz FP8R_%=\n\t"
      "FP8B_%=:\n\t"
      "v_add_f32_e32 v113, v113, v117\n\t"
      "v_add_f32_e32 v114, v114, v118\n\t"
      "v_mfma_f32_16x16x32_bf16 v[212:215], v[128:131], v[12:15], 0\n\t"
      "global_load_dwordx4 v[128:131], v224, s[56:57] sc1\n\t"
      "v_add_f32_e32 v115, v115, v119\n\t"
      "v_mfma_f32_16x16x32_bf16 v[200:203], v[132:135], v[16:19], v[200:203]\n\t"
      "s_waitcnt vmcnt(5)\n\t"
      "v_cmp_eq_u32_e32 vcc, -1, v136\n\t"
      "s_waitcnt lgkmcnt(4)\n\t"
      "v_add_f32_e32 v120, v120, v124\n\t"
      "v_mfma_f32_16x16x32_bf16 v[204:207], v[132:135], v[20:23], v[204:207]\n\t"
      "v_add_f32_e32 v121, v121, v125\n\t"
      "v_add_f32_e32 v122, v122, v126\n\t"
      "v_mfma_f32_16x16x32_bf16 v[208:211], v[132:135], v[24:27], v[208:211]\n\t"
      "s_cbranch_vccnz FP9R_%=\n\t"
      "FP9B_%=:\n\t"
      "v_add_f32_e32 v123, v123, v127\n\t"
      "s_waitcnt lgkmcnt(2)\n\t"
      "v_add_f32_e32 v184, v184, v188\n\t"
      "v_mfma_f32_16x16x32_bf16 v[212:215], v[132:135], v[28:31], v[212:215]\n\t"
      "global_load_dwordx4 v[132:135], v225, s[56:57] sc1\n\t"
      "v_add_f32_e32 v185, v185, v189\n\t"
      "v_mfma_f32_16x16x32_bf16 v[200:203], v[136:139], v[32:35], v[200:203]\n\t"
      "s_waitcnt vmcnt(5)\n\t"
      "v_cmp_eq_u32_e32 vcc, -1, v140\n\t"
      "v_add_f32_e32 v186, v186, v190\n\t"
      "v_mfma_f32_16x16x32_bf16 v[204:207], v[136:139], v[36:39], v[204:207]\n\t"
      "v_add_f32_e32 v187, v187, v191\n\t"
      "v_add_f32_e32 v112, v112, v120\n\t"
      "v_mfma_f32_16x16x32_bf16 v[208:211], v[136:139], v[40:43], v[208:211]\n\t"
      "s_cbranch_vccnz FP10R_%=\n\t"
      "FP10B_%=:\n\t"
      "v_add_f32_e32 v113, v113, v121\n\t"
      "v_add_f32_e32 v114, v114, v122\n\t"
      "v_mfma_f32_16x16x32_bf16 v[212:215], v[136:139], v[44:47], v[212:215]\n\t"
      "global_load_dwordx4 v[136:139], v226, s[56:57] sc1\n\t"
      "v_add_f32_e32 v115, v115, v123\n\t"
      "v_mfma_f32_16x16x32_bf16 v[200:203], v[140:143], v[48:51], v[200:203]\n\t"
      "s_waitcnt vmcnt(5)\n\t"
      "v_cmp_eq_u32_e32 vcc, -1, v144\n\t"
      "s_waitcnt lgkmcnt(0)\n\t"
      "v_add_f32_e32 v192, v192, v196\n\t"
      "v_mfma_f32_16x16x32_bf16 v[204:207], v[140:143], v[52:55], v[204:207]\n\t"
      "v_add_f32_e32 v193, v193, v197\n\t"
      "v_add_f32_e32 v194, v194, v198\n\t"
      "v_mfma_f32_16x16x32_bf16 v[208:211], v[140:143], v[56:59], v[208:211]\n\t"
      "s_cbranch_vccnz FP11R_%=\n\t"
      "FP11B_%=:\n\t"
      "v_add_f32_e32 v195, v195, v199\n\t"
      "v_add_f32_e32 v184, v184, v192\n\t"
      "v_mfma_f32_16x16x32_bf16 v[212:215], v[140:143], v[60:63], v[212:215]\n\t"
      "global_load_dwordx4 v[140:143], v227, s[56:57] sc1\n\t"
      "v_add_f32_e32 v185, v185, v193\n\t"
      "v_mfma_f32_16x16x32_bf16 v[200:203], v[144:147], v[64:67], v[200:203]\n\t"
      "s_waitcnt vmcnt(5)\n\t"
      "v_cmp_eq_u32_e32 vcc, -1, v148\n\t"
      "v_add_f32_e32 v186, v186, v194\n\t"
      "v_mfma_f32_16x16x32_bf16 v[204:207], v[144:147], v[68:71], v[204:207]\n\t"
      "v_add_f32_e32 v187, v187, v195\n\t"
      "v_add_f32_e32 v112, v112, v184\n\t"
      "v_mfma_f32_16x16x32_bf16 v[208:211], v[144:147], v[72:75], v[208:211]\n\t"
      "s_cbranch_vccnz FP12R_%=\n\t"
      "FP12B_%=:\n\t"
      "v_add_f32_e32 v113, v113, v185\n\t"
      "v_add_f32_e32 v114, v114, v186\n\t"
      "v_mfma_f32_16x16x32_bf16 v[212:215], v[144:147], v[76:79], v[212:215]\n\t"
      "global_load_dwordx4 v[144:147], v228, s[56:57] sc1\n\t"
      "v_add_f32_e32 v115, v115, v187\n\t"
      "v_mfma_f32_16x16x32_bf16 v[200:203], v[148:151], v[80:83], v[200:203]\n\t"
      "s_waitcnt vmcnt(5)\n\t"
      "v_cmp_eq_u32_e32 vcc, -1, v152\n\t"
      "v_cvt_pk_bf16_f32 v116, v112, v113\n\t"
      "v_mfma_f32_16x16x32_bf16 v[204:207], v[148:151], v[84:87], v[204:207]\n\t"
      "v_cvt_pk_bf16_f32 v117, v114, v115\n\t"
      "global_store_dwordx2 v245, v[116:117], s[8:9]\n\t"
      "v_mfma_f32_16x16x32_bf16 v[208:211], v[148:151], v[88:91], v[208:211]\n\t"
      "s_cbranch_vccnz FP13R_%=\n\t"
      "FP13B_%=:\n\t"
      "v_mfma_f32_16x16x32_bf16 v[212:215], v[148:151], v[92:95], v[212:215]\n\t"
      "global_load_dwordx4 v[148:151], v229, s[56:57] sc1\n\t"
      "v_mfma_f32_16x16x32_bf16 v[200:203], v[152:155], v[96:99], v[200:203]\n\t"
      "s_waitcnt vmcnt(6)\n\t"
      "v_cmp_eq_u32_e32 vcc, -1, v128\n\t"
      "v_mfma_f32_16x16x32_bf16 v[204:207], v[152:155], v[100:103], v[204:207]\n\t"
      "v_mfma_f32_16x16x32_bf16 v[208:211], v[152:155], v[104:107], v[208:211]\n\t"
      "s_cbranch_vccnz FP14R_%=\n\t"
      "FP14B_%=:\n\t"
      "v_mfma_f32_16x16x32_bf16 v[212:215], v[152:155], v[108:111], v[212:215]\n\t"
      "global_load_dwordx4 v[152:155], v230, s[56:57] sc1\n\t"
      "s_nop 0\n\t"
      "v_cmp_u_f32_e32 vcc, v200, v201\n\t"
      "v_cmp_u_f32_e64 s[48:49], v202, v203\n\t"
      "s_nop 0\n\t"
      "s_or_b64 vcc, vcc, s[48:49]\n\t"
      "s_cbranch_scc1 NP1R_%=\n\t"
      "NP1B_%=:\n\t"
      "ds_write_b128 v243, v[200:203] offset:32768\n\t"
      "ds_write_b128 v220, v[204:207] offset:32768\n\t"
      "ds_write_b128 v221, v[208:211] offset:32768\n\t"
      "ds_write_b128 v222, v[212:215] offset:32768\n\t"
      "s_waitcnt lgkmcnt(0)\n\t"
      "s_barrier\n\t"
      "ds_read_b128 v[112:115], v244 offset:32768\n\t"
      "ds_read_b128 v[116:119], v244 offset:36864\n\t"
      "ds_read_b128 v[120:123], v244 offset:40960\n\t"
      "ds_read_b128 v[124:127], v244 offset:45056\n\t"
      "ds_read_b128 v[200:203], v244 offset:49152\n\t"
      "ds_read_b128 v[204:207], v244 offset:53248\n\t"
      "ds_read_b128 v[208:211], v244 offset:57344\n\t"
      "ds_read_b128 v[212:215], v244 offset:61440\n\t"
      "v_mfma_f32_16x16x32_bf16 v[184:187], v[128:131], v[0:3], v[164:167]\n\t"
      "s_waitcnt vmcnt(6)\n\t"
      "v_cmp_eq_u32_e32 vcc, -1, v132\n\t"
      "v_mfma_f32_16x16x32_bf16 v[188:191], v[128:131], v[4:7], 0\n\t"
      "s_waitcnt lgkmcnt(6)\n\t"
      "v_add_f32_e32 v112, v112, v116\n\t"
      "v_mfma_f32_16x16x32_bf16 v[192:195], v[128:131], v[8:11], 0\n\t"
      "s_cbranch_vccnz FP15R_%=\n\t"
      "FP15B_%=:\n\t"
      "v_add_f32_e32 v113, v113, v117\n\t"
      "v_add_f32_e32 v114, v114, v118\n\t"
      "v_mfma_f32_16x16x32_bf16 v[196:199], v[128:131], v[12:15], 0\n\t"
      "global_load_dwordx4 v[128:131], v224, s[58:59] sc1\n\t"
      "v_add_f32_e32 v115, v115, v119\n\t"
      "v_mfma_f32_16x16x32_bf16 v[184:187], v[132:135], v[16:19], v[184:187]\n\t"
      "s_waitcnt vmcnt(6)\n\t"
      "v_cmp_eq_u32_e32 vcc, -1, v136\n\t"
      "s_waitcnt lgkmcnt(4)\n\t"
      "v_add_f32_e32 v120, v120, v124\n\t"
      "v_mfma_f32_16x16x32_bf16 v[188:191], v[132:135], v[20:23], v[188:191]\n\t"
      "v_add_f32_e32 v121, v121, v125\n\t"
      "v_add_f32_e32 v122, v122, v126\n\t"
      "v_mfma_f32_16x16x32_bf16 v[192:195], v[132:135], v[24:27], v[192:195]\n\t"
      "s_cbranch_vccnz FP16R_%=\n\t"
      "FP16B_%=:\n\t"
      "v_add_f32_e32 v123, v123, v127\n\t"
      "s_waitcnt lgkmcnt(2)\n\t"
      "v_add_f32_e32 v200, v200, v204\n\t"
      "v_mfma_f32_16x16x32_bf16 v[196:199], v[132:135], v[28:31], v[196:199]\n\t"
      "global_load_dwordx4 v[132:135], v225, s[58:59] sc1\n\t"
      "v_add_f32_e32 v201, v201, v205\n\t"
      "v_mfma_f32_16x16x32_bf16 v[184:187], v[136:139], v[32:35], v[184:187]\n\t"
      "s_waitcnt vmcnt(6)\n\t"
      "v_cmp_eq_u32_e32 vcc, -1, v140\n\t"
      "v_add_f32_e32 v202, v202, v206\n\t"
      "v_mfma_f32_16x16x32_bf16 v[188:191], v[136:139], v[36:39], v[188:191]\n\t"
      "v_add_f32_e32 v203, v203, v207\n\t"
      "v_add_f32_e32 v112, v112, v120\n\t"
      "v_mfma_f32_16x16x32_bf16 v[192:195], v[136:139], v[40:43], v[192:195]\n\t"
      "s_cbranch_vccnz FP17R_%=\n\t"
      "FP17B_%=:\n\t"
      "v_add_f32_e32 v113, v113, v121\n\t"
      "v_add_f32_e32 v114, v114, v122\n\t"
      "v_mfma_f32_16x16x32_bf16 v[196:199], v[136:139], v[44:47], v[196:199]\n\t"
      "global_load_dwordx4 v[136:139], v226, s[58:59] sc1\n\t"
      "v_add_f32_e32 v115, v115, v123\n\t"
      "v_mfma_f32_16x16x32_bf16 v[184:187], v[140:143], v[48:51], v[184:187]\n\t"
      "s_waitcnt vmcnt(6)\n\t"
      "v_cmp_eq_u32_e32 vcc, -1, v144\n\t"
      "s_waitcnt lgkmcnt(0)\n\t"
      "v_add_f32_e32 v208, v208, v212\n\t"
      "v_mfma_f32_16x16x32_bf16 v[188:191], v[140:143], v[52:55], v[188:191]\n\t"
      "v_add_f32_e32 v209, v209, v213\n\t"
      "v_add_f32_e32 v210, v210, v214\n\t"
      "v_mfma_f32_16x16x32_bf16 v[192:195], v[140:143], v[56:59], v[192:195]\n\t"
      "s_cbranch_vccnz FP18R_%=\n\t"
      "FP18B_%=:\n\t"
      "v_add_f32_e32 v211, v211, v215\n\t"
      "v_add_f32_e32 v200, v200, v208\n\t"
      "v_mfma_f32_16x16x32_bf16 v[196:199], v[140:143], v[60:63], v[196:199]\n\t"
      "global_load_dwordx4 v[140:143], v227, s[58:59] sc1\n\t"
      "v_add_f32_e32 v201, v201, v209\n\t"
      "v_mfma_f32_16x16x32_bf16 v[184:187], v[144:147], v[64:67], v[184:187]\n\t"
      "s_waitcnt vmcnt(5)\n\t"
      "v_cmp_eq_u32_e32 vcc, -1, v148\n\t"
      "v_add_f32_e32 v202, v202, v210\n\t"
      "v_mfma_f32_16x16x32_bf16 v[188:191], v[144:147], v[68:71], v[188:191]\n\t"
      "v_add_f32_e32 v203, v203, v211\n\t"
      "v_add_f32_e32 v112, v112, v200\n\t"
      "v_mfma_f32_16x16x32_bf16 v[192:195], v[144:147], v[72:75], v[192:195]\n\t"
      "s_cbranch_vccnz FP19R_%=\n\t"
      "FP19B_%=:\n\t"
      "v_add_f32_e32 v113, v113, v201\n\t"
      "v_add_f32_e32 v114, v114, v202\n\t"
      "v_mfma_f32_16x16x32_bf16 v[196:199], v[144:147], v[76:79], v[196:199]\n\t"
      "global_load_dwordx4 v[144:147], v228, s[58:59] sc1\n\t"
      "v_add_f32_e32 v115, v115, v203\n\t"
      "v_mfma_f32_16x16x32_bf16 v[184:187], v[148:151], v[80:83], v[184:187]\n\t"
      "s_waitcnt vmcnt(5)\n\t"
      "v_cmp_eq_u32_e32 vcc, -1, v152\n\t"
      "v_cvt_pk_bf16_f32 v116, v112, v113\n\t"
      "v_mfma_f32_16x16x32_bf16 v[188:191], v[148:151], v[84:87], v[188:191]\n\t"
      "v_cvt_pk_bf16_f32 v117, v114, v115\n\t"
      "global_store_dwordx2 v245, v[116:117], s[8:9] offset:32\n\t"
      "v_mfma_f32_16x16x32_bf16 v[192:195], v[148:151], v[88:91], v[192:195]\n\t"
      "s_cbranch_vccnz FP20R_%=\n\t"
      "FP20B_%=:\n\t"
      "v_mfma_f32_16x16x32_bf16 v[196:199], v[148:151], v[92:95], v[196:199]\n\t"
      "global_load_dwordx4 v[148:151], v229, s[58:59] sc1\n\t"
      "v_mfma_f32_16x16x32_bf16 v[184:187], v[152:155], v[96:99], v[184:187]\n\t"
      "s_waitcnt vmcnt(6)\n\t"
      "v_cmp_eq_u32_e32 vcc, -1, v128\n\t"
      "v_mfma_f32_16x16x32_bf16 v[188:191], v[152:155], v[100:103], v[188:191]\n\t"
      "v_mfma_f32_16x16x32_bf16 v[192:195], v[152:155], v[104:107], v[192:195]\n\t"
      "s_cbranch_vccnz FP21R_%=\n\t"
      "FP21B_%=:\n\t"
      "v_mfma_f32_16x16x32_bf16 v[196:199], v[152:155], v[108:111], v[196:199]\n\t"
      "global_load_dwordx4 v[152:155], v230, s[58:59] sc1\n\t"
      "s_nop 0\n\t"
      "v_cmp_u_f32_e32 vcc, v184, v185\n\t"
      "v_cmp_u_f32_e64 s[48:49], v186, v187\n\t"
      "s_nop 0\n\t"
      "s_or_b64 vcc, vcc, s[48:49]\n\t"
      "s_cbranch_scc1 NP2R_%=\n\t"
      "NP2B_%=:\n\t"
      "ds_write_b128 v243, v[184:187]\n\t"
      "ds_write_b128 v220, v[188:191]\n\t"
      "ds_write_b128 v221, v[192:195]\n\t"
      "ds_write_b128 v222, v[196:199]\n\t"
      "s_waitcnt lgkmcnt(0)\n\t"
      "s_barrier\n\t"
      "ds_read_b128 v[112:115], v244\n\t"
      "ds_read_b128 v[116:119], v244 offset:4096\n\t"
      "ds_read_b128 v[120:123], v244 offset:8192\n\t"
      "ds_read_b128 v[124:127], v244 offset:12288\n\t"
      "ds_read_b128 v[184:187], v244 offset:16384\n\t"
      "ds_read_b128 v[188:191], v244 offset:20480\n\t"
      "ds_read_b128 v[192:195], v244 offset:24576\n\t"
      "ds_read_b128 v[196:199], v244 offset:28672\n\t"
      "v_mfma_f32_16x16x32_bf16 v[200:203], v[128:131], v[0:3], v[168:171]\n\t"
      "s_waitcnt vmcnt(6)\n\t"
      "v_cmp_eq_u32_e32 vcc, -1, v132\n\t"
      "v_mfma_f32_16x16x32_bf16 v[204:207], v[128:131], v[4:7], 0\n\t"
      "s_waitcnt lgkmcnt(6)\n\t"
      "v_add_f32_e32 v112, v112, v116\n\t"
      "v_mfma_f32_16x16x32_bf16 v[208:211], v[128:131], v[8:11], 0\n\t"
      "s_cbranch_vccnz FP22R_%=\n\t"
      "FP22B_%=:\n\t"
      "v_add_f32_e32 v113, v113, v117\n\t"
      "v_add_f32_e32 v114, v114, v118\n\t"
      "v_mfma_f32_16x16x32_bf16 v[212:215], v[128:131], v[12:15], 0\n\t"
      "global_load_dwordx4 v[128:131], v224, s[60:61] sc1\n\t"
      "v_add_f32_e32 v115, v115, v119\n\t"
      "v_mfma_f32_16x16x32_bf16 v[200:203], v[132:135], v[16:19], v[200:203]\n\t"
      "s_waitcnt vmcnt(6)\n\t"
      "v_cmp_eq_u32_e32 vcc, -1, v136\n\t"
      "s_waitcnt lgkmcnt(4)\n\t"
      "v_add_f32_e32 v120, v120, v124\n\t"
      "v_mfma_f32_16x16x32_bf16 v[204:207], v[132:135], v[20:23], v[204:207]\n\t"
      "v_add_f32_e32 v121, v121, v125\n\t"
      "v_add_f32_e32 v122, v122, v126\n\t"
      "v_mfma_f32_16x16x32_bf16 v[208:211], v[132:135], v[24:27], v[208:211]\n\t"
      "s_cbranch_vccnz FP23R_%=\n\t"
      "FP23B_%=:\n\t"
      "v_add_f32_e32 v123, v123, v127\n\t"
      "s_waitcnt lgkmcnt(2)\n\t"
      "v_add_f32_e32 v184, v184, v188\n\t"
      "v_mfma_f32_16x16x32_bf16 v[212:215], v[132:135], v[28:31], v[212:215]\n\t"
      "global_load_dwordx4 v[132:135], v225, s[60:61] sc1\n\t"
      "v_add_f32_e32 v185, v185, v189\n\t"
      "v_mfma_f32_16x16x32_bf16 v[200:203], v[136:139], v[32:35], v[200:203]\n\t"
      "s_waitcnt vmcnt(6)\n\t"
      "v_cmp_eq_u32_e32 vcc, -1, v140\n\t"
      "v_add_f32_e32 v186, v186, v190\n\t"
      "v_mfma_f32_16x16x32_bf16 v[204:207], v[136:139], v[36:39], v[204:207]\n\t"
      "v_add_f32_e32 v187, v187, v191\n\t"
      "v_add_f32_e32 v112, v112, v120\n\t"
      "v_mfma_f32_16x16x32_bf16 v[208:211], v[136:139], v[40:43], v[208:211]\n\t"
      "s_cbranch_vccnz FP24R_%=\n\t"
      "FP24B_%=:\n\t"
      "v_add_f32_e32 v113, v113, v121\n\t"
      "v_add_f32_e32 v114, v114, v122\n\t"
      "v_mfma_f32_16x16x32_bf16 v[212:215], v[136:139], v[44:47], v[212:215]\n\t"
      "global_load_dwordx4 v[136:139], v226, s[60:61] sc1\n\t"
      "v_add_f32_e32 v115, v115, v123\n\t"
      "v_mfma_f32_16x16x32_bf16 v[200:203], v[140:143], v[48:51], v[200:203]\n\t"
      "s_waitcnt vmcnt(6)\n\t"
      "v_cmp_eq_u32_e32 vcc, -1, v144\n\t"
      "s_waitcnt lgkmcnt(0)\n\t"
      "v_add_f32_e32 v192, v192, v196\n\t"
      "v_mfma_f32_16x16x32_bf16 v[204:207], v[140:143], v[52:55], v[204:207]\n\t"
      "v_add_f32_e32 v193, v193, v197\n\t"
      "v_add_f32_e32 v194, v194, v198\n\t"
      "v_mfma_f32_16x16x32_bf16 v[208:211], v[140:143], v[56:59], v[208:211]\n\t"
      "s_cbranch_vccnz FP25R_%=\n\t"
      "FP25B_%=:\n\t"
      "v_add_f32_e32 v195, v195, v199\n\t"
      "v_add_f32_e32 v184, v184, v192\n\t"
      "v_mfma_f32_16x16x32_bf16 v[212:215], v[140:143], v[60:63], v[212:215]\n\t"
      "global_load_dwordx4 v[140:143], v227, s[60:61] sc1\n\t"
      "v_add_f32_e32 v185, v185, v193\n\t"
      "v_mfma_f32_16x16x32_bf16 v[200:203], v[144:147], v[64:67], v[200:203]\n\t"
      "s_waitcnt vmcnt(5)\n\t"
      "v_cmp_eq_u32_e32 vcc, -1, v148\n\t"
      "v_add_f32_e32 v186, v186, v194\n\t"
      "v_mfma_f32_16x16x32_bf16 v[204:207], v[144:147], v[68:71], v[204:207]\n\t"
      "v_add_f32_e32 v187, v187, v195\n\t"
      "v_add_f32_e32 v112, v112, v184\n\t"
      "v_mfma_f32_16x16x32_bf16 v[208:211], v[144:147], v[72:75], v[208:211]\n\t"
      "s_cbranch_vccnz FP26R_%=\n\t"
      "FP26B_%=:\n\t"
      "v_add_f32_e32 v113, v113, v185\n\t"
      "v_add_f32_e32 v114, v114, v186\n\t"
      "v_mfma_f32_16x16x32_bf16 v[212:215], v[144:147], v[76:79], v[212:215]\n\t"
      "global_load_dwordx4 v[144:147], v228, s[60:61] sc1\n\t"
      "v_add_f32_e32 v115, v115, v187\n\t"
      "v_mfma_f32_16x16x32_bf16 v[200:203], v[148:151], v[80:83], v[200:203]\n\t"
      "s_waitcnt vmcnt(5)\n\t"
      "v_cmp_eq_u32_e32 vcc, -1, v152\n\t"
      "v_cvt_pk_bf16_f32 v116, v112, v113\n\t"
      "v_mfma_f32_16x16x32_bf16 v[204:207], v[148:151], v[84:87], v[204:207]\n\t"
      "v_cvt_pk_bf16_f32 v117, v114, v115\n\t"
      "global_store_dwordx2 v245, v[116:117], s[8:9] offset:64\n\t"
      "v_mfma_f32_16x16x32_bf16 v[208:211], v[148:151], v[88:91], v[208:211]\n\t"
      "s_cbranch_vccnz FP27R_%=\n\t"
      "FP27B_%=:\n\t"
      "v_mfma_f32_16x16x32_bf16 v[212:215], v[148:151], v[92:95], v[212:215]\n\t"
      "global_load_dwordx4 v[148:151], v229, s[60:61] sc1\n\t"
      "v_mfma_f32_16x16x32_bf16 v[200:203], v[152:155], v[96:99], v[200:203]\n\t"
      "s_waitcnt vmcnt(6)\n\t"
      "v_cmp_eq_u32_e32 vcc, -1, v128\n\t"
      "v_mfma_f32_16x16x32_bf16 v[204:207], v[152:155], v[100:103], v[204:207]\n\t"
      "v_mfma_f32_16x16x32_bf16 v[208:211], v[152:155], v[104:107], v[208:211]\n\t"
      "s_cbranch_vccnz FP28R_%=\n\t"
      "FP28B_%=:\n\t"
      "v_mfma_f32_16x16x32_bf16 v[212:215], v[152:155], v[108:111], v[212:215]\n\t"
      "global_load_dwordx4 v[152:155], v230, s[60:61] sc1\n\t"
      "s_nop 0\n\t"
      "v_cmp_u_f32_e32 vcc, v200, v201\n\t"
      "v_cmp_u_f32_e64 s[48:49], v202, v203\n\t"
      "s_nop 0\n\t"
      "s_or_b64 vcc, vcc, s[48:49]\n\t"
      "s_cbranch_scc1 NP3R_%=\n\t"
      "NP3B_%=:\n\t"
      "ds_write_b128 v243, v[200:203] offset:32768\n\t"
      "ds_write_b128 v220, v[204:207] offset:32768\n\t"
      "ds_write_b128 v221, v[208:211] offset:32768\n\t"
      "ds_write_b128 v222, v[212:215] offset:32768\n\t"
      "s_waitcnt lgkmcnt(0)\n\t"
      "s_barrier\n\t"
      "ds_read_b128 v[112:115], v244 offset:32768\n\t"
      "ds_read_b128 v[116:119], v244 offset:36864\n\t"
      "ds_read_b128 v[120:123], v244 offset:40960\n\t"
      "ds_read_b128 v[124:127], v244 offset:45056\n\t"
      "ds_read_b128 v[200:203], v244 offset:49152\n\t"
      "ds_read_b128 v[204:207], v244 offset:53248\n\t"
      "ds_read_b128 v[208:211], v244 offset:57344\n\t"
      "ds_read_b128 v[212:215], v244 offset:61440\n\t"
      "v_mfma_f32_16x16x32_bf16 v[184:187], v[128:131], v[0:3], v[172:175]\n\t"
      "s_waitcnt vmcnt(6)\n\t"
      "v_cmp_eq_u32_e32 vcc, -1, v132\n\t"
      "v_mfma_f32_16x16x32_bf16 v[188:191], v[128:131], v[4:7], 0\n\t"
      "s_waitcnt lgkmcnt(6)\n\t"
      "v_add_f32_e32 v112, v112, v116\n\t"
      "v_mfma_f32_16x16x32_bf16 v[192:195], v[128:131], v[8:11], 0\n\t"
      "s_cbranch_vccnz FP29R_%=\n\t"
      "FP29B_%=:\n\t"
      "v_add_f32_e32 v113, v113, v117\n\t"
      "v_add_f32_e32 v114, v114, v118\n\t"
      "v_mfma_f32_16x16x32_bf16 v[196:199], v[128:131], v[12:15], 0\n\t"
      "global_load_dwordx4 v[128:131], v224, s[62:63] sc1\n\t"
      "v_add_f32_e32 v115, v115, v119\n\t"
      "v_mfma_f32_16x16x32_bf16 v[184:187], v[132:135], v[16:19], v[184:187]\n\t"
      "s_waitcnt vmcnt(6)\n\t"
      "v_cmp_eq_u32_e32 vcc, -1, v136\n\t"
      "s_waitcnt lgkmcnt(4)\n\t"
      "v_add_f32_e32 v120, v120, v124\n\t"
      "v_mfma_f32_16x16x32_bf16 v[188:191], v[132:135], v[20:23], v[188:191]\n\t"
      "v_add_f32_e32 v121, v121, v125\n\t"
      "v_add_f32_e32 v122, v122, v126\n\t"
      "v_mfma_f32_16x16x32_bf16 v[192:195], v[132:135], v[24:27], v[192:195]\n\t"
      "s_cbranch_vccnz FP30R_%=\n\t"
      "FP30B_%=:\n\t"
      "v_add_f32_e32 v123, v123, v127\n\t"
      "s_waitcnt lgkmcnt(2)\n\t"
      "v_add_f32_e32 v200, v200, v204\n\t"
      "v_mfma_f32_16x16x32_bf16 v[196:199], v[132:135], v[28:31], v[196:199]\n\t"
      "global_load_dwordx4 v[132:135], v225, s[62:63] sc1\n\t"
      "v_add_f32_e32 v201, v201, v205\n\t"
      "v_mfma_f32_16x16x32_bf16 v[184:187], v[136:139], v[32:35], v[184:187]\n\t"
      "s_waitcnt vmcnt(6)\n\t"
      "v_cmp_eq_u32_e32 vcc, -1, v140\n\t"
      "v_add_f32_e32 v202, v202, v206\n\t"
      "v_mfma_f32_16x16x32_bf16 v[188:191], v[136:139], v[36:39], v[188:191]\n\t"
      "v_add_f32_e32 v203, v203, v207\n\t"
      "v_add_f32_e32 v112, v112, v120\n\t"
      "v_mfma_f32_16x16x32_bf16 v[192:195], v[136:139], v[40:43], v[192:195]\n\t"
      "s_cbranch_vccnz FP31R_%=\n\t"
      "FP31B_%=:\n\t"
      "v_add_f32_e32 v113, v113, v121\n\t"
      "v_add_f32_e32 v114, v114, v122\n\t"
      "v_mfma_f32_16x16x32_bf16 v[196:199], v[136:139], v[44:47], v[196:199]\n\t"
      "global_load_dwordx4 v[136:139], v226, s[62:63] sc1\n\t"
      "v_add_f32_e32 v115, v115, v123\n\t"
      "v_mfma_f32_16x16x32_bf16 v[184:187], v[140:143], v[48:51], v[184:187]\n\t"
      "s_waitcnt vmcnt(6)\n\t"
      "v_cmp_eq_u32_e32 vcc, -1, v144\n\t"
      "s_waitcnt lgkmcnt(0)\n\t"
      "v_add_f32_e32 v208, v208, v212\n\t"
      "v_mfma_f32_16x16x32_bf16 v[188:191], v[140:143], v[52:55], v[188:191]\n\t"
      "v_add_f32_e32 v209, v209, v213\n\t"
      "v_add_f32_e32 v210, v210, v214\n\t"
      "v_mfma_f32_16x16x32_bf16 v[192:195], v[140:143], v[56:59], v[192:195]\n\t"
      "s_cbranch_vccnz FP32R_%=\n\t"
      "FP32B_%=:\n\t"
      "v_add_f32_e32 v211, v211, v215\n\t"
      "v_add_f32_e32 v200, v200, v208\n\t"
      "v_mfma_f32_16x16x32_bf16 v[196:199], v[140:143], v[60:63], v[196:199]\n\t"
      "global_load_dwordx4 v[140:143], v227, s[62:63] sc1\n\t"
      "v_add_f32_e32 v201, v201, v209\n\t"
      "v_mfma_f32_16x16x32_bf16 v[184:187], v[144:147], v[64:67], v[184:187]\n\t"
      "s_waitcnt vmcnt(5)\n\t"
      "v_cmp_eq_u32_e32 vcc, -1, v148\n\t"
      "v_add_f32_e32 v202, v202, v210\n\t"
      "v_mfma_f32_16x16x32_bf16 v[188:191], v[144:147], v[68:71], v[188:191]\n\t"
      "v_add_f32_e32 v203, v203, v211\n\t"
      "v_add_f32_e32 v112, v112, v200\n\t"
      "v_mfma_f32_16x16x32_bf16 v[192:195], v[144:147], v[72:75], v[192:195]\n\t"
      "s_cbranch_vccnz FP33R_%=\n\t"
      "FP33B_%=:\n\t"
      "v_add_f32_e32 v113, v113, v201\n\t"
      "v_add_f32_e32 v114, v114, v202\n\t"
      "v_mfma_f32_16x16x32_bf16 v[196:199], v[144:147], v[76:79], v[196:199]\n\t"
      "global_load_dwordx4 v[144:147], v228, s[62:63] sc1\n\t"
      "v_add_f32_e32 v115, v115, v203\n\t"
      "v_mfma_f32_16x16x32_bf16 v[184:187], v[148:151], v[80:83], v[184:187]\n\t"
      "s_waitcnt vmcnt(5)\n\t"
      "v_cmp_eq_u32_e32 vcc, -1, v152\n\t"
      "v_cvt_pk_bf16_f32 v116, v112, v113\n\t"
      "v_mfma_f32_16x16x32_bf16 v[188:191], v[148:151], v[84:87], v[188:191]\n\t"
      "v_cvt_pk_bf16_f32 v117, v114, v115\n\t"
      "global_store_dwordx2 v245, v[116:117], s[8:9] offset:96\n\t"
      "v_mfma_f32_16x16x32_bf16 v[192:195], v[148:151], v[88:91], v[192:195]\n\t"
      "s_cbranch_vccnz FP34R_%=\n\t"
      "FP34B_%=:\n\t"
      "v_mfma_f32_16x16x32_bf16 v[196:199], v[148:151], v[92:95], v[196:199]\n\t"
      "global_load_dwordx4 v[148:151], v229, s[62:63] sc1\n\t"
      "v_mfma_f32_16x16x32_bf16 v[184:187], v[152:155], v[96:99], v[184:187]\n\t"
      "s_waitcnt vmcnt(6)\n\t"
      "v_cmp_eq_u32_e32 vcc, -1, v128\n\t"
      "v_mfma_f32_16x16x32_bf16 v[188:191], v[152:155], v[100:103], v[188:191]\n\t"
      "v_mfma_f32_16x16x32_bf16 v[192:195], v[152:155], v[104:107], v[192:195]\n\t"
      "s_cbranch_vccnz FP35R_%=\n\t"
      "FP35B_%=:\n\t"
      "v_mfma_f32_16x16x32_bf16 v[196:199], v[152:155], v[108:111], v[196:199]\n\t"
      "global_load_dwordx4 v[152:155], v230, s[62:63] sc1\n\t"
      "s_nop 0\n\t"
      "v_cmp_u_f32_e32 vcc, v184, v185\n\t"
      "v_cmp_u_f32_e64 s[48:49], v186, v187\n\t"
      "s_nop 0\n\t"
      "s_or_b64 vcc, vcc, s[48:49]\n\t"
      "s_cbranch_scc1 NP4R_%=\n\t"
      "NP4B_%=:\n\t"
      "ds_write_b128 v243, v[184:187]\n\t"
      "ds_write_b128 v220, v[188:191]\n\t"
      "ds_write_b128 v221, v[192:195]\n\t"
      "ds_write_b128 v222, v[196:199]\n\t"
      "s_waitcnt lgkmcnt(0)\n\t"
      "s_barrier\n\t"
      "ds_read_b128 v[112:115], v244\n\t"
      "ds_read_b128 v[116:119], v244 offset:4096\n\t"
      "ds_read_b128 v[120:123], v244 offset:8192\n\t"
      "ds_read_b128 v[124:127], v244 offset:12288\n\t"
      "ds_read_b128 v[184:187], v244 offset:16384\n\t"
      "ds_read_b128 v[188:191], v244 offset:20480\n\t"
      "ds_read_b128 v[192:195], v244 offset:24576\n\t"
      "ds_read_b128 v[196:199], v244 offset:28672\n\t"
      "v_mfma_f32_16x16x32_bf16 v[200:203], v[128:131], v[0:3], v[176:179]\n\t"
      "s_waitcnt vmcnt(6)\n\t"
      "v_cmp_eq_u32_e32 vcc, -1, v132\n\t"
      "v_mfma_f32_16x16x32_bf16 v[204:207], v[128:131], v[4:7], 0\n\t"
      "s_waitcnt lgkmcnt(6)\n\t"
      "v_add_f32_e32 v112, v112, v116\n\t"
      "v_mfma_f32_16x16x32_bf16 v[208:211], v[128:131], v[8:11], 0\n\t"
      "s_cbranch_vccnz FP36R_%=\n\t"
      "FP36B_%=:\n\t"
      "v_add_f32_e32 v113, v113, v117\n\t"
      "v_add_f32_e32 v114, v114, v118\n\t"
      "v_mfma_f32_16x16x32_bf16 v[212:215], v[128:131], v[12:15], 0\n\t"
      "global_load_dwordx4 v[128:131], v224, s[64:65] sc1\n\t"
      "v_add_f32_e32 v115, v115, v119\n\t"
      "v_mfma_f32_16x16x32_bf16 v[200:203], v[132:135], v[16:19], v[200:203]\n\t"
      "s_waitcnt vmcnt(6)\n\t"
      "v_cmp_eq_u32_e32 vcc, -1, v136\n\t"
      "s_waitcnt lgkmcnt(4)\n\t"
      "v_add_f32_e32 v120, v120, v124\n\t"
      "v_mfma_f32_16x16x32_bf16 v[204:207], v[132:135], v[20:23], v[204:207]\n\t"
      "v_add_f32_e32 v121, v121, v125\n\t"
      "v_add_f32_e32 v122, v122, v126\n\t"
      "v_mfma_f32_16x16x32_bf16 v[208:211], v[132:135], v[24:27], v[208:211]\n\t"
      "s_cbranch_vccnz FP37R_%=\n\t"
      "FP37B_%=:\n\t"
      "v_add_f32_e32 v123, v123, v127\n\t"
      "s_waitcnt lgkmcnt(2)\n\t"
      "v_add_f32_e32 v184, v184, v188\n\t"
      "v_mfma_f32_16x16x32_bf16 v[212:215], v[132:135], v[28:31], v[212:215]\n\t"
      "global_load_dwordx4 v[132:135], v225, s[64:65] sc1\n\t"
      "v_add_f32_e32 v185, v185, v189\n\t"
      "v_mfma_f32_16x16x32_bf16 v[200:203], v[136:139], v[32:35], v[200:203]\n\t"
      "s_waitcnt vmcnt(6)\n\t"
      "v_cmp_eq_u32_e32 vcc, -1, v140\n\t"
      "v_add_f32_e32 v186, v186, v190\n\t"
      "v_mfma_f32_16x16x32_bf16 v[204:207], v[136:139], v[36:39], v[204:207]\n\t"
      "v_add_f32_e32 v187, v187, v191\n\t"
      "v_add_f32_e32 v112, v112, v120\n\t"
      "v_mfma_f32_16x16x32_bf16 v[208:211], v[136:139], v[40:43], v[208:211]\n\t"
      "s_cbranch_vccnz FP38R_%=\n\t"
      "FP38B_%=:\n\t"
      "v_add_f32_e32 v113, v113, v121\n\t"
      "v_add_f32_e32 v114, v114, v122\n\t"
      "v_mfma_f32_16x16x32_bf16 v[212:215], v[136:139], v[44:47], v[212:215]\n\t"
      "global_load_dwordx4 v[136:139], v226, s[64:65] sc1\n\t"
      "v_add_f32_e32 v115, v115, v123\n\t"
      "v_mfma_f32_16x16x32_bf16 v[200:203], v[140:143], v[48:51], v[200:203]\n\t"
      "s_waitcnt vmcnt(6)\n\t"
      "v_cmp_eq_u32_e32 vcc, -1, v144\n\t"
      "s_waitcnt lgkmcnt(0)\n\t"
      "v_add_f32_e32 v192, v192, v196\n\t"
      "v_mfma_f32_16x16x32_bf16 v[204:207], v[140:143], v[52:55], v[204:207]\n\t"
      "v_add_f32_e32 v193, v193, v197\n\t"
      "v_add_f32_e32 v194, v194, v198\n\t"
      "v_mfma_f32_16x16x32_bf16 v[208:211], v[140:143], v[56:59], v[208:211]\n\t"
      "s_cbranch_vccnz FP39R_%=\n\t"
      "FP39B_%=:\n\t"
      "v_add_f32_e32 v195, v195, v199\n\t"
      "v_add_f32_e32 v184, v184, v192\n\t"
      "v_mfma_f32_16x16x32_bf16 v[212:215], v[140:143], v[60:63], v[212:215]\n\t"
      "global_load_dwordx4 v[140:143], v227, s[64:65] sc1\n\t"
      "v_add_f32_e32 v185, v185, v193\n\t"
      "v_mfma_f32_16x16x32_bf16 v[200:203], v[144:147], v[64:67], v[200:203]\n\t"
      "s_waitcnt vmcnt(5)\n\t"
      "v_cmp_eq_u32_e32 vcc, -1, v148\n\t"
      "v_add_f32_e32 v186, v186, v194\n\t"
      "v_mfma_f32_16x16x32_bf16 v[204:207], v[144:147], v[68:71], v[204:207]\n\t"
      "v_add_f32_e32 v187, v187, v195\n\t"
      "v_add_f32_e32 v112, v112, v184\n\t"
      "v_mfma_f32_16x16x32_bf16 v[208:211], v[144:147], v[72:75], v[208:211]\n\t"
      "s_cbranch_vccnz FP40R_%=\n\t"
      "FP40B_%=:\n\t"
      "v_add_f32_e32 v113, v113, v185\n\t"
      "v_add_f32_e32 v114, v114, v186\n\t"
      "v_mfma_f32_16x16x32_bf16 v[212:215], v[144:147], v[76:79], v[212:215]\n\t"
      "global_load_dwordx4 v[144:147], v228, s[64:65] sc1\n\t"
      "v_add_f32_e32 v115, v115, v187\n\t"
      "v_mfma_f32_16x16x32_bf16 v[200:203], v[148:151], v[80:83], v[200:203]\n\t"
      "s_waitcnt vmcnt(5)\n\t"
      "v_cmp_eq_u32_e32 vcc, -1, v152\n\t"
      "v_cvt_pk_bf16_f32 v116, v112, v113\n\t"
      "v_mfma_f32_16x16x32_bf16 v[204:207], v[148:151], v[84:87], v[204:207]\n\t"
      "v_cvt_pk_bf16_f32 v117, v114, v115\n\t"
      "global_store_dwordx2 v245, v[116:117], s[8:9] offset:128\n\t"
      "v_mfma_f32_16x16x32_bf16 v[208:211], v[148:151], v[88:91], v[208:211]\n\t"
      "s_cbranch_vccnz FP41R_%=\n\t"
      "FP41B_%=:\n\t"
      "v_mfma_f32_16x16x32_bf16 v[212:215], v[148:151], v[92:95], v[212:215]\n\t"
      "global_load_dwordx4 v[148:151], v229, s[64:65] sc1\n\t"
      "v_mfma_f32_16x16x32_bf16 v[200:203], v[152:155], v[96:99], v[200:203]\n\t"
      "s_waitcnt vmcnt(6)\n\t"
      "v_cmp_eq_u32_e32 vcc, -1, v128\n\t"
      "v_mfma_f32_16x16x32_bf16 v[204:207], v[152:155], v[100:103], v[204:207]\n\t"
      "v_mfma_f32_16x16x32_bf16 v[208:211], v[152:155], v[104:107], v[208:211]\n\t"
      "s_cbranch_vccnz FP42R_%=\n\t"
      "FP42B_%=:\n\t"
      "v_mfma_f32_16x16x32_bf16 v[212:215], v[152:155], v[108:111], v[212:215]\n\t"
      "global_load_dwordx4 v[152:155], v230, s[64:65] sc1\n\t"
      "s_nop 0\n\t"
      "v_cmp_u_f32_e32 vcc, v200, v201\n\t"
      "v_cmp_u_f32_e64 s[48:49], v202, v203\n\t"
      "s_nop 0\n\t"
      "s_or_b64 vcc, vcc, s[48:49]\n\t"
      "s_cbranch_scc1 NP5R_%=\n\t"
      "NP5B_%=:\n\t"
      "ds_write_b128 v243, v[200:203] offset:32768\n\t"
      "ds_write_b128 v220, v[204:207] offset:32768\n\t"
      "ds_write_b128 v221, v[208:211] offset:32768\n\t"
      "ds_write_b128 v222, v[212:215] offset:32768\n\t"
      "s_waitcnt lgkmcnt(0)\n\t"
      "s_barrier\n\t"
      "ds_read_b128 v[112:115], v244 offset:32768\n\t"
      "ds_read_b128 v[116:119], v244 offset:36864\n\t"
      "ds_read_b128 v[120:123], v244 offset:40960\n\t"
      "ds_read_b128 v[124:127], v244 offset:45056\n\t"
      "ds_read_b128 v[200:203], v244 offset:49152\n\t"
      "ds_read_b128 v[204:207], v244 offset:53248\n\t"
      "ds_read_b128 v[208:211], v244 offset:57344\n\t"
      "ds_read_b128 v[212:215], v244 offset:61440\n\t"
      "v_mfma_f32_16x16x32_bf16 v[184:187], v[128:131], v[0:3], v[180:183]\n\t"
      "s_waitcnt vmcnt(6)\n\t"
      "v_cmp_eq_u32_e32 vcc, -1, v132\n\t"
      "v_mfma_f32_16x16x32_bf16 v[188:191], v[128:131], v[4:7], 0\n\t"
      "v_mfma_f32_16x16x32_bf16 v[192:195], v[128:131], v[8:11], 0\n\t"
      "s_cbranch_vccnz FP43R_%=\n\t"
      "FP43B_%=:\n\t"
      "v_mfma_f32_16x16x32_bf16 v[196:199], v[128:131], v[12:15], 0\n\t"
      "v_mfma_f32_16x16x32_bf16 v[184:187], v[132:135], v[16:19], v[184:187]\n\t"
      "s_waitcnt vmcnt(5)\n\t"
      "v_cmp_eq_u32_e32 vcc, -1, v136\n\t"
      "s_waitcnt lgkmcnt(6)\n\t"
      "v_add_f32_e32 v112, v112, v116\n\t"
      "v_mfma_f32_16x16x32_bf16 v[188:191], v[132:135], v[20:23], v[188:191]\n\t"
      "v_add_f32_e32 v113, v113, v117\n\t"
      "v_add_f32_e32 v114, v114, v118\n\t"
      "v_mfma_f32_16x16x32_bf16 v[192:195], v[132:135], v[24:27], v[192:195]\n\t"
      "s_cbranch_vccnz FP44R_%=\n\t"
      "FP44B_%=:\n\t"
      "v_add_f32_e32 v115, v115, v119\n\t"
      "s_waitcnt lgkmcnt(4)\n\t"
      "v_add_f32_e32 v120, v120, v124\n\t"
      "v_mfma_f32_16x16x32_bf16 v[196:199], v[132:135], v[28:31], v[196:199]\n\t"
      "v_add_f32_e32 v121, v121, v125\n\t"
      "v_add_f32_e32 v122, v122, v126\n\t"
      "v_mfma_f32_16x16x32_bf16 v[184:187], v[136:139], v[32:35], v[184:187]\n\t"
      "s_waitcnt vmcnt(4)\n\t"
      "v_cmp_eq_u32_e32 vcc, -1, v140\n\t"
      "v_add_f32_e32 v123, v123, v127\n\t"
      "v_mfma_f32_16x16x32_bf16 v[188:191], v[136:139], v[36:39], v[188:191]\n\t"
      "s_waitcnt lgkmcnt(2)\n\t"
      "v_add_f32_e32 v200, v200, v204\n\t"
      "v_add_f32_e32 v201, v201, v205\n\t"
      "v_mfma_f32_16x16x32_bf16 v[192:195], v[136:139], v[40:43], v[192:195]\n\t"
      "s_cbranch_vccnz FP45R_%=\n\t"
      "FP45B_%=:\n\t"
      "v_add_f32_e32 v202, v202, v206\n\t"
      "v_add_f32_e32 v203, v203, v207\n\t"
      "v_mfma_f32_16x16x32_bf16 v[196:199], v[136:139], v[44:47], v[196:199]\n\t"
      "v_add_f32_e32 v112, v112, v120\n\t"
      "v_add_f32_e32 v113, v113, v121\n\t"
      "v_mfma_f32_16x16x32_bf16 v[184:187], v[140:143], v[48:51], v[184:187]\n\t"
      "s_waitcnt vmcnt(3)\n\t"
      "v_cmp_eq_u32_e32 vcc, -1, v144\n\t"
      "v_add_f32_e32 v114, v114, v122\n\t"
      "v_mfma_f32_16x16x32_bf16 v[188:191], v[140:143], v[52:55], v[188:191]\n\t"
      "v_add_f32_e32 v115, v115, v123\n\t"
      "s_waitcnt lgkmcnt(0)\n\t"
      "v_add_f32_e32 v208, v208, v212\n\t"
      "v_mfma_f32_16x16x32_bf16 v[192:195], v[140:143], v[56:59], v[192:195]\n\t"
      "s_cbranch_vccnz FP46R_%=\n\t"
      "FP46B_%=:\n\t"
      "v_add_f32_e32 v209, v209, v213\n\t"
      "v_add_f32_e32 v210, v210, v214\n\t"
      "v_mfma_f32_16x16x32_bf16 v[196:199], v[140:143], v[60:63], v[196:199]\n\t"
      "v_add_f32_e32 v211, v211, v215\n\t"
      "v_add_f32_e32 v200, v200, v208\n\t"
      "v_mfma_f32_16x16x32_bf16 v[184:187], v[144:147], v[64:67], v[184:187]\n\t"
      "s_waitcnt vmcnt(1)\n\t"
      "v_cmp_eq_u32_e32 vcc, -1, v148\n\t"
      "v_add_f32_e32 v201, v201, v209\n\t"
      "v_mfma_f32_16x16x32_bf16 v[188:191], v[144:147], v[68:71], v[188:191]\n\t"
      "v_add_f32_e32 v202, v202, v210\n\t"
      "v_add_f32_e32 v203, v203, v211\n\t"
      "v_mfma_f32_16x16x32_bf16 v[192:195], v[144:147], v[72:75], v[192:195]\n\t"
      "s_cbranch_vccnz FP47R_%=\n\t"
      "FP47B_%=:\n\t"
      "v_add_f32_e32 v112, v112, v200\n\t"
      "v_add_f32_e32 v113, v113, v201\n\t"
      "v_mfma_f32_16x16x32_bf16 v[196:199], v[144:147], v[76:79], v[196:199]\n\t"
      "v_add_f32_e32 v114, v114, v202\n\t"
      "v_add_f32_e32 v115, v115, v203\n\t"
      "v_mfma_f32_16x16x32_bf16 v[184:187], v[148:151], v[80:83], v[184:187]\n\t"
      "s_waitcnt vmcnt(0)\n\t"
      "v_cmp_eq_u32_e32 vcc, -1, v152\n\t"
      "v_cvt_pk_bf16_f32 v116, v112, v113\n\t"
      "v_mfma_f32_16x16x32_bf16 v[188:191], v[148:151], v[84:87], v[188:191]\n\t"
      "v_cvt_pk_bf16_f32 v117, v114, v115\n\t"
      "global_store_dwordx2 v245, v[116:117], s[8:9] offset:160\n\t"
      "v_mfma_f32_16x16x32_bf16 v[192:195], v[148:151], v[88:91], v[192:195]\n\t"
      "s_cbranch_vccnz FP48R_%=\n\t"
      "FP48B_%=:\n\t"
      "v_mfma_f32_16x16x32_bf16 v[196:199], v[148:151], v[92:95], v[196:199]\n\t"
      "v_mfma_f32_16x16x32_bf16 v[184:187], v[152:155], v[96:99], v[184:187]\n\t"
      "v_mfma_f32_16x16x32_bf16 v[188:191], v[152:155], v[100:103], v[188:191]\n\t"
      "v_mfma_f32_16x16x32_bf16 v[192:195], v[152:155], v[104:107], v[192:195]\n\t"
      "v_mfma_f32_16x16x32_bf16 v[196:199], v[152:155], v[108:111], v[196:199]\n\t"
      "s_nop 4\n\t"
      "v_cmp_u_f32_e32 vcc, v184, v185\n\t"
      "v_cmp_u_f32_e64 s[48:49], v186, v187\n\t"
      "s_nop 0\n\t"
      "s_or_b64 vcc, vcc, s[48:49]\n\t"
      "s_cbranch_scc1 NP6R_%=\n\t"
      "NP6B_%=:\n\t"
      "ds_write_b128 v243, v[184:187]\n\t"
      "ds_write_b128 v220, v[188:191]\n\t"
      "ds_write_b128 v221, v[192:195]\n\t"
      "ds_write_b128 v222, v[196:199]\n\t"
      "s_waitcnt lgkmcnt(0)\n\t"
      "s_barrier\n\t"
      "ds_read_b128 v[112:115], v244\n\t"
      "ds_read_b128 v[116:119], v244 offset:4096\n\t"
      "ds_read_b128 v[120:123], v244 offset:8192\n\t"
      "ds_read_b128 v[124:127], v244 offset:12288\n\t"
      "ds_read_b128 v[184:187], v244 offset:16384\n\t"
      "ds_read_b128 v[188:191], v244 offset:20480\n\t"
      "ds_read_b128 v[192:195], v244 offset:24576\n\t"
      "ds_read_b128 v[196:199], v244 offset:28672\n\t"
      "s_waitcnt lgkmcnt(6)\n\t"
      "v_add_f32_e32 v112, v112, v116\n\t"
      "v_add_f32_e32 v113, v113, v117\n\t"
      "v_add_f32_e32 v114, v114, v118\n\t"
      "v_add_f32_e32 v115, v115, v119\n\t"
      "s_waitcnt lgkmcnt(4)\n\t"
      "v_add_f32_e32 v120, v120, v124\n\t"
      "v_add_f32_e32 v121, v121, v125\n\t"
      "v_add_f32_e32 v122, v122, v126\n\t"
      "v_add_f32_e32 v123, v123, v127\n\t"
      "s_waitcnt lgkmcnt(2)\n\t"
      "v_add_f32_e32 v184, v184, v188\n\t"
      "v_add_f32_e32 v185, v185, v189\n\t"
      "v_add_f32_e32 v186, v186, v190\n\t"
      "v_add_f32_e32 v187, v187, v191\n\t"
      "v_add_f32_e32 v112, v112, v120\n\t"
      "v_add_f32_e32 v113, v113, v121\n\t"
      "v_add_f32_e32 v114, v114, v122\n\t"
      "v_add_f32_e32 v115, v115, v123\n\t"
      "s_waitcnt lgkmcnt(0)\n\t"
      "v_add_f32_e32 v192, v192, v196\n\t"
      "v_add_f32_e32 v193, v193, v197\n\t"
      "v_add_f32_e32 v194, v194, v198\n\t"
      "v_add_f32_e32 v195, v195, v199\n\t"
      "v_add_f32_e32 v184, v184, v192\n\t"
      "v_add_f32_e32 v185, v185, v193\n\t"
      "v_add_f32_e32 v186, v186, v194\n\t"
      "v_add_f32_e32 v187, v187, v195\n\t"
      "v_add_f32_e32 v112, v112, v184\n\t"
      "v_add_f32_e32 v113, v113, v185\n\t"
      "v_add_f32_e32 v114, v114, v186\n\t"
      "v_add_f32_e32 v115, v115, v187\n\t"
      "v_cvt_pk_bf16_f32 v116, v112, v113\n\t"
      "v_cvt_pk_bf16_f32 v117, v114, v115\n\t"
      "global_store_dwordx2 v245, v[116:117], s[8:9] offset:192\n\t"
      "GX_%=:\n\t"
      "s_bitcmp1_b32 s16, 5\n\t"
      "s_cbranch_scc0 EP_%=\n\t"
      "v_mov_b32_e32 v0, s26\n\t"
      "v_mov_b32_e32 v1, s27\n\t"
      "v_mov_b32_e32 v2, 0\n\t"
      "s_mov_b64 exec, 1\n\t"
      "global_store_dwordx2 v2, v[0:1], s[24:25] sc1\n\t"
      "s_mov_b64 exec, -1\n\t"
      "EP_%=:\n\t"
      "s_endpgm\n\t"
      ".p2align 6\n\t"
      "GQ_%=:\n\t"
      "s_waitcnt vmcnt(6)\n\t"
      "v_max3_u32 v234, v128, v129, v130\n\t"
      "v_max_u32_e32 v234, v234, v131\n\t"
      "v_cmp_eq_u32_e32 vcc, -1, v234\n\t"
      "s_cbranch_vccnz FQ0R_%=\n\t"
      "FQ0B_%=:\n\t"
      "v_mfma_f32_16x16x32_bf16 v[184:187], v[128:131], v[0:3], 0\n\t"
      "v_mfma_f32_16x16x32_bf16 v[188:191], v[128:131], v[4:7], 0\n\t"
      "v_mfma_f32_16x16x32_bf16 v[192:195], v[128:131], v[8:11], 0\n\t"
      "v_mfma_f32_16x16x32_bf16 v[196:199], v[128:131], v[12:15], 0\n\t"
      "global_load_dwordx4 v[128:131], v224, s[54:55] sc1\n\t"
      "s_waitcnt vmcnt(6)\n\t"
      "v_max3_u32 v234, v132, v133, v134\n\t"
      "v_max_u32_e32 v234, v234, v135\n\t"
      "v_cmp_eq_u32_e32 vcc, -1, v234\n\t"
      "s_cbranch_vccnz FQ1R_%=\n\t"
      "FQ1B_%=:\n\t"
      "v_mfma_f32_16x16x32_bf16 v[184:187], v[132:135], v[16:19], v[184:187]\n\t"
      "v_mfma_f32_16x16x32_bf16 v[188:191], v[132:135], v[20:23], v[188:191]\n\t"
      "v_mfma_f32_16x16x32_bf16 v[192:195], v[132:135], v[24:27], v[192:195]\n\t"
      "v_mfma_f32_16x16x32_bf16 v[196:199], v[132:135], v[28:31], v[196:199]\n\t"
      "global_load_dwordx4 v[132:135], v225, s[54:55] sc1\n\t"
      "s_waitcnt vmcnt(6)\n\t"
      "v_max3_u32 v234, v136, v137, v138\n\t"
      "v_max_u32_e32 v234, v234, v139\n\t"
      "v_cmp_eq_u32_e32 vcc, -1, v234\n\t"
      "s_cbranch_vccnz FQ2R_%=\n\t"
      "FQ2B_%=:\n\t"
      "v_mfma_f32_16x16x32_bf16 v[184:187], v[136:139], v[32:35], v[184:187]\n\t"
      "v_mfma_f32_16x16x32_bf16 v[188:191], v[136:139], v[36:39], v[188:191]\n\t"
      "v_mfma_f32_16x16x32_bf16 v[192:195], v[136:139], v[40:43], v[192:195]\n\t"
      "v_mfma_f32_16x16x32_bf16 v[196:199], v[136:139], v[44:47], v[196:199]\n\t"
      "global_load_dwordx4 v[136:139], v226, s[54:55] sc1\n\t"
      "s_waitcnt vmcnt(6)\n\t"
      "v_max3_u32 v234, v140, v141, v142\n\t"
      "v_max_u32_e32 v234, v234, v143\n\t"
      "v_cmp_eq_u32_e32 vcc, -1, v234\n\t"
      "s_cbranch_vccnz FQ3R_%=\n\t"
      "FQ3B_%=:\n\t"
      "v_mfma_f32_16x16x32_bf16 v[184:187], v[140:143], v[48:51], v[184:187]\n\t"
      "v_mfma_f32_16x16x32_bf16 v[188:191], v[140:143], v[52:55], v[188:191]\n\t"
      "v_mfma_f32_16x16x32_bf16 v[192:195], v[140:143], v[56:59], v[192:195]\n\t"
      "v_mfma_f32_16x16x32_bf16 v[196:199], v[140:143], v[60:63], v[196:199]\n\t"
      "global_load_dwordx4 v[140:143], v227, s[54:55] sc1\n\t"
      "s_waitcnt vmcnt(6)\n\t"
      "v_max3_u32 v234, v144, v145, v146\n\t"
      "v_max_u32_e32 v234, v234, v147\n\t"
      "v_cmp_eq_u32_e32 vcc, -1, v234\n\t"
      "s_cbranch_vccnz FQ4R_%=\n\t"
      "FQ4B_%=:\n\t"
      "v_mfma_f32_16x16x32_bf16 v[184:187], v[144:147], v[64:67], v[184:187]\n\t"
      "v_mfma_f32_16x16x32_bf16 v[188:191], v[144:147], v[68:71], v[188:191]\n\t"
      "v_mfma_f32_16x16x32_bf16 v[192:195], v[144:147], v[72:75], v[192:195]\n\t"
      "v_mfma_f32_16x16x32_bf16 v[196:199], v[144:147], v[76:79], v[196:199]\n\t"
      "global_load_dwordx4 v[144:147], v228, s[54:55] sc1\n\t"
      "s_waitcnt vmcnt(6)\n\t"
      "v_max3_u32 v234, v148, v149, v150\n\t"
      "v_max_u32_e32 v234, v234, v151\n\t"
      "v_cmp_eq_u32_e32 vcc, -1, v234\n\t"
      "s_cbranch_vccnz FQ5R_%=\n\t"
      "FQ5B_%=:\n\t"
      "v_mfma_f32_16x16x32_bf16 v[184:187], v[148:151], v[80:83], v[184:187]\n\t"
      "v_mfma_f32_16x16x32_bf16 v[188:191], v[148:151], v[84:87], v[188:191]\n\t"
      "v_mfma_f32_16x16x32_bf16 v[192:195], v[148:151], v[88:91], v[192:195]\n\t"
      "v_mfma_f32_16x16x32_bf16 v[196:199], v[148:151], v[92:95], v[196:199]\n\t"
      "global_load_dwordx4 v[148:151], v229, s[54:55] sc1\n\t"
      "s_waitcnt vmcnt(6)\n\t"
      "v_max3_u32 v234, v152, v153, v154\n\t"
      "v_max_u32_e32 v234, v234, v155\n\t"
      "v_cmp_eq_u32_e32 vcc, -1, v234\n\t"
      "s_cbranch_vccnz FQ6R_%=\n\t"
      "FQ6B_%=:\n\t"
      "v_mfma_f32_16x16x32_bf16 v[184:187], v[152:155], v[96:99], v[184:187]\n\t"
      "s_waitcnt vmcnt(5)\n\t"
      "v_cmp_eq_u32_e32 vcc, -1, v128\n\t"
      "v_mfma_f32_16x16x32_bf16 v[188:191], v[152:155], v[100:103], v[188:191]\n\t"
      "v_mfma_f32_16x16x32_bf16 v[192:195], v[152:155], v[104:107], v[192:195]\n\t"
      "s_cbranch_vccnz FQ7R_%=\n\t"
      "FQ7B_%=:\n\t"
      "v_mfma_f32_16x16x32_bf16 v[196:199], v[152:155], v[108:111], v[196:199]\n\t"
      "global_load_dwordx4 v[152:155], v230, s[54:55] sc1\n\t"
      "s_nop 0\n\t"
      "ds_write_b128 v243, v[184:187]\n\t"
      "s_nop 1\n\t"
      "ds_write_b128 v220, v[188:191]\n\t"
      "ds_write_b128 v221, v[192:195]\n\t"
      "s_nop 0\n\t"
      "ds_write_b128 v222, v[196:199]\n\t"
      "v_mfma_f32_16x16x32_bf16 v[200:203], v[128:131], v[0:3], 0\n\t"
      "s_waitcnt vmcnt(5)\n\t"
      "v_cmp_eq_u32_e32 vcc, -1, v132\n\t"
      "v_mfma_f32_16x16x32_bf16 v[204:207], v[128:131], v[4:7], 0\n\t"
      "v_mfma_f32_16x16x32_bf16 v[208:211], v[128:131], v[8:11], 0\n\t"
      "s_cbranch_vccnz FQ8R_%=\n\t"
      "FQ8B_%=:\n\t"
      "v_mfma_f32_16x16x32_bf16 v[212:215], v[128:131], v[12:15], 0\n\t"
      "global_load_dwordx4 v[128:131], v224, s[56:57] sc1\n\t"
      "v_mfma_f32_16x16x32_bf16 v[200:203], v[132:135], v[16:19], v[200:203]\n\t"
      "s_waitcnt vmcnt(5)\n\t"
      "v_cmp_eq_u32_e32 vcc, -1, v136\n\t"
      "v_mfma_f32_16x16x32_bf16 v[204:207], v[132:135], v[20:23], v[204:207]\n\t"
      "v_mfma_f32_16x16x32_bf16 v[208:211], v[132:135], v[24:27], v[208:211]\n\t"
      "s_cbranch_vccnz FQ9R_%=\n\t"
      "FQ9B_%=:\n\t"
      "v_mfma_f32_16x16x32_bf16 v[212:215], v[132:135], v[28:31], v[212:215]\n\t"
      "global_load_dwordx4 v[132:135], v225, s[56:57] sc1\n\t"
      "v_mfma_f32_16x16x32_bf16 v[200:203], v[136:139], v[32:35], v[200:203]\n\t"
      "s_waitcnt vmcnt(5)\n\t"
      "v_cmp_eq_u32_e32 vcc, -1, v140\n\t"
      "v_mfma_f32_16x16x32_bf16 v[204:207], v[136:139], v[36:39], v[204:207]\n\t"
      "v_mfma_f32_16x16x32_bf16 v[208:211], v[136:139], v[40:43], v[208:211]\n\t"
      "s_cbranch_vccnz FQ10R_%=\n\t"
      "FQ10B_%=:\n\t"
      "v_mfma_f32_16x16x32_bf16 v[212:215], v[136:139], v[44:47], v[212:215]\n\t"
      "global_load_dwordx4 v[136:139], v226, s[56:57] sc1\n\t"
      "v_mfma_f32_16x16x32_bf16 v[200:203], v[140:143], v[48:51], v[200:203]\n\t"
      "s_waitcnt vmcnt(5)\n\t"
      "v_cmp_eq_u32_e32 vcc, -1, v144\n\t"
      "v_mfma_f32_16x16x32_bf16 v[204:207], v[140:143], v[52:55], v[204:207]\n\t"
      "v_mfma_f32_16x16x32_bf16 v[208:211], v[140:143], v[56:59], v[208:211]\n\t"
      "s_cbranch_vccnz FQ11R_%=\n\t"
      "FQ11B_%=:\n\t"
      "v_mfma_f32_16x16x32_bf16 v[212:215], v[140:143], v[60:63], v[212:215]\n\t"
      "global_load_dwordx4 v[140:143], v227, s[56:57] sc1\n\t"
      "v_mfma_f32_16x16x32_bf16 v[200:203], v[144:147], v[64:67], v[200:203]\n\t"
      "s_waitcnt vmcnt(5)\n\t"
      "v_cmp_eq_u32_e32 vcc, -1, v148\n\t"
      "v_mfma_f32_16x16x32_bf16 v[204:207], v[144:147], v[68:71], v[204:207]\n\t"
      "v_mfma_f32_16x16x32_bf16 v[208:211], v[144:147], v[72:75], v[208:211]\n\t"
      "s_cbranch_vccnz FQ12R_%=\n\t"
      "FQ12B_%=:\n\t"
      "v_mfma_f32_16x16x32_bf16 v[212:215], v[144:147], v[76:79], v[212:215]\n\t"
      "global_load_dwordx4 v[144:147], v228, s[56:57] sc1\n\t"
      "s_waitcnt lgkmcnt(0)\n\t"
      "s_barrier\n\t"
      "s_setprio 2\n\t"
      "v_mfma_f32_16x16x32_bf16 v[200:203], v[148:151], v[80:83], v[200:203]\n\t"
      "s_waitcnt vmcnt(5)\n\t"
      "v_cmp_eq_u32_e32 vcc, -1, v152\n\t"
      "v_mfma_f32_16x16x32_bf16 v[204:207], v[148:151], v[84:87], v[204:207]\n\t"
      "v_mfma_f32_16x16x32_bf16 v[208:211], v[148:151], v[88:91], v[208:211]\n\t"
      "s_cbranch_vccnz FQ13R_%=\n\t"
      "FQ13B_%=:\n\t"
      "v_mfma_f32_16x16x32_bf16 v[212:215], v[148:151], v[92:95], v[212:215]\n\t"
      "global_load_dwordx4 v[148:151], v229, s[56:57] sc1\n\t"
      "v_mfma_f32_16x16x32_bf16 v[200:203], v[152:155], v[96:99], v[200:203]\n\t"
      "s_waitcnt vmcnt(5)\n\t"
      "v_cmp_eq_u32_e32 vcc, -1, v128\n\t"
      "v_mfma_f32_16x16x32_bf16 v[204:207], v[152:155], v[100:103], v[204:207]\n\t"
      "v_mfma_f32_16x16x32_bf16 v[208:211], v[152:155], v[104:107], v[208:211]\n\t"
      "s_cbranch_vccnz FQ14R_%=\n\t"
      "FQ14B_%=:\n\t"
      "v_mfma_f32_16x16x32_bf16 v[212:215], v[152:155], v[108:111], v[212:215]\n\t"
      "global_load_dwordx4 v[152:155], v230, s[56:57] sc1\n\t"
      "s_setprio 0\n\t"
      "v_cmp_u_f32_e32 vcc, v200, v201\n\t"
      "v_cmp_u_f32_e64 s[48:49], v202, v203\n\t"
      "s_nop 0\n\t"
      "s_or_b64 vcc, vcc, s[48:49]\n\t"
      "s_cbranch_scc1 NQ1R_%=\n\t"
      "NQ1B_%=:\n\t"
      "ds_write_b128 v243, v[200:203] offset:32768\n\t"
      "ds_write_b128 v220, v[204:207] offset:32768\n\t"
      "ds_write_b128 v221, v[208:211] offset:32768\n\t"
      "ds_write_b128 v222, v[212:215] offset:32768\n\t"
      "v_mfma_f32_16x16x32_bf16 v[184:187], v[128:131], v[0:3], 0\n\t"
      "s_waitcnt vmcnt(5)\n\t"
      "v_cmp_eq_u32_e32 vcc, -1, v132\n\t"
      "v_mfma_f32_16x16x32_bf16 v[188:191], v[128:131], v[4:7], 0\n\t"
      "v_mfma_f32_16x16x32_bf16 v[192:195], v[128:131], v[8:11], 0\n\t"
      "s_cbranch_vccnz FQ15R_%=\n\t"
      "FQ15B_%=:\n\t"
      "v_mfma_f32_16x16x32_bf16 v[196:199], v[128:131], v[12:15], 0\n\t"
      "global_load_dwordx4 v[128:131], v224, s[58:59] sc1\n\t"
      "v_mfma_f32_16x16x32_bf16 v[184:187], v[132:135], v[16:19], v[184:187]\n\t"
      "s_waitcnt vmcnt(5)\n\t"
      "v_cmp_eq_u32_e32 vcc, -1, v136\n\t"
      "v_mfma_f32_16x16x32_bf16 v[188:191], v[132:135], v[20:23], v[188:191]\n\t"
      "v_mfma_f32_16x16x32_bf16 v[192:195], v[132:135], v[24:27], v[192:195]\n\t"
      "s_cbranch_vccnz FQ16R_%=\n\t"
      "FQ16B_%=:\n\t"
      "v_mfma_f32_16x16x32_bf16 v[196:199], v[132:135], v[28:31], v[196:199]\n\t"
      "global_load_dwordx4 v[132:135], v225, s[58:59] sc1\n\t"
      "v_mfma_f32_16x16x32_bf16 v[184:187], v[136:139], v[32:35], v[184:187]\n\t"
      "s_waitcnt vmcnt(5)\n\t"
      "v_cmp_eq_u32_e32 vcc, -1, v140\n\t"
      "v_mfma_f32_16x16x32_bf16 v[188:191], v[136:139], v[36:39], v[188:191]\n\t"
      "v_mfma_f32_16x16x32_bf16 v[192:195], v[136:139], v[40:43], v[192:195]\n\t"
      "s_cbranch_vccnz FQ17R_%=\n\t"
      "FQ17B_%=:\n\t"
      "v_mfma_f32_16x16x32_bf16 v[196:199], v[136:139], v[44:47], v[196:199]\n\t"
      "global_load_dwordx4 v[136:139], v226, s[58:59] sc1\n\t"
      "v_mfma_f32_16x16x32_bf16 v[184:187], v[140:143], v[48:51], v[184:187]\n\t"
      "s_waitcnt vmcnt(5)\n\t"
      "v_cmp_eq_u32_e32 vcc, -1, v144\n\t"
      "v_mfma_f32_16x16x32_bf16 v[188:191], v[140:143], v[52:55], v[188:191]\n\t"
      "v_mfma_f32_16x16x32_bf16 v[192:195], v[140:143], v[56:59], v[192:195]\n\t"
      "s_cbranch_vccnz FQ18R_%=\n\t"
      "FQ18B_%=:\n\t"
      "v_mfma_f32_16x16x32_bf16 v[196:199], v[140:143], v[60:63], v[196:199]\n\t"
      "global_load_dwordx4 v[140:143], v227, s[58:59] sc1\n\t"
      "v_mfma_f32_16x16x32_bf16 v[184:187], v[144:147], v[64:67], v[184:187]\n\t"
      "s_waitcnt vmcnt(5)\n\t"
      "v_cmp_eq_u32_e32 vcc, -1, v148\n\t"
      "v_mfma_f32_16x16x32_bf16 v[188:191], v[144:147], v[68:71], v[188:191]\n\t"
      "v_mfma_f32_16x16x32_bf16 v[192:195], v[144:147], v[72:75], v[192:195]\n\t"
      "s_cbranch_vccnz FQ19R_%=\n\t"
      "FQ19B_%=:\n\t"
      "v_mfma_f32_16x16x32_bf16 v[196:199], v[144:147], v[76:79], v[196:199]\n\t"
      "global_load_dwordx4 v[144:147], v228, s[58:59] sc1\n\t"
      "s_waitcnt lgkmcnt(0)\n\t"
      "s_barrier\n\t"
      "s_setprio 2\n\t"
      "v_mfma_f32_16x16x32_bf16 v[184:187], v[148:151], v[80:83], v[184:187]\n\t"
      "s_waitcnt vmcnt(5)\n\t"
      "v_cmp_eq_u32_e32 vcc, -1, v152\n\t"
      "v_mfma_f32_16x16x32_bf16 v[188:191], v[148:151], v[84:87], v[188:191]\n\t"
      "v_mfma_f32_16x16x32_bf16 v[192:195], v[148:151], v[88:91], v[192:195]\n\t"
      "s_cbranch_vccnz FQ20R_%=\n\t"
      "FQ20B_%=:\n\t"
      "v_mfma_f32_16x16x32_bf16 v[196:199], v[148:151], v[92:95], v[196:199]\n\t"
      "global_load_dwordx4 v[148:151], v229, s[58:59] sc1\n\t"
      "v_mfma_f32_16x16x32_bf16 v[184:187], v[152:155], v[96:99], v[184:187]\n\t"
      "s_waitcnt vmcnt(5)\n\t"
      "v_cmp_eq_u32_e32 vcc, -1, v128\n\t"
      "v_mfma_f32_16x16x32_bf16 v[188:191], v[152:155], v[100:103], v[188:191]\n\t"
      "v_mfma_f32_16x16x32_bf16 v[192:195], v[152:155], v[104:107], v[192:195]\n\t"
      "s_cbranch_vccnz FQ21R_%=\n\t"
      "FQ21B_%=:\n\t"
      "v_mfma_f32_16x16x32_bf16 v[196:199], v[152:155], v[108:111], v[196:199]\n\t"
      "global_load_dwordx4 v[152:155], v230, s[58:59] sc1\n\t"
      "s_setprio 0\n\t"
      "v_cmp_u_f32_e32 vcc, v184, v185\n\t"
      "v_cmp_u_f32_e64 s[48:49], v186, v187\n\t"
      "s_nop 0\n\t"
      "s_or_b64 vcc, vcc, s[48:49]\n\t"
      "s_cbranch_scc1 NQ2R_%=\n\t"
      "NQ2B_%=:\n\t"
      "ds_write_b128 v243, v[184:187]\n\t"
      "ds_write_b128 v220, v[188:191]\n\t"
      "ds_write_b128 v221, v[192:195]\n\t"
      "ds_write_b128 v222, v[196:199]\n\t"
      "v_mfma_f32_16x16x32_bf16 v[200:203], v[128:131], v[0:3], 0\n\t"
      "s_waitcnt vmcnt(5)\n\t"
      "v_cmp_eq_u32_e32 vcc, -1, v132\n\t"
      "v_mfma_f32_16x16x32_bf16 v[204:207], v[128:131], v[4:7], 0\n\t"
      "v_mfma_f32_16x16x32_bf16 v[208:211], v[128:131], v[8:11], 0\n\t"
      "s_cbranch_vccnz FQ22R_%=\n\t"
      "FQ22B_%=:\n\t"
      "v_mfma_f32_16x16x32_bf16 v[212:215], v[128:131], v[12:15], 0\n\t"
      "global_load_dwordx4 v[128:131], v224, s[60:61] sc1\n\t"
      "v_mfma_f32_16x16x32_bf16 v[200:203], v[132:135], v[16:19], v[200:203]\n\t"
      "s_waitcnt vmcnt(5)\n\t"
      "v_cmp_eq_u32_e32 vcc, -1, v136\n\t"
      "v_mfma_f32_16x16x32_bf16 v[204:207], v[132:135], v[20:23], v[204:207]\n\t"
      "v_mfma_f32_16x16x32_bf16 v[208:211], v[132:135], v[24:27], v[208:211]\n\t"
      "s_cbranch_vccnz FQ23R_%=\n\t"
      "FQ23B_%=:\n\t"
      "v_mfma_f32_16x16x32_bf16 v[212:215], v[132:135], v[28:31], v[212:215]\n\t"
      "global_load_dwordx4 v[132:135], v225, s[60:61] sc1\n\t"
      "v_mfma_f32_16x16x32_bf16 v[200:203], v[136:139], v[32:35], v[200:203]\n\t"
      "s_waitcnt vmcnt(5)\n\t"
      "v_cmp_eq_u32_e32 vcc, -1, v140\n\t"
      "v_mfma_f32_16x16x32_bf16 v[204:207], v[136:139], v[36:39], v[204:207]\n\t"
      "v_mfma_f32_16x16x32_bf16 v[208:211], v[136:139], v[40:43], v[208:211]\n\t"
      "s_cbranch_vccnz FQ24R_%=\n\t"
      "FQ24B_%=:\n\t"
      "v_mfma_f32_16x16x32_bf16 v[212:215], v[136:139], v[44:47], v[212:215]\n\t"
      "global_load_dwordx4 v[136:139], v226, s[60:61] sc1\n\t"
      "v_mfma_f32_16x16x32_bf16 v[200:203], v[140:143], v[48:51], v[200:203]\n\t"
      "s_waitcnt vmcnt(5)\n\t"
      "v_cmp_eq_u32_e32 vcc, -1, v144\n\t"
      "v_mfma_f32_16x16x32_bf16 v[204:207], v[140:143], v[52:55], v[204:207]\n\t"
      "v_mfma_f32_16x16x32_bf16 v[208:211], v[140:143], v[56:59], v[208:211]\n\t"
      "s_cbranch_vccnz FQ25R_%=\n\t"
      "FQ25B_%=:\n\t"
      "v_mfma_f32_16x16x32_bf16 v[212:215], v[140:143], v[60:63], v[212:215]\n\t"
      "global_load_dwordx4 v[140:143], v227, s[60:61] sc1\n\t"
      "v_mfma_f32_16x16x32_bf16 v[200:203], v[144:147], v[64:67], v[200:203]\n\t"
      "s_waitcnt vmcnt(5)\n\t"
      "v_cmp_eq_u32_e32 vcc, -1, v148\n\t"
      "v_mfma_f32_16x16x32_bf16 v[204:207], v[144:147], v[68:71], v[204:207]\n\t"
      "v_mfma_f32_16x16x32_bf16 v[208:211], v[144:147], v[72:75], v[208:211]\n\t"
      "s_cbranch_vccnz FQ26R_%=\n\t"
      "FQ26B_%=:\n\t"
      "v_mfma_f32_16x16x32_bf16 v[212:215], v[144:147], v[76:79], v[212:215]\n\t"
      "global_load_dwordx4 v[144:147], v228, s[60:61] sc1\n\t"
      "s_waitcnt lgkmcnt(0)\n\t"
      "s_barrier\n\t"
      "s_setprio 2\n\t"
      "v_mfma_f32_16x16x32_bf16 v[200:203], v[148:151], v[80:83], v[200:203]\n\t"
      "s_waitcnt vmcnt(5)\n\t"
      "v_cmp_eq_u32_e32 vcc, -1, v152\n\t"
      "v_mfma_f32_16x16x32_bf16 v[204:207], v[148:151], v[84:87], v[204:207]\n\t"
      "v_mfma_f32_16x16x32_bf16 v[208:211], v[148:151], v[88:91], v[208:211]\n\t"
      "s_cbranch_vccnz FQ27R_%=\n\t"
      "FQ27B_%=:\n\t"
      "v_mfma_f32_16x16x32_bf16 v[212:215], v[148:151], v[92:95], v[212:215]\n\t"
      "global_load_dwordx4 v[148:151], v229, s[60:61] sc1\n\t"
      "v_mfma_f32_16x16x32_bf16 v[200:203], v[152:155], v[96:99], v[200:203]\n\t"
      "s_waitcnt vmcnt(5)\n\t"
      "v_cmp_eq_u32_e32 vcc, -1, v128\n\t"
      "v_mfma_f32_16x16x32_bf16 v[204:207], v[152:155], v[100:103], v[204:207]\n\t"
      "v_mfma_f32_16x16x32_bf16 v[208:211], v[152:155], v[104:107], v[208:211]\n\t"
      "s_cbranch_vccnz FQ28R_%=\n\t"
      "FQ28B_%=:\n\t"
      "v_mfma_f32_16x16x32_bf16 v[212:215], v[152:155], v[108:111], v[212:215]\n\t"
      "global_load_dwordx4 v[152:155], v230, s[60:61] sc1\n\t"
      "s_setprio 0\n\t"
      "v_cmp_u_f32_e32 vcc, v200, v201\n\t"
      "v_cmp_u_f32_e64 s[48:49], v202, v203\n\t"
      "s_nop 0\n\t"
      "s_or_b64 vcc, vcc, s[48:49]\n\t"
      "s_cbranch_scc1 NQ3R_%=\n\t"
      "NQ3B_%=:\n\t"
      "ds_write_b128 v243, v[200:203] offset:32768\n\t"
      "ds_write_b128 v220, v[204:207] offset:32768\n\t"
      "ds_write_b128 v221, v[208:211] offset:32768\n\t"
      "ds_write_b128 v222, v[212:215] offset:32768\n\t"
      "v_mfma_f32_16x16x32_bf16 v[184:187], v[128:131], v[0:3], 0\n\t"
      "s_waitcnt vmcnt(5)\n\t"
      "v_cmp_eq_u32_e32 vcc, -1, v132\n\t"
      "v_mfma_f32_16x16x32_bf16 v[188:191], v[128:131], v[4:7], 0\n\t"
      "v_mfma_f32_16x16x32_bf16 v[192:195], v[128:131], v[8:11], 0\n\t"
      "s_cbranch_vccnz FQ29R_%=\n\t"
      "FQ29B_%=:\n\t"
      "v_mfma_f32_16x16x32_bf16 v[196:199], v[128:131], v[12:15], 0\n\t"
      "global_load_dwordx4 v[128:131], v224, s[62:63] sc1\n\t"
      "v_mfma_f32_16x16x32_bf16 v[184:187], v[132:135], v[16:19], v[184:187]\n\t"
      "s_waitcnt vmcnt(5)\n\t"
      "v_cmp_eq_u32_e32 vcc, -1, v136\n\t"
      "v_mfma_f32_16x16x32_bf16 v[188:191], v[132:135], v[20:23], v[188:191]\n\t"
      "v_mfma_f32_16x16x32_bf16 v[192:195], v[132:135], v[24:27], v[192:195]\n\t"
      "s_cbranch_vccnz FQ30R_%=\n\t"
      "FQ30B_%=:\n\t"
      "v_mfma_f32_16x16x32_bf16 v[196:199], v[132:135], v[28:31], v[196:199]\n\t"
      "global_load_dwordx4 v[132:135], v225, s[62:63] sc1\n\t"
      "v_mfma_f32_16x16x32_bf16 v[184:187], v[136:139], v[32:35], v[184:187]\n\t"
      "s_waitcnt vmcnt(5)\n\t"
      "v_cmp_eq_u32_e32 vcc, -1, v140\n\t"
      "v_mfma_f32_16x16x32_bf16 v[188:191], v[136:139], v[36:39], v[188:191]\n\t"
      "v_mfma_f32_16x16x32_bf16 v[192:195], v[136:139], v[40:43], v[192:195]\n\t"
      "s_cbranch_vccnz FQ31R_%=\n\t"
      "FQ31B_%=:\n\t"
      "v_mfma_f32_16x16x32_bf16 v[196:199], v[136:139], v[44:47], v[196:199]\n\t"
      "global_load_dwordx4 v[136:139], v226, s[62:63] sc1\n\t"
      "v_mfma_f32_16x16x32_bf16 v[184:187], v[140:143], v[48:51], v[184:187]\n\t"
      "s_waitcnt vmcnt(5)\n\t"
      "v_cmp_eq_u32_e32 vcc, -1, v144\n\t"
      "v_mfma_f32_16x16x32_bf16 v[188:191], v[140:143], v[52:55], v[188:191]\n\t"
      "v_mfma_f32_16x16x32_bf16 v[192:195], v[140:143], v[56:59], v[192:195]\n\t"
      "s_cbranch_vccnz FQ32R_%=\n\t"
      "FQ32B_%=:\n\t"
      "v_mfma_f32_16x16x32_bf16 v[196:199], v[140:143], v[60:63], v[196:199]\n\t"
      "global_load_dwordx4 v[140:143], v227, s[62:63] sc1\n\t"
      "v_mfma_f32_16x16x32_bf16 v[184:187], v[144:147], v[64:67], v[184:187]\n\t"
      "s_waitcnt vmcnt(5)\n\t"
      "v_cmp_eq_u32_e32 vcc, -1, v148\n\t"
      "v_mfma_f32_16x16x32_bf16 v[188:191], v[144:147], v[68:71], v[188:191]\n\t"
      "v_mfma_f32_16x16x32_bf16 v[192:195], v[144:147], v[72:75], v[192:195]\n\t"
      "s_cbranch_vccnz FQ33R_%=\n\t"
      "FQ33B_%=:\n\t"
      "v_mfma_f32_16x16x32_bf16 v[196:199], v[144:147], v[76:79], v[196:199]\n\t"
      "global_load_dwordx4 v[144:147], v228, s[62:63] sc1\n\t"
      "s_waitcnt lgkmcnt(0)\n\t"
      "s_barrier\n\t"
      "s_setprio 2\n\t"
      "v_mfma_f32_16x16x32_bf16 v[184:187], v[148:151], v[80:83], v[184:187]\n\t"
      "s_waitcnt vmcnt(5)\n\t"
      "v_cmp_eq_u32_e32 vcc, -1, v152\n\t"
      "v_mfma_f32_16x16x32_bf16 v[188:191], v[148:151], v[84:87], v[188:191]\n\t"
      "v_mfma_f32_16x16x32_bf16 v[192:195], v[148:151], v[88:91], v[192:195]\n\t"
      "s_cbranch_vccnz FQ34R_%=\n\t"
      "FQ34B_%=:\n\t"
      "v_mfma_f32_16x16x32_bf16 v[196:199], v[148:151], v[92:95], v[196:199]\n\t"
      "global_load_dwordx4 v[148:151], v229, s[62:63] sc1\n\t"
      "v_mfma_f32_16x16x32_bf16 v[184:187], v[152:155], v[96:99], v[184:187]\n\t"
      "s_waitcnt vmcnt(5)\n\t"
      "v_cmp_eq_u32_e32 vcc, -1, v128\n\t"
      "v_mfma_f32_16x16x32_bf16 v[188:191], v[152:155], v[100:103], v[188:191]\n\t"
      "v_mfma_f32_16x16x32_bf16 v[192:195], v[152:155], v[104:107], v[192:195]\n\t"
      "s_cbranch_vccnz FQ35R_%=\n\t"
      "FQ35B_%=:\n\t"
      "v_mfma_f32_16x16x32_bf16 v[196:199], v[152:155], v[108:111], v[196:199]\n\t"
      "global_load_dwordx4 v[152:155], v230, s[62:63] sc1\n\t"
      "s_setprio 0\n\t"
      "v_cmp_u_f32_e32 vcc, v184, v185\n\t"
      "v_cmp_u_f32_e64 s[48:49], v186, v187\n\t"
      "s_nop 0\n\t"
      "s_or_b64 vcc, vcc, s[48:49]\n\t"
      "s_cbranch_scc1 NQ4R_%=\n\t"
      "NQ4B_%=:\n\t"
      "ds_write_b128 v243, v[184:187]\n\t"
      "ds_write_b128 v220, v[188:191]\n\t"
      "ds_write_b128 v221, v[192:195]\n\t"
      "ds_write_b128 v222, v[196:199]\n\t"
      "v_mfma_f32_16x16x32_bf16 v[200:203], v[128:131], v[0:3], 0\n\t"
      "s_waitcnt vmcnt(5)\n\t"
      "v_cmp_eq_u32_e32 vcc, -1, v132\n\t"
      "v_mfma_f32_16x16x32_bf16 v[204:207], v[128:131], v[4:7], 0\n\t"
      "v_mfma_f32_16x16x32_bf16 v[208:211], v[128:131], v[8:11], 0\n\t"
      "s_cbranch_vccnz FQ36R_%=\n\t"
      "FQ36B_%=:\n\t"
      "v_mfma_f32_16x16x32_bf16 v[212:215], v[128:131], v[12:15], 0\n\t"
      "global_load_dwordx4 v[128:131], v224, s[64:65] sc1\n\t"
      "v_mfma_f32_16x16x32_bf16 v[200:203], v[132:135], v[16:19], v[200:203]\n\t"
      "s_waitcnt vmcnt(5)\n\t"
      "v_cmp_eq_u32_e32 vcc, -1, v136\n\t"
      "v_mfma_f32_16x16x32_bf16 v[204:207], v[132:135], v[20:23], v[204:207]\n\t"
      "v_mfma_f32_16x16x32_bf16 v[208:211], v[132:135], v[24:27], v[208:211]\n\t"
      "s_cbranch_vccnz FQ37R_%=\n\t"
      "FQ37B_%=:\n\t"
      "v_mfma_f32_16x16x32_bf16 v[212:215], v[132:135], v[28:31], v[212:215]\n\t"
      "global_load_dwordx4 v[132:135], v225, s[64:65] sc1\n\t"
      "v_mfma_f32_16x16x32_bf16 v[200:203], v[136:139], v[32:35], v[200:203]\n\t"
      "s_waitcnt vmcnt(5)\n\t"
      "v_cmp_eq_u32_e32 vcc, -1, v140\n\t"
      "v_mfma_f32_16x16x32_bf16 v[204:207], v[136:139], v[36:39], v[204:207]\n\t"
      "v_mfma_f32_16x16x32_bf16 v[208:211], v[136:139], v[40:43], v[208:211]\n\t"
      "s_cbranch_vccnz FQ38R_%=\n\t"
      "FQ38B_%=:\n\t"
      "v_mfma_f32_16x16x32_bf16 v[212:215], v[136:139], v[44:47], v[212:215]\n\t"
      "global_load_dwordx4 v[136:139], v226, s[64:65] sc1\n\t"
      "v_mfma_f32_16x16x32_bf16 v[200:203], v[140:143], v[48:51], v[200:203]\n\t"
      "s_waitcnt vmcnt(5)\n\t"
      "v_cmp_eq_u32_e32 vcc, -1, v144\n\t"
      "v_mfma_f32_16x16x32_bf16 v[204:207], v[140:143], v[52:55], v[204:207]\n\t"
      "v_mfma_f32_16x16x32_bf16 v[208:211], v[140:143], v[56:59], v[208:211]\n\t"
      "s_cbranch_vccnz FQ39R_%=\n\t"
      "FQ39B_%=:\n\t"
      "v_mfma_f32_16x16x32_bf16 v[212:215], v[140:143], v[60:63], v[212:215]\n\t"
      "global_load_dwordx4 v[140:143], v227, s[64:65] sc1\n\t"
      "v_mfma_f32_16x16x32_bf16 v[200:203], v[144:147], v[64:67], v[200:203]\n\t"
      "s_waitcnt vmcnt(5)\n\t"
      "v_cmp_eq_u32_e32 vcc, -1, v148\n\t"
      "v_mfma_f32_16x16x32_bf16 v[204:207], v[144:147], v[68:71], v[204:207]\n\t"
      "v_mfma_f32_16x16x32_bf16 v[208:211], v[144:147], v[72:75], v[208:211]\n\t"
      "s_cbranch_vccnz FQ40R_%=\n\t"
      "FQ40B_%=:\n\t"
      "v_mfma_f32_16x16x32_bf16 v[212:215], v[144:147], v[76:79], v[212:215]\n\t"
      "global_load_dwordx4 v[144:147], v228, s[64:65] sc1\n\t"
      "s_waitcnt lgkmcnt(0)\n\t"
      "s_barrier\n\t"
      "s_setprio 2\n\t"
      "v_mfma_f32_16x16x32_bf16 v[200:203], v[148:151], v[80:83], v[200:203]\n\t"
      "s_waitcnt vmcnt(5)\n\t"
      "v_cmp_eq_u32_e32 vcc, -1, v152\n\t"
      "v_mfma_f32_16x16x32_bf16 v[204:207], v[148:151], v[84:87], v[204:207]\n\t"
      "v_mfma_f32_16x16x32_bf16 v[208:211], v[148:151], v[88:91], v[208:211]\n\t"
      "s_cbranch_vccnz FQ41R_%=\n\t"
      "FQ41B_%=:\n\t"
      "v_mfma_f32_16x16x32_bf16 v[212:215], v[148:151], v[92:95], v[212:215]\n\t"
      "global_load_dwordx4 v[148:151], v229, s[64:65] sc1\n\t"
      "v_mfma_f32_16x16x32_bf16 v[200:203], v[152:155], v[96:99], v[200:203]\n\t"
      "s_waitcnt vmcnt(5)\n\t"
      "v_cmp_eq_u32_e32 vcc, -1, v128\n\t"
      "v_mfma_f32_16x16x32_bf16 v[204:207], v[152:155], v[100:103], v[204:207]\n\t"
      "v_mfma_f32_16x16x32_bf16 v[208:211], v[152:155], v[104:107], v[208:211]\n\t"
      "s_cbranch_vccnz FQ42R_%=\n\t"
      "FQ42B_%=:\n\t"
      "v_mfma_f32_16x16x32_bf16 v[212:215], v[152:155], v[108:111], v[212:215]\n\t"
      "global_load_dwordx4 v[152:155], v230, s[64:65] sc1\n\t"
      "s_setprio 0\n\t"
      "v_cmp_u_f32_e32 vcc, v200, v201\n\t"
      "v_cmp_u_f32_e64 s[48:49], v202, v203\n\t"
      "s_nop 0\n\t"
      "s_or_b64 vcc, vcc, s[48:49]\n\t"
      "s_cbranch_scc1 NQ5R_%=\n\t"
      "NQ5B_%=:\n\t"
      "ds_write_b128 v243, v[200:203] offset:32768\n\t"
      "ds_write_b128 v220, v[204:207] offset:32768\n\t"
      "ds_write_b128 v221, v[208:211] offset:32768\n\t"
      "ds_write_b128 v222, v[212:215] offset:32768\n\t"
      "v_mfma_f32_16x16x32_bf16 v[184:187], v[128:131], v[0:3], 0\n\t"
      "s_waitcnt vmcnt(5)\n\t"
      "v_cmp_eq_u32_e32 vcc, -1, v132\n\t"
      "v_mfma_f32_16x16x32_bf16 v[188:191], v[128:131], v[4:7], 0\n\t"
      "v_mfma_f32_16x16x32_bf16 v[192:195], v[128:131], v[8:11], 0\n\t"
      "s_cbranch_vccnz FQ43R_%=\n\t"
      "FQ43B_%=:\n\t"
      "v_mfma_f32_16x16x32_bf16 v[196:199], v[128:131], v[12:15], 0\n\t"
      "v_mfma_f32_16x16x32_bf16 v[184:187], v[132:135], v[16:19], v[184:187]\n\t"
      "s_waitcnt vmcnt(4)\n\t"
      "v_cmp_eq_u32_e32 vcc, -1, v136\n\t"
      "v_mfma_f32_16x16x32_bf16 v[188:191], v[132:135], v[20:23], v[188:191]\n\t"
      "v_mfma_f32_16x16x32_bf16 v[192:195], v[132:135], v[24:27], v[192:195]\n\t"
      "s_cbranch_vccnz FQ44R_%=\n\t"
      "FQ44B_%=:\n\t"
      "v_mfma_f32_16x16x32_bf16 v[196:199], v[132:135], v[28:31], v[196:199]\n\t"
      "v_mfma_f32_16x16x32_bf16 v[184:187], v[136:139], v[32:35], v[184:187]\n\t"
      "s_waitcnt vmcnt(3)\n\t"
      "v_cmp_eq_u32_e32 vcc, -1, v140\n\t"
      "v_mfma_f32_16x16x32_bf16 v[188:191], v[136:139], v[36:39], v[188:191]\n\t"
      "v_mfma_f32_16x16x32_bf16 v[192:195], v[136:139], v[40:43], v[192:195]\n\t"
      "s_cbranch_vccnz FQ45R_%=\n\t"
      "FQ45B_%=:\n\t"
      "v_mfma_f32_16x16x32_bf16 v[196:199], v[136:139], v[44:47], v[196:199]\n\t"
      "v_mfma_f32_16x16x32_bf16 v[184:187], v[140:143], v[48:51], v[184:187]\n\t"
      "s_waitcnt vmcnt(2)\n\t"
      "v_cmp_eq_u32_e32 vcc, -1, v144\n\t"
      "v_mfma_f32_16x16x32_bf16 v[188:191], v[140:143], v[52:55], v[188:191]\n\t"
      "v_mfma_f32_16x16x32_bf16 v[192:195], v[140:143], v[56:59], v[192:195]\n\t"
      "s_cbranch_vccnz FQ46R_%=\n\t"
      "FQ46B_%=:\n\t"
      "v_mfma_f32_16x16x32_bf16 v[196:199], v[140:143], v[60:63], v[196:199]\n\t"
      "v_mfma_f32_16x16x32_bf16 v[184:187], v[144:147], v[64:67], v[184:187]\n\t"
      "s_waitcnt vmcnt(1)\n\t"
      "v_cmp_eq_u32_e32 vcc, -1, v148\n\t"
      "v_mfma_f32_16x16x32_bf16 v[188:191], v[144:147], v[68:71], v[188:191]\n\t"
      "v_mfma_f32_16x16x32_bf16 v[192:195], v[144:147], v[72:75], v[192:195]\n\t"
      "s_cbranch_vccnz FQ47R_%=\n\t"
      "FQ47B_%=:\n\t"
      "v_mfma_f32_16x16x32_bf16 v[196:199], v[144:147], v[76:79], v[196:199]\n\t"
      "s_waitcnt lgkmcnt(0)\n\t"
      "s_barrier\n\t"
      "s_setprio 2\n\t"
      "v_mfma_f32_16x16x32_bf16 v[184:187], v[148:151], v[80:83], v[184:187]\n\t"
      "s_waitcnt vmcnt(0)\n\t"
      "v_cmp_eq_u32_e32 vcc, -1, v152\n\t"
      "v_mfma_f32_16x16x32_bf16 v[188:191], v[148:151], v[84:87], v[188:191]\n\t"
      "v_mfma_f32_16x16x32_bf16 v[192:195], v[148:151], v[88:91], v[192:195]\n\t"
      "s_cbranch_vccnz FQ48R_%=\n\t"
      "FQ48B_%=:\n\t"
      "v_mfma_f32_16x16x32_bf16 v[196:199], v[148:151], v[92:95], v[196:199]\n\t"
      "v_mfma_f32_16x16x32_bf16 v[184:187], v[152:155], v[96:99], v[184:187]\n\t"
      "v_mfma_f32_16x16x32_bf16 v[188:191], v[152:155], v[100:103], v[188:191]\n\t"
      "v_mfma_f32_16x16x32_bf16 v[192:195], v[152:155], v[104:107], v[192:195]\n\t"
      "v_mfma_f32_16x16x32_bf16 v[196:199], v[152:155], v[108:111], v[196:199]\n\t"
      "s_nop 4\n\t"
      "v_cmp_u_f32_e32 vcc, v184, v185\n\t"
      "v_cmp_u_f32_e64 s[48:49], v186, v187\n\t"
      "s_nop 0\n\t"
      "s_or_b64 vcc, vcc, s[48:49]\n\t"
      "s_cbranch_scc1 NQ6R_%=\n\t"
      "NQ6B_%=:\n\t"
      "ds_write_b128 v243, v[184:187]\n\t"
      "ds_write_b128 v220, v[188:191]\n\t"
      "ds_write_b128 v221, v[192:195]\n\t"
      "ds_write_b128 v222, v[196:199]\n\t"
      "s_waitcnt lgkmcnt(0)\n\t"
      "s_barrier\n\t"
      "s_branch GX_%=\n\t"
      "HWR_%=:\n\t"
      "s_mov_b32 s51, 0\n\t"
      "HWL_%=:\n\t"
      "s_add_u32 s51, s51, 1\n\t"
      "s_cmp_lt_u32 s51, 262144\n\t"
      "s_cbranch_scc0 HWX_%=\n\t"
      "s_sleep 2\n\t"
      "global_load_dwordx4 v[112:115], v116, s[12:13] sc1\n\t"
      "s_waitcnt vmcnt(0)\n\t"
      "v_xor_b32_e32 v114, v114, v112\n\t"
      "v_xor_b32_e32 v115, v115, v113\n\t"
      "v_cmp_ne_u64_e32 vcc, s[14:15], v[114:115]\n\t"
      "s_cbranch_vccnz HWL_%=\n\t"
      "HWX_%=:\n\t"
      "s_branch HWB_%=\n\t"
      "HXR_%=:\n\t"
      "s_mov_b32 s51, 0\n\t"
      "HXL_%=:\n\t"
      "s_add_u32 s51, s51, 1\n\t"
      "s_cmp_lt_u32 s51, 262144\n\t"
      "s_cbranch_scc0 HXX_%=\n\t"
      "s_sleep 2\n\t"
      "s_mov_b32 exec_hi, s23\n\t"
      "s_not_b32 exec_lo, s23\n\t"
      "s_add_u32 s68, s20, 0\n\t"
      "s_addc_u32 s69, s21, 0\n\t"
      "global_load_dwordx4 v[184:187], v240, s[68:69] sc1\n\t"
      "s_add_u32 s68, s20, 131072\n\t"
      "s_addc_u32 s69, s21, 0\n\t"
      "global_load_dwordx4 v[188:191], v240, s[68:69] sc1\n\t"
      "s_add_u32 s68, s20, 262144\n\t"
      "s_addc_u32 s69, s21, 0\n\t"
      "global_load_dwordx4 v[192:195], v240, s[68:69] sc1\n\t"
      "s_add_u32 s68, s20, 393216\n\t"
      "s_addc_u32 s69, s21, 0\n\t"
      "global_load_dwordx4 v[196:199], v240, s[68:69] sc1\n\t"
      "s_add_u32 s68, s20, 524288\n\t"
      "s_addc_u32 s69, s21, 0\n\t"
      "global_load_dwordx4 v[200:203], v240, s[68:69] sc1\n\t"
      "s_add_u32 s68, s20, 655360\n\t"
      "s_addc_u32 s69, s21, 0\n\t"
      "global_load_dwordx4 v[204:207], v240, s[68:69] sc1\n\t"
      "s_add_u32 s68, s20, 786432\n\t"
      "s_addc_u32 s69, s21, 0\n\t"
      "global_load_dwordx4 v[208:211], v240, s[68:69] sc1\n\t"
      "s_mov_b64 exec, -1\n\t"
      "s_waitcnt vmcnt(0)\n\t"
      "v_max3_u32 v234, v184, v185, v186\n\t"
      "v_max3_u32 v234, v234, v187, v188\n\t"
      "v_max3_u32 v234, v234, v189, v190\n\t"
      "v_max3_u32 v234, v234, v191, v192\n\t"
      "v_max3_u32 v234, v234, v193, v194\n\t"
      "v_max3_u32 v234, v234, v195, v196\n\t"
      "v_max3_u32 v234, v234, v197, v198\n\t"
      "v_max3_u32 v234, v234, v199, v200\n\t"
      "v_max3_u32 v234, v234, v201, v202\n\t"
      "v_max3_u32 v234, v234, v203, v204\n\t"
      "v_max3_u32 v234, v234, v205, v206\n\t"
      "v_max3_u32 v234, v234, v207, v208\n\t"
      "v_max3_u32 v234, v234, v209, v210\n\t"
      "v_max_u32_e32 v234, v234, v211\n\t"
      "v_cmp_eq_u32_e32 vcc, -1, v234\n\t"
      "s_cbranch_vccnz HXL_%=\n\t"
      "HXX_%=:\n\t"
      "s_nop 1\n\t"
      "s_branch HXB_%=\n\t"
      "FQ0R_%=:\n\t"
      "s_mov_b32 s51, 0\n\t"
      "FQ0L_%=:\n\t"
      "s_add_u32 s51, s51, 1\n\t"
      "s_cmp_lt_u32 s51, 262144\n\t"
      "s_cbranch_scc0 FQ0X_%=\n\t"
      "s_waitcnt vmcnt(0)\n\t"
      "s_sleep 1\n\t"
      "global_load_dwordx4 v[128:131], v224, s[52:53] sc1\n\t"
      "global_load_dwordx4 v[132:135], v225, s[52:53] sc1\n\t"
      "global_load_dwordx4 v[136:139], v226, s[52:53] sc1\n\t"
      "global_load_dwordx4 v[140:143], v227, s[52:53] sc1\n\t"
      "global_load_dwordx4 v[144:147], v228, s[52:53] sc1\n\t"
      "global_load_dwordx4 v[148:151], v229, s[52:53] sc1\n\t"
      "global_load_dwordx4 v[152:155], v230, s[52:53] sc1\n\t"
      "s_waitcnt vmcnt(6)\n\t"
      "v_max3_u32 v234, v128, v129, v130\n\t"
      "v_max_u32_e32 v234, v234, v131\n\t"
      "v_cmp_eq_u32_e32 vcc, -1, v234\n\t"
      "s_cbranch_vccnz FQ0L_%=\n\t"
      "FQ0X_%=:\n\t"
      "s_branch FQ0B_%=\n\t"
      "FQ1R_%=:\n\t"
      "s_mov_b32 s51, 0\n\t"
      "FQ1L_%=:\n\t"
      "s_add_u32 s51, s51, 1\n\t"
      "s_cmp_lt_u32 s51, 262144\n\t"
      "s_cbranch_scc0 FQ1X_%=\n\t"
      "s_waitcnt vmcnt(0)\n\t"
      "s_sleep 1\n\t"
      "global_load_dwordx4 v[132:135], v225, s[52:53] sc1\n\t"
      "global_load_dwordx4 v[136:139], v226, s[52:53] sc1\n\t"
      "global_load_dwordx4 v[140:143], v227, s[52:53] sc1\n\t"
      "global_load_dwordx4 v[144:147], v228, s[52:53] sc1\n\t"
      "global_load_dwordx4 v[148:151], v229, s[52:53] sc1\n\t"
      "global_load_dwordx4 v[152:155], v230, s[52:53] sc1\n\t"
      "global_load_dwordx4 v[128:131], v224, s[54:55] sc1\n\t"
      "s_waitcnt vmcnt(6)\n\t"
      "v_max3_u32 v234, v132, v133, v134\n\t"
      "v_max_u32_e32 v234, v234, v135\n\t"
      "v_cmp_eq_u32_e32 vcc, -1, v234\n\t"
      "s_cbranch_vccnz FQ1L_%=\n\t"
      "FQ1X_%=:\n\t"
      "s_branch FQ1B_%=\n\t"
      "FQ2R_%=:\n\t"
      "s_mov_b32 s51, 0\n\t"
      "FQ2L_%=:\n\t"
      "s_add_u32 s51, s51, 1\n\t"
      "s_cmp_lt_u32 s51, 262144\n\t"
      "s_cbranch_scc0 FQ2X_%=\n\t"
      "s_waitcnt vmcnt(0)\n\t"
      "s_sleep 1\n\t"
      "global_load_dwordx4 v[136:139], v226, s[52:53] sc1\n\t"
      "global_load_dwordx4 v[140:143], v227, s[52:53] sc1\n\t"
      "global_load_dwordx4 v[144:147], v228, s[52:53] sc1\n\t"
      "global_load_dwordx4 v[148:151], v229, s[52:53] sc1\n\t"
      "global_load_dwordx4 v[152:155], v230, s[52:53] sc1\n\t"
      "global_load_dwordx4 v[128:131], v224, s[54:55] sc1\n\t"
      "global_load_dwordx4 v[132:135], v225, s[54:55] sc1\n\t"
      "s_waitcnt vmcnt(6)\n\t"
      "v_max3_u32 v234, v136, v137, v138\n\t"
      "v_max_u32_e32 v234, v234, v139\n\t"
      "v_cmp_eq_u32_e32 vcc, -1, v234\n\t"
      "s_cbranch_vccnz FQ2L_%=\n\t"
      "FQ2X_%=:\n\t"
      "s_branch FQ2B_%=\n\t"
      "FQ3R_%=:\n\t"
      "s_mov_b32 s51, 0\n\t"
      "FQ3L_%=:\n\t"
      "s_add_u32 s51, s51, 1\n\t"
      "s_cmp_lt_u32 s51, 262144\n\t"
      "s_cbranch_scc0 FQ3X_%=\n\t"
      "s_waitcnt vmcnt(0)\n\t"
      "s_sleep 1\n\t"
      "global_load_dwordx4 v[140:143], v227, s[52:53] sc1\n\t"
      "global_load_dwordx4 v[144:147], v228, s[52:53] sc1\n\t"
      "global_load_dwordx4 v[148:151], v229, s[52:53] sc1\n\t"
      "global_load_dwordx4 v[152:155], v230, s[52:53] sc1\n\t"
      "global_load_dwordx4 v[128:131], v224, s[54:55] sc1\n\t"
      "global_load_dwordx4 v[132:135], v225, s[54:55] sc1\n\t"
      "global_load_dwordx4 v[136:139], v226, s[54:55] sc1\n\t"
      "s_waitcnt vmcnt(6)\n\t"
      "v_max3_u32 v234, v140, v141, v142\n\t"
      "v_max_u32_e32 v234, v234, v143\n\t"
      "v_cmp_eq_u32_e32 vcc, -1, v234\n\t"
      "s_cbranch_vccnz FQ3L_%=\n\t"
      "FQ3X_%=:\n\t"
      "s_branch FQ3B_%=\n\t"
      "FQ4R_%=:\n\t"
      "s_mov_b32 s51, 0\n\t"
      "FQ4L_%=:\n\t"
      "s_add_u32 s51, s51, 1\n\t"
      "s_cmp_lt_u32 s51, 262144\n\t"
      "s_cbranch_scc0 FQ4X_%=\n\t"
      "s_waitcnt vmcnt(0)\n\t"
      "s_sleep 1\n\t"
      "global_load_dwordx4 v[144:147], v228, s[52:53] sc1\n\t"
      "global_load_dwordx4 v[148:151], v229, s[52:53] sc1\n\t"
      "global_load_dwordx4 v[152:155], v230, s[52:53] sc1\n\t"
      "global_load_dwordx4 v[128:131], v224, s[54:55] sc1\n\t"
      "global_load_dwordx4 v[132:135], v225, s[54:55] sc1\n\t"
      "global_load_dwordx4 v[136:139], v226, s[54:55] sc1\n\t"
      "global_load_dwordx4 v[140:143], v227, s[54:55] sc1\n\t"
      "s_waitcnt vmcnt(6)\n\t"
      "v_max3_u32 v234, v144, v145, v146\n\t"
      "v_max_u32_e32 v234, v234, v147\n\t"
      "v_cmp_eq_u32_e32 vcc, -1, v234\n\t"
      "s_cbranch_vccnz FQ4L_%=\n\t"
      "FQ4X_%=:\n\t"
      "s_branch FQ4B_%=\n\t"
      "FQ5R_%=:\n\t"
      "s_mov_b32 s51, 0\n\t"
      "FQ5L_%=:\n\t"
      "s_add_u32 s51, s51, 1\n\t"
      "s_cmp_lt_u32 s51, 262144\n\t"
      "s_cbranch_scc0 FQ5X_%=\n\t"
      "s_waitcnt vmcnt(0)\n\t"
      "s_sleep 1\n\t"
      "global_load_dwordx4 v[148:151], v229, s[52:53] sc1\n\t"
      "global_load_dwordx4 v[152:155], v230, s[52:53] sc1\n\t"
      "global_load_dwordx4 v[128:131], v224, s[54:55] sc1\n\t"
      "global_load_dwordx4 v[132:135], v225, s[54:55] sc1\n\t"
      "global_load_dwordx4 v[136:139], v226, s[54:55] sc1\n\t"
      "global_load_dwordx4 v[140:143], v227, s[54:55] sc1\n\t"
      "global_load_dwordx4 v[144:147], v228, s[54:55] sc1\n\t"
      "s_waitcnt vmcnt(6)\n\t"
      "v_max3_u32 v234, v148, v149, v150\n\t"
      "v_max_u32_e32 v234, v234, v151\n\t"
      "v_cmp_eq_u32_e32 vcc, -1, v234\n\t"
      "s_cbranch_vccnz FQ5L_%=\n\t"
      "FQ5X_%=:\n\t"
      "s_branch FQ5B_%=\n\t"
      "FQ6R_%=:\n\t"
      "s_mov_b32 s51, 0\n\t"
      "FQ6L_%=:\n\t"
      "s_add_u32 s51, s51, 1\n\t"
      "s_cmp_lt_u32 s51, 262144\n\t"
      "s_cbranch_scc0 FQ6X_%=\n\t"
      "s_waitcnt vmcnt(0)\n\t"
      "s_sleep 1\n\t"
      "global_load_dwordx4 v[152:155], v230, s[52:53] sc1\n\t"
      "global_load_dwordx4 v[128:131], v224, s[54:55] sc1\n\t"
      "global_load_dwordx4 v[132:135], v225, s[54:55] sc1\n\t"
      "global_load_dwordx4 v[136:139], v226, s[54:55] sc1\n\t"
      "global_load_dwordx4 v[140:143], v227, s[54:55] sc1\n\t"
      "global_load_dwordx4 v[144:147], v228, s[54:55] sc1\n\t"
      "global_load_dwordx4 v[148:151], v229, s[54:55] sc1\n\t"
      "s_waitcnt vmcnt(6)\n\t"
      "v_max3_u32 v234, v152, v153, v154\n\t"
      "v_max_u32_e32 v234, v234, v155\n\t"
      "v_cmp_eq_u32_e32 vcc, -1, v234\n\t"
      "s_cbranch_vccnz FQ6L_%=\n\t"
      "FQ6X_%=:\n\t"
      "s_branch FQ6B_%=\n\t"
      "FQ7R_%=:\n\t"
      "s_mov_b32 s51, 0\n\t"
      "FQ7L_%=:\n\t"
      "s_add_u32 s51, s51, 1\n\t"
      "s_cmp_lt_u32 s51, 262144\n\t"
      "s_cbranch_scc0 FQ7X_%=\n\t"
      "s_waitcnt vmcnt(0)\n\t"
      "s_sleep 4\n\t"
      "global_load_dwordx4 v[128:131], v224, s[54:55] sc1\n\t"
      "global_load_dwordx4 v[132:135], v225, s[54:55] sc1\n\t"
      "global_load_dwordx4 v[136:139], v226, s[54:55] sc1\n\t"
      "global_load_dwordx4 v[140:143], v227, s[54:55] sc1\n\t"
      "global_load_dwordx4 v[144:147], v228, s[54:55] sc1\n\t"
      "global_load_dwordx4 v[148:151], v229, s[54:55] sc1\n\t"
      "s_waitcnt vmcnt(5)\n\t"
      "v_max3_u32 v234, v128, v129, v130\n\t"
      "v_max_u32_e32 v234, v234, v131\n\t"
      "v_cmp_eq_u32_e32 vcc, -1, v234\n\t"
      "s_cbranch_vccnz FQ7L_%=\n\t"
      "FQ7X_%=:\n\t"
      "s_branch FQ7B_%=\n\t"
      "FQ8R_%=:\n\t"
      "s_mov_b32 s51, 0\n\t"
      "FQ8L_%=:\n\t"
      "s_add_u32 s51, s51, 1\n\t"
      "s_cmp_lt_u32 s51, 262144\n\t"
      "s_cbranch_scc0 FQ8X_%=\n\t"
      "s_waitcnt vmcnt(0)\n\t"
      "s_sleep 4\n\t"
      "global_load_dwordx4 v[132:135], v225, s[54:55] sc1\n\t"
      "global_load_dwordx4 v[136:139], v226, s[54:55] sc1\n\t"
      "global_load_dwordx4 v[140:143], v227, s[54:55] sc1\n\t"
      "global_load_dwordx4 v[144:147], v228, s[54:55] sc1\n\t"
      "global_load_dwordx4 v[148:151], v229, s[54:55] sc1\n\t"
      "global_load_dwordx4 v[152:155], v230, s[54:55] sc1\n\t"
      "s_waitcnt vmcnt(5)\n\t"
      "v_max3_u32 v234, v132, v133, v134\n\t"
      "v_max_u32_e32 v234, v234, v135\n\t"
      "v_cmp_eq_u32_e32 vcc, -1, v234\n\t"
      "s_cbranch_vccnz FQ8L_%=\n\t"
      "FQ8X_%=:\n\t"
      "s_branch FQ8B_%=\n\t"
      "FQ9R_%=:\n\t"
      "s_mov_b32 s51, 0\n\t"
      "FQ9L_%=:\n\t"
      "s_add_u32 s51, s51, 1\n\t"
      "s_cmp_lt_u32 s51, 262144\n\t"
      "s_cbranch_scc0 FQ9X_%=\n\t"
      "s_waitcnt vmcnt(0)\n\t"
      "s_sleep 4\n\t"
      "global_load_dwordx4 v[136:139], v226, s[54:55] sc1\n\t"
      "global_load_dwordx4 v[140:143], v227, s[54:55] sc1\n\t"
      "global_load_dwordx4 v[144:147], v228, s[54:55] sc1\n\t"
      "global_load_dwordx4 v[148:151], v229, s[54:55] sc1\n\t"
      "global_load_dwordx4 v[152:155], v230, s[54:55] sc1\n\t"
      "global_load_dwordx4 v[128:131], v224, s[56:57] sc1\n\t"
      "s_waitcnt vmcnt(5)\n\t"
      "v_max3_u32 v234, v136, v137, v138\n\t"
      "v_max_u32_e32 v234, v234, v139\n\t"
      "v_cmp_eq_u32_e32 vcc, -1, v234\n\t"
      "s_cbranch_vccnz FQ9L_%=\n\t"
      "FQ9X_%=:\n\t"
      "s_branch FQ9B_%=\n\t"
      "FQ10R_%=:\n\t"
      "s_mov_b32 s51, 0\n\t"
      "FQ10L_%=:\n\t"
      "s_add_u32 s51, s51, 1\n\t"
      "s_cmp_lt_u32 s51, 262144\n\t"
      "s_cbranch_scc0 FQ10X_%=\n\t"
      "s_waitcnt vmcnt(0)\n\t"
      "s_sleep 4\n\t"
      "global_load_dwordx4 v[140:143], v227, s[54:55] sc1\n\t"
      "global_load_dwordx4 v[144:147], v228, s[54:55] sc1\n\t"
      "global_load_dwordx4 v[148:151], v229, s[54:55] sc1\n\t"
      "global_load_dwordx4 v[152:155], v230, s[54:55] sc1\n\t"
      "global_load_dwordx4 v[128:131], v224, s[56:57] sc1\n\t"
      "global_load_dwordx4 v[132:135], v225, s[56:57] sc1\n\t"
      "s_waitcnt vmcnt(5)\n\t"
      "v_max3_u32 v234, v140, v141, v142\n\t"
      "v_max_u32_e32 v234, v234, v143\n\t"
      "v_cmp_eq_u32_e32 vcc, -1, v234\n\t"
      "s_cbranch_vccnz FQ10L_%=\n\t"
      "FQ10X_%=:\n\t"
      "s_branch FQ10B_%=\n\t"
      "FQ11R_%=:\n\t"
      "s_mov_b32 s51, 0\n\t"
      "FQ11L_%=:\n\t"
      "s_add_u32 s51, s51, 1\n\t"
      "s_cmp_lt_u32 s51, 262144\n\t"
      "s_cbranch_scc0 FQ11X_%=\n\t"
      "s_waitcnt vmcnt(0)\n\t"
      "s_sleep 4\n\t"
      "global_load_dwordx4 v[144:147], v228, s[54:55] sc1\n\t"
      "global_load_dwordx4 v[148:151], v229, s[54:55] sc1\n\t"
      "global_load_dwordx4 v[152:155], v230, s[54:55] sc1\n\t"
      "global_load_dwordx4 v[128:131], v224, s[56:57] sc1\n\t"
      "global_load_dwordx4 v[132:135], v225, s[56:57] sc1\n\t"
      "global_load_dwordx4 v[136:139], v226, s[56:57] sc1\n\t"
      "s_waitcnt vmcnt(5)\n\t"
      "v_max3_u32 v234, v144, v145, v146\n\t"
      "v_max_u32_e32 v234, v234, v147\n\t"
      "v_cmp_eq_u32_e32 vcc, -1, v234\n\t"
      "s_cbranch_vccnz FQ11L_%=\n\t"
      "FQ11X_%=:\n\t"
      "s_branch FQ11B_%=\n\t"
      "FQ12R_%=:\n\t"
      "s_mov_b32 s51, 0\n\t"
      "FQ12L_%=:\n\t"
      "s_add_u32 s51, s51, 1\n\t"
      "s_cmp_lt_u32 s51, 262144\n\t"
      "s_cbranch_scc0 FQ12X_%=\n\t"
      "s_waitcnt vmcnt(0)\n\t"
      "s_sleep 4\n\t"
      "global_load_dwordx4 v[148:151], v229, s[54:55] sc1\n\t"
      "global_load_dwordx4 v[152:155], v230, s[54:55] sc1\n\t"
      "global_load_dwordx4 v[128:131], v224, s[56:57] sc1\n\t"
      "global_load_dwordx4 v[132:135], v225, s[56:57] sc1\n\t"
      "global_load_dwordx4 v[136:139], v226, s[56:57] sc1\n\t"
      "global_load_dwordx4 v[140:143], v227, s[56:57] sc1\n\t"
      "s_waitcnt vmcnt(5)\n\t"
      "v_max3_u32 v234, v148, v149, v150\n\t"
      "v_max_u32_e32 v234, v234, v151\n\t"
      "v_cmp_eq_u32_e32 vcc, -1, v234\n\t"
      "s_cbranch_vccnz FQ12L_%=\n\t"
      "FQ12X_%=:\n\t"
      "s_branch FQ12B_%=\n\t"
      "FQ13R_%=:\n\t"
      "s_mov_b32 s51, 0\n\t"
      "FQ13L_%=:\n\t"
      "s_add_u32 s51, s51, 1\n\t"
      "s_cmp_lt_u32 s51, 262144\n\t"
      "s_cbranch_scc0 FQ13X_%=\n\t"
      "s_waitcnt vmcnt(0)\n\t"
      "s_sleep 4\n\t"
      "global_load_dwordx4 v[152:155], v230, s[54:55] sc1\n\t"
      "global_load_dwordx4 v[128:131], v224, s[56:57] sc1\n\t"
      "global_load_dwordx4 v[132:135], v225, s[56:57] sc1\n\t"
      "global_load_dwordx4 v[136:139], v226, s[56:57] sc1\n\t"
      "global_load_dwordx4 v[140:143], v227, s[56:57] sc1\n\t"
      "global_load_dwordx4 v[144:147], v228, s[56:57] sc1\n\t"
      "s_waitcnt vmcnt(5)\n\t"
      "v_max3_u32 v234, v152, v153, v154\n\t"
      "v_max_u32_e32 v234, v234, v155\n\t"
      "v_cmp_eq_u32_e32 vcc, -1, v234\n\t"
      "s_cbranch_vccnz FQ13L_%=\n\t"
      "FQ13X_%=:\n\t"
      "s_branch FQ13B_%=\n\t"
      "FQ14R_%=:\n\t"
      "s_mov_b32 s51, 0\n\t"
      "FQ14L_%=:\n\t"
      "s_add_u32 s51, s51, 1\n\t"
      "s_cmp_lt_u32 s51, 262144\n\t"
      "s_cbranch_scc0 FQ14X_%=\n\t"
      "s_waitcnt vmcnt(0)\n\t"
      "s_sleep 4\n\t"
      "global_load_dwordx4 v[128:131], v224, s[56:57] sc1\n\t"
      "global_load_dwordx4 v[132:135], v225, s[56:57] sc1\n\t"
      "global_load_dwordx4 v[136:139], v226, s[56:57] sc1\n\t"
      "global_load_dwordx4 v[140:143], v227, s[56:57] sc1\n\t"
      "global_load_dwordx4 v[144:147], v228, s[56:57] sc1\n\t"
      "global_load_dwordx4 v[148:151], v229, s[56:57] sc1\n\t"
      "s_waitcnt vmcnt(5)\n\t"
      "v_max3_u32 v234, v128, v129, v130\n\t"
      "v_max_u32_e32 v234, v234, v131\n\t"
      "v_cmp_eq_u32_e32 vcc, -1, v234\n\t"
      "s_cbranch_vccnz FQ14L_%=\n\t"
      "FQ14X_%=:\n\t"
      "s_branch FQ14B_%=\n\t"
      "NQ1R_%=:\n\t"
      "s_mov_b32 s51, 0\n\t"
      "NQ1L_%=:\n\t"
      "s_add_u32 s51, s51, 1\n\t"
      "s_cmp_lt_u32 s51, 262144\n\t"
      "s_cbranch_scc0 NQ1X_%=\n\t"
      "s_waitcnt vmcnt(0)\n\t"
      "s_sleep 1\n\t"
      "global_load_dwordx4 v[128:131], v224, s[54:55] sc1\n\t"
      "global_load_dwordx4 v[132:135], v225, s[54:55] sc1\n\t"
      "global_load_dwordx4 v[136:139], v226, s[54:55] sc1\n\t"
      "global_load_dwordx4 v[140:143], v227, s[54:55] sc1\n\t"
      "global_load_dwordx4 v[144:147], v228, s[54:55] sc1\n\t"
      "global_load_dwordx4 v[148:151], v229, s[54:55] sc1\n\t"
      "global_load_dwordx4 v[152:155], v230, s[54:55] sc1\n\t"
      "s_waitcnt vmcnt(0)\n\t"
      "v_max3_u32 v234, v128, v129, v130\n\t"
      "v_max3_u32 v234, v234, v131, v132\n\t"
      "v_max3_u32 v234, v234, v133, v134\n\t"
      "v_max3_u32 v234, v234, v135, v136\n\t"
      "v_max3_u32 v234, v234, v137, v138\n\t"
      "v_max3_u32 v234, v234, v139, v140\n\t"
      "v_max3_u32 v234, v234, v141, v142\n\t"
      "v_max3_u32 v234, v234, v143, v144\n\t"
      "v_max3_u32 v234, v234, v145, v146\n\t"
      "v_max3_u32 v234, v234, v147, v148\n\t"
      "v_max3_u32 v234, v234, v149, v150\n\t"
      "v_max3_u32 v234, v234, v151, v152\n\t"
      "v_max3_u32 v234, v234, v153, v154\n\t"
      "v_max_u32_e32 v234, v234, v155\n\t"
      "v_cmp_eq_u32_e32 vcc, -1, v234\n\t"
      "s_cbranch_vccnz NQ1L_%=\n\t"
      "NQ1X_%=:\n\t"
      "v_mfma_f32_16x16x32_bf16 v[200:203], v[128:131], v[0:3], 0\n\t"
      "v_mfma_f32_16x16x32_bf16 v[204:207], v[128:131], v[4:7], 0\n\t"
      "v_mfma_f32_16x16x32_bf16 v[208:211], v[128:131], v[8:11], 0\n\t"
      "v_mfma_f32_16x16x32_bf16 v[212:215], v[128:131], v[12:15], 0\n\t"
      "v_mfma_f32_16x16x32_bf16 v[200:203], v[132:135], v[16:19], v[200:203]\n\t"
      "v_mfma_f32_16x16x32_bf16 v[204:207], v[132:135], v[20:23], v[204:207]\n\t"
      "v_mfma_f32_16x16x32_bf16 v[208:211], v[132:135], v[24:27], v[208:211]\n\t"
      "v_mfma_f32_16x16x32_bf16 v[212:215], v[132:135], v[28:31], v[212:215]\n\t"
      "v_mfma_f32_16x16x32_bf16 v[200:203], v[136:139], v[32:35], v[200:203]\n\t"
      "v_mfma_f32_16x16x32_bf16 v[204:207], v[136:139], v[36:39], v[204:207]\n\t"
      "v_mfma_f32_16x16x32_bf16 v[208:211], v[136:139], v[40:43], v[208:211]\n\t"
      "v_mfma_f32_16x16x32_bf16 v[212:215], v[136:139], v[44:47], v[212:215]\n\t"
      "v_mfma_f32_16x16x32_bf16 v[200:203], v[140:143], v[48:51], v[200:203]\n\t"
      "v_mfma_f32_16x16x32_bf16 v[204:207], v[140:143], v[52:55], v[204:207]\n\t"
      "v_mfma_f32_16x16x32_bf16 v[208:211], v[140:143], v[56:59], v[208:211]\n\t"
      "v_mfma_f32_16x16x32_bf16 v[212:215], v[140:143], v[60:63], v[212:215]\n\t"
      "v_mfma_f32_16x16x32_bf16 v[200:203], v[144:147], v[64:67], v[200:203]\n\t"
      "v_mfma_f32_16x16x32_bf16 v[204:207], v[144:147], v[68:71], v[204:207]\n\t"
      "v_mfma_f32_16x16x32_bf16 v[208:211], v[144:147], v[72:75], v[208:211]\n\t"
      "v_mfma_f32_16x16x32_bf16 v[212:215], v[144:147], v[76:79], v[212:215]\n\t"
      "v_mfma_f32_16x16x32_bf16 v[200:203], v[148:151], v[80:83], v[200:203]\n\t"
      "v_mfma_f32_16x16x32_bf16 v[204:207], v[148:151], v[84:87], v[204:207]\n\t"
      "v_mfma_f32_16x16x32_bf16 v[208:211], v[148:151], v[88:91], v[208:211]\n\t"
      "v_mfma_f32_16x16x32_bf16 v[212:215], v[148:151], v[92:95], v[212:215]\n\t"
      "v_mfma_f32_16x16x32_bf16 v[200:203], v[152:155], v[96:99], v[200:203]\n\t"
      "v_mfma_f32_16x16x32_bf16 v[204:207], v[152:155], v[100:103], v[204:207]\n\t"
      "v_mfma_f32_16x16x32_bf16 v[208:211], v[152:155], v[104:107], v[208:211]\n\t"
      "v_mfma_f32_16x16x32_bf16 v[212:215], v[152:155], v[108:111], v[212:215]\n\t"
      "global_load_dwordx4 v[128:131], v224, s[56:57] sc1\n\t"
      "global_load_dwordx4 v[132:135], v225, s[56:57] sc1\n\t"
      "global_load_dwordx4 v[136:139], v226, s[56:57] sc1\n\t"
      "global_load_dwordx4 v[140:143], v227, s[56:57] sc1\n\t"
      "global_load_dwordx4 v[144:147], v228, s[56:57] sc1\n\t"
      "global_load_dwordx4 v[148:151], v229, s[56:57] sc1\n\t"
      "global_load_dwordx4 v[152:155], v230, s[56:57] sc1\n\t"
      "s_waitcnt vmcnt(0)\n\t"
      "s_nop 7\n\t"
      "s_branch NQ1B_%=\n\t"
      "FQ15R_%=:\n\t"
      "s_mov_b32 s51, 0\n\t"
      "FQ15L_%=:\n\t"
      "s_add_u32 s51, s51, 1\n\t"
      "s_cmp_lt_u32 s51, 262144\n\t"
      "s_cbranch_scc0 FQ15X_%=\n\t"
      "s_waitcnt vmcnt(0)\n\t"
      "s_sleep 4\n\t"
      "global_load_dwordx4 v[132:135], v225, s[56:57] sc1\n\t"
      "global_load_dwordx4 v[136:139], v226, s[56:57] sc1\n\t"
      "global_load_dwordx4 v[140:143], v227, s[56:57] sc1\n\t"
      "global_load_dwordx4 v[144:147], v228, s[56:57] sc1\n\t"
      "global_load_dwordx4 v[148:151], v229, s[56:57] sc1\n\t"
      "global_load_dwordx4 v[152:155], v230, s[56:57] sc1\n\t"
      "s_waitcnt vmcnt(5)\n\t"
      "v_max3_u32 v234, v132, v133, v134\n\t"
      "v_max_u32_e32 v234, v234, v135\n\t"
      "v_cmp_eq_u32_e32 vcc, -1, v234\n\t"
      "s_cbranch_vccnz FQ15L_%=\n\t"
      "FQ15X_%=:\n\t"
      "s_branch FQ15B_%=\n\t"
      "FQ16R_%=:\n\t"
      "s_mov_b32 s51, 0\n\t"
      "FQ16L_%=:\n\t"
      "s_add_u32 s51, s51, 1\n\t"
      "s_cmp_lt_u32 s51, 262144\n\t"
      "s_cbranch_scc0 FQ16X_%=\n\t"
      "s_waitcnt vmcnt(0)\n\t"
      "s_sleep 4\n\t"
      "global_load_dwordx4 v[136:139], v226, s[56:57] sc1\n\t"
      "global_load_dwordx4 v[140:143], v227, s[56:57] sc1\n\t"
      "global_load_dwordx4 v[144:147], v228, s[56:57] sc1\n\t"
      "global_load_dwordx4 v[148:151], v229, s[56:57] sc1\n\t"
      "global_load_dwordx4 v[152:155], v230, s[56:57] sc1\n\t"
      "global_load_dwordx4 v[128:131], v224, s[58:59] sc1\n\t"
      "s_waitcnt vmcnt(5)\n\t"
      "v_max3_u32 v234, v136, v137, v138\n\t"
      "v_max_u32_e32 v234, v234, v139\n\t"
      "v_cmp_eq_u32_e32 vcc, -1, v234\n\t"
      "s_cbranch_vccnz FQ16L_%=\n\t"
      "FQ16X_%=:\n\t"
      "s_branch FQ16B_%=\n\t"
      "FQ17R_%=:\n\t"
      "s_mov_b32 s51, 0\n\t"
      "FQ17L_%=:\n\t"
      "s_add_u32 s51, s51, 1\n\t"
      "s_cmp_lt_u32 s51, 262144\n\t"
      "s_cbranch_scc0 FQ17X_%=\n\t"
      "s_waitcnt vmcnt(0)\n\t"
      "s_sleep 4\n\t"
      "global_load_dwordx4 v[140:143], v227, s[56:57] sc1\n\t"
      "global_load_dwordx4 v[144:147], v228, s[56:57] sc1\n\t"
      "global_load_dwordx4 v[148:151], v229, s[56:57] sc1\n\t"
      "global_load_dwordx4 v[152:155], v230, s[56:57] sc1\n\t"
      "global_load_dwordx4 v[128:131], v224, s[58:59] sc1\n\t"
      "global_load_dwordx4 v[132:135], v225, s[58:59] sc1\n\t"
      "s_waitcnt vmcnt(5)\n\t"
      "v_max3_u32 v234, v140, v141, v142\n\t"
      "v_max_u32_e32 v234, v234, v143\n\t"
      "v_cmp_eq_u32_e32 vcc, -1, v234\n\t"
      "s_cbranch_vccnz FQ17L_%=\n\t"
      "FQ17X_%=:\n\t"
      "s_branch FQ17B_%=\n\t"
      "FQ18R_%=:\n\t"
      "s_mov_b32 s51, 0\n\t"
      "FQ18L_%=:\n\t"
      "s_add_u32 s51, s51, 1\n\t"
      "s_cmp_lt_u32 s51, 262144\n\t"
      "s_cbranch_scc0 FQ18X_%=\n\t"
      "s_waitcnt vmcnt(0)\n\t"
      "s_sleep 4\n\t"
      "global_load_dwordx4 v[144:147], v228, s[56:57] sc1\n\t"
      "global_load_dwordx4 v[148:151], v229, s[56:57] sc1\n\t"
      "global_load_dwordx4 v[152:155], v230, s[56:57] sc1\n\t"
      "global_load_dwordx4 v[128:131], v224, s[58:59] sc1\n\t"
      "global_load_dwordx4 v[132:135], v225, s[58:59] sc1\n\t"
      "global_load_dwordx4 v[136:139], v226, s[58:59] sc1\n\t"
      "s_waitcnt vmcnt(5)\n\t"
      "v_max3_u32 v234, v144, v145, v146\n\t"
      "v_max_u32_e32 v234, v234, v147\n\t"
      "v_cmp_eq_u32_e32 vcc, -1, v234\n\t"
      "s_cbranch_vccnz FQ18L_%=\n\t"
      "FQ18X_%=:\n\t"
      "s_branch FQ18B_%=\n\t"
      "FQ19R_%=:\n\t"
      "s_mov_b32 s51, 0\n\t"
      "FQ19L_%=:\n\t"
      "s_add_u32 s51, s51, 1\n\t"
      "s_cmp_lt_u32 s51, 262144\n\t"
      "s_cbranch_scc0 FQ19X_%=\n\t"
      "s_waitcnt vmcnt(0)\n\t"
      "s_sleep 4\n\t"
      "global_load_dwordx4 v[148:151], v229, s[56:57] sc1\n\t"
      "global_load_dwordx4 v[152:155], v230, s[56:57] sc1\n\t"
      "global_load_dwordx4 v[128:131], v224, s[58:59] sc1\n\t"
      "global_load_dwordx4 v[132:135], v225, s[58:59] sc1\n\t"
      "global_load_dwordx4 v[136:139], v226, s[58:59] sc1\n\t"
      "global_load_dwordx4 v[140:143], v227, s[58:59] sc1\n\t"
      "s_waitcnt vmcnt(5)\n\t"
      "v_max3_u32 v234, v148, v149, v150\n\t"
      "v_max_u32_e32 v234, v234, v151\n\t"
      "v_cmp_eq_u32_e32 vcc, -1, v234\n\t"
      "s_cbranch_vccnz FQ19L_%=\n\t"
      "FQ19X_%=:\n\t"
      "s_branch FQ19B_%=\n\t"
      "FQ20R_%=:\n\t"
      "s_mov_b32 s51, 0\n\t"
      "FQ20L_%=:\n\t"
      "s_add_u32 s51, s51, 1\n\t"
      "s_cmp_lt_u32 s51, 262144\n\t"
      "s_cbranch_scc0 FQ20X_%=\n\t"
      "s_waitcnt vmcnt(0)\n\t"
      "s_sleep 4\n\t"
      "global_load_dwordx4 v[152:155], v230, s[56:57] sc1\n\t"
      "global_load_dwordx4 v[128:131], v224, s[58:59] sc1\n\t"
      "global_load_dwordx4 v[132:135], v225, s[58:59] sc1\n\t"
      "global_load_dwordx4 v[136:139], v226, s[58:59] sc1\n\t"
      "global_load_dwordx4 v[140:143], v227, s[58:59] sc1\n\t"
      "global_load_dwordx4 v[144:147], v228, s[58:59] sc1\n\t"
      "s_waitcnt vmcnt(5)\n\t"
      "v_max3_u32 v234, v152, v153, v154\n\t"
      "v_max_u32_e32 v234, v234, v155\n\t"
      "v_cmp_eq_u32_e32 vcc, -1, v234\n\t"
      "s_cbranch_vccnz FQ20L_%=\n\t"
      "FQ20X_%=:\n\t"
      "s_branch FQ20B_%=\n\t"
      "FQ21R_%=:\n\t"
      "s_mov_b32 s51, 0\n\t"
      "FQ21L_%=:\n\t"
      "s_add_u32 s51, s51, 1\n\t"
      "s_cmp_lt_u32 s51, 262144\n\t"
      "s_cbranch_scc0 FQ21X_%=\n\t"
      "s_waitcnt vmcnt(0)\n\t"
      "s_sleep 4\n\t"
      "global_load_dwordx4 v[128:131], v224, s[58:59] sc1\n\t"
      "global_load_dwordx4 v[132:135], v225, s[58:59] sc1\n\t"
      "global_load_dwordx4 v[136:139], v226, s[58:59] sc1\n\t"
      "global_load_dwordx4 v[140:143], v227, s[58:59] sc1\n\t"
      "global_load_dwordx4 v[144:147], v228, s[58:59] sc1\n\t"
      "global_load_dwordx4 v[148:151], v229, s[58:59] sc1\n\t"
      "s_waitcnt vmcnt(5)\n\t"
      "v_max3_u32 v234, v128, v129, v130\n\t"
      "v_max_u32_e32 v234, v234, v131\n\t"
      "v_cmp_eq_u32_e32 vcc, -1, v234\n\t"
      "s_cbranch_vccnz FQ21L_%=\n\t"
      "FQ21X_%=:\n\t"
      "s_branch FQ21B_%=\n\t"
      "NQ2R_%=:\n\t"
      "s_mov_b32 s51, 0\n\t"
      "NQ2L_%=:\n\t"
      "s_add_u32 s51, s51, 1\n\t"
      "s_cmp_lt_u32 s51, 262144\n\t"
      "s_cbranch_scc0 NQ2X_%=\n\t"
      "s_waitcnt vmcnt(0)\n\t"
      "s_sleep 1\n\t"
      "global_load_dwordx4 v[128:131], v224, s[56:57] sc1\n\t"
      "global_load_dwordx4 v[132:135], v225, s[56:57] sc1\n\t"
      "global_load_dwordx4 v[136:139], v226, s[56:57] sc1\n\t"
      "global_load_dwordx4 v[140:143], v227, s[56:57] sc1\n\t"
      "global_load_dwordx4 v[144:147], v228, s[56:57] sc1\n\t"
      "global_load_dwordx4 v[148:151], v229, s[56:57] sc1\n\t"
      "global_load_dwordx4 v[152:155], v230, s[56:57] sc1\n\t"
      "s_waitcnt vmcnt(0)\n\t"
      "v_max3_u32 v234, v128, v129, v130\n\t"
      "v_max3_u32 v234, v234, v131, v132\n\t"
      "v_max3_u32 v234, v234, v133, v134\n\t"
      "v_max3_u32 v234, v234, v135, v136\n\t"
      "v_max3_u32 v234, v234, v137, v138\n\t"
      "v_max3_u32 v234, v234, v139, v140\n\t"
      "v_max3_u32 v234, v234, v141, v142\n\t"
      "v_max3_u32 v234, v234, v143, v144\n\t"
      "v_max3_u32 v234, v234, v145, v146\n\t"
      "v_max3_u32 v234, v234, v147, v148\n\t"
      "v_max3_u32 v234, v234, v149, v150\n\t"
      "v_max3_u32 v234, v234, v151, v152\n\t"
      "v_max3_u32 v234, v234, v153, v154\n\t"
      "v_max_u32_e32 v234, v234, v155\n\t"
      "v_cmp_eq_u32_e32 vcc, -1, v234\n\t"
      "s_cbranch_vccnz NQ2L_%=\n\t"
      "NQ2X_%=:\n\t"
      "v_mfma_f32_16x16x32_bf16 v[184:187], v[128:131], v[0:3], 0\n\t"
      "v_mfma_f32_16x16x32_bf16 v[188:191], v[128:131], v[4:7], 0\n\t"
      "v_mfma_f32_16x16x32_bf16 v[192:195], v[128:131], v[8:11], 0\n\t"
      "v_mfma_f32_16x16x32_bf16 v[196:199], v[128:131], v[12:15], 0\n\t"
      "v_mfma_f32_16x16x32_bf16 v[184:187], v[132:135], v[16:19], v[184:187]\n\t"
      "v_mfma_f32_16x16x32_bf16 v[188:191], v[132:135], v[20:23], v[188:191]\n\t"
      "v_mfma_f32_16x16x32_bf16 v[192:195], v[132:135], v[24:27], v[192:195]\n\t"
      "v_mfma_f32_16x16x32_bf16 v[196:199], v[132:135], v[28:31], v[196:199]\n\t"
      "v_mfma_f32_16x16x32_bf16 v[184:187], v[136:139], v[32:35], v[184:187]\n\t"
      "v_mfma_f32_16x16x32_bf16 v[188:191], v[136:139], v[36:39], v[188:191]\n\t"
      "v_mfma_f32_16x16x32_bf16 v[192:195], v[136:139], v[40:43], v[192:195]\n\t"
      "v_mfma_f32_16x16x32_bf16 v[196:199], v[136:139], v[44:47], v[196:199]\n\t"
      "v_mfma_f32_16x16x32_bf16 v[184:187], v[140:143], v[48:51], v[184:187]\n\t"
      "v_mfma_f32_16x16x32_bf16 v[188:191], v[140:143], v[52:55], v[188:191]\n\t"
      "v_mfma_f32_16x16x32_bf16 v[192:195], v[140:143], v[56:59], v[192:195]\n\t"
      "v_mfma_f32_16x16x32_bf16 v[196:199], v[140:143], v[60:63], v[196:199]\n\t"
      "v_mfma_f32_16x16x32_bf16 v[184:187], v[144:147], v[64:67], v[184:187]\n\t"
      "v_mfma_f32_16x16x32_bf16 v[188:191], v[144:147], v[68:71], v[188:191]\n\t"
      "v_mfma_f32_16x16x32_bf16 v[192:195], v[144:147], v[72:75], v[192:195]\n\t"
      "v_mfma_f32_16x16x32_bf16 v[196:199], v[144:147], v[76:79], v[196:199]\n\t"
      "v_mfma_f32_16x16x32_bf16 v[184:187], v[148:151], v[80:83], v[184:187]\n\t"
      "v_mfma_f32_16x16x32_bf16 v[188:191], v[148:151], v[84:87], v[188:191]\n\t"
      "v_mfma_f32_16x16x32_bf16 v[192:195], v[148:151], v[88:91], v[192:195]\n\t"
      "v_mfma_f32_16x16x32_bf16 v[196:199], v[148:151], v[92:95], v[196:199]\n\t"
      "v_mfma_f32_16x16x32_bf16 v[184:187], v[152:155], v[96:99], v[184:187]\n\t"
      "v_mfma_f32_16x16x32_bf16 v[188:191], v[152:155], v[100:103], v[188:191]\n\t"
      "v_mfma_f32_16x16x32_bf16 v[192:195], v[152:155], v[104:107], v[192:195]\n\t"
      "v_mfma_f32_16x16x32_bf16 v[196:199], v[152:155], v[108:111], v[196:199]\n\t"
      "global_load_dwordx4 v[128:131], v224, s[58:59] sc1\n\t"
      "global_load_dwordx4 v[132:135], v225, s[58:59] sc1\n\t"
      "global_load_dwordx4 v[136:139], v226, s[58:59] sc1\n\t"
      "global_load_dwordx4 v[140:143], v227, s[58:59] sc1\n\t"
      "global_load_dwordx4 v[144:147], v228, s[58:59] sc1\n\t"
      "global_load_dwordx4 v[148:151], v229, s[58:59] sc1\n\t"
      "global_load_dwordx4 v[152:155], v230, s[58:59] sc1\n\t"
      "s_waitcnt vmcnt(0)\n\t"
      "s_nop 7\n\t"
      "s_branch NQ2B_%=\n\t"
      "FQ22R_%=:\n\t"
      "s_mov_b32 s51, 0\n\t"
      "FQ22L_%=:\n\t"
      "s_add_u32 s51, s51, 1\n\t"
      "s_cmp_lt_u32 s51, 262144\n\t"
      "s_cbranch_scc0 FQ22X_%=\n\t"
      "s_waitcnt vmcnt(0)\n\t"
      "s_sleep 4\n\t"
      "global_load_dwordx4 v[132:135], v225, s[58:59] sc1\n\t"
      "global_load_dwordx4 v[136:139], v226, s[58:59] sc1\n\t"
      "global_load_dwordx4 v[140:143], v227, s[58:59] sc1\n\t"
      "global_load_dwordx4 v[144:147], v228, s[58:59] sc1\n\t"
      "global_load_dwordx4 v[148:151], v229, s[58:59] sc1\n\t"
      "global_load_dwordx4 v[152:155], v230, s[58:59] sc1\n\t"
      "s_waitcnt vmcnt(5)\n\t"
      "v_max3_u32 v234, v132, v133, v134\n\t"
      "v_max_u32_e32 v234, v234, v135\n\t"
      "v_cmp_eq_u32_e32 vcc, -1, v234\n\t"
      "s_cbranch_vccnz FQ22L_%=\n\t"
      "FQ22X_%=:\n\t"
      "s_branch FQ22B_%=\n\t"
      "FQ23R_%=:\n\t"
      "s_mov_b32 s51, 0\n\t"
      "FQ23L_%=:\n\t"
      "s_add_u32 s51, s51, 1\n\t"
      "s_cmp_lt_u32 s51, 262144\n\t"
      "s_cbranch_scc0 FQ23X_%=\n\t"
      "s_waitcnt vmcnt(0)\n\t"
      "s_sleep 4\n\t"
      "global_load_dwordx4 v[136:139], v226, s[58:59] sc1\n\t"
      "global_load_dwordx4 v[140:143], v227, s[58:59] sc1\n\t"
      "global_load_dwordx4 v[144:147], v228, s[58:59] sc1\n\t"
      "global_load_dwordx4 v[148:151], v229, s[58:59] sc1\n\t"
      "global_load_dwordx4 v[152:155], v230, s[58:59] sc1\n\t"
      "global_load_dwordx4 v[128:131], v224, s[60:61] sc1\n\t"
      "s_waitcnt vmcnt(5)\n\t"
      "v_max3_u32 v234, v136, v137, v138\n\t"
      "v_max_u32_e32 v234, v234, v139\n\t"
      "v_cmp_eq_u32_e32 vcc, -1, v234\n\t"
      "s_cbranch_vccnz FQ23L_%=\n\t"
      "FQ23X_%=:\n\t"
      "s_branch FQ23B_%=\n\t"
      "FQ24R_%=:\n\t"
      "s_mov_b32 s51, 0\n\t"
      "FQ24L_%=:\n\t"
      "s_add_u32 s51, s51, 1\n\t"
      "s_cmp_lt_u32 s51, 262144\n\t"
      "s_cbranch_scc0 FQ24X_%=\n\t"
      "s_waitcnt vmcnt(0)\n\t"
      "s_sleep 4\n\t"
      "global_load_dwordx4 v[140:143], v227, s[58:59] sc1\n\t"
      "global_load_dwordx4 v[144:147], v228, s[58:59] sc1\n\t"
      "global_load_dwordx4 v[148:151], v229, s[58:59] sc1\n\t"
      "global_load_dwordx4 v[152:155], v230, s[58:59] sc1\n\t"
      "global_load_dwordx4 v[128:131], v224, s[60:61] sc1\n\t"
      "global_load_dwordx4 v[132:135], v225, s[60:61] sc1\n\t"
      "s_waitcnt vmcnt(5)\n\t"
      "v_max3_u32 v234, v140, v141, v142\n\t"
      "v_max_u32_e32 v234, v234, v143\n\t"
      "v_cmp_eq_u32_e32 vcc, -1, v234\n\t"
      "s_cbranch_vccnz FQ24L_%=\n\t"
      "FQ24X_%=:\n\t"
      "s_branch FQ24B_%=\n\t"
      "FQ25R_%=:\n\t"
      "s_mov_b32 s51, 0\n\t"
      "FQ25L_%=:\n\t"
      "s_add_u32 s51, s51, 1\n\t"
      "s_cmp_lt_u32 s51, 262144\n\t"
      "s_cbranch_scc0 FQ25X_%=\n\t"
      "s_waitcnt vmcnt(0)\n\t"
      "s_sleep 4\n\t"
      "global_load_dwordx4 v[144:147], v228, s[58:59] sc1\n\t"
      "global_load_dwordx4 v[148:151], v229, s[58:59] sc1\n\t"
      "global_load_dwordx4 v[152:155], v230, s[58:59] sc1\n\t"
      "global_load_dwordx4 v[128:131], v224, s[60:61] sc1\n\t"
      "global_load_dwordx4 v[132:135], v225, s[60:61] sc1\n\t"
      "global_load_dwordx4 v[136:139], v226, s[60:61] sc1\n\t"
      "s_waitcnt vmcnt(5)\n\t"
      "v_max3_u32 v234, v144, v145, v146\n\t"
      "v_max_u32_e32 v234, v234, v147\n\t"
      "v_cmp_eq_u32_e32 vcc, -1, v234\n\t"
      "s_cbranch_vccnz FQ25L_%=\n\t"
      "FQ25X_%=:\n\t"
      "s_branch FQ25B_%=\n\t"
      "FQ26R_%=:\n\t"
      "s_mov_b32 s51, 0\n\t"
      "FQ26L_%=:\n\t"
      "s_add_u32 s51, s51, 1\n\t"
      "s_cmp_lt_u32 s51, 262144\n\t"
      "s_cbranch_scc0 FQ26X_%=\n\t"
      "s_waitcnt vmcnt(0)\n\t"
      "s_sleep 4\n\t"
      "global_load_dwordx4 v[148:151], v229, s[58:59] sc1\n\t"
      "global_load_dwordx4 v[152:155], v230, s[58:59] sc1\n\t"
      "global_load_dwordx4 v[128:131], v224, s[60:61] sc1\n\t"
      "global_load_dwordx4 v[132:135], v225, s[60:61] sc1\n\t"
      "global_load_dwordx4 v[136:139], v226, s[60:61] sc1\n\t"
      "global_load_dwordx4 v[140:143], v227, s[60:61] sc1\n\t"
      "s_waitcnt vmcnt(5)\n\t"
      "v_max3_u32 v234, v148, v149, v150\n\t"
      "v_max_u32_e32 v234, v234, v151\n\t"
      "v_cmp_eq_u32_e32 vcc, -1, v234\n\t"
      "s_cbranch_vccnz FQ26L_%=\n\t"
      "FQ26X_%=:\n\t"
      "s_branch FQ26B_%=\n\t"
      "FQ27R_%=:\n\t"
      "s_mov_b32 s51, 0\n\t"
      "FQ27L_%=:\n\t"
      "s_add_u32 s51, s51, 1\n\t"
      "s_cmp_lt_u32 s51, 262144\n\t"
      "s_cbranch_scc0 FQ27X_%=\n\t"
      "s_waitcnt vmcnt(0)\n\t"
      "s_sleep 4\n\t"
      "global_load_dwordx4 v[152:155], v230, s[58:59] sc1\n\t"
      "global_load_dwordx4 v[128:131], v224, s[60:61] sc1\n\t"
      "global_load_dwordx4 v[132:135], v225, s[60:61] sc1\n\t"
      "global_load_dwordx4 v[136:139], v226, s[60:61] sc1\n\t"
      "global_load_dwordx4 v[140:143], v227, s[60:61] sc1\n\t"
      "global_load_dwordx4 v[144:147], v228, s[60:61] sc1\n\t"
      "s_waitcnt vmcnt(5)\n\t"
      "v_max3_u32 v234, v152, v153, v154\n\t"
      "v_max_u32_e32 v234, v234, v155\n\t"
      "v_cmp_eq_u32_e32 vcc, -1, v234\n\t"
      "s_cbranch_vccnz FQ27L_%=\n\t"
      "FQ27X_%=:\n\t"
      "s_branch FQ27B_%=\n\t"
      "FQ28R_%=:\n\t"
      "s_mov_b32 s51, 0\n\t"
      "FQ28L_%=:\n\t"
      "s_add_u32 s51, s51, 1\n\t"
      "s_cmp_lt_u32 s51, 262144\n\t"
      "s_cbranch_scc0 FQ28X_%=\n\t"
      "s_waitcnt vmcnt(0)\n\t"
      "s_sleep 4\n\t"
      "global_load_dwordx4 v[128:131], v224, s[60:61] sc1\n\t"
      "global_load_dwordx4 v[132:135], v225, s[60:61] sc1\n\t"
      "global_load_dwordx4 v[136:139], v226, s[60:61] sc1\n\t"
      "global_load_dwordx4 v[140:143], v227, s[60:61] sc1\n\t"
      "global_load_dwordx4 v[144:147], v228, s[60:61] sc1\n\t"
      "global_load_dwordx4 v[148:151], v229, s[60:61] sc1\n\t"
      "s_waitcnt vmcnt(5)\n\t"
      "v_max3_u32 v234, v128, v129, v130\n\t"
      "v_max_u32_e32 v234, v234, v131\n\t"
      "v_cmp_eq_u32_e32 vcc, -1, v234\n\t"
      "s_cbranch_vccnz FQ28L_%=\n\t"
      "FQ28X_%=:\n\t"
      "s_branch FQ28B_%=\n\t"
      "NQ3R_%=:\n\t"
      "s_mov_b32 s51, 0\n\t"
      "NQ3L_%=:\n\t"
      "s_add_u32 s51, s51, 1\n\t"
      "s_cmp_lt_u32 s51, 262144\n\t"
      "s_cbranch_scc0 NQ3X_%=\n\t"
      "s_waitcnt vmcnt(0)\n\t"
      "s_sleep 1\n\t"
      "global_load_dwordx4 v[128:131], v224, s[58:59] sc1\n\t"
      "global_load_dwordx4 v[132:135], v225, s[58:59] sc1\n\t"
      "global_load_dwordx4 v[136:139], v226, s[58:59] sc1\n\t"
      "global_load_dwordx4 v[140:143], v227, s[58:59] sc1\n\t"
      "global_load_dwordx4 v[144:147], v228, s[58:59] sc1\n\t"
      "global_load_dwordx4 v[148:151], v229, s[58:59] sc1\n\t"
      "global_load_dwordx4 v[152:155], v230, s[58:59] sc1\n\t"
      "s_waitcnt vmcnt(0)\n\t"
      "v_max3_u32 v234, v128, v129, v130\n\t"
      "v_max3_u32 v234, v234, v131, v132\n\t"
      "v_max3_u32 v234, v234, v133, v134\n\t"
      "v_max3_u32 v234, v234, v135, v136\n\t"
      "v_max3_u32 v234, v234, v137, v138\n\t"
      "v_max3_u32 v234, v234, v139, v140\n\t"
      "v_max3_u32 v234, v234, v141, v142\n\t"
      "v_max3_u32 v234, v234, v143, v144\n\t"
      "v_max3_u32 v234, v234, v145, v146\n\t"
      "v_max3_u32 v234, v234, v147, v148\n\t"
      "v_max3_u32 v234, v234, v149, v150\n\t"
      "v_max3_u32 v234, v234, v151, v152\n\t"
      "v_max3_u32 v234, v234, v153, v154\n\t"
      "v_max_u32_e32 v234, v234, v155\n\t"
      "v_cmp_eq_u32_e32 vcc, -1, v234\n\t"
      "s_cbranch_vccnz NQ3L_%=\n\t"
      "NQ3X_%=:\n\t"
      "v_mfma_f32_16x16x32_bf16 v[200:203], v[128:131], v[0:3], 0\n\t"
      "v_mfma_f32_16x16x32_bf16 v[204:207], v[128:131], v[4:7], 0\n\t"
      "v_mfma_f32_16x16x32_bf16 v[208:211], v[128:131], v[8:11], 0\n\t"
      "v_mfma_f32_16x16x32_bf16 v[212:215], v[128:131], v[12:15], 0\n\t"
      "v_mfma_f32_16x16x32_bf16 v[200:203], v[132:135], v[16:19], v[200:203]\n\t"
      "v_mfma_f32_16x16x32_bf16 v[204:207], v[132:135], v[20:23], v[204:207]\n\t"
      "v_mfma_f32_16x16x32_bf16 v[208:211], v[132:135], v[24:27], v[208:211]\n\t"
      "v_mfma_f32_16x16x32_bf16 v[212:215], v[132:135], v[28:31], v[212:215]\n\t"
      "v_mfma_f32_16x16x32_bf16 v[200:203], v[136:139], v[32:35], v[200:203]\n\t"
      "v_mfma_f32_16x16x32_bf16 v[204:207], v[136:139], v[36:39], v[204:207]\n\t"
      "v_mfma_f32_16x16x32_bf16 v[208:211], v[136:139], v[40:43], v[208:211]\n\t"
      "v_mfma_f32_16x16x32_bf16 v[212:215], v[136:139], v[44:47], v[212:215]\n\t"
      "v_mfma_f32_16x16x32_bf16 v[200:203], v[140:143], v[48:51], v[200:203]\n\t"
      "v_mfma_f32_16x16x32_bf16 v[204:207], v[140:143], v[52:55], v[204:207]\n\t"
      "v_mfma_f32_16x16x32_bf16 v[208:211], v[140:143], v[56:59], v[208:211]\n\t"
      "v_mfma_f32_16x16x32_bf16 v[212:215], v[140:143], v[60:63], v[212:215]\n\t"
      "v_mfma_f32_16x16x32_bf16 v[200:203], v[144:147], v[64:67], v[200:203]\n\t"
      "v_mfma_f32_16x16x32_bf16 v[204:207], v[144:147], v[68:71], v[204:207]\n\t"
      "v_mfma_f32_16x16x32_bf16 v[208:211], v[144:147], v[72:75], v[208:211]\n\t"
      "v_mfma_f32_16x16x32_bf16 v[212:215], v[144:147], v[76:79], v[212:215]\n\t"
      "v_mfma_f32_16x16x32_bf16 v[200:203], v[148:151], v[80:83], v[200:203]\n\t"
      "v_mfma_f32_16x16x32_bf16 v[204:207], v[148:151], v[84:87], v[204:207]\n\t"
      "v_mfma_f32_16x16x32_bf16 v[208:211], v[148:151], v[88:91], v[208:211]\n\t"
      "v_mfma_f32_16x16x32_bf16 v[212:215], v[148:151], v[92:95], v[212:215]\n\t"
      "v_mfma_f32_16x16x32_bf16 v[200:203], v[152:155], v[96:99], v[200:203]\n\t"
      "v_mfma_f32_16x16x32_bf16 v[204:207], v[152:155], v[100:103], v[204:207]\n\t"
      "v_mfma_f32_16x16x32_bf16 v[208:211], v[152:155], v[104:107], v[208:211]\n\t"
      "v_mfma_f32_16x16x32_bf16 v[212:215], v[152:155], v[108:111], v[212:215]\n\t"
      "global_load_dwordx4 v[128:131], v224, s[60:61] sc1\n\t"
      "global_load_dwordx4 v[132:135], v225, s[60:61] sc1\n\t"
      "global_load_dwordx4 v[136:139], v226, s[60:61] sc1\n\t"
      "global_load_dwordx4 v[140:143], v227, s[60:61] sc1\n\t"
      "global_load_dwordx4 v[144:147], v228, s[60:61] sc1\n\t"
      "global_load_dwordx4 v[148:151], v229, s[60:61] sc1\n\t"
      "global_load_dwordx4 v[152:155], v230, s[60:61] sc1\n\t"
      "s_waitcnt vmcnt(0)\n\t"
      "s_nop 7\n\t"
      "s_branch NQ3B_%=\n\t"
      "FQ29R_%=:\n\t"
      "s_mov_b32 s51, 0\n\t"
      "FQ29L_%=:\n\t"
      "s_add_u32 s51, s51, 1\n\t"
      "s_cmp_lt_u32 s51, 262144\n\t"
      "s_cbranch_scc0 FQ29X_%=\n\t"
      "s_waitcnt vmcnt(0)\n\t"
      "s_sleep 4\n\t"
      "global_load_dwordx4 v[132:135], v225, s[60:61] sc1\n\t"
      "global_load_dwordx4 v[136:139], v226, s[60:61] sc1\n\t"
      "global_load_dwordx4 v[140:143], v227, s[60:61] sc1\n\t"
      "global_load_dwordx4 v[144:147], v228, s[60:61] sc1\n\t"
      "global_load_dwordx4 v[148:151], v229, s[60:61] sc1\n\t"
      "global_load_dwordx4 v[152:155], v230, s[60:61] sc1\n\t"
      "s_waitcnt vmcnt(5)\n\t"
      "v_max3_u32 v234, v132, v133, v134\n\t"
      "v_max_u32_e32 v234, v234, v135\n\t"
      "v_cmp_eq_u32_e32 vcc, -1, v234\n\t"
      "s_cbranch_vccnz FQ29L_%=\n\t"
      "FQ29X_%=:\n\t"
      "s_branch FQ29B_%=\n\t"
      "FQ30R_%=:\n\t"
      "s_mov_b32 s51, 0\n\t"
      "FQ30L_%=:\n\t"
      "s_add_u32 s51, s51, 1\n\t"
      "s_cmp_lt_u32 s51, 262144\n\t"
      "s_cbranch_scc0 FQ30X_%=\n\t"
      "s_waitcnt vmcnt(0)\n\t"
      "s_sleep 4\n\t"
      "global_load_dwordx4 v[136:139], v226, s[60:61] sc1\n\t"
      "global_load_dwordx4 v[140:143], v227, s[60:61] sc1\n\t"
      "global_load_dwordx4 v[144:147], v228, s[60:61] sc1\n\t"
      "global_load_dwordx4 v[148:151], v229, s[60:61] sc1\n\t"
      "global_load_dwordx4 v[152:155], v230, s[60:61] sc1\n\t"
      "global_load_dwordx4 v[128:131], v224, s[62:63] sc1\n\t"
      "s_waitcnt vmcnt(5)\n\t"
      "v_max3_u32 v234, v136, v137, v138\n\t"
      "v_max_u32_e32 v234, v234, v139\n\t"
      "v_cmp_eq_u32_e32 vcc, -1, v234\n\t"
      "s_cbranch_vccnz FQ30L_%=\n\t"
      "FQ30X_%=:\n\t"
      "s_branch FQ30B_%=\n\t"
      "FQ31R_%=:\n\t"
      "s_mov_b32 s51, 0\n\t"
      "FQ31L_%=:\n\t"
      "s_add_u32 s51, s51, 1\n\t"
      "s_cmp_lt_u32 s51, 262144\n\t"
      "s_cbranch_scc0 FQ31X_%=\n\t"
      "s_waitcnt vmcnt(0)\n\t"
      "s_sleep 4\n\t"
      "global_load_dwordx4 v[140:143], v227, s[60:61] sc1\n\t"
      "global_load_dwordx4 v[144:147], v228, s[60:61] sc1\n\t"
      "global_load_dwordx4 v[148:151], v229, s[60:61] sc1\n\t"
      "global_load_dwordx4 v[152:155], v230, s[60:61] sc1\n\t"
      "global_load_dwordx4 v[128:131], v224, s[62:63] sc1\n\t"
      "global_load_dwordx4 v[132:135], v225, s[62:63] sc1\n\t"
      "s_waitcnt vmcnt(5)\n\t"
      "v_max3_u32 v234, v140, v141, v142\n\t"
      "v_max_u32_e32 v234, v234, v143\n\t"
      "v_cmp_eq_u32_e32 vcc, -1, v234\n\t"
      "s_cbranch_vccnz FQ31L_%=\n\t"
      "FQ31X_%=:\n\t"
      "s_branch FQ31B_%=\n\t"
      "FQ32R_%=:\n\t"
      "s_mov_b32 s51, 0\n\t"
      "FQ32L_%=:\n\t"
      "s_add_u32 s51, s51, 1\n\t"
      "s_cmp_lt_u32 s51, 262144\n\t"
      "s_cbranch_scc0 FQ32X_%=\n\t"
      "s_waitcnt vmcnt(0)\n\t"
      "s_sleep 4\n\t"
      "global_load_dwordx4 v[144:147], v228, s[60:61] sc1\n\t"
      "global_load_dwordx4 v[148:151], v229, s[60:61] sc1\n\t"
      "global_load_dwordx4 v[152:155], v230, s[60:61] sc1\n\t"
      "global_load_dwordx4 v[128:131], v224, s[62:63] sc1\n\t"
      "global_load_dwordx4 v[132:135], v225, s[62:63] sc1\n\t"
      "global_load_dwordx4 v[136:139], v226, s[62:63] sc1\n\t"
      "s_waitcnt vmcnt(5)\n\t"
      "v_max3_u32 v234, v144, v145, v146\n\t"
      "v_max_u32_e32 v234, v234, v147\n\t"
      "v_cmp_eq_u32_e32 vcc, -1, v234\n\t"
      "s_cbranch_vccnz FQ32L_%=\n\t"
      "FQ32X_%=:\n\t"
      "s_branch FQ32B_%=\n\t"
      "FQ33R_%=:\n\t"
      "s_mov_b32 s51, 0\n\t"
      "FQ33L_%=:\n\t"
      "s_add_u32 s51, s51, 1\n\t"
      "s_cmp_lt_u32 s51, 262144\n\t"
      "s_cbranch_scc0 FQ33X_%=\n\t"
      "s_waitcnt vmcnt(0)\n\t"
      "s_sleep 4\n\t"
      "global_load_dwordx4 v[148:151], v229, s[60:61] sc1\n\t"
      "global_load_dwordx4 v[152:155], v230, s[60:61] sc1\n\t"
      "global_load_dwordx4 v[128:131], v224, s[62:63] sc1\n\t"
      "global_load_dwordx4 v[132:135], v225, s[62:63] sc1\n\t"
      "global_load_dwordx4 v[136:139], v226, s[62:63] sc1\n\t"
      "global_load_dwordx4 v[140:143], v227, s[62:63] sc1\n\t"
      "s_waitcnt vmcnt(5)\n\t"
      "v_max3_u32 v234, v148, v149, v150\n\t"
      "v_max_u32_e32 v234, v234, v151\n\t"
      "v_cmp_eq_u32_e32 vcc, -1, v234\n\t"
      "s_cbranch_vccnz FQ33L_%=\n\t"
      "FQ33X_%=:\n\t"
      "s_branch FQ33B_%=\n\t"
      "FQ34R_%=:\n\t"
      "s_mov_b32 s51, 0\n\t"
      "FQ34L_%=:\n\t"
      "s_add_u32 s51, s51, 1\n\t"
      "s_cmp_lt_u32 s51, 262144\n\t"
      "s_cbranch_scc0 FQ34X_%=\n\t"
      "s_waitcnt vmcnt(0)\n\t"
      "s_sleep 4\n\t"
      "global_load_dwordx4 v[152:155], v230, s[60:61] sc1\n\t"
      "global_load_dwordx4 v[128:131], v224, s[62:63] sc1\n\t"
      "global_load_dwordx4 v[132:135], v225, s[62:63] sc1\n\t"
      "global_load_dwordx4 v[136:139], v226, s[62:63] sc1\n\t"
      "global_load_dwordx4 v[140:143], v227, s[62:63] sc1\n\t"
      "global_load_dwordx4 v[144:147], v228, s[62:63] sc1\n\t"
      "s_waitcnt vmcnt(5)\n\t"
      "v_max3_u32 v234, v152, v153, v154\n\t"
      "v_max_u32_e32 v234, v234, v155\n\t"
      "v_cmp_eq_u32_e32 vcc, -1, v234\n\t"
      "s_cbranch_vccnz FQ34L_%=\n\t"
      "FQ34X_%=:\n\t"
      "s_branch FQ34B_%=\n\t"
      "FQ35R_%=:\n\t"
      "s_mov_b32 s51, 0\n\t"
      "FQ35L_%=:\n\t"
      "s_add_u32 s51, s51, 1\n\t"
      "s_cmp_lt_u32 s51, 262144\n\t"
      "s_cbranch_scc0 FQ35X_%=\n\t"
      "s_waitcnt vmcnt(0)\n\t"
      "s_sleep 4\n\t"
      "global_load_dwordx4 v[128:131], v224, s[62:63] sc1\n\t"
      "global_load_dwordx4 v[132:135], v225, s[62:63] sc1\n\t"
      "global_load_dwordx4 v[136:139], v226, s[62:63] sc1\n\t"
      "global_load_dwordx4 v[140:143], v227, s[62:63] sc1\n\t"
      "global_load_dwordx4 v[144:147], v228, s[62:63] sc1\n\t"
      "global_load_dwordx4 v[148:151], v229, s[62:63] sc1\n\t"
      "s_waitcnt vmcnt(5)\n\t"
      "v_max3_u32 v234, v128, v129, v130\n\t"
      "v_max_u32_e32 v234, v234, v131\n\t"
      "v_cmp_eq_u32_e32 vcc, -1, v234\n\t"
      "s_cbranch_vccnz FQ35L_%=\n\t"
      "FQ35X_%=:\n\t"
      "s_branch FQ35B_%=\n\t"
      "NQ4R_%=:\n\t"
      "s_mov_b32 s51, 0\n\t"
      "NQ4L_%=:\n\t"
      "s_add_u32 s51, s51, 1\n\t"
      "s_cmp_lt_u32 s51, 262144\n\t"
      "s_cbranch_scc0 NQ4X_%=\n\t"
      "s_waitcnt vmcnt(0)\n\t"
      "s_sleep 1\n\t"
      "global_load_dwordx4 v[128:131], v224, s[60:61] sc1\n\t"
      "global_load_dwordx4 v[132:135], v225, s[60:61] sc1\n\t"
      "global_load_dwordx4 v[136:139], v226, s[60:61] sc1\n\t"
      "global_load_dwordx4 v[140:143], v227, s[60:61] sc1\n\t"
      "global_load_dwordx4 v[144:147], v228, s[60:61] sc1\n\t"
      "global_load_dwordx4 v[148:151], v229, s[60:61] sc1\n\t"
      "global_load_dwordx4 v[152:155], v230, s[60:61] sc1\n\t"
      "s_waitcnt vmcnt(0)\n\t"
      "v_max3_u32 v234, v128, v129, v130\n\t"
      "v_max3_u32 v234, v234, v131, v132\n\t"
      "v_max3_u32 v234, v234, v133, v134\n\t"
      "v_max3_u32 v234, v234, v135, v136\n\t"
      "v_max3_u32 v234, v234, v137, v138\n\t"
      "v_max3_u32 v234, v234, v139, v140\n\t"
      "v_max3_u32 v234, v234, v141, v142\n\t"
      "v_max3_u32 v234, v234, v143, v144\n\t"
      "v_max3_u32 v234, v234, v145, v146\n\t"
      "v_max3_u32 v234, v234, v147, v148\n\t"
      "v_max3_u32 v234, v234, v149, v150\n\t"
      "v_max3_u32 v234, v234, v151, v152\n\t"
      "v_max3_u32 v234, v234, v153, v154\n\t"
      "v_max_u32_e32 v234, v234, v155\n\t"
      "v_cmp_eq_u32_e32 vcc, -1, v234\n\t"
      "s_cbranch_vccnz NQ4L_%=\n\t"
      "NQ4X_%=:\n\t"
      "v_mfma_f32_16x16x32_bf16 v[184:187], v[128:131], v[0:3], 0\n\t"
      "v_mfma_f32_16x16x32_bf16 v[188:191], v[128:131], v[4:7], 0\n\t"
      "v_mfma_f32_16x16x32_bf16 v[192:195], v[128:131], v[8:11], 0\n\t"
      "v_mfma_f32_16x16x32_bf16 v[196:199], v[128:131], v[12:15], 0\n\t"
      "v_mfma_f32_16x16x32_bf16 v[184:187], v[132:135], v[16:19], v[184:187]\n\t"
      "v_mfma_f32_16x16x32_bf16 v[188:191], v[132:135], v[20:23], v[188:191]\n\t"
      "v_mfma_f32_16x16x32_bf16 v[192:195], v[132:135], v[24:27], v[192:195]\n\t"
      "v_mfma_f32_16x16x32_bf16 v[196:199], v[132:135], v[28:31], v[196:199]\n\t"
      "v_mfma_f32_16x16x32_bf16 v[184:187], v[136:139], v[32:35], v[184:187]\n\t"
      "v_mfma_f32_16x16x32_bf16 v[188:191], v[136:139], v[36:39], v[188:191]\n\t"
      "v_mfma_f32_16x16x32_bf16 v[192:195], v[136:139], v[40:43], v[192:195]\n\t"
      "v_mfma_f32_16x16x32_bf16 v[196:199], v[136:139], v[44:47], v[196:199]\n\t"
      "v_mfma_f32_16x16x32_bf16 v[184:187], v[140:143], v[48:51], v[184:187]\n\t"
      "v_mfma_f32_16x16x32_bf16 v[188:191], v[140:143], v[52:55], v[188:191]\n\t"
      "v_mfma_f32_16x16x32_bf16 v[192:195], v[140:143], v[56:59], v[192:195]\n\t"
      "v_mfma_f32_16x16x32_bf16 v[196:199], v[140:143], v[60:63], v[196:199]\n\t"
      "v_mfma_f32_16x16x32_bf16 v[184:187], v[144:147], v[64:67], v[184:187]\n\t"
      "v_mfma_f32_16x16x32_bf16 v[188:191], v[144:147], v[68:71], v[188:191]\n\t"
      "v_mfma_f32_16x16x32_bf16 v[192:195], v[144:147], v[72:75], v[192:195]\n\t"
      "v_mfma_f32_16x16x32_bf16 v[196:199], v[144:147], v[76:79], v[196:199]\n\t"
      "v_mfma_f32_16x16x32_bf16 v[184:187], v[148:151], v[80:83], v[184:187]\n\t"
      "v_mfma_f32_16x16x32_bf16 v[188:191], v[148:151], v[84:87], v[188:191]\n\t"
      "v_mfma_f32_16x16x32_bf16 v[192:195], v[148:151], v[88:91], v[192:195]\n\t"
      "v_mfma_f32_16x16x32_bf16 v[196:199], v[148:151], v[92:95], v[196:199]\n\t"
      "v_mfma_f32_16x16x32_bf16 v[184:187], v[152:155], v[96:99], v[184:187]\n\t"
      "v_mfma_f32_16x16x32_bf16 v[188:191], v[152:155], v[100:103], v[188:191]\n\t"
      "v_mfma_f32_16x16x32_bf16 v[192:195], v[152:155], v[104:107], v[192:195]\n\t"
      "v_mfma_f32_16x16x32_bf16 v[196:199], v[152:155], v[108:111], v[196:199]\n\t"
      "global_load_dwordx4 v[128:131], v224, s[62:63] sc1\n\t"
      "global_load_dwordx4 v[132:135], v225, s[62:63] sc1\n\t"
      "global_load_dwordx4 v[136:139], v226, s[62:63] sc1\n\t"
      "global_load_dwordx4 v[140:143], v227, s[62:63] sc1\n\t"
      "global_load_dwordx4 v[144:147], v228, s[62:63] sc1\n\t"
      "global_load_dwordx4 v[148:151], v229, s[62:63] sc1\n\t"
      "global_load_dwordx4 v[152:155], v230, s[62:63] sc1\n\t"
      "s_waitcnt vmcnt(0)\n\t"
      "s_nop 7\n\t"
      "s_branch NQ4B_%=\n\t"
      "FQ36R_%=:\n\t"
      "s_mov_b32 s51, 0\n\t"
      "FQ36L_%=:\n\t"
      "s_add_u32 s51, s51, 1\n\t"
      "s_cmp_lt_u32 s51, 262144\n\t"
      "s_cbranch_scc0 FQ36X_%=\n\t"
      "s_waitcnt vmcnt(0)\n\t"
      "s_sleep 4\n\t"
      "global_load_dwordx4 v[132:135], v225, s[62:63] sc1\n\t"
      "global_load_dwordx4 v[136:139], v226, s[62:63] sc1\n\t"
      "global_load_dwordx4 v[140:143], v227, s[62:63] sc1\n\t"
      "global_load_dwordx4 v[144:147], v228, s[62:63] sc1\n\t"
      "global_load_dwordx4 v[148:151], v229, s[62:63] sc1\n\t"
      "global_load_dwordx4 v[152:155], v230, s[62:63] sc1\n\t"
      "s_waitcnt vmcnt(5)\n\t"
      "v_max3_u32 v234, v132, v133, v134\n\t"
      "v_max_u32_e32 v234, v234, v135\n\t"
      "v_cmp_eq_u32_e32 vcc, -1, v234\n\t"
      "s_cbranch_vccnz FQ36L_%=\n\t"
      "FQ36X_%=:\n\t"
      "s_branch FQ36B_%=\n\t"
      "FQ37R_%=:\n\t"
      "s_mov_b32 s51, 0\n\t"
      "FQ37L_%=:\n\t"
      "s_add_u32 s51, s51, 1\n\t"
      "s_cmp_lt_u32 s51, 262144\n\t"
      "s_cbranch_scc0 FQ37X_%=\n\t"
      "s_waitcnt vmcnt(0)\n\t"
      "s_sleep 4\n\t"
      "global_load_dwordx4 v[136:139], v226, s[62:63] sc1\n\t"
      "global_load_dwordx4 v[140:143], v227, s[62:63] sc1\n\t"
      "global_load_dwordx4 v[144:147], v228, s[62:63] sc1\n\t"
      "global_load_dwordx4 v[148:151], v229, s[62:63] sc1\n\t"
      "global_load_dwordx4 v[152:155], v230, s[62:63] sc1\n\t"
      "global_load_dwordx4 v[128:131], v224, s[64:65] sc1\n\t"
      "s_waitcnt vmcnt(5)\n\t"
      "v_max3_u32 v234, v136, v137, v138\n\t"
      "v_max_u32_e32 v234, v234, v139\n\t"
      "v_cmp_eq_u32_e32 vcc, -1, v234\n\t"
      "s_cbranch_vccnz FQ37L_%=\n\t"
      "FQ37X_%=:\n\t"
      "s_branch FQ37B_%=\n\t"
      "FQ38R_%=:\n\t"
      "s_mov_b32 s51, 0\n\t"
      "FQ38L_%=:\n\t"
      "s_add_u32 s51, s51, 1\n\t"
      "s_cmp_lt_u32 s51, 262144\n\t"
      "s_cbranch_scc0 FQ38X_%=\n\t"
      "s_waitcnt vmcnt(0)\n\t"
      "s_sleep 4\n\t"
      "global_load_dwordx4 v[140:143], v227, s[62:63] sc1\n\t"
      "global_load_dwordx4 v[144:147], v228, s[62:63] sc1\n\t"
      "global_load_dwordx4 v[148:151], v229, s[62:63] sc1\n\t"
      "global_load_dwordx4 v[152:155], v230, s[62:63] sc1\n\t"
      "global_load_dwordx4 v[128:131], v224, s[64:65] sc1\n\t"
      "global_load_dwordx4 v[132:135], v225, s[64:65] sc1\n\t"
      "s_waitcnt vmcnt(5)\n\t"
      "v_max3_u32 v234, v140, v141, v142\n\t"
      "v_max_u32_e32 v234, v234, v143\n\t"
      "v_cmp_eq_u32_e32 vcc, -1, v234\n\t"
      "s_cbranch_vccnz FQ38L_%=\n\t"
      "FQ38X_%=:\n\t"
      "s_branch FQ38B_%=\n\t"
      "FQ39R_%=:\n\t"
      "s_mov_b32 s51, 0\n\t"
      "FQ39L_%=:\n\t"
      "s_add_u32 s51, s51, 1\n\t"
      "s_cmp_lt_u32 s51, 262144\n\t"
      "s_cbranch_scc0 FQ39X_%=\n\t"
      "s_waitcnt vmcnt(0)\n\t"
      "s_sleep 4\n\t"
      "global_load_dwordx4 v[144:147], v228, s[62:63] sc1\n\t"
      "global_load_dwordx4 v[148:151], v229, s[62:63] sc1\n\t"
      "global_load_dwordx4 v[152:155], v230, s[62:63] sc1\n\t"
      "global_load_dwordx4 v[128:131], v224, s[64:65] sc1\n\t"
      "global_load_dwordx4 v[132:135], v225, s[64:65] sc1\n\t"
      "global_load_dwordx4 v[136:139], v226, s[64:65] sc1\n\t"
      "s_waitcnt vmcnt(5)\n\t"
      "v_max3_u32 v234, v144, v145, v146\n\t"
      "v_max_u32_e32 v234, v234, v147\n\t"
      "v_cmp_eq_u32_e32 vcc, -1, v234\n\t"
      "s_cbranch_vccnz FQ39L_%=\n\t"
      "FQ39X_%=:\n\t"
      "s_branch FQ39B_%=\n\t"
      "FQ40R_%=:\n\t"
      "s_mov_b32 s51, 0\n\t"
      "FQ40L_%=:\n\t"
      "s_add_u32 s51, s51, 1\n\t"
      "s_cmp_lt_u32 s51, 262144\n\t"
      "s_cbranch_scc0 FQ40X_%=\n\t"
      "s_waitcnt vmcnt(0)\n\t"
      "s_sleep 4\n\t"
      "global_load_dwordx4 v[148:151], v229, s[62:63] sc1\n\t"
      "global_load_dwordx4 v[152:155], v230, s[62:63] sc1\n\t"
      "global_load_dwordx4 v[128:131], v224, s[64:65] sc1\n\t"
      "global_load_dwordx4 v[132:135], v225, s[64:65] sc1\n\t"
      "global_load_dwordx4 v[136:139], v226, s[64:65] sc1\n\t"
      "global_load_dwordx4 v[140:143], v227, s[64:65] sc1\n\t"
      "s_waitcnt vmcnt(5)\n\t"
      "v_max3_u32 v234, v148, v149, v150\n\t"
      "v_max_u32_e32 v234, v234, v151\n\t"
      "v_cmp_eq_u32_e32 vcc, -1, v234\n\t"
      "s_cbranch_vccnz FQ40L_%=\n\t"
      "FQ40X_%=:\n\t"
      "s_branch FQ40B_%=\n\t"
      "FQ41R_%=:\n\t"
      "s_mov_b32 s51, 0\n\t"
      "FQ41L_%=:\n\t"
      "s_add_u32 s51, s51, 1\n\t"
      "s_cmp_lt_u32 s51, 262144\n\t"
      "s_cbranch_scc0 FQ41X_%=\n\t"
      "s_waitcnt vmcnt(0)\n\t"
      "s_sleep 4\n\t"
      "global_load_dwordx4 v[152:155], v230, s[62:63] sc1\n\t"
      "global_load_dwordx4 v[128:131], v224, s[64:65] sc1\n\t"
      "global_load_dwordx4 v[132:135], v225, s[64:65] sc1\n\t"
      "global_load_dwordx4 v[136:139], v226, s[64:65] sc1\n\t"
      "global_load_dwordx4 v[140:143], v227, s[64:65] sc1\n\t"
      "global_load_dwordx4 v[144:147], v228, s[64:65] sc1\n\t"
      "s_waitcnt vmcnt(5)\n\t"
      "v_max3_u32 v234, v152, v153, v154\n\t"
      "v_max_u32_e32 v234, v234, v155\n\t"
      "v_cmp_eq_u32_e32 vcc, -1, v234\n\t"
      "s_cbranch_vccnz FQ41L_%=\n\t"
      "FQ41X_%=:\n\t"
      "s_branch FQ41B_%=\n\t"
      "FQ42R_%=:\n\t"
      "s_mov_b32 s51, 0\n\t"
      "FQ42L_%=:\n\t"
      "s_add_u32 s51, s51, 1\n\t"
      "s_cmp_lt_u32 s51, 262144\n\t"
      "s_cbranch_scc0 FQ42X_%=\n\t"
      "s_sleep 1\n\t"
      "s_mov_b64 exec, 15\n\t"
      "v_mov_b64_e32 v[232:233], 0\n\t"
      "global_atomic_or_x2 v[232:233], v247, v[232:233], s[12:13] sc0 sc1\n\t"
      "s_waitcnt vmcnt(0)\n\t"
      "v_subrev_u32_e32 v232, s14, v232\n\t"
      "v_cmp_ne_u32_e64 s[48:49], s15, v233\n\t"
      "v_subrev_u32_e32 v232, 6, v232\n\t"
      "v_cmp_lt_u32_e32 vcc, 7, v232\n\t"
      "s_mov_b64 exec, -1\n\t"
      "s_or_b64 vcc, vcc, s[48:49]\n\t"
      "s_cbranch_vccnz FQ42L_%=\n\t"
      "FQ42X_%=:\n\t"
      "FQ42DR_%=:\n\t"
      "s_mov_b32 s51, 0\n\t"
      "FQ42DL_%=:\n\t"
      "s_add_u32 s51, s51, 1\n\t"
      "s_cmp_lt_u32 s51, 262144\n\t"
      "s_cbranch_scc0 FQ42DX_%=\n\t"
      "s_waitcnt vmcnt(0)\n\t"
      "s_sleep 1\n\t"
      "global_load_dwordx4 v[128:131], v224, s[64:65] sc1\n\t"
      "global_load_dwordx4 v[132:135], v225, s[64:65] sc1\n\t"
      "global_load_dwordx4 v[136:139], v226, s[64:65] sc1\n\t"
      "global_load_dwordx4 v[140:143], v227, s[64:65] sc1\n\t"
      "global_load_dwordx4 v[144:147], v228, s[64:65] sc1\n\t"
      "global_load_dwordx4 v[148:151], v229, s[64:65] sc1\n\t"
      "s_waitcnt vmcnt(5)\n\t"
      "v_max3_u32 v234, v128, v129, v130\n\t"
      "v_max_u32_e32 v234, v234, v131\n\t"
      "v_cmp_eq_u32_e32 vcc, -1, v234\n\t"
      "s_cbranch_vccnz FQ42DL_%=\n\t"
      "FQ42DX_%=:\n\t"
      "s_branch FQ42B_%=\n\t"
      "NQ5R_%=:\n\t"
      "s_mov_b32 s51, 0\n\t"
      "NQ5L_%=:\n\t"
      "s_add_u32 s51, s51, 1\n\t"
      "s_cmp_lt_u32 s51, 262144\n\t"
      "s_cbranch_scc0 NQ5X_%=\n\t"
      "s_waitcnt vmcnt(0)\n\t"
      "s_sleep 1\n\t"
      "global_load_dwordx4 v[128:131], v224, s[62:63] sc1\n\t"
      "global_load_dwordx4 v[132:135], v225, s[62:63] sc1\n\t"
      "global_load_dwordx4 v[136:139], v226, s[62:63] sc1\n\t"
      "global_load_dwordx4 v[140:143], v227, s[62:63] sc1\n\t"
      "global_load_dwordx4 v[144:147], v228, s[62:63] sc1\n\t"
      "global_load_dwordx4 v[148:151], v229, s[62:63] sc1\n\t"
      "global_load_dwordx4 v[152:155], v230, s[62:63] sc1\n\t"
      "s_waitcnt vmcnt(0)\n\t"
      "v_max3_u32 v234, v128, v129, v130\n\t"
      "v_max3_u32 v234, v234, v131, v132\n\t"
      "v_max3_u32 v234, v234, v133, v134\n\t"
      "v_max3_u32 v234, v234, v135, v136\n\t"
      "v_max3_u32 v234, v234, v137, v138\n\t"
      "v_max3_u32 v234, v234, v139, v140\n\t"
      "v_max3_u32 v234, v234, v141, v142\n\t"
      "v_max3_u32 v234, v234, v143, v144\n\t"
      "v_max3_u32 v234, v234, v145, v146\n\t"
      "v_max3_u32 v234, v234, v147, v148\n\t"
      "v_max3_u32 v234, v234, v149, v150\n\t"
      "v_max3_u32 v234, v234, v151, v152\n\t"
      "v_max3_u32 v234, v234, v153, v154\n\t"
      "v_max_u32_e32 v234, v234, v155\n\t"
      "v_cmp_eq_u32_e32 vcc, -1, v234\n\t"
      "s_cbranch_vccnz NQ5L_%=\n\t"
      "NQ5X_%=:\n\t"
      "v_mfma_f32_16x16x32_bf16 v[200:203], v[128:131], v[0:3], 0\n\t"
      "v_mfma_f32_16x16x32_bf16 v[204:207], v[128:131], v[4:7], 0\n\t"
      "v_mfma_f32_16x16x32_bf16 v[208:211], v[128:131], v[8:11], 0\n\t"
      "v_mfma_f32_16x16x32_bf16 v[212:215], v[128:131], v[12:15], 0\n\t"
      "v_mfma_f32_16x16x32_bf16 v[200:203], v[132:135], v[16:19], v[200:203]\n\t"
      "v_mfma_f32_16x16x32_bf16 v[204:207], v[132:135], v[20:23], v[204:207]\n\t"
      "v_mfma_f32_16x16x32_bf16 v[208:211], v[132:135], v[24:27], v[208:211]\n\t"
      "v_mfma_f32_16x16x32_bf16 v[212:215], v[132:135], v[28:31], v[212:215]\n\t"
      "v_mfma_f32_16x16x32_bf16 v[200:203], v[136:139], v[32:35], v[200:203]\n\t"
      "v_mfma_f32_16x16x32_bf16 v[204:207], v[136:139], v[36:39], v[204:207]\n\t"
      "v_mfma_f32_16x16x32_bf16 v[208:211], v[136:139], v[40:43], v[208:211]\n\t"
      "v_mfma_f32_16x16x32_bf16 v[212:215], v[136:139], v[44:47], v[212:215]\n\t"
      "v_mfma_f32_16x16x32_bf16 v[200:203], v[140:143], v[48:51], v[200:203]\n\t"
      "v_mfma_f32_16x16x32_bf16 v[204:207], v[140:143], v[52:55], v[204:207]\n\t"
      "v_mfma_f32_16x16x32_bf16 v[208:211], v[140:143], v[56:59], v[208:211]\n\t"
      "v_mfma_f32_16x16x32_bf16 v[212:215], v[140:143], v[60:63], v[212:215]\n\t"
      "v_mfma_f32_16x16x32_bf16 v[200:203], v[144:147], v[64:67], v[200:203]\n\t"
      "v_mfma_f32_16x16x32_bf16 v[204:207], v[144:147], v[68:71], v[204:207]\n\t"
      "v_mfma_f32_16x16x32_bf16 v[208:211], v[144:147], v[72:75], v[208:211]\n\t"
      "v_mfma_f32_16x16x32_bf16 v[212:215], v[144:147], v[76:79], v[212:215]\n\t"
      "v_mfma_f32_16x16x32_bf16 v[200:203], v[148:151], v[80:83], v[200:203]\n\t"
      "v_mfma_f32_16x16x32_bf16 v[204:207], v[148:151], v[84:87], v[204:207]\n\t"
      "v_mfma_f32_16x16x32_bf16 v[208:211], v[148:151], v[88:91], v[208:211]\n\t"
      "v_mfma_f32_16x16x32_bf16 v[212:215], v[148:151], v[92:95], v[212:215]\n\t"
      "v_mfma_f32_16x16x32_bf16 v[200:203], v[152:155], v[96:99], v[200:203]\n\t"
      "v_mfma_f32_16x16x32_bf16 v[204:207], v[152:155], v[100:103], v[204:207]\n\t"
      "v_mfma_f32_16x16x32_bf16 v[208:211], v[152:155], v[104:107], v[208:211]\n\t"
      "v_mfma_f32_16x16x32_bf16 v[212:215], v[152:155], v[108:111], v[212:215]\n\t"
      "global_load_dwordx4 v[128:131], v224, s[64:65] sc1\n\t"
      "global_load_dwordx4 v[132:135], v225, s[64:65] sc1\n\t"
      "global_load_dwordx4 v[136:139], v226, s[64:65] sc1\n\t"
      "global_load_dwordx4 v[140:143], v227, s[64:65] sc1\n\t"
      "global_load_dwordx4 v[144:147], v228, s[64:65] sc1\n\t"
      "global_load_dwordx4 v[148:151], v229, s[64:65] sc1\n\t"
      "global_load_dwordx4 v[152:155], v230, s[64:65] sc1\n\t"
      "s_waitcnt vmcnt(0)\n\t"
      "s_nop 7\n\t"
      "s_branch NQ5B_%=\n\t"
      "FQ43R_%=:\n\t"
      "s_mov_b32 s51, 0\n\t"
      "FQ43L_%=:\n\t"
      "s_add_u32 s51, s51, 1\n\t"
      "s_cmp_lt_u32 s51, 262144\n\t"
      "s_cbranch_scc0 FQ43X_%=\n\t"
      "s_sleep 1\n\t"
      "s_mov_b64 exec, 15\n\t"
      "v_mov_b64_e32 v[232:233], 0\n\t"
      "global_atomic_or_x2 v[232:233], v247, v[232:233], s[12:13] sc0 sc1\n\t"
      "s_waitcnt vmcnt(0)\n\t"
      "v_subrev_u32_e32 v232, s14, v232\n\t"
      "v_cmp_ne_u32_e64 s[48:49], s15, v233\n\t"
      "v_subrev_u32_e32 v232, 6, v232\n\t"
      "v_cmp_lt_u32_e32 vcc, 7, v232\n\t"
      "s_mov_b64 exec, -1\n\t"
      "s_or_b64 vcc, vcc, s[48:49]\n\t"
      "s_cbranch_vccnz FQ43L_%=\n\t"
      "FQ43X_%=:\n\t"
      "FQ43DR_%=:\n\t"
      "s_mov_b32 s51, 0\n\t"
      "FQ43DL_%=:\n\t"
      "s_add_u32 s51, s51, 1\n\t"
      "s_cmp_lt_u32 s51, 262144\n\t"
      "s_cbranch_scc0 FQ43DX_%=\n\t"
      "s_waitcnt vmcnt(0)\n\t"
      "s_sleep 1\n\t"
      "global_load_dwordx4 v[132:135], v225, s[64:65] sc1\n\t"
      "global_load_dwordx4 v[136:139], v226, s[64:65] sc1\n\t"
      "global_load_dwordx4 v[140:143], v227, s[64:65] sc1\n\t"
      "global_load_dwordx4 v[144:147], v228, s[64:65] sc1\n\t"
      "global_load_dwordx4 v[148:151], v229, s[64:65] sc1\n\t"
      "global_load_dwordx4 v[152:155], v230, s[64:65] sc1\n\t"
      "s_waitcnt vmcnt(5)\n\t"
      "v_max3_u32 v234, v132, v133, v134\n\t"
      "v_max_u32_e32 v234, v234, v135\n\t"
      "v_cmp_eq_u32_e32 vcc, -1, v234\n\t"
      "s_cbranch_vccnz FQ43DL_%=\n\t"
      "FQ43DX_%=:\n\t"
      "s_branch FQ43B_%=\n\t"
      "FQ44R_%=:\n\t"
      "s_mov_b32 s51, 0\n\t"
      "FQ44L_%=:\n\t"
      "s_add_u32 s51, s51, 1\n\t"
      "s_cmp_lt_u32 s51, 262144\n\t"
      "s_cbranch_scc0 FQ44X_%=\n\t"
      "s_sleep 1\n\t"
      "s_mov_b64 exec, 15\n\t"
      "v_mov_b64_e32 v[232:233], 0\n\t"
      "global_atomic_or_x2 v[232:233], v247, v[232:233], s[12:13] sc0 sc1\n\t"
      "s_waitcnt vmcnt(0)\n\t"
      "v_subrev_u32_e32 v232, s14, v232\n\t"
      "v_cmp_ne_u32_e64 s[48:49], s15, v233\n\t"
      "v_subrev_u32_e32 v232, 6, v232\n\t"
      "v_cmp_lt_u32_e32 vcc, 7, v232\n\t"
      "s_mov_b64 exec, -1\n\t"
      "s_or_b64 vcc, vcc, s[48:49]\n\t"
      "s_cbranch_vccnz FQ44L_%=\n\t"
      "FQ44X_%=:\n\t"
      "FQ44DR_%=:\n\t"
      "s_mov_b32 s51, 0\n\t"
      "FQ44DL_%=:\n\t"
      "s_add_u32 s51, s51, 1\n\t"
      "s_cmp_lt_u32 s51, 262144\n\t"
      "s_cbranch_scc0 FQ44DX_%=\n\t"
      "s_waitcnt vmcnt(0)\n\t"
      "s_sleep 1\n\t"
      "global_load_dwordx4 v[136:139], v226, s[64:65] sc1\n\t"
      "global_load_dwordx4 v[140:143], v227, s[64:65] sc1\n\t"
      "global_load_dwordx4 v[144:147], v228, s[64:65] sc1\n\t"
      "global_load_dwordx4 v[148:151], v229, s[64:65] sc1\n\t"
      "global_load_dwordx4 v[152:155], v230, s[64:65] sc1\n\t"
      "s_waitcnt vmcnt(4)\n\t"
      "v_max3_u32 v234, v136, v137, v138\n\t"
      "v_max_u32_e32 v234, v234, v139\n\t"
      "v_cmp_eq_u32_e32 vcc, -1, v234\n\t"
      "s_cbranch_vccnz FQ44DL_%=\n\t"
      "FQ44DX_%=:\n\t"
      "s_branch FQ44B_%=\n\t"
      "FQ45R_%=:\n\t"
      "s_mov_b32 s51, 0\n\t"
      "FQ45L_%=:\n\t"
      "s_add_u32 s51, s51, 1\n\t"
      "s_cmp_lt_u32 s51, 262144\n\t"
      "s_cbranch_scc0 FQ45X_%=\n\t"
      "s_sleep 1\n\t"
      "s_mov_b64 exec, 15\n\t"
      "v_mov_b64_e32 v[232:233], 0\n\t"
      "global_atomic_or_x2 v[232:233], v247, v[232:233], s[12:13] sc0 sc1\n\t"
      "s_waitcnt vmcnt(0)\n\t"
      "v_subrev_u32_e32 v232, s14, v232\n\t"
      "v_cmp_ne_u32_e64 s[48:49], s15, v233\n\t"
      "v_subrev_u32_e32 v232, 6, v232\n\t"
      "v_cmp_lt_u32_e32 vcc, 7, v232\n\t"
      "s_mov_b64 exec, -1\n\t"
      "s_or_b64 vcc, vcc, s[48:49]\n\t"
      "s_cbranch_vccnz FQ45L_%=\n\t"
      "FQ45X_%=:\n\t"
      "FQ45DR_%=:\n\t"
      "s_mov_b32 s51, 0\n\t"
      "FQ45DL_%=:\n\t"
      "s_add_u32 s51, s51, 1\n\t"
      "s_cmp_lt_u32 s51, 262144\n\t"
      "s_cbranch_scc0 FQ45DX_%=\n\t"
      "s_waitcnt vmcnt(0)\n\t"
      "s_sleep 1\n\t"
      "global_load_dwordx4 v[140:143], v227, s[64:65] sc1\n\t"
      "global_load_dwordx4 v[144:147], v228, s[64:65] sc1\n\t"
      "global_load_dwordx4 v[148:151], v229, s[64:65] sc1\n\t"
      "global_load_dwordx4 v[152:155], v230, s[64:65] sc1\n\t"
      "s_waitcnt vmcnt(3)\n\t"
      "v_max3_u32 v234, v140, v141, v142\n\t"
      "v_max_u32_e32 v234, v234, v143\n\t"
      "v_cmp_eq_u32_e32 vcc, -1, v234\n\t"
      "s_cbranch_vccnz FQ45DL_%=\n\t"
      "FQ45DX_%=:\n\t"
      "s_branch FQ45B_%=\n\t"
      "FQ46R_%=:\n\t"
      "s_mov_b32 s51, 0\n\t"
      "FQ46L_%=:\n\t"
      "s_add_u32 s51, s51, 1\n\t"
      "s_cmp_lt_u32 s51, 262144\n\t"
      "s_cbranch_scc0 FQ46X_%=\n\t"
      "s_sleep 1\n\t"
      "s_mov_b64 exec, 15\n\t"
      "v_mov_b64_e32 v[232:233], 0\n\t"
      "global_atomic_or_x2 v[232:233], v247, v[232:233], s[12:13] sc0 sc1\n\t"
      "s_waitcnt vmcnt(0)\n\t"
      "v_subrev_u32_e32 v232, s14, v232\n\t"
      "v_cmp_ne_u32_e64 s[48:49], s15, v233\n\t"
      "v_subrev_u32_e32 v232, 6, v232\n\t"
      "v_cmp_lt_u32_e32 vcc, 7, v232\n\t"
      "s_mov_b64 exec, -1\n\t"
      "s_or_b64 vcc, vcc, s[48:49]\n\t"
      "s_cbranch_vccnz FQ46L_%=\n\t"
      "FQ46X_%=:\n\t"
      "FQ46DR_%=:\n\t"
      "s_mov_b32 s51, 0\n\t"
      "FQ46DL_%=:\n\t"
      "s_add_u32 s51, s51, 1\n\t"
      "s_cmp_lt_u32 s51, 262144\n\t"
      "s_cbranch_scc0 FQ46DX_%=\n\t"
      "s_waitcnt vmcnt(0)\n\t"
      "s_sleep 1\n\t"
      "global_load_dwordx4 v[144:147], v228, s[64:65] sc1\n\t"
      "global_load_dwordx4 v[148:151], v229, s[64:65] sc1\n\t"
      "global_load_dwordx4 v[152:155], v230, s[64:65] sc1\n\t"
      "s_waitcnt vmcnt(2)\n\t"
      "v_max3_u32 v234, v144, v145, v146\n\t"
      "v_max_u32_e32 v234, v234, v147\n\t"
      "v_cmp_eq_u32_e32 vcc, -1, v234\n\t"
      "s_cbranch_vccnz FQ46DL_%=\n\t"
      "FQ46DX_%=:\n\t"
      "s_branch FQ46B_%=\n\t"
      "FQ47R_%=:\n\t"
      "s_mov_b32 s51, 0\n\t"
      "FQ47L_%=:\n\t"
      "s_add_u32 s51, s51, 1\n\t"
      "s_cmp_lt_u32 s51, 262144\n\t"
      "s_cbranch_scc0 FQ47X_%=\n\t"
      "s_sleep 1\n\t"
      "s_mov_b64 exec, 15\n\t"
      "v_mov_b64_e32 v[232:233], 0\n\t"
      "global_atomic_or_x2 v[232:233], v247, v[232:233], s[12:13] sc0 sc1\n\t"
      "s_waitcnt vmcnt(0)\n\t"
      "v_subrev_u32_e32 v232, s14, v232\n\t"
      "v_cmp_ne_u32_e64 s[48:49], s15, v233\n\t"
      "v_subrev_u32_e32 v232, 6, v232\n\t"
      "v_cmp_lt_u32_e32 vcc, 7, v232\n\t"
      "s_mov_b64 exec, -1\n\t"
      "s_or_b64 vcc, vcc, s[48:49]\n\t"
      "s_cbranch_vccnz FQ47L_%=\n\t"
      "FQ47X_%=:\n\t"
      "FQ47DR_%=:\n\t"
      "s_mov_b32 s51, 0\n\t"
      "FQ47DL_%=:\n\t"
      "s_add_u32 s51, s51, 1\n\t"
      "s_cmp_lt_u32 s51, 262144\n\t"
      "s_cbranch_scc0 FQ47DX_%=\n\t"
      "s_waitcnt vmcnt(0)\n\t"
      "s_sleep 1\n\t"
      "global_load_dwordx4 v[148:151], v229, s[64:65] sc1\n\t"
      "global_load_dwordx4 v[152:155], v230, s[64:65] sc1\n\t"
      "s_waitcnt vmcnt(1)\n\t"
      "v_max3_u32 v234, v148, v149, v150\n\t"
      "v_max_u32_e32 v234, v234, v151\n\t"
      "v_cmp_eq_u32_e32 vcc, -1, v234\n\t"
      "s_cbranch_vccnz FQ47DL_%=\n\t"
      "FQ47DX_%=:\n\t"
      "s_branch FQ47B_%=\n\t"
      "FQ48R_%=:\n\t"
      "s_mov_b32 s51, 0\n\t"
      "FQ48L_%=:\n\t"
      "s_add_u32 s51, s51, 1\n\t"
      "s_cmp_lt_u32 s51, 262144\n\t"
      "s_cbranch_scc0 FQ48X_%=\n\t"
      "s_sleep 1\n\t"
      "s_mov_b64 exec, 15\n\t"
      "v_mov_b64_e32 v[232:233], 0\n\t"
      "global_atomic_or_x2 v[232:233], v247, v[232:233], s[12:13] sc0 sc1\n\t"
      "s_waitcnt vmcnt(0)\n\t"
      "v_subrev_u32_e32 v232, s14, v232\n\t"
      "v_cmp_ne_u32_e64 s[48:49], s15, v233\n\t"
      "v_subrev_u32_e32 v232, 6, v232\n\t"
      "v_cmp_lt_u32_e32 vcc, 7, v232\n\t"
      "s_mov_b64 exec, -1\n\t"
      "s_or_b64 vcc, vcc, s[48:49]\n\t"
      "s_cbranch_vccnz FQ48L_%=\n\t"
      "FQ48X_%=:\n\t"
      "FQ48DR_%=:\n\t"
      "s_mov_b32 s51, 0\n\t"
      "FQ48DL_%=:\n\t"
      "s_add_u32 s51, s51, 1\n\t"
      "s_cmp_lt_u32 s51, 262144\n\t"
      "s_cbranch_scc0 FQ48DX_%=\n\t"
      "s_waitcnt vmcnt(0)\n\t"
      "s_sleep 1\n\t"
      "global_load_dwordx4 v[152:155], v230, s[64:65] sc1\n\t"
      "s_waitcnt vmcnt(0)\n\t"
      "v_max3_u32 v234, v152, v153, v154\n\t"
      "v_max_u32_e32 v234, v234, v155\n\t"
      "v_cmp_eq_u32_e32 vcc, -1, v234\n\t"
      "s_cbranch_vccnz FQ48DL_%=\n\t"
      "FQ48DX_%=:\n\t"
      "s_branch FQ48B_%=\n\t"
      "NQ6R_%=:\n\t"
      "s_mov_b32 s51, 0\n\t"
      "NQ6L_%=:\n\t"
      "s_add_u32 s51, s51, 1\n\t"
      "s_cmp_lt_u32 s51, 262144\n\t"
      "s_cbranch_scc0 NQ6X_%=\n\t"
      "s_waitcnt vmcnt(0)\n\t"
      "s_sleep 1\n\t"
      "global_load_dwordx4 v[128:131], v224, s[64:65] sc1\n\t"
      "global_load_dwordx4 v[132:135], v225, s[64:65] sc1\n\t"
      "global_load_dwordx4 v[136:139], v226, s[64:65] sc1\n\t"
      "global_load_dwordx4 v[140:143], v227, s[64:65] sc1\n\t"
      "global_load_dwordx4 v[144:147], v228, s[64:65] sc1\n\t"
      "global_load_dwordx4 v[148:151], v229, s[64:65] sc1\n\t"
      "global_load_dwordx4 v[152:155], v230, s[64:65] sc1\n\t"
      "s_waitcnt vmcnt(0)\n\t"
      "v_max3_u32 v234, v128, v129, v130\n\t"
      "v_max3_u32 v234, v234, v131, v132\n\t"
      "v_max3_u32 v234, v234, v133, v134\n\t"
      "v_max3_u32 v234, v234, v135, v136\n\t"
      "v_max3_u32 v234, v234, v137, v138\n\t"
      "v_max3_u32 v234, v234, v139, v140\n\t"
      "v_max3_u32 v234, v234, v141, v142\n\t"
      "v_max3_u32 v234, v234, v143, v144\n\t"
      "v_max3_u32 v234, v234, v145, v146\n\t"
      "v_max3_u32 v234, v234, v147, v148\n\t"
      "v_max3_u32 v234, v234, v149, v150\n\t"
      "v_max3_u32 v234, v234, v151, v152\n\t"
      "v_max3_u32 v234, v234, v153, v154\n\t"
      "v_max_u32_e32 v234, v234, v155\n\t"
      "v_cmp_eq_u32_e32 vcc, -1, v234\n\t"
      "s_cbranch_vccnz NQ6L_%=\n\t"
      "NQ6X_%=:\n\t"
      "v_mfma_f32_16x16x32_bf16 v[184:187], v[128:131], v[0:3], 0\n\t"
      "v_mfma_f32_16x16x32_bf16 v[188:191], v[128:131], v[4:7], 0\n\t"
      "v_mfma_f32_16x16x32_bf16 v[192:195], v[128:131], v[8:11], 0\n\t"
      "v_mfma_f32_16x16x32_bf16 v[196:199], v[128:131], v[12:15], 0\n\t"
      "v_mfma_f32_16x16x32_bf16 v[184:187], v[132:135], v[16:19], v[184:187]\n\t"
      "v_mfma_f32_16x16x32_bf16 v[188:191], v[132:135], v[20:23], v[188:191]\n\t"
      "v_mfma_f32_16x16x32_bf16 v[192:195], v[132:135], v[24:27], v[192:195]\n\t"
      "v_mfma_f32_16x16x32_bf16 v[196:199], v[132:135], v[28:31], v[196:199]\n\t"
      "v_mfma_f32_16x16x32_bf16 v[184:187], v[136:139], v[32:35], v[184:187]\n\t"
      "v_mfma_f32_16x16x32_bf16 v[188:191], v[136:139], v[36:39], v[188:191]\n\t"
      "v_mfma_f32_16x16x32_bf16 v[192:195], v[136:139], v[40:43], v[192:195]\n\t"
      "v_mfma_f32_16x16x32_bf16 v[196:199], v[136:139], v[44:47], v[196:199]\n\t"
      "v_mfma_f32_16x16x32_bf16 v[184:187], v[140:143], v[48:51], v[184:187]\n\t"
      "v_mfma_f32_16x16x32_bf16 v[188:191], v[140:143], v[52:55], v[188:191]\n\t"
      "v_mfma_f32_16x16x32_bf16 v[192:195], v[140:143], v[56:59], v[192:195]\n\t"
      "v_mfma_f32_16x16x32_bf16 v[196:199], v[140:143], v[60:63], v[196:199]\n\t"
      "v_mfma_f32_16x16x32_bf16 v[184:187], v[144:147], v[64:67], v[184:187]\n\t"
      "v_mfma_f32_16x16x32_bf16 v[188:191], v[144:147], v[68:71], v[188:191]\n\t"
      "v_mfma_f32_16x16x32_bf16 v[192:195], v[144:147], v[72:75], v[192:195]\n\t"
      "v_mfma_f32_16x16x32_bf16 v[196:199], v[144:147], v[76:79], v[196:199]\n\t"
      "v_mfma_f32_16x16x32_bf16 v[184:187], v[148:151], v[80:83], v[184:187]\n\t"
      "v_mfma_f32_16x16x32_bf16 v[188:191], v[148:151], v[84:87], v[188:191]\n\t"
      "v_mfma_f32_16x16x32_bf16 v[192:195], v[148:151], v[88:91], v[192:195]\n\t"
      "v_mfma_f32_16x16x32_bf16 v[196:199], v[148:151], v[92:95], v[196:199]\n\t"
      "v_mfma_f32_16x16x32_bf16 v[184:187], v[152:155], v[96:99], v[184:187]\n\t"
      "v_mfma_f32_16x16x32_bf16 v[188:191], v[152:155], v[100:103], v[188:191]\n\t"
      "v_mfma_f32_16x16x32_bf16 v[192:195], v[152:155], v[104:107], v[192:195]\n\t"
      "v_mfma_f32_16x16x32_bf16 v[196:199], v[152:155], v[108:111], v[196:199]\n\t"
      "s_waitcnt vmcnt(0)\n\t"
      "s_nop 7\n\t"
      "s_branch NQ6B_%=\n\t"
      "FP0R_%=:\n\t"
      "s_mov_b32 s51, 0\n\t"
      "FP0L_%=:\n\t"
      "s_add_u32 s51, s51, 1\n\t"
      "s_cmp_lt_u32 s51, 262144\n\t"
      "s_cbranch_scc0 FP0X_%=\n\t"
      "s_waitcnt vmcnt(0)\n\t"
      "s_sleep 1\n\t"
      "global_load_dwordx4 v[128:131], v224, s[52:53] sc1\n\t"
      "global_load_dwordx4 v[132:135], v225, s[52:53] sc1\n\t"
      "global_load_dwordx4 v[136:139], v226, s[52:53] sc1\n\t"
      "global_load_dwordx4 v[140:143], v227, s[52:53] sc1\n\t"
      "global_load_dwordx4 v[144:147], v228, s[52:53] sc1\n\t"
      "global_load_dwordx4 v[148:151], v229, s[52:53] sc1\n\t"
      "global_load_dwordx4 v[152:155], v230, s[52:53] sc1\n\t"
      "s_waitcnt vmcnt(6)\n\t"
      "v_max3_u32 v234, v128, v129, v130\n\t"
      "v_max_u32_e32 v234, v234, v131\n\t"
      "v_cmp_eq_u32_e32 vcc, -1, v234\n\t"
      "s_cbranch_vccnz FP0L_%=\n\t"
      "FP0X_%=:\n\t"
      "s_branch FP0B_%=\n\t"
      "FP1R_%=:\n\t"
      "s_mov_b32 s51, 0\n\t"
      "FP1L_%=:\n\t"
      "s_add_u32 s51, s51, 1\n\t"
      "s_cmp_lt_u32 s51, 262144\n\t"
      "s_cbranch_scc0 FP1X_%=\n\t"
      "s_waitcnt vmcnt(0)\n\t"
      "s_sleep 1\n\t"
      "global_load_dwordx4 v[132:135], v225, s[52:53] sc1\n\t"
      "global_load_dwordx4 v[136:139], v226, s[52:53] sc1\n\t"
      "global_load_dwordx4 v[140:143], v227, s[52:53] sc1\n\t"
      "global_load_dwordx4 v[144:147], v228, s[52:53] sc1\n\t"
      "global_load_dwordx4 v[148:151], v229, s[52:53] sc1\n\t"
      "global_load_dwordx4 v[152:155], v230, s[52:53] sc1\n\t"
      "global_load_dwordx4 v[128:131], v224, s[54:55] sc1\n\t"
      "s_waitcnt vmcnt(6)\n\t"
      "v_max3_u32 v234, v132, v133, v134\n\t"
      "v_max_u32_e32 v234, v234, v135\n\t"
      "v_cmp_eq_u32_e32 vcc, -1, v234\n\t"
      "s_cbranch_vccnz FP1L_%=\n\t"
      "FP1X_%=:\n\t"
      "s_branch FP1B_%=\n\t"
      "FP2R_%=:\n\t"
      "s_mov_b32 s51, 0\n\t"
      "FP2L_%=:\n\t"
      "s_add_u32 s51, s51, 1\n\t"
      "s_cmp_lt_u32 s51, 262144\n\t"
      "s_cbranch_scc0 FP2X_%=\n\t"
      "s_waitcnt vmcnt(0)\n\t"
      "s_sleep 1\n\t"
      "global_load_dwordx4 v[136:139], v226, s[52:53] sc1\n\t"
      "global_load_dwordx4 v[140:143], v227, s[52:53] sc1\n\t"
      "global_load_dwordx4 v[144:147], v228, s[52:53] sc1\n\t"
      "global_load_dwordx4 v[148:151], v229, s[52:53] sc1\n\t"
      "global_load_dwordx4 v[152:155], v230, s[52:53] sc1\n\t"
      "global_load_dwordx4 v[128:131], v224, s[54:55] sc1\n\t"
      "global_load_dwordx4 v[132:135], v225, s[54:55] sc1\n\t"
      "s_waitcnt vmcnt(6)\n\t"
      "v_max3_u32 v234, v136, v137, v138\n\t"
      "v_max_u32_e32 v234, v234, v139\n\t"
      "v_cmp_eq_u32_e32 vcc, -1, v234\n\t"
      "s_cbranch_vccnz FP2L_%=\n\t"
      "FP2X_%=:\n\t"
      "s_branch FP2B_%=\n\t"
      "FP3R_%=:\n\t"
      "s_mov_b32 s51, 0\n\t"
      "FP3L_%=:\n\t"
      "s_add_u32 s51, s51, 1\n\t"
      "s_cmp_lt_u32 s51, 262144\n\t"
      "s_cbranch_scc0 FP3X_%=\n\t"
      "s_waitcnt vmcnt(0)\n\t"
      "s_sleep 1\n\t"
      "global_load_dwordx4 v[140:143], v227, s[52:53] sc1\n\t"
      "global_load_dwordx4 v[144:147], v228, s[52:53] sc1\n\t"
      "global_load_dwordx4 v[148:151], v229, s[52:53] sc1\n\t"
      "global_load_dwordx4 v[152:155], v230, s[52:53] sc1\n\t"
      "global_load_dwordx4 v[128:131], v224, s[54:55] sc1\n\t"
      "global_load_dwordx4 v[132:135], v225, s[54:55] sc1\n\t"
      "global_load_dwordx4 v[136:139], v226, s[54:55] sc1\n\t"
      "s_waitcnt vmcnt(6)\n\t"
      "v_max3_u32 v234, v140, v141, v142\n\t"
      "v_max_u32_e32 v234, v234, v143\n\t"
      "v_cmp_eq_u32_e32 vcc, -1, v234\n\t"
      "s_cbranch_vccnz FP3L_%=\n\t"
      "FP3X_%=:\n\t"
      "s_branch FP3B_%=\n\t"
      "FP4R_%=:\n\t"
      "s_mov_b32 s51, 0\n\t"
      "FP4L_%=:\n\t"
      "s_add_u32 s51, s51, 1\n\t"
      "s_cmp_lt_u32 s51, 262144\n\t"
      "s_cbranch_scc0 FP4X_%=\n\t"
      "s_waitcnt vmcnt(0)\n\t"
      "s_sleep 1\n\t"
      "global_load_dwordx4 v[144:147], v228, s[52:53] sc1\n\t"
      "global_load_dwordx4 v[148:151], v229, s[52:53] sc1\n\t"
      "global_load_dwordx4 v[152:155], v230, s[52:53] sc1\n\t"
      "global_load_dwordx4 v[128:131], v224, s[54:55] sc1\n\t"
      "global_load_dwordx4 v[132:135], v225, s[54:55] sc1\n\t"
      "global_load_dwordx4 v[136:139], v226, s[54:55] sc1\n\t"
      "global_load_dwordx4 v[140:143], v227, s[54:55] sc1\n\t"
      "s_waitcnt vmcnt(6)\n\t"
      "v_max3_u32 v234, v144, v145, v146\n\t"
      "v_max_u32_e32 v234, v234, v147\n\t"
      "v_cmp_eq_u32_e32 vcc, -1, v234\n\t"
      "s_cbranch_vccnz FP4L_%=\n\t"
      "FP4X_%=:\n\t"
      "s_branch FP4B_%=\n\t"
      "FP5R_%=:\n\t"
      "s_mov_b32 s51, 0\n\t"
      "FP5L_%=:\n\t"
      "s_add_u32 s51, s51, 1\n\t"
      "s_cmp_lt_u32 s51, 262144\n\t"
      "s_cbranch_scc0 FP5X_%=\n\t"
      "s_waitcnt vmcnt(0)\n\t"
      "s_sleep 1\n\t"
      "global_load_dwordx4 v[148:151], v229, s[52:53] sc1\n\t"
      "global_load_dwordx4 v[152:155], v230, s[52:53] sc1\n\t"
      "global_load_dwordx4 v[128:131], v224, s[54:55] sc1\n\t"
      "global_load_dwordx4 v[132:135], v225, s[54:55] sc1\n\t"
      "global_load_dwordx4 v[136:139], v226, s[54:55] sc1\n\t"
      "global_load_dwordx4 v[140:143], v227, s[54:55] sc1\n\t"
      "global_load_dwordx4 v[144:147], v228, s[54:55] sc1\n\t"
      "s_waitcnt vmcnt(6)\n\t"
      "v_max3_u32 v234, v148, v149, v150\n\t"
      "v_max_u32_e32 v234, v234, v151\n\t"
      "v_cmp_eq_u32_e32 vcc, -1, v234\n\t"
      "s_cbranch_vccnz FP5L_%=\n\t"
      "FP5X_%=:\n\t"
      "s_branch FP5B_%=\n\t"
      "FP6R_%=:\n\t"
      "s_mov_b32 s51, 0\n\t"
      "FP6L_%=:\n\t"
      "s_add_u32 s51, s51, 1\n\t"
      "s_cmp_lt_u32 s51, 262144\n\t"
      "s_cbranch_scc0 FP6X_%=\n\t"
      "s_waitcnt vmcnt(0)\n\t"
      "s_sleep 1\n\t"
      "global_load_dwordx4 v[152:155], v230, s[52:53] sc1\n\t"
      "global_load_dwordx4 v[128:131], v224, s[54:55] sc1\n\t"
      "global_load_dwordx4 v[132:135], v225, s[54:55] sc1\n\t"
      "global_load_dwordx4 v[136:139], v226, s[54:55] sc1\n\t"
      "global_load_dwordx4 v[140:143], v227, s[54:55] sc1\n\t"
      "global_load_dwordx4 v[144:147], v228, s[54:55] sc1\n\t"
      "global_load_dwordx4 v[148:151], v229, s[54:55] sc1\n\t"
      "s_waitcnt vmcnt(6)\n\t"
      "v_max3_u32 v234, v152, v153, v154\n\t"
      "v_max_u32_e32 v234, v234, v155\n\t"
      "v_cmp_eq_u32_e32 vcc, -1, v234\n\t"
      "s_cbranch_vccnz FP6L_%=\n\t"
      "FP6X_%=:\n\t"
      "s_branch FP6B_%=\n\t"
      "FP7R_%=:\n\t"
      "s_mov_b32 s51, 0\n\t"
      "FP7L_%=:\n\t"
      "s_add_u32 s51, s51, 1\n\t"
      "s_cmp_lt_u32 s51, 262144\n\t"
      "s_cbranch_scc0 FP7X_%=\n\t"
      "s_waitcnt vmcnt(0)\n\t"
      "s_sleep 4\n\t"
      "global_load_dwordx4 v[128:131], v224, s[54:55] sc1\n\t"
      "global_load_dwordx4 v[132:135], v225, s[54:55] sc1\n\t"
      "global_load_dwordx4 v[136:139], v226, s[54:55] sc1\n\t"
      "global_load_dwordx4 v[140:143], v227, s[54:55] sc1\n\t"
      "global_load_dwordx4 v[144:147], v228, s[54:55] sc1\n\t"
      "global_load_dwordx4 v[148:151], v229, s[54:55] sc1\n\t"
      "s_waitcnt vmcnt(5)\n\t"
      "v_max3_u32 v234, v128, v129, v130\n\t"
      "v_max_u32_e32 v234, v234, v131\n\t"
      "v_cmp_eq_u32_e32 vcc, -1, v234\n\t"
      "s_cbranch_vccnz FP7L_%=\n\t"
      "FP7X_%=:\n\t"
      "s_branch FP7B_%=\n\t"
      "FP8R_%=:\n\t"
      "s_mov_b32 s51, 0\n\t"
      "FP8L_%=:\n\t"
      "s_add_u32 s51, s51, 1\n\t"
      "s_cmp_lt_u32 s51, 262144\n\t"
      "s_cbranch_scc0 FP8X_%=\n\t"
      "s_waitcnt vmcnt(0)\n\t"
      "s_sleep 4\n\t"
      "global_load_dwordx4 v[132:135], v225, s[54:55] sc1\n\t"
      "global_load_dwordx4 v[136:139], v226, s[54:55] sc1\n\t"
      "global_load_dwordx4 v[140:143], v227, s[54:55] sc1\n\t"
      "global_load_dwordx4 v[144:147], v228, s[54:55] sc1\n\t"
      "global_load_dwordx4 v[148:151], v229, s[54:55] sc1\n\t"
      "global_load_dwordx4 v[152:155], v230, s[54:55] sc1\n\t"
      "s_waitcnt vmcnt(5)\n\t"
      "v_max3_u32 v234, v132, v133, v134\n\t"
      "v_max_u32_e32 v234, v234, v135\n\t"
      "v_cmp_eq_u32_e32 vcc, -1, v234\n\t"
      "s_cbranch_vccnz FP8L_%=\n\t"
      "FP8X_%=:\n\t"
      "s_branch FP8B_%=\n\t"
      "FP9R_%=:\n\t"
      "s_mov_b32 s51, 0\n\t"
      "FP9L_%=:\n\t"
      "s_add_u32 s51, s51, 1\n\t"
      "s_cmp_lt_u32 s51, 262144\n\t"
      "s_cbranch_scc0 FP9X_%=\n\t"
      "s_waitcnt vmcnt(0)\n\t"
      "s_sleep 4\n\t"
      "global_load_dwordx4 v[136:139], v226, s[54:55] sc1\n\t"
      "global_load_dwordx4 v[140:143], v227, s[54:55] sc1\n\t"
      "global_load_dwordx4 v[144:147], v228, s[54:55] sc1\n\t"
      "global_load_dwordx4 v[148:151], v229, s[54:55] sc1\n\t"
      "global_load_dwordx4 v[152:155], v230, s[54:55] sc1\n\t"
      "global_load_dwordx4 v[128:131], v224, s[56:57] sc1\n\t"
      "s_waitcnt vmcnt(5)\n\t"
      "v_max3_u32 v234, v136, v137, v138\n\t"
      "v_max_u32_e32 v234, v234, v139\n\t"
      "v_cmp_eq_u32_e32 vcc, -1, v234\n\t"
      "s_cbranch_vccnz FP9L_%=\n\t"
      "FP9X_%=:\n\t"
      "s_branch FP9B_%=\n\t"
      "FP10R_%=:\n\t"
      "s_mov_b32 s51, 0\n\t"
      "FP10L_%=:\n\t"
      "s_add_u32 s51, s51, 1\n\t"
      "s_cmp_lt_u32 s51, 262144\n\t"
      "s_cbranch_scc0 FP10X_%=\n\t"
      "s_waitcnt vmcnt(0)\n\t"
      "s_sleep 4\n\t"
      "global_load_dwordx4 v[140:143], v227, s[54:55] sc1\n\t"
      "global_load_dwordx4 v[144:147], v228, s[54:55] sc1\n\t"
      "global_load_dwordx4 v[148:151], v229, s[54:55] sc1\n\t"
      "global_load_dwordx4 v[152:155], v230, s[54:55] sc1\n\t"
      "global_load_dwordx4 v[128:131], v224, s[56:57] sc1\n\t"
      "global_load_dwordx4 v[132:135], v225, s[56:57] sc1\n\t"
      "s_waitcnt vmcnt(5)\n\t"
      "v_max3_u32 v234, v140, v141, v142\n\t"
      "v_max_u32_e32 v234, v234, v143\n\t"
      "v_cmp_eq_u32_e32 vcc, -1, v234\n\t"
      "s_cbranch_vccnz FP10L_%=\n\t"
      "FP10X_%=:\n\t"
      "s_branch FP10B_%=\n\t"
      "FP11R_%=:\n\t"
      "s_mov_b32 s51, 0\n\t"
      "FP11L_%=:\n\t"
      "s_add_u32 s51, s51, 1\n\t"
      "s_cmp_lt_u32 s51, 262144\n\t"
      "s_cbranch_scc0 FP11X_%=\n\t"
      "s_waitcnt vmcnt(0)\n\t"
      "s_sleep 4\n\t"
      "global_load_dwordx4 v[144:147], v228, s[54:55] sc1\n\t"
      "global_load_dwordx4 v[148:151], v229, s[54:55] sc1\n\t"
      "global_load_dwordx4 v[152:155], v230, s[54:55] sc1\n\t"
      "global_load_dwordx4 v[128:131], v224, s[56:57] sc1\n\t"
      "global_load_dwordx4 v[132:135], v225, s[56:57] sc1\n\t"
      "global_load_dwordx4 v[136:139], v226, s[56:57] sc1\n\t"
      "s_waitcnt vmcnt(5)\n\t"
      "v_max3_u32 v234, v144, v145, v146\n\t"
      "v_max_u32_e32 v234, v234, v147\n\t"
      "v_cmp_eq_u32_e32 vcc, -1, v234\n\t"
      "s_cbranch_vccnz FP11L_%=\n\t"
      "FP11X_%=:\n\t"
      "s_branch FP11B_%=\n\t"
      "FP12R_%=:\n\t"
      "s_mov_b32 s51, 0\n\t"
      "FP12L_%=:\n\t"
      "s_add_u32 s51, s51, 1\n\t"
      "s_cmp_lt_u32 s51, 262144\n\t"
      "s_cbranch_scc0 FP12X_%=\n\t"
      "s_waitcnt vmcnt(0)\n\t"
      "s_sleep 4\n\t"
      "global_load_dwordx4 v[148:151], v229, s[54:55] sc1\n\t"
      "global_load_dwordx4 v[152:155], v230, s[54:55] sc1\n\t"
      "global_load_dwordx4 v[128:131], v224, s[56:57] sc1\n\t"
      "global_load_dwordx4 v[132:135], v225, s[56:57] sc1\n\t"
      "global_load_dwordx4 v[136:139], v226, s[56:57] sc1\n\t"
      "global_load_dwordx4 v[140:143], v227, s[56:57] sc1\n\t"
      "s_waitcnt vmcnt(5)\n\t"
      "v_max3_u32 v234, v148, v149, v150\n\t"
      "v_max_u32_e32 v234, v234, v151\n\t"
      "v_cmp_eq_u32_e32 vcc, -1, v234\n\t"
      "s_cbranch_vccnz FP12L_%=\n\t"
      "FP12X_%=:\n\t"
      "s_branch FP12B_%=\n\t"
      "FP13R_%=:\n\t"
      "s_mov_b32 s51, 0\n\t"
      "FP13L_%=:\n\t"
      "s_add_u32 s51, s51, 1\n\t"
      "s_cmp_lt_u32 s51, 262144\n\t"
      "s_cbranch_scc0 FP13X_%=\n\t"
      "s_waitcnt vmcnt(0)\n\t"
      "s_sleep 4\n\t"
      "global_load_dwordx4 v[152:155], v230, s[54:55] sc1\n\t"
      "global_load_dwordx4 v[128:131], v224, s[56:57] sc1\n\t"
      "global_load_dwordx4 v[132:135], v225, s[56:57] sc1\n\t"
      "global_load_dwordx4 v[136:139], v226, s[56:57] sc1\n\t"
      "global_load_dwordx4 v[140:143], v227, s[56:57] sc1\n\t"
      "global_load_dwordx4 v[144:147], v228, s[56:57] sc1\n\t"
      "s_waitcnt vmcnt(5)\n\t"
      "v_max3_u32 v234, v152, v153, v154\n\t"
      "v_max_u32_e32 v234, v234, v155\n\t"
      "v_cmp_eq_u32_e32 vcc, -1, v234\n\t"
      "s_cbranch_vccnz FP13L_%=\n\t"
      "FP13X_%=:\n\t"
      "s_waitcnt vmcnt(0)\n\t"
      "s_branch FP13B_%=\n\t"
      "FP14R_%=:\n\t"
      "s_mov_b32 s51, 0\n\t"
      "FP14L_%=:\n\t"
      "s_add_u32 s51, s51, 1\n\t"
      "s_cmp_lt_u32 s51, 262144\n\t"
      "s_cbranch_scc0 FP14X_%=\n\t"
      "s_waitcnt vmcnt(0)\n\t"
      "s_sleep 4\n\t"
      "global_load_dwordx4 v[128:131], v224, s[56:57] sc1\n\t"
      "global_load_dwordx4 v[132:135], v225, s[56:57] sc1\n\t"
      "global_load_dwordx4 v[136:139], v226, s[56:57] sc1\n\t"
      "global_load_dwordx4 v[140:143], v227, s[56:57] sc1\n\t"
      "global_load_dwordx4 v[144:147], v228, s[56:57] sc1\n\t"
      "global_load_dwordx4 v[148:151], v229, s[56:57] sc1\n\t"
      "s_waitcnt vmcnt(5)\n\t"
      "v_max3_u32 v234, v128, v129, v130\n\t"
      "v_max_u32_e32 v234, v234, v131\n\t"
      "v_cmp_eq_u32_e32 vcc, -1, v234\n\t"
      "s_cbranch_vccnz FP14L_%=\n\t"
      "FP14X_%=:\n\t"
      "s_waitcnt vmcnt(0)\n\t"
      "s_branch FP14B_%=\n\t"
      "NP1R_%=:\n\t"
      "s_mov_b32 s51, 0\n\t"
      "NP1L_%=:\n\t"
      "s_add_u32 s51, s51, 1\n\t"
      "s_cmp_lt_u32 s51, 262144\n\t"
      "s_cbranch_scc0 NP1X_%=\n\t"
      "s_waitcnt vmcnt(0)\n\t"
      "s_sleep 1\n\t"
      "global_load_dwordx4 v[128:131], v224, s[54:55] sc1\n\t"
      "global_load_dwordx4 v[132:135], v225, s[54:55] sc1\n\t"
      "global_load_dwordx4 v[136:139], v226, s[54:55] sc1\n\t"
      "global_load_dwordx4 v[140:143], v227, s[54:55] sc1\n\t"
      "global_load_dwordx4 v[144:147], v228, s[54:55] sc1\n\t"
      "global_load_dwordx4 v[148:151], v229, s[54:55] sc1\n\t"
      "global_load_dwordx4 v[152:155], v230, s[54:55] sc1\n\t"
      "s_waitcnt vmcnt(0)\n\t"
      "v_max3_u32 v234, v128, v129, v130\n\t"
      "v_max3_u32 v234, v234, v131, v132\n\t"
      "v_max3_u32 v234, v234, v133, v134\n\t"
      "v_max3_u32 v234, v234, v135, v136\n\t"
      "v_max3_u32 v234, v234, v137, v138\n\t"
      "v_max3_u32 v234, v234, v139, v140\n\t"
      "v_max3_u32 v234, v234, v141, v142\n\t"
      "v_max3_u32 v234, v234, v143, v144\n\t"
      "v_max3_u32 v234, v234, v145, v146\n\t"
      "v_max3_u32 v234, v234, v147, v148\n\t"
      "v_max3_u32 v234, v234, v149, v150\n\t"
      "v_max3_u32 v234, v234, v151, v152\n\t"
      "v_max3_u32 v234, v234, v153, v154\n\t"
      "v_max_u32_e32 v234, v234, v155\n\t"
      "v_cmp_eq_u32_e32 vcc, -1, v234\n\t"
      "s_cbranch_vccnz NP1L_%=\n\t"
      "NP1X_%=:\n\t"
      "v_mfma_f32_16x16x32_bf16 v[200:203], v[128:131], v[0:3], v[160:163]\n\t"
      "v_mfma_f32_16x16x32_bf16 v[204:207], v[128:131], v[4:7], 0\n\t"
      "v_mfma_f32_16x16x32_bf16 v[208:211], v[128:131], v[8:11], 0\n\t"
      "v_mfma_f32_16x16x32_bf16 v[212:215], v[128:131], v[12:15], 0\n\t"
      "v_mfma_f32_16x16x32_bf16 v[200:203], v[132:135], v[16:19], v[200:203]\n\t"
      "v_mfma_f32_16x16x32_bf16 v[204:207], v[132:135], v[20:23], v[204:207]\n\t"
      "v_mfma_f32_16x16x32_bf16 v[208:211], v[132:135], v[24:27], v[208:211]\n\t"
      "v_mfma_f32_16x16x32_bf16 v[212:215], v[132:135], v[28:31], v[212:215]\n\t"
      "v_mfma_f32_16x16x32_bf16 v[200:203], v[136:139], v[32:35], v[200:203]\n\t"
      "v_mfma_f32_16x16x32_bf16 v[204:207], v[136:139], v[36:39], v[204:207]\n\t"
      "v_mfma_f32_16x16x32_bf16 v[208:211], v[136:139], v[40:43], v[208:211]\n\t"
      "v_mfma_f32_16x16x32_bf16 v[212:215], v[136:139], v[44:47], v[212:215]\n\t"
      "v_mfma_f32_16x16x32_bf16 v[200:203], v[140:143], v[48:51], v[200:203]\n\t"
      "v_mfma_f32_16x16x32_bf16 v[204:207], v[140:143], v[52:55], v[204:207]\n\t"
      "v_mfma_f32_16x16x32_bf16 v[208:211], v[140:143], v[56:59], v[208:211]\n\t"
      "v_mfma_f32_16x16x32_bf16 v[212:215], v[140:143], v[60:63], v[212:215]\n\t"
      "v_mfma_f32_16x16x32_bf16 v[200:203], v[144:147], v[64:67], v[200:203]\n\t"
      "v_mfma_f32_16x16x32_bf16 v[204:207], v[144:147], v[68:71], v[204:207]\n\t"
      "v_mfma_f32_16x16x32_bf16 v[208:211], v[144:147], v[72:75], v[208:211]\n\t"
      "v_mfma_f32_16x16x32_bf16 v[212:215], v[144:147], v[76:79], v[212:215]\n\t"
      "v_mfma_f32_16x16x32_bf16 v[200:203], v[148:151], v[80:83], v[200:203]\n\t"
      "v_mfma_f32_16x16x32_bf16 v[204:207], v[148:151], v[84:87], v[204:207]\n\t"
      "v_mfma_f32_16x16x32_bf16 v[208:211], v[148:151], v[88:91], v[208:211]\n\t"
      "v_mfma_f32_16x16x32_bf16 v[212:215], v[148:151], v[92:95], v[212:215]\n\t"
      "v_mfma_f32_16x16x32_bf16 v[200:203], v[152:155], v[96:99], v[200:203]\n\t"
      "v_mfma_f32_16x16x32_bf16 v[204:207], v[152:155], v[100:103], v[204:207]\n\t"
      "v_mfma_f32_16x16x32_bf16 v[208:211], v[152:155], v[104:107], v[208:211]\n\t"
      "v_mfma_f32_16x16x32_bf16 v[212:215], v[152:155], v[108:111], v[212:215]\n\t"
      "global_load_dwordx4 v[128:131], v224, s[56:57] sc1\n\t"
      "global_load_dwordx4 v[132:135], v225, s[56:57] sc1\n\t"
      "global_load_dwordx4 v[136:139], v226, s[56:57] sc1\n\t"
      "global_load_dwordx4 v[140:143], v227, s[56:57] sc1\n\t"
      "global_load_dwordx4 v[144:147], v228, s[56:57] sc1\n\t"
      "global_load_dwordx4 v[148:151], v229, s[56:57] sc1\n\t"
      "global_load_dwordx4 v[152:155], v230, s[56:57] sc1\n\t"
      "s_waitcnt vmcnt(0)\n\t"
      "s_nop 7\n\t"
      "s_branch NP1B_%=\n\t"
      "FP15R_%=:\n\t"
      "s_mov_b32 s51, 0\n\t"
      "FP15L_%=:\n\t"
      "s_add_u32 s51, s51, 1\n\t"
      "s_cmp_lt_u32 s51, 262144\n\t"
      "s_cbranch_scc0 FP15X_%=\n\t"
      "s_waitcnt vmcnt(0)\n\t"
      "s_sleep 4\n\t"
      "global_load_dwordx4 v[132:135], v225, s[56:57] sc1\n\t"
      "global_load_dwordx4 v[136:139], v226, s[56:57] sc1\n\t"
      "global_load_dwordx4 v[140:143], v227, s[56:57] sc1\n\t"
      "global_load_dwordx4 v[144:147], v228, s[56:57] sc1\n\t"
      "global_load_dwordx4 v[148:151], v229, s[56:57] sc1\n\t"
      "global_load_dwordx4 v[152:155], v230, s[56:57] sc1\n\t"
      "s_waitcnt vmcnt(5)\n\t"
      "v_max3_u32 v234, v132, v133, v134\n\t"
      "v_max_u32_e32 v234, v234, v135\n\t"
      "v_cmp_eq_u32_e32 vcc, -1, v234\n\t"
      "s_cbranch_vccnz FP15L_%=\n\t"
      "FP15X_%=:\n\t"
      "s_waitcnt vmcnt(0)\n\t"
      "s_branch FP15B_%=\n\t"
      "FP16R_%=:\n\t"
      "s_mov_b32 s51, 0\n\t"
      "FP16L_%=:\n\t"
      "s_add_u32 s51, s51, 1\n\t"
      "s_cmp_lt_u32 s51, 262144\n\t"
      "s_cbranch_scc0 FP16X_%=\n\t"
      "s_waitcnt vmcnt(0)\n\t"
      "s_sleep 4\n\t"
      "global_load_dwordx4 v[136:139], v226, s[56:57] sc1\n\t"
      "global_load_dwordx4 v[140:143], v227, s[56:57] sc1\n\t"
      "global_load_dwordx4 v[144:147], v228, s[56:57] sc1\n\t"
      "global_load_dwordx4 v[148:151], v229, s[56:57] sc1\n\t"
      "global_load_dwordx4 v[152:155], v230, s[56:57] sc1\n\t"
      "global_load_dwordx4 v[128:131], v224, s[58:59] sc1\n\t"
      "s_waitcnt vmcnt(5)\n\t"
      "v_max3_u32 v234, v136, v137, v138\n\t"
      "v_max_u32_e32 v234, v234, v139\n\t"
      "v_cmp_eq_u32_e32 vcc, -1, v234\n\t"
      "s_cbranch_vccnz FP16L_%=\n\t"
      "FP16X_%=:\n\t"
      "s_waitcnt vmcnt(0)\n\t"
      "s_branch FP16B_%=\n\t"
      "FP17R_%=:\n\t"
      "s_mov_b32 s51, 0\n\t"
      "FP17L_%=:\n\t"
      "s_add_u32 s51, s51, 1\n\t"
      "s_cmp_lt_u32 s51, 262144\n\t"
      "s_cbranch_scc0 FP17X_%=\n\t"
      "s_waitcnt vmcnt(0)\n\t"
      "s_sleep 4\n\t"
      "global_load_dwordx4 v[140:143], v227, s[56:57] sc1\n\t"
      "global_load_dwordx4 v[144:147], v228, s[56:57] sc1\n\t"
      "global_load_dwordx4 v[148:151], v229, s[56:57] sc1\n\t"
      "global_load_dwordx4 v[152:155], v230, s[56:57] sc1\n\t"
      "global_load_dwordx4 v[128:131], v224, s[58:59] sc1\n\t"
      "global_load_dwordx4 v[132:135], v225, s[58:59] sc1\n\t"
      "s_waitcnt vmcnt(5)\n\t"
      "v_max3_u32 v234, v140, v141, v142\n\t"
      "v_max_u32_e32 v234, v234, v143\n\t"
      "v_cmp_eq_u32_e32 vcc, -1, v234\n\t"
      "s_cbranch_vccnz FP17L_%=\n\t"
      "FP17X_%=:\n\t"
      "s_waitcnt vmcnt(0)\n\t"
      "s_branch FP17B_%=\n\t"
      "FP18R_%=:\n\t"
      "s_mov_b32 s51, 0\n\t"
      "FP18L_%=:\n\t"
      "s_add_u32 s51, s51, 1\n\t"
      "s_cmp_lt_u32 s51, 262144\n\t"
      "s_cbranch_scc0 FP18X_%=\n\t"
      "s_waitcnt vmcnt(0)\n\t"
      "s_sleep 4\n\t"
      "global_load_dwordx4 v[144:147], v228, s[56:57] sc1\n\t"
      "global_load_dwordx4 v[148:151], v229, s[56:57] sc1\n\t"
      "global_load_dwordx4 v[152:155], v230, s[56:57] sc1\n\t"
      "global_load_dwordx4 v[128:131], v224, s[58:59] sc1\n\t"
      "global_load_dwordx4 v[132:135], v225, s[58:59] sc1\n\t"
      "global_load_dwordx4 v[136:139], v226, s[58:59] sc1\n\t"
      "s_waitcnt vmcnt(5)\n\t"
      "v_max3_u32 v234, v144, v145, v146\n\t"
      "v_max_u32_e32 v234, v234, v147\n\t"
      "v_cmp_eq_u32_e32 vcc, -1, v234\n\t"
      "s_cbranch_vccnz FP18L_%=\n\t"
      "FP18X_%=:\n\t"
      "s_waitcnt vmcnt(0)\n\t"
      "s_branch FP18B_%=\n\t"
      "FP19R_%=:\n\t"
      "s_mov_b32 s51, 0\n\t"
      "FP19L_%=:\n\t"
      "s_add_u32 s51, s51, 1\n\t"
      "s_cmp_lt_u32 s51, 262144\n\t"
      "s_cbranch_scc0 FP19X_%=\n\t"
      "s_waitcnt vmcnt(0)\n\t"
      "s_sleep 4\n\t"
      "global_load_dwordx4 v[148:151], v229, s[56:57] sc1\n\t"
      "global_load_dwordx4 v[152:155], v230, s[56:57] sc1\n\t"
      "global_load_dwordx4 v[128:131], v224, s[58:59] sc1\n\t"
      "global_load_dwordx4 v[132:135], v225, s[58:59] sc1\n\t"
      "global_load_dwordx4 v[136:139], v226, s[58:59] sc1\n\t"
      "global_load_dwordx4 v[140:143], v227, s[58:59] sc1\n\t"
      "s_waitcnt vmcnt(5)\n\t"
      "v_max3_u32 v234, v148, v149, v150\n\t"
      "v_max_u32_e32 v234, v234, v151\n\t"
      "v_cmp_eq_u32_e32 vcc, -1, v234\n\t"
      "s_cbranch_vccnz FP19L_%=\n\t"
      "FP19X_%=:\n\t"
      "s_branch FP19B_%=\n\t"
      "FP20R_%=:\n\t"
      "s_mov_b32 s51, 0\n\t"
      "FP20L_%=:\n\t"
      "s_add_u32 s51, s51, 1\n\t"
      "s_cmp_lt_u32 s51, 262144\n\t"
      "s_cbranch_scc0 FP20X_%=\n\t"
      "s_waitcnt vmcnt(0)\n\t"
      "s_sleep 4\n\t"
      "global_load_dwordx4 v[152:155], v230, s[56:57] sc1\n\t"
      "global_load_dwordx4 v[128:131], v224, s[58:59] sc1\n\t"
      "global_load_dwordx4 v[132:135], v225, s[58:59] sc1\n\t"
      "global_load_dwordx4 v[136:139], v226, s[58:59] sc1\n\t"
      "global_load_dwordx4 v[140:143], v227, s[58:59] sc1\n\t"
      "global_load_dwordx4 v[144:147], v228, s[58:59] sc1\n\t"
      "s_waitcnt vmcnt(5)\n\t"
      "v_max3_u32 v234, v152, v153, v154\n\t"
      "v_max_u32_e32 v234, v234, v155\n\t"
      "v_cmp_eq_u32_e32 vcc, -1, v234\n\t"
      "s_cbranch_vccnz FP20L_%=\n\t"
      "FP20X_%=:\n\t"
      "s_waitcnt vmcnt(0)\n\t"
      "s_branch FP20B_%=\n\t"
      "FP21R_%=:\n\t"
      "s_mov_b32 s51, 0\n\t"
      "FP21L_%=:\n\t"
      "s_add_u32 s51, s51, 1\n\t"
      "s_cmp_lt_u32 s51, 262144\n\t"
      "s_cbranch_scc0 FP21X_%=\n\t"
      "s_waitcnt vmcnt(0)\n\t"
      "s_sleep 4\n\t"
      "global_load_dwordx4 v[128:131], v224, s[58:59] sc1\n\t"
      "global_load_dwordx4 v[132:135], v225, s[58:59] sc1\n\t"
      "global_load_dwordx4 v[136:139], v226, s[58:59] sc1\n\t"
      "global_load_dwordx4 v[140:143], v227, s[58:59] sc1\n\t"
      "global_load_dwordx4 v[144:147], v228, s[58:59] sc1\n\t"
      "global_load_dwordx4 v[148:151], v229, s[58:59] sc1\n\t"
      "s_waitcnt vmcnt(5)\n\t"
      "v_max3_u32 v234, v128, v129, v130\n\t"
      "v_max_u32_e32 v234, v234, v131\n\t"
      "v_cmp_eq_u32_e32 vcc, -1, v234\n\t"
      "s_cbranch_vccnz FP21L_%=\n\t"
      "FP21X_%=:\n\t"
      "s_waitcnt vmcnt(0)\n\t"
      "s_branch FP21B_%=\n\t"
      "NP2R_%=:\n\t"
      "s_mov_b32 s51, 0\n\t"
      "NP2L_%=:\n\t"
      "s_add_u32 s51, s51, 1\n\t"
      "s_cmp_lt_u32 s51, 262144\n\t"
      "s_cbranch_scc0 NP2X_%=\n\t"
      "s_waitcnt vmcnt(0)\n\t"
      "s_sleep 1\n\t"
      "global_load_dwordx4 v[128:131], v224, s[56:57] sc1\n\t"
      "global_load_dwordx4 v[132:135], v225, s[56:57] sc1\n\t"
      "global_load_dwordx4 v[136:139], v226, s[56:57] sc1\n\t"
      "global_load_dwordx4 v[140:143], v227, s[56:57] sc1\n\t"
      "global_load_dwordx4 v[144:147], v228, s[56:57] sc1\n\t"
      "global_load_dwordx4 v[148:151], v229, s[56:57] sc1\n\t"
      "global_load_dwordx4 v[152:155], v230, s[56:57] sc1\n\t"
      "s_waitcnt vmcnt(0)\n\t"
      "v_max3_u32 v234, v128, v129, v130\n\t"
      "v_max3_u32 v234, v234, v131, v132\n\t"
      "v_max3_u32 v234, v234, v133, v134\n\t"
      "v_max3_u32 v234, v234, v135, v136\n\t"
      "v_max3_u32 v234, v234, v137, v138\n\t"
      "v_max3_u32 v234, v234, v139, v140\n\t"
      "v_max3_u32 v234, v234, v141, v142\n\t"
      "v_max3_u32 v234, v234, v143, v144\n\t"
      "v_max3_u32 v234, v234, v145, v146\n\t"
      "v_max3_u32 v234, v234, v147, v148\n\t"
      "v_max3_u32 v234, v234, v149, v150\n\t"
      "v_max3_u32 v234, v234, v151, v152\n\t"
      "v_max3_u32 v234, v234, v153, v154\n\t"
      "v_max_u32_e32 v234, v234, v155\n\t"
      "v_cmp_eq_u32_e32 vcc, -1, v234\n\t"
      "s_cbranch_vccnz NP2L_%=\n\t"
      "NP2X_%=:\n\t"
      "v_mfma_f32_16x16x32_bf16 v[184:187], v[128:131], v[0:3], v[164:167]\n\t"
      "v_mfma_f32_16x16x32_bf16 v[188:191], v[128:131], v[4:7], 0\n\t"
      "v_mfma_f32_16x16x32_bf16 v[192:195], v[128:131], v[8:11], 0\n\t"
      "v_mfma_f32_16x16x32_bf16 v[196:199], v[128:131], v[12:15], 0\n\t"
      "v_mfma_f32_16x16x32_bf16 v[184:187], v[132:135], v[16:19], v[184:187]\n\t"
      "v_mfma_f32_16x16x32_bf16 v[188:191], v[132:135], v[20:23], v[188:191]\n\t"
      "v_mfma_f32_16x16x32_bf16 v[192:195], v[132:135], v[24:27], v[192:195]\n\t"
      "v_mfma_f32_16x16x32_bf16 v[196:199], v[132:135], v[28:31], v[196:199]\n\t"
      "v_mfma_f32_16x16x32_bf16 v[184:187], v[136:139], v[32:35], v[184:187]\n\t"
      "v_mfma_f32_16x16x32_bf16 v[188:191], v[136:139], v[36:39], v[188:191]\n\t"
      "v_mfma_f32_16x16x32_bf16 v[192:195], v[136:139], v[40:43], v[192:195]\n\t"
      "v_mfma_f32_16x16x32_bf16 v[196:199], v[136:139], v[44:47], v[196:199]\n\t"
      "v_mfma_f32_16x16x32_bf16 v[184:187], v[140:143], v[48:51], v[184:187]\n\t"
      "v_mfma_f32_16x16x32_bf16 v[188:191], v[140:143], v[52:55], v[188:191]\n\t"
      "v_mfma_f32_16x16x32_bf16 v[192:195], v[140:143], v[56:59], v[192:195]\n\t"
      "v_mfma_f32_16x16x32_bf16 v[196:199], v[140:143], v[60:63], v[196:199]\n\t"
      "v_mfma_f32_16x16x32_bf16 v[184:187], v[144:147], v[64:67], v[184:187]\n\t"
      "v_mfma_f32_16x16x32_bf16 v[188:191], v[144:147], v[68:71], v[188:191]\n\t"
      "v_mfma_f32_16x16x32_bf16 v[192:195], v[144:147], v[72:75], v[192:195]\n\t"
      "v_mfma_f32_16x16x32_bf16 v[196:199], v[144:147], v[76:79], v[196:199]\n\t"
      "v_mfma_f32_16x16x32_bf16 v[184:187], v[148:151], v[80:83], v[184:187]\n\t"
      "v_mfma_f32_16x16x32_bf16 v[188:191], v[148:151], v[84:87], v[188:191]\n\t"
      "v_mfma_f32_16x16x32_bf16 v[192:195], v[148:151], v[88:91], v[192:195]\n\t"
      "v_mfma_f32_16x16x32_bf16 v[196:199], v[148:151], v[92:95], v[196:199]\n\t"
      "v_mfma_f32_16x16x32_bf16 v[184:187], v[152:155], v[96:99], v[184:187]\n\t"
      "v_mfma_f32_16x16x32_bf16 v[188:191], v[152:155], v[100:103], v[188:191]\n\t"
      "v_mfma_f32_16x16x32_bf16 v[192:195], v[152:155], v[104:107], v[192:195]\n\t"
      "v_mfma_f32_16x16x32_bf16 v[196:199], v[152:155], v[108:111], v[196:199]\n\t"
      "global_load_dwordx4 v[128:131], v224, s[58:59] sc1\n\t"
      "global_load_dwordx4 v[132:135], v225, s[58:59] sc1\n\t"
      "global_load_dwordx4 v[136:139], v226, s[58:59] sc1\n\t"
      "global_load_dwordx4 v[140:143], v227, s[58:59] sc1\n\t"
      "global_load_dwordx4 v[144:147], v228, s[58:59] sc1\n\t"
      "global_load_dwordx4 v[148:151], v229, s[58:59] sc1\n\t"
      "global_load_dwordx4 v[152:155], v230, s[58:59] sc1\n\t"
      "s_waitcnt vmcnt(0)\n\t"
      "s_nop 7\n\t"
      "s_branch NP2B_%=\n\t"
      "FP22R_%=:\n\t"
      "s_mov_b32 s51, 0\n\t"
      "FP22L_%=:\n\t"
      "s_add_u32 s51, s51, 1\n\t"
      "s_cmp_lt_u32 s51, 262144\n\t"
      "s_cbranch_scc0 FP22X_%=\n\t"
      "s_waitcnt vmcnt(0)\n\t"
      "s_sleep 4\n\t"
      "global_load_dwordx4 v[132:135], v225, s[58:59] sc1\n\t"
      "global_load_dwordx4 v[136:139], v226, s[58:59] sc1\n\t"
      "global_load_dwordx4 v[140:143], v227, s[58:59] sc1\n\t"
      "global_load_dwordx4 v[144:147], v228, s[58:59] sc1\n\t"
      "global_load_dwordx4 v[148:151], v229, s[58:59] sc1\n\t"
      "global_load_dwordx4 v[152:155], v230, s[58:59] sc1\n\t"
      "s_waitcnt vmcnt(5)\n\t"
      "v_max3_u32 v234, v132, v133, v134\n\t"
      "v_max_u32_e32 v234, v234, v135\n\t"
      "v_cmp_eq_u32_e32 vcc, -1, v234\n\t"
      "s_cbranch_vccnz FP22L_%=\n\t"
      "FP22X_%=:\n\t"
      "s_waitcnt vmcnt(0)\n\t"
      "s_branch FP22B_%=\n\t"
      "FP23R_%=:\n\t"
      "s_mov_b32 s51, 0\n\t"
      "FP23L_%=:\n\t"
      "s_add_u32 s51, s51, 1\n\t"
      "s_cmp_lt_u32 s51, 262144\n\t"
      "s_cbranch_scc0 FP23X_%=\n\t"
      "s_waitcnt vmcnt(0)\n\t"
      "s_sleep 4\n\t"
      "global_load_dwordx4 v[136:139], v226, s[58:59] sc1\n\t"
      "global_load_dwordx4 v[140:143], v227, s[58:59] sc1\n\t"
      "global_load_dwordx4 v[144:147], v228, s[58:59] sc1\n\t"
      "global_load_dwordx4 v[148:151], v229, s[58:59] sc1\n\t"
      "global_load_dwordx4 v[152:155], v230, s[58:59] sc1\n\t"
      "global_load_dwordx4 v[128:131], v224, s[60:61] sc1\n\t"
      "s_waitcnt vmcnt(5)\n\t"
      "v_max3_u32 v234, v136, v137, v138\n\t"
      "v_max_u32_e32 v234, v234, v139\n\t"
      "v_cmp_eq_u32_e32 vcc, -1, v234\n\t"
      "s_cbranch_vccnz FP23L_%=\n\t"
      "FP23X_%=:\n\t"
      "s_waitcnt vmcnt(0)\n\t"
      "s_branch FP23B_%=\n\t"
      "FP24R_%=:\n\t"
      "s_mov_b32 s51, 0\n\t"
      "FP24L_%=:\n\t"
      "s_add_u32 s51, s51, 1\n\t"
      "s_cmp_lt_u32 s51, 262144\n\t"
      "s_cbranch_scc0 FP24X_%=\n\t"
      "s_waitcnt vmcnt(0)\n\t"
      "s_sleep 4\n\t"
      "global_load_dwordx4 v[140:143], v227, s[58:59] sc1\n\t"
      "global_load_dwordx4 v[144:147], v228, s[58:59] sc1\n\t"
      "global_load_dwordx4 v[148:151], v229, s[58:59] sc1\n\t"
      "global_load_dwordx4 v[152:155], v230, s[58:59] sc1\n\t"
      "global_load_dwordx4 v[128:131], v224, s[60:61] sc1\n\t"
      "global_load_dwordx4 v[132:135], v225, s[60:61] sc1\n\t"
      "s_waitcnt vmcnt(5)\n\t"
      "v_max3_u32 v234, v140, v141, v142\n\t"
      "v_max_u32_e32 v234, v234, v143\n\t"
      "v_cmp_eq_u32_e32 vcc, -1, v234\n\t"
      "s_cbranch_vccnz FP24L_%=\n\t"
      "FP24X_%=:\n\t"
      "s_waitcnt vmcnt(0)\n\t"
      "s_branch FP24B_%=\n\t"
      "FP25R_%=:\n\t"
      "s_mov_b32 s51, 0\n\t"
      "FP25L_%=:\n\t"
      "s_add_u32 s51, s51, 1\n\t"
      "s_cmp_lt_u32 s51, 262144\n\t"
      "s_cbranch_scc0 FP25X_%=\n\t"
      "s_waitcnt vmcnt(0)\n\t"
      "s_sleep 4\n\t"
      "global_load_dwordx4 v[144:147], v228, s[58:59] sc1\n\t"
      "global_load_dwordx4 v[148:151], v229, s[58:59] sc1\n\t"
      "global_load_dwordx4 v[152:155], v230, s[58:59] sc1\n\t"
      "global_load_dwordx4 v[128:131], v224, s[60:61] sc1\n\t"
      "global_load_dwordx4 v[132:135], v225, s[60:61] sc1\n\t"
      "global_load_dwordx4 v[136:139], v226, s[60:61] sc1\n\t"
      "s_waitcnt vmcnt(5)\n\t"
      "v_max3_u32 v234, v144, v145, v146\n\t"
      "v_max_u32_e32 v234, v234, v147\n\t"
      "v_cmp_eq_u32_e32 vcc, -1, v234\n\t"
      "s_cbranch_vccnz FP25L_%=\n\t"
      "FP25X_%=:\n\t"
      "s_waitcnt vmcnt(0)\n\t"
      "s_branch FP25B_%=\n\t"
      "FP26R_%=:\n\t"
      "s_mov_b32 s51, 0\n\t"
      "FP26L_%=:\n\t"
      "s_add_u32 s51, s51, 1\n\t"
      "s_cmp_lt_u32 s51, 262144\n\t"
      "s_cbranch_scc0 FP26X_%=\n\t"
      "s_waitcnt vmcnt(0)\n\t"
      "s_sleep 4\n\t"
      "global_load_dwordx4 v[148:151], v229, s[58:59] sc1\n\t"
      "global_load_dwordx4 v[152:155], v230, s[58:59] sc1\n\t"
      "global_load_dwordx4 v[128:131], v224, s[60:61] sc1\n\t"
      "global_load_dwordx4 v[132:135], v225, s[60:61] sc1\n\t"
      "global_load_dwordx4 v[136:139], v226, s[60:61] sc1\n\t"
      "global_load_dwordx4 v[140:143], v227, s[60:61] sc1\n\t"
      "s_waitcnt vmcnt(5)\n\t"
      "v_max3_u32 v234, v148, v149, v150\n\t"
      "v_max_u32_e32 v234, v234, v151\n\t"
      "v_cmp_eq_u32_e32 vcc, -1, v234\n\t"
      "s_cbranch_vccnz FP26L_%=\n\t"
      "FP26X_%=:\n\t"
      "s_branch FP26B_%=\n\t"
      "FP27R_%=:\n\t"
      "s_mov_b32 s51, 0\n\t"
      "FP27L_%=:\n\t"
      "s_add_u32 s51, s51, 1\n\t"
      "s_cmp_lt_u32 s51, 262144\n\t"
      "s_cbranch_scc0 FP27X_%=\n\t"
      "s_waitcnt vmcnt(0)\n\t"
      "s_sleep 4\n\t"
      "global_load_dwordx4 v[152:155], v230, s[58:59] sc1\n\t"
      "global_load_dwordx4 v[128:131], v224, s[60:61] sc1\n\t"
      "global_load_dwordx4 v[132:135], v225, s[60:61] sc1\n\t"
      "global_load_dwordx4 v[136:139], v226, s[60:61] sc1\n\t"
      "global_load_dwordx4 v[140:143], v227, s[60:61] sc1\n\t"
      "global_load_dwordx4 v[144:147], v228, s[60:61] sc1\n\t"
      "s_waitcnt vmcnt(5)\n\t"
      "v_max3_u32 v234, v152, v153, v154\n\t"
      "v_max_u32_e32 v234, v234, v155\n\t"
      "v_cmp_eq_u32_e32 vcc, -1, v234\n\t"
      "s_cbranch_vccnz FP27L_%=\n\t"
      "FP27X_%=:\n\t"
      "s_waitcnt vmcnt(0)\n\t"
      "s_branch FP27B_%=\n\t"
      "FP28R_%=:\n\t"
      "s_mov_b32 s51, 0\n\t"
      "FP28L_%=:\n\t"
      "s_add_u32 s51, s51, 1\n\t"
      "s_cmp_lt_u32 s51, 262144\n\t"
      "s_cbranch_scc0 FP28X_%=\n\t"
      "s_waitcnt vmcnt(0)\n\t"
      "s_sleep 4\n\t"
      "global_load_dwordx4 v[128:131], v224, s[60:61] sc1\n\t"
      "global_load_dwordx4 v[132:135], v225, s[60:61] sc1\n\t"
      "global_load_dwordx4 v[136:139], v226, s[60:61] sc1\n\t"
      "global_load_dwordx4 v[140:143], v227, s[60:61] sc1\n\t"
      "global_load_dwordx4 v[144:147], v228, s[60:61] sc1\n\t"
      "global_load_dwordx4 v[148:151], v229, s[60:61] sc1\n\t"
      "s_waitcnt vmcnt(5)\n\t"
      "v_max3_u32 v234, v128, v129, v130\n\t"
      "v_max_u32_e32 v234, v234, v131\n\t"
      "v_cmp_eq_u32_e32 vcc, -1, v234\n\t"
      "s_cbranch_vccnz FP28L_%=\n\t"
      "FP28X_%=:\n\t"
      "s_waitcnt vmcnt(0)\n\t"
      "s_branch FP28B_%=\n\t"
      "NP3R_%=:\n\t"
      "s_mov_b32 s51, 0\n\t"
      "NP3L_%=:\n\t"
      "s_add_u32 s51, s51, 1\n\t"
      "s_cmp_lt_u32 s51, 262144\n\t"
      "s_cbranch_scc0 NP3X_%=\n\t"
      "s_waitcnt vmcnt(0)\n\t"
      "s_sleep 1\n\t"
      "global_load_dwordx4 v[128:131], v224, s[58:59] sc1\n\t"
      "global_load_dwordx4 v[132:135], v225, s[58:59] sc1\n\t"
      "global_load_dwordx4 v[136:139], v226, s[58:59] sc1\n\t"
      "global_load_dwordx4 v[140:143], v227, s[58:59] sc1\n\t"
      "global_load_dwordx4 v[144:147], v228, s[58:59] sc1\n\t"
      "global_load_dwordx4 v[148:151], v229, s[58:59] sc1\n\t"
      "global_load_dwordx4 v[152:155], v230, s[58:59] sc1\n\t"
      "s_waitcnt vmcnt(0)\n\t"
      "v_max3_u32 v234, v128, v129, v130\n\t"
      "v_max3_u32 v234, v234, v131, v132\n\t"
      "v_max3_u32 v234, v234, v133, v134\n\t"
      "v_max3_u32 v234, v234, v135, v136\n\t"
      "v_max3_u32 v234, v234, v137, v138\n\t"
      "v_max3_u32 v234, v234, v139, v140\n\t"
      "v_max3_u32 v234, v234, v141, v142\n\t"
      "v_max3_u32 v234, v234, v143, v144\n\t"
      "v_max3_u32 v234, v234, v145, v146\n\t"
      "v_max3_u32 v234, v234, v147, v148\n\t"
      "v_max3_u32 v234, v234, v149, v150\n\t"
      "v_max3_u32 v234, v234, v151, v152\n\t"
      "v_max3_u32 v234, v234, v153, v154\n\t"
      "v_max_u32_e32 v234, v234, v155\n\t"
      "v_cmp_eq_u32_e32 vcc, -1, v234\n\t"
      "s_cbranch_vccnz NP3L_%=\n\t"
      "NP3X_%=:\n\t"
      "v_mfma_f32_16x16x32_bf16 v[200:203], v[128:131], v[0:3], v[168:171]\n\t"
      "v_mfma_f32_16x16x32_bf16 v[204:207], v[128:131], v[4:7], 0\n\t"
      "v_mfma_f32_16x16x32_bf16 v[208:211], v[128:131], v[8:11], 0\n\t"
      "v_mfma_f32_16x16x32_bf16 v[212:215], v[128:131], v[12:15], 0\n\t"
      "v_mfma_f32_16x16x32_bf16 v[200:203], v[132:135], v[16:19], v[200:203]\n\t"
      "v_mfma_f32_16x16x32_bf16 v[204:207], v[132:135], v[20:23], v[204:207]\n\t"
      "v_mfma_f32_16x16x32_bf16 v[208:211], v[132:135], v[24:27], v[208:211]\n\t"
      "v_mfma_f32_16x16x32_bf16 v[212:215], v[132:135], v[28:31], v[212:215]\n\t"
      "v_mfma_f32_16x16x32_bf16 v[200:203], v[136:139], v[32:35], v[200:203]\n\t"
      "v_mfma_f32_16x16x32_bf16 v[204:207], v[136:139], v[36:39], v[204:207]\n\t"
      "v_mfma_f32_16x16x32_bf16 v[208:211], v[136:139], v[40:43], v[208:211]\n\t"
      "v_mfma_f32_16x16x32_bf16 v[212:215], v[136:139], v[44:47], v[212:215]\n\t"
      "v_mfma_f32_16x16x32_bf16 v[200:203], v[140:143], v[48:51], v[200:203]\n\t"
      "v_mfma_f32_16x16x32_bf16 v[204:207], v[140:143], v[52:55], v[204:207]\n\t"
      "v_mfma_f32_16x16x32_bf16 v[208:211], v[140:143], v[56:59], v[208:211]\n\t"
      "v_mfma_f32_16x16x32_bf16 v[212:215], v[140:143], v[60:63], v[212:215]\n\t"
      "v_mfma_f32_16x16x32_bf16 v[200:203], v[144:147], v[64:67], v[200:203]\n\t"
      "v_mfma_f32_16x16x32_bf16 v[204:207], v[144:147], v[68:71], v[204:207]\n\t"
      "v_mfma_f32_16x16x32_bf16 v[208:211], v[144:147], v[72:75], v[208:211]\n\t"
      "v_mfma_f32_16x16x32_bf16 v[212:215], v[144:147], v[76:79], v[212:215]\n\t"
      "v_mfma_f32_16x16x32_bf16 v[200:203], v[148:151], v[80:83], v[200:203]\n\t"
      "v_mfma_f32_16x16x32_bf16 v[204:207], v[148:151], v[84:87], v[204:207]\n\t"
      "v_mfma_f32_16x16x32_bf16 v[208:211], v[148:151], v[88:91], v[208:211]\n\t"
      "v_mfma_f32_16x16x32_bf16 v[212:215], v[148:151], v[92:95], v[212:215]\n\t"
      "v_mfma_f32_16x16x32_bf16 v[200:203], v[152:155], v[96:99], v[200:203]\n\t"
      "v_mfma_f32_16x16x32_bf16 v[204:207], v[152:155], v[100:103], v[204:207]\n\t"
      "v_mfma_f32_16x16x32_bf16 v[208:211], v[152:155], v[104:107], v[208:211]\n\t"
      "v_mfma_f32_16x16x32_bf16 v[212:215], v[152:155], v[108:111], v[212:215]\n\t"
      "global_load_dwordx4 v[128:131], v224, s[60:61] sc1\n\t"
      "global_load_dwordx4 v[132:135], v225, s[60:61] sc1\n\t"
      "global_load_dwordx4 v[136:139], v226, s[60:61] sc1\n\t"
      "global_load_dwordx4 v[140:143], v227, s[60:61] sc1\n\t"
      "global_load_dwordx4 v[144:147], v228, s[60:61] sc1\n\t"
      "global_load_dwordx4 v[148:151], v229, s[60:61] sc1\n\t"
      "global_load_dwordx4 v[152:155], v230, s[60:61] sc1\n\t"
      "s_waitcnt vmcnt(0)\n\t"
      "s_nop 7\n\t"
      "s_branch NP3B_%=\n\t"
      "FP29R_%=:\n\t"
      "s_mov_b32 s51, 0\n\t"
      "FP29L_%=:\n\t"
      "s_add_u32 s51, s51, 1\n\t"
      "s_cmp_lt_u32 s51, 262144\n\t"
      "s_cbranch_scc0 FP29X_%=\n\t"
      "s_waitcnt vmcnt(0)\n\t"
      "s_sleep 4\n\t"
      "global_load_dwordx4 v[132:135], v225, s[60:61] sc1\n\t"
      "global_load_dwordx4 v[136:139], v226, s[60:61] sc1\n\t"
      "global_load_dwordx4 v[140:143], v227, s[60:61] sc1\n\t"
      "global_load_dwordx4 v[144:147], v228, s[60:61] sc1\n\t"
      "global_load_dwordx4 v[148:151], v229, s[60:61] sc1\n\t"
      "global_load_dwordx4 v[152:155], v230, s[60:61] sc1\n\t"
      "s_waitcnt vmcnt(5)\n\t"
      "v_max3_u32 v234, v132, v133, v134\n\t"
      "v_max_u32_e32 v234, v234, v135\n\t"
      "v_cmp_eq_u32_e32 vcc, -1, v234\n\t"
      "s_cbranch_vccnz FP29L_%=\n\t"
      "FP29X_%=:\n\t"
      "s_waitcnt vmcnt(0)\n\t"
      "s_branch FP29B_%=\n\t"
      "FP30R_%=:\n\t"
      "s_mov_b32 s51, 0\n\t"
      "FP30L_%=:\n\t"
      "s_add_u32 s51, s51, 1\n\t"
      "s_cmp_lt_u32 s51, 262144\n\t"
      "s_cbranch_scc0 FP30X_%=\n\t"
      "s_waitcnt vmcnt(0)\n\t"
      "s_sleep 4\n\t"
      "global_load_dwordx4 v[136:139], v226, s[60:61] sc1\n\t"
      "global_load_dwordx4 v[140:143], v227, s[60:61] sc1\n\t"
      "global_load_dwordx4 v[144:147], v228, s[60:61] sc1\n\t"
      "global_load_dwordx4 v[148:151], v229, s[60:61] sc1\n\t"
      "global_load_dwordx4 v[152:155], v230, s[60:61] sc1\n\t"
      "global_load_dwordx4 v[128:131], v224, s[62:63] sc1\n\t"
      "s_waitcnt vmcnt(5)\n\t"
      "v_max3_u32 v234, v136, v137, v138\n\t"
      "v_max_u32_e32 v234, v234, v139\n\t"
      "v_cmp_eq_u32_e32 vcc, -1, v234\n\t"
      "s_cbranch_vccnz FP30L_%=\n\t"
      "FP30X_%=:\n\t"
      "s_waitcnt vmcnt(0)\n\t"
      "s_branch FP30B_%=\n\t"
      "FP31R_%=:\n\t"
      "s_mov_b32 s51, 0\n\t"
      "FP31L_%=:\n\t"
      "s_add_u32 s51, s51, 1\n\t"
      "s_cmp_lt_u32 s51, 262144\n\t"
      "s_cbranch_scc0 FP31X_%=\n\t"
      "s_waitcnt vmcnt(0)\n\t"
      "s_sleep 4\n\t"
      "global_load_dwordx4 v[140:143], v227, s[60:61] sc1\n\t"
      "global_load_dwordx4 v[144:147], v228, s[60:61] sc1\n\t"
      "global_load_dwordx4 v[148:151], v229, s[60:61] sc1\n\t"
      "global_load_dwordx4 v[152:155], v230, s[60:61] sc1\n\t"
      "global_load_dwordx4 v[128:131], v224, s[62:63] sc1\n\t"
      "global_load_dwordx4 v[132:135], v225, s[62:63] sc1\n\t"
      "s_waitcnt vmcnt(5)\n\t"
      "v_max3_u32 v234, v140, v141, v142\n\t"
      "v_max_u32_e32 v234, v234, v143\n\t"
      "v_cmp_eq_u32_e32 vcc, -1, v234\n\t"
      "s_cbranch_vccnz FP31L_%=\n\t"
      "FP31X_%=:\n\t"
      "s_waitcnt vmcnt(0)\n\t"
      "s_branch FP31B_%=\n\t"
      "FP32R_%=:\n\t"
      "s_mov_b32 s51, 0\n\t"
      "FP32L_%=:\n\t"
      "s_add_u32 s51, s51, 1\n\t"
      "s_cmp_lt_u32 s51, 262144\n\t"
      "s_cbranch_scc0 FP32X_%=\n\t"
      "s_waitcnt vmcnt(0)\n\t"
      "s_sleep 4\n\t"
      "global_load_dwordx4 v[144:147], v228, s[60:61] sc1\n\t"
      "global_load_dwordx4 v[148:151], v229, s[60:61] sc1\n\t"
      "global_load_dwordx4 v[152:155], v230, s[60:61] sc1\n\t"
      "global_load_dwordx4 v[128:131], v224, s[62:63] sc1\n\t"
      "global_load_dwordx4 v[132:135], v225, s[62:63] sc1\n\t"
      "global_load_dwordx4 v[136:139], v226, s[62:63] sc1\n\t"
      "s_waitcnt vmcnt(5)\n\t"
      "v_max3_u32 v234, v144, v145, v146\n\t"
      "v_max_u32_e32 v234, v234, v147\n\t"
      "v_cmp_eq_u32_e32 vcc, -1, v234\n\t"
      "s_cbranch_vccnz FP32L_%=\n\t"
      "FP32X_%=:\n\t"
      "s_waitcnt vmcnt(0)\n\t"
      "s_branch FP32B_%=\n\t"
      "FP33R_%=:\n\t"
      "s_mov_b32 s51, 0\n\t"
      "FP33L_%=:\n\t"
      "s_add_u32 s51, s51, 1\n\t"
      "s_cmp_lt_u32 s51, 262144\n\t"
      "s_cbranch_scc0 FP33X_%=\n\t"
      "s_waitcnt vmcnt(0)\n\t"
      "s_sleep 4\n\t"
      "global_load_dwordx4 v[148:151], v229, s[60:61] sc1\n\t"
      "global_load_dwordx4 v[152:155], v230, s[60:61] sc1\n\t"
      "global_load_dwordx4 v[128:131], v224, s[62:63] sc1\n\t"
      "global_load_dwordx4 v[132:135], v225, s[62:63] sc1\n\t"
      "global_load_dwordx4 v[136:139], v226, s[62:63] sc1\n\t"
      "global_load_dwordx4 v[140:143], v227, s[62:63] sc1\n\t"
      "s_waitcnt vmcnt(5)\n\t"
      "v_max3_u32 v234, v148, v149, v150\n\t"
      "v_max_u32_e32 v234, v234, v151\n\t"
      "v_cmp_eq_u32_e32 vcc, -1, v234\n\t"
      "s_cbranch_vccnz FP33L_%=\n\t"
      "FP33X_%=:\n\t"
      "s_branch FP33B_%=\n\t"
      "FP34R_%=:\n\t"
      "s_mov_b32 s51, 0\n\t"
      "FP34L_%=:\n\t"
      "s_add_u32 s51, s51, 1\n\t"
      "s_cmp_lt_u32 s51, 262144\n\t"
      "s_cbranch_scc0 FP34X_%=\n\t"
      "s_waitcnt vmcnt(0)\n\t"
      "s_sleep 4\n\t"
      "global_load_dwordx4 v[152:155], v230, s[60:61] sc1\n\t"
      "global_load_dwordx4 v[128:131], v224, s[62:63] sc1\n\t"
      "global_load_dwordx4 v[132:135], v225, s[62:63] sc1\n\t"
      "global_load_dwordx4 v[136:139], v226, s[62:63] sc1\n\t"
      "global_load_dwordx4 v[140:143], v227, s[62:63] sc1\n\t"
      "global_load_dwordx4 v[144:147], v228, s[62:63] sc1\n\t"
      "s_waitcnt vmcnt(5)\n\t"
      "v_max3_u32 v234, v152, v153, v154\n\t"
      "v_max_u32_e32 v234, v234, v155\n\t"
      "v_cmp_eq_u32_e32 vcc, -1, v234\n\t"
      "s_cbranch_vccnz FP34L_%=\n\t"
      "FP34X_%=:\n\t"
      "s_waitcnt vmcnt(0)\n\t"
      "s_branch FP34B_%=\n\t"
      "FP35R_%=:\n\t"
      "s_mov_b32 s51, 0\n\t"
      "FP35L_%=:\n\t"
      "s_add_u32 s51, s51, 1\n\t"
      "s_cmp_lt_u32 s51, 262144\n\t"
      "s_cbranch_scc0 FP35X_%=\n\t"
      "s_waitcnt vmcnt(0)\n\t"
      "s_sleep 4\n\t"
      "global_load_dwordx4 v[128:131], v224, s[62:63] sc1\n\t"
      "global_load_dwordx4 v[132:135], v225, s[62:63] sc1\n\t"
      "global_load_dwordx4 v[136:139], v226, s[62:63] sc1\n\t"
      "global_load_dwordx4 v[140:143], v227, s[62:63] sc1\n\t"
      "global_load_dwordx4 v[144:147], v228, s[62:63] sc1\n\t"
      "global_load_dwordx4 v[148:151], v229, s[62:63] sc1\n\t"
      "s_waitcnt vmcnt(5)\n\t"
      "v_max3_u32 v234, v128, v129, v130\n\t"
      "v_max_u32_e32 v234, v234, v131\n\t"
      "v_cmp_eq_u32_e32 vcc, -1, v234\n\t"
      "s_cbranch_vccnz FP35L_%=\n\t"
      "FP35X_%=:\n\t"
      "s_waitcnt vmcnt(0)\n\t"
      "s_branch FP35B_%=\n\t"
      "NP4R_%=:\n\t"
      "s_mov_b32 s51, 0\n\t"
      "NP4L_%=:\n\t"
      "s_add_u32 s51, s51, 1\n\t"
      "s_cmp_lt_u32 s51, 262144\n\t"
      "s_cbranch_scc0 NP4X_%=\n\t"
      "s_waitcnt vmcnt(0)\n\t"
      "s_sleep 1\n\t"
      "global_load_dwordx4 v[128:131], v224, s[60:61] sc1\n\t"
      "global_load_dwordx4 v[132:135], v225, s[60:61] sc1\n\t"
      "global_load_dwordx4 v[136:139], v226, s[60:61] sc1\n\t"
      "global_load_dwordx4 v[140:143], v227, s[60:61] sc1\n\t"
      "global_load_dwordx4 v[144:147], v228, s[60:61] sc1\n\t"
      "global_load_dwordx4 v[148:151], v229, s[60:61] sc1\n\t"
      "global_load_dwordx4 v[152:155], v230, s[60:61] sc1\n\t"
      "s_waitcnt vmcnt(0)\n\t"
      "v_max3_u32 v234, v128, v129, v130\n\t"
      "v_max3_u32 v234, v234, v131, v132\n\t"
      "v_max3_u32 v234, v234, v133, v134\n\t"
      "v_max3_u32 v234, v234, v135, v136\n\t"
      "v_max3_u32 v234, v234, v137, v138\n\t"
      "v_max3_u32 v234, v234, v139, v140\n\t"
      "v_max3_u32 v234, v234, v141, v142\n\t"
      "v_max3_u32 v234, v234, v143, v144\n\t"
      "v_max3_u32 v234, v234, v145, v146\n\t"
      "v_max3_u32 v234, v234, v147, v148\n\t"
      "v_max3_u32 v234, v234, v149, v150\n\t"
      "v_max3_u32 v234, v234, v151, v152\n\t"
      "v_max3_u32 v234, v234, v153, v154\n\t"
      "v_max_u32_e32 v234, v234, v155\n\t"
      "v_cmp_eq_u32_e32 vcc, -1, v234\n\t"
      "s_cbranch_vccnz NP4L_%=\n\t"
      "NP4X_%=:\n\t"
      "v_mfma_f32_16x16x32_bf16 v[184:187], v[128:131], v[0:3], v[172:175]\n\t"
      "v_mfma_f32_16x16x32_bf16 v[188:191], v[128:131], v[4:7], 0\n\t"
      "v_mfma_f32_16x16x32_bf16 v[192:195], v[128:131], v[8:11], 0\n\t"
      "v_mfma_f32_16x16x32_bf16 v[196:199], v[128:131], v[12:15], 0\n\t"
      "v_mfma_f32_16x16x32_bf16 v[184:187], v[132:135], v[16:19], v[184:187]\n\t"
      "v_mfma_f32_16x16x32_bf16 v[188:191], v[132:135], v[20:23], v[188:191]\n\t"
      "v_mfma_f32_16x16x32_bf16 v[192:195], v[132:135], v[24:27], v[192:195]\n\t"
      "v_mfma_f32_16x16x32_bf16 v[196:199], v[132:135], v[28:31], v[196:199]\n\t"
      "v_mfma_f32_16x16x32_bf16 v[184:187], v[136:139], v[32:35], v[184:187]\n\t"
      "v_mfma_f32_16x16x32_bf16 v[188:191], v[136:139], v[36:39], v[188:191]\n\t"
      "v_mfma_f32_16x16x32_bf16 v[192:195], v[136:139], v[40:43], v[192:195]\n\t"
      "v_mfma_f32_16x16x32_bf16 v[196:199], v[136:139], v[44:47], v[196:199]\n\t"
      "v_mfma_f32_16x16x32_bf16 v[184:187], v[140:143], v[48:51], v[184:187]\n\t"
      "v_mfma_f32_16x16x32_bf16 v[188:191], v[140:143], v[52:55], v[188:191]\n\t"
      "v_mfma_f32_16x16x32_bf16 v[192:195], v[140:143], v[56:59], v[192:195]\n\t"
      "v_mfma_f32_16x16x32_bf16 v[196:199], v[140:143], v[60:63], v[196:199]\n\t"
      "v_mfma_f32_16x16x32_bf16 v[184:187], v[144:147], v[64:67], v[184:187]\n\t"
      "v_mfma_f32_16x16x32_bf16 v[188:191], v[144:147], v[68:71], v[188:191]\n\t"
      "v_mfma_f32_16x16x32_bf16 v[192:195], v[144:147], v[72:75], v[192:195]\n\t"
      "v_mfma_f32_16x16x32_bf16 v[196:199], v[144:147], v[76:79], v[196:199]\n\t"
      "v_mfma_f32_16x16x32_bf16 v[184:187], v[148:151], v[80:83], v[184:187]\n\t"
      "v_mfma_f32_16x16x32_bf16 v[188:191], v[148:151], v[84:87], v[188:191]\n\t"
      "v_mfma_f32_16x16x32_bf16 v[192:195], v[148:151], v[88:91], v[192:195]\n\t"
      "v_mfma_f32_16x16x32_bf16 v[196:199], v[148:151], v[92:95], v[196:199]\n\t"
      "v_mfma_f32_16x16x32_bf16 v[184:187], v[152:155], v[96:99], v[184:187]\n\t"
      "v_mfma_f32_16x16x32_bf16 v[188:191], v[152:155], v[100:103], v[188:191]\n\t"
      "v_mfma_f32_16x16x32_bf16 v[192:195], v[152:155], v[104:107], v[192:195]\n\t"
      "v_mfma_f32_16x16x32_bf16 v[196:199], v[152:155], v[108:111], v[196:199]\n\t"
      "global_load_dwordx4 v[128:131], v224, s[62:63] sc1\n\t"
      "global_load_dwordx4 v[132:135], v225, s[62:63] sc1\n\t"
      "global_load_dwordx4 v[136:139], v226, s[62:63] sc1\n\t"
      "global_load_dwordx4 v[140:143], v227, s[62:63] sc1\n\t"
      "global_load_dwordx4 v[144:147], v228, s[62:63] sc1\n\t"
      "global_load_dwordx4 v[148:151], v229, s[62:63] sc1\n\t"
      "global_load_dwordx4 v[152:155], v230, s[62:63] sc1\n\t"
      "s_waitcnt vmcnt(0)\n\t"
      "s_nop 7\n\t"
      "s_branch NP4B_%=\n\t"
      "FP36R_%=:\n\t"
      "s_mov_b32 s51, 0\n\t"
      "FP36L_%=:\n\t"
      "s_add_u32 s51, s51, 1\n\t"
      "s_cmp_lt_u32 s51, 262144\n\t"
      "s_cbranch_scc0 FP36X_%=\n\t"
      "s_waitcnt vmcnt(0)\n\t"
      "s_sleep 4\n\t"
      "global_load_dwordx4 v[132:135], v225, s[62:63] sc1\n\t"
      "global_load_dwordx4 v[136:139], v226, s[62:63] sc1\n\t"
      "global_load_dwordx4 v[140:143], v227, s[62:63] sc1\n\t"
      "global_load_dwordx4 v[144:147], v228, s[62:63] sc1\n\t"
      "global_load_dwordx4 v[148:151], v229, s[62:63] sc1\n\t"
      "global_load_dwordx4 v[152:155], v230, s[62:63] sc1\n\t"
      "s_waitcnt vmcnt(5)\n\t"
      "v_max3_u32 v234, v132, v133, v134\n\t"
      "v_max_u32_e32 v234, v234, v135\n\t"
      "v_cmp_eq_u32_e32 vcc, -1, v234\n\t"
      "s_cbranch_vccnz FP36L_%=\n\t"
      "FP36X_%=:\n\t"
      "s_waitcnt vmcnt(0)\n\t"
      "s_branch FP36B_%=\n\t"
      "FP37R_%=:\n\t"
      "s_mov_b32 s51, 0\n\t"
      "FP37L_%=:\n\t"
      "s_add_u32 s51, s51, 1\n\t"
      "s_cmp_lt_u32 s51, 262144\n\t"
      "s_cbranch_scc0 FP37X_%=\n\t"
      "s_waitcnt vmcnt(0)\n\t"
      "s_sleep 4\n\t"
      "global_load_dwordx4 v[136:139], v226, s[62:63] sc1\n\t"
      "global_load_dwordx4 v[140:143], v227, s[62:63] sc1\n\t"
      "global_load_dwordx4 v[144:147], v228, s[62:63] sc1\n\t"
      "global_load_dwordx4 v[148:151], v229, s[62:63] sc1\n\t"
      "global_load_dwordx4 v[152:155], v230, s[62:63] sc1\n\t"
      "global_load_dwordx4 v[128:131], v224, s[64:65] sc1\n\t"
      "s_waitcnt vmcnt(5)\n\t"
      "v_max3_u32 v234, v136, v137, v138\n\t"
      "v_max_u32_e32 v234, v234, v139\n\t"
      "v_cmp_eq_u32_e32 vcc, -1, v234\n\t"
      "s_cbranch_vccnz FP37L_%=\n\t"
      "FP37X_%=:\n\t"
      "s_waitcnt vmcnt(0)\n\t"
      "s_branch FP37B_%=\n\t"
      "FP38R_%=:\n\t"
      "s_mov_b32 s51, 0\n\t"
      "FP38L_%=:\n\t"
      "s_add_u32 s51, s51, 1\n\t"
      "s_cmp_lt_u32 s51, 262144\n\t"
      "s_cbranch_scc0 FP38X_%=\n\t"
      "s_waitcnt vmcnt(0)\n\t"
      "s_sleep 4\n\t"
      "global_load_dwordx4 v[140:143], v227, s[62:63] sc1\n\t"
      "global_load_dwordx4 v[144:147], v228, s[62:63] sc1\n\t"
      "global_load_dwordx4 v[148:151], v229, s[62:63] sc1\n\t"
      "global_load_dwordx4 v[152:155], v230, s[62:63] sc1\n\t"
      "global_load_dwordx4 v[128:131], v224, s[64:65] sc1\n\t"
      "global_load_dwordx4 v[132:135], v225, s[64:65] sc1\n\t"
      "s_waitcnt vmcnt(5)\n\t"
      "v_max3_u32 v234, v140, v141, v142\n\t"
      "v_max_u32_e32 v234, v234, v143\n\t"
      "v_cmp_eq_u32_e32 vcc, -1, v234\n\t"
      "s_cbranch_vccnz FP38L_%=\n\t"
      "FP38X_%=:\n\t"
      "s_waitcnt vmcnt(0)\n\t"
      "s_branch FP38B_%=\n\t"
      "FP39R_%=:\n\t"
      "s_mov_b32 s51, 0\n\t"
      "FP39L_%=:\n\t"
      "s_add_u32 s51, s51, 1\n\t"
      "s_cmp_lt_u32 s51, 262144\n\t"
      "s_cbranch_scc0 FP39X_%=\n\t"
      "s_waitcnt vmcnt(0)\n\t"
      "s_sleep 4\n\t"
      "global_load_dwordx4 v[144:147], v228, s[62:63] sc1\n\t"
      "global_load_dwordx4 v[148:151], v229, s[62:63] sc1\n\t"
      "global_load_dwordx4 v[152:155], v230, s[62:63] sc1\n\t"
      "global_load_dwordx4 v[128:131], v224, s[64:65] sc1\n\t"
      "global_load_dwordx4 v[132:135], v225, s[64:65] sc1\n\t"
      "global_load_dwordx4 v[136:139], v226, s[64:65] sc1\n\t"
      "s_waitcnt vmcnt(5)\n\t"
      "v_max3_u32 v234, v144, v145, v146\n\t"
      "v_max_u32_e32 v234, v234, v147\n\t"
      "v_cmp_eq_u32_e32 vcc, -1, v234\n\t"
      "s_cbranch_vccnz FP39L_%=\n\t"
      "FP39X_%=:\n\t"
      "s_waitcnt vmcnt(0)\n\t"
      "s_branch FP39B_%=\n\t"
      "FP40R_%=:\n\t"
      "s_mov_b32 s51, 0\n\t"
      "FP40L_%=:\n\t"
      "s_add_u32 s51, s51, 1\n\t"
      "s_cmp_lt_u32 s51, 262144\n\t"
      "s_cbranch_scc0 FP40X_%=\n\t"
      "s_waitcnt vmcnt(0)\n\t"
      "s_sleep 4\n\t"
      "global_load_dwordx4 v[148:151], v229, s[62:63] sc1\n\t"
      "global_load_dwordx4 v[152:155], v230, s[62:63] sc1\n\t"
      "global_load_dwordx4 v[128:131], v224, s[64:65] sc1\n\t"
      "global_load_dwordx4 v[132:135], v225, s[64:65] sc1\n\t"
      "global_load_dwordx4 v[136:139], v226, s[64:65] sc1\n\t"
      "global_load_dwordx4 v[140:143], v227, s[64:65] sc1\n\t"
      "s_waitcnt vmcnt(5)\n\t"
      "v_max3_u32 v234, v148, v149, v150\n\t"
      "v_max_u32_e32 v234, v234, v151\n\t"
      "v_cmp_eq_u32_e32 vcc, -1, v234\n\t"
      "s_cbranch_vccnz FP40L_%=\n\t"
      "FP40X_%=:\n\t"
      "s_branch FP40B_%=\n\t"
      "FP41R_%=:\n\t"
      "s_mov_b32 s51, 0\n\t"
      "FP41L_%=:\n\t"
      "s_add_u32 s51, s51, 1\n\t"
      "s_cmp_lt_u32 s51, 262144\n\t"
      "s_cbranch_scc0 FP41X_%=\n\t"
      "s_waitcnt vmcnt(0)\n\t"
      "s_sleep 4\n\t"
      "global_load_dwordx4 v[152:155], v230, s[62:63] sc1\n\t"
      "global_load_dwordx4 v[128:131], v224, s[64:65] sc1\n\t"
      "global_load_dwordx4 v[132:135], v225, s[64:65] sc1\n\t"
      "global_load_dwordx4 v[136:139], v226, s[64:65] sc1\n\t"
      "global_load_dwordx4 v[140:143], v227, s[64:65] sc1\n\t"
      "global_load_dwordx4 v[144:147], v228, s[64:65] sc1\n\t"
      "s_waitcnt vmcnt(5)\n\t"
      "v_max3_u32 v234, v152, v153, v154\n\t"
      "v_max_u32_e32 v234, v234, v155\n\t"
      "v_cmp_eq_u32_e32 vcc, -1, v234\n\t"
      "s_cbranch_vccnz FP41L_%=\n\t"
      "FP41X_%=:\n\t"
      "s_waitcnt vmcnt(0)\n\t"
      "s_branch FP41B_%=\n\t"
      "FP42R_%=:\n\t"
      "s_mov_b32 s51, 0\n\t"
      "FP42L_%=:\n\t"
      "s_add_u32 s51, s51, 1\n\t"
      "s_cmp_lt_u32 s51, 262144\n\t"
      "s_cbranch_scc0 FP42X_%=\n\t"
      "s_sleep 1\n\t"
      "s_mov_b64 exec, 15\n\t"
      "v_mov_b64_e32 v[232:233], 0\n\t"
      "global_atomic_or_x2 v[232:233], v247, v[232:233], s[12:13] sc0 sc1\n\t"
      "s_waitcnt vmcnt(0)\n\t"
      "v_subrev_u32_e32 v232, s14, v232\n\t"
      "v_cmp_ne_u32_e64 s[48:49], s15, v233\n\t"
      "v_subrev_u32_e32 v232, 6, v232\n\t"
      "v_cmp_lt_u32_e32 vcc, 7, v232\n\t"
      "s_mov_b64 exec, -1\n\t"
      "s_or_b64 vcc, vcc, s[48:49]\n\t"
      "s_cbranch_vccnz FP42L_%=\n\t"
      "FP42X_%=:\n\t"
      "FP42DR_%=:\n\t"
      "s_mov_b32 s51, 0\n\t"
      "FP42DL_%=:\n\t"
      "s_add_u32 s51, s51, 1\n\t"
      "s_cmp_lt_u32 s51, 262144\n\t"
      "s_cbranch_scc0 FP42DX_%=\n\t"
      "s_waitcnt vmcnt(0)\n\t"
      "s_sleep 1\n\t"
      "global_load_dwordx4 v[128:131], v224, s[64:65] sc1\n\t"
      "global_load_dwordx4 v[132:135], v225, s[64:65] sc1\n\t"
      "global_load_dwordx4 v[136:139], v226, s[64:65] sc1\n\t"
      "global_load_dwordx4 v[140:143], v227, s[64:65] sc1\n\t"
      "global_load_dwordx4 v[144:147], v228, s[64:65] sc1\n\t"
      "global_load_dwordx4 v[148:151], v229, s[64:65] sc1\n\t"
      "s_waitcnt vmcnt(5)\n\t"
      "v_max3_u32 v234, v128, v129, v130\n\t"
      "v_max_u32_e32 v234, v234, v131\n\t"
      "v_cmp_eq_u32_e32 vcc, -1, v234\n\t"
      "s_cbranch_vccnz FP42DL_%=\n\t"
      "FP42DX_%=:\n\t"
      "s_waitcnt vmcnt(0)\n\t"
      "s_branch FP42B_%=\n\t"
      "NP5R_%=:\n\t"
      "s_mov_b32 s51, 0\n\t"
      "NP5L_%=:\n\t"
      "s_add_u32 s51, s51, 1\n\t"
      "s_cmp_lt_u32 s51, 262144\n\t"
      "s_cbranch_scc0 NP5X_%=\n\t"
      "s_waitcnt vmcnt(0)\n\t"
      "s_sleep 1\n\t"
      "global_load_dwordx4 v[128:131], v224, s[62:63] sc1\n\t"
      "global_load_dwordx4 v[132:135], v225, s[62:63] sc1\n\t"
      "global_load_dwordx4 v[136:139], v226, s[62:63] sc1\n\t"
      "global_load_dwordx4 v[140:143], v227, s[62:63] sc1\n\t"
      "global_load_dwordx4 v[144:147], v228, s[62:63] sc1\n\t"
      "global_load_dwordx4 v[148:151], v229, s[62:63] sc1\n\t"
      "global_load_dwordx4 v[152:155], v230, s[62:63] sc1\n\t"
      "s_waitcnt vmcnt(0)\n\t"
      "v_max3_u32 v234, v128, v129, v130\n\t"
      "v_max3_u32 v234, v234, v131, v132\n\t"
      "v_max3_u32 v234, v234, v133, v134\n\t"
      "v_max3_u32 v234, v234, v135, v136\n\t"
      "v_max3_u32 v234, v234, v137, v138\n\t"
      "v_max3_u32 v234, v234, v139, v140\n\t"
      "v_max3_u32 v234, v234, v141, v142\n\t"
      "v_max3_u32 v234, v234, v143, v144\n\t"
      "v_max3_u32 v234, v234, v145, v146\n\t"
      "v_max3_u32 v234, v234, v147, v148\n\t"
      "v_max3_u32 v234, v234, v149, v150\n\t"
      "v_max3_u32 v234, v234, v151, v152\n\t"
      "v_max3_u32 v234, v234, v153, v154\n\t"
      "v_max_u32_e32 v234, v234, v155\n\t"
      "v_cmp_eq_u32_e32 vcc, -1, v234\n\t"
      "s_cbranch_vccnz NP5L_%=\n\t"
      "NP5X_%=:\n\t"
      "v_mfma_f32_16x16x32_bf16 v[200:203], v[128:131], v[0:3], v[176:179]\n\t"
      "v_mfma_f32_16x16x32_bf16 v[204:207], v[128:131], v[4:7], 0\n\t"
      "v_mfma_f32_16x16x32_bf16 v[208:211], v[128:131], v[8:11], 0\n\t"
      "v_mfma_f32_16x16x32_bf16 v[212:215], v[128:131], v[12:15], 0\n\t"
      "v_mfma_f32_16x16x32_bf16 v[200:203], v[132:135], v[16:19], v[200:203]\n\t"
      "v_mfma_f32_16x16x32_bf16 v[204:207], v[132:135], v[20:23], v[204:207]\n\t"
      "v_mfma_f32_16x16x32_bf16 v[208:211], v[132:135], v[24:27], v[208:211]\n\t"
      "v_mfma_f32_16x16x32_bf16 v[212:215], v[132:135], v[28:31], v[212:215]\n\t"
      "v_mfma_f32_16x16x32_bf16 v[200:203], v[136:139], v[32:35], v[200:203]\n\t"
      "v_mfma_f32_16x16x32_bf16 v[204:207], v[136:139], v[36:39], v[204:207]\n\t"
      "v_mfma_f32_16x16x32_bf16 v[208:211], v[136:139], v[40:43], v[208:211]\n\t"
      "v_mfma_f32_16x16x32_bf16 v[212:215], v[136:139], v[44:47], v[212:215]\n\t"
      "v_mfma_f32_16x16x32_bf16 v[200:203], v[140:143], v[48:51], v[200:203]\n\t"
      "v_mfma_f32_16x16x32_bf16 v[204:207], v[140:143], v[52:55], v[204:207]\n\t"
      "v_mfma_f32_16x16x32_bf16 v[208:211], v[140:143], v[56:59], v[208:211]\n\t"
      "v_mfma_f32_16x16x32_bf16 v[212:215], v[140:143], v[60:63], v[212:215]\n\t"
      "v_mfma_f32_16x16x32_bf16 v[200:203], v[144:147], v[64:67], v[200:203]\n\t"
      "v_mfma_f32_16x16x32_bf16 v[204:207], v[144:147], v[68:71], v[204:207]\n\t"
      "v_mfma_f32_16x16x32_bf16 v[208:211], v[144:147], v[72:75], v[208:211]\n\t"
      "v_mfma_f32_16x16x32_bf16 v[212:215], v[144:147], v[76:79], v[212:215]\n\t"
      "v_mfma_f32_16x16x32_bf16 v[200:203], v[148:151], v[80:83], v[200:203]\n\t"
      "v_mfma_f32_16x16x32_bf16 v[204:207], v[148:151], v[84:87], v[204:207]\n\t"
      "v_mfma_f32_16x16x32_bf16 v[208:211], v[148:151], v[88:91], v[208:211]\n\t"
      "v_mfma_f32_16x16x32_bf16 v[212:215], v[148:151], v[92:95], v[212:215]\n\t"
      "v_mfma_f32_16x16x32_bf16 v[200:203], v[152:155], v[96:99], v[200:203]\n\t"
      "v_mfma_f32_16x16x32_bf16 v[204:207], v[152:155], v[100:103], v[204:207]\n\t"
      "v_mfma_f32_16x16x32_bf16 v[208:211], v[152:155], v[104:107], v[208:211]\n\t"
      "v_mfma_f32_16x16x32_bf16 v[212:215], v[152:155], v[108:111], v[212:215]\n\t"
      "global_load_dwordx4 v[128:131], v224, s[64:65] sc1\n\t"
      "global_load_dwordx4 v[132:135], v225, s[64:65] sc1\n\t"
      "global_load_dwordx4 v[136:139], v226, s[64:65] sc1\n\t"
      "global_load_dwordx4 v[140:143], v227, s[64:65] sc1\n\t"
      "global_load_dwordx4 v[144:147], v228, s[64:65] sc1\n\t"
      "global_load_dwordx4 v[148:151], v229, s[64:65] sc1\n\t"
      "global_load_dwordx4 v[152:155], v230, s[64:65] sc1\n\t"
      "s_waitcnt vmcnt(0)\n\t"
      "s_nop 7\n\t"
      "s_branch NP5B_%=\n\t"
      "FP43R_%=:\n\t"
      "s_mov_b32 s51, 0\n\t"
      "FP43L_%=:\n\t"
      "s_add_u32 s51, s51, 1\n\t"
      "s_cmp_lt_u32 s51, 262144\n\t"
      "s_cbranch_scc0 FP43X_%=\n\t"
      "s_sleep 1\n\t"
      "s_mov_b64 exec, 15\n\t"
      "v_mov_b64_e32 v[232:233], 0\n\t"
      "global_atomic_or_x2 v[232:233], v247, v[232:233], s[12:13] sc0 sc1\n\t"
      "s_waitcnt vmcnt(0)\n\t"
      "v_subrev_u32_e32 v232, s14, v232\n\t"
      "v_cmp_ne_u32_e64 s[48:49], s15, v233\n\t"
      "v_subrev_u32_e32 v232, 6, v232\n\t"
      "v_cmp_lt_u32_e32 vcc, 7, v232\n\t"
      "s_mov_b64 exec, -1\n\t"
      "s_or_b64 vcc, vcc, s[48:49]\n\t"
      "s_cbranch_vccnz FP43L_%=\n\t"
      "FP43X_%=:\n\t"
      "FP43DR_%=:\n\t"
      "s_mov_b32 s51, 0\n\t"
      "FP43DL_%=:\n\t"
      "s_add_u32 s51, s51, 1\n\t"
      "s_cmp_lt_u32 s51, 262144\n\t"
      "s_cbranch_scc0 FP43DX_%=\n\t"
      "s_waitcnt vmcnt(0)\n\t"
      "s_sleep 1\n\t"
      "global_load_dwordx4 v[132:135], v225, s[64:65] sc1\n\t"
      "global_load_dwordx4 v[136:139], v226, s[64:65] sc1\n\t"
      "global_load_dwordx4 v[140:143], v227, s[64:65] sc1\n\t"
      "global_load_dwordx4 v[144:147], v228, s[64:65] sc1\n\t"
      "global_load_dwordx4 v[148:151], v229, s[64:65] sc1\n\t"
      "global_load_dwordx4 v[152:155], v230, s[64:65] sc1\n\t"
      "s_waitcnt vmcnt(5)\n\t"
      "v_max3_u32 v234, v132, v133, v134\n\t"
      "v_max_u32_e32 v234, v234, v135\n\t"
      "v_cmp_eq_u32_e32 vcc, -1, v234\n\t"
      "s_cbranch_vccnz FP43DL_%=\n\t"
      "FP43DX_%=:\n\t"
      "s_waitcnt vmcnt(0)\n\t"
      "s_branch FP43B_%=\n\t"
      "FP44R_%=:\n\t"
      "s_mov_b32 s51, 0\n\t"
      "FP44L_%=:\n\t"
      "s_add_u32 s51, s51, 1\n\t"
      "s_cmp_lt_u32 s51, 262144\n\t"
      "s_cbranch_scc0 FP44X_%=\n\t"
      "s_sleep 1\n\t"
      "s_mov_b64 exec, 15\n\t"
      "v_mov_b64_e32 v[232:233], 0\n\t"
      "global_atomic_or_x2 v[232:233], v247, v[232:233], s[12:13] sc0 sc1\n\t"
      "s_waitcnt vmcnt(0)\n\t"
      "v_subrev_u32_e32 v232, s14, v232\n\t"
      "v_cmp_ne_u32_e64 s[48:49], s15, v233\n\t"
      "v_subrev_u32_e32 v232, 6, v232\n\t"
      "v_cmp_lt_u32_e32 vcc, 7, v232\n\t"
      "s_mov_b64 exec, -1\n\t"
      "s_or_b64 vcc, vcc, s[48:49]\n\t"
      "s_cbranch_vccnz FP44L_%=\n\t"
      "FP44X_%=:\n\t"
      "FP44DR_%=:\n\t"
      "s_mov_b32 s51, 0\n\t"
      "FP44DL_%=:\n\t"
      "s_add_u32 s51, s51, 1\n\t"
      "s_cmp_lt_u32 s51, 262144\n\t"
      "s_cbranch_scc0 FP44DX_%=\n\t"
      "s_waitcnt vmcnt(0)\n\t"
      "s_sleep 1\n\t"
      "global_load_dwordx4 v[136:139], v226, s[64:65] sc1\n\t"
      "global_load_dwordx4 v[140:143], v227, s[64:65] sc1\n\t"
      "global_load_dwordx4 v[144:147], v228, s[64:65] sc1\n\t"
      "global_load_dwordx4 v[148:151], v229, s[64:65] sc1\n\t"
      "global_load_dwordx4 v[152:155], v230, s[64:65] sc1\n\t"
      "s_waitcnt vmcnt(4)\n\t"
      "v_max3_u32 v234, v136, v137, v138\n\t"
      "v_max_u32_e32 v234, v234, v139\n\t"
      "v_cmp_eq_u32_e32 vcc, -1, v234\n\t"
      "s_cbranch_vccnz FP44DL_%=\n\t"
      "FP44DX_%=:\n\t"
      "s_waitcnt vmcnt(0)\n\t"
      "s_branch FP44B_%=\n\t"
      "FP45R_%=:\n\t"
      "s_mov_b32 s51, 0\n\t"
      "FP45L_%=:\n\t"
      "s_add_u32 s51, s51, 1\n\t"
      "s_cmp_lt_u32 s51, 262144\n\t"
      "s_cbranch_scc0 FP45X_%=\n\t"
      "s_sleep 1\n\t"
      "s_mov_b64 exec, 15\n\t"
      "v_mov_b64_e32 v[232:233], 0\n\t"
      "global_atomic_or_x2 v[232:233], v247, v[232:233], s[12:13] sc0 sc1\n\t"
      "s_waitcnt vmcnt(0)\n\t"
      "v_subrev_u32_e32 v232, s14, v232\n\t"
      "v_cmp_ne_u32_e64 s[48:49], s15, v233\n\t"
      "v_subrev_u32_e32 v232, 6, v232\n\t"
      "v_cmp_lt_u32_e32 vcc, 7, v232\n\t"
      "s_mov_b64 exec, -1\n\t"
      "s_or_b64 vcc, vcc, s[48:49]\n\t"
      "s_cbranch_vccnz FP45L_%=\n\t"
      "FP45X_%=:\n\t"
      "FP45DR_%=:\n\t"
      "s_mov_b32 s51, 0\n\t"
      "FP45DL_%=:\n\t"
      "s_add_u32 s51, s51, 1\n\t"
      "s_cmp_lt_u32 s51, 262144\n\t"
      "s_cbranch_scc0 FP45DX_%=\n\t"
      "s_waitcnt vmcnt(0)\n\t"
      "s_sleep 1\n\t"
      "global_load_dwordx4 v[140:143], v227, s[64:65] sc1\n\t"
      "global_load_dwordx4 v[144:147], v228, s[64:65] sc1\n\t"
      "global_load_dwordx4 v[148:151], v229, s[64:65] sc1\n\t"
      "global_load_dwordx4 v[152:155], v230, s[64:65] sc1\n\t"
      "s_waitcnt vmcnt(3)\n\t"
      "v_max3_u32 v234, v140, v141, v142\n\t"
      "v_max_u32_e32 v234, v234, v143\n\t"
      "v_cmp_eq_u32_e32 vcc, -1, v234\n\t"
      "s_cbranch_vccnz FP45DL_%=\n\t"
      "FP45DX_%=:\n\t"
      "s_waitcnt vmcnt(0)\n\t"
      "s_branch FP45B_%=\n\t"
      "FP46R_%=:\n\t"
      "s_mov_b32 s51, 0\n\t"
      "FP46L_%=:\n\t"
      "s_add_u32 s51, s51, 1\n\t"
      "s_cmp_lt_u32 s51, 262144\n\t"
      "s_cbranch_scc0 FP46X_%=\n\t"
      "s_sleep 1\n\t"
      "s_mov_b64 exec, 15\n\t"
      "v_mov_b64_e32 v[232:233], 0\n\t"
      "global_atomic_or_x2 v[232:233], v247, v[232:233], s[12:13] sc0 sc1\n\t"
      "s_waitcnt vmcnt(0)\n\t"
      "v_subrev_u32_e32 v232, s14, v232\n\t"
      "v_cmp_ne_u32_e64 s[48:49], s15, v233\n\t"
      "v_subrev_u32_e32 v232, 6, v232\n\t"
      "v_cmp_lt_u32_e32 vcc, 7, v232\n\t"
      "s_mov_b64 exec, -1\n\t"
      "s_or_b64 vcc, vcc, s[48:49]\n\t"
      "s_cbranch_vccnz FP46L_%=\n\t"
      "FP46X_%=:\n\t"
      "FP46DR_%=:\n\t"
      "s_mov_b32 s51, 0\n\t"
      "FP46DL_%=:\n\t"
      "s_add_u32 s51, s51, 1\n\t"
      "s_cmp_lt_u32 s51, 262144\n\t"
      "s_cbranch_scc0 FP46DX_%=\n\t"
      "s_waitcnt vmcnt(0)\n\t"
      "s_sleep 1\n\t"
      "global_load_dwordx4 v[144:147], v228, s[64:65] sc1\n\t"
      "global_load_dwordx4 v[148:151], v229, s[64:65] sc1\n\t"
      "global_load_dwordx4 v[152:155], v230, s[64:65] sc1\n\t"
      "s_waitcnt vmcnt(2)\n\t"
      "v_max3_u32 v234, v144, v145, v146\n\t"
      "v_max_u32_e32 v234, v234, v147\n\t"
      "v_cmp_eq_u32_e32 vcc, -1, v234\n\t"
      "s_cbranch_vccnz FP46DL_%=\n\t"
      "FP46DX_%=:\n\t"
      "s_waitcnt vmcnt(0)\n\t"
      "s_branch FP46B_%=\n\t"
      "FP47R_%=:\n\t"
      "s_mov_b32 s51, 0\n\t"
      "FP47L_%=:\n\t"
      "s_add_u32 s51, s51, 1\n\t"
      "s_cmp_lt_u32 s51, 262144\n\t"
      "s_cbranch_scc0 FP47X_%=\n\t"
      "s_sleep 1\n\t"
      "s_mov_b64 exec, 15\n\t"
      "v_mov_b64_e32 v[232:233], 0\n\t"
      "global_atomic_or_x2 v[232:233], v247, v[232:233], s[12:13] sc0 sc1\n\t"
      "s_waitcnt vmcnt(0)\n\t"
      "v_subrev_u32_e32 v232, s14, v232\n\t"
      "v_cmp_ne_u32_e64 s[48:49], s15, v233\n\t"
      "v_subrev_u32_e32 v232, 6, v232\n\t"
      "v_cmp_lt_u32_e32 vcc, 7, v232\n\t"
      "s_mov_b64 exec, -1\n\t"
      "s_or_b64 vcc, vcc, s[48:49]\n\t"
      "s_cbranch_vccnz FP47L_%=\n\t"
      "FP47X_%=:\n\t"
      "FP47DR_%=:\n\t"
      "s_mov_b32 s51, 0\n\t"
      "FP47DL_%=:\n\t"
      "s_add_u32 s51, s51, 1\n\t"
      "s_cmp_lt_u32 s51, 262144\n\t"
      "s_cbranch_scc0 FP47DX_%=\n\t"
      "s_waitcnt vmcnt(0)\n\t"
      "s_sleep 1\n\t"
      "global_load_dwordx4 v[148:151], v229, s[64:65] sc1\n\t"
      "global_load_dwordx4 v[152:155], v230, s[64:65] sc1\n\t"
      "s_waitcnt vmcnt(1)\n\t"
      "v_max3_u32 v234, v148, v149, v150\n\t"
      "v_max_u32_e32 v234, v234, v151\n\t"
      "v_cmp_eq_u32_e32 vcc, -1, v234\n\t"
      "s_cbranch_vccnz FP47DL_%=\n\t"
      "FP47DX_%=:\n\t"
      "s_branch FP47B_%=\n\t"
      "FP48R_%=:\n\t"
      "s_mov_b32 s51, 0\n\t"
      "FP48L_%=:\n\t"
      "s_add_u32 s51, s51, 1\n\t"
      "s_cmp_lt_u32 s51, 262144\n\t"
      "s_cbranch_scc0 FP48X_%=\n\t"
      "s_sleep 1\n\t"
      "s_mov_b64 exec, 15\n\t"
      "v_mov_b64_e32 v[232:233], 0\n\t"
      "global_atomic_or_x2 v[232:233], v247, v[232:233], s[12:13] sc0 sc1\n\t"
      "s_waitcnt vmcnt(0)\n\t"
      "v_subrev_u32_e32 v232, s14, v232\n\t"
      "v_cmp_ne_u32_e64 s[48:49], s15, v233\n\t"
      "v_subrev_u32_e32 v232, 6, v232\n\t"
      "v_cmp_lt_u32_e32 vcc, 7, v232\n\t"
      "s_mov_b64 exec, -1\n\t"
      "s_or_b64 vcc, vcc, s[48:49]\n\t"
      "s_cbranch_vccnz FP48L_%=\n\t"
      "FP48X_%=:\n\t"
      "FP48DR_%=:\n\t"
      "s_mov_b32 s51, 0\n\t"
      "FP48DL_%=:\n\t"
      "s_add_u32 s51, s51, 1\n\t"
      "s_cmp_lt_u32 s51, 262144\n\t"
      "s_cbranch_scc0 FP48DX_%=\n\t"
      "s_waitcnt vmcnt(0)\n\t"
      "s_sleep 1\n\t"
      "global_load_dwordx4 v[152:155], v230, s[64:65] sc1\n\t"
      "s_waitcnt vmcnt(0)\n\t"
      "v_max3_u32 v234, v152, v153, v154\n\t"
      "v_max_u32_e32 v234, v234, v155\n\t"
      "v_cmp_eq_u32_e32 vcc, -1, v234\n\t"
      "s_cbranch_vccnz FP48DL_%=\n\t"
      "FP48DX_%=:\n\t"
      "s_waitcnt vmcnt(0)\n\t"
      "s_branch FP48B_%=\n\t"
      "NP6R_%=:\n\t"
      "s_mov_b32 s51, 0\n\t"
      "NP6L_%=:\n\t"
      "s_add_u32 s51, s51, 1\n\t"
      "s_cmp_lt_u32 s51, 262144\n\t"
      "s_cbranch_scc0 NP6X_%=\n\t"
      "s_waitcnt vmcnt(0)\n\t"
      "s_sleep 1\n\t"
      "global_load_dwordx4 v[128:131], v224, s[64:65] sc1\n\t"
      "global_load_dwordx4 v[132:135], v225, s[64:65] sc1\n\t"
      "global_load_dwordx4 v[136:139], v226, s[64:65] sc1\n\t"
      "global_load_dwordx4 v[140:143], v227, s[64:65] sc1\n\t"
      "global_load_dwordx4 v[144:147], v228, s[64:65] sc1\n\t"
      "global_load_dwordx4 v[148:151], v229, s[64:65] sc1\n\t"
      "global_load_dwordx4 v[152:155], v230, s[64:65] sc1\n\t"
      "s_waitcnt vmcnt(0)\n\t"
      "v_max3_u32 v234, v128, v129, v130\n\t"
      "v_max3_u32 v234, v234, v131, v132\n\t"
      "v_max3_u32 v234, v234, v133, v134\n\t"
      "v_max3_u32 v234, v234, v135, v136\n\t"
      "v_max3_u32 v234, v234, v137, v138\n\t"
      "v_max3_u32 v234, v234, v139, v140\n\t"
      "v_max3_u32 v234, v234, v141, v142\n\t"
      "v_max3_u32 v234, v234, v143, v144\n\t"
      "v_max3_u32 v234, v234, v145, v146\n\t"
      "v_max3_u32 v234, v234, v147, v148\n\t"
      "v_max3_u32 v234, v234, v149, v150\n\t"
      "v_max3_u32 v234, v234, v151, v152\n\t"
      "v_max3_u32 v234, v234, v153, v154\n\t"
      "v_max_u32_e32 v234, v234, v155\n\t"
      "v_cmp_eq_u32_e32 vcc, -1, v234\n\t"
      "s_cbranch_vccnz NP6L_%=\n\t"
      "NP6X_%=:\n\t"
      "v_mfma_f32_16x16x32_bf16 v[184:187], v[128:131], v[0:3], v[180:183]\n\t"
      "v_mfma_f32_16x16x32_bf16 v[188:191], v[128:131], v[4:7], 0\n\t"
      "v_mfma_f32_16x16x32_bf16 v[192:195], v[128:131], v[8:11], 0\n\t"
      "v_mfma_f32_16x16x32_bf16 v[196:199], v[128:131], v[12:15], 0\n\t"
      "v_mfma_f32_16x16x32_bf16 v[184:187], v[132:135], v[16:19], v[184:187]\n\t"
      "v_mfma_f32_16x16x32_bf16 v[188:191], v[132:135], v[20:23], v[188:191]\n\t"
      "v_mfma_f32_16x16x32_bf16 v[192:195], v[132:135], v[24:27], v[192:195]\n\t"
      "v_mfma_f32_16x16x32_bf16 v[196:199], v[132:135], v[28:31], v[196:199]\n\t"
      "v_mfma_f32_16x16x32_bf16 v[184:187], v[136:139], v[32:35], v[184:187]\n\t"
      "v_mfma_f32_16x16x32_bf16 v[188:191], v[136:139], v[36:39], v[188:191]\n\t"
      "v_mfma_f32_16x16x32_bf16 v[192:195], v[136:139], v[40:43], v[192:195]\n\t"
      "v_mfma_f32_16x16x32_bf16 v[196:199], v[136:139], v[44:47], v[196:199]\n\t"
      "v_mfma_f32_16x16x32_bf16 v[184:187], v[140:143], v[48:51], v[184:187]\n\t"
      "v_mfma_f32_16x16x32_bf16 v[188:191], v[140:143], v[52:55], v[188:191]\n\t"
      "v_mfma_f32_16x16x32_bf16 v[192:195], v[140:143], v[56:59], v[192:195]\n\t"
      "v_mfma_f32_16x16x32_bf16 v[196:199], v[140:143], v[60:63], v[196:199]\n\t"
      "v_mfma_f32_16x16x32_bf16 v[184:187], v[144:147], v[64:67], v[184:187]\n\t"
      "v_mfma_f32_16x16x32_bf16 v[188:191], v[144:147], v[68:71], v[188:191]\n\t"
      "v_mfma_f32_16x16x32_bf16 v[192:195], v[144:147], v[72:75], v[192:195]\n\t"
      "v_mfma_f32_16x16x32_bf16 v[196:199], v[144:147], v[76:79], v[196:199]\n\t"
      "v_mfma_f32_16x16x32_bf16 v[184:187], v[148:151], v[80:83], v[184:187]\n\t"
      "v_mfma_f32_16x16x32_bf16 v[188:191], v[148:151], v[84:87], v[188:191]\n\t"
      "v_mfma_f32_16x16x32_bf16 v[192:195], v[148:151], v[88:91], v[192:195]\n\t"
      "v_mfma_f32_16x16x32_bf16 v[196:199], v[148:151], v[92:95], v[196:199]\n\t"
      "v_mfma_f32_16x16x32_bf16 v[184:187], v[152:155], v[96:99], v[184:187]\n\t"
      "v_mfma_f32_16x16x32_bf16 v[188:191], v[152:155], v[100:103], v[188:191]\n\t"
      "v_mfma_f32_16x16x32_bf16 v[192:195], v[152:155], v[104:107], v[192:195]\n\t"
      "v_mfma_f32_16x16x32_bf16 v[196:199], v[152:155], v[108:111], v[196:199]\n\t"
      "s_waitcnt vmcnt(0)\n\t"
      "s_nop 7\n\t"
      "s_branch NP6B_%=\n\t"
      : "+{s[4:19]}"(s), "+{s[20:27]}"(s2), "+{s[28:29]}"(kr), "+{v[240:247]}"(v), "+{v[136:139]}"(vo), "+{v140}"(vq)
      :
      : "memory", "vcc", "scc",
        "v0", "v1", "v2", "v3", "v4", "v5", "v6", "v7", "v8", "v9", "v10", "v11", "v12", "v13", "v14", "v15", "v16",
        "v17", "v18", "v19", "v20", "v21", "v22", "v23", "v24", "v25", "v26", "v27", "v28", "v29", "v30", "v31", "v32",
        "v33", "v34", "v35", "v36", "v37", "v38", "v39", "v40", "v41", "v42", "v43", "v44", "v45", "v46", "v47", "v48",
        "v49", "v50", "v51", "v52", "v53", "v54", "v55", "v56", "v57", "v58", "v59", "v60", "v61", "v62", "v63", "v64",
        "v65", "v66", "v67", "v68", "v69", "v70", "v71", "v72", "v73", "v74", "v75", "v76", "v77", "v78", "v79", "v80",
        "v81", "v82", "v83", "v84", "v85", "v86", "v87", "v88", "v89", "v90", "v91", "v92", "v93", "v94", "v95", "v96",
        "v97", "v98", "v99", "v100", "v101", "v102", "v103", "v104", "v105", "v106", "v107", "v108", "v109", "v110",
        "v111", "v112", "v113", "v114", "v115", "v116", "v117", "v118", "v119", "v120", "v121", "v122", "v123", "v124",
        "v125", "v126", "v127", "v128", "v129", "v130", "v131", "v132", "v133", "v134", "v135", "v141", "v142", "v143",
        "v144", "v145", "v146", "v147", "v148", "v149", "v150", "v151", "v152", "v153", "v154", "v155", "v156", "v157",
        "v158", "v159", "v160", "v161", "v162", "v163", "v164", "v165", "v166", "v167", "v168", "v169", "v170", "v171",
        "v172", "v173", "v174", "v175", "v176", "v177", "v178", "v179", "v180", "v181", "v182", "v183", "v184", "v185",
        "v186", "v187", "v188", "v189", "v190", "v191", "v192", "v193", "v194", "v195", "v196", "v197", "v198", "v199",
        "v200", "v201", "v202", "v203", "v204", "v205", "v206", "v207", "v208", "v209", "v210", "v211", "v212", "v213",
        "v214", "v215", "v216", "v217", "v218", "v219", "v220", "v221", "v222", "v223", "v224", "v225", "v226", "v227",
        "v228", "v229", "v230", "v231", "v232", "v233", "v234", "v235", "v236", "v237", "v238", "v239", "v248", "v249",
        "v250", "v251", "v252", "v253", "s44", "s45", "s46", "s47", "s48", "s49", "s50", "s51", "s52", "s53", "s54",
        "s55", "s56", "s57", "s58", "s59", "s60", "s61", "s62", "s63", "s64", "s65", "s66", "s67", "s68", "s69", "s70"
    );
    __builtin_amdgcn_endpgm();
  }
}

extern "C" void kernel_launch(void* const* d_in, const int* in_sizes, int n_in, void* d_out, int out_size,
                              void* d_ws, size_t ws_size, hipStream_t stream) {
  (void)hipFuncSetAttribute((const void*)fused_block, hipFuncAttributeMaxDynamicSharedMemorySize, LDS_BYTES);
  fused_block<<<256, 512, LDS_BYTES, stream>>>((const char*)d_in[0], (const char*)d_in[1], (const char*)d_in[2],
                                               (const char*)d_in[4], (const char*)d_in[5], (const char*)d_in[6],
                                               (const char*)d_in[7], (char*)d_out, (char*)d_ws);
}
```

```cpp
#include <hip/hip_runtime.h>
#include <stdint.h>

#define WS_ST 0
#define WS_HN 2097152
#define WS_O 4194304
#define WS_G 6291456
#define ST_FL 65536
#define ST_PW 114688
#define WS_EP 131072
#define PROBE 1835008
#define ST_CK 3981312
#define ST_H2 4112384
#define ST_H3 8175616
#define ST_H4 8306688
#define A_UNIT 5120
#define A_HALO 40960
#define A_SLOT 43008
#define A_CS 131072
#define A_ST 132096
#define A_EX 142336
#define A_FL 154624
#define A_MK 154752
#define B_UNIT 7168
#define B_SLOT 57344
#define B_X 114688
#define B_OP 928
#define B_PS 129536
#define B_WX 130560
#define B_WY 66048
#define G_WV 16384
#define A_W0 49152
#define A_WD 40960
#define G_RED 0
#define G_RED2 32768
#define LDS_KEY 163600
#define LDS_SINK 163328
#define LDS_BYTES 163840
#define XO 2
#define KROT 14
#define KMUL 1
#define HP4 1
#define W2E 1
#define R4 0
#define SP3 1

typedef unsigned short u16;
typedef unsigned long long u64;
typedef __attribute__((ext_vector_type(4))) unsigned int u32x4;
typedef __attribute__((ext_vector_type(8))) unsigned int u32x8;
typedef __attribute__((ext_vector_type(16))) unsigned int u32x16;
#define UNI(x) (unsigned)__builtin_amdgcn_readfirstlane((int)(x))
#define PIN() __builtin_amdgcn_sched_barrier(0)

__device__ __forceinline__ int swz_tr(int row) { return (((row >> 1) & 1) | (((row >> 3) & 1) << 1)) << 1; }
__device__ __forceinline__ int swz_row(int row) { return (row >> 1) & 7; }
__device__ __forceinline__ int unit(int blk, int slice, int par) { return (((blk < 14 ? blk : blk - 14) * 8 + slice) * 2 + ((blk < 14 ? 0 : 1) ^ par)) * 8192; }
__device__ __forceinline__ int part(int n, int side) { return (n >> 13) * 16384 + side * 8192 + (n & 8191); }
template <class T>
__device__ __forceinline__ void put(T& s, int i, u64 p) { s[i] = (unsigned)p; s[i + 1] = (unsigned)(p >> 32); }
template <class T>
__device__ __forceinline__ void put2(T& s, int i, unsigned lo, unsigned hi) { s[i] = UNI(lo); s[i + 1] = UNI(hi); }

__global__ __launch_bounds__(512, 2) void fused_block(const char* __restrict__ x, const char* __restrict__ wqkv,
                                                   const char* __restrict__ wo, const char* __restrict__ w1,
                                                   const char* __restrict__ b1, const char* __restrict__ w2,
                                                   const char* __restrict__ b2, char* __restrict__ out,
                                                   char* __restrict__ ws0) {
  u32x8 f;
  asm volatile(
      "s_load_dwordx4 s[4:7], s[0:1], 0x0\n\t"
      "v_readfirstlane_b32 s3, v0\n\t"
      "s_getpc_b64 s[40:41]\n\t"
      "s_lshr_b32 s12, s2, 3\n\t"
      "s_or_b32 s13, s3, s12\n\t"
      "s_and_b32 s13, s13, 0x1d0\n\t"
      "s_cmp_lg_u32 s13, 16\n\t"
      "s_cbranch_scc1 ET2_%=\n\t"
      "s_and_b32 s12, s12, 7\n\t"
      "s_mov_b32 m0, 163328\n\t"
      "v_lshl_add_u32 v3, s12, 6, v0\n\t"
      "v_min_u32_e32 v3, 509, v3\n\t"
      "v_lshlrev_b32_e32 v3, 7, v3\n\t"
      "global_load_lds_dword v3, s[40:41]\n\t"
      "ET2_%=:\n\t"
      "v_lshrrev_b32_e32 v1, 3, v0\n\t"
      "v_and_b32_e32 v2, 7, v0\n\t"
      "s_and_b32 s17, s2, 7\n\t"
      "s_lshr_b32 s18, s2, 3\n\t"
      "s_lshr_b32 s3, s3, 6\n\t"
      "s_cmpk_gt_u32 s2, 0x7f\n\t"
      "s_cbranch_scc1 EB_%=\n\t"
      "v_and_b32_e32 v3, 2, v1\n\t"
      "v_lshrrev_b32_e32 v4, 1, v1\n\t"
      "s_lshr_b32 s19, s18, 3\n\t"
      "s_lshl1_add_u32 s19, s17, s19\n\t"
      "v_and_or_b32 v3, v4, 4, v3\n\t"
      "v_xor_b32_e32 v3, v3, v2\n\t"
      "v_min_u32_e32 v3, 6, v3\n\t"
      "v_mul_u32_u24_e32 v5, 2688, v1\n\t"
      "v_lshl_add_u32 v3, v3, 4, v5\n\t"
      "v_add_u32_e32 v195, 2048, v3\n\t"
      "v_add_u32_e32 v196, 1920, v3\n\t"
      "v_add_u32_e32 v197, 5888, v3\n\t"
      "s_mul_i32 s12, s19, 114688\n\t"
      "v_and_b32_e32 v5, 0x38, v1\n\t"
      "v_add_u32_e32 v5, v5, v1\n\t"
      "v_mul_u32_u24_e32 v5, 896, v5\n\t"
      "s_and_b32 s13, s18, 7\n\t"
      "s_mul_i32 s13, s13, 112\n\t"
      "v_and_b32_e32 v6, 3, v4\n\t"
      "v_xor_b32_e32 v6, v6, v2\n\t"
      "v_lshl_add_u32 v5, v6, 4, v5\n\t"
      "s_sub_u32 s12, s12, 15360\n\t"
      "s_ashr_i32 s14, s12, 31\n\t"
      "s_sub_u32 s13, s13, 4096\n\t"
      "v_add_u32_e32 v192, 15360, v5\n\t"
      "v_xor_b32_e32 v5, 64, v5\n\t"
      "v_add_u32_e32 v193, 21504, v5\n\t"
      "s_mul_i32 s9, s3, 5120\n\t"
      "s_waitcnt lgkmcnt(0)\n\t"
      "s_add_u32 s4, s4, s12\n\t"
      "s_addc_u32 s5, s5, s14\n\t"
      "s_add_u32 s6, s6, s13\n\t"
      "s_addc_u32 s7, s7, -1\n\t"
      "s_add_u32 m0, s9, 0\n\t"
      "s_nop 0\n\t"
      "global_load_lds_dwordx4 v195, s[6:7] offset:2048\n\t"
      "global_load_lds_dwordx4 v192, s[4:5]\n\t"
      "global_load_lds_dwordx4 v193, s[4:5] offset:1024\n\t"
      "global_load_lds_dwordx4 v196, s[6:7] offset:3072\n\t"
      "s_add_u32 m0, s9, 4096\n\t"
      "s_nop 0\n\t"
      "global_load_lds_dwordx4 v197, s[6:7]\n\t"
      "s_sub_u32 s3, s3, 4\n\t"
      "s_cbranch_scc1 EX_%=\n\t"
      "s_cmp_eq_u32 s19, 0\n\t"
      "s_cselect_b32 s13, 16, 0\n\t"
      "s_lshl2_add_u32 s13, s3, s13\n\t"
      "v_and_or_b32 v3, v1, 3, s13\n\t"
      "v_bfe_u32 v4, v3, 1, 3\n\t"
      "v_mul_u32_u24_e32 v3, 896, v3\n\t"
      "s_cmp_eq_u32 s3, 0\n\t"
      "s_cselect_b32 s10, 0xffffff00, -1\n\t"
      "s_mov_b32 s11, 0\n\t"
      "v_xor_b32_e32 v4, v4, v2\n\t"
      "v_lshl_add_u32 v3, v4, 4, v3\n\t"
      "v_add_u32_e32 v194, 1024, v3\n\t"
      "s_lshl_b32 s12, s3, 9\n\t"
      "s_add_u32 s12, s12, 40960\n\t"
      "s_add_u32 m0, s12, 0\n\t"
      "s_mov_b64 exec, s[10:11]\n\t"
      "global_load_lds_dwordx4 v194, s[4:5]\n\t"
      "s_mov_b64 exec, -1\n\t"
      "s_sub_u32 s40, s4, 57344\n\t"
      "s_subb_u32 s41, s5, 0\n\t"
      "s_sub_u32 s42, s6, 86016\n\t"
      "s_subb_u32 s43, s7, 0\n\t"
      "s_add_u32 s40, s40, 128\n\t"
      "s_addc_u32 s41, s41, 0\n\t"
      "s_add_u32 s42, s42, 172032\n\t"
      "s_addc_u32 s43, s43, 0\n\t"
      "s_add_u32 m0, s9, 22528\n\t"
      "s_nop 0\n\t"
      "global_load_lds_dwordx4 v195, s[42:43] offset:2048\n\t"
      "global_load_lds_dwordx4 v192, s[40:41]\n\t"
      "global_load_lds_dwordx4 v193, s[40:41] offset:1024\n\t"
      "global_load_lds_dwordx4 v196, s[42:43] offset:3072\n\t"
      "s_add_u32 m0, s9, 26624\n\t"
      "s_nop 0\n\t"
      "global_load_lds_dwordx4 v197, s[42:43]\n\t"
      "s_branch EX_%=\n\t"
      "EB_%=:\n\t"
      "v_and_b32_e32 v3, 63, v0\n\t"
      "v_lshlrev_b32_e32 v3, 4, v3\n\t"
      "s_sub_u32 s18, s18, 16\n\t"
      "s_min_u32 s16, s3, 6\n\t"
      "s_lshl4_add_u32 s15, s17, s18\n\t"
      "s_mul_i32 s15, s15, 14336\n\t"
      "s_lshl_b32 s12, s16, 7\n\t"
      "s_add_u32 s15, s15, s12\n\t"
      "v_mul_u32_u24_e32 v6, 56, v3\n\t"
      "v_add_u32_e32 v6, s15, v6\n\t"
      "s_add_u32 s19, s17, 2\n\t"
      "s_and_b32 s19, s19, 3\n\t"
      "s_and_b32 s12, s17, 4\n\t"
      "s_or_b32 s19, s19, s12\n\t"
      "s_lshl4_add_u32 s19, s19, s18\n\t"
      "s_lshl3_add_u32 s12, s19, s3\n\t"
      "s_lshl_b32 s12, s12, 11\n\t"
      "s_mul_i32 s19, s19, 14336\n\t"
      "s_lshl_b32 s16, s16, 11\n\t"
      "s_add_u32 s19, s19, s16\n\t"
      "s_add_u32 s8, s16, 114688\n\t"
      "v_add_u32_e32 v1, s19, v3\n\t"
      "s_lshl4_add_u32 s16, s3, s18\n\t"
      "s_mul_i32 s13, s16, 6784\n\t"
      "s_add_u32 s13, s13, 344064\n\t"
      "s_mul_i32 s14, s16, 2688\n\t"
      "s_add_u32 s14, s14, 0\n\t"
      "v_min_u32_e32 v4, 832, v3\n\t"
      "v_lshl_add_u32 v4, v4, 3, s13\n\t"
      "v_min_u32_e32 v4, 1204096, v4\n\t"
      "v_min_u32_e32 v5, 320, v3\n\t"
      "v_lshl_add_u32 v5, v5, 3, s14\n\t"
      "s_mov_b32 m0, 163328\n\t"
      "s_waitcnt lgkmcnt(0)\n\t"
      "s_mov_b32 m0, s8\n\t"
      "s_nop 0\n\t"
      "global_load_lds_dwordx4 v1, s[4:5]\n\t"
      "global_load_lds_dwordx4 v1, s[4:5] offset:1024\n\t"
      "s_mov_b32 m0, 163328\n\t"
      "s_nop 0\n\t"
      "global_load_lds_dword v5, s[6:7]\n\t"
      "s_barrier\n\t"
      "global_load_lds_dword v4, s[6:7]\n\t"
      "s_mov_b64 exec, 0xffff\n\t"
      "global_load_lds_dword v6, s[4:5]\n\t"
      "s_mov_b64 exec, -1\n\t"
      "EX_%=:\n\t"
      : "={v[192:199]}"(f)
      : "{s[0:1]}"(__builtin_amdgcn_kernarg_segment_ptr()), "{s2}"(blockIdx.x), "{v0}"(threadIdx.x)
      : "memory", "scc", "s3", "s4", "s5", "s6", "s7", "s8", "s9", "s10", "s11", "s12", "s13", "s14", "s15", "s16", "s17", "s18", "s19", "v1", "v2",
        "v3", "v4", "v5", "v6", "s40", "s41", "s42", "s43", "s48", "s49", "s50"
      );
  PIN();
  char* const ws = ws0 + (-(unsigned)(uintptr_t)ws0 & ((2u << 20) - 1));
  const int wave = __builtin_amdgcn_readfirstlane(threadIdx.x >> 6);
  const int xcd = blockIdx.x & 7, idx = blockIdx.x >> 3, side = xcd >> 2, xp = xcd & 3;
#define LANE_BITS(l) \
  const int lane = (l), g = lane >> 4, c = lane & 15, q4 = c >> 2, p4 = lane & 3, lr = lane >> 3, lc = lane & 7

  if (__builtin_expect(idx < 16, 1)) {
    const int rt = 2 * xcd + (idx >> 3), hp = idx & 7, hh = (wave >> 1) & 1, half = wave & 1;
    const unsigned sta = A_ST + half * 1024;
    if (wave < 4) {
      LANE_BITS(threadIdx.x & 63);
      u32x16 v;
      u32x8 s;
      v[0] = 4 * half * A_UNIT + c * 128 + ((g ^ swz_row(c)) << 4);
      v[1] = A_HALO + c * 128 + ((g ^ swz_row(c)) << 4);
#pragma unroll
      for (int j = 0; j < 2; ++j) {
        const int by = 56 * hh + 32 * j + 8 * p4;
        v[2 + j] = g * A_UNIT + 2048 + q4 * 128 + (((by >> 4) ^ swz_tr(8 * g + q4)) << 4) + (by & 8);
      }
      const unsigned csa = A_CS + hh * 384, exa = A_EX + hh * 3 * 2048;
      v[4] = csa + g * 16;
      v[5] = csa + c * 4;
      v[6] = v[4] + 64 * half;
      v[7] = v[5] + 64 * half;
      v[8] = A_FL + (wave ^ 1) * 8;
      v[9] = sta + g * 16;
      v[10] = sta + c * 4;
      const int key = 4 * g + (c & 1);
      v[11] = sta + 384 + (key <= c ? 128 : 64) + key * 4;
      unsigned vk1 = sta + 384 + (key + 2 <= c ? 128 : 64) + key * 4 + 8;
      v[12] = exa + half * 2048 + lane * 16;
      v[13] = exa + half * 2048 + lane * 8;
      v[14] = exa + lane * 16 + 8 * half;
      v[15] = lane * 16;
      put(s, 0, (u64)(ws + WS_O + part(((8 * (rt & 7) + 4 * half) * 16 + 2 * hp + hh) * 1024, 0)));
      s[2] = half;
      s[3] = UNI(rt == 0 && half == 0 ? 0xff800000u : 0u);
      s[4] = exa + 1024 + 512 * half;
      s[5] = A_FL + 32 + 4 * half;
      put(s, 6, (u64)(ws + WS_ST));
      unsigned hint = ST_H2 + part(((((rt & 7) * 8 + hp) * 2 + half) * 2 + hh) * 64, side);
      unsigned chk = ST_CK + part((((rt & 7) * 2 + half) * 16 + 2 * hp + hh) * 256, side);
    asm volatile(
      "s_waitcnt lgkmcnt(0)\n\ts_nop 4\n\t"
      "v_xor_b32_e32 v248, 64, v224\n\t"
      "v_xor_b32_e32 v249, 64, v225\n\t"
      "s_mov_b32 s36, 0xaaaaaaaa\n\t"
      "s_mov_b32 s37, 0xaaaaaaaa\n\t"
      "s_mov_b32 s38, 0xfff0ffff\n\t"
      "s_mov_b32 s39, 0xf000ff00\n\t"
      "s_mov_b32 s40, 0xffc0fffc\n\t"
      "s_mov_b32 s41, 0xc000fc00\n\t"
      "s_mov_b32 s42, 0x0\n\t"
      "s_mov_b32 s43, 0xffff0000\n\t"
      "s_mov_b32 s44, 0x10001\n\t"
      "s_mov_b32 s45, 0x10001\n\t"
      "v_mov_b32_e32 v216, 0xffff\n\t"
      "v_mov_b32_e32 v217, 0xffff0000\n\t"
      "v_cndmask_b32_e64 v216, v216, v217, s[36:37]\n\t"
      "v_cndmask_b32_e64 v240, v216, 0, s[38:39]\n\t"
      "v_cndmask_b32_e64 v241, v216, 0, s[40:41]\n\t"
      "v_cndmask_b32_e64 v242, 0, v216, s[38:39]\n\t"
      "v_cndmask_b32_e64 v243, 0, v216, s[40:41]\n\t"
      "v_mov_b32_e32 v244, s7\n\t"
      "v_mov_b32_e32 v245, s7\n\t"
      "v_mov_b32_e32 v246, s7\n\t"
      "v_mov_b32_e32 v247, s7\n\t"
      "v_mov_b64_e32 v[0:1], 0\n\t"
      "v_mov_b64_e32 v[2:3], 0\n\t"
      "v_mov_b64_e32 v[4:5], 0\n\t"
      "v_mov_b64_e32 v[6:7], 0\n\t"
      "v_mov_b64_e32 v[8:9], 0\n\t"
      "v_mov_b64_e32 v[10:11], 0\n\t"
      "v_mov_b64_e32 v[12:13], 0\n\t"
      "v_mov_b64_e32 v[14:15], 0\n\t"
      "v_mov_b64_e32 v[16:17], 0\n\t"
      "v_mov_b64_e32 v[18:19], 0\n\t"
      "v_mov_b64_e32 v[20:21], 0\n\t"
      "v_mov_b64_e32 v[22:23], 0\n\t"
      "v_mov_b64_e32 v[24:25], 0\n\t"
      "v_mov_b64_e32 v[26:27], 0\n\t"
      "v_mov_b64_e32 v[28:29], 0\n\t"
      "v_mov_b64_e32 v[30:31], 0\n\t"
      "v_mov_b64_e32 v[32:33], 0\n\t"
      "v_mov_b64_e32 v[34:35], 0\n\t"
      "v_mov_b64_e32 v[36:37], 0\n\t"
      "v_mov_b64_e32 v[38:39], 0\n\t"
      "v_mov_b64_e32 v[40:41], 0\n\t"
      "v_mov_b64_e32 v[42:43], 0\n\t"
      "v_mov_b64_e32 v[44:45], 0\n\t"
      "v_mov_b64_e32 v[46:47], 0\n\t"
      "v_mov_b64_e32 v[48:49], 0\n\t"
      "v_mov_b64_e32 v[50:51], 0\n\t"
      "v_mov_b64_e32 v[52:53], 0\n\t"
      "v_mov_b64_e32 v[54:55], 0\n\t"
      "v_mov_b64_e32 v[56:57], 0\n\t"
      "v_mov_b64_e32 v[58:59], 0\n\t"
      "v_mov_b64_e32 v[60:61], 0\n\t"
      "v_mov_b64_e32 v[62:63], 0\n\t"
      "v_mov_b64_e32 v[64:65], 0\n\t"
      "v_mov_b64_e32 v[66:67], 0\n\t"
      "v_mov_b64_e32 v[68:69], 0\n\t"
      "v_mov_b64_e32 v[70:71], 0\n\t"
      "v_mov_b64_e32 v[72:73], 0\n\t"
      "v_mov_b64_e32 v[74:75], 0\n\t"
      "v_mov_b64_e32 v[76:77], 0\n\t"
      "v_mov_b64_e32 v[78:79], 0\n\t"
      "v_mov_b64_e32 v[80:81], 0\n\t"
      "v_mov_b64_e32 v[82:83], 0\n\t"
      "v_mov_b64_e32 v[84:85], 0\n\t"
      "v_mov_b64_e32 v[86:87], 0\n\t"
      "v_mov_b64_e32 v[88:89], 0\n\t"
      "v_mov_b64_e32 v[90:91], 0\n\t"
      "v_mov_b64_e32 v[92:93], 0\n\t"
      "v_mov_b64_e32 v[94:95], 0\n\t"
      "v_mov_b64_e32 v[96:97], 0\n\t"
      "v_mov_b64_e32 v[98:99], 0\n\t"
      "v_mov_b64_e32 v[100:101], 0\n\t"
      "v_mov_b64_e32 v[102:103], 0\n\t"
      "v_mov_b64_e32 v[104:105], 0\n\t"
      "v_mov_b64_e32 v[106:107], 0\n\t"
      "s_mov_b32 s47, 6\n\t"
      "s_cmp_eq_u32 s6, 0\n\t"
      "s_cbranch_scc0 LOOP1_%=\n\t"
      "s_waitcnt vmcnt(0)\n\t"
      "s_barrier\n\t"
      "ds_read_b64_tr_b16 v[108:109], v226\n\t"
      "ds_read_b64_tr_b16 v[110:111], v226 offset:512\n\t"
      "ds_read_b128 v[132:135], v224\n\t"
      "ds_read_b128 v[136:139], v224 offset:5120\n\t"
      "ds_read_b128 v[140:143], v224 offset:10240\n\t"
      "ds_read_b128 v[144:147], v224 offset:15360\n\t"
      "ds_read_b64_tr_b16 v[112:113], v227\n\t"
      "ds_read_b64_tr_b16 v[114:115], v227 offset:512\n\t"
      "ds_read_b128 v[148:151], v225\n\t"
      "ds_read_b64_tr_b16 v[116:117], v226 offset:1024\n\t"
      "ds_read_b64_tr_b16 v[118:119], v226 offset:1536\n\t"
      "ds_read_b64_tr_b16 v[120:121], v227 offset:1024\n\t"
      "ds_read_b64_tr_b16 v[122:123], v227 offset:1536\n\t"
      "ds_read_b64_tr_b16 v[124:125], v226 offset:2048\n\t"
      "ds_read_b64_tr_b16 v[126:127], v226 offset:2560\n\t"
      "ds_read_b64_tr_b16 v[128:129], v227 offset:2048\n\t"
      "ds_read_b64_tr_b16 v[130:131], v227 offset:2560\n\t"
      "s_branch EVEN0_%=\n\t"
      "s_waitcnt lgkmcnt(0)\n\t"
      ".p2align 6\n\t"
      "ODD0_%=:\n\t"
      "s_waitcnt lgkmcnt(0)\n\t"
      "v_mfma_f32_16x16x32_bf16 v[0:3], v[152:155], v[176:179], v[0:3]\n\t"
      "v_add_u32_e32 v224, s46, v224\n\t"
      "v_add_u32_e32 v226, s46, v226\n\t"
      "v_mfma_f32_16x16x32_bf16 v[24:27], v[152:155], v[180:183], v[24:27]\n\t"
      "v_add_u32_e32 v227, s46, v227\n\t"
      "v_add_u32_e32 v225, s46, v225\n\t"
      "v_mfma_f32_16x16x32_bf16 v[48:51], v[152:155], v[184:187], v[48:51]\n\t"
      "s_barrier\n\t"
      "ds_read_b64_tr_b16 v[108:109], v226\n\t"
      "v_mfma_f32_16x16x32_bf16 v[72:75], v[152:155], v[188:191], v[72:75]\n\t"
      "ds_read_b64_tr_b16 v[110:111], v226 offset:512\n\t"
      "v_mfma_f32_16x16x32_bf16 v[4:7], v[156:159], v[176:179], v[4:7]\n\t"
      "ds_read_b128 v[132:135], v224\n\t"
      "v_mfma_f32_16x16x32_bf16 v[28:31], v[156:159], v[180:183], v[28:31]\n\t"
      "ds_read_b128 v[136:139], v224 offset:5120\n\t"
      "v_mfma_f32_16x16x32_bf16 v[52:55], v[156:159], v[184:187], v[52:55]\n\t"
      "ds_read_b128 v[140:143], v224 offset:10240\n\t"
      "v_mfma_f32_16x16x32_bf16 v[76:79], v[156:159], v[188:191], v[76:79]\n\t"
      "ds_read_b128 v[144:147], v224 offset:15360\n\t"
      "v_mfma_f32_16x16x32_bf16 v[96:99], v[152:155], v[192:195], v[96:99]\n\t"
      "v_mfma_f32_16x16x32_bf16 v[8:11], v[160:163], v[176:179], v[8:11]\n\t"
      "ds_read_b64_tr_b16 v[112:113], v227\n\t"
      "v_mfma_f32_16x16x32_bf16 v[32:35], v[160:163], v[180:183], v[32:35]\n\t"
      "ds_read_b64_tr_b16 v[114:115], v227 offset:512\n\t"
      "v_mfma_f32_16x16x32_bf16 v[56:59], v[160:163], v[184:187], v[56:59]\n\t"
      "ds_read_b128 v[148:151], v225\n\t"
      "v_mfma_f32_16x16x32_bf16 v[80:83], v[160:163], v[188:191], v[80:83]\n\t"
      "ds_read_b64_tr_b16 v[116:117], v226 offset:1024\n\t"
      "v_mfma_f32_16x16x32_bf16 v[12:15], v[164:167], v[176:179], v[12:15]\n\t"
      "ds_read_b64_tr_b16 v[118:119], v226 offset:1536\n\t"
      "v_mfma_f32_16x16x32_bf16 v[36:39], v[164:167], v[180:183], v[36:39]\n\t"
      "ds_read_b64_tr_b16 v[120:121], v227 offset:1024\n\t"
      "v_mfma_f32_16x16x32_bf16 v[60:63], v[164:167], v[184:187], v[60:63]\n\t"
      "v_mfma_f32_16x16x32_bf16 v[84:87], v[164:167], v[188:191], v[84:87]\n\t"
      "ds_read_b64_tr_b16 v[122:123], v227 offset:1536\n\t"
      "v_mfma_f32_16x16x32_bf16 v[100:103], v[160:163], v[192:195], v[100:103]\n\t"
      "ds_read_b64_tr_b16 v[124:125], v226 offset:2048\n\t"
      "v_mfma_f32_16x16x32_bf16 v[16:19], v[176:179], v[168:171], v[16:19]\n\t"
      "ds_read_b64_tr_b16 v[126:127], v226 offset:2560\n\t"
      "v_mfma_f32_16x16x32_bf16 v[40:43], v[180:183], v[168:171], v[40:43]\n\t"
      "ds_read_b64_tr_b16 v[128:129], v227 offset:2048\n\t"
      "v_mfma_f32_16x16x32_bf16 v[64:67], v[184:187], v[168:171], v[64:67]\n\t"
      "ds_read_b64_tr_b16 v[130:131], v227 offset:2560\n\t"
      "v_mfma_f32_16x16x32_bf16 v[88:91], v[188:191], v[168:171], v[88:91]\n\t"
      "v_mfma_f32_16x16x32_bf16 v[20:23], v[176:179], v[172:175], v[20:23]\n\t"
      "v_mfma_f32_16x16x32_bf16 v[44:47], v[180:183], v[172:175], v[44:47]\n\t"
      "v_add_u32_e32 v248, s46, v248\n\t"
      "v_add_u32_e32 v249, s46, v249\n\t"
      "v_mfma_f32_16x16x32_bf16 v[68:71], v[184:187], v[172:175], v[68:71]\n\t"
      "v_mfma_f32_16x16x32_bf16 v[92:95], v[188:191], v[172:175], v[92:95]\n\t"
      "v_mfma_f32_16x16x32_bf16 v[104:107], v[192:195], v[168:171], v[104:107]\n\t"
      "EVEN0_%=:\n\t"
      "s_waitcnt lgkmcnt(0)\n\t"
      "v_mfma_f32_16x16x32_bf16 v[0:3], v[108:111], v[132:135], v[0:3]\n\t"
      "ds_read_b64_tr_b16 v[152:153], v226 offset:20480\n\t"
      "v_mfma_f32_16x16x32_bf16 v[24:27], v[108:111], v[136:139], v[24:27]\n\t"
      "ds_read_b64_tr_b16 v[154:155], v226 offset:20992\n\t"
      "v_mfma_f32_16x16x32_bf16 v[48:51], v[108:111], v[140:143], v[48:51]\n\t"
      "ds_read_b128 v[176:179], v248\n\t"
      "v_mfma_f32_16x16x32_bf16 v[72:75], v[108:111], v[144:147], v[72:75]\n\t"
      "ds_read_b128 v[180:183], v248 offset:5120\n\t"
      "v_mfma_f32_16x16x32_bf16 v[4:7], v[112:115], v[132:135], v[4:7]\n\t"
      "v_mfma_f32_16x16x32_bf16 v[28:31], v[112:115], v[136:139], v[28:31]\n\t"
      "ds_read_b128 v[184:187], v248 offset:10240\n\t"
      "v_mfma_f32_16x16x32_bf16 v[52:55], v[112:115], v[140:143], v[52:55]\n\t"
      "ds_read_b128 v[188:191], v248 offset:15360\n\t"
      "v_mfma_f32_16x16x32_bf16 v[76:79], v[112:115], v[144:147], v[76:79]\n\t"
      "ds_read_b64_tr_b16 v[156:157], v227 offset:20480\n\t"
      "v_mfma_f32_16x16x32_bf16 v[96:99], v[108:111], v[148:151], v[96:99]\n\t"
      "v_mfma_f32_16x16x32_bf16 v[8:11], v[116:119], v[132:135], v[8:11]\n\t"
      "ds_read_b64_tr_b16 v[158:159], v227 offset:20992\n\t"
      "v_mfma_f32_16x16x32_bf16 v[32:35], v[116:119], v[136:139], v[32:35]\n\t"
      "ds_read_b128 v[192:195], v249\n\t"
      "v_mfma_f32_16x16x32_bf16 v[56:59], v[116:119], v[140:143], v[56:59]\n\t"
      "ds_read_b64_tr_b16 v[160:161], v226 offset:21504\n\t"
      "v_mfma_f32_16x16x32_bf16 v[80:83], v[116:119], v[144:147], v[80:83]\n\t"
      "ds_read_b64_tr_b16 v[162:163], v226 offset:22016\n\t"
      "v_mfma_f32_16x16x32_bf16 v[12:15], v[120:123], v[132:135], v[12:15]\n\t"
      "v_mfma_f32_16x16x32_bf16 v[36:39], v[120:123], v[136:139], v[36:39]\n\t"
      "ds_read_b64_tr_b16 v[164:165], v227 offset:21504\n\t"
      "v_mfma_f32_16x16x32_bf16 v[60:63], v[120:123], v[140:143], v[60:63]\n\t"
      "ds_read_b64_tr_b16 v[166:167], v227 offset:22016\n\t"
      "v_mfma_f32_16x16x32_bf16 v[84:87], v[120:123], v[144:147], v[84:87]\n\t"
      "ds_read_b64_tr_b16 v[168:169], v226 offset:22528\n\t"
      "v_mfma_f32_16x16x32_bf16 v[100:103], v[116:119], v[148:151], v[100:103]\n\t"
      "v_mfma_f32_16x16x32_bf16 v[16:19], v[132:135], v[124:127], v[16:19]\n\t"
      "ds_read_b64_tr_b16 v[170:171], v226 offset:23040\n\t"
      "v_mfma_f32_16x16x32_bf16 v[40:43], v[136:139], v[124:127], v[40:43]\n\t"
      "ds_read_b64_tr_b16 v[172:173], v227 offset:22528\n\t"
      "v_mfma_f32_16x16x32_bf16 v[64:67], v[140:143], v[124:127], v[64:67]\n\t"
      "ds_read_b64_tr_b16 v[174:175], v227 offset:23040\n\t"
      "s_cmp_eq_u32 s47, 4\n\t"
      "s_cselect_b32 s46, -2, 1\n\t"
      "v_mfma_f32_16x16x32_bf16 v[88:91], v[144:147], v[124:127], v[88:91]\n\t"
      "s_cmp_eq_u32 s47, 1\n\t"
      "s_cselect_b32 s46, -2, s46\n\t"
      "v_mfma_f32_16x16x32_bf16 v[20:23], v[132:135], v[128:131], v[20:23]\n\t"
      "s_mul_i32 s46, s46, 43008\n\t"
      "v_mfma_f32_16x16x32_bf16 v[44:47], v[136:139], v[128:131], v[44:47]\n\t"
      "v_mfma_f32_16x16x32_bf16 v[68:71], v[140:143], v[128:131], v[68:71]\n\t"
      "v_mfma_f32_16x16x32_bf16 v[92:95], v[144:147], v[128:131], v[92:95]\n\t"
      "s_sub_u32 s47, s47, 1\n\t"
      "v_mfma_f32_16x16x32_bf16 v[104:107], v[148:151], v[124:127], v[104:107]\n\t"
      "s_cbranch_scc0 ODD0_%=\n\t"
      "s_waitcnt lgkmcnt(0)\n\t"
      "v_mfma_f32_16x16x32_bf16 v[96:99], v[152:155], v[192:195], v[96:99]\n\t"
      "v_mov_b32_e32 v212, s9\n\t"
      "ds_read_b32 v212, v212\n\t"
      "ds_read2_b32 v[128:129], v234 offset0:32 offset1:32\n\t"
      "v_mfma_f32_16x16x32_bf16 v[100:103], v[160:163], v[192:195], v[100:103]\n\t"
      "ds_read2_b32 v[130:131], v234 offset0:48 offset1:48\n\t"
      "v_mfma_f32_16x16x32_bf16 v[104:107], v[192:195], v[168:171], v[104:107]\n\t"
      "ds_read2_b32 v[132:133], v234 offset0:64 offset1:64\n\t"
      "ds_read2_b32 v[134:135], v234 offset0:80 offset1:80\n\t"
      "v_mfma_f32_16x16x32_bf16 v[80:83], v[160:163], v[188:191], v[80:83]\n\t"
      "ds_read2_b32 v[248:249], v234 offset0:0 offset1:0\n\t"
      "v_mfma_f32_16x16x32_bf16 v[84:87], v[164:167], v[188:191], v[84:87]\n\t"
      "ds_read_b128 v[196:199], v233\n\t"
      "ds_read_b128 v[136:139], v233 offset:128\n\t"
      "v_mfma_f32_16x16x32_bf16 v[88:91], v[188:191], v[168:171], v[88:91]\n\t"
      "ds_read_b128 v[140:143], v233 offset:192\n\t"
      "v_mfma_f32_16x16x32_bf16 v[92:95], v[188:191], v[172:175], v[92:95]\n\t"
      "ds_read_b128 v[144:147], v233 offset:256\n\t"
      "ds_read_b128 v[148:151], v233 offset:320\n\t"
      "v_mfma_f32_16x16x32_bf16 v[56:59], v[160:163], v[184:187], v[56:59]\n\t"
      "s_mov_b64 exec, s[44:45]\n\t"
      "ds_write_b128 v230, v[96:99]\n\t"
      "ds_write_b128 v230, v[100:103] offset:128\n\t"
      "s_mov_b64 exec, 0xffff\n\t"
      "ds_write_b32 v231, v104 offset:256\n\t"
      "v_xor_b32_e32 v227, 8, v232\n\t"
      "ds_write_b32 v227, v227\n\t"
      "s_mov_b64 exec, -1\n\t"
      "v_mfma_f32_16x16x32_bf16 v[60:63], v[164:167], v[184:187], v[60:63]\n\t"
      "v_mfma_f32_16x16x32_bf16 v[64:67], v[184:187], v[168:171], v[64:67]\n\t"
      "v_mfma_f32_16x16x32_bf16 v[68:71], v[184:187], v[172:175], v[68:71]\n\t"
      "s_waitcnt lgkmcnt(4)\n\t"
      "v_mfma_f32_16x16x32_bf16 v[32:35], v[160:163], v[180:183], v[32:35]\n\t"
      "ds_read_b32 v226, v232\n\t"
      "ds_read_b128 v[116:119], v228 offset:128\n\t"
      "v_mfma_f32_16x16x32_bf16 v[36:39], v[164:167], v[180:183], v[36:39]\n\t"
      "ds_read_b128 v[120:123], v228 offset:192\n\t"
      "ds_read2_b32 v[250:251], v229 offset0:64 offset1:64\n\t"
      "v_mfma_f32_16x16x32_bf16 v[40:43], v[180:183], v[168:171], v[40:43]\n\t"
      "ds_read2_b32 v[252:253], v229 offset0:80 offset1:80\n\t"
      "ds_read_b128 v[124:127], v230 offset:128\n\t"
      "v_mfma_f32_16x16x32_bf16 v[44:47], v[180:183], v[172:175], v[44:47]\n\t"
      "ds_read2_b32 v[224:225], v231 offset0:64 offset1:64\n\t"
      "ds_read_b128 v[108:111], v228\n\t"
      "v_mfma_f32_16x16x32_bf16 v[8:11], v[160:163], v[176:179], v[8:11]\n\t"
      "ds_read_b128 v[112:115], v228 offset:64\n\t"
      "ds_read2_b32 v[204:205], v235 offset0:0 offset1:16\n\t"
      "v_mfma_f32_16x16x32_bf16 v[12:15], v[164:167], v[176:179], v[12:15]\n\t"
      "ds_read2_b32 v[206:207], v235 offset0:32 offset1:48\n\t"
      "ds_read2_b32 v[208:209], v254 offset0:0 offset1:16\n\t"
      "v_mfma_f32_16x16x32_bf16 v[16:19], v[176:179], v[168:171], v[16:19]\n\t"
      "ds_read2_b32 v[210:211], v254 offset0:32 offset1:48\n\t"
      "ds_read2_b32 v[200:201], v234 offset0:192 offset1:208\n\t"
      "v_mfma_f32_16x16x32_bf16 v[20:23], v[176:179], v[172:175], v[20:23]\n\t"
      "ds_read2_b32 v[202:203], v234 offset0:224 offset1:240\n\t"
      "LOOPX_%=:\n\t"
      "s_waitcnt lgkmcnt(14)\n\t"
      "v_min_u32_e32 v226, v226, v212\n\t"
      "v_cmp_eq_u32_e32 vcc, 0, v226\n\t"
      "s_cbranch_vccnz XAR_%=\n\t"
      "XAB_%=:\n\t"
      "s_waitcnt lgkmcnt(12)\n\t"
      "v_pk_fma_f32 v[80:81], v[134:135], v[116:117], v[80:81]\n\t"
      "v_pk_fma_f32 v[82:83], v[134:135], v[118:119], v[82:83]\n\t"
      "v_pk_fma_f32 v[84:85], v[134:135], v[120:121], v[84:85]\n\t"
      "v_pk_fma_f32 v[86:87], v[134:135], v[122:123], v[86:87]\n\t"
      "v_mfma_f32_16x16x32_bf16 v[72:75], v[152:155], v[188:191], v[72:75]\n\t"
      "v_cvt_pk_bf16_f32 v80, v80, v81\n\t"
      "v_cvt_pk_bf16_f32 v81, v82, v83\n\t"
      "v_cvt_pk_bf16_f32 v82, v84, v85\n\t"
      "v_cvt_pk_bf16_f32 v83, v86, v87\n\t"
      "v_mfma_f32_16x16x32_bf16 v[76:79], v[156:159], v[188:191], v[76:79]\n\t"
      "ds_write_b128 v236, v[80:83] offset:2048\n\t"
      "s_waitcnt lgkmcnt(11)\n\t"
      "v_pk_fma_f32 v[88:89], v[148:149], v[250:251], v[88:89]\n\t"
      "v_pk_fma_f32 v[90:91], v[150:151], v[250:251], v[90:91]\n\t"
      "v_pk_fma_f32 v[92:93], v[148:149], v[252:253], v[92:93]\n\t"
      "v_pk_fma_f32 v[94:95], v[150:151], v[252:253], v[94:95]\n\t"
      "v_mfma_f32_16x16x32_bf16 v[48:51], v[152:155], v[184:187], v[48:51]\n\t"
      "v_cvt_pk_bf16_f32 v216, v88, v89\n\t"
      "v_cvt_pk_bf16_f32 v217, v90, v91\n\t"
      "v_cvt_pk_bf16_f32 v218, v92, v93\n\t"
      "v_cvt_pk_bf16_f32 v219, v94, v95\n\t"
      "v_mfma_f32_16x16x32_bf16 v[52:55], v[156:159], v[184:187], v[52:55]\n\t"
      "ds_write_b64 v237, v[216:217] offset:3072\n\t"
      "ds_write_b64 v237, v[218:219] offset:3584\n\t"
      "s_waitcnt lgkmcnt(11)\n\t"
      "v_pk_fma_f32 v[100:101], v[248:249], v[124:125], v[100:101]\n\t"
      "v_pk_fma_f32 v[104:105], v[196:197], v[224:225], v[104:105]\n\t"
      "v_pk_fma_f32 v[102:103], v[248:249], v[126:127], v[102:103]\n\t"
      "v_pk_fma_f32 v[106:107], v[198:199], v[224:225], v[106:107]\n\t"
      "v_mfma_f32_16x16x32_bf16 v[24:27], v[152:155], v[180:183], v[24:27]\n\t"
      "v_cvt_pk_bf16_f32 v220, v100, v101\n\t"
      "v_cvt_pk_bf16_f32 v221, v102, v103\n\t"
      "v_cvt_pk_bf16_f32 v222, v104, v105\n\t"
      "v_cvt_pk_bf16_f32 v223, v106, v107\n\t"
      "v_mfma_f32_16x16x32_bf16 v[28:31], v[156:159], v[180:183], v[28:31]\n\t"
      "ds_write_b64 v238, v[220:221]\n\t"
      "v_lshrrev_b32_e32 v96, 1, v239\n\t"
      "v_add_u32_e32 v96, s8, v96\n\t"
      "ds_write_b64 v96, v[222:223]\n\t"
      "v_xor_b32_e32 v227, 8, v232\n\t"
      "ds_write_b32 v227, v227 offset:4\n\t"
      "v_pk_fma_f32 v[56:57], v[132:133], v[116:117], v[56:57]\n\t"
      "v_pk_fma_f32 v[58:59], v[132:133], v[118:119], v[58:59]\n\t"
      "v_pk_fma_f32 v[60:61], v[132:133], v[120:121], v[60:61]\n\t"
      "v_pk_fma_f32 v[62:63], v[132:133], v[122:123], v[62:63]\n\t"
      "v_mfma_f32_16x16x32_bf16 v[0:3], v[152:155], v[176:179], v[0:3]\n\t"
      "v_cvt_pk_bf16_f32 v56, v56, v57\n\t"
      "v_cvt_pk_bf16_f32 v57, v58, v59\n\t"
      "v_cvt_pk_bf16_f32 v58, v60, v61\n\t"
      "v_cvt_pk_bf16_f32 v59, v62, v63\n\t"
      "v_mfma_f32_16x16x32_bf16 v[4:7], v[156:159], v[176:179], v[4:7]\n\t"
      "v_mov_b32_e32 v164, v216\n\t"
      "v_mov_b32_e32 v165, v217\n\t"
      "v_mov_b32_e32 v174, v218\n\t"
      "v_mov_b32_e32 v175, v219\n\t"
      "s_waitcnt lgkmcnt(12)\n\t"
      "v_pk_fma_f32 v[72:73], v[134:135], v[108:109], v[72:73]\n\t"
      "v_pk_fma_f32 v[74:75], v[134:135], v[110:111], v[74:75]\n\t"
      "v_pk_fma_f32 v[76:77], v[134:135], v[112:113], v[76:77]\n\t"
      "v_pk_fma_f32 v[78:79], v[134:135], v[114:115], v[78:79]\n\t"
      "v_cvt_pk_bf16_f32 v72, v72, v73\n\t"
      "v_cvt_pk_bf16_f32 v73, v74, v75\n\t"
      "v_cvt_pk_bf16_f32 v74, v76, v77\n\t"
      "v_cvt_pk_bf16_f32 v75, v78, v79\n\t"
      "v_cndmask_b32_e64 v74, v74, 0, s[42:43]\n\t"
      "v_cndmask_b32_e64 v75, v75, 0, s[42:43]\n\t"
      "s_nop 1\n\t"
      "v_mfma_f32_16x16x32_bf16 v[76:79], v[56:59], v[72:75], 0\n\t"
      "v_mfma_f32_16x16x32_bf16 v[84:87], v[80:83], v[72:75], 0\n\t"
      "v_pk_fma_f32 v[32:33], v[130:131], v[116:117], v[32:33]\n\t"
      "v_pk_fma_f32 v[34:35], v[130:131], v[118:119], v[34:35]\n\t"
      "v_pk_fma_f32 v[36:37], v[130:131], v[120:121], v[36:37]\n\t"
      "v_pk_fma_f32 v[38:39], v[130:131], v[122:123], v[38:39]\n\t"
      "v_cvt_pk_bf16_f32 v32, v32, v33\n\t"
      "v_cvt_pk_bf16_f32 v33, v34, v35\n\t"
      "v_cvt_pk_bf16_f32 v34, v36, v37\n\t"
      "v_cvt_pk_bf16_f32 v35, v38, v39\n\t"
      "v_pk_fma_f32 v[48:49], v[132:133], v[108:109], v[48:49]\n\t"
      "v_pk_fma_f32 v[50:51], v[132:133], v[110:111], v[50:51]\n\t"
      "v_pk_fma_f32 v[52:53], v[132:133], v[112:113], v[52:53]\n\t"
      "v_pk_fma_f32 v[54:55], v[132:133], v[114:115], v[54:55]\n\t"
      "v_cvt_pk_bf16_f32 v48, v48, v49\n\t"
      "v_cvt_pk_bf16_f32 v49, v50, v51\n\t"
      "v_cvt_pk_bf16_f32 v50, v52, v53\n\t"
      "v_cvt_pk_bf16_f32 v51, v54, v55\n\t"
      "v_cndmask_b32_e64 v50, v50, 0, s[42:43]\n\t"
      "v_cndmask_b32_e64 v51, v51, 0, s[42:43]\n\t"
      "v_pk_fma_f32 v[64:65], v[144:145], v[250:251], v[64:65]\n\t"
      "v_pk_fma_f32 v[66:67], v[146:147], v[250:251], v[66:67]\n\t"
      "v_pk_fma_f32 v[68:69], v[144:145], v[252:253], v[68:69]\n\t"
      "v_pk_fma_f32 v[70:71], v[146:147], v[252:253], v[70:71]\n\t"
      "v_cvt_pk_bf16_f32 v162, v64, v65\n\t"
      "v_cvt_pk_bf16_f32 v163, v66, v67\n\t"
      "v_cvt_pk_bf16_f32 v172, v68, v69\n\t"
      "v_cvt_pk_bf16_f32 v173, v70, v71\n\t"
      "ds_read_b32 v226, v232 offset:4\n\t"
      "ds_read_b128 v[152:155], v236\n\t"
      "ds_read_b64 v[156:157], v237 offset:1024\n\t"
      "ds_read_b64 v[166:167], v237 offset:1536\n\t"
      "v_mfma_f32_16x16x32_bf16 v[52:55], v[32:35], v[48:51], 0\n\t"
      "v_mfma_f32_16x16x32_bf16 v[60:63], v[56:59], v[48:51], 0\n\t"
      "v_pk_fma_f32 v[8:9], v[128:129], v[116:117], v[8:9]\n\t"
      "v_pk_fma_f32 v[10:11], v[128:129], v[118:119], v[10:11]\n\t"
      "v_pk_fma_f32 v[12:13], v[128:129], v[120:121], v[12:13]\n\t"
      "v_pk_fma_f32 v[14:15], v[128:129], v[122:123], v[14:15]\n\t"
      "v_cvt_pk_bf16_f32 v8, v8, v9\n\t"
      "v_cvt_pk_bf16_f32 v9, v10, v11\n\t"
      "v_cvt_pk_bf16_f32 v10, v12, v13\n\t"
      "v_cvt_pk_bf16_f32 v11, v14, v15\n\t"
      "v_pk_fma_f32 v[24:25], v[130:131], v[108:109], v[24:25]\n\t"
      "v_pk_fma_f32 v[26:27], v[130:131], v[110:111], v[26:27]\n\t"
      "v_pk_fma_f32 v[28:29], v[130:131], v[112:113], v[28:29]\n\t"
      "v_pk_fma_f32 v[30:31], v[130:131], v[114:115], v[30:31]\n\t"
      "v_cvt_pk_bf16_f32 v24, v24, v25\n\t"
      "v_cvt_pk_bf16_f32 v25, v26, v27\n\t"
      "v_cvt_pk_bf16_f32 v26, v28, v29\n\t"
      "v_cvt_pk_bf16_f32 v27, v30, v31\n\t"
      "v_cndmask_b32_e64 v26, v26, 0, s[42:43]\n\t"
      "v_cndmask_b32_e64 v27, v27, 0, s[42:43]\n\t"
      "v_pk_fma_f32 v[40:41], v[140:141], v[250:251], v[40:41]\n\t"
      "v_pk_fma_f32 v[42:43], v[142:143], v[250:251], v[42:43]\n\t"
      "v_pk_fma_f32 v[44:45], v[140:141], v[252:253], v[44:45]\n\t"
      "v_pk_fma_f32 v[46:47], v[142:143], v[252:253], v[46:47]\n\t"
      "v_cvt_pk_bf16_f32 v160, v40, v41\n\t"
      "v_cvt_pk_bf16_f32 v161, v42, v43\n\t"
      "v_cvt_pk_bf16_f32 v170, v44, v45\n\t"
      "v_cvt_pk_bf16_f32 v171, v46, v47\n\t"
      "v_mfma_f32_16x16x32_bf16 v[28:31], v[8:11], v[24:27], 0\n\t"
      "v_mfma_f32_16x16x32_bf16 v[36:39], v[32:35], v[24:27], 0\n\t"
      "v_pk_fma_f32 v[0:1], v[128:129], v[108:109], v[0:1]\n\t"
      "v_pk_fma_f32 v[2:3], v[128:129], v[110:111], v[2:3]\n\t"
      "v_pk_fma_f32 v[4:5], v[128:129], v[112:113], v[4:5]\n\t"
      "v_pk_fma_f32 v[6:7], v[128:129], v[114:115], v[6:7]\n\t"
      "v_cvt_pk_bf16_f32 v0, v0, v1\n\t"
      "v_cvt_pk_bf16_f32 v1, v2, v3\n\t"
      "v_cvt_pk_bf16_f32 v2, v4, v5\n\t"
      "v_cvt_pk_bf16_f32 v3, v6, v7\n\t"
      "v_cndmask_b32_e64 v2, v2, 0, s[42:43]\n\t"
      "v_cndmask_b32_e64 v3, v3, 0, s[42:43]\n\t"
      "v_pk_fma_f32 v[16:17], v[136:137], v[250:251], v[16:17]\n\t"
      "v_pk_fma_f32 v[18:19], v[138:139], v[250:251], v[18:19]\n\t"
      "v_pk_fma_f32 v[20:21], v[136:137], v[252:253], v[20:21]\n\t"
      "v_pk_fma_f32 v[22:23], v[138:139], v[252:253], v[22:23]\n\t"
      "v_cvt_pk_bf16_f32 v158, v16, v17\n\t"
      "v_cvt_pk_bf16_f32 v159, v18, v19\n\t"
      "v_cvt_pk_bf16_f32 v168, v20, v21\n\t"
      "v_cvt_pk_bf16_f32 v169, v22, v23\n\t"
      "s_waitcnt lgkmcnt(3)\n\t"
      "v_cmp_eq_u32_e32 vcc, 0, v226\n\t"
      "s_cbranch_vccnz XBR_%=\n\t"
      "XBB_%=:\n\t"
      "s_waitcnt lgkmcnt(2)\n\t"
      "v_mfma_f32_16x16x32_bf16 v[4:7], v[152:155], v[0:3], v[244:247]\n\t"
      "v_mfma_f32_16x16x32_bf16 v[12:15], v[8:11], v[0:3], 0\n\t"
      "v_mov_b32_e32 v252, 154752\n\t"
      "ds_read_b64 v[250:251], v252 offset:8848\n\t"
      "ds_read_b64 v[196:197], v252\n\t"
      "s_and_b32 s46, s12, 0x2000\n\t"
      "v_cndmask_b32_e64 v76, v76, v77, s[36:37]\n\t"
      "v_cndmask_b32_e64 v84, v84, v85, s[36:37]\n\t"
      "v_cndmask_b32_e64 v78, v78, v79, s[36:37]\n\t"
      "v_cndmask_b32_e64 v86, v86, v87, s[36:37]\n\t"
      "v_cndmask_b32_e64 v179, v76, v84, s[38:39]\n\t"
      "v_cndmask_b32_e64 v183, v78, v86, s[40:41]\n\t"
      "v_cndmask_b32_e64 v52, v52, v53, s[36:37]\n\t"
      "v_cndmask_b32_e64 v60, v60, v61, s[36:37]\n\t"
      "v_cndmask_b32_e64 v54, v54, v55, s[36:37]\n\t"
      "v_cndmask_b32_e64 v62, v62, v63, s[36:37]\n\t"
      "v_cndmask_b32_e64 v178, v52, v60, s[38:39]\n\t"
      "v_cndmask_b32_e64 v182, v54, v62, s[40:41]\n\t"
      "v_pk_mul_f32 v[178:179], v[178:179], v[206:207]\n\t"
      "v_pk_mul_f32 v[182:183], v[182:183], v[210:211]\n\t"
      "v_max_f32_e32 v219, v179, v183\n\t"
      "v_max_f32_e32 v218, v178, v182\n\t"
      "v_cndmask_b32_e64 v28, v28, v29, s[36:37]\n\t"
      "v_cndmask_b32_e64 v36, v36, v37, s[36:37]\n\t"
      "v_cndmask_b32_e64 v30, v30, v31, s[36:37]\n\t"
      "v_cndmask_b32_e64 v38, v38, v39, s[36:37]\n\t"
      "v_cndmask_b32_e64 v177, v28, v36, s[38:39]\n\t"
      "v_cndmask_b32_e64 v181, v30, v38, s[40:41]\n\t"
      "v_cndmask_b32_e64 v4, v4, v5, s[36:37]\n\t"
      "v_cndmask_b32_e64 v12, v12, v13, s[36:37]\n\t"
      "v_cndmask_b32_e64 v6, v6, v7, s[36:37]\n\t"
      "v_cndmask_b32_e64 v14, v14, v15, s[36:37]\n\t"
      "v_cndmask_b32_e64 v176, v4, v12, s[38:39]\n\t"
      "v_cndmask_b32_e64 v180, v6, v14, s[40:41]\n\t"
      "v_pk_mul_f32 v[176:177], v[176:177], v[204:205]\n\t"
      "v_pk_mul_f32 v[180:181], v[180:181], v[208:209]\n\t"
      "v_max_f32_e32 v217, v177, v181\n\t"
      "v_max_f32_e32 v216, v176, v180\n\t"
      "v_permlane16_swap_b32_e32 v218, v219\n\t"
      "s_nop 0\n\t"
      "v_permlane16_swap_b32_e32 v216, v217\n\t"
      "v_max_f32_e32 v218, v218, v219\n\t"
      "v_max_f32_e32 v216, v216, v217\n\t"
      "v_mov_b32_e32 v219, v218\n\t"
      "v_mov_b32_e32 v217, v216\n\t"
      "s_nop 0\n\t"
      "v_permlane32_swap_b32_e32 v218, v219\n\t"
      "v_permlane32_swap_b32_e32 v216, v217\n\t"
      "v_max_f32_e32 v218, v218, v219\n\t"
      "v_max_f32_e32 v216, v216, v217\n\t"
      "v_mov_b32_e32 v219, v218\n\t"
      "v_mov_b32_e32 v217, v216\n\t"
      "s_nop 0\n\t"
      "v_permlane16_swap_b32_e32 v218, v219\n\t"
      "v_permlane16_swap_b32_e32 v216, v217\n\t"
      "v_pk_add_f32 v[178:179], v[178:179], v[218:219] neg_lo:[0,1] neg_hi:[0,1]\n\t"
      "v_pk_add_f32 v[182:183], v[182:183], v[218:219] neg_lo:[0,1] neg_hi:[0,1]\n\t"
      "v_pk_add_f32 v[176:177], v[176:177], v[216:217] neg_lo:[0,1] neg_hi:[0,1]\n\t"
      "v_pk_add_f32 v[180:181], v[180:181], v[216:217] neg_lo:[0,1] neg_hi:[0,1]\n\t"
      "v_pk_mul_f32 v[178:179], v[178:179], v[202:203]\n\t"
      "v_pk_mul_f32 v[182:183], v[182:183], v[202:203]\n\t"
      "v_pk_mul_f32 v[176:177], v[176:177], v[200:201]\n\t"
      "v_pk_mul_f32 v[180:181], v[180:181], v[200:201]\n\t"
      "v_exp_f32_e32 v179, v179\n\t"
      "v_exp_f32_e32 v183, v183\n\t"
      "v_exp_f32_e32 v178, v178\n\t"
      "v_exp_f32_e32 v182, v182\n\t"
      "v_exp_f32_e32 v177, v177\n\t"
      "v_exp_f32_e32 v181, v181\n\t"
      "v_exp_f32_e32 v176, v176\n\t"
      "v_exp_f32_e32 v180, v180\n\t"
      "s_waitcnt lgkmcnt(0)\n\t"
      "v_xor_b32_e32 v253, s12, v197\n\t"
      "s_mov_b64 exec, 1\n\t"
      "global_store_dwordx2 v253, v[250:251], s[10:11] sc1\n\t"
      "s_mov_b64 exec, -1\n\t"
      "v_pk_add_f32 v[218:219], v[178:179], v[182:183]\n\t"
      "v_pk_mul_f32 v[178:179], v[178:179], v[206:207]\n\t"
      "v_pk_mul_f32 v[182:183], v[182:183], v[210:211]\n\t"
      "v_cvt_pk_bf16_f32 v179, v179, v179\n\t"
      "v_cvt_pk_bf16_f32 v183, v183, v183\n\t"
      "v_and_b32_e32 v72, v240, v179\n\t"
      "v_and_b32_e32 v73, v241, v183\n\t"
      "v_and_b32_e32 v74, v242, v179\n\t"
      "v_and_b32_e32 v75, v243, v183\n\t"
      "v_cvt_pk_bf16_f32 v178, v178, v178\n\t"
      "v_cvt_pk_bf16_f32 v182, v182, v182\n\t"
      "v_and_b32_e32 v48, v240, v178\n\t"
      "v_and_b32_e32 v49, v241, v182\n\t"
      "v_and_b32_e32 v50, v242, v178\n\t"
      "v_and_b32_e32 v51, v243, v182\n\t"
      "v_mfma_f32_16x16x32_bf16 v[88:91], v[162:165], v[72:75], 0\n\t"
      "v_mfma_f32_16x16x32_bf16 v[92:95], v[172:175], v[72:75], 0\n\t"
      "v_pk_add_f32 v[216:217], v[176:177], v[180:181]\n\t"
      "v_pk_mul_f32 v[176:177], v[176:177], v[204:205]\n\t"
      "v_pk_mul_f32 v[180:181], v[180:181], v[208:209]\n\t"
      "v_cvt_pk_bf16_f32 v177, v177, v177\n\t"
      "v_cvt_pk_bf16_f32 v181, v181, v181\n\t"
      "v_and_b32_e32 v24, v240, v177\n\t"
      "v_and_b32_e32 v25, v241, v181\n\t"
      "v_and_b32_e32 v26, v242, v177\n\t"
      "v_and_b32_e32 v27, v243, v181\n\t"
      "v_mfma_f32_16x16x32_bf16 v[64:67], v[160:163], v[48:51], 0\n\t"
      "v_mfma_f32_16x16x32_bf16 v[68:71], v[170:173], v[48:51], 0\n\t"
      "v_cvt_pk_bf16_f32 v176, v176, v176\n\t"
      "v_cvt_pk_bf16_f32 v180, v180, v180\n\t"
      "v_and_b32_e32 v0, v240, v176\n\t"
      "v_and_b32_e32 v1, v241, v180\n\t"
      "v_and_b32_e32 v2, v242, v176\n\t"
      "v_and_b32_e32 v3, v243, v180\n\t"
      "v_mfma_f32_16x16x32_bf16 v[40:43], v[158:161], v[24:27], 0\n\t"
      "v_mfma_f32_16x16x32_bf16 v[44:47], v[168:171], v[24:27], 0\n\t"
      "s_add_u32 s48, s4, 98304\n\t"
      "s_addc_u32 s49, s5, 0\n\t"
      "v_mfma_f32_16x16x32_bf16 v[16:19], v[156:159], v[0:3], 0\n\t"
      "v_mfma_f32_16x16x32_bf16 v[20:23], v[166:169], v[0:3], 0\n\t"
      "v_permlane16_swap_b32_e32 v218, v219\n\t"
      "v_permlane16_swap_b32_e32 v216, v217\n\t"
      "v_add_f32_e32 v218, v218, v219\n\t"
      "v_add_f32_e32 v216, v216, v217\n\t"
      "v_mov_b32_e32 v219, v218\n\t"
      "v_mov_b32_e32 v217, v216\n\t"
      "s_nop 0\n\t"
      "v_permlane32_swap_b32_e32 v218, v219\n\t"
      "v_permlane32_swap_b32_e32 v216, v217\n\t"
      "v_add_f32_e32 v218, v218, v219\n\t"
      "v_add_f32_e32 v216, v216, v217\n\t"
      "v_mov_b32_e32 v219, v218\n\t"
      "v_mov_b32_e32 v217, v216\n\t"
      "s_nop 0\n\t"
      "v_permlane16_swap_b32_e32 v218, v219\n\t"
      "v_permlane16_swap_b32_e32 v216, v217\n\t"
      "v_rcp_f32_e32 v190, v219\n\t"
      "v_rcp_f32_e32 v188, v218\n\t"
      "v_rcp_f32_e32 v186, v217\n\t"
      "v_rcp_f32_e32 v184, v216\n\t"
      "v_mov_b32_e32 v191, v190\n\t"
      "v_mov_b32_e32 v189, v188\n\t"
      "v_mov_b32_e32 v187, v186\n\t"
      "v_mov_b32_e32 v185, v184\n\t"
      "v_lshrrev_b32_e32 v252, 2, v239\n\t"
      "v_xad_u32 v252, s13, v197, v252\n\t"
      "v_xad_u32 v196, s46, v196, v239\n\t"
      "v_pk_mul_f32 v[88:89], v[190:191], v[88:89]\n\t"
      "v_pk_mul_f32 v[90:91], v[190:191], v[90:91]\n\t"
      "v_pk_mul_f32 v[92:93], v[190:191], v[92:93]\n\t"
      "v_pk_mul_f32 v[94:95], v[190:191], v[94:95]\n\t"
      "v_cvt_pk_bf16_f32 v80, v88, v89\n\t"
      "v_cvt_pk_bf16_f32 v81, v90, v91\n\t"
      "v_cvt_pk_bf16_f32 v82, v92, v93\n\t"
      "v_cvt_pk_bf16_f32 v83, v94, v95\n\t"
      "global_store_dwordx4 v196, v[80:83], s[48:49] sc1\n\t"
      "s_sub_u32 s48, s48, 32768\n\t"
      "s_subb_u32 s49, s49, 0\n\t"
      "v_add3_u32 v219, v80, v81, v82\n\t"
      "v_add_u32_e32 v219, v219, v83\n\t"
      "v_pk_mul_f32 v[64:65], v[188:189], v[64:65]\n\t"
      "v_pk_mul_f32 v[66:67], v[188:189], v[66:67]\n\t"
      "v_pk_mul_f32 v[68:69], v[188:189], v[68:69]\n\t"
      "v_pk_mul_f32 v[70:71], v[188:189], v[70:71]\n\t"
      "v_cvt_pk_bf16_f32 v56, v64, v65\n\t"
      "v_cvt_pk_bf16_f32 v57, v66, v67\n\t"
      "v_cvt_pk_bf16_f32 v58, v68, v69\n\t"
      "v_cvt_pk_bf16_f32 v59, v70, v71\n\t"
      "global_store_dwordx4 v196, v[56:59], s[48:49] sc1\n\t"
      "s_sub_u32 s48, s48, 32768\n\t"
      "s_subb_u32 s49, s49, 0\n\t"
      "v_add3_u32 v218, v56, v57, v58\n\t"
      "v_add_u32_e32 v218, v218, v59\n\t"
      "v_pk_mul_f32 v[40:41], v[186:187], v[40:41]\n\t"
      "v_pk_mul_f32 v[42:43], v[186:187], v[42:43]\n\t"
      "v_pk_mul_f32 v[44:45], v[186:187], v[44:45]\n\t"
      "v_pk_mul_f32 v[46:47], v[186:187], v[46:47]\n\t"
      "v_cvt_pk_bf16_f32 v32, v40, v41\n\t"
      "v_cvt_pk_bf16_f32 v33, v42, v43\n\t"
      "v_cvt_pk_bf16_f32 v34, v44, v45\n\t"
      "v_cvt_pk_bf16_f32 v35, v46, v47\n\t"
      "global_store_dwordx4 v196, v[32:35], s[48:49] sc1\n\t"
      "s_sub_u32 s48, s48, 32768\n\t"
      "s_subb_u32 s49, s49, 0\n\t"
      "v_add3_u32 v217, v32, v33, v34\n\t"
      "v_add_u32_e32 v217, v217, v35\n\t"
      "s_nop 1\n\t"
      "v_permlane32_swap_b32_e32 v217, v219\n\t"
      "v_add_u32_e32 v217, v217, v219\n\t"
      "v_pk_mul_f32 v[16:17], v[184:185], v[16:17]\n\t"
      "v_pk_mul_f32 v[18:19], v[184:185], v[18:19]\n\t"
      "v_pk_mul_f32 v[20:21], v[184:185], v[20:21]\n\t"
      "v_pk_mul_f32 v[22:23], v[184:185], v[22:23]\n\t"
      "v_cvt_pk_bf16_f32 v8, v16, v17\n\t"
      "v_cvt_pk_bf16_f32 v9, v18, v19\n\t"
      "v_cvt_pk_bf16_f32 v10, v20, v21\n\t"
      "v_cvt_pk_bf16_f32 v11, v22, v23\n\t"
      "global_store_dwordx4 v196, v[8:11], s[48:49] sc1\n\t"
      "v_add3_u32 v216, v8, v9, v10\n\t"
      "v_add_u32_e32 v216, v216, v11\n\t"
      "s_nop 1\n\t"
      "v_permlane32_swap_b32_e32 v216, v218\n\t"
      "v_add_u32_e32 v216, v216, v218\n\t"
      "s_nop 1\n\t"
      "v_permlane16_swap_b32_e32 v216, v217\n\t"
      "v_add3_u32 v216, v216, v217, v250\n\t"
      "global_store_dword v252, v216, s[10:11] sc1\n\t"
      "s_barrier\n\t"
      "s_nop 1\n\t"
      "s_branch CEND_%=\n\t"
      "LOOP1_%=:\n\t"
      "s_waitcnt vmcnt(0)\n\t"
      "s_barrier\n\t"
      "ds_read_b64_tr_b16 v[108:109], v226\n\t"
      "ds_read_b64_tr_b16 v[110:111], v226 offset:512\n\t"
      "ds_read_b128 v[132:135], v224\n\t"
      "ds_read_b128 v[136:139], v224 offset:5120\n\t"
      "ds_read_b128 v[140:143], v224 offset:10240\n\t"
      "ds_read_b128 v[144:147], v224 offset:15360\n\t"
      "ds_read_b64_tr_b16 v[112:113], v227\n\t"
      "ds_read_b64_tr_b16 v[114:115], v227 offset:512\n\t"
      "ds_read_b128 v[148:151], v225\n\t"
      "ds_read_b64_tr_b16 v[116:117], v226 offset:1024\n\t"
      "ds_read_b64_tr_b16 v[118:119], v226 offset:1536\n\t"
      "ds_read_b64_tr_b16 v[120:121], v227 offset:1024\n\t"
      "ds_read_b64_tr_b16 v[122:123], v227 offset:1536\n\t"
      "ds_read_b64_tr_b16 v[124:125], v226 offset:2048\n\t"
      "ds_read_b64_tr_b16 v[126:127], v226 offset:2560\n\t"
      "ds_read_b64_tr_b16 v[128:129], v227 offset:2048\n\t"
      "ds_read_b64_tr_b16 v[130:131], v227 offset:2560\n\t"
      "s_branch EVEN1_%=\n\t"
      "s_waitcnt lgkmcnt(0)\n\t"
      ".p2align 6\n\t"
      "ODD1_%=:\n\t"
      "s_waitcnt lgkmcnt(0)\n\t"
      "v_mfma_f32_16x16x32_bf16 v[0:3], v[152:155], v[176:179], v[0:3]\n\t"
      "v_add_u32_e32 v224, s46, v224\n\t"
      "v_add_u32_e32 v226, s46, v226\n\t"
      "v_mfma_f32_16x16x32_bf16 v[24:27], v[152:155], v[180:183], v[24:27]\n\t"
      "v_add_u32_e32 v227, s46, v227\n\t"
      "v_add_u32_e32 v225, s46, v225\n\t"
      "v_mfma_f32_16x16x32_bf16 v[48:51], v[152:155], v[184:187], v[48:51]\n\t"
      "s_barrier\n\t"
      "ds_read_b64_tr_b16 v[108:109], v226\n\t"
      "v_mfma_f32_16x16x32_bf16 v[72:75], v[152:155], v[188:191], v[72:75]\n\t"
      "ds_read_b64_tr_b16 v[110:111], v226 offset:512\n\t"
      "v_mfma_f32_16x16x32_bf16 v[4:7], v[156:159], v[176:179], v[4:7]\n\t"
      "ds_read_b128 v[132:135], v224\n\t"
      "v_mfma_f32_16x16x32_bf16 v[28:31], v[156:159], v[180:183], v[28:31]\n\t"
      "ds_read_b128 v[136:139], v224 offset:5120\n\t"
      "v_mfma_f32_16x16x32_bf16 v[52:55], v[156:159], v[184:187], v[52:55]\n\t"
      "ds_read_b128 v[140:143], v224 offset:10240\n\t"
      "v_mfma_f32_16x16x32_bf16 v[76:79], v[156:159], v[188:191], v[76:79]\n\t"
      "ds_read_b128 v[144:147], v224 offset:15360\n\t"
      "v_mfma_f32_16x16x32_bf16 v[96:99], v[156:159], v[192:195], v[96:99]\n\t"
      "v_mfma_f32_16x16x32_bf16 v[8:11], v[160:163], v[176:179], v[8:11]\n\t"
      "ds_read_b64_tr_b16 v[112:113], v227\n\t"
      "v_mfma_f32_16x16x32_bf16 v[32:35], v[160:163], v[180:183], v[32:35]\n\t"
      "ds_read_b64_tr_b16 v[114:115], v227 offset:512\n\t"
      "v_mfma_f32_16x16x32_bf16 v[56:59], v[160:163], v[184:187], v[56:59]\n\t"
      "ds_read_b128 v[148:151], v225\n\t"
      "v_mfma_f32_16x16x32_bf16 v[80:83], v[160:163], v[188:191], v[80:83]\n\t"
      "ds_read_b64_tr_b16 v[116:117], v226 offset:1024\n\t"
      "v_mfma_f32_16x16x32_bf16 v[12:15], v[164:167], v[176:179], v[12:15]\n\t"
      "ds_read_b64_tr_b16 v[118:119], v226 offset:1536\n\t"
      "v_mfma_f32_16x16x32_bf16 v[36:39], v[164:167], v[180:183], v[36:39]\n\t"
      "ds_read_b64_tr_b16 v[120:121], v227 offset:1024\n\t"
      "v_mfma_f32_16x16x32_bf16 v[60:63], v[164:167], v[184:187], v[60:63]\n\t"
      "v_mfma_f32_16x16x32_bf16 v[84:87], v[164:167], v[188:191], v[84:87]\n\t"
      "ds_read_b64_tr_b16 v[122:123], v227 offset:1536\n\t"
      "v_mfma_f32_16x16x32_bf16 v[100:103], v[164:167], v[192:195], v[100:103]\n\t"
      "ds_read_b64_tr_b16 v[124:125], v226 offset:2048\n\t"
      "v_mfma_f32_16x16x32_bf16 v[16:19], v[176:179], v[168:171], v[16:19]\n\t"
      "ds_read_b64_tr_b16 v[126:127], v226 offset:2560\n\t"
      "v_mfma_f32_16x16x32_bf16 v[40:43], v[180:183], v[168:171], v[40:43]\n\t"
      "ds_read_b64_tr_b16 v[128:129], v227 offset:2048\n\t"
      "v_mfma_f32_16x16x32_bf16 v[64:67], v[184:187], v[168:171], v[64:67]\n\t"
      "ds_read_b64_tr_b16 v[130:131], v227 offset:2560\n\t"
      "v_mfma_f32_16x16x32_bf16 v[88:91], v[188:191], v[168:171], v[88:91]\n\t"
      "v_mfma_f32_16x16x32_bf16 v[20:23], v[176:179], v[172:175], v[20:23]\n\t"
      "v_mfma_f32_16x16x32_bf16 v[44:47], v[180:183], v[172:175], v[44:47]\n\t"
      "v_add_u32_e32 v248, s46, v248\n\t"
      "v_add_u32_e32 v249, s46, v249\n\t"
      "v_mfma_f32_16x16x32_bf16 v[68:71], v[184:187], v[172:175], v[68:71]\n\t"
      "v_mfma_f32_16x16x32_bf16 v[92:95], v[188:191], v[172:175], v[92:95]\n\t"
      "v_mfma_f32_16x16x32_bf16 v[104:107], v[192:195], v[172:175], v[104:107]\n\t"
      "EVEN1_%=:\n\t"
      "s_waitcnt lgkmcnt(0)\n\t"
      "v_mfma_f32_16x16x32_bf16 v[0:3], v[108:111], v[132:135], v[0:3]\n\t"
      "ds_read_b64_tr_b16 v[152:153], v226 offset:20480\n\t"
      "v_mfma_f32_16x16x32_bf16 v[24:27], v[108:111], v[136:139], v[24:27]\n\t"
      "ds_read_b64_tr_b16 v[154:155], v226 offset:20992\n\t"
      "v_mfma_f32_16x16x32_bf16 v[48:51], v[108:111], v[140:143], v[48:51]\n\t"
      "ds_read_b128 v[176:179], v248\n\t"
      "v_mfma_f32_16x16x32_bf16 v[72:75], v[108:111], v[144:147], v[72:75]\n\t"
      "ds_read_b128 v[180:183], v248 offset:5120\n\t"
      "v_mfma_f32_16x16x32_bf16 v[4:7], v[112:115], v[132:135], v[4:7]\n\t"
      "v_mfma_f32_16x16x32_bf16 v[28:31], v[112:115], v[136:139], v[28:31]\n\t"
      "ds_read_b128 v[184:187], v248 offset:10240\n\t"
      "v_mfma_f32_16x16x32_bf16 v[52:55], v[112:115], v[140:143], v[52:55]\n\t"
      "ds_read_b128 v[188:191], v248 offset:15360\n\t"
      "v_mfma_f32_16x16x32_bf16 v[76:79], v[112:115], v[144:147], v[76:79]\n\t"
      "ds_read_b64_tr_b16 v[156:157], v227 offset:20480\n\t"
      "v_mfma_f32_16x16x32_bf16 v[96:99], v[112:115], v[148:151], v[96:99]\n\t"
      "v_mfma_f32_16x16x32_bf16 v[8:11], v[116:119], v[132:135], v[8:11]\n\t"
      "ds_read_b64_tr_b16 v[158:159], v227 offset:20992\n\t"
      "v_mfma_f32_16x16x32_bf16 v[32:35], v[116:119], v[136:139], v[32:35]\n\t"
      "ds_read_b128 v[192:195], v249\n\t"
      "v_mfma_f32_16x16x32_bf16 v[56:59], v[116:119], v[140:143], v[56:59]\n\t"
      "ds_read_b64_tr_b16 v[160:161], v226 offset:21504\n\t"
      "v_mfma_f32_16x16x32_bf16 v[80:83], v[116:119], v[144:147], v[80:83]\n\t"
      "ds_read_b64_tr_b16 v[162:163], v226 offset:22016\n\t"
      "v_mfma_f32_16x16x32_bf16 v[12:15], v[120:123], v[132:135], v[12:15]\n\t"
      "v_mfma_f32_16x16x32_bf16 v[36:39], v[120:123], v[136:139], v[36:39]\n\t"
      "ds_read_b64_tr_b16 v[164:165], v227 offset:21504\n\t"
      "v_mfma_f32_16x16x32_bf16 v[60:63], v[120:123], v[140:143], v[60:63]\n\t"
      "ds_read_b64_tr_b16 v[166:167], v227 offset:22016\n\t"
      "v_mfma_f32_16x16x32_bf16 v[84:87], v[120:123], v[144:147], v[84:87]\n\t"
      "ds_read_b64_tr_b16 v[168:169], v226 offset:22528\n\t"
      "v_mfma_f32_16x16x32_bf16 v[100:103], v[120:123], v[148:151], v[100:103]\n\t"
      "v_mfma_f32_16x16x32_bf16 v[16:19], v[132:135], v[124:127], v[16:19]\n\t"
      "ds_read_b64_tr_b16 v[170:171], v226 offset:23040\n\t"
      "v_mfma_f32_16x16x32_bf16 v[40:43], v[136:139], v[124:127], v[40:43]\n\t"
      "ds_read_b64_tr_b16 v[172:173], v227 offset:22528\n\t"
      "v_mfma_f32_16x16x32_bf16 v[64:67], v[140:143], v[124:127], v[64:67]\n\t"
      "ds_read_b64_tr_b16 v[174:175], v227 offset:23040\n\t"
      "s_cmp_eq_u32 s47, 4\n\t"
      "s_cselect_b32 s46, -2, 1\n\t"
      "v_mfma_f32_16x16x32_bf16 v[88:91], v[144:147], v[124:127], v[88:91]\n\t"
      "s_cmp_eq_u32 s47, 1\n\t"
      "s_cselect_b32 s46, -2, s46\n\t"
      "v_mfma_f32_16x16x32_bf16 v[20:23], v[132:135], v[128:131], v[20:23]\n\t"
      "s_mul_i32 s46, s46, 43008\n\t"
      "v_mfma_f32_16x16x32_bf16 v[44:47], v[136:139], v[128:131], v[44:47]\n\t"
      "v_mfma_f32_16x16x32_bf16 v[68:71], v[140:143], v[128:131], v[68:71]\n\t"
      "v_mfma_f32_16x16x32_bf16 v[92:95], v[144:147], v[128:131], v[92:95]\n\t"
      "s_sub_u32 s47, s47, 1\n\t"
      "v_mfma_f32_16x16x32_bf16 v[104:107], v[148:151], v[128:131], v[104:107]\n\t"
      "s_cbranch_scc0 ODD1_%=\n\t"
      "s_waitcnt lgkmcnt(0)\n\t"
      "v_mfma_f32_16x16x32_bf16 v[96:99], v[156:159], v[192:195], v[96:99]\n\t"
      "v_mov_b32_e32 v212, s9\n\t"
      "ds_read_b32 v212, v212\n\t"
      "ds_read2_b32 v[128:129], v234 offset0:32 offset1:32\n\t"
      "v_mfma_f32_16x16x32_bf16 v[100:103], v[164:167], v[192:195], v[100:103]\n\t"
      "ds_read2_b32 v[130:131], v234 offset0:48 offset1:48\n\t"
      "v_mfma_f32_16x16x32_bf16 v[104:107], v[192:195], v[172:175], v[104:107]\n\t"
      "ds_read2_b32 v[132:133], v234 offset0:64 offset1:64\n\t"
      "ds_read2_b32 v[134:135], v234 offset0:80 offset1:80\n\t"
      "v_mfma_f32_16x16x32_bf16 v[80:83], v[160:163], v[188:191], v[80:83]\n\t"
      "ds_read2_b32 v[248:249], v234 offset0:0 offset1:0\n\t"
      "v_mfma_f32_16x16x32_bf16 v[84:87], v[164:167], v[188:191], v[84:87]\n\t"
      "ds_read_b128 v[196:199], v233\n\t"
      "ds_read_b128 v[136:139], v233 offset:128\n\t"
      "v_mfma_f32_16x16x32_bf16 v[88:91], v[188:191], v[168:171], v[88:91]\n\t"
      "ds_read_b128 v[140:143], v233 offset:192\n\t"
      "v_mfma_f32_16x16x32_bf16 v[92:95], v[188:191], v[172:175], v[92:95]\n\t"
      "ds_read_b128 v[144:147], v233 offset:256\n\t"
      "ds_read_b128 v[148:151], v233 offset:320\n\t"
      "v_mfma_f32_16x16x32_bf16 v[56:59], v[160:163], v[184:187], v[56:59]\n\t"
      "s_mov_b64 exec, s[44:45]\n\t"
      "ds_write_b128 v230, v[96:99]\n\t"
      "ds_write_b128 v230, v[100:103] offset:128\n\t"
      "s_mov_b64 exec, 0xffff\n\t"
      "ds_write_b32 v231, v104 offset:256\n\t"
      "v_xor_b32_e32 v227, 8, v232\n\t"
      "ds_write_b32 v227, v227\n\t"
      "s_mov_b64 exec, -1\n\t"
      "v_mfma_f32_16x16x32_bf16 v[60:63], v[164:167], v[184:187], v[60:63]\n\t"
      "v_mfma_f32_16x16x32_bf16 v[64:67], v[184:187], v[168:171], v[64:67]\n\t"
      "v_mfma_f32_16x16x32_bf16 v[68:71], v[184:187], v[172:175], v[68:71]\n\t"
      "s_waitcnt lgkmcnt(4)\n\t"
      "v_mfma_f32_16x16x32_bf16 v[32:35], v[160:163], v[180:183], v[32:35]\n\t"
      "ds_read_b32 v226, v232\n\t"
      "ds_read_b128 v[116:119], v228 offset:128\n\t"
      "v_mfma_f32_16x16x32_bf16 v[36:39], v[164:167], v[180:183], v[36:39]\n\t"
      "ds_read_b128 v[120:123], v228 offset:192\n\t"
      "ds_read2_b32 v[250:251], v229 offset0:64 offset1:64\n\t"
      "v_mfma_f32_16x16x32_bf16 v[40:43], v[180:183], v[168:171], v[40:43]\n\t"
      "ds_read2_b32 v[252:253], v229 offset0:80 offset1:80\n\t"
      "ds_read_b128 v[124:127], v230 offset:128\n\t"
      "v_mfma_f32_16x16x32_bf16 v[44:47], v[180:183], v[172:175], v[44:47]\n\t"
      "ds_read2_b32 v[224:225], v231 offset0:64 offset1:64\n\t"
      "ds_read_b128 v[108:111], v228\n\t"
      "v_mfma_f32_16x16x32_bf16 v[8:11], v[160:163], v[176:179], v[8:11]\n\t"
      "ds_read_b128 v[112:115], v228 offset:64\n\t"
      "ds_read2_b32 v[204:205], v235 offset0:0 offset1:16\n\t"
      "v_mfma_f32_16x16x32_bf16 v[12:15], v[164:167], v[176:179], v[12:15]\n\t"
      "ds_read2_b32 v[206:207], v235 offset0:32 offset1:48\n\t"
      "ds_read2_b32 v[208:209], v254 offset0:0 offset1:16\n\t"
      "v_mfma_f32_16x16x32_bf16 v[16:19], v[176:179], v[168:171], v[16:19]\n\t"
      "ds_read2_b32 v[210:211], v254 offset0:32 offset1:48\n\t"
      "ds_read2_b32 v[200:201], v234 offset0:192 offset1:208\n\t"
      "v_mfma_f32_16x16x32_bf16 v[20:23], v[176:179], v[172:175], v[20:23]\n\t"
      "ds_read2_b32 v[202:203], v234 offset0:224 offset1:240\n\t"
      "s_branch LOOPX_%=\n\t"
      "XAR_%=:\n\t"
      "s_mov_b32 s51, 0\n\t"
      "XAL_%=:\n\t"
      "s_add_u32 s51, s51, 1\n\t"
      "s_cmp_lt_u32 s51, 262144\n\t"
      "s_cbranch_scc0 XAX_%=\n\t"
      "s_waitcnt lgkmcnt(0)\n\t"
      "v_mov_b32_e32 v212, s9\n\t"
      "ds_read_b32 v212, v212\n\t"
      "ds_read2_b32 v[128:129], v234 offset0:32 offset1:32\n\t"
      "ds_read2_b32 v[130:131], v234 offset0:48 offset1:48\n\t"
      "ds_read2_b32 v[132:133], v234 offset0:64 offset1:64\n\t"
      "ds_read2_b32 v[134:135], v234 offset0:80 offset1:80\n\t"
      "ds_read2_b32 v[248:249], v234 offset0:0 offset1:0\n\t"
      "ds_read_b128 v[196:199], v233\n\t"
      "ds_read_b128 v[136:139], v233 offset:128\n\t"
      "ds_read_b128 v[140:143], v233 offset:192\n\t"
      "ds_read_b128 v[144:147], v233 offset:256\n\t"
      "ds_read_b128 v[148:151], v233 offset:320\n\t"
      "s_waitcnt lgkmcnt(0)\n\t"
      "ds_read_b32 v226, v232\n\t"
      "ds_read_b128 v[116:119], v228 offset:128\n\t"
      "ds_read_b128 v[120:123], v228 offset:192\n\t"
      "ds_read2_b32 v[250:251], v229 offset0:64 offset1:64\n\t"
      "ds_read2_b32 v[252:253], v229 offset0:80 offset1:80\n\t"
      "ds_read_b128 v[124:127], v230 offset:128\n\t"
      "ds_read2_b32 v[224:225], v231 offset0:64 offset1:64\n\t"
      "ds_read_b128 v[108:111], v228\n\t"
      "ds_read_b128 v[112:115], v228 offset:64\n\t"
      "ds_read2_b32 v[204:205], v235 offset0:0 offset1:16\n\t"
      "ds_read2_b32 v[206:207], v235 offset0:32 offset1:48\n\t"
      "ds_read2_b32 v[208:209], v254 offset0:0 offset1:16\n\t"
      "ds_read2_b32 v[210:211], v254 offset0:32 offset1:48\n\t"
      "ds_read2_b32 v[200:201], v234 offset0:192 offset1:208\n\t"
      "ds_read2_b32 v[202:203], v234 offset0:224 offset1:240\n\t"
      "s_waitcnt lgkmcnt(0)\n\t"
      "v_min_u32_e32 v226, v226, v212\n\t"
      "v_cmp_eq_u32_e32 vcc, 0, v226\n\t"
      "s_cbranch_vccnz XAL_%=\n\t"
      "XAX_%=:\n\t"
      "s_branch XAB_%=\n\t"
      "XBR_%=:\n\t"
      "s_mov_b32 s51, 0\n\t"
      "XBL_%=:\n\t"
      "s_add_u32 s51, s51, 1\n\t"
      "s_cmp_lt_u32 s51, 262144\n\t"
      "s_cbranch_scc0 XBX_%=\n\t"
      "s_waitcnt lgkmcnt(0)\n\t"
      "ds_read_b32 v226, v232 offset:4\n\t"
      "ds_read_b128 v[152:155], v236\n\t"
      "ds_read_b64 v[156:157], v237 offset:1024\n\t"
      "ds_read_b64 v[166:167], v237 offset:1536\n\t"
      "s_waitcnt lgkmcnt(0)\n\t"
      "v_cmp_eq_u32_e32 vcc, 0, v226\n\t"
      "s_cbranch_vccnz XBL_%=\n\t"
      "XBX_%=:\n\t"
      "s_branch XBB_%=\n\t"
      "CEND_%=:\n\t"
      : "+{s[4:11]}"(s), "+{s12}"(hint), "+{s13}"(chk), "+{v[224:239]}"(v), "+{v254}"(vk1)
      :
      : "memory", "vcc", "scc",
        "v0", "v1", "v2", "v3", "v4", "v5", "v6", "v7", "v8", "v9", "v10", "v11", "v12", "v13", "v14", "v15", "v16",
        "v17", "v18", "v19", "v20", "v21", "v22", "v23", "v24", "v25", "v26", "v27", "v28", "v29", "v30", "v31", "v32",
        "v33", "v34", "v35", "v36", "v37", "v38", "v39", "v40", "v41", "v42", "v43", "v44", "v45", "v46", "v47", "v48",
        "v49", "v50", "v51", "v52", "v53", "v54", "v55", "v56", "v57", "v58", "v59", "v60", "v61", "v62", "v63", "v64",
        "v65", "v66", "v67", "v68", "v69", "v70", "v71", "v72", "v73", "v74", "v75", "v76", "v77", "v78", "v79", "v80",
        "v81", "v82", "v83", "v84", "v85", "v86", "v87", "v88", "v89", "v90", "v91", "v92", "v93", "v94", "v95", "v96",
        "v97", "v98", "v99", "v100", "v101", "v102", "v103", "v104", "v105", "v106", "v107", "v108", "v109", "v110",
        "v111", "v112", "v113", "v114", "v115", "v116", "v117", "v118", "v119", "v120", "v121", "v122", "v123", "v124",
        "v125", "v126", "v127", "v128", "v129", "v130", "v131", "v132", "v133", "v134", "v135", "v136", "v137", "v138",
        "v139", "v140", "v141", "v142", "v143", "v144", "v145", "v146", "v147", "v148", "v149", "v150", "v151", "v152",
        "v153", "v154", "v155", "v156", "v157", "v158", "v159", "v160", "v161", "v162", "v163", "v164", "v165", "v166",
        "v167", "v168", "v169", "v170", "v171", "v172", "v173", "v174", "v175", "v176", "v177", "v178", "v179", "v180",
        "v181", "v182", "v183", "v184", "v185", "v186", "v187", "v188", "v189", "v190", "v191", "v192", "v193", "v194",
        "v195", "v196", "v197", "v198", "v199", "v200", "v201", "v202", "v203", "v204", "v205", "v206", "v207", "v208",
        "v209", "v210", "v211", "v212", "v213", "v214", "v215", "v216", "v217", "v218", "v219", "v220", "v221", "v222",
        "v223", "v240", "v241", "v242", "v243", "v244", "v245", "v246", "v247", "v248", "v249", "v250", "v251", "v252",
        "v253", "s36", "s37", "s38", "s39", "s40", "s41", "s42", "s43", "s44", "s45", "s46", "s47", "s48", "s49", "s50",
        "s51", "s52", "s53", "s54", "s55"
    );
    } else {
      LANE_BITS(threadIdx.x & 63);
      u32x8 v;
      u32x16 s;
      v[0] = ((8 * rt + 4 * half + g) * 16 + c) * 16;
      const int hi = -(lane >> 5), m3 = min(c, 2);
      v[1] = (((max(8 * rt + ((g & 1) ? 0 : 4 * half) - 1, 0) * 16 + c) * 16) & ~hi) | ((ST_PW + xcd * 128 + m3 * 16) & hi);
      v[2] = sta + 128 + lane * 4;
      v[3] = ((sta + ((g & 1) ? 0 : 64) + c * 4) & ~hi) | ((A_MK + m3 * 4) & hi);
      v[4] = lr * A_SLOT + A_HALO + lc * 16;
      v[6] = (side * 1024 + (idx * 4 + wave - 4) * 8 + lr) * 3584 + (7 * xp + min(lc, 6)) * 128;
      v[7] = v[6] + (1 - 2 * side) * 1024 * 3584;
      u64 tw = (u64)w1;
      const int n = idx * 4 + wave - 4;
      u64 pf = (u64)(n < 14 ? b1 + xp * 896 + n * 64 : n < 30 ? b2 + xp * 1024 + (n - 14) * 64 : n < 38 ? w1 + (n - 30) * 1000000
                : n < 46 ? w2 + (n - 38) * 1000000 : n < 48 ? out + (n - 46) * 1800000 : ws + (n & 1) * (4 << 20) + (4 << 20) - 4096);
      s[4] = UNI(idx == 15 && xcd < 3 && wave == 4 ? (xcd == 0 ? WS_O : xcd == 1 ? WS_HN + 16 : WS_G + 32) + PROBE : 0);
      v[5] = lane * (8192 + 640);
      put(s, 0, (u64)(x + (rt * 128 * 896 + 128) - (16 * 896 + 1024)));
      put(s, 2, (u64)(wqkv + (hp * 112 + 64 * 2688) - 4096));
      s[5] = wave * A_UNIT;
      put2(s, 6, wave > 4 ? 0xffffffffu : 0xffffff00u, 0u);
      s[8] = A_HALO + (wave - 4) * 512;
      s[9] = A_FL + 32 + 4 * half;
      put(s, 10, (u64)(ws + WS_ST));
    asm volatile(
      "s_waitcnt lgkmcnt(0)\n\ts_nop 4\n\t"
      "s_load_dwordx2 s[18:19], s[14:15], 0x20000\n\t"
      "s_sub_u32 s40, s4, 57344\n\t"
      "s_subb_u32 s41, s5, 0\n\t"
      "s_sub_u32 s42, s6, 86016\n\t"
      "s_subb_u32 s43, s7, 0\n\t"
      "s_setprio 2\n\t"
      "v_mov_b32_e32 v0, 0x3f803f80\n\t"
      "v_mov_b32_e32 v1, 0x3f803f80\n\t"
      "v_mov_b32_e32 v2, 0x3f803f80\n\t"
      "v_mov_b32_e32 v3, 0x3f803f80\n\t"
      "s_mov_b64 exec, 0xffffff\n\t"
      "ds_write_b128 v204, v[0:3]\n\t"
      "s_mov_b64 exec, -1\n\t"
      "v_mov_b32_e32 v32, 154624\n\t"
      "v_mov_b32_e32 v33, s13\n\t"
      "v_mov_b32_e32 v36, 0\n\t"
      "v_mov_b32_e32 v37, 0\n\t"
      "v_mov_b32_e32 v38, 0\n\t"
      "v_mov_b32_e32 v39, 0\n\t"
      "ds_write_b128 v32, v[36:39]\n\t"
      "ds_write_b128 v32, v[36:39] offset:16\n\t"
      "ds_write_b128 v32, v[36:39] offset:32\n\t"
      "s_cmp_eq_u32 s8, 0\n\t"
      "s_cbranch_scc1 PRt_%=\n\t"
      "s_add_u32 s52, s14, s8\n\t"
      "s_addc_u32 s53, s15, 0\n\t"
      "s_mov_b32 s54, 0\n\t"
      "s_mov_b64 s[56:57], -1\n\t"
      "s_load_dword s50, s[52:53], 0x1f80\n\t"
      "PRt_%=:\n\t"
      "s_cmp_eq_u32 s8, 0\n\t"
      "s_cbranch_scc1 PRu_%=\n\t"
      "s_mov_b64 exec, 15\n\t"
      "global_store_dword v205, v0, s[52:53] sc1\n\t"
      "s_mov_b64 exec, -1\n\t"
      "PRu_%=:\n\t"
      "s_add_u32 m0, s9, 43008\n\t"
      "s_nop 0\n\t"
      "global_load_lds_dwordx4 v195, s[6:7] offset:2048\n\t"
      "global_load_lds_dwordx4 v192, s[4:5]\n\t"
      "global_load_lds_dwordx4 v193, s[4:5] offset:1024\n\t"
      "global_load_lds_dwordx4 v196, s[6:7] offset:3072\n\t"
      "s_add_u32 m0, s9, 47104\n\t"
      "s_nop 0\n\t"
      "global_load_lds_dwordx4 v197, s[6:7]\n\t"
      "s_add_u32 m0, s12, 43008\n\t"
      "s_mov_b64 exec, s[10:11]\n\t"
      "global_load_lds_dwordx4 v194, s[4:5]\n\t"
      "s_mov_b64 exec, -1\n\t"
      "s_waitcnt lgkmcnt(0)\n\t"
      "s_add_u32 s18, s18, 1\n\t"
      "s_addc_u32 s19, s19, 0\n\t"
      "s_mul_i32 s48, s18, 0x9e3779b9\n\t"
      "s_mul_hi_u32 s17, s18, 0x7f4a7c15\n\t"
      "s_add_u32 s17, s17, s48\n\t"
      "s_mul_i32 s48, s19, 0x7f4a7c15\n\t"
      "s_add_u32 s17, s17, s48\n\t"
      "s_mul_i32 s16, s18, 0x7f4a7c15\n\t"
      "s_add_u32 s16, s16, 0xd192ed03\n\t"
      "s_addc_u32 s17, s17, 0xd1b54a32\n\t"
      "v_mov_b32_e32 v12, s16\n\t"
      "v_mov_b32_e32 v13, s17\n\t"
      "v_mov_b32_e32 v14, s18\n\t"
      "v_mov_b32_e32 v15, s19\n\t"
      "v_mov_b32_e32 v16, 163600\n\t"
      "s_mov_b64 exec, 1\n\t"
      "ds_write_b128 v16, v[12:15]\n\t"
      "s_mov_b64 exec, -1\n\t"
      "s_waitcnt vmcnt(11) lgkmcnt(0)\n\t"
      "s_barrier\n\t"
      "s_cmp_eq_u32 s8, 0\n\t"
      "s_cbranch_scc0 PRB0_%=\n\t"
      "PRC0_%=:\n\t"
      "s_add_u32 s4, s4, 128\n\t"
      "s_addc_u32 s5, s5, 0\n\t"
      "s_add_u32 s6, s6, 172032\n\t"
      "s_addc_u32 s7, s7, 0\n\t"
      "s_add_u32 s40, s40, 128\n\t"
      "s_addc_u32 s41, s41, 0\n\t"
      "s_add_u32 s42, s42, 172032\n\t"
      "s_addc_u32 s43, s43, 0\n\t"
      "s_add_u32 m0, s9, 65536\n\t"
      "s_nop 0\n\t"
      "global_load_lds_dwordx4 v195, s[42:43] offset:2048\n\t"
      "global_load_lds_dwordx4 v192, s[40:41]\n\t"
      "global_load_lds_dwordx4 v193, s[40:41] offset:1024\n\t"
      "global_load_lds_dwordx4 v196, s[42:43] offset:3072\n\t"
      "s_add_u32 m0, s9, 69632\n\t"
      "s_nop 0\n\t"
      "global_load_lds_dwordx4 v197, s[42:43]\n\t"
      "s_add_u32 m0, s9, 86016\n\t"
      "s_nop 0\n\t"
      "global_load_lds_dwordx4 v195, s[6:7] offset:2048\n\t"
      "global_load_lds_dwordx4 v192, s[4:5]\n\t"
      "global_load_lds_dwordx4 v193, s[4:5] offset:1024\n\t"
      "global_load_lds_dwordx4 v196, s[6:7] offset:3072\n\t"
      "s_add_u32 m0, s9, 90112\n\t"
      "s_nop 0\n\t"
      "global_load_lds_dwordx4 v197, s[6:7]\n\t"
      "s_add_u32 m0, s12, 86016\n\t"
      "s_mov_b64 exec, s[10:11]\n\t"
      "global_load_lds_dwordx4 v194, s[4:5]\n\t"
      "s_mov_b64 exec, -1\n\t"
      "s_waitcnt vmcnt(11)\n\t"
      "s_barrier\n\t"
      "s_cmp_eq_u32 s8, 0\n\t"
      "s_cbranch_scc0 PRB1_%=\n\t"
      "PRC1_%=:\n\t"
      "s_add_u32 s4, s4, 128\n\t"
      "s_addc_u32 s5, s5, 0\n\t"
      "s_add_u32 s6, s6, 172032\n\t"
      "s_addc_u32 s7, s7, 0\n\t"
      "s_add_u32 s40, s40, 128\n\t"
      "s_addc_u32 s41, s41, 0\n\t"
      "s_add_u32 s42, s42, 172032\n\t"
      "s_addc_u32 s43, s43, 0\n\t"
      "s_add_u32 m0, s9, 4294946816\n\t"
      "s_nop 0\n\t"
      "global_load_lds_dwordx4 v195, s[42:43] offset:2048\n\t"
      "global_load_lds_dwordx4 v192, s[40:41]\n\t"
      "global_load_lds_dwordx4 v193, s[40:41] offset:1024\n\t"
      "global_load_lds_dwordx4 v196, s[42:43] offset:3072\n\t"
      "s_add_u32 m0, s9, 4294950912\n\t"
      "s_nop 0\n\t"
      "global_load_lds_dwordx4 v197, s[42:43]\n\t"
      "s_add_u32 m0, s9, 0\n\t"
      "s_nop 0\n\t"
      "global_load_lds_dwordx4 v195, s[6:7] offset:2048\n\t"
      "global_load_lds_dwordx4 v192, s[4:5]\n\t"
      "global_load_lds_dwordx4 v193, s[4:5] offset:1024\n\t"
      "global_load_lds_dwordx4 v196, s[6:7] offset:3072\n\t"
      "s_add_u32 m0, s9, 4096\n\t"
      "s_nop 0\n\t"
      "global_load_lds_dwordx4 v197, s[6:7]\n\t"
      "s_add_u32 m0, s12, 0\n\t"
      "s_mov_b64 exec, s[10:11]\n\t"
      "global_load_lds_dwordx4 v194, s[4:5]\n\t"
      "s_mov_b64 exec, -1\n\t"
      "s_waitcnt vmcnt(11)\n\t"
      "s_barrier\n\t"
      "s_add_u32 s4, s4, 128\n\t"
      "s_addc_u32 s5, s5, 0\n\t"
      "s_add_u32 s6, s6, 172032\n\t"
      "s_addc_u32 s7, s7, 0\n\t"
      "s_add_u32 s40, s40, 128\n\t"
      "s_addc_u32 s41, s41, 0\n\t"
      "s_add_u32 s42, s42, 172032\n\t"
      "s_addc_u32 s43, s43, 0\n\t"
      "s_add_u32 m0, s9, 22528\n\t"
      "s_nop 0\n\t"
      "global_load_lds_dwordx4 v195, s[42:43] offset:2048\n\t"
      "global_load_lds_dwordx4 v192, s[40:41]\n\t"
      "global_load_lds_dwordx4 v193, s[40:41] offset:1024\n\t"
      "global_load_lds_dwordx4 v196, s[42:43] offset:3072\n\t"
      "s_add_u32 m0, s9, 26624\n\t"
      "s_nop 0\n\t"
      "global_load_lds_dwordx4 v197, s[42:43]\n\t"
      "s_add_u32 m0, s9, 43008\n\t"
      "s_nop 0\n\t"
      "global_load_lds_dwordx4 v195, s[6:7] offset:2048\n\t"
      "global_load_lds_dwordx4 v192, s[4:5]\n\t"
      "global_load_lds_dwordx4 v193, s[4:5] offset:1024\n\t"
      "global_load_lds_dwordx4 v196, s[6:7] offset:3072\n\t"
      "s_add_u32 m0, s9, 47104\n\t"
      "s_nop 0\n\t"
      "global_load_lds_dwordx4 v197, s[6:7]\n\t"
      "s_add_u32 m0, s12, 43008\n\t"
      "s_mov_b64 exec, s[10:11]\n\t"
      "global_load_lds_dwordx4 v194, s[4:5]\n\t"
      "s_mov_b64 exec, -1\n\t"
      "s_load_dword s50, s[22:23], 0x0\n\t"
      "s_waitcnt vmcnt(11)\n\t"
      "s_barrier\n\t"
      "s_add_u32 s4, s4, 128\n\t"
      "s_addc_u32 s5, s5, 0\n\t"
      "s_add_u32 s6, s6, 172032\n\t"
      "s_addc_u32 s7, s7, 0\n\t"
      "s_add_u32 s40, s40, 128\n\t"
      "s_addc_u32 s41, s41, 0\n\t"
      "s_add_u32 s42, s42, 172032\n\t"
      "s_addc_u32 s43, s43, 0\n\t"
      "s_add_u32 m0, s9, 65536\n\t"
      "s_nop 0\n\t"
      "global_load_lds_dwordx4 v195, s[42:43] offset:2048\n\t"
      "global_load_lds_dwordx4 v192, s[40:41]\n\t"
      "global_load_lds_dwordx4 v193, s[40:41] offset:1024\n\t"
      "global_load_lds_dwordx4 v196, s[42:43] offset:3072\n\t"
      "s_add_u32 m0, s9, 69632\n\t"
      "s_nop 0\n\t"
      "global_load_lds_dwordx4 v197, s[42:43]\n\t"
      "s_add_u32 m0, s9, 86016\n\t"
      "s_nop 0\n\t"
      "global_load_lds_dwordx4 v195, s[6:7] offset:2048\n\t"
      "global_load_lds_dwordx4 v192, s[4:5]\n\t"
      "global_load_lds_dwordx4 v193, s[4:5] offset:1024\n\t"
      "global_load_lds_dwordx4 v196, s[6:7] offset:3072\n\t"
      "s_add_u32 m0, s9, 90112\n\t"
      "s_nop 0\n\t"
      "global_load_lds_dwordx4 v197, s[6:7]\n\t"
      "s_add_u32 m0, s12, 86016\n\t"
      "s_mov_b64 exec, s[10:11]\n\t"
      "global_load_lds_dwordx4 v194, s[4:5]\n\t"
      "s_mov_b64 exec, -1\n\t"
      "s_waitcnt vmcnt(11)\n\t"
      "s_barrier\n\t"
      "s_add_u32 s4, s4, 128\n\t"
      "s_addc_u32 s5, s5, 0\n\t"
      "s_add_u32 s6, s6, 172032\n\t"
      "s_addc_u32 s7, s7, 0\n\t"
      "s_add_u32 s40, s40, 128\n\t"
      "s_addc_u32 s41, s41, 0\n\t"
      "s_add_u32 s42, s42, 172032\n\t"
      "s_addc_u32 s43, s43, 0\n\t"
      "s_add_u32 m0, s9, 4294946816\n\t"
      "s_nop 0\n\t"
      "global_load_lds_dwordx4 v195, s[42:43] offset:2048\n\t"
      "global_load_lds_dwordx4 v192, s[40:41]\n\t"
      "global_load_lds_dwordx4 v193, s[40:41] offset:1024\n\t"
      "global_load_lds_dwordx4 v196, s[42:43] offset:3072\n\t"
      "s_add_u32 m0, s9, 4294950912\n\t"
      "s_nop 0\n\t"
      "global_load_lds_dwordx4 v197, s[42:43]\n\t"
      "s_add_u32 m0, s9, 0\n\t"
      "s_nop 0\n\t"
      "global_load_lds_dwordx4 v195, s[6:7] offset:2048\n\t"
      "global_load_lds_dwordx4 v192, s[4:5]\n\t"
      "global_load_lds_dwordx4 v193, s[4:5] offset:1024\n\t"
      "global_load_lds_dwordx4 v196, s[6:7] offset:3072\n\t"
      "s_add_u32 m0, s9, 4096\n\t"
      "s_nop 0\n\t"
      "global_load_lds_dwordx4 v197, s[6:7]\n\t"
      "s_add_u32 m0, s12, 0\n\t"
      "s_mov_b64 exec, s[10:11]\n\t"
      "global_load_lds_dwordx4 v194, s[4:5]\n\t"
      "s_mov_b64 exec, -1\n\t"
      "global_load_dwordx4 v[4:7], v200, s[14:15] sc1\n\t"
      "global_load_dwordx4 v[8:11], v201, s[14:15] sc1\n\t"
      "s_mov_b32 m0, 163328\n\t"
      "s_add_u32 s44, s20, 0\n\t"
      "s_addc_u32 s45, s21, 0\n\t"
      "global_load_lds_dword v206, s[44:45]\n\t"
      "s_waitcnt vmcnt(14)\n\t"
      "s_barrier\n\t"
      "s_mov_b32 m0, 163328\n\t"
      "s_add_u32 s44, s20, 1835008\n\t"
      "s_addc_u32 s45, s21, 0\n\t"
      "global_load_lds_dword v206, s[44:45]\n\t"
      "s_waitcnt vmcnt(4)\n\t"
      "s_barrier\n\t"
      "s_waitcnt vmcnt(2)\n\t"
      "v_xor_b32_e32 v6, v6, v4\n\t"
      "v_xor_b32_e32 v7, v7, v5\n\t"
      "v_xor_b32_e32 v10, v10, v8\n\t"
      "v_xor_b32_e32 v11, v11, v9\n\t"
      "v_cmp_ne_u64_e64 s[48:49], s[16:17], v[6:7]\n\t"
      "v_cmp_ne_u64_e32 vcc, s[16:17], v[10:11]\n\t"
      "s_nop 0\n\t"
      "s_or_b64 vcc, vcc, s[48:49]\n\t"
      "s_cbranch_scc1 GRR_%=\n\t"
      "GRB_%=:\n\t"
      "v_mul_f32_e32 v6, 0x3e8b97f6, v5\n\t"
      "ds_write2_b32 v202, v4, v5 offset0:0 offset1:96\n\t"
      "ds_write2_b32 v203, v8, v9 offset0:0 offset1:96\n\t"
      "ds_write_b32 v202, v6 offset:640\n\t"
      "s_mov_b64 exec, 1\n\t"
      "ds_write_b32 v33, v33\n\t"
      "s_mov_b64 exec, -1\n\t"
      "s_setprio 0\n\t"
      "s_mov_b32 m0, 163328\n\t"
      "s_add_u32 s44, s20, 0\n\t"
      "s_addc_u32 s45, s21, 0\n\t"
      "global_load_lds_dword v207, s[44:45]\n\t"
      "s_mov_b32 m0, 163328\n\t"
      "s_add_u32 s44, s20, 1835008\n\t"
      "s_addc_u32 s45, s21, 0\n\t"
      "global_load_lds_dword v207, s[44:45]\n\t"
      "s_waitcnt lgkmcnt(0)\n\t"
      "s_branch HEND_%=\n\t"
      ".p2align 6\n\t"
      "PRB0_%=:\n\t"
      "s_waitcnt lgkmcnt(0)\n\t"
      "s_memtime s[58:59]\n\t"
      "s_waitcnt lgkmcnt(0)\n\t"
      "s_load_dword s50, s[52:53], s54\n\t"
      "s_waitcnt lgkmcnt(0)\n\t"
      "s_memtime s[46:47]\n\t"
      "s_add_u32 s54, s54, 8832\n\t"
      "s_waitcnt lgkmcnt(0)\n\t"
      "s_sub_u32 s46, s46, s58\n\t"
      "s_min_u32 s46, s56, s46\n\t"
      "s_mov_b32 s56, s57\n\t"
      "s_mov_b32 s57, s46\n\t"
      "s_cmp_lt_u32 s54, 17664\n\t"
      "s_cbranch_scc1 PRB0_%=\n\t"
      "s_branch PRC0_%=\n\t"
      ".p2align 6\n\t"
      "PRB1_%=:\n\t"
      "s_waitcnt lgkmcnt(0)\n\t"
      "s_memtime s[58:59]\n\t"
      "s_waitcnt lgkmcnt(0)\n\t"
      "s_load_dword s50, s[52:53], s54\n\t"
      "s_waitcnt lgkmcnt(0)\n\t"
      "s_memtime s[46:47]\n\t"
      "s_add_u32 s54, s54, 8832\n\t"
      "s_waitcnt lgkmcnt(0)\n\t"
      "s_sub_u32 s46, s46, s58\n\t"
      "s_min_u32 s46, s56, s46\n\t"
      "s_mov_b32 s56, s57\n\t"
      "s_mov_b32 s57, s46\n\t"
      "s_cmp_lt_u32 s54, 35328\n\t"
      "s_cbranch_scc1 PRB1_%=\n\t"
      "s_cmp_lt_u32 s57, s56\n\t"
      "s_cselect_b32 s46, 0x2000, 0\n\t"
      "s_and_b32 s47, s8, 48\n\t"
      "s_add_u32 s47, s47, 114688\n\t"
      "s_xor_b32 s58, s46, s16\n\t"
      "v_mbcnt_lo_u32_b32 v20, -1, 0\n\t"
      "v_mov_b32_e32 v16, s46\n\t"
      "v_mov_b32_e32 v17, 0\n\t"
      "v_mov_b32_e32 v18, s58\n\t"
      "v_mov_b32_e32 v19, s17\n\t"
      "v_lshl_add_u32 v20, v20, 7, s47\n\t"
      "s_mov_b64 exec, 0xff\n\t"
      "global_store_dwordx4 v20, v[16:19], s[14:15] sc1\n\t"
      "s_mov_b64 exec, -1\n\t"
      "s_branch PRC1_%=\n\t"
      "GRR_%=:\n\t"
      "s_mov_b32 s51, 0\n\t"
      "GRL_%=:\n\t"
      "s_add_u32 s51, s51, 1\n\t"
      "s_cmp_lt_u32 s51, 262144\n\t"
      "s_cbranch_scc0 GRX_%=\n\t"
      "s_sleep 2\n\t"
      "global_load_dwordx4 v[4:7], v200, s[14:15] sc1\n\t"
      "global_load_dwordx4 v[8:11], v201, s[14:15] sc1\n\t"
      "s_waitcnt vmcnt(0)\n\t"
      "v_xor_b32_e32 v6, v6, v4\n\t"
      "v_xor_b32_e32 v7, v7, v5\n\t"
      "v_xor_b32_e32 v10, v10, v8\n\t"
      "v_xor_b32_e32 v11, v11, v9\n\t"
      "v_cmp_ne_u64_e64 s[48:49], s[16:17], v[6:7]\n\t"
      "v_cmp_ne_u64_e32 vcc, s[16:17], v[10:11]\n\t"
      "s_nop 0\n\t"
      "s_or_b64 vcc, vcc, s[48:49]\n\t"
      "s_cbranch_scc1 GRL_%=\n\t"
      "GRX_%=:\n\t"
      "s_mov_b32 exec_lo, 0\n\t"
      "v_and_b32_e32 v8, 0x2000, v8\n\t"
      "s_mov_b32 exec_lo, -1\n\t"
      "s_branch GRB_%=\n\t"
      "HEND_%=:\n\t"
      : "+{s[4:19]}"(s), "+{s[20:21]}"(tw), "+{s[22:23]}"(pf), "+{v[192:199]}"(f), "+{v[200:207]}"(v)
      :
      : "memory", "vcc", "scc",
        "v0", "v1", "v2", "v3", "v4", "v5", "v6", "v7", "v8", "v9", "v10", "v11", "v12", "v13", "v14", "v15", "v16",
        "v17", "v18", "v19", "v20", "v21", "v22", "v23", "v24", "v25", "v26", "v27", "v28", "v29", "v30", "v31", "v32",
        "v33", "v34", "v35", "v36", "v37", "v38", "v39", "s40", "s41", "s42", "s43", "s44", "s45", "s46", "s47", "s48",
        "s49", "s50", "s51", "s52", "s53", "s54", "s55", "s56", "s57", "s58", "s59"
    );
    }
    PIN();
    LANE_BITS(__builtin_amdgcn_mbcnt_hi(~0u, __builtin_amdgcn_mbcnt_lo(~0u, 0u)));
    const bool has3 = idx < 14;
    const int ft = has3 ? 7 * xp + (idx >> 1) : 0, blk0 = 7 * (2 * side + (idx & 1));
    u32x8 v;
    u32x16 s;
    v[0] = lane * 16;
    const int rot = 4 * (idx & 1), kg = 2 * ft + ((wave >> 1) & 1);
    u64 kr = (u64)(7 - rot) | ((u64)rot << 32);
    v[1] = has3 ? lr * 3584 + ((lc ^ swz_tr(lr)) << 4) : SP3 ? (min(lane, 55) + 96 * (idx & 1)) * 4096 + 384 + xp * 1024 : (lr + 8 * side) * 4096 + lc * 128 + xp * 1024;
    v[2] = wave * G_WV + g * 1024 + q4 * 128 + (((2 * (wave & 3) + (p4 >> 1)) ^ swz_tr(8 * g + q4)) << 4) + (p4 & 1) * 8;
    v[3] = wave * 4096 + (wave & 3) * 1024 + ((lane * 16) ^ ((g & 1) << 7));
    const int gg = 2 * (lane >> 5) + (lane & 1);
    v[4] = (wave & 3) * 1024 + (((gg * 16 + ((lane >> 1) & 15)) * 16) ^ ((gg & 1) << 7));
    v[5] = (wave & 1) * 512 + lane * 8;
    v[6] = g * 8;
    v[7] = ST_H3 + part(xp * 16384 + (wave * 16 + c) * 128, side);
    put(s, 0, has3 ? (u64)(w1 + ft * 128 + (wave * 256 + rot * 32) * 3584) : (u64)(w2 + (wave * 224 + (idx & 1) * 16) * 4096));
    put(s, 2, (u64)(ws + WS_HN + unit(blk0, wave, 0)));
    put(s, 4, (u64)(ws + WS_G + unit(blk0, kg / 7, 0) + kg % 7 * 1024));
    put(s, 6, (u64)(b1 + (64 * ft + 16 * (wave & 3)) * 2));
    put(s, 8, (u64)(ws + WS_ST));
    unsigned hint = ST_H4 + side * 8192 + ((idx & 1) * 28 + ft) * 128;
    s[12] = UNI((has3 ? 4 : 0) |  (wave == 4 * side + ((xp + XO) & 3) ? 16 : 0));
    s[13] = wave * G_WV;
    asm volatile(
      "s_waitcnt lgkmcnt(0)\n\ts_nop 4\n\t"
      "s_bitcmp0_b32 s16, 2\n\t"
      "s_cbranch_scc1 ASP_%=\n\t"
      "v_mov_b32_e32 v216, 163600\n\t"
      "ds_read_b128 v[216:219], v216\n\t"
      "v_mov_b32_e32 v220, 154756\n\t"
      "ds_read2_b32 v[220:221], v220 offset0:0 offset1:1\n\t"
      "v_xor_b32_e32 v248, 64, v241\n\t"
      "v_add_u32_e32 v249, 55296, v241\n\t"
      "v_add_u32_e32 v250, 82944, v248\n\t"
      "v_add_u32_e32 v248, 27648, v248\n\t"
      "v_xor_b32_e32 v251, 32, v242\n\t"
      "v_xor_b32_e32 v252, 64, v242\n\t"
      "v_xor_b32_e32 v253, 96, v242\n\t"
      "s_cmp_ge_u32 s17, 65536\n\t"
      "s_cbranch_scc0 BOH_%=\n\t"
      "s_barrier\n\t"
      "BOH_%=:\n\t"
      "s_add_u32 m0, s17, 0\n\t"
      "s_nop 0\n\t"
      "global_load_lds_dwordx4 v241, s[4:5]\n\t"
      "global_load_lds_dwordx4 v248, s[4:5] offset:1024\n\t"
      "global_load_lds_dwordx4 v249, s[4:5] offset:2048\n\t"
      "global_load_lds_dwordx4 v250, s[4:5] offset:3072\n\t"
      "s_sub_u32 s20, s20, 1\n\t"
      "s_cselect_b32 s48, -7, 1\n\t"
      "s_cselect_b32 s49, -1, 0\n\t"
      "s_mul_i32 s48, s48, 114688\n\t"
      "s_add_u32 s4, s4, s48\n\t"
      "s_addc_u32 s5, s5, s49\n\t"
      "global_load_dwordx2 v[238:239], v246, s[10:11]\n\t"
      "s_waitcnt lgkmcnt(0)\n\t"
      "v_readfirstlane_b32 s14, v216\n\t"
      "v_readfirstlane_b32 s15, v217\n\t"
      "v_readfirstlane_b32 s46, v218\n\t"
      "v_readfirstlane_b32 s47, v219\n\t"
      "v_readfirstlane_b32 s48, v220\n\t"
      "v_readfirstlane_b32 s49, v221\n\t"
      "v_xor_b32_e32 v247, v247, v221\n\t"
      "s_nop 0\n\t"
      "s_xor_b32 s6, s6, s48\n\t"
      "s_xor_b32 s8, s8, s49\n\t"
      "s_xor_b32 s22, s22, s49\n\t"
      "s_add_u32 s52, s6, 0\n\t"
      "s_addc_u32 s53, s7, 0\n\t"
      "s_add_u32 s54, s6, 131072\n\t"
      "s_addc_u32 s55, s7, 0\n\t"
      "s_add_u32 s56, s6, 262144\n\t"
      "s_addc_u32 s57, s7, 0\n\t"
      "s_add_u32 s58, s6, 393216\n\t"
      "s_addc_u32 s59, s7, 0\n\t"
      "s_add_u32 s60, s6, 524288\n\t"
      "s_addc_u32 s61, s7, 0\n\t"
      "s_add_u32 s62, s6, 655360\n\t"
      "s_addc_u32 s63, s7, 0\n\t"
      "s_add_u32 s64, s6, 786432\n\t"
      "s_addc_u32 s65, s7, 0\n\t"
      "s_add_u32 s48, s21, 0\n\t"
      "s_cmp_ge_u32 s48, 8\n\t"
      "s_cselect_b32 s49, 8, 0\n\t"
      "s_sub_u32 s48, s48, s49\n\t"
      "v_lshl_add_u32 v176, s48, 10, v240\n\t"
      "s_add_u32 s48, s21, 1\n\t"
      "s_cmp_ge_u32 s48, 8\n\t"
      "s_cselect_b32 s49, 8, 0\n\t"
      "s_sub_u32 s48, s48, s49\n\t"
      "v_lshl_add_u32 v177, s48, 10, v240\n\t"
      "s_add_u32 s48, s21, 2\n\t"
      "s_cmp_ge_u32 s48, 8\n\t"
      "s_cselect_b32 s49, 8, 0\n\t"
      "s_sub_u32 s48, s48, s49\n\t"
      "v_lshl_add_u32 v178, s48, 10, v240\n\t"
      "s_add_u32 s48, s21, 3\n\t"
      "s_cmp_ge_u32 s48, 8\n\t"
      "s_cselect_b32 s49, 8, 0\n\t"
      "s_sub_u32 s48, s48, s49\n\t"
      "v_lshl_add_u32 v179, s48, 10, v240\n\t"
      "s_add_u32 s48, s21, 4\n\t"
      "s_cmp_ge_u32 s48, 8\n\t"
      "s_cselect_b32 s49, 8, 0\n\t"
      "s_sub_u32 s48, s48, s49\n\t"
      "v_lshl_add_u32 v180, s48, 10, v240\n\t"
      "s_add_u32 s48, s21, 5\n\t"
      "s_cmp_ge_u32 s48, 8\n\t"
      "s_cselect_b32 s49, 8, 0\n\t"
      "s_sub_u32 s48, s48, s49\n\t"
      "v_lshl_add_u32 v181, s48, 10, v240\n\t"
      "s_add_u32 s48, s21, 6\n\t"
      "s_cmp_ge_u32 s48, 8\n\t"
      "s_cselect_b32 s49, 8, 0\n\t"
      "s_sub_u32 s48, s48, s49\n\t"
      "v_lshl_add_u32 v182, s48, 10, v240\n\t"
      "s_add_u32 s48, s21, 7\n\t"
      "s_cmp_ge_u32 s48, 8\n\t"
      "s_cselect_b32 s49, 8, 0\n\t"
      "s_sub_u32 s48, s48, s49\n\t"
      "v_lshl_add_u32 v183, s48, 10, v240\n\t"
      "s_add_u32 m0, s17, 4096\n\t"
      "s_nop 0\n\t"
      "global_load_lds_dwordx4 v241, s[4:5]\n\t"
      "global_load_lds_dwordx4 v248, s[4:5] offset:1024\n\t"
      "global_load_lds_dwordx4 v249, s[4:5] offset:2048\n\t"
      "global_load_lds_dwordx4 v250, s[4:5] offset:3072\n\t"
      "s_sub_u32 s20, s20, 1\n\t"
      "s_cselect_b32 s48, -7, 1\n\t"
      "s_cselect_b32 s49, -1, 0\n\t"
      "s_mul_i32 s48, s48, 114688\n\t"
      "s_add_u32 s4, s4, s48\n\t"
      "s_addc_u32 s5, s5, s49\n\t"
      "s_waitcnt vmcnt(5)\n\t"
      "s_add_u32 m0, s17, 8192\n\t"
      "s_nop 0\n\t"
      "global_load_lds_dwordx4 v241, s[4:5]\n\t"
      "global_load_lds_dwordx4 v248, s[4:5] offset:1024\n\t"
      "global_load_lds_dwordx4 v249, s[4:5] offset:2048\n\t"
      "global_load_lds_dwordx4 v250, s[4:5] offset:3072\n\t"
      "s_sub_u32 s20, s20, 1\n\t"
      "s_cselect_b32 s48, -7, 1\n\t"
      "s_cselect_b32 s49, -1, 0\n\t"
      "s_mul_i32 s48, s48, 114688\n\t"
      "s_add_u32 s4, s4, s48\n\t"
      "s_addc_u32 s5, s5, s49\n\t"
      "ds_read_b64_tr_b16 v[0:1], v242\n\t"
      "ds_read_b64_tr_b16 v[2:3], v242 offset:512\n\t"
      "ds_read_b64_tr_b16 v[4:5], v251\n\t"
      "ds_read_b64_tr_b16 v[6:7], v251 offset:512\n\t"
      "ds_read_b64_tr_b16 v[8:9], v252\n\t"
      "ds_read_b64_tr_b16 v[10:11], v252 offset:512\n\t"
      "ds_read_b64_tr_b16 v[12:13], v253\n\t"
      "ds_read_b64_tr_b16 v[14:15], v253 offset:512\n\t"
      "s_waitcnt vmcnt(4)\n\t"
      "s_add_u32 m0, s17, 12288\n\t"
      "s_nop 0\n\t"
      "global_load_lds_dwordx4 v241, s[4:5]\n\t"
      "global_load_lds_dwordx4 v248, s[4:5] offset:1024\n\t"
      "global_load_lds_dwordx4 v249, s[4:5] offset:2048\n\t"
      "global_load_lds_dwordx4 v250, s[4:5] offset:3072\n\t"
      "s_sub_u32 s20, s20, 1\n\t"
      "s_cselect_b32 s48, -7, 1\n\t"
      "s_cselect_b32 s49, -1, 0\n\t"
      "s_mul_i32 s48, s48, 114688\n\t"
      "s_add_u32 s4, s4, s48\n\t"
      "s_addc_u32 s5, s5, s49\n\t"
      "ds_read_b64_tr_b16 v[16:17], v242 offset:4096\n\t"
      "ds_read_b64_tr_b16 v[18:19], v242 offset:4608\n\t"
      "ds_read_b64_tr_b16 v[20:21], v251 offset:4096\n\t"
      "ds_read_b64_tr_b16 v[22:23], v251 offset:4608\n\t"
      "ds_read_b64_tr_b16 v[24:25], v252 offset:4096\n\t"
      "ds_read_b64_tr_b16 v[26:27], v252 offset:4608\n\t"
      "ds_read_b64_tr_b16 v[28:29], v253 offset:4096\n\t"
      "ds_read_b64_tr_b16 v[30:31], v253 offset:4608\n\t"
      "v_xor_b32_e32 v164, 1024, v243\n\t"
      "v_xor_b32_e32 v165, 2048, v243\n\t"
      "v_xor_b32_e32 v166, 3072, v243\n\t"
      "v_lshlrev_b32_e32 v236, 16, v238\n\t"
      "v_and_b32_e32 v237, 0xffff0000, v238\n\t"
      "v_lshlrev_b32_e32 v238, 16, v239\n\t"
      "v_and_b32_e32 v239, 0xffff0000, v239\n\t"
      "v_mov_b32_e32 v235, 0xbdd2d3e8\n\t"
      "s_waitcnt vmcnt(4)\n\t"
      "s_waitcnt lgkmcnt(8)\n\t"
      "s_add_u32 m0, s17, 0\n\t"
      "s_nop 0\n\t"
      "global_load_lds_dwordx4 v241, s[4:5]\n\t"
      "global_load_lds_dwordx4 v248, s[4:5] offset:1024\n\t"
      "global_load_lds_dwordx4 v249, s[4:5] offset:2048\n\t"
      "global_load_lds_dwordx4 v250, s[4:5] offset:3072\n\t"
      "s_sub_u32 s20, s20, 1\n\t"
      "s_cselect_b32 s48, -7, 1\n\t"
      "s_cselect_b32 s49, -1, 0\n\t"
      "s_mul_i32 s48, s48, 114688\n\t"
      "s_add_u32 s4, s4, s48\n\t"
      "s_addc_u32 s5, s5, s49\n\t"
      "ds_read_b64_tr_b16 v[32:33], v242 offset:8192\n\t"
      "ds_read_b64_tr_b16 v[34:35], v242 offset:8704\n\t"
      "ds_read_b64_tr_b16 v[36:37], v251 offset:8192\n\t"
      "ds_read_b64_tr_b16 v[38:39], v251 offset:8704\n\t"
      "ds_read_b64_tr_b16 v[40:41], v252 offset:8192\n\t"
      "ds_read_b64_tr_b16 v[42:43], v252 offset:8704\n\t"
      "ds_read_b64_tr_b16 v[44:45], v253 offset:8192\n\t"
      "ds_read_b64_tr_b16 v[46:47], v253 offset:8704\n\t"
      "s_waitcnt vmcnt(4)\n\t"
      "s_waitcnt lgkmcnt(8)\n\t"
      "s_add_u32 m0, s17, 4096\n\t"
      "s_nop 0\n\t"
      "global_load_lds_dwordx4 v241, s[4:5]\n\t"
      "global_load_lds_dwordx4 v248, s[4:5] offset:1024\n\t"
      "global_load_lds_dwordx4 v249, s[4:5] offset:2048\n\t"
      "global_load_lds_dwordx4 v250, s[4:5] offset:3072\n\t"
      "s_sub_u32 s20, s20, 1\n\t"
      "s_cselect_b32 s48, -7, 1\n\t"
      "s_cselect_b32 s49, -1, 0\n\t"
      "s_mul_i32 s48, s48, 114688\n\t"
      "s_add_u32 s4, s4, s48\n\t"
      "s_addc_u32 s5, s5, s49\n\t"
      "ds_read_b64_tr_b16 v[48:49], v242 offset:12288\n\t"
      "ds_read_b64_tr_b16 v[50:51], v242 offset:12800\n\t"
      "ds_read_b64_tr_b16 v[52:53], v251 offset:12288\n\t"
      "ds_read_b64_tr_b16 v[54:55], v251 offset:12800\n\t"
      "ds_read_b64_tr_b16 v[56:57], v252 offset:12288\n\t"
      "ds_read_b64_tr_b16 v[58:59], v252 offset:12800\n\t"
      "ds_read_b64_tr_b16 v[60:61], v253 offset:12288\n\t"
      "ds_read_b64_tr_b16 v[62:63], v253 offset:12800\n\t"
      "s_waitcnt vmcnt(4)\n\t"
      "s_waitcnt lgkmcnt(8)\n\t"
      "s_add_u32 m0, s17, 8192\n\t"
      "s_nop 0\n\t"
      "global_load_lds_dwordx4 v241, s[4:5]\n\t"
      "global_load_lds_dwordx4 v248, s[4:5] offset:1024\n\t"
      "global_load_lds_dwordx4 v249, s[4:5] offset:2048\n\t"
      "global_load_lds_dwordx4 v250, s[4:5] offset:3072\n\t"
      "s_sub_u32 s20, s20, 1\n\t"
      "s_cselect_b32 s48, -7, 1\n\t"
      "s_cselect_b32 s49, -1, 0\n\t"
      "s_mul_i32 s48, s48, 114688\n\t"
      "s_add_u32 s4, s4, s48\n\t"
      "s_addc_u32 s5, s5, s49\n\t"
      "ds_read_b64_tr_b16 v[64:65], v242\n\t"
      "ds_read_b64_tr_b16 v[66:67], v242 offset:512\n\t"
      "ds_read_b64_tr_b16 v[68:69], v251\n\t"
      "ds_read_b64_tr_b16 v[70:71], v251 offset:512\n\t"
      "ds_read_b64_tr_b16 v[72:73], v252\n\t"
      "ds_read_b64_tr_b16 v[74:75], v252 offset:512\n\t"
      "ds_read_b64_tr_b16 v[76:77], v253\n\t"
      "ds_read_b64_tr_b16 v[78:79], v253 offset:512\n\t"
      "s_waitcnt vmcnt(4)\n\t"
      "s_waitcnt lgkmcnt(8)\n\t"
      "s_add_u32 m0, s17, 12288\n\t"
      "s_nop 0\n\t"
      "global_load_lds_dwordx4 v241, s[4:5]\n\t"
      "global_load_lds_dwordx4 v248, s[4:5] offset:1024\n\t"
      "global_load_lds_dwordx4 v249, s[4:5] offset:2048\n\t"
      "global_load_lds_dwordx4 v250, s[4:5] offset:3072\n\t"
      "ds_read_b64_tr_b16 v[80:81], v242 offset:4096\n\t"
      "ds_read_b64_tr_b16 v[82:83], v242 offset:4608\n\t"
      "ds_read_b64_tr_b16 v[84:85], v251 offset:4096\n\t"
      "ds_read_b64_tr_b16 v[86:87], v251 offset:4608\n\t"
      "ds_read_b64_tr_b16 v[88:89], v252 offset:4096\n\t"
      "ds_read_b64_tr_b16 v[90:91], v252 offset:4608\n\t"
      "ds_read_b64_tr_b16 v[92:93], v253 offset:4096\n\t"
      "ds_read_b64_tr_b16 v[94:95], v253 offset:4608\n\t"
      "s_waitcnt vmcnt(4)\n\t"
      "ds_read_b64_tr_b16 v[96:97], v242 offset:8192\n\t"
      "ds_read_b64_tr_b16 v[98:99], v242 offset:8704\n\t"
      "ds_read_b64_tr_b16 v[100:101], v251 offset:8192\n\t"
      "ds_read_b64_tr_b16 v[102:103], v251 offset:8704\n\t"
      "ds_read_b64_tr_b16 v[104:105], v252 offset:8192\n\t"
      "ds_read_b64_tr_b16 v[106:107], v252 offset:8704\n\t"
      "ds_read_b64_tr_b16 v[108:109], v253 offset:8192\n\t"
      "ds_read_b64_tr_b16 v[110:111], v253 offset:8704\n\t"
      "s_waitcnt vmcnt(0)\n\t"
      "ds_read_b64_tr_b16 v[112:113], v242 offset:12288\n\t"
      "ds_read_b64_tr_b16 v[114:115], v242 offset:12800\n\t"
      "ds_read_b64_tr_b16 v[116:117], v251 offset:12288\n\t"
      "ds_read_b64_tr_b16 v[118:119], v251 offset:12800\n\t"
      "ds_read_b64_tr_b16 v[120:121], v252 offset:12288\n\t"
      "ds_read_b64_tr_b16 v[122:123], v252 offset:12800\n\t"
      "ds_read_b64_tr_b16 v[124:125], v253 offset:12288\n\t"
      "ds_read_b64_tr_b16 v[126:127], v253 offset:12800\n\t"
      "s_waitcnt lgkmcnt(0)\n\t"
      "s_barrier\n\t"
      "s_mov_b32 s51, 0\n\t"
      "s_branch PRE_%=\n\t"
      "PRL_%=:\n\t"
      "s_sleep 4\n\t"
      "s_add_u32 s51, s51, 1\n\t"
      "s_cmp_lt_u32 s51, 262144\n\t"
      "s_cbranch_scc0 PRX_%=\n\t"
      "PRE_%=:\n\t"
      "s_mov_b64 exec, 65535\n\t"
      "s_bitcmp1_b32 s16, 4\n\t"
      "s_cbranch_scc1 PM_%=\n\t"
      "global_load_dwordx2 v[232:233], v247, s[12:13] sc1\n\t"
      "s_waitcnt vmcnt(0)\n\t"
      "PM_%=:\n\t"
      "s_cbranch_scc0 PN_%=\n\t"
      "v_mov_b64_e32 v[232:233], 0\n\t"
      "global_atomic_or_x2 v[232:233], v247, v[232:233], s[12:13] sc0 sc1\n\t"
      "s_waitcnt vmcnt(0)\n\t"
      "PN_%=:\n\t"
      "v_subrev_u32_e32 v232, s14, v232\n\t"
      "v_cmp_ne_u32_e64 s[48:49], s15, v233\n\t"
      "v_subrev_u32_e32 v232, 0, v232\n\t"
      "v_cmp_lt_u32_e32 vcc, 7, v232\n\t"
      "s_mov_b64 exec, -1\n\t"
      "s_or_b64 vcc, vcc, s[48:49]\n\t"
      "s_cbranch_vccnz PRL_%=\n\t"
      "PRX_%=:\n\t"
      "PRY_%=:\n\t"
      "global_load_dwordx4 v[128:131], v176, s[52:53] sc1\n\t"
      "global_load_dwordx4 v[132:135], v177, s[52:53] sc1\n\t"
      "global_load_dwordx4 v[136:139], v178, s[52:53] sc1\n\t"
      "global_load_dwordx4 v[140:143], v179, s[52:53] sc1\n\t"
      "global_load_dwordx4 v[144:147], v180, s[52:53] sc1\n\t"
      "global_load_dwordx4 v[148:151], v181, s[52:53] sc1\n\t"
      "global_load_dwordx4 v[152:155], v182, s[52:53] sc1\n\t"
      "global_load_dwordx4 v[156:159], v183, s[52:53] sc1\n\t"
      "s_cmp_ge_u32 s17, 65536\n\t"
      "s_cbranch_scc1 GQ_%=\n\t"
      ".p2align 6\n\t"
      "s_setprio 1\n\t"
      "v_mov_b32_e32 v232, s14\n\t"
      "v_mov_b32_e32 v233, s15\n\t"
      "v_lshl_add_u32 v247, v240, 10, s22\n\t"
      "s_waitcnt vmcnt(7)\n\t"
      "v_max3_u32 v234, v128, v129, v130\n\t"
      "v_max_u32_e32 v234, v234, v131\n\t"
      "v_cmp_eq_u32_e32 vcc, -1, v234\n\t"
      "s_cbranch_vccnz FP0R_%=\n\t"
      "FP0B_%=:\n\t"
      "v_mfma_f32_16x16x32_bf16 v[184:187], v[0:3], v[128:131], 0\n\t"
      "v_mfma_f32_16x16x32_bf16 v[188:191], v[4:7], v[128:131], 0\n\t"
      "v_mfma_f32_16x16x32_bf16 v[192:195], v[8:11], v[128:131], 0\n\t"
      "v_mfma_f32_16x16x32_bf16 v[196:199], v[12:15], v[128:131], 0\n\t"
      "global_load_dwordx4 v[128:131], v176, s[54:55] sc1\n\t"
      "s_waitcnt vmcnt(7)\n\t"
      "v_max3_u32 v234, v132, v133, v134\n\t"
      "v_max_u32_e32 v234, v234, v135\n\t"
      "v_cmp_eq_u32_e32 vcc, -1, v234\n\t"
      "s_cbranch_vccnz FP1R_%=\n\t"
      "FP1B_%=:\n\t"
      "v_mfma_f32_16x16x32_bf16 v[184:187], v[16:19], v[132:135], v[184:187]\n\t"
      "v_mfma_f32_16x16x32_bf16 v[188:191], v[20:23], v[132:135], v[188:191]\n\t"
      "v_mfma_f32_16x16x32_bf16 v[192:195], v[24:27], v[132:135], v[192:195]\n\t"
      "v_mfma_f32_16x16x32_bf16 v[196:199], v[28:31], v[132:135], v[196:199]\n\t"
      "global_load_dwordx4 v[132:135], v177, s[54:55] sc1\n\t"
      "s_waitcnt vmcnt(7)\n\t"
      "v_max3_u32 v234, v136, v137, v138\n\t"
      "v_max_u32_e32 v234, v234, v139\n\t"
      "v_cmp_eq_u32_e32 vcc, -1, v234\n\t"
      "s_cbranch_vccnz FP2R_%=\n\t"
      "FP2B_%=:\n\t"
      "v_mfma_f32_16x16x32_bf16 v[184:187], v[32:35], v[136:139], v[184:187]\n\t"
      "v_mfma_f32_16x16x32_bf16 v[188:191], v[36:39], v[136:139], v[188:191]\n\t"
      "v_mfma_f32_16x16x32_bf16 v[192:195], v[40:43], v[136:139], v[192:195]\n\t"
      "v_mfma_f32_16x16x32_bf16 v[196:199], v[44:47], v[136:139], v[196:199]\n\t"
      "global_load_dwordx4 v[136:139], v178, s[54:55] sc1\n\t"
      "s_waitcnt vmcnt(7)\n\t"
      "v_max3_u32 v234, v140, v141, v142\n\t"
      "v_max_u32_e32 v234, v234, v143\n\t"
      "v_cmp_eq_u32_e32 vcc, -1, v234\n\t"
      "s_cbranch_vccnz FP3R_%=\n\t"
      "FP3B_%=:\n\t"
      "v_mfma_f32_16x16x32_bf16 v[184:187], v[48:51], v[140:143], v[184:187]\n\t"
      "v_mfma_f32_16x16x32_bf16 v[188:191], v[52:55], v[140:143], v[188:191]\n\t"
      "v_mfma_f32_16x16x32_bf16 v[192:195], v[56:59], v[140:143], v[192:195]\n\t"
      "v_mfma_f32_16x16x32_bf16 v[196:199], v[60:63], v[140:143], v[196:199]\n\t"
      "global_load_dwordx4 v[140:143], v179, s[54:55] sc1\n\t"
      "s_waitcnt vmcnt(7)\n\t"
      "v_max3_u32 v234, v144, v145, v146\n\t"
      "v_max_u32_e32 v234, v234, v147\n\t"
      "v_cmp_eq_u32_e32 vcc, -1, v234\n\t"
      "s_cbranch_vccnz FP4R_%=\n\t"
      "FP4B_%=:\n\t"
      "v_mfma_f32_16x16x32_bf16 v[184:187], v[64:67], v[144:147], v[184:187]\n\t"
      "v_mfma_f32_16x16x32_bf16 v[188:191], v[68:71], v[144:147], v[188:191]\n\t"
      "v_mfma_f32_16x16x32_bf16 v[192:195], v[72:75], v[144:147], v[192:195]\n\t"
      "v_mfma_f32_16x16x32_bf16 v[196:199], v[76:79], v[144:147], v[196:199]\n\t"
      "global_load_dwordx4 v[144:147], v180, s[54:55] sc1\n\t"
      "s_waitcnt vmcnt(7)\n\t"
      "v_max3_u32 v234, v148, v149, v150\n\t"
      "v_max_u32_e32 v234, v234, v151\n\t"
      "v_cmp_eq_u32_e32 vcc, -1, v234\n\t"
      "s_cbranch_vccnz FP5R_%=\n\t"
      "FP5B_%=:\n\t"
      "v_mfma_f32_16x16x32_bf16 v[184:187], v[80:83], v[148:151], v[184:187]\n\t"
      "v_mfma_f32_16x16x32_bf16 v[188:191], v[84:87], v[148:151], v[188:191]\n\t"
      "v_mfma_f32_16x16x32_bf16 v[192:195], v[88:91], v[148:151], v[192:195]\n\t"
      "v_mfma_f32_16x16x32_bf16 v[196:199], v[92:95], v[148:151], v[196:199]\n\t"
      "global_load_dwordx4 v[148:151], v181, s[54:55] sc1\n\t"
      "s_waitcnt vmcnt(7)\n\t"
      "v_max3_u32 v234, v152, v153, v154\n\t"
      "v_max_u32_e32 v234, v234, v155\n\t"
      "v_cmp_eq_u32_e32 vcc, -1, v234\n\t"
      "s_cbranch_vccnz FP6R_%=\n\t"
      "FP6B_%=:\n\t"
      "v_mfma_f32_16x16x32_bf16 v[184:187], v[96:99], v[152:155], v[184:187]\n\t"
      "v_mfma_f32_16x16x32_bf16 v[188:191], v[100:103], v[152:155], v[188:191]\n\t"
      "v_mfma_f32_16x16x32_bf16 v[192:195], v[104:107], v[152:155], v[192:195]\n\t"
      "v_mfma_f32_16x16x32_bf16 v[196:199], v[108:111], v[152:155], v[196:199]\n\t"
      "global_load_dwordx4 v[152:155], v182, s[54:55] sc1\n\t"
      "s_waitcnt vmcnt(7)\n\t"
      "v_max3_u32 v234, v156, v157, v158\n\t"
      "v_max_u32_e32 v234, v234, v159\n\t"
      "v_cmp_eq_u32_e32 vcc, -1, v234\n\t"
      "s_cbranch_vccnz FP7R_%=\n\t"
      "FP7B_%=:\n\t"
      "v_mfma_f32_16x16x32_bf16 v[184:187], v[112:115], v[156:159], v[184:187]\n\t"
      "v_mfma_f32_16x16x32_bf16 v[188:191], v[116:119], v[156:159], v[188:191]\n\t"
      "v_mfma_f32_16x16x32_bf16 v[192:195], v[120:123], v[156:159], v[192:195]\n\t"
      "v_mfma_f32_16x16x32_bf16 v[196:199], v[124:127], v[156:159], v[196:199]\n\t"
      "global_load_dwordx4 v[156:159], v183, s[54:55] sc1\n\t"
      "s_nop 3\n\t"
      "ds_write_b128 v243, v[184:187]\n\t"
      "ds_write_b128 v164, v[188:191]\n\t"
      "ds_write_b128 v165, v[192:195]\n\t"
      "ds_write_b128 v166, v[196:199]\n\t"
      "s_waitcnt lgkmcnt(0)\n\t"
      "s_barrier\n\t"
      "ds_read_b128 v[216:219], v244\n\t"
      "ds_read_b128 v[220:223], v244 offset:4096\n\t"
      "ds_read_b128 v[224:227], v244 offset:8192\n\t"
      "ds_read_b128 v[228:231], v244 offset:12288\n\t"
      "ds_read_b128 v[184:187], v244 offset:16384\n\t"
      "ds_read_b128 v[188:191], v244 offset:20480\n\t"
      "ds_read_b128 v[192:195], v244 offset:24576\n\t"
      "ds_read_b128 v[196:199], v244 offset:28672\n\t"
      "s_waitcnt lgkmcnt(6)\n\t"
      "v_add_f32_e32 v216, v216, v220\n\t"
      "v_add_f32_e32 v217, v217, v221\n\t"
      "v_add_f32_e32 v218, v218, v222\n\t"
      "v_add_f32_e32 v219, v219, v223\n\t"
      "s_waitcnt lgkmcnt(4)\n\t"
      "v_add_f32_e32 v224, v224, v228\n\t"
      "v_add_f32_e32 v225, v225, v229\n\t"
      "v_add_f32_e32 v226, v226, v230\n\t"
      "v_add_f32_e32 v227, v227, v231\n\t"
      "s_waitcnt lgkmcnt(2)\n\t"
      "v_add_f32_e32 v184, v184, v188\n\t"
      "v_add_f32_e32 v185, v185, v189\n\t"
      "v_add_f32_e32 v186, v186, v190\n\t"
      "v_add_f32_e32 v187, v187, v191\n\t"
      "v_add_f32_e32 v216, v216, v224\n\t"
      "v_add_f32_e32 v217, v217, v225\n\t"
      "v_add_f32_e32 v218, v218, v226\n\t"
      "v_add_f32_e32 v219, v219, v227\n\t"
      "s_waitcnt lgkmcnt(0)\n\t"
      "v_add_f32_e32 v192, v192, v196\n\t"
      "s_waitcnt vmcnt(7)\n\t"
      "v_mfma_f32_16x16x32_bf16 v[200:203], v[0:3], v[128:131], 0\n\t"
      "v_add_f32_e32 v193, v193, v197\n\t"
      "v_add_f32_e32 v194, v194, v198\n\t"
      "v_mfma_f32_16x16x32_bf16 v[204:207], v[4:7], v[128:131], 0\n\t"
      "v_add_f32_e32 v195, v195, v199\n\t"
      "v_add_f32_e32 v184, v184, v192\n\t"
      "v_mfma_f32_16x16x32_bf16 v[208:211], v[8:11], v[128:131], 0\n\t"
      "v_add_f32_e32 v185, v185, v193\n\t"
      "v_add_f32_e32 v186, v186, v194\n\t"
      "v_mfma_f32_16x16x32_bf16 v[212:215], v[12:15], v[128:131], 0\n\t"
      "global_load_dwordx4 v[128:131], v176, s[56:57] sc1\n\t"
      "v_add_f32_e32 v187, v187, v195\n\t"
      "s_waitcnt vmcnt(7)\n\t"
      "v_mfma_f32_16x16x32_bf16 v[200:203], v[16:19], v[132:135], v[200:203]\n\t"
      "v_add_f32_e32 v216, v216, v184\n\t"
      "v_add_f32_e32 v217, v217, v185\n\t"
      "v_mfma_f32_16x16x32_bf16 v[204:207], v[20:23], v[132:135], v[204:207]\n\t"
      "v_add_f32_e32 v218, v218, v186\n\t"
      "v_add_f32_e32 v219, v219, v187\n\t"
      "v_mfma_f32_16x16x32_bf16 v[208:211], v[24:27], v[132:135], v[208:211]\n\t"
      "v_mul_f32_e32 v220, v216, v216\n\t"
      "v_mul_f32_e32 v221, v217, v217\n\t"
      "v_mfma_f32_16x16x32_bf16 v[212:215], v[28:31], v[132:135], v[212:215]\n\t"
      "global_load_dwordx4 v[132:135], v177, s[56:57] sc1\n\t"
      "v_mul_f32_e32 v222, v218, v218\n\t"
      "s_waitcnt vmcnt(7)\n\t"
      "v_mfma_f32_16x16x32_bf16 v[200:203], v[32:35], v[136:139], v[200:203]\n\t"
      "v_mul_f32_e32 v223, v219, v219\n\t"
      "v_fmaak_f32 v220, v235, v220, 0xc0135761\n\t"
      "v_mfma_f32_16x16x32_bf16 v[204:207], v[36:39], v[136:139], v[204:207]\n\t"
      "v_fmaak_f32 v221, v235, v221, 0xc0135761\n\t"
      "v_fmaak_f32 v222, v235, v222, 0xc0135761\n\t"
      "v_mfma_f32_16x16x32_bf16 v[208:211], v[40:43], v[136:139], v[208:211]\n\t"
      "v_fmaak_f32 v223, v235, v223, 0xc0135761\n\t"
      "v_mul_f32_e32 v220, v216, v220\n\t"
      "v_mfma_f32_16x16x32_bf16 v[212:215], v[44:47], v[136:139], v[212:215]\n\t"
      "global_load_dwordx4 v[136:139], v178, s[56:57] sc1\n\t"
      "v_mul_f32_e32 v221, v217, v221\n\t"
      "s_waitcnt vmcnt(7)\n\t"
      "v_mfma_f32_16x16x32_bf16 v[200:203], v[48:51], v[140:143], v[200:203]\n\t"
      "v_mul_f32_e32 v222, v218, v222\n\t"
      "v_mul_f32_e32 v223, v219, v223\n\t"
      "v_mfma_f32_16x16x32_bf16 v[204:207], v[52:55], v[140:143], v[204:207]\n\t"
      "v_exp_f32_e32 v220, v220\n\t"
      "v_mfma_f32_16x16x32_bf16 v[208:211], v[56:59], v[140:143], v[208:211]\n\t"
      "v_exp_f32_e32 v221, v221\n\t"
      "v_mfma_f32_16x16x32_bf16 v[212:215], v[60:63], v[140:143], v[212:215]\n\t"
      "global_load_dwordx4 v[140:143], v179, s[56:57] sc1\n\t"
      "s_waitcnt vmcnt(7)\n\t"
      "v_mfma_f32_16x16x32_bf16 v[200:203], v[64:67], v[144:147], v[200:203]\n\t"
      "v_exp_f32_e32 v222, v222\n\t"
      "v_mfma_f32_16x16x32_bf16 v[204:207], v[68:71], v[144:147], v[204:207]\n\t"
      "v_exp_f32_e32 v223, v223\n\t"
      "v_mfma_f32_16x16x32_bf16 v[208:211], v[72:75], v[144:147], v[208:211]\n\t"
      "v_add_f32_e32 v220, 1.0, v220\n\t"
      "v_mfma_f32_16x16x32_bf16 v[212:215], v[76:79], v[144:147], v[212:215]\n\t"
      "global_load_dwordx4 v[144:147], v180, s[56:57] sc1\n\t"
      "v_add_f32_e32 v221, 1.0, v221\n\t"
      "s_waitcnt vmcnt(7)\n\t"
      "v_mfma_f32_16x16x32_bf16 v[200:203], v[80:83], v[148:151], v[200:203]\n\t"
      "v_add_f32_e32 v222, 1.0, v222\n\t"
      "v_add_f32_e32 v223, 1.0, v223\n\t"
      "v_mfma_f32_16x16x32_bf16 v[204:207], v[84:87], v[148:151], v[204:207]\n\t"
      "v_rcp_f32_e32 v220, v220\n\t"
      "v_mfma_f32_16x16x32_bf16 v[208:211], v[88:91], v[148:151], v[208:211]\n\t"
      "v_rcp_f32_e32 v221, v221\n\t"
      "v_mfma_f32_16x16x32_bf16 v[212:215], v[92:95], v[148:151], v[212:215]\n\t"
      "global_load_dwordx4 v[148:151], v181, s[56:57] sc1\n\t"
      "s_waitcnt vmcnt(7)\n\t"
      "v_mfma_f32_16x16x32_bf16 v[200:203], v[96:99], v[152:155], v[200:203]\n\t"
      "v_rcp_f32_e32 v222, v222\n\t"
      "v_mfma_f32_16x16x32_bf16 v[204:207], v[100:103], v[152:155], v[204:207]\n\t"
      "v_rcp_f32_e32 v223, v223\n\t"
      "v_mfma_f32_16x16x32_bf16 v[208:211], v[104:107], v[152:155], v[208:211]\n\t"
      "v_mul_f32_e32 v216, v216, v220\n\t"
      "v_mfma_f32_16x16x32_bf16 v[212:215], v[108:111], v[152:155], v[212:215]\n\t"
      "global_load_dwordx4 v[152:155], v182, s[56:57] sc1\n\t"
      "v_mul_f32_e32 v217, v217, v221\n\t"
      "s_waitcnt vmcnt(7)\n\t"
      "v_mfma_f32_16x16x32_bf16 v[200:203], v[112:115], v[156:159], v[200:203]\n\t"
      "v_mul_f32_e32 v218, v218, v222\n\t"
      "v_mul_f32_e32 v219, v219, v223\n\t"
      "v_mfma_f32_16x16x32_bf16 v[204:207], v[116:119], v[156:159], v[204:207]\n\t"
      "v_cvt_pk_bf16_f32 v220, v216, v217\n\t"
      "v_cvt_pk_bf16_f32 v221, v218, v219\n\t"
      "v_mfma_f32_16x16x32_bf16 v[208:211], v[120:123], v[156:159], v[208:211]\n\t"
      "v_min_u32_e32 v220, -2, v220\n\t"
      "v_min_u32_e32 v221, -2, v221\n\t"
      "v_mfma_f32_16x16x32_bf16 v[212:215], v[124:127], v[156:159], v[212:215]\n\t"
      "global_load_dwordx4 v[156:159], v183, s[56:57] sc1\n\t"
      "global_store_dwordx2 v245, v[220:221], s[8:9] sc1\n\t"
      "s_add_u32 s8, s8, 131072\n\t"
      "s_addc_u32 s9, s9, 0\n\t"
      "v_cmp_u_f32_e32 vcc, v200, v200\n\t"
      "s_cbranch_vccnz NP1R_%=\n\t"
      "NP1B_%=:\n\t"
      "ds_write_b128 v243, v[200:203] offset:32768\n\t"
      "ds_write_b128 v164, v[204:207] offset:32768\n\t"
      "ds_write_b128 v165, v[208:211] offset:32768\n\t"
      "ds_write_b128 v166, v[212:215] offset:32768\n\t"
      "s_waitcnt lgkmcnt(0)\n\t"
      "s_barrier\n\t"
      "ds_read_b128 v[216:219], v244 offset:32768\n\t"
      "ds_read_b128 v[220:223], v244 offset:36864\n\t"
      "ds_read_b128 v[224:227], v244 offset:40960\n\t"
      "ds_read_b128 v[228:231], v244 offset:45056\n\t"
      "ds_read_b128 v[200:203], v244 offset:49152\n\t"
      "ds_read_b128 v[204:207], v244 offset:53248\n\t"
      "ds_read_b128 v[208:211], v244 offset:57344\n\t"
      "ds_read_b128 v[212:215], v244 offset:61440\n\t"
      "s_waitcnt lgkmcnt(6)\n\t"
      "v_add_f32_e32 v216, v216, v220\n\t"
      "v_add_f32_e32 v217, v217, v221\n\t"
      "v_add_f32_e32 v218, v218, v222\n\t"
      "v_add_f32_e32 v219, v219, v223\n\t"
      "s_waitcnt lgkmcnt(4)\n\t"
      "v_add_f32_e32 v224, v224, v228\n\t"
      "v_add_f32_e32 v225, v225, v229\n\t"
      "v_add_f32_e32 v226, v226, v230\n\t"
      "v_add_f32_e32 v227, v227, v231\n\t"
      "s_waitcnt lgkmcnt(2)\n\t"
      "v_add_f32_e32 v200, v200, v204\n\t"
      "v_add_f32_e32 v201, v201, v205\n\t"
      "v_add_f32_e32 v202, v202, v206\n\t"
      "v_add_f32_e32 v203, v203, v207\n\t"
      "v_add_f32_e32 v216, v216, v224\n\t"
      "v_add_f32_e32 v217, v217, v225\n\t"
      "v_add_f32_e32 v218, v218, v226\n\t"
      "v_add_f32_e32 v219, v219, v227\n\t"
      "s_waitcnt lgkmcnt(0)\n\t"
      "v_add_f32_e32 v208, v208, v212\n\t"
      "s_waitcnt vmcnt(8)\n\t"
      "v_mfma_f32_16x16x32_bf16 v[184:187], v[0:3], v[128:131], 0\n\t"
      "v_add_f32_e32 v209, v209, v213\n\t"
      "v_add_f32_e32 v210, v210, v214\n\t"
      "s_cmp_eq_u32 s17, 0\n\t"
      "s_cbranch_scc0 HI0_%=\n\t"
      "s_mov_b64 exec, 0xf\n\t"
      "global_store_dwordx2 v247, v[232:233], s[12:13] sc1\n\t"
      "s_mov_b64 exec, -1\n\t"
      "s_nop 0\n\t"
      "v_add_u32_e32 v232, 6, v232\n\t"
      "HI0_%=:\n\t"
      "v_mfma_f32_16x16x32_bf16 v[188:191], v[4:7], v[128:131], 0\n\t"
      "v_add_f32_e32 v211, v211, v215\n\t"
      "v_add_f32_e32 v200, v200, v208\n\t"
      "v_mfma_f32_16x16x32_bf16 v[192:195], v[8:11], v[128:131], 0\n\t"
      "v_add_f32_e32 v201, v201, v209\n\t"
      "v_add_f32_e32 v202, v202, v210\n\t"
      "v_mfma_f32_16x16x32_bf16 v[196:199], v[12:15], v[128:131], 0\n\t"
      "global_load_dwordx4 v[128:131], v176, s[58:59] sc1\n\t"
      "v_add_f32_e32 v203, v203, v211\n\t"
      "s_waitcnt vmcnt(8)\n\t"
      "v_mfma_f32_16x16x32_bf16 v[184:187], v[16:19], v[132:135], v[184:187]\n\t"
      "v_add_f32_e32 v216, v216, v200\n\t"
      "v_add_f32_e32 v217, v217, v201\n\t"
      "v_mfma_f32_16x16x32_bf16 v[188:191], v[20:23], v[132:135], v[188:191]\n\t"
      "v_add_f32_e32 v218, v218, v202\n\t"
      "v_add_f32_e32 v219, v219, v203\n\t"
      "v_mfma_f32_16x16x32_bf16 v[192:195], v[24:27], v[132:135], v[192:195]\n\t"
      "v_mul_f32_e32 v220, v216, v216\n\t"
      "v_mul_f32_e32 v221, v217, v217\n\t"
      "v_mfma_f32_16x16x32_bf16 v[196:199], v[28:31], v[132:135], v[196:199]\n\t"
      "global_load_dwordx4 v[132:135], v177, s[58:59] sc1\n\t"
      "v_mul_f32_e32 v222, v218, v218\n\t"
      "s_waitcnt vmcnt(8)\n\t"
      "v_mfma_f32_16x16x32_bf16 v[184:187], v[32:35], v[136:139], v[184:187]\n\t"
      "v_mul_f32_e32 v223, v219, v219\n\t"
      "v_fmaak_f32 v220, v235, v220, 0xc0135761\n\t"
      "v_mfma_f32_16x16x32_bf16 v[188:191], v[36:39], v[136:139], v[188:191]\n\t"
      "v_fmaak_f32 v221, v235, v221, 0xc0135761\n\t"
      "v_fmaak_f32 v222, v235, v222, 0xc0135761\n\t"
      "v_mfma_f32_16x16x32_bf16 v[192:195], v[40:43], v[136:139], v[192:195]\n\t"
      "v_fmaak_f32 v223, v235, v223, 0xc0135761\n\t"
      "v_mul_f32_e32 v220, v216, v220\n\t"
      "v_mfma_f32_16x16x32_bf16 v[196:199], v[44:47], v[136:139], v[196:199]\n\t"
      "global_load_dwordx4 v[136:139], v178, s[58:59] sc1\n\t"
      "v_mul_f32_e32 v221, v217, v221\n\t"
      "s_waitcnt vmcnt(8)\n\t"
      "v_mfma_f32_16x16x32_bf16 v[184:187], v[48:51], v[140:143], v[184:187]\n\t"
      "v_mul_f32_e32 v222, v218, v222\n\t"
      "v_mul_f32_e32 v223, v219, v223\n\t"
      "v_mfma_f32_16x16x32_bf16 v[188:191], v[52:55], v[140:143], v[188:191]\n\t"
      "v_exp_f32_e32 v220, v220\n\t"
      "v_mfma_f32_16x16x32_bf16 v[192:195], v[56:59], v[140:143], v[192:195]\n\t"
      "v_exp_f32_e32 v221, v221\n\t"
      "v_mfma_f32_16x16x32_bf16 v[196:199], v[60:63], v[140:143], v[196:199]\n\t"
      "global_load_dwordx4 v[140:143], v179, s[58:59] sc1\n\t"
      "s_waitcnt vmcnt(8)\n\t"
      "v_mfma_f32_16x16x32_bf16 v[184:187], v[64:67], v[144:147], v[184:187]\n\t"
      "v_exp_f32_e32 v222, v222\n\t"
      "v_mfma_f32_16x16x32_bf16 v[188:191], v[68:71], v[144:147], v[188:191]\n\t"
      "v_exp_f32_e32 v223, v223\n\t"
      "v_mfma_f32_16x16x32_bf16 v[192:195], v[72:75], v[144:147], v[192:195]\n\t"
      "v_add_f32_e32 v220, 1.0, v220\n\t"
      "v_mfma_f32_16x16x32_bf16 v[196:199], v[76:79], v[144:147], v[196:199]\n\t"
      "global_load_dwordx4 v[144:147], v180, s[58:59] sc1\n\t"
      "v_add_f32_e32 v221, 1.0, v221\n\t"
      "s_waitcnt vmcnt(8)\n\t"
      "v_mfma_f32_16x16x32_bf16 v[184:187], v[80:83], v[148:151], v[184:187]\n\t"
      "v_add_f32_e32 v222, 1.0, v222\n\t"
      "v_add_f32_e32 v223, 1.0, v223\n\t"
      "v_mfma_f32_16x16x32_bf16 v[188:191], v[84:87], v[148:151], v[188:191]\n\t"
      "v_rcp_f32_e32 v220, v220\n\t"
      "v_mfma_f32_16x16x32_bf16 v[192:195], v[88:91], v[148:151], v[192:195]\n\t"
      "v_rcp_f32_e32 v221, v221\n\t"
      "v_mfma_f32_16x16x32_bf16 v[196:199], v[92:95], v[148:151], v[196:199]\n\t"
      "global_load_dwordx4 v[148:151], v181, s[58:59] sc1\n\t"
      "s_waitcnt vmcnt(8)\n\t"
      "v_mfma_f32_16x16x32_bf16 v[184:187], v[96:99], v[152:155], v[184:187]\n\t"
      "v_rcp_f32_e32 v222, v222\n\t"
      "v_mfma_f32_16x16x32_bf16 v[188:191], v[100:103], v[152:155], v[188:191]\n\t"
      "v_rcp_f32_e32 v223, v223\n\t"
      "v_mfma_f32_16x16x32_bf16 v[192:195], v[104:107], v[152:155], v[192:195]\n\t"
      "v_mul_f32_e32 v216, v216, v220\n\t"
      "v_mfma_f32_16x16x32_bf16 v[196:199], v[108:111], v[152:155], v[196:199]\n\t"
      "global_load_dwordx4 v[152:155], v182, s[58:59] sc1\n\t"
      "v_mul_f32_e32 v217, v217, v221\n\t"
      "s_waitcnt vmcnt(8)\n\t"
      "v_mfma_f32_16x16x32_bf16 v[184:187], v[112:115], v[156:159], v[184:187]\n\t"
      "v_mul_f32_e32 v218, v218, v222\n\t"
      "v_mul_f32_e32 v219, v219, v223\n\t"
      "v_mfma_f32_16x16x32_bf16 v[188:191], v[116:119], v[156:159], v[188:191]\n\t"
      "v_cvt_pk_bf16_f32 v220, v216, v217\n\t"
      "v_cvt_pk_bf16_f32 v221, v218, v219\n\t"
      "v_mfma_f32_16x16x32_bf16 v[192:195], v[120:123], v[156:159], v[192:195]\n\t"
      "v_min_u32_e32 v220, -2, v220\n\t"
      "v_min_u32_e32 v221, -2, v221\n\t"
      "v_mfma_f32_16x16x32_bf16 v[196:199], v[124:127], v[156:159], v[196:199]\n\t"
      "global_load_dwordx4 v[156:159], v183, s[58:59] sc1\n\t"
      "global_store_dwordx2 v245, v[220:221], s[8:9] sc1\n\t"
      "s_add_u32 s8, s8, 131072\n\t"
      "s_addc_u32 s9, s9, 0\n\t"
      "v_cmp_u_f32_e32 vcc, v184, v184\n\t"
      "s_cbranch_vccnz NP2R_%=\n\t"
      "NP2B_%=:\n\t"
      "ds_write_b128 v243, v[184:187]\n\t"
      "ds_write_b128 v164, v[188:191]\n\t"
      "ds_write_b128 v165, v[192:195]\n\t"
      "ds_write_b128 v166, v[196:199]\n\t"
      "s_waitcnt lgkmcnt(0)\n\t"
      "s_barrier\n\t"
      "ds_read_b128 v[216:219], v244\n\t"
      "ds_read_b128 v[220:223], v244 offset:4096\n\t"
      "ds_read_b128 v[224:227], v244 offset:8192\n\t"
      "ds_read_b128 v[228:231], v244 offset:12288\n\t"
      "ds_read_b128 v[184:187], v244 offset:16384\n\t"
      "ds_read_b128 v[188:191], v244 offset:20480\n\t"
      "ds_read_b128 v[192:195], v244 offset:24576\n\t"
      "ds_read_b128 v[196:199], v244 offset:28672\n\t"
      "s_waitcnt lgkmcnt(6)\n\t"
      "v_add_f32_e32 v216, v216, v220\n\t"
      "v_add_f32_e32 v217, v217, v221\n\t"
      "v_add_f32_e32 v218, v218, v222\n\t"
      "v_add_f32_e32 v219, v219, v223\n\t"
      "s_waitcnt lgkmcnt(4)\n\t"
      "v_add_f32_e32 v224, v224, v228\n\t"
      "v_add_f32_e32 v225, v225, v229\n\t"
      "v_add_f32_e32 v226, v226, v230\n\t"
      "v_add_f32_e32 v227, v227, v231\n\t"
      "s_waitcnt lgkmcnt(2)\n\t"
      "v_add_f32_e32 v184, v184, v188\n\t"
      "v_add_f32_e32 v185, v185, v189\n\t"
      "v_add_f32_e32 v186, v186, v190\n\t"
      "v_add_f32_e32 v187, v187, v191\n\t"
      "v_add_f32_e32 v216, v216, v224\n\t"
      "v_add_f32_e32 v217, v217, v225\n\t"
      "v_add_f32_e32 v218, v218, v226\n\t"
      "v_add_f32_e32 v219, v219, v227\n\t"
      "s_waitcnt lgkmcnt(0)\n\t"
      "v_add_f32_e32 v192, v192, v196\n\t"
      "s_waitcnt vmcnt(8)\n\t"
      "v_mfma_f32_16x16x32_bf16 v[200:203], v[0:3], v[128:131], 0\n\t"
      "v_add_f32_e32 v193, v193, v197\n\t"
      "v_add_f32_e32 v194, v194, v198\n\t"
      "v_mfma_f32_16x16x32_bf16 v[204:207], v[4:7], v[128:131], 0\n\t"
      "v_add_f32_e32 v195, v195, v199\n\t"
      "v_add_f32_e32 v184, v184, v192\n\t"
      "v_mfma_f32_16x16x32_bf16 v[208:211], v[8:11], v[128:131], 0\n\t"
      "v_add_f32_e32 v185, v185, v193\n\t"
      "v_add_f32_e32 v186, v186, v194\n\t"
      "v_mfma_f32_16x16x32_bf16 v[212:215], v[12:15], v[128:131], 0\n\t"
      "global_load_dwordx4 v[128:131], v176, s[60:61] sc1\n\t"
      "v_add_f32_e32 v187, v187, v195\n\t"
      "s_waitcnt vmcnt(8)\n\t"
      "v_mfma_f32_16x16x32_bf16 v[200:203], v[16:19], v[132:135], v[200:203]\n\t"
      "v_add_f32_e32 v216, v216, v184\n\t"
      "v_add_f32_e32 v217, v217, v185\n\t"
      "v_mfma_f32_16x16x32_bf16 v[204:207], v[20:23], v[132:135], v[204:207]\n\t"
      "v_add_f32_e32 v218, v218, v186\n\t"
      "v_add_f32_e32 v219, v219, v187\n\t"
      "v_mfma_f32_16x16x32_bf16 v[208:211], v[24:27], v[132:135], v[208:211]\n\t"
      "v_mul_f32_e32 v220, v216, v216\n\t"
      "v_mul_f32_e32 v221, v217, v217\n\t"
      "v_mfma_f32_16x16x32_bf16 v[212:215], v[28:31], v[132:135], v[212:215]\n\t"
      "global_load_dwordx4 v[132:135], v177, s[60:61] sc1\n\t"
      "v_mul_f32_e32 v222, v218, v218\n\t"
      "s_waitcnt vmcnt(8)\n\t"
      "v_mfma_f32_16x16x32_bf16 v[200:203], v[32:35], v[136:139], v[200:203]\n\t"
      "v_mul_f32_e32 v223, v219, v219\n\t"
      "v_fmaak_f32 v220, v235, v220, 0xc0135761\n\t"
      "v_mfma_f32_16x16x32_bf16 v[204:207], v[36:39], v[136:139], v[204:207]\n\t"
      "v_fmaak_f32 v221, v235, v221, 0xc0135761\n\t"
      "v_fmaak_f32 v222, v235, v222, 0xc0135761\n\t"
      "v_mfma_f32_16x16x32_bf16 v[208:211], v[40:43], v[136:139], v[208:211]\n\t"
      "v_fmaak_f32 v223, v235, v223, 0xc0135761\n\t"
      "v_mul_f32_e32 v220, v216, v220\n\t"
      "v_mfma_f32_16x16x32_bf16 v[212:215], v[44:47], v[136:139], v[212:215]\n\t"
      "global_load_dwordx4 v[136:139], v178, s[60:61] sc1\n\t"
      "v_mul_f32_e32 v221, v217, v221\n\t"
      "s_waitcnt vmcnt(8)\n\t"
      "v_mfma_f32_16x16x32_bf16 v[200:203], v[48:51], v[140:143], v[200:203]\n\t"
      "v_mul_f32_e32 v222, v218, v222\n\t"
      "v_mul_f32_e32 v223, v219, v223\n\t"
      "v_mfma_f32_16x16x32_bf16 v[204:207], v[52:55], v[140:143], v[204:207]\n\t"
      "v_exp_f32_e32 v220, v220\n\t"
      "v_mfma_f32_16x16x32_bf16 v[208:211], v[56:59], v[140:143], v[208:211]\n\t"
      "v_exp_f32_e32 v221, v221\n\t"
      "v_mfma_f32_16x16x32_bf16 v[212:215], v[60:63], v[140:143], v[212:215]\n\t"
      "global_load_dwordx4 v[140:143], v179, s[60:61] sc1\n\t"
      "s_waitcnt vmcnt(8)\n\t"
      "v_mfma_f32_16x16x32_bf16 v[200:203], v[64:67], v[144:147], v[200:203]\n\t"
      "v_exp_f32_e32 v222, v222\n\t"
      "v_mfma_f32_16x16x32_bf16 v[204:207], v[68:71], v[144:147], v[204:207]\n\t"
      "v_exp_f32_e32 v223, v223\n\t"
      "v_mfma_f32_16x16x32_bf16 v[208:211], v[72:75], v[144:147], v[208:211]\n\t"
      "v_add_f32_e32 v220, 1.0, v220\n\t"
      "v_mfma_f32_16x16x32_bf16 v[212:215], v[76:79], v[144:147], v[212:215]\n\t"
      "global_load_dwordx4 v[144:147], v180, s[60:61] sc1\n\t"
      "v_add_f32_e32 v221, 1.0, v221\n\t"
      "s_waitcnt vmcnt(8)\n\t"
      "v_mfma_f32_16x16x32_bf16 v[200:203], v[80:83], v[148:151], v[200:203]\n\t"
      "v_add_f32_e32 v222, 1.0, v222\n\t"
      "v_add_f32_e32 v223, 1.0, v223\n\t"
      "v_mfma_f32_16x16x32_bf16 v[204:207], v[84:87], v[148:151], v[204:207]\n\t"
      "v_rcp_f32_e32 v220, v220\n\t"
      "v_mfma_f32_16x16x32_bf16 v[208:211], v[88:91], v[148:151], v[208:211]\n\t"
      "v_rcp_f32_e32 v221, v221\n\t"
      "v_mfma_f32_16x16x32_bf16 v[212:215], v[92:95], v[148:151], v[212:215]\n\t"
      "global_load_dwordx4 v[148:151], v181, s[60:61] sc1\n\t"
      "s_waitcnt vmcnt(8)\n\t"
      "v_mfma_f32_16x16x32_bf16 v[200:203], v[96:99], v[152:155], v[200:203]\n\t"
      "v_rcp_f32_e32 v222, v222\n\t"
      "v_mfma_f32_16x16x32_bf16 v[204:207], v[100:103], v[152:155], v[204:207]\n\t"
      "v_rcp_f32_e32 v223, v223\n\t"
      "v_mfma_f32_16x16x32_bf16 v[208:211], v[104:107], v[152:155], v[208:211]\n\t"
      "v_mul_f32_e32 v216, v216, v220\n\t"
      "v_mfma_f32_16x16x32_bf16 v[212:215], v[108:111], v[152:155], v[212:215]\n\t"
      "global_load_dwordx4 v[152:155], v182, s[60:61] sc1\n\t"
      "v_mul_f32_e32 v217, v217, v221\n\t"
      "s_waitcnt vmcnt(8)\n\t"
      "v_mfma_f32_16x16x32_bf16 v[200:203], v[112:115], v[156:159], v[200:203]\n\t"
      "v_mul_f32_e32 v218, v218, v222\n\t"
      "v_mul_f32_e32 v219, v219, v223\n\t"
      "v_mfma_f32_16x16x32_bf16 v[204:207], v[116:119], v[156:159], v[204:207]\n\t"
      "v_cvt_pk_bf16_f32 v220, v216, v217\n\t"
      "v_cvt_pk_bf16_f32 v221, v218, v219\n\t"
      "v_mfma_f32_16x16x32_bf16 v[208:211], v[120:123], v[156:159], v[208:211]\n\t"
      "v_min_u32_e32 v220, -2, v220\n\t"
      "v_min_u32_e32 v221, -2, v221\n\t"
      "v_mfma_f32_16x16x32_bf16 v[212:215], v[124:127], v[156:159], v[212:215]\n\t"
      "global_load_dwordx4 v[156:159], v183, s[60:61] sc1\n\t"
      "global_store_dwordx2 v245, v[220:221], s[8:9] sc1\n\t"
      "s_add_u32 s8, s8, 131072\n\t"
      "s_addc_u32 s9, s9, 0\n\t"
      "v_cmp_u_f32_e32 vcc, v200, v200\n\t"
      "s_cbranch_vccnz NP3R_%=\n\t"
      "NP3B_%=:\n\t"
      "ds_write_b128 v243, v[200:203] offset:32768\n\t"
      "ds_write_b128 v164, v[204:207] offset:32768\n\t"
      "ds_write_b128 v165, v[208:211] offset:32768\n\t"
      "ds_write_b128 v166, v[212:215] offset:32768\n\t"
      "s_waitcnt lgkmcnt(0)\n\t"
      "s_barrier\n\t"
      "ds_read_b128 v[216:219], v244 offset:32768\n\t"
      "ds_read_b128 v[220:223], v244 offset:36864\n\t"
      "ds_read_b128 v[224:227], v244 offset:40960\n\t"
      "ds_read_b128 v[228:231], v244 offset:45056\n\t"
      "ds_read_b128 v[200:203], v244 offset:49152\n\t"
      "ds_read_b128 v[204:207], v244 offset:53248\n\t"
      "ds_read_b128 v[208:211], v244 offset:57344\n\t"
      "ds_read_b128 v[212:215], v244 offset:61440\n\t"
      "s_waitcnt lgkmcnt(6)\n\t"
      "v_add_f32_e32 v216, v216, v220\n\t"
      "v_add_f32_e32 v217, v217, v221\n\t"
      "v_add_f32_e32 v218, v218, v222\n\t"
      "v_add_f32_e32 v219, v219, v223\n\t"
      "s_waitcnt lgkmcnt(4)\n\t"
      "v_add_f32_e32 v224, v224, v228\n\t"
      "v_add_f32_e32 v225, v225, v229\n\t"
      "v_add_f32_e32 v226, v226, v230\n\t"
      "v_add_f32_e32 v227, v227, v231\n\t"
      "s_waitcnt lgkmcnt(2)\n\t"
      "v_add_f32_e32 v200, v200, v204\n\t"
      "v_add_f32_e32 v201, v201, v205\n\t"
      "v_add_f32_e32 v202, v202, v206\n\t"
      "v_add_f32_e32 v203, v203, v207\n\t"
      "v_add_f32_e32 v216, v216, v224\n\t"
      "v_add_f32_e32 v217, v217, v225\n\t"
      "v_add_f32_e32 v218, v218, v226\n\t"
      "v_add_f32_e32 v219, v219, v227\n\t"
      "s_waitcnt lgkmcnt(0)\n\t"
      "v_add_f32_e32 v208, v208, v212\n\t"
      "s_waitcnt vmcnt(8)\n\t"
      "v_mfma_f32_16x16x32_bf16 v[184:187], v[0:3], v[128:131], 0\n\t"
      "v_add_f32_e32 v209, v209, v213\n\t"
      "v_add_f32_e32 v210, v210, v214\n\t"
      "v_mfma_f32_16x16x32_bf16 v[188:191], v[4:7], v[128:131], 0\n\t"
      "v_add_f32_e32 v211, v211, v215\n\t"
      "v_add_f32_e32 v200, v200, v208\n\t"
      "v_mfma_f32_16x16x32_bf16 v[192:195], v[8:11], v[128:131], 0\n\t"
      "v_add_f32_e32 v201, v201, v209\n\t"
      "v_add_f32_e32 v202, v202, v210\n\t"
      "v_mfma_f32_16x16x32_bf16 v[196:199], v[12:15], v[128:131], 0\n\t"
      "global_load_dwordx4 v[128:131], v176, s[62:63] sc1\n\t"
      "v_add_f32_e32 v203, v203, v211\n\t"
      "s_waitcnt vmcnt(8)\n\t"
      "v_mfma_f32_16x16x32_bf16 v[184:187], v[16:19], v[132:135], v[184:187]\n\t"
      "v_add_f32_e32 v216, v216, v200\n\t"
      "v_add_f32_e32 v217, v217, v201\n\t"
      "v_mfma_f32_16x16x32_bf16 v[188:191], v[20:23], v[132:135], v[188:191]\n\t"
      "v_add_f32_e32 v218, v218, v202\n\t"
      "v_add_f32_e32 v219, v219, v203\n\t"
      "v_mfma_f32_16x16x32_bf16 v[192:195], v[24:27], v[132:135], v[192:195]\n\t"
      "v_mul_f32_e32 v220, v216, v216\n\t"
      "v_mul_f32_e32 v221, v217, v217\n\t"
      "v_mfma_f32_16x16x32_bf16 v[196:199], v[28:31], v[132:135], v[196:199]\n\t"
      "global_load_dwordx4 v[132:135], v177, s[62:63] sc1\n\t"
      "v_mul_f32_e32 v222, v218, v218\n\t"
      "s_waitcnt vmcnt(8)\n\t"
      "v_mfma_f32_16x16x32_bf16 v[184:187], v[32:35], v[136:139], v[184:187]\n\t"
      "v_mul_f32_e32 v223, v219, v219\n\t"
      "v_fmaak_f32 v220, v235, v220, 0xc0135761\n\t"
      "v_mfma_f32_16x16x32_bf16 v[188:191], v[36:39], v[136:139], v[188:191]\n\t"
      "v_fmaak_f32 v221, v235, v221, 0xc0135761\n\t"
      "v_fmaak_f32 v222, v235, v222, 0xc0135761\n\t"
      "v_mfma_f32_16x16x32_bf16 v[192:195], v[40:43], v[136:139], v[192:195]\n\t"
      "v_fmaak_f32 v223, v235, v223, 0xc0135761\n\t"
      "v_mul_f32_e32 v220, v216, v220\n\t"
      "v_mfma_f32_16x16x32_bf16 v[196:199], v[44:47], v[136:139], v[196:199]\n\t"
      "global_load_dwordx4 v[136:139], v178, s[62:63] sc1\n\t"
      "v_mul_f32_e32 v221, v217, v221\n\t"
      "s_waitcnt vmcnt(8)\n\t"
      "v_mfma_f32_16x16x32_bf16 v[184:187], v[48:51], v[140:143], v[184:187]\n\t"
      "v_mul_f32_e32 v222, v218, v222\n\t"
      "v_mul_f32_e32 v223, v219, v223\n\t"
      "v_mfma_f32_16x16x32_bf16 v[188:191], v[52:55], v[140:143], v[188:191]\n\t"
      "v_exp_f32_e32 v220, v220\n\t"
      "v_mfma_f32_16x16x32_bf16 v[192:195], v[56:59], v[140:143], v[192:195]\n\t"
      "v_exp_f32_e32 v221, v221\n\t"
      "v_mfma_f32_16x16x32_bf16 v[196:199], v[60:63], v[140:143], v[196:199]\n\t"
      "global_load_dwordx4 v[140:143], v179, s[62:63] sc1\n\t"
      "s_waitcnt vmcnt(8)\n\t"
      "v_mfma_f32_16x16x32_bf16 v[184:187], v[64:67], v[144:147], v[184:187]\n\t"
      "v_exp_f32_e32 v222, v222\n\t"
      "v_mfma_f32_16x16x32_bf16 v[188:191], v[68:71], v[144:147], v[188:191]\n\t"
      "v_exp_f32_e32 v223, v223\n\t"
      "v_mfma_f32_16x16x32_bf16 v[192:195], v[72:75], v[144:147], v[192:195]\n\t"
      "v_add_f32_e32 v220, 1.0, v220\n\t"
      "v_mfma_f32_16x16x32_bf16 v[196:199], v[76:79], v[144:147], v[196:199]\n\t"
      "global_load_dwordx4 v[144:147], v180, s[62:63] sc1\n\t"
      "v_add_f32_e32 v221, 1.0, v221\n\t"
      "s_waitcnt vmcnt(8)\n\t"
      "v_mfma_f32_16x16x32_bf16 v[184:187], v[80:83], v[148:151], v[184:187]\n\t"
      "v_add_f32_e32 v222, 1.0, v222\n\t"
      "v_add_f32_e32 v223, 1.0, v223\n\t"
      "v_mfma_f32_16x16x32_bf16 v[188:191], v[84:87], v[148:151], v[188:191]\n\t"
      "v_rcp_f32_e32 v220, v220\n\t"
      "v_mfma_f32_16x16x32_bf16 v[192:195], v[88:91], v[148:151], v[192:195]\n\t"
      "v_rcp_f32_e32 v221, v221\n\t"
      "v_mfma_f32_16x16x32_bf16 v[196:199], v[92:95], v[148:151], v[196:199]\n\t"
      "global_load_dwordx4 v[148:151], v181, s[62:63] sc1\n\t"
      "s_waitcnt vmcnt(8)\n\t"
      "v_mfma_f32_16x16x32_bf16 v[184:187], v[96:99], v[152:155], v[184:187]\n\t"
      "v_rcp_f32_e32 v222, v222\n\t"
      "v_mfma_f32_16x16x32_bf16 v[188:191], v[100:103], v[152:155], v[188:191]\n\t"
      "v_rcp_f32_e32 v223, v223\n\t"
      "v_mfma_f32_16x16x32_bf16 v[192:195], v[104:107], v[152:155], v[192:195]\n\t"
      "v_mul_f32_e32 v216, v216, v220\n\t"
      "v_mfma_f32_16x16x32_bf16 v[196:199], v[108:111], v[152:155], v[196:199]\n\t"
      "global_load_dwordx4 v[152:155], v182, s[62:63] sc1\n\t"
      "v_mul_f32_e32 v217, v217, v221\n\t"
      "s_waitcnt vmcnt(8)\n\t"
      "v_mfma_f32_16x16x32_bf16 v[184:187], v[112:115], v[156:159], v[184:187]\n\t"
      "v_mul_f32_e32 v218, v218, v222\n\t"
      "v_mul_f32_e32 v219, v219, v223\n\t"
      "v_mfma_f32_16x16x32_bf16 v[188:191], v[116:119], v[156:159], v[188:191]\n\t"
      "v_cvt_pk_bf16_f32 v220, v216, v217\n\t"
      "v_cvt_pk_bf16_f32 v221, v218, v219\n\t"
      "v_mfma_f32_16x16x32_bf16 v[192:195], v[120:123], v[156:159], v[192:195]\n\t"
      "v_min_u32_e32 v220, -2, v220\n\t"
      "v_min_u32_e32 v221, -2, v221\n\t"
      "v_mfma_f32_16x16x32_bf16 v[196:199], v[124:127], v[156:159], v[196:199]\n\t"
      "global_load_dwordx4 v[156:159], v183, s[62:63] sc1\n\t"
      "global_store_dwordx2 v245, v[220:221], s[8:9] sc1\n\t"
      "s_add_u32 s8, s8, 131072\n\t"
      "s_addc_u32 s9, s9, 0\n\t"
      "v_cmp_u_f32_e32 vcc, v184, v184\n\t"
      "s_cbranch_vccnz NP4R_%=\n\t"
      "NP4B_%=:\n\t"
      "ds_write_b128 v243, v[184:187]\n\t"
      "ds_write_b128 v164, v[188:191]\n\t"
      "ds_write_b128 v165, v[192:195]\n\t"
      "ds_write_b128 v166, v[196:199]\n\t"
      "s_waitcnt lgkmcnt(0)\n\t"
      "s_barrier\n\t"
      "ds_read_b128 v[216:219], v244\n\t"
      "ds_read_b128 v[220:223], v244 offset:4096\n\t"
      "ds_read_b128 v[224:227], v244 offset:8192\n\t"
      "ds_read_b128 v[228:231], v244 offset:12288\n\t"
      "ds_read_b128 v[184:187], v244 offset:16384\n\t"
      "ds_read_b128 v[188:191], v244 offset:20480\n\t"
      "ds_read_b128 v[192:195], v244 offset:24576\n\t"
      "ds_read_b128 v[196:199], v244 offset:28672\n\t"
      "s_waitcnt lgkmcnt(6)\n\t"
      "v_add_f32_e32 v216, v216, v220\n\t"
      "v_add_f32_e32 v217, v217, v221\n\t"
      "v_add_f32_e32 v218, v218, v222\n\t"
      "v_add_f32_e32 v219, v219, v223\n\t"
      "s_waitcnt lgkmcnt(4)\n\t"
      "v_add_f32_e32 v224, v224, v228\n\t"
      "v_add_f32_e32 v225, v225, v229\n\t"
      "v_add_f32_e32 v226, v226, v230\n\t"
      "v_add_f32_e32 v227, v227, v231\n\t"
      "s_waitcnt lgkmcnt(2)\n\t"
      "v_add_f32_e32 v184, v184, v188\n\t"
      "v_add_f32_e32 v185, v185, v189\n\t"
      "v_add_f32_e32 v186, v186, v190\n\t"
      "v_add_f32_e32 v187, v187, v191\n\t"
      "v_add_f32_e32 v216, v216, v224\n\t"
      "v_add_f32_e32 v217, v217, v225\n\t"
      "v_add_f32_e32 v218, v218, v226\n\t"
      "v_add_f32_e32 v219, v219, v227\n\t"
      "s_waitcnt lgkmcnt(0)\n\t"
      "v_add_f32_e32 v192, v192, v196\n\t"
      "s_waitcnt vmcnt(8)\n\t"
      "v_mfma_f32_16x16x32_bf16 v[200:203], v[0:3], v[128:131], 0\n\t"
      "v_add_f32_e32 v193, v193, v197\n\t"
      "v_add_f32_e32 v194, v194, v198\n\t"
      "v_mfma_f32_16x16x32_bf16 v[204:207], v[4:7], v[128:131], 0\n\t"
      "v_add_f32_e32 v195, v195, v199\n\t"
      "v_add_f32_e32 v184, v184, v192\n\t"
      "v_mfma_f32_16x16x32_bf16 v[208:211], v[8:11], v[128:131], 0\n\t"
      "v_add_f32_e32 v185, v185, v193\n\t"
      "v_add_f32_e32 v186, v186, v194\n\t"
      "v_mfma_f32_16x16x32_bf16 v[212:215], v[12:15], v[128:131], 0\n\t"
      "global_load_dwordx4 v[128:131], v176, s[64:65] sc1\n\t"
      "v_add_f32_e32 v187, v187, v195\n\t"
      "s_waitcnt vmcnt(8)\n\t"
      "v_mfma_f32_16x16x32_bf16 v[200:203], v[16:19], v[132:135], v[200:203]\n\t"
      "v_add_f32_e32 v216, v216, v184\n\t"
      "v_add_f32_e32 v217, v217, v185\n\t"
      "v_mfma_f32_16x16x32_bf16 v[204:207], v[20:23], v[132:135], v[204:207]\n\t"
      "v_add_f32_e32 v218, v218, v186\n\t"
      "v_add_f32_e32 v219, v219, v187\n\t"
      "v_mfma_f32_16x16x32_bf16 v[208:211], v[24:27], v[132:135], v[208:211]\n\t"
      "v_mul_f32_e32 v220, v216, v216\n\t"
      "v_mul_f32_e32 v221, v217, v217\n\t"
      "v_mfma_f32_16x16x32_bf16 v[212:215], v[28:31], v[132:135], v[212:215]\n\t"
      "global_load_dwordx4 v[132:135], v177, s[64:65] sc1\n\t"
      "v_mul_f32_e32 v222, v218, v218\n\t"
      "s_waitcnt vmcnt(8)\n\t"
      "v_mfma_f32_16x16x32_bf16 v[200:203], v[32:35], v[136:139], v[200:203]\n\t"
      "v_mul_f32_e32 v223, v219, v219\n\t"
      "v_fmaak_f32 v220, v235, v220, 0xc0135761\n\t"
      "v_mfma_f32_16x16x32_bf16 v[204:207], v[36:39], v[136:139], v[204:207]\n\t"
      "v_fmaak_f32 v221, v235, v221, 0xc0135761\n\t"
      "v_fmaak_f32 v222, v235, v222, 0xc0135761\n\t"
      "v_mfma_f32_16x16x32_bf16 v[208:211], v[40:43], v[136:139], v[208:211]\n\t"
      "v_fmaak_f32 v223, v235, v223, 0xc0135761\n\t"
      "v_mul_f32_e32 v220, v216, v220\n\t"
      "v_mfma_f32_16x16x32_bf16 v[212:215], v[44:47], v[136:139], v[212:215]\n\t"
      "global_load_dwordx4 v[136:139], v178, s[64:65] sc1\n\t"
      "v_mul_f32_e32 v221, v217, v221\n\t"
      "s_waitcnt vmcnt(8)\n\t"
      "v_mfma_f32_16x16x32_bf16 v[200:203], v[48:51], v[140:143], v[200:203]\n\t"
      "v_mul_f32_e32 v222, v218, v222\n\t"
      "v_mul_f32_e32 v223, v219, v223\n\t"
      "v_mfma_f32_16x16x32_bf16 v[204:207], v[52:55], v[140:143], v[204:207]\n\t"
      "v_exp_f32_e32 v220, v220\n\t"
      "v_mfma_f32_16x16x32_bf16 v[208:211], v[56:59], v[140:143], v[208:211]\n\t"
      "v_exp_f32_e32 v221, v221\n\t"
      "v_mfma_f32_16x16x32_bf16 v[212:215], v[60:63], v[140:143], v[212:215]\n\t"
      "global_load_dwordx4 v[140:143], v179, s[64:65] sc1\n\t"
      "s_waitcnt vmcnt(8)\n\t"
      "v_mfma_f32_16x16x32_bf16 v[200:203], v[64:67], v[144:147], v[200:203]\n\t"
      "v_exp_f32_e32 v222, v222\n\t"
      "v_mfma_f32_16x16x32_bf16 v[204:207], v[68:71], v[144:147], v[204:207]\n\t"
      "v_exp_f32_e32 v223, v223\n\t"
      "v_mfma_f32_16x16x32_bf16 v[208:211], v[72:75], v[144:147], v[208:211]\n\t"
      "v_add_f32_e32 v220, 1.0, v220\n\t"
      "v_mfma_f32_16x16x32_bf16 v[212:215], v[76:79], v[144:147], v[212:215]\n\t"
      "global_load_dwordx4 v[144:147], v180, s[64:65] sc1\n\t"
      "v_add_f32_e32 v221, 1.0, v221\n\t"
      "s_waitcnt vmcnt(8)\n\t"
      "v_mfma_f32_16x16x32_bf16 v[200:203], v[80:83], v[148:151], v[200:203]\n\t"
      "v_add_f32_e32 v222, 1.0, v222\n\t"
      "v_add_f32_e32 v223, 1.0, v223\n\t"
      "v_mfma_f32_16x16x32_bf16 v[204:207], v[84:87], v[148:151], v[204:207]\n\t"
      "v_rcp_f32_e32 v220, v220\n\t"
      "v_mfma_f32_16x16x32_bf16 v[208:211], v[88:91], v[148:151], v[208:211]\n\t"
      "v_rcp_f32_e32 v221, v221\n\t"
      "v_mfma_f32_16x16x32_bf16 v[212:215], v[92:95], v[148:151], v[212:215]\n\t"
      "global_load_dwordx4 v[148:151], v181, s[64:65] sc1\n\t"
      "s_waitcnt vmcnt(8)\n\t"
      "v_mfma_f32_16x16x32_bf16 v[200:203], v[96:99], v[152:155], v[200:203]\n\t"
      "v_rcp_f32_e32 v222, v222\n\t"
      "v_mfma_f32_16x16x32_bf16 v[204:207], v[100:103], v[152:155], v[204:207]\n\t"
      "v_rcp_f32_e32 v223, v223\n\t"
      "v_mfma_f32_16x16x32_bf16 v[208:211], v[104:107], v[152:155], v[208:211]\n\t"
      "v_mul_f32_e32 v216, v216, v220\n\t"
      "v_mfma_f32_16x16x32_bf16 v[212:215], v[108:111], v[152:155], v[212:215]\n\t"
      "global_load_dwordx4 v[152:155], v182, s[64:65] sc1\n\t"
      "v_mul_f32_e32 v217, v217, v221\n\t"
      "s_waitcnt vmcnt(8)\n\t"
      "v_mfma_f32_16x16x32_bf16 v[200:203], v[112:115], v[156:159], v[200:203]\n\t"
      "v_mul_f32_e32 v218, v218, v222\n\t"
      "v_mul_f32_e32 v219, v219, v223\n\t"
      "v_mfma_f32_16x16x32_bf16 v[204:207], v[116:119], v[156:159], v[204:207]\n\t"
      "v_cvt_pk_bf16_f32 v220, v216, v217\n\t"
      "v_cvt_pk_bf16_f32 v221, v218, v219\n\t"
      "v_mfma_f32_16x16x32_bf16 v[208:211], v[120:123], v[156:159], v[208:211]\n\t"
      "v_min_u32_e32 v220, -2, v220\n\t"
      "v_min_u32_e32 v221, -2, v221\n\t"
      "v_mfma_f32_16x16x32_bf16 v[212:215], v[124:127], v[156:159], v[212:215]\n\t"
      "global_load_dwordx4 v[156:159], v183, s[64:65] sc1\n\t"
      "global_store_dwordx2 v245, v[220:221], s[8:9] sc1\n\t"
      "s_add_u32 s8, s8, 131072\n\t"
      "s_addc_u32 s9, s9, 0\n\t"
      "v_cmp_u_f32_e32 vcc, v200, v200\n\t"
      "s_cbranch_vccnz NP5R_%=\n\t"
      "NP5B_%=:\n\t"
      "ds_write_b128 v243, v[200:203] offset:32768\n\t"
      "ds_write_b128 v164, v[204:207] offset:32768\n\t"
      "ds_write_b128 v165, v[208:211] offset:32768\n\t"
      "ds_write_b128 v166, v[212:215] offset:32768\n\t"
      "s_waitcnt lgkmcnt(0)\n\t"
      "s_barrier\n\t"
      "ds_read_b128 v[216:219], v244 offset:32768\n\t"
      "ds_read_b128 v[220:223], v244 offset:36864\n\t"
      "ds_read_b128 v[224:227], v244 offset:40960\n\t"
      "ds_read_b128 v[228:231], v244 offset:45056\n\t"
      "ds_read_b128 v[200:203], v244 offset:49152\n\t"
      "ds_read_b128 v[204:207], v244 offset:53248\n\t"
      "ds_read_b128 v[208:211], v244 offset:57344\n\t"
      "ds_read_b128 v[212:215], v244 offset:61440\n\t"
      "s_waitcnt lgkmcnt(6)\n\t"
      "v_add_f32_e32 v216, v216, v220\n\t"
      "v_add_f32_e32 v217, v217, v221\n\t"
      "v_add_f32_e32 v218, v218, v222\n\t"
      "v_add_f32_e32 v219, v219, v223\n\t"
      "s_waitcnt lgkmcnt(4)\n\t"
      "v_add_f32_e32 v224, v224, v228\n\t"
      "v_add_f32_e32 v225, v225, v229\n\t"
      "s_waitcnt vmcnt(8)\n\t"
      "v_mfma_f32_16x16x32_bf16 v[184:187], v[0:3], v[128:131], 0\n\t"
      "v_add_f32_e32 v226, v226, v230\n\t"
      "v_add_f32_e32 v227, v227, v231\n\t"
      "v_mfma_f32_16x16x32_bf16 v[188:191], v[4:7], v[128:131], 0\n\t"
      "s_waitcnt lgkmcnt(2)\n\t"
      "v_add_f32_e32 v200, v200, v204\n\t"
      "v_add_f32_e32 v201, v201, v205\n\t"
      "v_mfma_f32_16x16x32_bf16 v[192:195], v[8:11], v[128:131], 0\n\t"
      "v_add_f32_e32 v202, v202, v206\n\t"
      "v_add_f32_e32 v203, v203, v207\n\t"
      "v_mfma_f32_16x16x32_bf16 v[196:199], v[12:15], v[128:131], 0\n\t"
      "v_add_f32_e32 v216, v216, v224\n\t"
      "v_add_f32_e32 v217, v217, v225\n\t"
      "s_waitcnt vmcnt(7)\n\t"
      "v_mfma_f32_16x16x32_bf16 v[184:187], v[16:19], v[132:135], v[184:187]\n\t"
      "v_add_f32_e32 v218, v218, v226\n\t"
      "v_add_f32_e32 v219, v219, v227\n\t"
      "v_mfma_f32_16x16x32_bf16 v[188:191], v[20:23], v[132:135], v[188:191]\n\t"
      "s_waitcnt lgkmcnt(0)\n\t"
      "v_add_f32_e32 v208, v208, v212\n\t"
      "v_add_f32_e32 v209, v209, v213\n\t"
      "v_mfma_f32_16x16x32_bf16 v[192:195], v[24:27], v[132:135], v[192:195]\n\t"
      "v_add_f32_e32 v210, v210, v214\n\t"
      "v_add_f32_e32 v211, v211, v215\n\t"
      "v_mfma_f32_16x16x32_bf16 v[196:199], v[28:31], v[132:135], v[196:199]\n\t"
      "v_add_f32_e32 v200, v200, v208\n\t"
      "v_add_f32_e32 v201, v201, v209\n\t"
      "s_waitcnt vmcnt(6)\n\t"
      "v_mfma_f32_16x16x32_bf16 v[184:187], v[32:35], v[136:139], v[184:187]\n\t"
      "v_add_f32_e32 v202, v202, v210\n\t"
      "v_add_f32_e32 v203, v203, v211\n\t"
      "v_mfma_f32_16x16x32_bf16 v[188:191], v[36:39], v[136:139], v[188:191]\n\t"
      "v_add_f32_e32 v216, v216, v200\n\t"
      "v_add_f32_e32 v217, v217, v201\n\t"
      "v_mfma_f32_16x16x32_bf16 v[192:195], v[40:43], v[136:139], v[192:195]\n\t"
      "v_add_f32_e32 v218, v218, v202\n\t"
      "v_add_f32_e32 v219, v219, v203\n\t"
      "v_mfma_f32_16x16x32_bf16 v[196:199], v[44:47], v[136:139], v[196:199]\n\t"
      "v_mul_f32_e32 v220, v216, v216\n\t"
      "v_mul_f32_e32 v221, v217, v217\n\t"
      "s_waitcnt vmcnt(5)\n\t"
      "v_mfma_f32_16x16x32_bf16 v[184:187], v[48:51], v[140:143], v[184:187]\n\t"
      "v_mul_f32_e32 v222, v218, v218\n\t"
      "v_mul_f32_e32 v223, v219, v219\n\t"
      "v_mfma_f32_16x16x32_bf16 v[188:191], v[52:55], v[140:143], v[188:191]\n\t"
      "v_fmaak_f32 v220, v235, v220, 0xc0135761\n\t"
      "v_fmaak_f32 v221, v235, v221, 0xc0135761\n\t"
      "v_mfma_f32_16x16x32_bf16 v[192:195], v[56:59], v[140:143], v[192:195]\n\t"
      "v_fmaak_f32 v222, v235, v222, 0xc0135761\n\t"
      "v_fmaak_f32 v223, v235, v223, 0xc0135761\n\t"
      "v_mfma_f32_16x16x32_bf16 v[196:199], v[60:63], v[140:143], v[196:199]\n\t"
      "v_mul_f32_e32 v220, v216, v220\n\t"
      "v_mul_f32_e32 v221, v217, v221\n\t"
      "s_waitcnt vmcnt(4)\n\t"
      "v_mfma_f32_16x16x32_bf16 v[184:187], v[64:67], v[144:147], v[184:187]\n\t"
      "v_mul_f32_e32 v222, v218, v222\n\t"
      "v_mul_f32_e32 v223, v219, v223\n\t"
      "v_mfma_f32_16x16x32_bf16 v[188:191], v[68:71], v[144:147], v[188:191]\n\t"
      "v_exp_f32_e32 v220, v220\n\t"
      "v_mfma_f32_16x16x32_bf16 v[192:195], v[72:75], v[144:147], v[192:195]\n\t"
      "v_exp_f32_e32 v221, v221\n\t"
      "v_mfma_f32_16x16x32_bf16 v[196:199], v[76:79], v[144:147], v[196:199]\n\t"
      "v_exp_f32_e32 v222, v222\n\t"
      "s_waitcnt vmcnt(3)\n\t"
      "v_mfma_f32_16x16x32_bf16 v[184:187], v[80:83], v[148:151], v[184:187]\n\t"
      "v_exp_f32_e32 v223, v223\n\t"
      "v_mfma_f32_16x16x32_bf16 v[188:191], v[84:87], v[148:151], v[188:191]\n\t"
      "v_add_f32_e32 v220, 1.0, v220\n\t"
      "v_add_f32_e32 v221, 1.0, v221\n\t"
      "v_mfma_f32_16x16x32_bf16 v[192:195], v[88:91], v[148:151], v[192:195]\n\t"
      "v_add_f32_e32 v222, 1.0, v222\n\t"
      "v_add_f32_e32 v223, 1.0, v223\n\t"
      "v_mfma_f32_16x16x32_bf16 v[196:199], v[92:95], v[148:151], v[196:199]\n\t"
      "v_rcp_f32_e32 v220, v220\n\t"
      "s_waitcnt vmcnt(2)\n\t"
      "v_mfma_f32_16x16x32_bf16 v[184:187], v[96:99], v[152:155], v[184:187]\n\t"
      "v_rcp_f32_e32 v221, v221\n\t"
      "v_mfma_f32_16x16x32_bf16 v[188:191], v[100:103], v[152:155], v[188:191]\n\t"
      "v_rcp_f32_e32 v222, v222\n\t"
      "v_mfma_f32_16x16x32_bf16 v[192:195], v[104:107], v[152:155], v[192:195]\n\t"
      "v_rcp_f32_e32 v223, v223\n\t"
      "v_mfma_f32_16x16x32_bf16 v[196:199], v[108:111], v[152:155], v[196:199]\n\t"
      "v_mul_f32_e32 v216, v216, v220\n\t"
      "s_waitcnt vmcnt(1)\n\t"
      "v_mfma_f32_16x16x32_bf16 v[184:187], v[112:115], v[156:159], v[184:187]\n\t"
      "v_mul_f32_e32 v217, v217, v221\n\t"
      "v_mul_f32_e32 v218, v218, v222\n\t"
      "v_mfma_f32_16x16x32_bf16 v[188:191], v[116:119], v[156:159], v[188:191]\n\t"
      "v_mul_f32_e32 v219, v219, v223\n\t"
      "v_cvt_pk_bf16_f32 v220, v216, v217\n\t"
      "v_mfma_f32_16x16x32_bf16 v[192:195], v[120:123], v[156:159], v[192:195]\n\t"
      "v_cvt_pk_bf16_f32 v221, v218, v219\n\t"
      "v_min_u32_e32 v220, -2, v220\n\t"
      "v_mfma_f32_16x16x32_bf16 v[196:199], v[124:127], v[156:159], v[196:199]\n\t"
      "v_min_u32_e32 v221, -2, v221\n\t"
      "global_store_dwordx2 v245, v[220:221], s[8:9] sc1\n\t"
      "s_add_u32 s8, s8, 131072\n\t"
      "s_addc_u32 s9, s9, 0\n\t"
      "v_cmp_u_f32_e32 vcc, v184, v184\n\t"
      "s_cbranch_vccnz NP6R_%=\n\t"
      "NP6B_%=:\n\t"
      "ds_write_b128 v243, v[184:187]\n\t"
      "ds_write_b128 v164, v[188:191]\n\t"
      "ds_write_b128 v165, v[192:195]\n\t"
      "ds_write_b128 v166, v[196:199]\n\t"
      "s_waitcnt lgkmcnt(0)\n\t"
      "s_barrier\n\t"
      "ds_read_b128 v[216:219], v244\n\t"
      "ds_read_b128 v[220:223], v244 offset:4096\n\t"
      "ds_read_b128 v[224:227], v244 offset:8192\n\t"
      "ds_read_b128 v[228:231], v244 offset:12288\n\t"
      "ds_read_b128 v[184:187], v244 offset:16384\n\t"
      "ds_read_b128 v[188:191], v244 offset:20480\n\t"
      "ds_read_b128 v[192:195], v244 offset:24576\n\t"
      "ds_read_b128 v[196:199], v244 offset:28672\n\t"
      "s_waitcnt lgkmcnt(6)\n\t"
      "v_add_f32_e32 v216, v216, v220\n\t"
      "v_add_f32_e32 v217, v217, v221\n\t"
      "v_add_f32_e32 v218, v218, v222\n\t"
      "v_add_f32_e32 v219, v219, v223\n\t"
      "s_waitcnt lgkmcnt(4)\n\t"
      "v_add_f32_e32 v224, v224, v228\n\t"
      "v_add_f32_e32 v225, v225, v229\n\t"
      "v_add_f32_e32 v226, v226, v230\n\t"
      "v_add_f32_e32 v227, v227, v231\n\t"
      "s_waitcnt lgkmcnt(2)\n\t"
      "v_add_f32_e32 v184, v184, v188\n\t"
      "v_add_f32_e32 v185, v185, v189\n\t"
      "v_add_f32_e32 v186, v186, v190\n\t"
      "v_add_f32_e32 v187, v187, v191\n\t"
      "v_add_f32_e32 v216, v216, v224\n\t"
      "v_add_f32_e32 v217, v217, v225\n\t"
      "v_add_f32_e32 v218, v218, v226\n\t"
      "v_add_f32_e32 v219, v219, v227\n\t"
      "s_waitcnt lgkmcnt(0)\n\t"
      "v_add_f32_e32 v192, v192, v196\n\t"
      "v_add_f32_e32 v193, v193, v197\n\t"
      "v_add_f32_e32 v194, v194, v198\n\t"
      "v_add_f32_e32 v195, v195, v199\n\t"
      "v_add_f32_e32 v184, v184, v192\n\t"
      "v_add_f32_e32 v185, v185, v193\n\t"
      "v_add_f32_e32 v186, v186, v194\n\t"
      "v_add_f32_e32 v187, v187, v195\n\t"
      "v_add_f32_e32 v216, v216, v184\n\t"
      "v_add_f32_e32 v217, v217, v185\n\t"
      "v_add_f32_e32 v218, v218, v186\n\t"
      "v_add_f32_e32 v219, v219, v187\n\t"
      "v_mul_f32_e32 v220, v216, v216\n\t"
      "v_mul_f32_e32 v221, v217, v217\n\t"
      "v_mul_f32_e32 v222, v218, v218\n\t"
      "v_mul_f32_e32 v223, v219, v219\n\t"
      "v_fmaak_f32 v220, v235, v220, 0xc0135761\n\t"
      "v_fmaak_f32 v221, v235, v221, 0xc0135761\n\t"
      "v_fmaak_f32 v222, v235, v222, 0xc0135761\n\t"
      "v_fmaak_f32 v223, v235, v223, 0xc0135761\n\t"
      "v_mul_f32_e32 v220, v216, v220\n\t"
      "v_mul_f32_e32 v221, v217, v221\n\t"
      "v_mul_f32_e32 v222, v218, v222\n\t"
      "v_mul_f32_e32 v223, v219, v223\n\t"
      "v_exp_f32_e32 v220, v220\n\t"
      "v_exp_f32_e32 v221, v221\n\t"
      "v_exp_f32_e32 v222, v222\n\t"
      "v_exp_f32_e32 v223, v223\n\t"
      "v_add_f32_e32 v220, 1.0, v220\n\t"
      "v_add_f32_e32 v221, 1.0, v221\n\t"
      "v_add_f32_e32 v222, 1.0, v222\n\t"
      "v_add_f32_e32 v223, 1.0, v223\n\t"
      "v_rcp_f32_e32 v220, v220\n\t"
      "v_rcp_f32_e32 v221, v221\n\t"
      "v_rcp_f32_e32 v222, v222\n\t"
      "v_rcp_f32_e32 v223, v223\n\t"
      "v_mul_f32_e32 v216, v216, v220\n\t"
      "v_mul_f32_e32 v217, v217, v221\n\t"
      "v_mul_f32_e32 v218, v218, v222\n\t"
      "v_mul_f32_e32 v219, v219, v223\n\t"
      "v_cvt_pk_bf16_f32 v220, v216, v217\n\t"
      "v_cvt_pk_bf16_f32 v221, v218, v219\n\t"
      "v_min_u32_e32 v220, -2, v220\n\t"
      "v_min_u32_e32 v221, -2, v221\n\t"
      "s_cmp_eq_u32 s17, 0\n\t"
      "s_cbranch_scc0 HI6_%=\n\t"
      "s_mov_b64 exec, 0xf\n\t"
      "global_store_dwordx2 v247, v[232:233], s[12:13] sc1\n\t"
      "s_mov_b64 exec, -1\n\t"
      "HI6_%=:\n\t"
      "global_store_dwordx2 v245, v[220:221], s[8:9] sc1\n\t"
      "GX_%=:\n\t"
      "AEND_%=:\n\t"
      "s_endpgm\n\t"
      ".p2align 6\n\t"
      "GQ_%=:\n\t"
      "s_waitcnt vmcnt(7)\n\t"
      "v_max3_u32 v234, v128, v129, v130\n\t"
      "v_max_u32_e32 v234, v234, v131\n\t"
      "v_cmp_eq_u32_e32 vcc, -1, v234\n\t"
      "s_cbranch_vccnz FQ0R_%=\n\t"
      "FQ0B_%=:\n\t"
      "v_mfma_f32_16x16x32_bf16 v[184:187], v[0:3], v[128:131], v[236:239]\n\t"
      "v_mfma_f32_16x16x32_bf16 v[188:191], v[4:7], v[128:131], 0\n\t"
      "v_mfma_f32_16x16x32_bf16 v[192:195], v[8:11], v[128:131], 0\n\t"
      "v_mfma_f32_16x16x32_bf16 v[196:199], v[12:15], v[128:131], 0\n\t"
      "global_load_dwordx4 v[128:131], v176, s[54:55] sc1\n\t"
      "s_waitcnt vmcnt(7)\n\t"
      "v_max3_u32 v234, v132, v133, v134\n\t"
      "v_max_u32_e32 v234, v234, v135\n\t"
      "v_cmp_eq_u32_e32 vcc, -1, v234\n\t"
      "s_cbranch_vccnz FQ1R_%=\n\t"
      "FQ1B_%=:\n\t"
      "v_mfma_f32_16x16x32_bf16 v[184:187], v[16:19], v[132:135], v[184:187]\n\t"
      "v_mfma_f32_16x16x32_bf16 v[188:191], v[20:23], v[132:135], v[188:191]\n\t"
      "v_mfma_f32_16x16x32_bf16 v[192:195], v[24:27], v[132:135], v[192:195]\n\t"
      "v_mfma_f32_16x16x32_bf16 v[196:199], v[28:31], v[132:135], v[196:199]\n\t"
      "global_load_dwordx4 v[132:135], v177, s[54:55] sc1\n\t"
      "s_waitcnt vmcnt(7)\n\t"
      "v_max3_u32 v234, v136, v137, v138\n\t"
      "v_max_u32_e32 v234, v234, v139\n\t"
      "v_cmp_eq_u32_e32 vcc, -1, v234\n\t"
      "s_cbranch_vccnz FQ2R_%=\n\t"
      "FQ2B_%=:\n\t"
      "v_mfma_f32_16x16x32_bf16 v[184:187], v[32:35], v[136:139], v[184:187]\n\t"
      "v_mfma_f32_16x16x32_bf16 v[188:191], v[36:39], v[136:139], v[188:191]\n\t"
      "v_mfma_f32_16x16x32_bf16 v[192:195], v[40:43], v[136:139], v[192:195]\n\t"
      "v_mfma_f32_16x16x32_bf16 v[196:199], v[44:47], v[136:139], v[196:199]\n\t"
      "global_load_dwordx4 v[136:139], v178, s[54:55] sc1\n\t"
      "s_waitcnt vmcnt(7)\n\t"
      "v_max3_u32 v234, v140, v141, v142\n\t"
      "v_max_u32_e32 v234, v234, v143\n\t"
      "v_cmp_eq_u32_e32 vcc, -1, v234\n\t"
      "s_cbranch_vccnz FQ3R_%=\n\t"
      "FQ3B_%=:\n\t"
      "v_mfma_f32_16x16x32_bf16 v[184:187], v[48:51], v[140:143], v[184:187]\n\t"
      "v_mfma_f32_16x16x32_bf16 v[188:191], v[52:55], v[140:143], v[188:191]\n\t"
      "v_mfma_f32_16x16x32_bf16 v[192:195], v[56:59], v[140:143], v[192:195]\n\t"
      "v_mfma_f32_16x16x32_bf16 v[196:199], v[60:63], v[140:143], v[196:199]\n\t"
      "global_load_dwordx4 v[140:143], v179, s[54:55] sc1\n\t"
      "s_waitcnt vmcnt(7)\n\t"
      "v_max3_u32 v234, v144, v145, v146\n\t"
      "v_max_u32_e32 v234, v234, v147\n\t"
      "v_cmp_eq_u32_e32 vcc, -1, v234\n\t"
      "s_cbranch_vccnz FQ4R_%=\n\t"
      "FQ4B_%=:\n\t"
      "v_mfma_f32_16x16x32_bf16 v[184:187], v[64:67], v[144:147], v[184:187]\n\t"
      "v_mfma_f32_16x16x32_bf16 v[188:191], v[68:71], v[144:147], v[188:191]\n\t"
      "v_mfma_f32_16x16x32_bf16 v[192:195], v[72:75], v[144:147], v[192:195]\n\t"
      "v_mfma_f32_16x16x32_bf16 v[196:199], v[76:79], v[144:147], v[196:199]\n\t"
      "global_load_dwordx4 v[144:147], v180, s[54:55] sc1\n\t"
      "s_waitcnt vmcnt(7)\n\t"
      "v_max3_u32 v234, v148, v149, v150\n\t"
      "v_max_u32_e32 v234, v234, v151\n\t"
      "v_cmp_eq_u32_e32 vcc, -1, v234\n\t"
      "s_cbranch_vccnz FQ5R_%=\n\t"
      "FQ5B_%=:\n\t"
      "v_mfma_f32_16x16x32_bf16 v[184:187], v[80:83], v[148:151], v[184:187]\n\t"
      "v_mfma_f32_16x16x32_bf16 v[188:191], v[84:87], v[148:151], v[188:191]\n\t"
      "v_mfma_f32_16x16x32_bf16 v[192:195], v[88:91], v[148:151], v[192:195]\n\t"
      "v_mfma_f32_16x16x32_bf16 v[196:199], v[92:95], v[148:151], v[196:199]\n\t"
      "global_load_dwordx4 v[148:151], v181, s[54:55] sc1\n\t"
      "s_waitcnt vmcnt(7)\n\t"
      "v_max3_u32 v234, v152, v153, v154\n\t"
      "v_max_u32_e32 v234, v234, v155\n\t"
      "v_cmp_eq_u32_e32 vcc, -1, v234\n\t"
      "s_cbranch_vccnz FQ6R_%=\n\t"
      "FQ6B_%=:\n\t"
      "v_mfma_f32_16x16x32_bf16 v[184:187], v[96:99], v[152:155], v[184:187]\n\t"
      "v_mfma_f32_16x16x32_bf16 v[188:191], v[100:103], v[152:155], v[188:191]\n\t"
      "v_mfma_f32_16x16x32_bf16 v[192:195], v[104:107], v[152:155], v[192:195]\n\t"
      "v_mfma_f32_16x16x32_bf16 v[196:199], v[108:111], v[152:155], v[196:199]\n\t"
      "global_load_dwordx4 v[152:155], v182, s[54:55] sc1\n\t"
      "s_waitcnt vmcnt(7)\n\t"
      "v_max3_u32 v234, v156, v157, v158\n\t"
      "v_max_u32_e32 v234, v234, v159\n\t"
      "v_cmp_eq_u32_e32 vcc, -1, v234\n\t"
      "s_cbranch_vccnz FQ7R_%=\n\t"
      "FQ7B_%=:\n\t"
      "v_mfma_f32_16x16x32_bf16 v[184:187], v[112:115], v[156:159], v[184:187]\n\t"
      "v_mfma_f32_16x16x32_bf16 v[188:191], v[116:119], v[156:159], v[188:191]\n\t"
      "v_mfma_f32_16x16x32_bf16 v[192:195], v[120:123], v[156:159], v[192:195]\n\t"
      "v_mfma_f32_16x16x32_bf16 v[196:199], v[124:127], v[156:159], v[196:199]\n\t"
      "global_load_dwordx4 v[156:159], v183, s[54:55] sc1\n\t"
      "s_nop 3\n\t"
      "ds_write_b128 v243, v[184:187]\n\t"
      "ds_write_b128 v164, v[188:191]\n\t"
      "ds_write_b128 v165, v[192:195]\n\t"
      "ds_write_b128 v166, v[196:199]\n\t"
      "s_waitcnt vmcnt(7)\n\t"
      "v_mfma_f32_16x16x32_bf16 v[200:203], v[0:3], v[128:131], v[236:239]\n\t"
      "v_mfma_f32_16x16x32_bf16 v[204:207], v[4:7], v[128:131], 0\n\t"
      "v_mfma_f32_16x16x32_bf16 v[208:211], v[8:11], v[128:131], 0\n\t"
      "v_mfma_f32_16x16x32_bf16 v[212:215], v[12:15], v[128:131], 0\n\t"
      "global_load_dwordx4 v[128:131], v176, s[56:57] sc1\n\t"
      "s_waitcnt vmcnt(7)\n\t"
      "v_mfma_f32_16x16x32_bf16 v[200:203], v[16:19], v[132:135], v[200:203]\n\t"
      "v_mfma_f32_16x16x32_bf16 v[204:207], v[20:23], v[132:135], v[204:207]\n\t"
      "v_mfma_f32_16x16x32_bf16 v[208:211], v[24:27], v[132:135], v[208:211]\n\t"
      "v_mfma_f32_16x16x32_bf16 v[212:215], v[28:31], v[132:135], v[212:215]\n\t"
      "global_load_dwordx4 v[132:135], v177, s[56:57] sc1\n\t"
      "s_waitcnt vmcnt(7)\n\t"
      "v_mfma_f32_16x16x32_bf16 v[200:203], v[32:35], v[136:139], v[200:203]\n\t"
      "v_mfma_f32_16x16x32_bf16 v[204:207], v[36:39], v[136:139], v[204:207]\n\t"
      "v_mfma_f32_16x16x32_bf16 v[208:211], v[40:43], v[136:139], v[208:211]\n\t"
      "v_mfma_f32_16x16x32_bf16 v[212:215], v[44:47], v[136:139], v[212:215]\n\t"
      "global_load_dwordx4 v[136:139], v178, s[56:57] sc1\n\t"
      "s_waitcnt vmcnt(7)\n\t"
      "v_mfma_f32_16x16x32_bf16 v[200:203], v[48:51], v[140:143], v[200:203]\n\t"
      "v_mfma_f32_16x16x32_bf16 v[204:207], v[52:55], v[140:143], v[204:207]\n\t"
      "v_mfma_f32_16x16x32_bf16 v[208:211], v[56:59], v[140:143], v[208:211]\n\t"
      "v_mfma_f32_16x16x32_bf16 v[212:215], v[60:63], v[140:143], v[212:215]\n\t"
      "global_load_dwordx4 v[140:143], v179, s[56:57] sc1\n\t"
      "s_waitcnt vmcnt(7)\n\t"
      "v_mfma_f32_16x16x32_bf16 v[200:203], v[64:67], v[144:147], v[200:203]\n\t"
      "v_mfma_f32_16x16x32_bf16 v[204:207], v[68:71], v[144:147], v[204:207]\n\t"
      "v_mfma_f32_16x16x32_bf16 v[208:211], v[72:75], v[144:147], v[208:211]\n\t"
      "v_mfma_f32_16x16x32_bf16 v[212:215], v[76:79], v[144:147], v[212:215]\n\t"
      "global_load_dwordx4 v[144:147], v180, s[56:57] sc1\n\t"
      "s_waitcnt vmcnt(7)\n\t"
      "v_mfma_f32_16x16x32_bf16 v[200:203], v[80:83], v[148:151], v[200:203]\n\t"
      "v_mfma_f32_16x16x32_bf16 v[204:207], v[84:87], v[148:151], v[204:207]\n\t"
      "v_mfma_f32_16x16x32_bf16 v[208:211], v[88:91], v[148:151], v[208:211]\n\t"
      "v_mfma_f32_16x16x32_bf16 v[212:215], v[92:95], v[148:151], v[212:215]\n\t"
      "global_load_dwordx4 v[148:151], v181, s[56:57] sc1\n\t"
      "s_waitcnt lgkmcnt(0)\n\t"
      "s_barrier\n\t"
      "s_setprio 2\n\t"
      "s_waitcnt vmcnt(7)\n\t"
      "v_mfma_f32_16x16x32_bf16 v[200:203], v[96:99], v[152:155], v[200:203]\n\t"
      "v_mfma_f32_16x16x32_bf16 v[204:207], v[100:103], v[152:155], v[204:207]\n\t"
      "v_mfma_f32_16x16x32_bf16 v[208:211], v[104:107], v[152:155], v[208:211]\n\t"
      "v_mfma_f32_16x16x32_bf16 v[212:215], v[108:111], v[152:155], v[212:215]\n\t"
      "global_load_dwordx4 v[152:155], v182, s[56:57] sc1\n\t"
      "s_waitcnt vmcnt(7)\n\t"
      "v_mfma_f32_16x16x32_bf16 v[200:203], v[112:115], v[156:159], v[200:203]\n\t"
      "v_mfma_f32_16x16x32_bf16 v[204:207], v[116:119], v[156:159], v[204:207]\n\t"
      "v_mfma_f32_16x16x32_bf16 v[208:211], v[120:123], v[156:159], v[208:211]\n\t"
      "v_mfma_f32_16x16x32_bf16 v[212:215], v[124:127], v[156:159], v[212:215]\n\t"
      "global_load_dwordx4 v[156:159], v183, s[56:57] sc1\n\t"
      "s_setprio 0\n\t"
      "s_nop 2\n\t"
      "v_cmp_u_f32_e32 vcc, v200, v200\n\t"
      "s_cbranch_vccnz NQ1R_%=\n\t"
      "NQ1B_%=:\n\t"
      "ds_write_b128 v243, v[200:203] offset:32768\n\t"
      "ds_write_b128 v164, v[204:207] offset:32768\n\t"
      "ds_write_b128 v165, v[208:211] offset:32768\n\t"
      "ds_write_b128 v166, v[212:215] offset:32768\n\t"
      "s_waitcnt vmcnt(7)\n\t"
      "v_mfma_f32_16x16x32_bf16 v[184:187], v[0:3], v[128:131], v[236:239]\n\t"
      "v_mfma_f32_16x16x32_bf16 v[188:191], v[4:7], v[128:131], 0\n\t"
      "v_mfma_f32_16x16x32_bf16 v[192:195], v[8:11], v[128:131], 0\n\t"
      "v_mfma_f32_16x16x32_bf16 v[196:199], v[12:15], v[128:131], 0\n\t"
      "global_load_dwordx4 v[128:131], v176, s[58:59] sc1\n\t"
      "s_waitcnt vmcnt(7)\n\t"
      "v_mfma_f32_16x16x32_bf16 v[184:187], v[16:19], v[132:135], v[184:187]\n\t"
      "v_mfma_f32_16x16x32_bf16 v[188:191], v[20:23], v[132:135], v[188:191]\n\t"
      "v_mfma_f32_16x16x32_bf16 v[192:195], v[24:27], v[132:135], v[192:195]\n\t"
      "v_mfma_f32_16x16x32_bf16 v[196:199], v[28:31], v[132:135], v[196:199]\n\t"
      "global_load_dwordx4 v[132:135], v177, s[58:59] sc1\n\t"
      "s_waitcnt vmcnt(7)\n\t"
      "v_mfma_f32_16x16x32_bf16 v[184:187], v[32:35], v[136:139], v[184:187]\n\t"
      "v_mfma_f32_16x16x32_bf16 v[188:191], v[36:39], v[136:139], v[188:191]\n\t"
      "v_mfma_f32_16x16x32_bf16 v[192:195], v[40:43], v[136:139], v[192:195]\n\t"
      "v_mfma_f32_16x16x32_bf16 v[196:199], v[44:47], v[136:139], v[196:199]\n\t"
      "global_load_dwordx4 v[136:139], v178, s[58:59] sc1\n\t"
      "s_waitcnt vmcnt(7)\n\t"
      "v_mfma_f32_16x16x32_bf16 v[184:187], v[48:51], v[140:143], v[184:187]\n\t"
      "v_mfma_f32_16x16x32_bf16 v[188:191], v[52:55], v[140:143], v[188:191]\n\t"
      "v_mfma_f32_16x16x32_bf16 v[192:195], v[56:59], v[140:143], v[192:195]\n\t"
      "v_mfma_f32_16x16x32_bf16 v[196:199], v[60:63], v[140:143], v[196:199]\n\t"
      "global_load_dwordx4 v[140:143], v179, s[58:59] sc1\n\t"
      "s_waitcnt vmcnt(7)\n\t"
      "v_mfma_f32_16x16x32_bf16 v[184:187], v[64:67], v[144:147], v[184:187]\n\t"
      "v_mfma_f32_16x16x32_bf16 v[188:191], v[68:71], v[144:147], v[188:191]\n\t"
      "v_mfma_f32_16x16x32_bf16 v[192:195], v[72:75], v[144:147], v[192:195]\n\t"
      "v_mfma_f32_16x16x32_bf16 v[196:199], v[76:79], v[144:147], v[196:199]\n\t"
      "global_load_dwordx4 v[144:147], v180, s[58:59] sc1\n\t"
      "s_waitcnt vmcnt(7)\n\t"
      "v_mfma_f32_16x16x32_bf16 v[184:187], v[80:83], v[148:151], v[184:187]\n\t"
      "v_mfma_f32_16x16x32_bf16 v[188:191], v[84:87], v[148:151], v[188:191]\n\t"
      "v_mfma_f32_16x16x32_bf16 v[192:195], v[88:91], v[148:151], v[192:195]\n\t"
      "v_mfma_f32_16x16x32_bf16 v[196:199], v[92:95], v[148:151], v[196:199]\n\t"
      "global_load_dwordx4 v[148:151], v181, s[58:59] sc1\n\t"
      "s_waitcnt lgkmcnt(0)\n\t"
      "s_barrier\n\t"
      "s_setprio 2\n\t"
      "s_waitcnt vmcnt(7)\n\t"
      "v_mfma_f32_16x16x32_bf16 v[184:187], v[96:99], v[152:155], v[184:187]\n\t"
      "v_mfma_f32_16x16x32_bf16 v[188:191], v[100:103], v[152:155], v[188:191]\n\t"
      "v_mfma_f32_16x16x32_bf16 v[192:195], v[104:107], v[152:155], v[192:195]\n\t"
      "v_mfma_f32_16x16x32_bf16 v[196:199], v[108:111], v[152:155], v[196:199]\n\t"
      "global_load_dwordx4 v[152:155], v182, s[58:59] sc1\n\t"
      "s_waitcnt vmcnt(7)\n\t"
      "v_mfma_f32_16x16x32_bf16 v[184:187], v[112:115], v[156:159], v[184:187]\n\t"
      "v_mfma_f32_16x16x32_bf16 v[188:191], v[116:119], v[156:159], v[188:191]\n\t"
      "v_mfma_f32_16x16x32_bf16 v[192:195], v[120:123], v[156:159], v[192:195]\n\t"
      "v_mfma_f32_16x16x32_bf16 v[196:199], v[124:127], v[156:159], v[196:199]\n\t"
      "global_load_dwordx4 v[156:159], v183, s[58:59] sc1\n\t"
      "s_setprio 0\n\t"
      "s_nop 2\n\t"
      "v_cmp_u_f32_e32 vcc, v184, v184\n\t"
      "s_cbranch_vccnz NQ2R_%=\n\t"
      "NQ2B_%=:\n\t"
      "ds_write_b128 v243, v[184:187]\n\t"
      "ds_write_b128 v164, v[188:191]\n\t"
      "ds_write_b128 v165, v[192:195]\n\t"
      "ds_write_b128 v166, v[196:199]\n\t"
      "s_waitcnt vmcnt(7)\n\t"
      "v_mfma_f32_16x16x32_bf16 v[200:203], v[0:3], v[128:131], v[236:239]\n\t"
      "v_mfma_f32_16x16x32_bf16 v[204:207], v[4:7], v[128:131], 0\n\t"
      "v_mfma_f32_16x16x32_bf16 v[208:211], v[8:11], v[128:131], 0\n\t"
      "v_mfma_f32_16x16x32_bf16 v[212:215], v[12:15], v[128:131], 0\n\t"
      "global_load_dwordx4 v[128:131], v176, s[60:61] sc1\n\t"
      "s_waitcnt vmcnt(7)\n\t"
      "v_mfma_f32_16x16x32_bf16 v[200:203], v[16:19], v[132:135], v[200:203]\n\t"
      "v_mfma_f32_16x16x32_bf16 v[204:207], v[20:23], v[132:135], v[204:207]\n\t"
      "v_mfma_f32_16x16x32_bf16 v[208:211], v[24:27], v[132:135], v[208:211]\n\t"
      "v_mfma_f32_16x16x32_bf16 v[212:215], v[28:31], v[132:135], v[212:215]\n\t"
      "global_load_dwordx4 v[132:135], v177, s[60:61] sc1\n\t"
      "s_waitcnt vmcnt(7)\n\t"
      "v_mfma_f32_16x16x32_bf16 v[200:203], v[32:35], v[136:139], v[200:203]\n\t"
      "v_mfma_f32_16x16x32_bf16 v[204:207], v[36:39], v[136:139], v[204:207]\n\t"
      "v_mfma_f32_16x16x32_bf16 v[208:211], v[40:43], v[136:139], v[208:211]\n\t"
      "v_mfma_f32_16x16x32_bf16 v[212:215], v[44:47], v[136:139], v[212:215]\n\t"
      "global_load_dwordx4 v[136:139], v178, s[60:61] sc1\n\t"
      "s_waitcnt vmcnt(7)\n\t"
      "v_mfma_f32_16x16x32_bf16 v[200:203], v[48:51], v[140:143], v[200:203]\n\t"
      "v_mfma_f32_16x16x32_bf16 v[204:207], v[52:55], v[140:143], v[204:207]\n\t"
      "v_mfma_f32_16x16x32_bf16 v[208:211], v[56:59], v[140:143], v[208:211]\n\t"
      "v_mfma_f32_16x16x32_bf16 v[212:215], v[60:63], v[140:143], v[212:215]\n\t"
      "global_load_dwordx4 v[140:143], v179, s[60:61] sc1\n\t"
      "s_waitcnt vmcnt(7)\n\t"
      "v_mfma_f32_16x16x32_bf16 v[200:203], v[64:67], v[144:147], v[200:203]\n\t"
      "v_mfma_f32_16x16x32_bf16 v[204:207], v[68:71], v[144:147], v[204:207]\n\t"
      "v_mfma_f32_16x16x32_bf16 v[208:211], v[72:75], v[144:147], v[208:211]\n\t"
      "v_mfma_f32_16x16x32_bf16 v[212:215], v[76:79], v[144:147], v[212:215]\n\t"
      "global_load_dwordx4 v[144:147], v180, s[60:61] sc1\n\t"
      "s_waitcnt vmcnt(7)\n\t"
      "v_mfma_f32_16x16x32_bf16 v[200:203], v[80:83], v[148:151], v[200:203]\n\t"
      "v_mfma_f32_16x16x32_bf16 v[204:207], v[84:87], v[148:151], v[204:207]\n\t"
      "v_mfma_f32_16x16x32_bf16 v[208:211], v[88:91], v[148:151], v[208:211]\n\t"
      "v_mfma_f32_16x16x32_bf16 v[212:215], v[92:95], v[148:151], v[212:215]\n\t"
      "global_load_dwordx4 v[148:151], v181, s[60:61] sc1\n\t"
      "s_waitcnt lgkmcnt(0)\n\t"
      "s_barrier\n\t"
      "s_setprio 2\n\t"
      "s_waitcnt vmcnt(7)\n\t"
      "v_mfma_f32_16x16x32_bf16 v[200:203], v[96:99], v[152:155], v[200:203]\n\t"
      "v_mfma_f32_16x16x32_bf16 v[204:207], v[100:103], v[152:155], v[204:207]\n\t"
      "v_mfma_f32_16x16x32_bf16 v[208:211], v[104:107], v[152:155], v[208:211]\n\t"
      "v_mfma_f32_16x16x32_bf16 v[212:215], v[108:111], v[152:155], v[212:215]\n\t"
      "global_load_dwordx4 v[152:155], v182, s[60:61] sc1\n\t"
      "s_waitcnt vmcnt(7)\n\t"
      "v_mfma_f32_16x16x32_bf16 v[200:203], v[112:115], v[156:159], v[200:203]\n\t"
      "v_mfma_f32_16x16x32_bf16 v[204:207], v[116:119], v[156:159], v[204:207]\n\t"
      "v_mfma_f32_16x16x32_bf16 v[208:211], v[120:123], v[156:159], v[208:211]\n\t"
      "v_mfma_f32_16x16x32_bf16 v[212:215], v[124:127], v[156:159], v[212:215]\n\t"
      "global_load_dwordx4 v[156:159], v183, s[60:61] sc1\n\t"
      "s_setprio 0\n\t"
      "s_nop 2\n\t"
      "v_cmp_u_f32_e32 vcc, v200, v200\n\t"
      "s_cbranch_vccnz NQ3R_%=\n\t"
      "NQ3B_%=:\n\t"
      "ds_write_b128 v243, v[200:203] offset:32768\n\t"
      "ds_write_b128 v164, v[204:207] offset:32768\n\t"
      "ds_write_b128 v165, v[208:211] offset:32768\n\t"
      "ds_write_b128 v166, v[212:215] offset:32768\n\t"
      "s_waitcnt vmcnt(7)\n\t"
      "v_mfma_f32_16x16x32_bf16 v[184:187], v[0:3], v[128:131], v[236:239]\n\t"
      "v_mfma_f32_16x16x32_bf16 v[188:191], v[4:7], v[128:131], 0\n\t"
      "v_mfma_f32_16x16x32_bf16 v[192:195], v[8:11], v[128:131], 0\n\t"
      "v_mfma_f32_16x16x32_bf16 v[196:199], v[12:15], v[128:131], 0\n\t"
      "global_load_dwordx4 v[128:131], v176, s[62:63] sc1\n\t"
      "s_waitcnt vmcnt(7)\n\t"
      "v_mfma_f32_16x16x32_bf16 v[184:187], v[16:19], v[132:135], v[184:187]\n\t"
      "v_mfma_f32_16x16x32_bf16 v[188:191], v[20:23], v[132:135], v[188:191]\n\t"
      "v_mfma_f32_16x16x32_bf16 v[192:195], v[24:27], v[132:135], v[192:195]\n\t"
      "v_mfma_f32_16x16x32_bf16 v[196:199], v[28:31], v[132:135], v[196:199]\n\t"
      "global_load_dwordx4 v[132:135], v177, s[62:63] sc1\n\t"
      "s_waitcnt vmcnt(7)\n\t"
      "v_mfma_f32_16x16x32_bf16 v[184:187], v[32:35], v[136:139], v[184:187]\n\t"
      "v_mfma_f32_16x16x32_bf16 v[188:191], v[36:39], v[136:139], v[188:191]\n\t"
      "v_mfma_f32_16x16x32_bf16 v[192:195], v[40:43], v[136:139], v[192:195]\n\t"
      "v_mfma_f32_16x16x32_bf16 v[196:199], v[44:47], v[136:139], v[196:199]\n\t"
      "global_load_dwordx4 v[136:139], v178, s[62:63] sc1\n\t"
      "s_waitcnt vmcnt(7)\n\t"
      "v_mfma_f32_16x16x32_bf16 v[184:187], v[48:51], v[140:143], v[184:187]\n\t"
      "v_mfma_f32_16x16x32_bf16 v[188:191], v[52:55], v[140:143], v[188:191]\n\t"
      "v_mfma_f32_16x16x32_bf16 v[192:195], v[56:59], v[140:143], v[192:195]\n\t"
      "v_mfma_f32_16x16x32_bf16 v[196:199], v[60:63], v[140:143], v[196:199]\n\t"
      "global_load_dwordx4 v[140:143], v179, s[62:63] sc1\n\t"
      "s_waitcnt vmcnt(7)\n\t"
      "v_mfma_f32_16x16x32_bf16 v[184:187], v[64:67], v[144:147], v[184:187]\n\t"
      "v_mfma_f32_16x16x32_bf16 v[188:191], v[68:71], v[144:147], v[188:191]\n\t"
      "v_mfma_f32_16x16x32_bf16 v[192:195], v[72:75], v[144:147], v[192:195]\n\t"
      "v_mfma_f32_16x16x32_bf16 v[196:199], v[76:79], v[144:147], v[196:199]\n\t"
      "global_load_dwordx4 v[144:147], v180, s[62:63] sc1\n\t"
      "s_waitcnt vmcnt(7)\n\t"
      "v_mfma_f32_16x16x32_bf16 v[184:187], v[80:83], v[148:151], v[184:187]\n\t"
      "v_mfma_f32_16x16x32_bf16 v[188:191], v[84:87], v[148:151], v[188:191]\n\t"
      "v_mfma_f32_16x16x32_bf16 v[192:195], v[88:91], v[148:151], v[192:195]\n\t"
      "v_mfma_f32_16x16x32_bf16 v[196:199], v[92:95], v[148:151], v[196:199]\n\t"
      "global_load_dwordx4 v[148:151], v181, s[62:63] sc1\n\t"
      "s_waitcnt lgkmcnt(0)\n\t"
      "s_barrier\n\t"
      "s_setprio 2\n\t"
      "s_waitcnt vmcnt(7)\n\t"
      "v_mfma_f32_16x16x32_bf16 v[184:187], v[96:99], v[152:155], v[184:187]\n\t"
      "v_mfma_f32_16x16x32_bf16 v[188:191], v[100:103], v[152:155], v[188:191]\n\t"
      "v_mfma_f32_16x16x32_bf16 v[192:195], v[104:107], v[152:155], v[192:195]\n\t"
      "v_mfma_f32_16x16x32_bf16 v[196:199], v[108:111], v[152:155], v[196:199]\n\t"
      "global_load_dwordx4 v[152:155], v182, s[62:63] sc1\n\t"
      "s_waitcnt vmcnt(7)\n\t"
      "v_mfma_f32_16x16x32_bf16 v[184:187], v[112:115], v[156:159], v[184:187]\n\t"
      "v_mfma_f32_16x16x32_bf16 v[188:191], v[116:119], v[156:159], v[188:191]\n\t"
      "v_mfma_f32_16x16x32_bf16 v[192:195], v[120:123], v[156:159], v[192:195]\n\t"
      "v_mfma_f32_16x16x32_bf16 v[196:199], v[124:127], v[156:159], v[196:199]\n\t"
      "global_load_dwordx4 v[156:159], v183, s[62:63] sc1\n\t"
      "s_setprio 0\n\t"
      "s_nop 2\n\t"
      "v_cmp_u_f32_e32 vcc, v184, v184\n\t"
      "s_cbranch_vccnz NQ4R_%=\n\t"
      "NQ4B_%=:\n\t"
      "ds_write_b128 v243, v[184:187]\n\t"
      "ds_write_b128 v164, v[188:191]\n\t"
      "ds_write_b128 v165, v[192:195]\n\t"
      "ds_write_b128 v166, v[196:199]\n\t"
      "s_waitcnt vmcnt(7)\n\t"
      "v_mfma_f32_16x16x32_bf16 v[200:203], v[0:3], v[128:131], v[236:239]\n\t"
      "v_mfma_f32_16x16x32_bf16 v[204:207], v[4:7], v[128:131], 0\n\t"
      "v_mfma_f32_16x16x32_bf16 v[208:211], v[8:11], v[128:131], 0\n\t"
      "v_mfma_f32_16x16x32_bf16 v[212:215], v[12:15], v[128:131], 0\n\t"
      "global_load_dwordx4 v[128:131], v176, s[64:65] sc1\n\t"
      "s_waitcnt vmcnt(7)\n\t"
      "v_mfma_f32_16x16x32_bf16 v[200:203], v[16:19], v[132:135], v[200:203]\n\t"
      "v_mfma_f32_16x16x32_bf16 v[204:207], v[20:23], v[132:135], v[204:207]\n\t"
      "v_mfma_f32_16x16x32_bf16 v[208:211], v[24:27], v[132:135], v[208:211]\n\t"
      "v_mfma_f32_16x16x32_bf16 v[212:215], v[28:31], v[132:135], v[212:215]\n\t"
      "global_load_dwordx4 v[132:135], v177, s[64:65] sc1\n\t"
      "s_waitcnt vmcnt(7)\n\t"
      "v_mfma_f32_16x16x32_bf16 v[200:203], v[32:35], v[136:139], v[200:203]\n\t"
      "v_mfma_f32_16x16x32_bf16 v[204:207], v[36:39], v[136:139], v[204:207]\n\t"
      "v_mfma_f32_16x16x32_bf16 v[208:211], v[40:43], v[136:139], v[208:211]\n\t"
      "v_mfma_f32_16x16x32_bf16 v[212:215], v[44:47], v[136:139], v[212:215]\n\t"
      "global_load_dwordx4 v[136:139], v178, s[64:65] sc1\n\t"
      "s_waitcnt vmcnt(7)\n\t"
      "v_mfma_f32_16x16x32_bf16 v[200:203], v[48:51], v[140:143], v[200:203]\n\t"
      "v_mfma_f32_16x16x32_bf16 v[204:207], v[52:55], v[140:143], v[204:207]\n\t"
      "v_mfma_f32_16x16x32_bf16 v[208:211], v[56:59], v[140:143], v[208:211]\n\t"
      "v_mfma_f32_16x16x32_bf16 v[212:215], v[60:63], v[140:143], v[212:215]\n\t"
      "global_load_dwordx4 v[140:143], v179, s[64:65] sc1\n\t"
      "s_waitcnt vmcnt(7)\n\t"
      "v_mfma_f32_16x16x32_bf16 v[200:203], v[64:67], v[144:147], v[200:203]\n\t"
      "v_mfma_f32_16x16x32_bf16 v[204:207], v[68:71], v[144:147], v[204:207]\n\t"
      "v_mfma_f32_16x16x32_bf16 v[208:211], v[72:75], v[144:147], v[208:211]\n\t"
      "v_mfma_f32_16x16x32_bf16 v[212:215], v[76:79], v[144:147], v[212:215]\n\t"
      "global_load_dwordx4 v[144:147], v180, s[64:65] sc1\n\t"
      "s_waitcnt vmcnt(7)\n\t"
      "v_mfma_f32_16x16x32_bf16 v[200:203], v[80:83], v[148:151], v[200:203]\n\t"
      "v_mfma_f32_16x16x32_bf16 v[204:207], v[84:87], v[148:151], v[204:207]\n\t"
      "v_mfma_f32_16x16x32_bf16 v[208:211], v[88:91], v[148:151], v[208:211]\n\t"
      "v_mfma_f32_16x16x32_bf16 v[212:215], v[92:95], v[148:151], v[212:215]\n\t"
      "global_load_dwordx4 v[148:151], v181, s[64:65] sc1\n\t"
      "s_waitcnt lgkmcnt(0)\n\t"
      "s_barrier\n\t"
      "s_setprio 2\n\t"
      "s_waitcnt vmcnt(7)\n\t"
      "v_mfma_f32_16x16x32_bf16 v[200:203], v[96:99], v[152:155], v[200:203]\n\t"
      "v_mfma_f32_16x16x32_bf16 v[204:207], v[100:103], v[152:155], v[204:207]\n\t"
      "v_mfma_f32_16x16x32_bf16 v[208:211], v[104:107], v[152:155], v[208:211]\n\t"
      "v_mfma_f32_16x16x32_bf16 v[212:215], v[108:111], v[152:155], v[212:215]\n\t"
      "global_load_dwordx4 v[152:155], v182, s[64:65] sc1\n\t"
      "s_waitcnt vmcnt(7)\n\t"
      "v_mfma_f32_16x16x32_bf16 v[200:203], v[112:115], v[156:159], v[200:203]\n\t"
      "v_mfma_f32_16x16x32_bf16 v[204:207], v[116:119], v[156:159], v[204:207]\n\t"
      "v_mfma_f32_16x16x32_bf16 v[208:211], v[120:123], v[156:159], v[208:211]\n\t"
      "v_mfma_f32_16x16x32_bf16 v[212:215], v[124:127], v[156:159], v[212:215]\n\t"
      "global_load_dwordx4 v[156:159], v183, s[64:65] sc1\n\t"
      "s_setprio 0\n\t"
      "s_nop 2\n\t"
      "v_cmp_u_f32_e32 vcc, v200, v200\n\t"
      "s_cbranch_vccnz NQ5R_%=\n\t"
      "NQ5B_%=:\n\t"
      "ds_write_b128 v243, v[200:203] offset:32768\n\t"
      "ds_write_b128 v164, v[204:207] offset:32768\n\t"
      "ds_write_b128 v165, v[208:211] offset:32768\n\t"
      "ds_write_b128 v166, v[212:215] offset:32768\n\t"
      "s_waitcnt vmcnt(7)\n\t"
      "v_mfma_f32_16x16x32_bf16 v[184:187], v[0:3], v[128:131], v[236:239]\n\t"
      "v_mfma_f32_16x16x32_bf16 v[188:191], v[4:7], v[128:131], 0\n\t"
      "v_mfma_f32_16x16x32_bf16 v[192:195], v[8:11], v[128:131], 0\n\t"
      "v_mfma_f32_16x16x32_bf16 v[196:199], v[12:15], v[128:131], 0\n\t"
      "s_waitcnt vmcnt(6)\n\t"
      "v_mfma_f32_16x16x32_bf16 v[184:187], v[16:19], v[132:135], v[184:187]\n\t"
      "v_mfma_f32_16x16x32_bf16 v[188:191], v[20:23], v[132:135], v[188:191]\n\t"
      "v_mfma_f32_16x16x32_bf16 v[192:195], v[24:27], v[132:135], v[192:195]\n\t"
      "v_mfma_f32_16x16x32_bf16 v[196:199], v[28:31], v[132:135], v[196:199]\n\t"
      "s_waitcnt vmcnt(5)\n\t"
      "v_mfma_f32_16x16x32_bf16 v[184:187], v[32:35], v[136:139], v[184:187]\n\t"
      "v_mfma_f32_16x16x32_bf16 v[188:191], v[36:39], v[136:139], v[188:191]\n\t"
      "v_mfma_f32_16x16x32_bf16 v[192:195], v[40:43], v[136:139], v[192:195]\n\t"
      "v_mfma_f32_16x16x32_bf16 v[196:199], v[44:47], v[136:139], v[196:199]\n\t"
      "s_waitcnt vmcnt(4)\n\t"
      "v_mfma_f32_16x16x32_bf16 v[184:187], v[48:51], v[140:143], v[184:187]\n\t"
      "v_mfma_f32_16x16x32_bf16 v[188:191], v[52:55], v[140:143], v[188:191]\n\t"
      "v_mfma_f32_16x16x32_bf16 v[192:195], v[56:59], v[140:143], v[192:195]\n\t"
      "v_mfma_f32_16x16x32_bf16 v[196:199], v[60:63], v[140:143], v[196:199]\n\t"
      "s_waitcnt vmcnt(3)\n\t"
      "v_mfma_f32_16x16x32_bf16 v[184:187], v[64:67], v[144:147], v[184:187]\n\t"
      "v_mfma_f32_16x16x32_bf16 v[188:191], v[68:71], v[144:147], v[188:191]\n\t"
      "v_mfma_f32_16x16x32_bf16 v[192:195], v[72:75], v[144:147], v[192:195]\n\t"
      "v_mfma_f32_16x16x32_bf16 v[196:199], v[76:79], v[144:147], v[196:199]\n\t"
      "s_waitcnt vmcnt(2)\n\t"
      "v_mfma_f32_16x16x32_bf16 v[184:187], v[80:83], v[148:151], v[184:187]\n\t"
      "v_mfma_f32_16x16x32_bf16 v[188:191], v[84:87], v[148:151], v[188:191]\n\t"
      "v_mfma_f32_16x16x32_bf16 v[192:195], v[88:91], v[148:151], v[192:195]\n\t"
      "v_mfma_f32_16x16x32_bf16 v[196:199], v[92:95], v[148:151], v[196:199]\n\t"
      "s_waitcnt lgkmcnt(0)\n\t"
      "s_barrier\n\t"
      "s_setprio 2\n\t"
      "s_waitcnt vmcnt(1)\n\t"
      "v_mfma_f32_16x16x32_bf16 v[184:187], v[96:99], v[152:155], v[184:187]\n\t"
      "v_mfma_f32_16x16x32_bf16 v[188:191], v[100:103], v[152:155], v[188:191]\n\t"
      "v_mfma_f32_16x16x32_bf16 v[192:195], v[104:107], v[152:155], v[192:195]\n\t"
      "v_mfma_f32_16x16x32_bf16 v[196:199], v[108:111], v[152:155], v[196:199]\n\t"
      "s_waitcnt vmcnt(0)\n\t"
      "v_mfma_f32_16x16x32_bf16 v[184:187], v[112:115], v[156:159], v[184:187]\n\t"
      "v_mfma_f32_16x16x32_bf16 v[188:191], v[116:119], v[156:159], v[188:191]\n\t"
      "v_mfma_f32_16x16x32_bf16 v[192:195], v[120:123], v[156:159], v[192:195]\n\t"
      "v_mfma_f32_16x16x32_bf16 v[196:199], v[124:127], v[156:159], v[196:199]\n\t"
      "s_nop 4\n\t"
      "v_cmp_u_f32_e32 vcc, v184, v184\n\t"
      "s_cbranch_vccnz NQ6R_%=\n\t"
      "NQ6B_%=:\n\t"
      "ds_write_b128 v243, v[184:187]\n\t"
      "ds_write_b128 v164, v[188:191]\n\t"
      "ds_write_b128 v165, v[192:195]\n\t"
      "ds_write_b128 v166, v[196:199]\n\t"
      "s_waitcnt lgkmcnt(0)\n\t"
      "s_barrier\n\t"
      "s_branch GX_%=\n\t"
      "ASP_%=:\n\t"
      "s_mov_b32 m0, 163328\n\t"
      "s_cmp_ge_u32 s17, 65536\n\t"
      "s_cbranch_scc0 BOT_%=\n\t"
      "s_barrier\n\t"
      "BOT_%=:\n\t"
      "global_load_lds_dword v241, s[4:5]\n\t"
      "s_add_u32 s4, s4, 229376\n\t"
      "s_addc_u32 s5, s5, 0\n\t"
      "global_load_lds_dword v241, s[4:5]\n\t"
      "s_add_u32 s4, s4, 229376\n\t"
      "s_addc_u32 s5, s5, 0\n\t"
      "s_branch AEND_%=\n\t"
      "FQ0R_%=:\n\t"
      "s_mov_b32 s51, 0\n\t"
      "FQ0L_%=:\n\t"
      "s_add_u32 s51, s51, 1\n\t"
      "s_cmp_lt_u32 s51, 262144\n\t"
      "s_cbranch_scc0 FQ0X_%=\n\t"
      "s_waitcnt vmcnt(0)\n\t"
      "s_sleep 1\n\t"
      "global_load_dwordx4 v[128:131], v176, s[52:53] sc1\n\t"
      "global_load_dwordx4 v[132:135], v177, s[52:53] sc1\n\t"
      "global_load_dwordx4 v[136:139], v178, s[52:53] sc1\n\t"
      "global_load_dwordx4 v[140:143], v179, s[52:53] sc1\n\t"
      "global_load_dwordx4 v[144:147], v180, s[52:53] sc1\n\t"
      "global_load_dwordx4 v[148:151], v181, s[52:53] sc1\n\t"
      "global_load_dwordx4 v[152:155], v182, s[52:53] sc1\n\t"
      "global_load_dwordx4 v[156:159], v183, s[52:53] sc1\n\t"
      "s_waitcnt vmcnt(7)\n\t"
      "v_max3_u32 v234, v128, v129, v130\n\t"
      "v_max_u32_e32 v234, v234, v131\n\t"
      "v_cmp_eq_u32_e32 vcc, -1, v234\n\t"
      "s_cbranch_vccnz FQ0L_%=\n\t"
      "FQ0X_%=:\n\t"
      "s_branch FQ0B_%=\n\t"
      "FQ1R_%=:\n\t"
      "s_mov_b32 s51, 0\n\t"
      "FQ1L_%=:\n\t"
      "s_add_u32 s51, s51, 1\n\t"
      "s_cmp_lt_u32 s51, 262144\n\t"
      "s_cbranch_scc0 FQ1X_%=\n\t"
      "s_waitcnt vmcnt(0)\n\t"
      "s_sleep 1\n\t"
      "global_load_dwordx4 v[132:135], v177, s[52:53] sc1\n\t"
      "global_load_dwordx4 v[136:139], v178, s[52:53] sc1\n\t"
      "global_load_dwordx4 v[140:143], v179, s[52:53] sc1\n\t"
      "global_load_dwordx4 v[144:147], v180, s[52:53] sc1\n\t"
      "global_load_dwordx4 v[148:151], v181, s[52:53] sc1\n\t"
      "global_load_dwordx4 v[152:155], v182, s[52:53] sc1\n\t"
      "global_load_dwordx4 v[156:159], v183, s[52:53] sc1\n\t"
      "global_load_dwordx4 v[128:131], v176, s[54:55] sc1\n\t"
      "s_waitcnt vmcnt(7)\n\t"
      "v_max3_u32 v234, v132, v133, v134\n\t"
      "v_max_u32_e32 v234, v234, v135\n\t"
      "v_cmp_eq_u32_e32 vcc, -1, v234\n\t"
      "s_cbranch_vccnz FQ1L_%=\n\t"
      "FQ1X_%=:\n\t"
      "s_branch FQ1B_%=\n\t"
      "FQ2R_%=:\n\t"
      "s_mov_b32 s51, 0\n\t"
      "FQ2L_%=:\n\t"
      "s_add_u32 s51, s51, 1\n\t"
      "s_cmp_lt_u32 s51, 262144\n\t"
      "s_cbranch_scc0 FQ2X_%=\n\t"
      "s_waitcnt vmcnt(0)\n\t"
      "s_sleep 1\n\t"
      "global_load_dwordx4 v[136:139], v178, s[52:53] sc1\n\t"
      "global_load_dwordx4 v[140:143], v179, s[52:53] sc1\n\t"
      "global_load_dwordx4 v[144:147], v180, s[52:53] sc1\n\t"
      "global_load_dwordx4 v[148:151], v181, s[52:53] sc1\n\t"
      "global_load_dwordx4 v[152:155], v182, s[52:53] sc1\n\t"
      "global_load_dwordx4 v[156:159], v183, s[52:53] sc1\n\t"
      "global_load_dwordx4 v[128:131], v176, s[54:55] sc1\n\t"
      "global_load_dwordx4 v[132:135], v177, s[54:55] sc1\n\t"
      "s_waitcnt vmcnt(7)\n\t"
      "v_max3_u32 v234, v136, v137, v138\n\t"
      "v_max_u32_e32 v234, v234, v139\n\t"
      "v_cmp_eq_u32_e32 vcc, -1, v234\n\t"
      "s_cbranch_vccnz FQ2L_%=\n\t"
      "FQ2X_%=:\n\t"
      "s_branch FQ2B_%=\n\t"
      "FQ3R_%=:\n\t"
      "s_mov_b32 s51, 0\n\t"
      "FQ3L_%=:\n\t"
      "s_add_u32 s51, s51, 1\n\t"
      "s_cmp_lt_u32 s51, 262144\n\t"
      "s_cbranch_scc0 FQ3X_%=\n\t"
      "s_waitcnt vmcnt(0)\n\t"
      "s_sleep 1\n\t"
      "global_load_dwordx4 v[140:143], v179, s[52:53] sc1\n\t"
      "global_load_dwordx4 v[144:147], v180, s[52:53] sc1\n\t"
      "global_load_dwordx4 v[148:151], v181, s[52:53] sc1\n\t"
      "global_load_dwordx4 v[152:155], v182, s[52:53] sc1\n\t"
      "global_load_dwordx4 v[156:159], v183, s[52:53] sc1\n\t"
      "global_load_dwordx4 v[128:131], v176, s[54:55] sc1\n\t"
      "global_load_dwordx4 v[132:135], v177, s[54:55] sc1\n\t"
      "global_load_dwordx4 v[136:139], v178, s[54:55] sc1\n\t"
      "s_waitcnt vmcnt(7)\n\t"
      "v_max3_u32 v234, v140, v141, v142\n\t"
      "v_max_u32_e32 v234, v234, v143\n\t"
      "v_cmp_eq_u32_e32 vcc, -1, v234\n\t"
      "s_cbranch_vccnz FQ3L_%=\n\t"
      "FQ3X_%=:\n\t"
      "s_branch FQ3B_%=\n\t"
      "FQ4R_%=:\n\t"
      "s_mov_b32 s51, 0\n\t"
      "FQ4L_%=:\n\t"
      "s_add_u32 s51, s51, 1\n\t"
      "s_cmp_lt_u32 s51, 262144\n\t"
      "s_cbranch_scc0 FQ4X_%=\n\t"
      "s_waitcnt vmcnt(0)\n\t"
      "s_sleep 1\n\t"
      "global_load_dwordx4 v[144:147], v180, s[52:53] sc1\n\t"
      "global_load_dwordx4 v[148:151], v181, s[52:53] sc1\n\t"
      "global_load_dwordx4 v[152:155], v182, s[52:53] sc1\n\t"
      "global_load_dwordx4 v[156:159], v183, s[52:53] sc1\n\t"
      "global_load_dwordx4 v[128:131], v176, s[54:55] sc1\n\t"
      "global_load_dwordx4 v[132:135], v177, s[54:55] sc1\n\t"
      "global_load_dwordx4 v[136:139], v178, s[54:55] sc1\n\t"
      "global_load_dwordx4 v[140:143], v179, s[54:55] sc1\n\t"
      "s_waitcnt vmcnt(7)\n\t"
      "v_max3_u32 v234, v144, v145, v146\n\t"
      "v_max_u32_e32 v234, v234, v147\n\t"
      "v_cmp_eq_u32_e32 vcc, -1, v234\n\t"
      "s_cbranch_vccnz FQ4L_%=\n\t"
      "FQ4X_%=:\n\t"
      "s_branch FQ4B_%=\n\t"
      "FQ5R_%=:\n\t"
      "s_mov_b32 s51, 0\n\t"
      "FQ5L_%=:\n\t"
      "s_add_u32 s51, s51, 1\n\t"
      "s_cmp_lt_u32 s51, 262144\n\t"
      "s_cbranch_scc0 FQ5X_%=\n\t"
      "s_waitcnt vmcnt(0)\n\t"
      "s_sleep 1\n\t"
      "global_load_dwordx4 v[148:151], v181, s[52:53] sc1\n\t"
      "global_load_dwordx4 v[152:155], v182, s[52:53] sc1\n\t"
      "global_load_dwordx4 v[156:159], v183, s[52:53] sc1\n\t"
      "global_load_dwordx4 v[128:131], v176, s[54:55] sc1\n\t"
      "global_load_dwordx4 v[132:135], v177, s[54:55] sc1\n\t"
      "global_load_dwordx4 v[136:139], v178, s[54:55] sc1\n\t"
      "global_load_dwordx4 v[140:143], v179, s[54:55] sc1\n\t"
      "global_load_dwordx4 v[144:147], v180, s[54:55] sc1\n\t"
      "s_waitcnt vmcnt(7)\n\t"
      "v_max3_u32 v234, v148, v149, v150\n\t"
      "v_max_u32_e32 v234, v234, v151\n\t"
      "v_cmp_eq_u32_e32 vcc, -1, v234\n\t"
      "s_cbranch_vccnz FQ5L_%=\n\t"
      "FQ5X_%=:\n\t"
      "s_branch FQ5B_%=\n\t"
      "FQ6R_%=:\n\t"
      "s_mov_b32 s51, 0\n\t"
      "FQ6L_%=:\n\t"
      "s_add_u32 s51, s51, 1\n\t"
      "s_cmp_lt_u32 s51, 262144\n\t"
      "s_cbranch_scc0 FQ6X_%=\n\t"
      "s_waitcnt vmcnt(0)\n\t"
      "s_sleep 1\n\t"
      "global_load_dwordx4 v[152:155], v182, s[52:53] sc1\n\t"
      "global_load_dwordx4 v[156:159], v183, s[52:53] sc1\n\t"
      "global_load_dwordx4 v[128:131], v176, s[54:55] sc1\n\t"
      "global_load_dwordx4 v[132:135], v177, s[54:55] sc1\n\t"
      "global_load_dwordx4 v[136:139], v178, s[54:55] sc1\n\t"
      "global_load_dwordx4 v[140:143], v179, s[54:55] sc1\n\t"
      "global_load_dwordx4 v[144:147], v180, s[54:55] sc1\n\t"
      "global_load_dwordx4 v[148:151], v181, s[54:55] sc1\n\t"
      "s_waitcnt vmcnt(7)\n\t"
      "v_max3_u32 v234, v152, v153, v154\n\t"
      "v_max_u32_e32 v234, v234, v155\n\t"
      "v_cmp_eq_u32_e32 vcc, -1, v234\n\t"
      "s_cbranch_vccnz FQ6L_%=\n\t"
      "FQ6X_%=:\n\t"
      "s_branch FQ6B_%=\n\t"
      "FQ7R_%=:\n\t"
      "s_mov_b32 s51, 0\n\t"
      "FQ7L_%=:\n\t"
      "s_add_u32 s51, s51, 1\n\t"
      "s_cmp_lt_u32 s51, 262144\n\t"
      "s_cbranch_scc0 FQ7X_%=\n\t"
      "s_waitcnt vmcnt(0)\n\t"
      "s_sleep 1\n\t"
      "global_load_dwordx4 v[156:159], v183, s[52:53] sc1\n\t"
      "global_load_dwordx4 v[128:131], v176, s[54:55] sc1\n\t"
      "global_load_dwordx4 v[132:135], v177, s[54:55] sc1\n\t"
      "global_load_dwordx4 v[136:139], v178, s[54:55] sc1\n\t"
      "global_load_dwordx4 v[140:143], v179, s[54:55] sc1\n\t"
      "global_load_dwordx4 v[144:147], v180, s[54:55] sc1\n\t"
      "global_load_dwordx4 v[148:151], v181, s[54:55] sc1\n\t"
      "global_load_dwordx4 v[152:155], v182, s[54:55] sc1\n\t"
      "s_waitcnt vmcnt(7)\n\t"
      "v_max3_u32 v234, v156, v157, v158\n\t"
      "v_max_u32_e32 v234, v234, v159\n\t"
      "v_cmp_eq_u32_e32 vcc, -1, v234\n\t"
      "s_cbranch_vccnz FQ7L_%=\n\t"
      "FQ7X_%=:\n\t"
      "s_branch FQ7B_%=\n\t"
      "NQ1R_%=:\n\t"
      "s_mov_b32 s51, 0\n\t"
      "NQ1L_%=:\n\t"
      "s_add_u32 s51, s51, 1\n\t"
      "s_cmp_lt_u32 s51, 262144\n\t"
      "s_cbranch_scc0 NQ1X_%=\n\t"
      "s_waitcnt vmcnt(0)\n\t"
      "s_sleep 1\n\t"
      "global_load_dwordx4 v[128:131], v176, s[54:55] sc1\n\t"
      "global_load_dwordx4 v[132:135], v177, s[54:55] sc1\n\t"
      "global_load_dwordx4 v[136:139], v178, s[54:55] sc1\n\t"
      "global_load_dwordx4 v[140:143], v179, s[54:55] sc1\n\t"
      "global_load_dwordx4 v[144:147], v180, s[54:55] sc1\n\t"
      "global_load_dwordx4 v[148:151], v181, s[54:55] sc1\n\t"
      "global_load_dwordx4 v[152:155], v182, s[54:55] sc1\n\t"
      "global_load_dwordx4 v[156:159], v183, s[54:55] sc1\n\t"
      "s_waitcnt vmcnt(0)\n\t"
      "v_max3_u32 v234, v128, v129, v130\n\t"
      "v_max3_u32 v234, v234, v131, v132\n\t"
      "v_max3_u32 v234, v234, v133, v134\n\t"
      "v_max3_u32 v234, v234, v135, v136\n\t"
      "v_max3_u32 v234, v234, v137, v138\n\t"
      "v_max3_u32 v234, v234, v139, v140\n\t"
      "v_max3_u32 v234, v234, v141, v142\n\t"
      "v_max3_u32 v234, v234, v143, v144\n\t"
      "v_max3_u32 v234, v234, v145, v146\n\t"
      "v_max3_u32 v234, v234, v147, v148\n\t"
      "v_max3_u32 v234, v234, v149, v150\n\t"
      "v_max3_u32 v234, v234, v151, v152\n\t"
      "v_max3_u32 v234, v234, v153, v154\n\t"
      "v_max3_u32 v234, v234, v155, v156\n\t"
      "v_max3_u32 v234, v234, v157, v158\n\t"
      "v_max_u32_e32 v234, v234, v159\n\t"
      "v_cmp_eq_u32_e32 vcc, -1, v234\n\t"
      "s_cbranch_vccnz NQ1L_%=\n\t"
      "NQ1X_%=:\n\t"
      "v_mfma_f32_16x16x32_bf16 v[200:203], v[0:3], v[128:131], v[236:239]\n\t"
      "v_mfma_f32_16x16x32_bf16 v[204:207], v[4:7], v[128:131], 0\n\t"
      "v_mfma_f32_16x16x32_bf16 v[208:211], v[8:11], v[128:131], 0\n\t"
      "v_mfma_f32_16x16x32_bf16 v[212:215], v[12:15], v[128:131], 0\n\t"
      "v_mfma_f32_16x16x32_bf16 v[200:203], v[16:19], v[132:135], v[200:203]\n\t"
      "v_mfma_f32_16x16x32_bf16 v[204:207], v[20:23], v[132:135], v[204:207]\n\t"
      "v_mfma_f32_16x16x32_bf16 v[208:211], v[24:27], v[132:135], v[208:211]\n\t"
      "v_mfma_f32_16x16x32_bf16 v[212:215], v[28:31], v[132:135], v[212:215]\n\t"
      "v_mfma_f32_16x16x32_bf16 v[200:203], v[32:35], v[136:139], v[200:203]\n\t"
      "v_mfma_f32_16x16x32_bf16 v[204:207], v[36:39], v[136:139], v[204:207]\n\t"
      "v_mfma_f32_16x16x32_bf16 v[208:211], v[40:43], v[136:139], v[208:211]\n\t"
      "v_mfma_f32_16x16x32_bf16 v[212:215], v[44:47], v[136:139], v[212:215]\n\t"
      "v_mfma_f32_16x16x32_bf16 v[200:203], v[48:51], v[140:143], v[200:203]\n\t"
      "v_mfma_f32_16x16x32_bf16 v[204:207], v[52:55], v[140:143], v[204:207]\n\t"
      "v_mfma_f32_16x16x32_bf16 v[208:211], v[56:59], v[140:143], v[208:211]\n\t"
      "v_mfma_f32_16x16x32_bf16 v[212:215], v[60:63], v[140:143], v[212:215]\n\t"
      "v_mfma_f32_16x16x32_bf16 v[200:203], v[64:67], v[144:147], v[200:203]\n\t"
      "v_mfma_f32_16x16x32_bf16 v[204:207], v[68:71], v[144:147], v[204:207]\n\t"
      "v_mfma_f32_16x16x32_bf16 v[208:211], v[72:75], v[144:147], v[208:211]\n\t"
      "v_mfma_f32_16x16x32_bf16 v[212:215], v[76:79], v[144:147], v[212:215]\n\t"
      "v_mfma_f32_16x16x32_bf16 v[200:203], v[80:83], v[148:151], v[200:203]\n\t"
      "v_mfma_f32_16x16x32_bf16 v[204:207], v[84:87], v[148:151], v[204:207]\n\t"
      "v_mfma_f32_16x16x32_bf16 v[208:211], v[88:91], v[148:151], v[208:211]\n\t"
      "v_mfma_f32_16x16x32_bf16 v[212:215], v[92:95], v[148:151], v[212:215]\n\t"
      "v_mfma_f32_16x16x32_bf16 v[200:203], v[96:99], v[152:155], v[200:203]\n\t"
      "v_mfma_f32_16x16x32_bf16 v[204:207], v[100:103], v[152:155], v[204:207]\n\t"
      "v_mfma_f32_16x16x32_bf16 v[208:211], v[104:107], v[152:155], v[208:211]\n\t"
      "v_mfma_f32_16x16x32_bf16 v[212:215], v[108:111], v[152:155], v[212:215]\n\t"
      "v_mfma_f32_16x16x32_bf16 v[200:203], v[112:115], v[156:159], v[200:203]\n\t"
      "v_mfma_f32_16x16x32_bf16 v[204:207], v[116:119], v[156:159], v[204:207]\n\t"
      "v_mfma_f32_16x16x32_bf16 v[208:211], v[120:123], v[156:159], v[208:211]\n\t"
      "v_mfma_f32_16x16x32_bf16 v[212:215], v[124:127], v[156:159], v[212:215]\n\t"
      "global_load_dwordx4 v[128:131], v176, s[56:57] sc1\n\t"
      "global_load_dwordx4 v[132:135], v177, s[56:57] sc1\n\t"
      "global_load_dwordx4 v[136:139], v178, s[56:57] sc1\n\t"
      "global_load_dwordx4 v[140:143], v179, s[56:57] sc1\n\t"
      "global_load_dwordx4 v[144:147], v180, s[56:57] sc1\n\t"
      "global_load_dwordx4 v[148:151], v181, s[56:57] sc1\n\t"
      "global_load_dwordx4 v[152:155], v182, s[56:57] sc1\n\t"
      "global_load_dwordx4 v[156:159], v183, s[56:57] sc1\n\t"
      "s_waitcnt vmcnt(0)\n\t"
      "s_nop 7\n\t"
      "s_branch NQ1B_%=\n\t"
      "NQ2R_%=:\n\t"
      "s_mov_b32 s51, 0\n\t"
      "NQ2L_%=:\n\t"
      "s_add_u32 s51, s51, 1\n\t"
      "s_cmp_lt_u32 s51, 262144\n\t"
      "s_cbranch_scc0 NQ2X_%=\n\t"
      "s_waitcnt vmcnt(0)\n\t"
      "s_sleep 1\n\t"
      "global_load_dwordx4 v[128:131], v176, s[56:57] sc1\n\t"
      "global_load_dwordx4 v[132:135], v177, s[56:57] sc1\n\t"
      "global_load_dwordx4 v[136:139], v178, s[56:57] sc1\n\t"
      "global_load_dwordx4 v[140:143], v179, s[56:57] sc1\n\t"
      "global_load_dwordx4 v[144:147], v180, s[56:57] sc1\n\t"
      "global_load_dwordx4 v[148:151], v181, s[56:57] sc1\n\t"
      "global_load_dwordx4 v[152:155], v182, s[56:57] sc1\n\t"
      "global_load_dwordx4 v[156:159], v183, s[56:57] sc1\n\t"
      "s_waitcnt vmcnt(0)\n\t"
      "v_max3_u32 v234, v128, v129, v130\n\t"
      "v_max3_u32 v234, v234, v131, v132\n\t"
      "v_max3_u32 v234, v234, v133, v134\n\t"
      "v_max3_u32 v234, v234, v135, v136\n\t"
      "v_max3_u32 v234, v234, v137, v138\n\t"
      "v_max3_u32 v234, v234, v139, v140\n\t"
      "v_max3_u32 v234, v234, v141, v142\n\t"
      "v_max3_u32 v234, v234, v143, v144\n\t"
      "v_max3_u32 v234, v234, v145, v146\n\t"
      "v_max3_u32 v234, v234, v147, v148\n\t"
      "v_max3_u32 v234, v234, v149, v150\n\t"
      "v_max3_u32 v234, v234, v151, v152\n\t"
      "v_max3_u32 v234, v234, v153, v154\n\t"
      "v_max3_u32 v234, v234, v155, v156\n\t"
      "v_max3_u32 v234, v234, v157, v158\n\t"
      "v_max_u32_e32 v234, v234, v159\n\t"
      "v_cmp_eq_u32_e32 vcc, -1, v234\n\t"
      "s_cbranch_vccnz NQ2L_%=\n\t"
      "NQ2X_%=:\n\t"
      "v_mfma_f32_16x16x32_bf16 v[184:187], v[0:3], v[128:131], v[236:239]\n\t"
      "v_mfma_f32_16x16x32_bf16 v[188:191], v[4:7], v[128:131], 0\n\t"
      "v_mfma_f32_16x16x32_bf16 v[192:195], v[8:11], v[128:131], 0\n\t"
      "v_mfma_f32_16x16x32_bf16 v[196:199], v[12:15], v[128:131], 0\n\t"
      "v_mfma_f32_16x16x32_bf16 v[184:187], v[16:19], v[132:135], v[184:187]\n\t"
      "v_mfma_f32_16x16x32_bf16 v[188:191], v[20:23], v[132:135], v[188:191]\n\t"
      "v_mfma_f32_16x16x32_bf16 v[192:195], v[24:27], v[132:135], v[192:195]\n\t"
      "v_mfma_f32_16x16x32_bf16 v[196:199], v[28:31], v[132:135], v[196:199]\n\t"
      "v_mfma_f32_16x16x32_bf16 v[184:187], v[32:35], v[136:139], v[184:187]\n\t"
      "v_mfma_f32_16x16x32_bf16 v[188:191], v[36:39], v[136:139], v[188:191]\n\t"
      "v_mfma_f32_16x16x32_bf16 v[192:195], v[40:43], v[136:139], v[192:195]\n\t"
      "v_mfma_f32_16x16x32_bf16 v[196:199], v[44:47], v[136:139], v[196:199]\n\t"
      "v_mfma_f32_16x16x32_bf16 v[184:187], v[48:51], v[140:143], v[184:187]\n\t"
      "v_mfma_f32_16x16x32_bf16 v[188:191], v[52:55], v[140:143], v[188:191]\n\t"
      "v_mfma_f32_16x16x32_bf16 v[192:195], v[56:59], v[140:143], v[192:195]\n\t"
      "v_mfma_f32_16x16x32_bf16 v[196:199], v[60:63], v[140:143], v[196:199]\n\t"
      "v_mfma_f32_16x16x32_bf16 v[184:187], v[64:67], v[144:147], v[184:187]\n\t"
      "v_mfma_f32_16x16x32_bf16 v[188:191], v[68:71], v[144:147], v[188:191]\n\t"
      "v_mfma_f32_16x16x32_bf16 v[192:195], v[72:75], v[144:147], v[192:195]\n\t"
      "v_mfma_f32_16x16x32_bf16 v[196:199], v[76:79], v[144:147], v[196:199]\n\t"
      "v_mfma_f32_16x16x32_bf16 v[184:187], v[80:83], v[148:151], v[184:187]\n\t"
      "v_mfma_f32_16x16x32_bf16 v[188:191], v[84:87], v[148:151], v[188:191]\n\t"
      "v_mfma_f32_16x16x32_bf16 v[192:195], v[88:91], v[148:151], v[192:195]\n\t"
      "v_mfma_f32_16x16x32_bf16 v[196:199], v[92:95], v[148:151], v[196:199]\n\t"
      "v_mfma_f32_16x16x32_bf16 v[184:187], v[96:99], v[152:155], v[184:187]\n\t"
      "v_mfma_f32_16x16x32_bf16 v[188:191], v[100:103], v[152:155], v[188:191]\n\t"
      "v_mfma_f32_16x16x32_bf16 v[192:195], v[104:107], v[152:155], v[192:195]\n\t"
      "v_mfma_f32_16x16x32_bf16 v[196:199], v[108:111], v[152:155], v[196:199]\n\t"
      "v_mfma_f32_16x16x32_bf16 v[184:187], v[112:115], v[156:159], v[184:187]\n\t"
      "v_mfma_f32_16x16x32_bf16 v[188:191], v[116:119], v[156:159], v[188:191]\n\t"
      "v_mfma_f32_16x16x32_bf16 v[192:195], v[120:123], v[156:159], v[192:195]\n\t"
      "v_mfma_f32_16x16x32_bf16 v[196:199], v[124:127], v[156:159], v[196:199]\n\t"
      "global_load_dwordx4 v[128:131], v176, s[58:59] sc1\n\t"
      "global_load_dwordx4 v[132:135], v177, s[58:59] sc1\n\t"
      "global_load_dwordx4 v[136:139], v178, s[58:59] sc1\n\t"
      "global_load_dwordx4 v[140:143], v179, s[58:59] sc1\n\t"
      "global_load_dwordx4 v[144:147], v180, s[58:59] sc1\n\t"
      "global_load_dwordx4 v[148:151], v181, s[58:59] sc1\n\t"
      "global_load_dwordx4 v[152:155], v182, s[58:59] sc1\n\t"
      "global_load_dwordx4 v[156:159], v183, s[58:59] sc1\n\t"
      "s_waitcnt vmcnt(0)\n\t"
      "s_nop 7\n\t"
      "s_branch NQ2B_%=\n\t"
      "NQ3R_%=:\n\t"
      "s_mov_b32 s51, 0\n\t"
      "NQ3L_%=:\n\t"
      "s_add_u32 s51, s51, 1\n\t"
      "s_cmp_lt_u32 s51, 262144\n\t"
      "s_cbranch_scc0 NQ3X_%=\n\t"
      "s_waitcnt vmcnt(0)\n\t"
      "s_sleep 1\n\t"
      "global_load_dwordx4 v[128:131], v176, s[58:59] sc1\n\t"
      "global_load_dwordx4 v[132:135], v177, s[58:59] sc1\n\t"
      "global_load_dwordx4 v[136:139], v178, s[58:59] sc1\n\t"
      "global_load_dwordx4 v[140:143], v179, s[58:59] sc1\n\t"
      "global_load_dwordx4 v[144:147], v180, s[58:59] sc1\n\t"
      "global_load_dwordx4 v[148:151], v181, s[58:59] sc1\n\t"
      "global_load_dwordx4 v[152:155], v182, s[58:59] sc1\n\t"
      "global_load_dwordx4 v[156:159], v183, s[58:59] sc1\n\t"
      "s_waitcnt vmcnt(0)\n\t"
      "v_max3_u32 v234, v128, v129, v130\n\t"
      "v_max3_u32 v234, v234, v131, v132\n\t"
      "v_max3_u32 v234, v234, v133, v134\n\t"
      "v_max3_u32 v234, v234, v135, v136\n\t"
      "v_max3_u32 v234, v234, v137, v138\n\t"
      "v_max3_u32 v234, v234, v139, v140\n\t"
      "v_max3_u32 v234, v234, v141, v142\n\t"
      "v_max3_u32 v234, v234, v143, v144\n\t"
      "v_max3_u32 v234, v234, v145, v146\n\t"
      "v_max3_u32 v234, v234, v147, v148\n\t"
      "v_max3_u32 v234, v234, v149, v150\n\t"
      "v_max3_u32 v234, v234, v151, v152\n\t"
      "v_max3_u32 v234, v234, v153, v154\n\t"
      "v_max3_u32 v234, v234, v155, v156\n\t"
      "v_max3_u32 v234, v234, v157, v158\n\t"
      "v_max_u32_e32 v234, v234, v159\n\t"
      "v_cmp_eq_u32_e32 vcc, -1, v234\n\t"
      "s_cbranch_vccnz NQ3L_%=\n\t"
      "NQ3X_%=:\n\t"
      "v_mfma_f32_16x16x32_bf16 v[200:203], v[0:3], v[128:131], v[236:239]\n\t"
      "v_mfma_f32_16x16x32_bf16 v[204:207], v[4:7], v[128:131], 0\n\t"
      "v_mfma_f32_16x16x32_bf16 v[208:211], v[8:11], v[128:131], 0\n\t"
      "v_mfma_f32_16x16x32_bf16 v[212:215], v[12:15], v[128:131], 0\n\t"
      "v_mfma_f32_16x16x32_bf16 v[200:203], v[16:19], v[132:135], v[200:203]\n\t"
      "v_mfma_f32_16x16x32_bf16 v[204:207], v[20:23], v[132:135], v[204:207]\n\t"
      "v_mfma_f32_16x16x32_bf16 v[208:211], v[24:27], v[132:135], v[208:211]\n\t"
      "v_mfma_f32_16x16x32_bf16 v[212:215], v[28:31], v[132:135], v[212:215]\n\t"
      "v_mfma_f32_16x16x32_bf16 v[200:203], v[32:35], v[136:139], v[200:203]\n\t"
      "v_mfma_f32_16x16x32_bf16 v[204:207], v[36:39], v[136:139], v[204:207]\n\t"
      "v_mfma_f32_16x16x32_bf16 v[208:211], v[40:43], v[136:139], v[208:211]\n\t"
      "v_mfma_f32_16x16x32_bf16 v[212:215], v[44:47], v[136:139], v[212:215]\n\t"
      "v_mfma_f32_16x16x32_bf16 v[200:203], v[48:51], v[140:143], v[200:203]\n\t"
      "v_mfma_f32_16x16x32_bf16 v[204:207], v[52:55], v[140:143], v[204:207]\n\t"
      "v_mfma_f32_16x16x32_bf16 v[208:211], v[56:59], v[140:143], v[208:211]\n\t"
      "v_mfma_f32_16x16x32_bf16 v[212:215], v[60:63], v[140:143], v[212:215]\n\t"
      "v_mfma_f32_16x16x32_bf16 v[200:203], v[64:67], v[144:147], v[200:203]\n\t"
      "v_mfma_f32_16x16x32_bf16 v[204:207], v[68:71], v[144:147], v[204:207]\n\t"
      "v_mfma_f32_16x16x32_bf16 v[208:211], v[72:75], v[144:147], v[208:211]\n\t"
      "v_mfma_f32_16x16x32_bf16 v[212:215], v[76:79], v[144:147], v[212:215]\n\t"
      "v_mfma_f32_16x16x32_bf16 v[200:203], v[80:83], v[148:151], v[200:203]\n\t"
      "v_mfma_f32_16x16x32_bf16 v[204:207], v[84:87], v[148:151], v[204:207]\n\t"
      "v_mfma_f32_16x16x32_bf16 v[208:211], v[88:91], v[148:151], v[208:211]\n\t"
      "v_mfma_f32_16x16x32_bf16 v[212:215], v[92:95], v[148:151], v[212:215]\n\t"
      "v_mfma_f32_16x16x32_bf16 v[200:203], v[96:99], v[152:155], v[200:203]\n\t"
      "v_mfma_f32_16x16x32_bf16 v[204:207], v[100:103], v[152:155], v[204:207]\n\t"
      "v_mfma_f32_16x16x32_bf16 v[208:211], v[104:107], v[152:155], v[208:211]\n\t"
      "v_mfma_f32_16x16x32_bf16 v[212:215], v[108:111], v[152:155], v[212:215]\n\t"
      "v_mfma_f32_16x16x32_bf16 v[200:203], v[112:115], v[156:159], v[200:203]\n\t"
      "v_mfma_f32_16x16x32_bf16 v[204:207], v[116:119], v[156:159], v[204:207]\n\t"
      "v_mfma_f32_16x16x32_bf16 v[208:211], v[120:123], v[156:159], v[208:211]\n\t"
      "v_mfma_f32_16x16x32_bf16 v[212:215], v[124:127], v[156:159], v[212:215]\n\t"
      "global_load_dwordx4 v[128:131], v176, s[60:61] sc1\n\t"
      "global_load_dwordx4 v[132:135], v177, s[60:61] sc1\n\t"
      "global_load_dwordx4 v[136:139], v178, s[60:61] sc1\n\t"
      "global_load_dwordx4 v[140:143], v179, s[60:61] sc1\n\t"
      "global_load_dwordx4 v[144:147], v180, s[60:61] sc1\n\t"
      "global_load_dwordx4 v[148:151], v181, s[60:61] sc1\n\t"
      "global_load_dwordx4 v[152:155], v182, s[60:61] sc1\n\t"
      "global_load_dwordx4 v[156:159], v183, s[60:61] sc1\n\t"
      "s_waitcnt vmcnt(0)\n\t"
      "s_nop 7\n\t"
      "s_branch NQ3B_%=\n\t"
      "NQ4R_%=:\n\t"
      "s_mov_b32 s51, 0\n\t"
      "NQ4L_%=:\n\t"
      "s_add_u32 s51, s51, 1\n\t"
      "s_cmp_lt_u32 s51, 262144\n\t"
      "s_cbranch_scc0 NQ4X_%=\n\t"
      "s_waitcnt vmcnt(0)\n\t"
      "s_sleep 1\n\t"
      "global_load_dwordx4 v[128:131], v176, s[60:61] sc1\n\t"
      "global_load_dwordx4 v[132:135], v177, s[60:61] sc1\n\t"
      "global_load_dwordx4 v[136:139], v178, s[60:61] sc1\n\t"
      "global_load_dwordx4 v[140:143], v179, s[60:61] sc1\n\t"
      "global_load_dwordx4 v[144:147], v180, s[60:61] sc1\n\t"
      "global_load_dwordx4 v[148:151], v181, s[60:61] sc1\n\t"
      "global_load_dwordx4 v[152:155], v182, s[60:61] sc1\n\t"
      "global_load_dwordx4 v[156:159], v183, s[60:61] sc1\n\t"
      "s_waitcnt vmcnt(0)\n\t"
      "v_max3_u32 v234, v128, v129, v130\n\t"
      "v_max3_u32 v234, v234, v131, v132\n\t"
      "v_max3_u32 v234, v234, v133, v134\n\t"
      "v_max3_u32 v234, v234, v135, v136\n\t"
      "v_max3_u32 v234, v234, v137, v138\n\t"
      "v_max3_u32 v234, v234, v139, v140\n\t"
      "v_max3_u32 v234, v234, v141, v142\n\t"
      "v_max3_u32 v234, v234, v143, v144\n\t"
      "v_max3_u32 v234, v234, v145, v146\n\t"
      "v_max3_u32 v234, v234, v147, v148\n\t"
      "v_max3_u32 v234, v234, v149, v150\n\t"
      "v_max3_u32 v234, v234, v151, v152\n\t"
      "v_max3_u32 v234, v234, v153, v154\n\t"
      "v_max3_u32 v234, v234, v155, v156\n\t"
      "v_max3_u32 v234, v234, v157, v158\n\t"
      "v_max_u32_e32 v234, v234, v159\n\t"
      "v_cmp_eq_u32_e32 vcc, -1, v234\n\t"
      "s_cbranch_vccnz NQ4L_%=\n\t"
      "NQ4X_%=:\n\t"
      "v_mfma_f32_16x16x32_bf16 v[184:187], v[0:3], v[128:131], v[236:239]\n\t"
      "v_mfma_f32_16x16x32_bf16 v[188:191], v[4:7], v[128:131], 0\n\t"
      "v_mfma_f32_16x16x32_bf16 v[192:195], v[8:11], v[128:131], 0\n\t"
      "v_mfma_f32_16x16x32_bf16 v[196:199], v[12:15], v[128:131], 0\n\t"
      "v_mfma_f32_16x16x32_bf16 v[184:187], v[16:19], v[132:135], v[184:187]\n\t"
      "v_mfma_f32_16x16x32_bf16 v[188:191], v[20:23], v[132:135], v[188:191]\n\t"
      "v_mfma_f32_16x16x32_bf16 v[192:195], v[24:27], v[132:135], v[192:195]\n\t"
      "v_mfma_f32_16x16x32_bf16 v[196:199], v[28:31], v[132:135], v[196:199]\n\t"
      "v_mfma_f32_16x16x32_bf16 v[184:187], v[32:35], v[136:139], v[184:187]\n\t"
      "v_mfma_f32_16x16x32_bf16 v[188:191], v[36:39], v[136:139], v[188:191]\n\t"
      "v_mfma_f32_16x16x32_bf16 v[192:195], v[40:43], v[136:139], v[192:195]\n\t"
      "v_mfma_f32_16x16x32_bf16 v[196:199], v[44:47], v[136:139], v[196:199]\n\t"
      "v_mfma_f32_16x16x32_bf16 v[184:187], v[48:51], v[140:143], v[184:187]\n\t"
      "v_mfma_f32_16x16x32_bf16 v[188:191], v[52:55], v[140:143], v[188:191]\n\t"
      "v_mfma_f32_16x16x32_bf16 v[192:195], v[56:59], v[140:143], v[192:195]\n\t"
      "v_mfma_f32_16x16x32_bf16 v[196:199], v[60:63], v[140:143], v[196:199]\n\t"
      "v_mfma_f32_16x16x32_bf16 v[184:187], v[64:67], v[144:147], v[184:187]\n\t"
      "v_mfma_f32_16x16x32_bf16 v[188:191], v[68:71], v[144:147], v[188:191]\n\t"
      "v_mfma_f32_16x16x32_bf16 v[192:195], v[72:75], v[144:147], v[192:195]\n\t"
      "v_mfma_f32_16x16x32_bf16 v[196:199], v[76:79], v[144:147], v[196:199]\n\t"
      "v_mfma_f32_16x16x32_bf16 v[184:187], v[80:83], v[148:151], v[184:187]\n\t"
      "v_mfma_f32_16x16x32_bf16 v[188:191], v[84:87], v[148:151], v[188:191]\n\t"
      "v_mfma_f32_16x16x32_bf16 v[192:195], v[88:91], v[148:151], v[192:195]\n\t"
      "v_mfma_f32_16x16x32_bf16 v[196:199], v[92:95], v[148:151], v[196:199]\n\t"
      "v_mfma_f32_16x16x32_bf16 v[184:187], v[96:99], v[152:155], v[184:187]\n\t"
      "v_mfma_f32_16x16x32_bf16 v[188:191], v[100:103], v[152:155], v[188:191]\n\t"
      "v_mfma_f32_16x16x32_bf16 v[192:195], v[104:107], v[152:155], v[192:195]\n\t"
      "v_mfma_f32_16x16x32_bf16 v[196:199], v[108:111], v[152:155], v[196:199]\n\t"
      "v_mfma_f32_16x16x32_bf16 v[184:187], v[112:115], v[156:159], v[184:187]\n\t"
      "v_mfma_f32_16x16x32_bf16 v[188:191], v[116:119], v[156:159], v[188:191]\n\t"
      "v_mfma_f32_16x16x32_bf16 v[192:195], v[120:123], v[156:159], v[192:195]\n\t"
      "v_mfma_f32_16x16x32_bf16 v[196:199], v[124:127], v[156:159], v[196:199]\n\t"
      "global_load_dwordx4 v[128:131], v176, s[62:63] sc1\n\t"
      "global_load_dwordx4 v[132:135], v177, s[62:63] sc1\n\t"
      "global_load_dwordx4 v[136:139], v178, s[62:63] sc1\n\t"
      "global_load_dwordx4 v[140:143], v179, s[62:63] sc1\n\t"
      "global_load_dwordx4 v[144:147], v180, s[62:63] sc1\n\t"
      "global_load_dwordx4 v[148:151], v181, s[62:63] sc1\n\t"
      "global_load_dwordx4 v[152:155], v182, s[62:63] sc1\n\t"
      "global_load_dwordx4 v[156:159], v183, s[62:63] sc1\n\t"
      "s_waitcnt vmcnt(0)\n\t"
      "s_nop 7\n\t"
      "s_branch NQ4B_%=\n\t"
      "NQ5R_%=:\n\t"
      "s_mov_b32 s51, 0\n\t"
      "NQ5L_%=:\n\t"
      "s_add_u32 s51, s51, 1\n\t"
      "s_cmp_lt_u32 s51, 262144\n\t"
      "s_cbranch_scc0 NQ5X_%=\n\t"
      "s_waitcnt vmcnt(0)\n\t"
      "s_sleep 1\n\t"
      "global_load_dwordx4 v[128:131], v176, s[62:63] sc1\n\t"
      "global_load_dwordx4 v[132:135], v177, s[62:63] sc1\n\t"
      "global_load_dwordx4 v[136:139], v178, s[62:63] sc1\n\t"
      "global_load_dwordx4 v[140:143], v179, s[62:63] sc1\n\t"
      "global_load_dwordx4 v[144:147], v180, s[62:63] sc1\n\t"
      "global_load_dwordx4 v[148:151], v181, s[62:63] sc1\n\t"
      "global_load_dwordx4 v[152:155], v182, s[62:63] sc1\n\t"
      "global_load_dwordx4 v[156:159], v183, s[62:63] sc1\n\t"
      "s_waitcnt vmcnt(0)\n\t"
      "v_max3_u32 v234, v128, v129, v130\n\t"
      "v_max3_u32 v234, v234, v131, v132\n\t"
      "v_max3_u32 v234, v234, v133, v134\n\t"
      "v_max3_u32 v234, v234, v135, v136\n\t"
      "v_max3_u32 v234, v234, v137, v138\n\t"
      "v_max3_u32 v234, v234, v139, v140\n\t"
      "v_max3_u32 v234, v234, v141, v142\n\t"
      "v_max3_u32 v234, v234, v143, v144\n\t"
      "v_max3_u32 v234, v234, v145, v146\n\t"
      "v_max3_u32 v234, v234, v147, v148\n\t"
      "v_max3_u32 v234, v234, v149, v150\n\t"
      "v_max3_u32 v234, v234, v151, v152\n\t"
      "v_max3_u32 v234, v234, v153, v154\n\t"
      "v_max3_u32 v234, v234, v155, v156\n\t"
      "v_max3_u32 v234, v234, v157, v158\n\t"
      "v_max_u32_e32 v234, v234, v159\n\t"
      "v_cmp_eq_u32_e32 vcc, -1, v234\n\t"
      "s_cbranch_vccnz NQ5L_%=\n\t"
      "NQ5X_%=:\n\t"
      "v_mfma_f32_16x16x32_bf16 v[200:203], v[0:3], v[128:131], v[236:239]\n\t"
      "v_mfma_f32_16x16x32_bf16 v[204:207], v[4:7], v[128:131], 0\n\t"
      "v_mfma_f32_16x16x32_bf16 v[208:211], v[8:11], v[128:131], 0\n\t"
      "v_mfma_f32_16x16x32_bf16 v[212:215], v[12:15], v[128:131], 0\n\t"
      "v_mfma_f32_16x16x32_bf16 v[200:203], v[16:19], v[132:135], v[200:203]\n\t"
      "v_mfma_f32_16x16x32_bf16 v[204:207], v[20:23], v[132:135], v[204:207]\n\t"
      "v_mfma_f32_16x16x32_bf16 v[208:211], v[24:27], v[132:135], v[208:211]\n\t"
      "v_mfma_f32_16x16x32_bf16 v[212:215], v[28:31], v[132:135], v[212:215]\n\t"
      "v_mfma_f32_16x16x32_bf16 v[200:203], v[32:35], v[136:139], v[200:203]\n\t"
      "v_mfma_f32_16x16x32_bf16 v[204:207], v[36:39], v[136:139], v[204:207]\n\t"
      "v_mfma_f32_16x16x32_bf16 v[208:211], v[40:43], v[136:139], v[208:211]\n\t"
      "v_mfma_f32_16x16x32_bf16 v[212:215], v[44:47], v[136:139], v[212:215]\n\t"
      "v_mfma_f32_16x16x32_bf16 v[200:203], v[48:51], v[140:143], v[200:203]\n\t"
      "v_mfma_f32_16x16x32_bf16 v[204:207], v[52:55], v[140:143], v[204:207]\n\t"
      "v_mfma_f32_16x16x32_bf16 v[208:211], v[56:59], v[140:143], v[208:211]\n\t"
      "v_mfma_f32_16x16x32_bf16 v[212:215], v[60:63], v[140:143], v[212:215]\n\t"
      "v_mfma_f32_16x16x32_bf16 v[200:203], v[64:67], v[144:147], v[200:203]\n\t"
      "v_mfma_f32_16x16x32_bf16 v[204:207], v[68:71], v[144:147], v[204:207]\n\t"
      "v_mfma_f32_16x16x32_bf16 v[208:211], v[72:75], v[144:147], v[208:211]\n\t"
      "v_mfma_f32_16x16x32_bf16 v[212:215], v[76:79], v[144:147], v[212:215]\n\t"
      "v_mfma_f32_16x16x32_bf16 v[200:203], v[80:83], v[148:151], v[200:203]\n\t"
      "v_mfma_f32_16x16x32_bf16 v[204:207], v[84:87], v[148:151], v[204:207]\n\t"
      "v_mfma_f32_16x16x32_bf16 v[208:211], v[88:91], v[148:151], v[208:211]\n\t"
      "v_mfma_f32_16x16x32_bf16 v[212:215], v[92:95], v[148:151], v[212:215]\n\t"
      "v_mfma_f32_16x16x32_bf16 v[200:203], v[96:99], v[152:155], v[200:203]\n\t"
      "v_mfma_f32_16x16x32_bf16 v[204:207], v[100:103], v[152:155], v[204:207]\n\t"
      "v_mfma_f32_16x16x32_bf16 v[208:211], v[104:107], v[152:155], v[208:211]\n\t"
      "v_mfma_f32_16x16x32_bf16 v[212:215], v[108:111], v[152:155], v[212:215]\n\t"
      "v_mfma_f32_16x16x32_bf16 v[200:203], v[112:115], v[156:159], v[200:203]\n\t"
      "v_mfma_f32_16x16x32_bf16 v[204:207], v[116:119], v[156:159], v[204:207]\n\t"
      "v_mfma_f32_16x16x32_bf16 v[208:211], v[120:123], v[156:159], v[208:211]\n\t"
      "v_mfma_f32_16x16x32_bf16 v[212:215], v[124:127], v[156:159], v[212:215]\n\t"
      "global_load_dwordx4 v[128:131], v176, s[64:65] sc1\n\t"
      "global_load_dwordx4 v[132:135], v177, s[64:65] sc1\n\t"
      "global_load_dwordx4 v[136:139], v178, s[64:65] sc1\n\t"
      "global_load_dwordx4 v[140:143], v179, s[64:65] sc1\n\t"
      "global_load_dwordx4 v[144:147], v180, s[64:65] sc1\n\t"
      "global_load_dwordx4 v[148:151], v181, s[64:65] sc1\n\t"
      "global_load_dwordx4 v[152:155], v182, s[64:65] sc1\n\t"
      "global_load_dwordx4 v[156:159], v183, s[64:65] sc1\n\t"
      "s_waitcnt vmcnt(0)\n\t"
      "s_nop 7\n\t"
      "s_branch NQ5B_%=\n\t"
      "NQ6R_%=:\n\t"
      "s_mov_b32 s51, 0\n\t"
      "NQ6L_%=:\n\t"
      "s_add_u32 s51, s51, 1\n\t"
      "s_cmp_lt_u32 s51, 262144\n\t"
      "s_cbranch_scc0 NQ6X_%=\n\t"
      "s_waitcnt vmcnt(0)\n\t"
      "s_sleep 1\n\t"
      "global_load_dwordx4 v[128:131], v176, s[64:65] sc1\n\t"
      "global_load_dwordx4 v[132:135], v177, s[64:65] sc1\n\t"
      "global_load_dwordx4 v[136:139], v178, s[64:65] sc1\n\t"
      "global_load_dwordx4 v[140:143], v179, s[64:65] sc1\n\t"
      "global_load_dwordx4 v[144:147], v180, s[64:65] sc1\n\t"
      "global_load_dwordx4 v[148:151], v181, s[64:65] sc1\n\t"
      "global_load_dwordx4 v[152:155], v182, s[64:65] sc1\n\t"
      "global_load_dwordx4 v[156:159], v183, s[64:65] sc1\n\t"
      "s_waitcnt vmcnt(0)\n\t"
      "v_max3_u32 v234, v128, v129, v130\n\t"
      "v_max3_u32 v234, v234, v131, v132\n\t"
      "v_max3_u32 v234, v234, v133, v134\n\t"
      "v_max3_u32 v234, v234, v135, v136\n\t"
      "v_max3_u32 v234, v234, v137, v138\n\t"
      "v_max3_u32 v234, v234, v139, v140\n\t"
      "v_max3_u32 v234, v234, v141, v142\n\t"
      "v_max3_u32 v234, v234, v143, v144\n\t"
      "v_max3_u32 v234, v234, v145, v146\n\t"
      "v_max3_u32 v234, v234, v147, v148\n\t"
      "v_max3_u32 v234, v234, v149, v150\n\t"
      "v_max3_u32 v234, v234, v151, v152\n\t"
      "v_max3_u32 v234, v234, v153, v154\n\t"
      "v_max3_u32 v234, v234, v155, v156\n\t"
      "v_max3_u32 v234, v234, v157, v158\n\t"
      "v_max_u32_e32 v234, v234, v159\n\t"
      "v_cmp_eq_u32_e32 vcc, -1, v234\n\t"
      "s_cbranch_vccnz NQ6L_%=\n\t"
      "NQ6X_%=:\n\t"
      "v_mfma_f32_16x16x32_bf16 v[184:187], v[0:3], v[128:131], v[236:239]\n\t"
      "v_mfma_f32_16x16x32_bf16 v[188:191], v[4:7], v[128:131], 0\n\t"
      "v_mfma_f32_16x16x32_bf16 v[192:195], v[8:11], v[128:131], 0\n\t"
      "v_mfma_f32_16x16x32_bf16 v[196:199], v[12:15], v[128:131], 0\n\t"
      "v_mfma_f32_16x16x32_bf16 v[184:187], v[16:19], v[132:135], v[184:187]\n\t"
      "v_mfma_f32_16x16x32_bf16 v[188:191], v[20:23], v[132:135], v[188:191]\n\t"
      "v_mfma_f32_16x16x32_bf16 v[192:195], v[24:27], v[132:135], v[192:195]\n\t"
      "v_mfma_f32_16x16x32_bf16 v[196:199], v[28:31], v[132:135], v[196:199]\n\t"
      "v_mfma_f32_16x16x32_bf16 v[184:187], v[32:35], v[136:139], v[184:187]\n\t"
      "v_mfma_f32_16x16x32_bf16 v[188:191], v[36:39], v[136:139], v[188:191]\n\t"
      "v_mfma_f32_16x16x32_bf16 v[192:195], v[40:43], v[136:139], v[192:195]\n\t"
      "v_mfma_f32_16x16x32_bf16 v[196:199], v[44:47], v[136:139], v[196:199]\n\t"
      "v_mfma_f32_16x16x32_bf16 v[184:187], v[48:51], v[140:143], v[184:187]\n\t"
      "v_mfma_f32_16x16x32_bf16 v[188:191], v[52:55], v[140:143], v[188:191]\n\t"
      "v_mfma_f32_16x16x32_bf16 v[192:195], v[56:59], v[140:143], v[192:195]\n\t"
      "v_mfma_f32_16x16x32_bf16 v[196:199], v[60:63], v[140:143], v[196:199]\n\t"
      "v_mfma_f32_16x16x32_bf16 v[184:187], v[64:67], v[144:147], v[184:187]\n\t"
      "v_mfma_f32_16x16x32_bf16 v[188:191], v[68:71], v[144:147], v[188:191]\n\t"
      "v_mfma_f32_16x16x32_bf16 v[192:195], v[72:75], v[144:147], v[192:195]\n\t"
      "v_mfma_f32_16x16x32_bf16 v[196:199], v[76:79], v[144:147], v[196:199]\n\t"
      "v_mfma_f32_16x16x32_bf16 v[184:187], v[80:83], v[148:151], v[184:187]\n\t"
      "v_mfma_f32_16x16x32_bf16 v[188:191], v[84:87], v[148:151], v[188:191]\n\t"
      "v_mfma_f32_16x16x32_bf16 v[192:195], v[88:91], v[148:151], v[192:195]\n\t"
      "v_mfma_f32_16x16x32_bf16 v[196:199], v[92:95], v[148:151], v[196:199]\n\t"
      "v_mfma_f32_16x16x32_bf16 v[184:187], v[96:99], v[152:155], v[184:187]\n\t"
      "v_mfma_f32_16x16x32_bf16 v[188:191], v[100:103], v[152:155], v[188:191]\n\t"
      "v_mfma_f32_16x16x32_bf16 v[192:195], v[104:107], v[152:155], v[192:195]\n\t"
      "v_mfma_f32_16x16x32_bf16 v[196:199], v[108:111], v[152:155], v[196:199]\n\t"
      "v_mfma_f32_16x16x32_bf16 v[184:187], v[112:115], v[156:159], v[184:187]\n\t"
      "v_mfma_f32_16x16x32_bf16 v[188:191], v[116:119], v[156:159], v[188:191]\n\t"
      "v_mfma_f32_16x16x32_bf16 v[192:195], v[120:123], v[156:159], v[192:195]\n\t"
      "v_mfma_f32_16x16x32_bf16 v[196:199], v[124:127], v[156:159], v[196:199]\n\t"
      "s_waitcnt vmcnt(0)\n\t"
      "s_nop 7\n\t"
      "s_branch NQ6B_%=\n\t"
      "FP0R_%=:\n\t"
      "s_mov_b32 s51, 0\n\t"
      "FP0L_%=:\n\t"
      "s_add_u32 s51, s51, 1\n\t"
      "s_cmp_lt_u32 s51, 262144\n\t"
      "s_cbranch_scc0 FP0X_%=\n\t"
      "s_waitcnt vmcnt(0)\n\t"
      "s_sleep 1\n\t"
      "global_load_dwordx4 v[128:131], v176, s[52:53] sc1\n\t"
      "global_load_dwordx4 v[132:135], v177, s[52:53] sc1\n\t"
      "global_load_dwordx4 v[136:139], v178, s[52:53] sc1\n\t"
      "global_load_dwordx4 v[140:143], v179, s[52:53] sc1\n\t"
      "global_load_dwordx4 v[144:147], v180, s[52:53] sc1\n\t"
      "global_load_dwordx4 v[148:151], v181, s[52:53] sc1\n\t"
      "global_load_dwordx4 v[152:155], v182, s[52:53] sc1\n\t"
      "global_load_dwordx4 v[156:159], v183, s[52:53] sc1\n\t"
      "s_waitcnt vmcnt(7)\n\t"
      "v_max3_u32 v234, v128, v129, v130\n\t"
      "v_max_u32_e32 v234, v234, v131\n\t"
      "v_cmp_eq_u32_e32 vcc, -1, v234\n\t"
      "s_cbranch_vccnz FP0L_%=\n\t"
      "FP0X_%=:\n\t"
      "s_branch FP0B_%=\n\t"
      "FP1R_%=:\n\t"
      "s_mov_b32 s51, 0\n\t"
      "FP1L_%=:\n\t"
      "s_add_u32 s51, s51, 1\n\t"
      "s_cmp_lt_u32 s51, 262144\n\t"
      "s_cbranch_scc0 FP1X_%=\n\t"
      "s_waitcnt vmcnt(0)\n\t"
      "s_sleep 1\n\t"
      "global_load_dwordx4 v[132:135], v177, s[52:53] sc1\n\t"
      "global_load_dwordx4 v[136:139], v178, s[52:53] sc1\n\t"
      "global_load_dwordx4 v[140:143], v179, s[52:53] sc1\n\t"
      "global_load_dwordx4 v[144:147], v180, s[52:53] sc1\n\t"
      "global_load_dwordx4 v[148:151], v181, s[52:53] sc1\n\t"
      "global_load_dwordx4 v[152:155], v182, s[52:53] sc1\n\t"
      "global_load_dwordx4 v[156:159], v183, s[52:53] sc1\n\t"
      "global_load_dwordx4 v[128:131], v176, s[54:55] sc1\n\t"
      "s_waitcnt vmcnt(7)\n\t"
      "v_max3_u32 v234, v132, v133, v134\n\t"
      "v_max_u32_e32 v234, v234, v135\n\t"
      "v_cmp_eq_u32_e32 vcc, -1, v234\n\t"
      "s_cbranch_vccnz FP1L_%=\n\t"
      "FP1X_%=:\n\t"
      "s_branch FP1B_%=\n\t"
      "FP2R_%=:\n\t"
      "s_mov_b32 s51, 0\n\t"
      "FP2L_%=:\n\t"
      "s_add_u32 s51, s51, 1\n\t"
      "s_cmp_lt_u32 s51, 262144\n\t"
      "s_cbranch_scc0 FP2X_%=\n\t"
      "s_waitcnt vmcnt(0)\n\t"
      "s_sleep 1\n\t"
      "global_load_dwordx4 v[136:139], v178, s[52:53] sc1\n\t"
      "global_load_dwordx4 v[140:143], v179, s[52:53] sc1\n\t"
      "global_load_dwordx4 v[144:147], v180, s[52:53] sc1\n\t"
      "global_load_dwordx4 v[148:151], v181, s[52:53] sc1\n\t"
      "global_load_dwordx4 v[152:155], v182, s[52:53] sc1\n\t"
      "global_load_dwordx4 v[156:159], v183, s[52:53] sc1\n\t"
      "global_load_dwordx4 v[128:131], v176, s[54:55] sc1\n\t"
      "global_load_dwordx4 v[132:135], v177, s[54:55] sc1\n\t"
      "s_waitcnt vmcnt(7)\n\t"
      "v_max3_u32 v234, v136, v137, v138\n\t"
      "v_max_u32_e32 v234, v234, v139\n\t"
      "v_cmp_eq_u32_e32 vcc, -1, v234\n\t"
      "s_cbranch_vccnz FP2L_%=\n\t"
      "FP2X_%=:\n\t"
      "s_branch FP2B_%=\n\t"
      "FP3R_%=:\n\t"
      "s_mov_b32 s51, 0\n\t"
      "FP3L_%=:\n\t"
      "s_add_u32 s51, s51, 1\n\t"
      "s_cmp_lt_u32 s51, 262144\n\t"
      "s_cbranch_scc0 FP3X_%=\n\t"
      "s_waitcnt vmcnt(0)\n\t"
      "s_sleep 1\n\t"
      "global_load_dwordx4 v[140:143], v179, s[52:53] sc1\n\t"
      "global_load_dwordx4 v[144:147], v180, s[52:53] sc1\n\t"
      "global_load_dwordx4 v[148:151], v181, s[52:53] sc1\n\t"
      "global_load_dwordx4 v[152:155], v182, s[52:53] sc1\n\t"
      "global_load_dwordx4 v[156:159], v183, s[52:53] sc1\n\t"
      "global_load_dwordx4 v[128:131], v176, s[54:55] sc1\n\t"
      "global_load_dwordx4 v[132:135], v177, s[54:55] sc1\n\t"
      "global_load_dwordx4 v[136:139], v178, s[54:55] sc1\n\t"
      "s_waitcnt vmcnt(7)\n\t"
      "v_max3_u32 v234, v140, v141, v142\n\t"
      "v_max_u32_e32 v234, v234, v143\n\t"
      "v_cmp_eq_u32_e32 vcc, -1, v234\n\t"
      "s_cbranch_vccnz FP3L_%=\n\t"
      "FP3X_%=:\n\t"
      "s_branch FP3B_%=\n\t"
      "FP4R_%=:\n\t"
      "s_mov_b32 s51, 0\n\t"
      "FP4L_%=:\n\t"
      "s_add_u32 s51, s51, 1\n\t"
      "s_cmp_lt_u32 s51, 262144\n\t"
      "s_cbranch_scc0 FP4X_%=\n\t"
      "s_waitcnt vmcnt(0)\n\t"
      "s_sleep 1\n\t"
      "global_load_dwordx4 v[144:147], v180, s[52:53] sc1\n\t"
      "global_load_dwordx4 v[148:151], v181, s[52:53] sc1\n\t"
      "global_load_dwordx4 v[152:155], v182, s[52:53] sc1\n\t"
      "global_load_dwordx4 v[156:159], v183, s[52:53] sc1\n\t"
      "global_load_dwordx4 v[128:131], v176, s[54:55] sc1\n\t"
      "global_load_dwordx4 v[132:135], v177, s[54:55] sc1\n\t"
      "global_load_dwordx4 v[136:139], v178, s[54:55] sc1\n\t"
      "global_load_dwordx4 v[140:143], v179, s[54:55] sc1\n\t"
      "s_waitcnt vmcnt(7)\n\t"
      "v_max3_u32 v234, v144, v145, v146\n\t"
      "v_max_u32_e32 v234, v234, v147\n\t"
      "v_cmp_eq_u32_e32 vcc, -1, v234\n\t"
      "s_cbranch_vccnz FP4L_%=\n\t"
      "FP4X_%=:\n\t"
      "s_branch FP4B_%=\n\t"
      "FP5R_%=:\n\t"
      "s_mov_b32 s51, 0\n\t"
      "FP5L_%=:\n\t"
      "s_add_u32 s51, s51, 1\n\t"
      "s_cmp_lt_u32 s51, 262144\n\t"
      "s_cbranch_scc0 FP5X_%=\n\t"
      "s_waitcnt vmcnt(0)\n\t"
      "s_sleep 1\n\t"
      "global_load_dwordx4 v[148:151], v181, s[52:53] sc1\n\t"
      "global_load_dwordx4 v[152:155], v182, s[52:53] sc1\n\t"
      "global_load_dwordx4 v[156:159], v183, s[52:53] sc1\n\t"
      "global_load_dwordx4 v[128:131], v176, s[54:55] sc1\n\t"
      "global_load_dwordx4 v[132:135], v177, s[54:55] sc1\n\t"
      "global_load_dwordx4 v[136:139], v178, s[54:55] sc1\n\t"
      "global_load_dwordx4 v[140:143], v179, s[54:55] sc1\n\t"
      "global_load_dwordx4 v[144:147], v180, s[54:55] sc1\n\t"
      "s_waitcnt vmcnt(7)\n\t"
      "v_max3_u32 v234, v148, v149, v150\n\t"
      "v_max_u32_e32 v234, v234, v151\n\t"
      "v_cmp_eq_u32_e32 vcc, -1, v234\n\t"
      "s_cbranch_vccnz FP5L_%=\n\t"
      "FP5X_%=:\n\t"
      "s_branch FP5B_%=\n\t"
      "FP6R_%=:\n\t"
      "s_mov_b32 s51, 0\n\t"
      "FP6L_%=:\n\t"
      "s_add_u32 s51, s51, 1\n\t"
      "s_cmp_lt_u32 s51, 262144\n\t"
      "s_cbranch_scc0 FP6X_%=\n\t"
      "s_waitcnt vmcnt(0)\n\t"
      "s_sleep 1\n\t"
      "global_load_dwordx4 v[152:155], v182, s[52:53] sc1\n\t"
      "global_load_dwordx4 v[156:159], v183, s[52:53] sc1\n\t"
      "global_load_dwordx4 v[128:131], v176, s[54:55] sc1\n\t"
      "global_load_dwordx4 v[132:135], v177, s[54:55] sc1\n\t"
      "global_load_dwordx4 v[136:139], v178, s[54:55] sc1\n\t"
      "global_load_dwordx4 v[140:143], v179, s[54:55] sc1\n\t"
      "global_load_dwordx4 v[144:147], v180, s[54:55] sc1\n\t"
      "global_load_dwordx4 v[148:151], v181, s[54:55] sc1\n\t"
      "s_waitcnt vmcnt(7)\n\t"
      "v_max3_u32 v234, v152, v153, v154\n\t"
      "v_max_u32_e32 v234, v234, v155\n\t"
      "v_cmp_eq_u32_e32 vcc, -1, v234\n\t"
      "s_cbranch_vccnz FP6L_%=\n\t"
      "FP6X_%=:\n\t"
      "s_branch FP6B_%=\n\t"
      "FP7R_%=:\n\t"
      "s_mov_b32 s51, 0\n\t"
      "FP7L_%=:\n\t"
      "s_add_u32 s51, s51, 1\n\t"
      "s_cmp_lt_u32 s51, 262144\n\t"
      "s_cbranch_scc0 FP7X_%=\n\t"
      "s_waitcnt vmcnt(0)\n\t"
      "s_sleep 1\n\t"
      "global_load_dwordx4 v[156:159], v183, s[52:53] sc1\n\t"
      "global_load_dwordx4 v[128:131], v176, s[54:55] sc1\n\t"
      "global_load_dwordx4 v[132:135], v177, s[54:55] sc1\n\t"
      "global_load_dwordx4 v[136:139], v178, s[54:55] sc1\n\t"
      "global_load_dwordx4 v[140:143], v179, s[54:55] sc1\n\t"
      "global_load_dwordx4 v[144:147], v180, s[54:55] sc1\n\t"
      "global_load_dwordx4 v[148:151], v181, s[54:55] sc1\n\t"
      "global_load_dwordx4 v[152:155], v182, s[54:55] sc1\n\t"
      "s_waitcnt vmcnt(7)\n\t"
      "v_max3_u32 v234, v156, v157, v158\n\t"
      "v_max_u32_e32 v234, v234, v159\n\t"
      "v_cmp_eq_u32_e32 vcc, -1, v234\n\t"
      "s_cbranch_vccnz FP7L_%=\n\t"
      "FP7X_%=:\n\t"
      "s_branch FP7B_%=\n\t"
      "NP1R_%=:\n\t"
      "s_mov_b32 s51, 0\n\t"
      "NP1L_%=:\n\t"
      "s_add_u32 s51, s51, 1\n\t"
      "s_cmp_lt_u32 s51, 262144\n\t"
      "s_cbranch_scc0 NP1X_%=\n\t"
      "s_waitcnt vmcnt(0)\n\t"
      "s_sleep 1\n\t"
      "global_load_dwordx4 v[128:131], v176, s[54:55] sc1\n\t"
      "global_load_dwordx4 v[132:135], v177, s[54:55] sc1\n\t"
      "global_load_dwordx4 v[136:139], v178, s[54:55] sc1\n\t"
      "global_load_dwordx4 v[140:143], v179, s[54:55] sc1\n\t"
      "global_load_dwordx4 v[144:147], v180, s[54:55] sc1\n\t"
      "global_load_dwordx4 v[148:151], v181, s[54:55] sc1\n\t"
      "global_load_dwordx4 v[152:155], v182, s[54:55] sc1\n\t"
      "global_load_dwordx4 v[156:159], v183, s[54:55] sc1\n\t"
      "s_waitcnt vmcnt(0)\n\t"
      "v_max3_u32 v234, v128, v129, v130\n\t"
      "v_max3_u32 v234, v234, v131, v132\n\t"
      "v_max3_u32 v234, v234, v133, v134\n\t"
      "v_max3_u32 v234, v234, v135, v136\n\t"
      "v_max3_u32 v234, v234, v137, v138\n\t"
      "v_max3_u32 v234, v234, v139, v140\n\t"
      "v_max3_u32 v234, v234, v141, v142\n\t"
      "v_max3_u32 v234, v234, v143, v144\n\t"
      "v_max3_u32 v234, v234, v145, v146\n\t"
      "v_max3_u32 v234, v234, v147, v148\n\t"
      "v_max3_u32 v234, v234, v149, v150\n\t"
      "v_max3_u32 v234, v234, v151, v152\n\t"
      "v_max3_u32 v234, v234, v153, v154\n\t"
      "v_max3_u32 v234, v234, v155, v156\n\t"
      "v_max3_u32 v234, v234, v157, v158\n\t"
      "v_max_u32_e32 v234, v234, v159\n\t"
      "v_cmp_eq_u32_e32 vcc, -1, v234\n\t"
      "s_cbranch_vccnz NP1L_%=\n\t"
      "NP1X_%=:\n\t"
      "v_mfma_f32_16x16x32_bf16 v[200:203], v[0:3], v[128:131], 0\n\t"
      "v_mfma_f32_16x16x32_bf16 v[204:207], v[4:7], v[128:131], 0\n\t"
      "v_mfma_f32_16x16x32_bf16 v[208:211], v[8:11], v[128:131], 0\n\t"
      "v_mfma_f32_16x16x32_bf16 v[212:215], v[12:15], v[128:131], 0\n\t"
      "v_mfma_f32_16x16x32_bf16 v[200:203], v[16:19], v[132:135], v[200:203]\n\t"
      "v_mfma_f32_16x16x32_bf16 v[204:207], v[20:23], v[132:135], v[204:207]\n\t"
      "v_mfma_f32_16x16x32_bf16 v[208:211], v[24:27], v[132:135], v[208:211]\n\t"
      "v_mfma_f32_16x16x32_bf16 v[212:215], v[28:31], v[132:135], v[212:215]\n\t"
      "v_mfma_f32_16x16x32_bf16 v[200:203], v[32:35], v[136:139], v[200:203]\n\t"
      "v_mfma_f32_16x16x32_bf16 v[204:207], v[36:39], v[136:139], v[204:207]\n\t"
      "v_mfma_f32_16x16x32_bf16 v[208:211], v[40:43], v[136:139], v[208:211]\n\t"
      "v_mfma_f32_16x16x32_bf16 v[212:215], v[44:47], v[136:139], v[212:215]\n\t"
      "v_mfma_f32_16x16x32_bf16 v[200:203], v[48:51], v[140:143], v[200:203]\n\t"
      "v_mfma_f32_16x16x32_bf16 v[204:207], v[52:55], v[140:143], v[204:207]\n\t"
      "v_mfma_f32_16x16x32_bf16 v[208:211], v[56:59], v[140:143], v[208:211]\n\t"
      "v_mfma_f32_16x16x32_bf16 v[212:215], v[60:63], v[140:143], v[212:215]\n\t"
      "v_mfma_f32_16x16x32_bf16 v[200:203], v[64:67], v[144:147], v[200:203]\n\t"
      "v_mfma_f32_16x16x32_bf16 v[204:207], v[68:71], v[144:147], v[204:207]\n\t"
      "v_mfma_f32_16x16x32_bf16 v[208:211], v[72:75], v[144:147], v[208:211]\n\t"
      "v_mfma_f32_16x16x32_bf16 v[212:215], v[76:79], v[144:147], v[212:215]\n\t"
      "v_mfma_f32_16x16x32_bf16 v[200:203], v[80:83], v[148:151], v[200:203]\n\t"
      "v_mfma_f32_16x16x32_bf16 v[204:207], v[84:87], v[148:151], v[204:207]\n\t"
      "v_mfma_f32_16x16x32_bf16 v[208:211], v[88:91], v[148:151], v[208:211]\n\t"
      "v_mfma_f32_16x16x32_bf16 v[212:215], v[92:95], v[148:151], v[212:215]\n\t"
      "v_mfma_f32_16x16x32_bf16 v[200:203], v[96:99], v[152:155], v[200:203]\n\t"
      "v_mfma_f32_16x16x32_bf16 v[204:207], v[100:103], v[152:155], v[204:207]\n\t"
      "v_mfma_f32_16x16x32_bf16 v[208:211], v[104:107], v[152:155], v[208:211]\n\t"
      "v_mfma_f32_16x16x32_bf16 v[212:215], v[108:111], v[152:155], v[212:215]\n\t"
      "v_mfma_f32_16x16x32_bf16 v[200:203], v[112:115], v[156:159], v[200:203]\n\t"
      "v_mfma_f32_16x16x32_bf16 v[204:207], v[116:119], v[156:159], v[204:207]\n\t"
      "v_mfma_f32_16x16x32_bf16 v[208:211], v[120:123], v[156:159], v[208:211]\n\t"
      "v_mfma_f32_16x16x32_bf16 v[212:215], v[124:127], v[156:159], v[212:215]\n\t"
      "global_load_dwordx4 v[128:131], v176, s[56:57] sc1\n\t"
      "global_load_dwordx4 v[132:135], v177, s[56:57] sc1\n\t"
      "global_load_dwordx4 v[136:139], v178, s[56:57] sc1\n\t"
      "global_load_dwordx4 v[140:143], v179, s[56:57] sc1\n\t"
      "global_load_dwordx4 v[144:147], v180, s[56:57] sc1\n\t"
      "global_load_dwordx4 v[148:151], v181, s[56:57] sc1\n\t"
      "global_load_dwordx4 v[152:155], v182, s[56:57] sc1\n\t"
      "global_load_dwordx4 v[156:159], v183, s[56:57] sc1\n\t"
      "s_waitcnt vmcnt(0)\n\t"
      "s_nop 7\n\t"
      "s_branch NP1B_%=\n\t"
      "NP2R_%=:\n\t"
      "s_mov_b32 s51, 0\n\t"
      "NP2L_%=:\n\t"
      "s_add_u32 s51, s51, 1\n\t"
      "s_cmp_lt_u32 s51, 262144\n\t"
      "s_cbranch_scc0 NP2X_%=\n\t"
      "s_waitcnt vmcnt(0)\n\t"
      "s_sleep 1\n\t"
      "global_load_dwordx4 v[128:131], v176, s[56:57] sc1\n\t"
      "global_load_dwordx4 v[132:135], v177, s[56:57] sc1\n\t"
      "global_load_dwordx4 v[136:139], v178, s[56:57] sc1\n\t"
      "global_load_dwordx4 v[140:143], v179, s[56:57] sc1\n\t"
      "global_load_dwordx4 v[144:147], v180, s[56:57] sc1\n\t"
      "global_load_dwordx4 v[148:151], v181, s[56:57] sc1\n\t"
      "global_load_dwordx4 v[152:155], v182, s[56:57] sc1\n\t"
      "global_load_dwordx4 v[156:159], v183, s[56:57] sc1\n\t"
      "s_waitcnt vmcnt(0)\n\t"
      "v_max3_u32 v234, v128, v129, v130\n\t"
      "v_max3_u32 v234, v234, v131, v132\n\t"
      "v_max3_u32 v234, v234, v133, v134\n\t"
      "v_max3_u32 v234, v234, v135, v136\n\t"
      "v_max3_u32 v234, v234, v137, v138\n\t"
      "v_max3_u32 v234, v234, v139, v140\n\t"
      "v_max3_u32 v234, v234, v141, v142\n\t"
      "v_max3_u32 v234, v234, v143, v144\n\t"
      "v_max3_u32 v234, v234, v145, v146\n\t"
      "v_max3_u32 v234, v234, v147, v148\n\t"
      "v_max3_u32 v234, v234, v149, v150\n\t"
      "v_max3_u32 v234, v234, v151, v152\n\t"
      "v_max3_u32 v234, v234, v153, v154\n\t"
      "v_max3_u32 v234, v234, v155, v156\n\t"
      "v_max3_u32 v234, v234, v157, v158\n\t"
      "v_max_u32_e32 v234, v234, v159\n\t"
      "v_cmp_eq_u32_e32 vcc, -1, v234\n\t"
      "s_cbranch_vccnz NP2L_%=\n\t"
      "NP2X_%=:\n\t"
      "v_mfma_f32_16x16x32_bf16 v[184:187], v[0:3], v[128:131], 0\n\t"
      "v_mfma_f32_16x16x32_bf16 v[188:191], v[4:7], v[128:131], 0\n\t"
      "v_mfma_f32_16x16x32_bf16 v[192:195], v[8:11], v[128:131], 0\n\t"
      "v_mfma_f32_16x16x32_bf16 v[196:199], v[12:15], v[128:131], 0\n\t"
      "v_mfma_f32_16x16x32_bf16 v[184:187], v[16:19], v[132:135], v[184:187]\n\t"
      "v_mfma_f32_16x16x32_bf16 v[188:191], v[20:23], v[132:135], v[188:191]\n\t"
      "v_mfma_f32_16x16x32_bf16 v[192:195], v[24:27], v[132:135], v[192:195]\n\t"
      "v_mfma_f32_16x16x32_bf16 v[196:199], v[28:31], v[132:135], v[196:199]\n\t"
      "v_mfma_f32_16x16x32_bf16 v[184:187], v[32:35], v[136:139], v[184:187]\n\t"
      "v_mfma_f32_16x16x32_bf16 v[188:191], v[36:39], v[136:139], v[188:191]\n\t"
      "v_mfma_f32_16x16x32_bf16 v[192:195], v[40:43], v[136:139], v[192:195]\n\t"
      "v_mfma_f32_16x16x32_bf16 v[196:199], v[44:47], v[136:139], v[196:199]\n\t"
      "v_mfma_f32_16x16x32_bf16 v[184:187], v[48:51], v[140:143], v[184:187]\n\t"
      "v_mfma_f32_16x16x32_bf16 v[188:191], v[52:55], v[140:143], v[188:191]\n\t"
      "v_mfma_f32_16x16x32_bf16 v[192:195], v[56:59], v[140:143], v[192:195]\n\t"
      "v_mfma_f32_16x16x32_bf16 v[196:199], v[60:63], v[140:143], v[196:199]\n\t"
      "v_mfma_f32_16x16x32_bf16 v[184:187], v[64:67], v[144:147], v[184:187]\n\t"
      "v_mfma_f32_16x16x32_bf16 v[188:191], v[68:71], v[144:147], v[188:191]\n\t"
      "v_mfma_f32_16x16x32_bf16 v[192:195], v[72:75], v[144:147], v[192:195]\n\t"
      "v_mfma_f32_16x16x32_bf16 v[196:199], v[76:79], v[144:147], v[196:199]\n\t"
      "v_mfma_f32_16x16x32_bf16 v[184:187], v[80:83], v[148:151], v[184:187]\n\t"
      "v_mfma_f32_16x16x32_bf16 v[188:191], v[84:87], v[148:151], v[188:191]\n\t"
      "v_mfma_f32_16x16x32_bf16 v[192:195], v[88:91], v[148:151], v[192:195]\n\t"
      "v_mfma_f32_16x16x32_bf16 v[196:199], v[92:95], v[148:151], v[196:199]\n\t"
      "v_mfma_f32_16x16x32_bf16 v[184:187], v[96:99], v[152:155], v[184:187]\n\t"
      "v_mfma_f32_16x16x32_bf16 v[188:191], v[100:103], v[152:155], v[188:191]\n\t"
      "v_mfma_f32_16x16x32_bf16 v[192:195], v[104:107], v[152:155], v[192:195]\n\t"
      "v_mfma_f32_16x16x32_bf16 v[196:199], v[108:111], v[152:155], v[196:199]\n\t"
      "v_mfma_f32_16x16x32_bf16 v[184:187], v[112:115], v[156:159], v[184:187]\n\t"
      "v_mfma_f32_16x16x32_bf16 v[188:191], v[116:119], v[156:159], v[188:191]\n\t"
      "v_mfma_f32_16x16x32_bf16 v[192:195], v[120:123], v[156:159], v[192:195]\n\t"
      "v_mfma_f32_16x16x32_bf16 v[196:199], v[124:127], v[156:159], v[196:199]\n\t"
      "global_load_dwordx4 v[128:131], v176, s[58:59] sc1\n\t"
      "global_load_dwordx4 v[132:135], v177, s[58:59] sc1\n\t"
      "global_load_dwordx4 v[136:139], v178, s[58:59] sc1\n\t"
      "global_load_dwordx4 v[140:143], v179, s[58:59] sc1\n\t"
      "global_load_dwordx4 v[144:147], v180, s[58:59] sc1\n\t"
      "global_load_dwordx4 v[148:151], v181, s[58:59] sc1\n\t"
      "global_load_dwordx4 v[152:155], v182, s[58:59] sc1\n\t"
      "global_load_dwordx4 v[156:159], v183, s[58:59] sc1\n\t"
      "s_waitcnt vmcnt(0)\n\t"
      "s_nop 7\n\t"
      "s_branch NP2B_%=\n\t"
      "NP3R_%=:\n\t"
      "s_mov_b32 s51, 0\n\t"
      "NP3L_%=:\n\t"
      "s_add_u32 s51, s51, 1\n\t"
      "s_cmp_lt_u32 s51, 262144\n\t"
      "s_cbranch_scc0 NP3X_%=\n\t"
      "s_waitcnt vmcnt(0)\n\t"
      "s_sleep 1\n\t"
      "global_load_dwordx4 v[128:131], v176, s[58:59] sc1\n\t"
      "global_load_dwordx4 v[132:135], v177, s[58:59] sc1\n\t"
      "global_load_dwordx4 v[136:139], v178, s[58:59] sc1\n\t"
      "global_load_dwordx4 v[140:143], v179, s[58:59] sc1\n\t"
      "global_load_dwordx4 v[144:147], v180, s[58:59] sc1\n\t"
      "global_load_dwordx4 v[148:151], v181, s[58:59] sc1\n\t"
      "global_load_dwordx4 v[152:155], v182, s[58:59] sc1\n\t"
      "global_load_dwordx4 v[156:159], v183, s[58:59] sc1\n\t"
      "s_waitcnt vmcnt(0)\n\t"
      "v_max3_u32 v234, v128, v129, v130\n\t"
      "v_max3_u32 v234, v234, v131, v132\n\t"
      "v_max3_u32 v234, v234, v133, v134\n\t"
      "v_max3_u32 v234, v234, v135, v136\n\t"
      "v_max3_u32 v234, v234, v137, v138\n\t"
      "v_max3_u32 v234, v234, v139, v140\n\t"
      "v_max3_u32 v234, v234, v141, v142\n\t"
      "v_max3_u32 v234, v234, v143, v144\n\t"
      "v_max3_u32 v234, v234, v145, v146\n\t"
      "v_max3_u32 v234, v234, v147, v148\n\t"
      "v_max3_u32 v234, v234, v149, v150\n\t"
      "v_max3_u32 v234, v234, v151, v152\n\t"
      "v_max3_u32 v234, v234, v153, v154\n\t"
      "v_max3_u32 v234, v234, v155, v156\n\t"
      "v_max3_u32 v234, v234, v157, v158\n\t"
      "v_max_u32_e32 v234, v234, v159\n\t"
      "v_cmp_eq_u32_e32 vcc, -1, v234\n\t"
      "s_cbranch_vccnz NP3L_%=\n\t"
      "NP3X_%=:\n\t"
      "v_mfma_f32_16x16x32_bf16 v[200:203], v[0:3], v[128:131], 0\n\t"
      "v_mfma_f32_16x16x32_bf16 v[204:207], v[4:7], v[128:131], 0\n\t"
      "v_mfma_f32_16x16x32_bf16 v[208:211], v[8:11], v[128:131], 0\n\t"
      "v_mfma_f32_16x16x32_bf16 v[212:215], v[12:15], v[128:131], 0\n\t"
      "v_mfma_f32_16x16x32_bf16 v[200:203], v[16:19], v[132:135], v[200:203]\n\t"
      "v_mfma_f32_16x16x32_bf16 v[204:207], v[20:23], v[132:135], v[204:207]\n\t"
      "v_mfma_f32_16x16x32_bf16 v[208:211], v[24:27], v[132:135], v[208:211]\n\t"
      "v_mfma_f32_16x16x32_bf16 v[212:215], v[28:31], v[132:135], v[212:215]\n\t"
      "v_mfma_f32_16x16x32_bf16 v[200:203], v[32:35], v[136:139], v[200:203]\n\t"
      "v_mfma_f32_16x16x32_bf16 v[204:207], v[36:39], v[136:139], v[204:207]\n\t"
      "v_mfma_f32_16x16x32_bf16 v[208:211], v[40:43], v[136:139], v[208:211]\n\t"
      "v_mfma_f32_16x16x32_bf16 v[212:215], v[44:47], v[136:139], v[212:215]\n\t"
      "v_mfma_f32_16x16x32_bf16 v[200:203], v[48:51], v[140:143], v[200:203]\n\t"
      "v_mfma_f32_16x16x32_bf16 v[204:207], v[52:55], v[140:143], v[204:207]\n\t"
      "v_mfma_f32_16x16x32_bf16 v[208:211], v[56:59], v[140:143], v[208:211]\n\t"
      "v_mfma_f32_16x16x32_bf16 v[212:215], v[60:63], v[140:143], v[212:215]\n\t"
      "v_mfma_f32_16x16x32_bf16 v[200:203], v[64:67], v[144:147], v[200:203]\n\t"
      "v_mfma_f32_16x16x32_bf16 v[204:207], v[68:71], v[144:147], v[204:207]\n\t"
      "v_mfma_f32_16x16x32_bf16 v[208:211], v[72:75], v[144:147], v[208:211]\n\t"
      "v_mfma_f32_16x16x32_bf16 v[212:215], v[76:79], v[144:147], v[212:215]\n\t"
      "v_mfma_f32_16x16x32_bf16 v[200:203], v[80:83], v[148:151], v[200:203]\n\t"
      "v_mfma_f32_16x16x32_bf16 v[204:207], v[84:87], v[148:151], v[204:207]\n\t"
      "v_mfma_f32_16x16x32_bf16 v[208:211], v[88:91], v[148:151], v[208:211]\n\t"
      "v_mfma_f32_16x16x32_bf16 v[212:215], v[92:95], v[148:151], v[212:215]\n\t"
      "v_mfma_f32_16x16x32_bf16 v[200:203], v[96:99], v[152:155], v[200:203]\n\t"
      "v_mfma_f32_16x16x32_bf16 v[204:207], v[100:103], v[152:155], v[204:207]\n\t"
      "v_mfma_f32_16x16x32_bf16 v[208:211], v[104:107], v[152:155], v[208:211]\n\t"
      "v_mfma_f32_16x16x32_bf16 v[212:215], v[108:111], v[152:155], v[212:215]\n\t"
      "v_mfma_f32_16x16x32_bf16 v[200:203], v[112:115], v[156:159], v[200:203]\n\t"
      "v_mfma_f32_16x16x32_bf16 v[204:207], v[116:119], v[156:159], v[204:207]\n\t"
      "v_mfma_f32_16x16x32_bf16 v[208:211], v[120:123], v[156:159], v[208:211]\n\t"
      "v_mfma_f32_16x16x32_bf16 v[212:215], v[124:127], v[156:159], v[212:215]\n\t"
      "global_load_dwordx4 v[128:131], v176, s[60:61] sc1\n\t"
      "global_load_dwordx4 v[132:135], v177, s[60:61] sc1\n\t"
      "global_load_dwordx4 v[136:139], v178, s[60:61] sc1\n\t"
      "global_load_dwordx4 v[140:143], v179, s[60:61] sc1\n\t"
      "global_load_dwordx4 v[144:147], v180, s[60:61] sc1\n\t"
      "global_load_dwordx4 v[148:151], v181, s[60:61] sc1\n\t"
      "global_load_dwordx4 v[152:155], v182, s[60:61] sc1\n\t"
      "global_load_dwordx4 v[156:159], v183, s[60:61] sc1\n\t"
      "s_waitcnt vmcnt(0)\n\t"
      "s_nop 7\n\t"
      "s_branch NP3B_%=\n\t"
      "NP4R_%=:\n\t"
      "s_mov_b32 s51, 0\n\t"
      "NP4L_%=:\n\t"
      "s_add_u32 s51, s51, 1\n\t"
      "s_cmp_lt_u32 s51, 262144\n\t"
      "s_cbranch_scc0 NP4X_%=\n\t"
      "s_waitcnt vmcnt(0)\n\t"
      "s_sleep 1\n\t"
      "global_load_dwordx4 v[128:131], v176, s[60:61] sc1\n\t"
      "global_load_dwordx4 v[132:135], v177, s[60:61] sc1\n\t"
      "global_load_dwordx4 v[136:139], v178, s[60:61] sc1\n\t"
      "global_load_dwordx4 v[140:143], v179, s[60:61] sc1\n\t"
      "global_load_dwordx4 v[144:147], v180, s[60:61] sc1\n\t"
      "global_load_dwordx4 v[148:151], v181, s[60:61] sc1\n\t"
      "global_load_dwordx4 v[152:155], v182, s[60:61] sc1\n\t"
      "global_load_dwordx4 v[156:159], v183, s[60:61] sc1\n\t"
      "s_waitcnt vmcnt(0)\n\t"
      "v_max3_u32 v234, v128, v129, v130\n\t"
      "v_max3_u32 v234, v234, v131, v132\n\t"
      "v_max3_u32 v234, v234, v133, v134\n\t"
      "v_max3_u32 v234, v234, v135, v136\n\t"
      "v_max3_u32 v234, v234, v137, v138\n\t"
      "v_max3_u32 v234, v234, v139, v140\n\t"
      "v_max3_u32 v234, v234, v141, v142\n\t"
      "v_max3_u32 v234, v234, v143, v144\n\t"
      "v_max3_u32 v234, v234, v145, v146\n\t"
      "v_max3_u32 v234, v234, v147, v148\n\t"
      "v_max3_u32 v234, v234, v149, v150\n\t"
      "v_max3_u32 v234, v234, v151, v152\n\t"
      "v_max3_u32 v234, v234, v153, v154\n\t"
      "v_max3_u32 v234, v234, v155, v156\n\t"
      "v_max3_u32 v234, v234, v157, v158\n\t"
      "v_max_u32_e32 v234, v234, v159\n\t"
      "v_cmp_eq_u32_e32 vcc, -1, v234\n\t"
      "s_cbranch_vccnz NP4L_%=\n\t"
      "NP4X_%=:\n\t"
      "v_mfma_f32_16x16x32_bf16 v[184:187], v[0:3], v[128:131], 0\n\t"
      "v_mfma_f32_16x16x32_bf16 v[188:191], v[4:7], v[128:131], 0\n\t"
      "v_mfma_f32_16x16x32_bf16 v[192:195], v[8:11], v[128:131], 0\n\t"
      "v_mfma_f32_16x16x32_bf16 v[196:199], v[12:15], v[128:131], 0\n\t"
      "v_mfma_f32_16x16x32_bf16 v[184:187], v[16:19], v[132:135], v[184:187]\n\t"
      "v_mfma_f32_16x16x32_bf16 v[188:191], v[20:23], v[132:135], v[188:191]\n\t"
      "v_mfma_f32_16x16x32_bf16 v[192:195], v[24:27], v[132:135], v[192:195]\n\t"
      "v_mfma_f32_16x16x32_bf16 v[196:199], v[28:31], v[132:135], v[196:199]\n\t"
      "v_mfma_f32_16x16x32_bf16 v[184:187], v[32:35], v[136:139], v[184:187]\n\t"
      "v_mfma_f32_16x16x32_bf16 v[188:191], v[36:39], v[136:139], v[188:191]\n\t"
      "v_mfma_f32_16x16x32_bf16 v[192:195], v[40:43], v[136:139], v[192:195]\n\t"
      "v_mfma_f32_16x16x32_bf16 v[196:199], v[44:47], v[136:139], v[196:199]\n\t"
      "v_mfma_f32_16x16x32_bf16 v[184:187], v[48:51], v[140:143], v[184:187]\n\t"
      "v_mfma_f32_16x16x32_bf16 v[188:191], v[52:55], v[140:143], v[188:191]\n\t"
      "v_mfma_f32_16x16x32_bf16 v[192:195], v[56:59], v[140:143], v[192:195]\n\t"
      "v_mfma_f32_16x16x32_bf16 v[196:199], v[60:63], v[140:143], v[196:199]\n\t"
      "v_mfma_f32_16x16x32_bf16 v[184:187], v[64:67], v[144:147], v[184:187]\n\t"
      "v_mfma_f32_16x16x32_bf16 v[188:191], v[68:71], v[144:147], v[188:191]\n\t"
      "v_mfma_f32_16x16x32_bf16 v[192:195], v[72:75], v[144:147], v[192:195]\n\t"
      "v_mfma_f32_16x16x32_bf16 v[196:199], v[76:79], v[144:147], v[196:199]\n\t"
      "v_mfma_f32_16x16x32_bf16 v[184:187], v[80:83], v[148:151], v[184:187]\n\t"
      "v_mfma_f32_16x16x32_bf16 v[188:191], v[84:87], v[148:151], v[188:191]\n\t"
      "v_mfma_f32_16x16x32_bf16 v[192:195], v[88:91], v[148:151], v[192:195]\n\t"
      "v_mfma_f32_16x16x32_bf16 v[196:199], v[92:95], v[148:151], v[196:199]\n\t"
      "v_mfma_f32_16x16x32_bf16 v[184:187], v[96:99], v[152:155], v[184:187]\n\t"
      "v_mfma_f32_16x16x32_bf16 v[188:191], v[100:103], v[152:155], v[188:191]\n\t"
      "v_mfma_f32_16x16x32_bf16 v[192:195], v[104:107], v[152:155], v[192:195]\n\t"
      "v_mfma_f32_16x16x32_bf16 v[196:199], v[108:111], v[152:155], v[196:199]\n\t"
      "v_mfma_f32_16x16x32_bf16 v[184:187], v[112:115], v[156:159], v[184:187]\n\t"
      "v_mfma_f32_16x16x32_bf16 v[188:191], v[116:119], v[156:159], v[188:191]\n\t"
      "v_mfma_f32_16x16x32_bf16 v[192:195], v[120:123], v[156:159], v[192:195]\n\t"
      "v_mfma_f32_16x16x32_bf16 v[196:199], v[124:127], v[156:159], v[196:199]\n\t"
      "global_load_dwordx4 v[128:131], v176, s[62:63] sc1\n\t"
      "global_load_dwordx4 v[132:135], v177, s[62:63] sc1\n\t"
      "global_load_dwordx4 v[136:139], v178, s[62:63] sc1\n\t"
      "global_load_dwordx4 v[140:143], v179, s[62:63] sc1\n\t"
      "global_load_dwordx4 v[144:147], v180, s[62:63] sc1\n\t"
      "global_load_dwordx4 v[148:151], v181, s[62:63] sc1\n\t"
      "global_load_dwordx4 v[152:155], v182, s[62:63] sc1\n\t"
      "global_load_dwordx4 v[156:159], v183, s[62:63] sc1\n\t"
      "s_waitcnt vmcnt(0)\n\t"
      "s_nop 7\n\t"
      "s_branch NP4B_%=\n\t"
      "NP5R_%=:\n\t"
      "s_mov_b32 s51, 0\n\t"
      "NP5L_%=:\n\t"
      "s_add_u32 s51, s51, 1\n\t"
      "s_cmp_lt_u32 s51, 262144\n\t"
      "s_cbranch_scc0 NP5X_%=\n\t"
      "s_waitcnt vmcnt(0)\n\t"
      "s_sleep 1\n\t"
      "global_load_dwordx4 v[128:131], v176, s[62:63] sc1\n\t"
      "global_load_dwordx4 v[132:135], v177, s[62:63] sc1\n\t"
      "global_load_dwordx4 v[136:139], v178, s[62:63] sc1\n\t"
      "global_load_dwordx4 v[140:143], v179, s[62:63] sc1\n\t"
      "global_load_dwordx4 v[144:147], v180, s[62:63] sc1\n\t"
      "global_load_dwordx4 v[148:151], v181, s[62:63] sc1\n\t"
      "global_load_dwordx4 v[152:155], v182, s[62:63] sc1\n\t"
      "global_load_dwordx4 v[156:159], v183, s[62:63] sc1\n\t"
      "s_waitcnt vmcnt(0)\n\t"
      "v_max3_u32 v234, v128, v129, v130\n\t"
      "v_max3_u32 v234, v234, v131, v132\n\t"
      "v_max3_u32 v234, v234, v133, v134\n\t"
      "v_max3_u32 v234, v234, v135, v136\n\t"
      "v_max3_u32 v234, v234, v137, v138\n\t"
      "v_max3_u32 v234, v234, v139, v140\n\t"
      "v_max3_u32 v234, v234, v141, v142\n\t"
      "v_max3_u32 v234, v234, v143, v144\n\t"
      "v_max3_u32 v234, v234, v145, v146\n\t"
      "v_max3_u32 v234, v234, v147, v148\n\t"
      "v_max3_u32 v234, v234, v149, v150\n\t"
      "v_max3_u32 v234, v234, v151, v152\n\t"
      "v_max3_u32 v234, v234, v153, v154\n\t"
      "v_max3_u32 v234, v234, v155, v156\n\t"
      "v_max3_u32 v234, v234, v157, v158\n\t"
      "v_max_u32_e32 v234, v234, v159\n\t"
      "v_cmp_eq_u32_e32 vcc, -1, v234\n\t"
      "s_cbranch_vccnz NP5L_%=\n\t"
      "NP5X_%=:\n\t"
      "v_mfma_f32_16x16x32_bf16 v[200:203], v[0:3], v[128:131], 0\n\t"
      "v_mfma_f32_16x16x32_bf16 v[204:207], v[4:7], v[128:131], 0\n\t"
      "v_mfma_f32_16x16x32_bf16 v[208:211], v[8:11], v[128:131], 0\n\t"
      "v_mfma_f32_16x16x32_bf16 v[212:215], v[12:15], v[128:131], 0\n\t"
      "v_mfma_f32_16x16x32_bf16 v[200:203], v[16:19], v[132:135], v[200:203]\n\t"
      "v_mfma_f32_16x16x32_bf16 v[204:207], v[20:23], v[132:135], v[204:207]\n\t"
      "v_mfma_f32_16x16x32_bf16 v[208:211], v[24:27], v[132:135], v[208:211]\n\t"
      "v_mfma_f32_16x16x32_bf16 v[212:215], v[28:31], v[132:135], v[212:215]\n\t"
      "v_mfma_f32_16x16x32_bf16 v[200:203], v[32:35], v[136:139], v[200:203]\n\t"
      "v_mfma_f32_16x16x32_bf16 v[204:207], v[36:39], v[136:139], v[204:207]\n\t"
      "v_mfma_f32_16x16x32_bf16 v[208:211], v[40:43], v[136:139], v[208:211]\n\t"
      "v_mfma_f32_16x16x32_bf16 v[212:215], v[44:47], v[136:139], v[212:215]\n\t"
      "v_mfma_f32_16x16x32_bf16 v[200:203], v[48:51], v[140:143], v[200:203]\n\t"
      "v_mfma_f32_16x16x32_bf16 v[204:207], v[52:55], v[140:143], v[204:207]\n\t"
      "v_mfma_f32_16x16x32_bf16 v[208:211], v[56:59], v[140:143], v[208:211]\n\t"
      "v_mfma_f32_16x16x32_bf16 v[212:215], v[60:63], v[140:143], v[212:215]\n\t"
      "v_mfma_f32_16x16x32_bf16 v[200:203], v[64:67], v[144:147], v[200:203]\n\t"
      "v_mfma_f32_16x16x32_bf16 v[204:207], v[68:71], v[144:147], v[204:207]\n\t"
      "v_mfma_f32_16x16x32_bf16 v[208:211], v[72:75], v[144:147], v[208:211]\n\t"
      "v_mfma_f32_16x16x32_bf16 v[212:215], v[76:79], v[144:147], v[212:215]\n\t"
      "v_mfma_f32_16x16x32_bf16 v[200:203], v[80:83], v[148:151], v[200:203]\n\t"
      "v_mfma_f32_16x16x32_bf16 v[204:207], v[84:87], v[148:151], v[204:207]\n\t"
      "v_mfma_f32_16x16x32_bf16 v[208:211], v[88:91], v[148:151], v[208:211]\n\t"
      "v_mfma_f32_16x16x32_bf16 v[212:215], v[92:95], v[148:151], v[212:215]\n\t"
      "v_mfma_f32_16x16x32_bf16 v[200:203], v[96:99], v[152:155], v[200:203]\n\t"
      "v_mfma_f32_16x16x32_bf16 v[204:207], v[100:103], v[152:155], v[204:207]\n\t"
      "v_mfma_f32_16x16x32_bf16 v[208:211], v[104:107], v[152:155], v[208:211]\n\t"
      "v_mfma_f32_16x16x32_bf16 v[212:215], v[108:111], v[152:155], v[212:215]\n\t"
      "v_mfma_f32_16x16x32_bf16 v[200:203], v[112:115], v[156:159], v[200:203]\n\t"
      "v_mfma_f32_16x16x32_bf16 v[204:207], v[116:119], v[156:159], v[204:207]\n\t"
      "v_mfma_f32_16x16x32_bf16 v[208:211], v[120:123], v[156:159], v[208:211]\n\t"
      "v_mfma_f32_16x16x32_bf16 v[212:215], v[124:127], v[156:159], v[212:215]\n\t"
      "global_load_dwordx4 v[128:131], v176, s[64:65] sc1\n\t"
      "global_load_dwordx4 v[132:135], v177, s[64:65] sc1\n\t"
      "global_load_dwordx4 v[136:139], v178, s[64:65] sc1\n\t"
      "global_load_dwordx4 v[140:143], v179, s[64:65] sc1\n\t"
      "global_load_dwordx4 v[144:147], v180, s[64:65] sc1\n\t"
      "global_load_dwordx4 v[148:151], v181, s[64:65] sc1\n\t"
      "global_load_dwordx4 v[152:155], v182, s[64:65] sc1\n\t"
      "global_load_dwordx4 v[156:159], v183, s[64:65] sc1\n\t"
      "s_waitcnt vmcnt(0)\n\t"
      "s_nop 7\n\t"
      "s_branch NP5B_%=\n\t"
      "NP6R_%=:\n\t"
      "s_mov_b32 s51, 0\n\t"
      "NP6L_%=:\n\t"
      "s_add_u32 s51, s51, 1\n\t"
      "s_cmp_lt_u32 s51, 262144\n\t"
      "s_cbranch_scc0 NP6X_%=\n\t"
      "s_waitcnt vmcnt(0)\n\t"
      "s_sleep 1\n\t"
      "global_load_dwordx4 v[128:131], v176, s[64:65] sc1\n\t"
      "global_load_dwordx4 v[132:135], v177, s[64:65] sc1\n\t"
      "global_load_dwordx4 v[136:139], v178, s[64:65] sc1\n\t"
      "global_load_dwordx4 v[140:143], v179, s[64:65] sc1\n\t"
      "global_load_dwordx4 v[144:147], v180, s[64:65] sc1\n\t"
      "global_load_dwordx4 v[148:151], v181, s[64:65] sc1\n\t"
      "global_load_dwordx4 v[152:155], v182, s[64:65] sc1\n\t"
      "global_load_dwordx4 v[156:159], v183, s[64:65] sc1\n\t"
      "s_waitcnt vmcnt(0)\n\t"
      "v_max3_u32 v234, v128, v129, v130\n\t"
      "v_max3_u32 v234, v234, v131, v132\n\t"
      "v_max3_u32 v234, v234, v133, v134\n\t"
      "v_max3_u32 v234, v234, v135, v136\n\t"
      "v_max3_u32 v234, v234, v137, v138\n\t"
      "v_max3_u32 v234, v234, v139, v140\n\t"
      "v_max3_u32 v234, v234, v141, v142\n\t"
      "v_max3_u32 v234, v234, v143, v144\n\t"
      "v_max3_u32 v234, v234, v145, v146\n\t"
      "v_max3_u32 v234, v234, v147, v148\n\t"
      "v_max3_u32 v234, v234, v149, v150\n\t"
      "v_max3_u32 v234, v234, v151, v152\n\t"
      "v_max3_u32 v234, v234, v153, v154\n\t"
      "v_max3_u32 v234, v234, v155, v156\n\t"
      "v_max3_u32 v234, v234, v157, v158\n\t"
      "v_max_u32_e32 v234, v234, v159\n\t"
      "v_cmp_eq_u32_e32 vcc, -1, v234\n\t"
      "s_cbranch_vccnz NP6L_%=\n\t"
      "NP6X_%=:\n\t"
      "v_mfma_f32_16x16x32_bf16 v[184:187], v[0:3], v[128:131], 0\n\t"
      "v_mfma_f32_16x16x32_bf16 v[188:191], v[4:7], v[128:131], 0\n\t"
      "v_mfma_f32_16x16x32_bf16 v[192:195], v[8:11], v[128:131], 0\n\t"
      "v_mfma_f32_16x16x32_bf16 v[196:199], v[12:15], v[128:131], 0\n\t"
      "v_mfma_f32_16x16x32_bf16 v[184:187], v[16:19], v[132:135], v[184:187]\n\t"
      "v_mfma_f32_16x16x32_bf16 v[188:191], v[20:23], v[132:135], v[188:191]\n\t"
      "v_mfma_f32_16x16x32_bf16 v[192:195], v[24:27], v[132:135], v[192:195]\n\t"
      "v_mfma_f32_16x16x32_bf16 v[196:199], v[28:31], v[132:135], v[196:199]\n\t"
      "v_mfma_f32_16x16x32_bf16 v[184:187], v[32:35], v[136:139], v[184:187]\n\t"
      "v_mfma_f32_16x16x32_bf16 v[188:191], v[36:39], v[136:139], v[188:191]\n\t"
      "v_mfma_f32_16x16x32_bf16 v[192:195], v[40:43], v[136:139], v[192:195]\n\t"
      "v_mfma_f32_16x16x32_bf16 v[196:199], v[44:47], v[136:139], v[196:199]\n\t"
      "v_mfma_f32_16x16x32_bf16 v[184:187], v[48:51], v[140:143], v[184:187]\n\t"
      "v_mfma_f32_16x16x32_bf16 v[188:191], v[52:55], v[140:143], v[188:191]\n\t"
      "v_mfma_f32_16x16x32_bf16 v[192:195], v[56:59], v[140:143], v[192:195]\n\t"
      "v_mfma_f32_16x16x32_bf16 v[196:199], v[60:63], v[140:143], v[196:199]\n\t"
      "v_mfma_f32_16x16x32_bf16 v[184:187], v[64:67], v[144:147], v[184:187]\n\t"
      "v_mfma_f32_16x16x32_bf16 v[188:191], v[68:71], v[144:147], v[188:191]\n\t"
      "v_mfma_f32_16x16x32_bf16 v[192:195], v[72:75], v[144:147], v[192:195]\n\t"
      "v_mfma_f32_16x16x32_bf16 v[196:199], v[76:79], v[144:147], v[196:199]\n\t"
      "v_mfma_f32_16x16x32_bf16 v[184:187], v[80:83], v[148:151], v[184:187]\n\t"
      "v_mfma_f32_16x16x32_bf16 v[188:191], v[84:87], v[148:151], v[188:191]\n\t"
      "v_mfma_f32_16x16x32_bf16 v[192:195], v[88:91], v[148:151], v[192:195]\n\t"
      "v_mfma_f32_16x16x32_bf16 v[196:199], v[92:95], v[148:151], v[196:199]\n\t"
      "v_mfma_f32_16x16x32_bf16 v[184:187], v[96:99], v[152:155], v[184:187]\n\t"
      "v_mfma_f32_16x16x32_bf16 v[188:191], v[100:103], v[152:155], v[188:191]\n\t"
      "v_mfma_f32_16x16x32_bf16 v[192:195], v[104:107], v[152:155], v[192:195]\n\t"
      "v_mfma_f32_16x16x32_bf16 v[196:199], v[108:111], v[152:155], v[196:199]\n\t"
      "v_mfma_f32_16x16x32_bf16 v[184:187], v[112:115], v[156:159], v[184:187]\n\t"
      "v_mfma_f32_16x16x32_bf16 v[188:191], v[116:119], v[156:159], v[188:191]\n\t"
      "v_mfma_f32_16x16x32_bf16 v[192:195], v[120:123], v[156:159], v[192:195]\n\t"
      "v_mfma_f32_16x16x32_bf16 v[196:199], v[124:127], v[156:159], v[196:199]\n\t"
      "s_waitcnt vmcnt(0)\n\t"
      "s_nop 7\n\t"
      "s_branch NP6B_%=\n\t"
      : "+{s[4:19]}"(s), "+{s[20:21]}"(kr), "+{s22}"(hint), "+{v[240:247]}"(v)
      :
      : "memory", "vcc", "scc",
        "v0", "v1", "v2", "v3", "v4", "v5", "v6", "v7", "v8", "v9", "v10", "v11", "v12", "v13", "v14", "v15", "v16",
        "v17", "v18", "v19", "v20", "v21", "v22", "v23", "v24", "v25", "v26", "v27", "v28", "v29", "v30", "v31", "v32",
        "v33", "v34", "v35", "v36", "v37", "v38", "v39", "v40", "v41", "v42", "v43", "v44", "v45", "v46", "v47", "v48",
        "v49", "v50", "v51", "v52", "v53", "v54", "v55", "v56", "v57", "v58", "v59", "v60", "v61", "v62", "v63", "v64",
        "v65", "v66", "v67", "v68", "v69", "v70", "v71", "v72", "v73", "v74", "v75", "v76", "v77", "v78", "v79", "v80",
        "v81", "v82", "v83", "v84", "v85", "v86", "v87", "v88", "v89", "v90", "v91", "v92", "v93", "v94", "v95", "v96",
        "v97", "v98", "v99", "v100", "v101", "v102", "v103", "v104", "v105", "v106", "v107", "v108", "v109", "v110",
        "v111", "v112", "v113", "v114", "v115", "v116", "v117", "v118", "v119", "v120", "v121", "v122", "v123", "v124",
        "v125", "v126", "v127", "v128", "v129", "v130", "v131", "v132", "v133", "v134", "v135", "v136", "v137", "v138",
        "v139", "v140", "v141", "v142", "v143", "v144", "v145", "v146", "v147", "v148", "v149", "v150", "v151", "v152",
        "v153", "v154", "v155", "v156", "v157", "v158", "v159", "v160", "v161", "v162", "v163", "v164", "v165", "v166",
        "v167", "v168", "v169", "v170", "v171", "v172", "v173", "v174", "v175", "v176", "v177", "v178", "v179", "v180",
        "v181", "v182", "v183", "v184", "v185", "v186", "v187", "v188", "v189", "v190", "v191", "v192", "v193", "v194",
        "v195", "v196", "v197", "v198", "v199", "v200", "v201", "v202", "v203", "v204", "v205", "v206", "v207", "v208",
        "v209", "v210", "v211", "v212", "v213", "v214", "v215", "v216", "v217", "v218", "v219", "v220", "v221", "v222",
        "v223", "v224", "v225", "v226", "v227", "v228", "v229", "v230", "v231", "v232", "v233", "v234", "v235", "v236",
        "v237", "v238", "v239", "v248", "v249", "v250", "v251", "v252", "v253", "s44", "s45", "s46", "s47", "s48",
        "s49", "s50", "s51", "s52", "s53", "s54", "s55", "s56", "s57", "s58", "s59", "s60", "s61", "s62", "s63", "s64",
        "s65", "s66", "s67", "s68", "s69", "s70"
    );
    __builtin_amdgcn_endpgm();
  } else {
    const int ib = idx - 16, t = 16 * (4 * side + ((xp + XO) & 3)) + ib;
    const int st = 8 * xp + (ib >> 1), dg = 2 * side + (ib & 1), blk0 = 7 * dg;
    const int rot = R4 == 0 ? 4 * (ib & 1) + 2 * side - (ib & side & 1) : R4 == 1 ? 3 + 2 * side : R4 == 2 ? 3 : (3 * (ib & 1) + (ib >> 1) + 2 * side) % 6, rn = (rot + W2E) % 7;
    const unsigned bits = UNI((wave >= 4 ? 1 : 0) | ((wave >= 4) != HP4 ? 2 : 0) | (wave == 0 ? 16 : 0) |
                              (t == 0 && wave == 0 ? 32 : 0) | (wave < 7 && t < 112 ? 64 : 0));
    u32x4 vo;
    unsigned vq;
    u64 par = ST_PW + xcd * 128;
    {
      LANE_BITS(threadIdx.x & 63);
      const int cb0 = wave < 4 ? 4 * wave : 4 + 3 * wave;
      const int k0 = (ib + 2 * xcd) % KROT * KMUL;
      u32x16 v, s;
      u32x8 s2;
      v[0] = (8 * (wave & 3) + lr) * 896 + (wave >> 2) * 128 + ((lc ^ swz_tr(8 * wave + lr)) << 4) + 4096;
#pragma unroll
      for (int b = 0; b < 4; ++b) {
        const int cb = min(cb0 + b, 27);
        v[1 + b] = g * B_UNIT + (cb >> 2) * 1024 + q4 * 128 + (((2 * (cb & 3) + (p4 >> 1)) ^ swz_tr(8 * g + q4)) << 4) + (p4 & 1) * 8;
        const int by = 112 * wave + 56 * (b >> 1) + 32 * (b & 1) + 8 * g - 64 * k0;
        vo[b] = B_X + c * B_OP + (by < 0 ? by + 896 : by);
      }
      v[5] = B_X + (4 * g + q4) * 896 + cb0 * 32 + p4 * 8;
      v[6] = B_X + c * B_OP + 16 * g;
      v[7] = unit(7 * (wave & 3) + (wave >> 2) * 2, t >> 4, 0) + (g & 1) * 131072 + (t & 15) * 512 + (g >> 1) * 256 + c * 16;
      v[13] = v[7] + 4 * 131072;
      v[8] = B_PS + (4 * g + q4) * 64 + wave * 8;
      v[9] = B_PS + (4 * g + p4) * 64 + q4 * 16;
      v[10] = (t * 16 + 4 * g + p4) * 16;
      v[11] = lane * 16 + ((2 * (t >> 2) + 1) % 7 ? 1024 : 16384 - 6144);
      v[12] = lane * 16;
      v[14] = (wave * 224 + 96 + 64 * (side ^ (lane >> 5)) + ib * 4 + (lr & 3)) * 4096 + lc * 128 + xp * 1024;
      v[15] = ST_H3 + part((lc & 3) * 16384 + t * 128, lc >> 2);
      put(s, 0, (u64)w2);
      put(s, 2, (u64)(wo + k0 * 32 * 896 - 4096));
      put(s, 4, (u64)(ws + WS_ST));
      put(s, 6, (u64)(ws + WS_O + part(((t & 63) * 16 + 2 * wave) * 1024, side)));
      put(s, 10, (u64)(ws + WS_HN));
      put(s, 12, (u64)(ws + WS_G + unit(7 * (t & 3), 2 * (t >> 2) / 7, 0) + wave * 131072 + 2 * (t >> 2) % 7 * 1024));
      put(s, 14, (u64)(w2 + st * 128 + (wave * 224 + rot * 32) * 4096));
      unsigned vn = lr * 4096 + ((lc ^ swz_tr(lr)) << 4), lw = wave * 4096;
      vq = ST_H2 + part((((((t >> 3) & 7) * 8 + wave) * 2 + ((t >> 2) & 1)) * 2) * 64, side) + (lane & 1) * 64;
      unsigned vck = ST_CK + part((((t >> 2) & 15) * 16 + 2 * wave) * 256, side) + ((g >> 1) * 4 + (t & 3)) * 64 + c * 4;
      s2[0] = bits;
      s2[1] = (wave & 3) * B_UNIT + (wave >> 2) * 1024;
      s2[2] = UNI(wave < 4 ? ~0u : 0u);
      s2[3] = 13 - k0;
      put2(s2, 4, wave < 4 ? ~0u : 0xffffu, wave < 4 ? ~0u : 0xffffu);
    asm volatile(
      "s_waitcnt lgkmcnt(0)\n\ts_nop 4\n\t"
      "s_load_dwordx2 s[26:27], s[8:9], 0x20000\n\t"
      "v_mov_b32_e32 v248, -1\n\t"
      "v_mov_b32_e32 v249, -1\n\t"
      "v_mov_b32_e32 v250, -1\n\t"
      "v_mov_b32_e32 v251, -1\n\t"
      "v_add_u32_e32 v221, 4294965504, v224\n\t"
      "v_add_u32_e32 v222, 512, v224\n\t"
      "v_add_u32_e32 v223, 4294966016, v224\n\t"
      "s_bitcmp1_b32 s20, 0\n\t"
      "s_cselect_b64 s[46:47], -1, 0\n\t"
      "s_mov_b64 vcc, s[46:47]\n\t"
      "s_mov_b32 s44, 0xffffffff\n\t"
      "s_mov_b32 s45, 0xffff\n\t"
      "s_waitcnt vmcnt(3)\n\t"
      "s_barrier\n\t"
      "ds_read_b64_tr_b16 v[204:205], v229\n\t"
      "ds_read_b64_tr_b16 v[206:207], v229 offset:32\n\t"
      "ds_read_b64_tr_b16 v[208:209], v229 offset:64\n\t"
      "ds_read_b64_tr_b16 v[210:211], v229 offset:96\n\t"
      "s_waitcnt lgkmcnt(0)\n\t"
      "s_add_u32 s26, s26, 1\n\t"
      "s_addc_u32 s27, s27, 0\n\t"
      "s_mul_i32 s48, s26, 0x9e3779b9\n\t"
      "s_mul_hi_u32 s13, s26, 0x7f4a7c15\n\t"
      "s_add_u32 s13, s13, s48\n\t"
      "s_mul_i32 s48, s27, 0x7f4a7c15\n\t"
      "s_add_u32 s13, s13, s48\n\t"
      "s_mul_i32 s12, s26, 0x7f4a7c15\n\t"
      "s_add_u32 s12, s12, 0xd192ed03\n\t"
      "s_addc_u32 s13, s13, 0xd1b54a32\n\t"
      "v_mov_b32_e32 v188, s12\n\t"
      "v_mov_b32_e32 v189, s13\n\t"
      "v_mov_b32_e32 v190, s26\n\t"
      "v_mov_b32_e32 v191, s27\n\t"
      "v_mov_b32_e32 v192, 163600\n\t"
      "s_mov_b64 exec, 1\n\t"
      "ds_write_b128 v192, v[188:191]\n\t"
      "s_mov_b64 exec, -1\n\t"
      "v_and_b32_e32 v210, s22, v210\n\t"
      "v_and_b32_e32 v211, s22, v211\n\t"
      "v_lshlrev_b32_e32 v160, 16, v204\n\t"
      "v_and_b32_e32 v161, 0xffff0000, v204\n\t"
      "v_lshlrev_b32_e32 v162, 16, v205\n\t"
      "v_and_b32_e32 v163, 0xffff0000, v205\n\t"
      "v_lshlrev_b32_e32 v164, 16, v206\n\t"
      "v_and_b32_e32 v165, 0xffff0000, v206\n\t"
      "v_lshlrev_b32_e32 v166, 16, v207\n\t"
      "v_and_b32_e32 v167, 0xffff0000, v207\n\t"
      "v_lshlrev_b32_e32 v168, 16, v208\n\t"
      "v_and_b32_e32 v169, 0xffff0000, v208\n\t"
      "v_lshlrev_b32_e32 v170, 16, v209\n\t"
      "v_and_b32_e32 v171, 0xffff0000, v209\n\t"
      "v_lshlrev_b32_e32 v172, 16, v210\n\t"
      "v_and_b32_e32 v173, 0xffff0000, v210\n\t"
      "v_lshlrev_b32_e32 v174, 16, v211\n\t"
      "v_and_b32_e32 v175, 0xffff0000, v211\n\t"
      "v_pk_add_f32 v[188:189], v[160:161], v[164:165]\n\t"
      "v_pk_mul_f32 v[192:193], v[160:161], v[160:161]\n\t"
      "v_pk_add_f32 v[190:191], v[162:163], v[166:167]\n\t"
      "v_pk_mul_f32 v[194:195], v[162:163], v[162:163]\n\t"
      "v_pk_fma_f32 v[192:193], v[164:165], v[164:165], v[192:193]\n\t"
      "v_pk_fma_f32 v[194:195], v[166:167], v[166:167], v[194:195]\n\t"
      "v_pk_add_f32 v[188:189], v[188:189], v[168:169]\n\t"
      "v_pk_fma_f32 v[192:193], v[168:169], v[168:169], v[192:193]\n\t"
      "v_pk_add_f32 v[190:191], v[190:191], v[170:171]\n\t"
      "v_pk_fma_f32 v[194:195], v[170:171], v[170:171], v[194:195]\n\t"
      "v_pk_add_f32 v[188:189], v[188:189], v[172:173]\n\t"
      "v_pk_fma_f32 v[192:193], v[172:173], v[172:173], v[192:193]\n\t"
      "v_pk_add_f32 v[190:191], v[190:191], v[174:175]\n\t"
      "v_pk_fma_f32 v[194:195], v[174:175], v[174:175], v[194:195]\n\t"
      "v_add_f32_dpp v196, v188, v188 row_ror:8 row_mask:0xf bank_mask:0x3\n\t"
      "s_nop 1\n\t"
      "v_add_f32_dpp v196, v190, v190 row_ror:8 row_mask:0xf bank_mask:0xc\n\t"
      "v_add_f32_dpp v197, v189, v189 row_ror:8 row_mask:0xf bank_mask:0x3\n\t"
      "s_nop 1\n\t"
      "v_add_f32_dpp v197, v191, v191 row_ror:8 row_mask:0xf bank_mask:0xc\n\t"
      "v_add_f32_dpp v198, v192, v192 row_ror:8 row_mask:0xf bank_mask:0x3\n\t"
      "s_nop 1\n\t"
      "v_add_f32_dpp v198, v194, v194 row_ror:8 row_mask:0xf bank_mask:0xc\n\t"
      "v_add_f32_dpp v199, v193, v193 row_ror:8 row_mask:0xf bank_mask:0x3\n\t"
      "s_nop 1\n\t"
      "v_add_f32_dpp v199, v195, v195 row_ror:8 row_mask:0xf bank_mask:0xc\n\t"
      "v_add_f32_dpp v188, v196, v196 row_shl:4 row_mask:0xf bank_mask:0x5\n\t"
      "s_nop 1\n\t"
      "v_add_f32_dpp v188, v197, v197 row_shr:4 row_mask:0xf bank_mask:0xa\n\t"
      "v_add_f32_dpp v189, v198, v198 row_shl:4 row_mask:0xf bank_mask:0x5\n\t"
      "s_nop 1\n\t"
      "v_add_f32_dpp v189, v199, v199 row_shr:4 row_mask:0xf bank_mask:0xa\n\t"
      "v_add_f32_dpp v188, v188, v188 quad_perm:[1,0,3,2] row_mask:0xf bank_mask:0xf\n\t"
      "s_nop 0\n\t"
      "v_add_f32_dpp v189, v189, v189 quad_perm:[1,0,3,2] row_mask:0xf bank_mask:0xf\n\t"
      "v_add_f32_dpp v188, v188, v188 quad_perm:[2,3,0,1] row_mask:0xf bank_mask:0xf\n\t"
      "s_nop 0\n\t"
      "v_add_f32_dpp v189, v189, v189 quad_perm:[2,3,0,1] row_mask:0xf bank_mask:0xf\n\t"
      "ds_write_b64 v232, v[188:189]\n\t"
      "s_add_u32 m0, s21, 0\n\t"
      "s_nop 0\n\t"
      "global_load_lds_dwordx4 v224, s[6:7]\n\t"
      "global_load_lds_dwordx4 v221, s[6:7] offset:2048\n\t"
      "s_add_u32 m0, s21, 4096\n\t"
      "s_nop 0\n\t"
      "global_load_lds_dwordx4 v222, s[6:7]\n\t"
      "s_cbranch_vccnz WR0_%=\n\t"
      "global_load_lds_dwordx4 v223, s[6:7] offset:2048\n\t"
      "WR0_%=:\n\t"
      "s_sub_u32 s23, s23, 1\n\t"
      "s_cselect_b32 s48, -13, 1\n\t"
      "s_cselect_b32 s49, -1, 0\n\t"
      "s_mul_i32 s48, s48, 28672\n\t"
      "s_add_u32 s6, s6, s48\n\t"
      "s_addc_u32 s7, s7, s49\n\t"
      "s_add_u32 m0, s21, 28672\n\t"
      "s_nop 0\n\t"
      "global_load_lds_dwordx4 v224, s[6:7]\n\t"
      "global_load_lds_dwordx4 v221, s[6:7] offset:2048\n\t"
      "s_add_u32 m0, s21, 32768\n\t"
      "s_nop 0\n\t"
      "global_load_lds_dwordx4 v222, s[6:7]\n\t"
      "s_cbranch_vccnz WR1_%=\n\t"
      "global_load_lds_dwordx4 v223, s[6:7] offset:2048\n\t"
      "WR1_%=:\n\t"
      "s_sub_u32 s23, s23, 1\n\t"
      "s_cselect_b32 s48, -13, 1\n\t"
      "s_cselect_b32 s49, -1, 0\n\t"
      "s_mul_i32 s48, s48, 28672\n\t"
      "s_add_u32 s6, s6, s48\n\t"
      "s_addc_u32 s7, s7, s49\n\t"
      "s_waitcnt lgkmcnt(0)\n\t"
      "s_barrier\n\t"
      "ds_read_b128 v[188:191], v233\n\t"
      "s_waitcnt lgkmcnt(0)\n\t"
      "v_pk_add_f32 v[188:189], v[188:189], v[190:191]\n\t"
      "s_nop 1\n\t"
      "v_add_f32_dpp v188, v188, v188 row_ror:4 row_mask:0xf bank_mask:0xf\n\t"
      "v_add_f32_dpp v189, v189, v189 row_ror:4 row_mask:0xf bank_mask:0xf\n\t"
      "s_nop 0\n\t"
      "v_add_f32_dpp v188, v188, v188 row_ror:8 row_mask:0xf bank_mask:0xf\n\t"
      "v_add_f32_dpp v189, v189, v189 row_ror:8 row_mask:0xf bank_mask:0xf\n\t"
      "v_mul_f32_e32 v188, 0x3b124925, v188\n\t"
      "v_mul_f32_e32 v189, 0x3b124925, v189\n\t"
      "v_fma_f32 v189, -v188, v188, v189\n\t"
      "v_max_f32_e32 v189, 0, v189\n\t"
      "s_bitcmp1_b32 s20, 4\n\t"
      "s_cbranch_scc0 GW_%=\n\t"
      "v_add_f32_e32 v189, 0x3727c5ac, v189\n\t"
      "v_rsq_f32_e32 v189, v189\n\t"
      "v_xor_b32_e32 v188, 0x80000000, v188\n\t"
      "v_xor_b32_e32 v190, s12, v188\n\t"
      "v_xor_b32_e32 v191, s13, v189\n\t"
      "s_mov_b32 s48, 0xf000f\n\t"
      "s_mov_b32 s49, 0xf000f\n\t"
      "s_mov_b64 exec, s[48:49]\n\t"
      "global_store_dwordx4 v234, v[188:191], s[8:9] sc1\n\t"
      "s_mov_b64 exec, -1\n\t"
      "GW_%=:\n\t"
      "v_xor_b32_e32 v176, 64, v245\n\t"
      "v_add_u32_e32 v177, 63488, v245\n\t"
      "v_add_u32_e32 v178, 95232, v176\n\t"
      "v_add_u32_e32 v176, 31744, v176\n\t"
      "s_add_u32 m0, s30, 130560\n\t"
      "s_nop 0\n\t"
      "global_load_lds_dwordx4 v245, s[18:19]\n\t"
      "global_load_lds_dwordx4 v176, s[18:19] offset:1024\n\t"
      "global_load_lds_dwordx4 v177, s[18:19] offset:2048\n\t"
      "global_load_lds_dwordx4 v178, s[18:19] offset:3072\n\t"
      "s_add_u32 s18, s18, 131072\n\t"
      "s_addc_u32 s19, s19, 0\n\t"
      "s_cbranch_vccnz SN3_%=\n\t"
      "global_store_dwordx4 v231, v[248:251], s[14:15] sc1\n\t"
      "SN3_%=:\n\t"
      "s_cbranch_vccnz WW0_%=\n\t"
      "s_waitcnt vmcnt(8)\n\t"
      "s_branch WX0_%=\n\t"
      "WW0_%=:\n\t"
      "s_waitcnt vmcnt(7)\n\t"
      "WX0_%=:\n\t"
      "s_waitcnt lgkmcnt(0)\n\t"
      "s_barrier\n\t"
      "ds_read_b64_tr_b16 v[0:1], v225\n\t"
      "ds_read_b64_tr_b16 v[2:3], v225 offset:512\n\t"
      "ds_read_b64_tr_b16 v[4:5], v226\n\t"
      "ds_read_b64_tr_b16 v[6:7], v226 offset:512\n\t"
      "ds_read_b64_tr_b16 v[8:9], v227\n\t"
      "ds_read_b64_tr_b16 v[10:11], v227 offset:512\n\t"
      "ds_read_b64_tr_b16 v[12:13], v228\n\t"
      "ds_read_b64_tr_b16 v[14:15], v228 offset:512\n\t"
      "s_add_u32 m0, s21, 57344\n\t"
      "s_nop 0\n\t"
      "global_load_lds_dwordx4 v224, s[6:7]\n\t"
      "global_load_lds_dwordx4 v221, s[6:7] offset:2048\n\t"
      "s_add_u32 m0, s21, 61440\n\t"
      "s_nop 0\n\t"
      "global_load_lds_dwordx4 v222, s[6:7]\n\t"
      "s_cbranch_vccnz WR2_%=\n\t"
      "global_load_lds_dwordx4 v223, s[6:7] offset:2048\n\t"
      "WR2_%=:\n\t"
      "s_sub_u32 s23, s23, 1\n\t"
      "s_cselect_b32 s48, -13, 1\n\t"
      "s_cselect_b32 s49, -1, 0\n\t"
      "s_mul_i32 s48, s48, 28672\n\t"
      "s_add_u32 s6, s6, s48\n\t"
      "s_addc_u32 s7, s7, s49\n\t"
      "s_cbranch_vccnz WW1_%=\n\t"
      "s_waitcnt vmcnt(8)\n\t"
      "s_branch WX1_%=\n\t"
      "WW1_%=:\n\t"
      "s_waitcnt vmcnt(7)\n\t"
      "WX1_%=:\n\t"
      "s_waitcnt lgkmcnt(0)\n\t"
      "s_barrier\n\t"
      "ds_read_b64_tr_b16 v[16:17], v225 offset:28672\n\t"
      "ds_read_b64_tr_b16 v[18:19], v225 offset:29184\n\t"
      "ds_read_b64_tr_b16 v[20:21], v226 offset:28672\n\t"
      "ds_read_b64_tr_b16 v[22:23], v226 offset:29184\n\t"
      "ds_read_b64_tr_b16 v[24:25], v227 offset:28672\n\t"
      "ds_read_b64_tr_b16 v[26:27], v227 offset:29184\n\t"
      "ds_read_b64_tr_b16 v[28:29], v228 offset:28672\n\t"
      "ds_read_b64_tr_b16 v[30:31], v228 offset:29184\n\t"
      "s_add_u32 m0, s21, 86016\n\t"
      "s_nop 0\n\t"
      "global_load_lds_dwordx4 v224, s[6:7]\n\t"
      "global_load_lds_dwordx4 v221, s[6:7] offset:2048\n\t"
      "s_add_u32 m0, s21, 90112\n\t"
      "s_nop 0\n\t"
      "global_load_lds_dwordx4 v222, s[6:7]\n\t"
      "s_cbranch_vccnz WR3_%=\n\t"
      "global_load_lds_dwordx4 v223, s[6:7] offset:2048\n\t"
      "WR3_%=:\n\t"
      "s_sub_u32 s23, s23, 1\n\t"
      "s_cselect_b32 s48, -13, 1\n\t"
      "s_cselect_b32 s49, -1, 0\n\t"
      "s_mul_i32 s48, s48, 28672\n\t"
      "s_add_u32 s6, s6, s48\n\t"
      "s_addc_u32 s7, s7, s49\n\t"
      "v_add_u32_e32 v225, 57344, v225\n\t"
      "v_add_u32_e32 v226, 57344, v226\n\t"
      "v_add_u32_e32 v227, 57344, v227\n\t"
      "v_add_u32_e32 v228, 57344, v228\n\t"
      "s_cbranch_vccnz WW2_%=\n\t"
      "s_waitcnt vmcnt(4)\n\t"
      "s_branch WX2_%=\n\t"
      "WW2_%=:\n\t"
      "s_waitcnt vmcnt(3)\n\t"
      "WX2_%=:\n\t"
      "s_waitcnt lgkmcnt(0)\n\t"
      "s_barrier\n\t"
      "ds_read_b64_tr_b16 v[32:33], v225\n\t"
      "ds_read_b64_tr_b16 v[34:35], v225 offset:512\n\t"
      "ds_read_b64_tr_b16 v[36:37], v226\n\t"
      "ds_read_b64_tr_b16 v[38:39], v226 offset:512\n\t"
      "ds_read_b64_tr_b16 v[40:41], v227\n\t"
      "ds_read_b64_tr_b16 v[42:43], v227 offset:512\n\t"
      "ds_read_b64_tr_b16 v[44:45], v228\n\t"
      "ds_read_b64_tr_b16 v[46:47], v228 offset:512\n\t"
      "s_add_u32 m0, s21, 0\n\t"
      "s_nop 0\n\t"
      "global_load_lds_dwordx4 v224, s[6:7]\n\t"
      "global_load_lds_dwordx4 v221, s[6:7] offset:2048\n\t"
      "s_add_u32 m0, s21, 4096\n\t"
      "s_nop 0\n\t"
      "global_load_lds_dwordx4 v222, s[6:7]\n\t"
      "s_cbranch_vccnz WR4_%=\n\t"
      "global_load_lds_dwordx4 v223, s[6:7] offset:2048\n\t"
      "WR4_%=:\n\t"
      "s_sub_u32 s23, s23, 1\n\t"
      "s_cselect_b32 s48, -13, 1\n\t"
      "s_cselect_b32 s49, -1, 0\n\t"
      "s_mul_i32 s48, s48, 28672\n\t"
      "s_add_u32 s6, s6, s48\n\t"
      "s_addc_u32 s7, s7, s49\n\t"
      "s_cbranch_vccnz WW3_%=\n\t"
      "s_waitcnt vmcnt(4)\n\t"
      "s_branch WX3_%=\n\t"
      "WW3_%=:\n\t"
      "s_waitcnt vmcnt(3)\n\t"
      "WX3_%=:\n\t"
      "s_waitcnt lgkmcnt(0)\n\t"
      "s_barrier\n\t"
      "ds_read_b64_tr_b16 v[48:49], v225 offset:28672\n\t"
      "ds_read_b64_tr_b16 v[50:51], v225 offset:29184\n\t"
      "ds_read_b64_tr_b16 v[52:53], v226 offset:28672\n\t"
      "ds_read_b64_tr_b16 v[54:55], v226 offset:29184\n\t"
      "ds_read_b64_tr_b16 v[56:57], v227 offset:28672\n\t"
      "ds_read_b64_tr_b16 v[58:59], v227 offset:29184\n\t"
      "ds_read_b64_tr_b16 v[60:61], v228 offset:28672\n\t"
      "ds_read_b64_tr_b16 v[62:63], v228 offset:29184\n\t"
      "s_add_u32 m0, s21, 28672\n\t"
      "s_nop 0\n\t"
      "global_load_lds_dwordx4 v224, s[6:7]\n\t"
      "global_load_lds_dwordx4 v221, s[6:7] offset:2048\n\t"
      "s_add_u32 m0, s21, 32768\n\t"
      "s_nop 0\n\t"
      "global_load_lds_dwordx4 v222, s[6:7]\n\t"
      "s_cbranch_vccnz WR5_%=\n\t"
      "global_load_lds_dwordx4 v223, s[6:7] offset:2048\n\t"
      "WR5_%=:\n\t"
      "s_sub_u32 s23, s23, 1\n\t"
      "s_cselect_b32 s48, -13, 1\n\t"
      "s_cselect_b32 s49, -1, 0\n\t"
      "s_mul_i32 s48, s48, 28672\n\t"
      "s_add_u32 s6, s6, s48\n\t"
      "s_addc_u32 s7, s7, s49\n\t"
      "v_add_u32_e32 v225, 4294909952, v225\n\t"
      "v_add_u32_e32 v226, 4294909952, v226\n\t"
      "v_add_u32_e32 v227, 4294909952, v227\n\t"
      "v_add_u32_e32 v228, 4294909952, v228\n\t"
      "s_cbranch_vccnz WW4_%=\n\t"
      "s_waitcnt vmcnt(4)\n\t"
      "s_branch WX4_%=\n\t"
      "WW4_%=:\n\t"
      "s_waitcnt vmcnt(3)\n\t"
      "WX4_%=:\n\t"
      "s_waitcnt lgkmcnt(0)\n\t"
      "s_barrier\n\t"
      "ds_read_b64_tr_b16 v[64:65], v225\n\t"
      "ds_read_b64_tr_b16 v[66:67], v225 offset:512\n\t"
      "ds_read_b64_tr_b16 v[68:69], v226\n\t"
      "ds_read_b64_tr_b16 v[70:71], v226 offset:512\n\t"
      "ds_read_b64_tr_b16 v[72:73], v227\n\t"
      "ds_read_b64_tr_b16 v[74:75], v227 offset:512\n\t"
      "ds_read_b64_tr_b16 v[76:77], v228\n\t"
      "ds_read_b64_tr_b16 v[78:79], v228 offset:512\n\t"
      "s_add_u32 m0, s21, 57344\n\t"
      "s_nop 0\n\t"
      "global_load_lds_dwordx4 v224, s[6:7]\n\t"
      "global_load_lds_dwordx4 v221, s[6:7] offset:2048\n\t"
      "s_add_u32 m0, s21, 61440\n\t"
      "s_nop 0\n\t"
      "global_load_lds_dwordx4 v222, s[6:7]\n\t"
      "s_cbranch_vccnz WR6_%=\n\t"
      "global_load_lds_dwordx4 v223, s[6:7] offset:2048\n\t"
      "WR6_%=:\n\t"
      "s_sub_u32 s23, s23, 1\n\t"
      "s_cselect_b32 s48, -13, 1\n\t"
      "s_cselect_b32 s49, -1, 0\n\t"
      "s_mul_i32 s48, s48, 28672\n\t"
      "s_add_u32 s6, s6, s48\n\t"
      "s_addc_u32 s7, s7, s49\n\t"
      "s_cbranch_vccnz WW5_%=\n\t"
      "s_waitcnt vmcnt(4)\n\t"
      "s_branch WX5_%=\n\t"
      "WW5_%=:\n\t"
      "s_waitcnt vmcnt(3)\n\t"
      "WX5_%=:\n\t"
      "s_waitcnt lgkmcnt(0)\n\t"
      "s_barrier\n\t"
      "ds_read_b64_tr_b16 v[80:81], v225 offset:28672\n\t"
      "ds_read_b64_tr_b16 v[82:83], v225 offset:29184\n\t"
      "ds_read_b64_tr_b16 v[84:85], v226 offset:28672\n\t"
      "ds_read_b64_tr_b16 v[86:87], v226 offset:29184\n\t"
      "ds_read_b64_tr_b16 v[88:89], v227 offset:28672\n\t"
      "ds_read_b64_tr_b16 v[90:91], v227 offset:29184\n\t"
      "ds_read_b64_tr_b16 v[92:93], v228 offset:28672\n\t"
      "ds_read_b64_tr_b16 v[94:95], v228 offset:29184\n\t"
      "s_add_u32 m0, s21, 86016\n\t"
      "s_nop 0\n\t"
      "global_load_lds_dwordx4 v224, s[6:7]\n\t"
      "global_load_lds_dwordx4 v221, s[6:7] offset:2048\n\t"
      "s_add_u32 m0, s21, 90112\n\t"
      "s_nop 0\n\t"
      "global_load_lds_dwordx4 v222, s[6:7]\n\t"
      "s_cbranch_vccnz WR7_%=\n\t"
      "global_load_lds_dwordx4 v223, s[6:7] offset:2048\n\t"
      "WR7_%=:\n\t"
      "s_sub_u32 s23, s23, 1\n\t"
      "s_cselect_b32 s48, -13, 1\n\t"
      "s_cselect_b32 s49, -1, 0\n\t"
      "s_mul_i32 s48, s48, 28672\n\t"
      "s_add_u32 s6, s6, s48\n\t"
      "s_addc_u32 s7, s7, s49\n\t"
      "v_add_u32_e32 v225, 57344, v225\n\t"
      "v_add_u32_e32 v226, 57344, v226\n\t"
      "v_add_u32_e32 v227, 57344, v227\n\t"
      "v_add_u32_e32 v228, 57344, v228\n\t"
      "s_cbranch_vccnz WW6_%=\n\t"
      "s_waitcnt vmcnt(4)\n\t"
      "s_branch WX6_%=\n\t"
      "WW6_%=:\n\t"
      "s_waitcnt vmcnt(3)\n\t"
      "WX6_%=:\n\t"
      "s_waitcnt lgkmcnt(0)\n\t"
      "s_barrier\n\t"
      "ds_read_b64_tr_b16 v[96:97], v225\n\t"
      "ds_read_b64_tr_b16 v[98:99], v225 offset:512\n\t"
      "ds_read_b64_tr_b16 v[100:101], v226\n\t"
      "ds_read_b64_tr_b16 v[102:103], v226 offset:512\n\t"
      "ds_read_b64_tr_b16 v[104:105], v227\n\t"
      "ds_read_b64_tr_b16 v[106:107], v227 offset:512\n\t"
      "ds_read_b64_tr_b16 v[108:109], v228\n\t"
      "ds_read_b64_tr_b16 v[110:111], v228 offset:512\n\t"
      "s_add_u32 m0, s21, 0\n\t"
      "s_nop 0\n\t"
      "global_load_lds_dwordx4 v224, s[6:7]\n\t"
      "global_load_lds_dwordx4 v221, s[6:7] offset:2048\n\t"
      "s_add_u32 m0, s21, 4096\n\t"
      "s_nop 0\n\t"
      "global_load_lds_dwordx4 v222, s[6:7]\n\t"
      "s_cbranch_vccnz WR8_%=\n\t"
      "global_load_lds_dwordx4 v223, s[6:7] offset:2048\n\t"
      "WR8_%=:\n\t"
      "s_sub_u32 s23, s23, 1\n\t"
      "s_cselect_b32 s48, -13, 1\n\t"
      "s_cselect_b32 s49, -1, 0\n\t"
      "s_mul_i32 s48, s48, 28672\n\t"
      "s_add_u32 s6, s6, s48\n\t"
      "s_addc_u32 s7, s7, s49\n\t"
      "s_cbranch_vccnz WW7_%=\n\t"
      "s_waitcnt vmcnt(4)\n\t"
      "s_branch WX7_%=\n\t"
      "WW7_%=:\n\t"
      "s_waitcnt vmcnt(3)\n\t"
      "WX7_%=:\n\t"
      "s_waitcnt lgkmcnt(0)\n\t"
      "s_barrier\n\t"
      "ds_read_b64_tr_b16 v[112:113], v225 offset:28672\n\t"
      "ds_read_b64_tr_b16 v[114:115], v225 offset:29184\n\t"
      "ds_read_b64_tr_b16 v[116:117], v226 offset:28672\n\t"
      "ds_read_b64_tr_b16 v[118:119], v226 offset:29184\n\t"
      "ds_read_b64_tr_b16 v[120:121], v227 offset:28672\n\t"
      "ds_read_b64_tr_b16 v[122:123], v227 offset:29184\n\t"
      "ds_read_b64_tr_b16 v[124:125], v228 offset:28672\n\t"
      "ds_read_b64_tr_b16 v[126:127], v228 offset:29184\n\t"
      "s_add_u32 m0, s21, 28672\n\t"
      "s_nop 0\n\t"
      "global_load_lds_dwordx4 v224, s[6:7]\n\t"
      "global_load_lds_dwordx4 v221, s[6:7] offset:2048\n\t"
      "s_add_u32 m0, s21, 32768\n\t"
      "s_nop 0\n\t"
      "global_load_lds_dwordx4 v222, s[6:7]\n\t"
      "s_cbranch_vccnz WR9_%=\n\t"
      "global_load_lds_dwordx4 v223, s[6:7] offset:2048\n\t"
      "WR9_%=:\n\t"
      "s_sub_u32 s23, s23, 1\n\t"
      "s_cselect_b32 s48, -13, 1\n\t"
      "s_cselect_b32 s49, -1, 0\n\t"
      "s_mul_i32 s48, s48, 28672\n\t"
      "s_add_u32 s6, s6, s48\n\t"
      "s_addc_u32 s7, s7, s49\n\t"
      "v_add_u32_e32 v225, 4294909952, v225\n\t"
      "v_add_u32_e32 v226, 4294909952, v226\n\t"
      "v_add_u32_e32 v227, 4294909952, v227\n\t"
      "v_add_u32_e32 v228, 4294909952, v228\n\t"
      "v_min_u32_e32 v184, 32, v236\n\t"
      "v_add_u32_e32 v184, s28, v184\n\t"
      "global_load_dwordx4 v[180:183], v184, s[8:9] sc1\n\t"
      "s_cbranch_vccnz WW8_%=\n\t"
      "s_waitcnt vmcnt(5)\n\t"
      "s_branch WX8_%=\n\t"
      "WW8_%=:\n\t"
      "s_waitcnt vmcnt(4)\n\t"
      "WX8_%=:\n\t"
      "s_waitcnt lgkmcnt(0)\n\t"
      "s_barrier\n\t"
      "ds_read_b64_tr_b16 v[128:129], v225\n\t"
      "ds_read_b64_tr_b16 v[130:131], v225 offset:512\n\t"
      "ds_read_b64_tr_b16 v[132:133], v226\n\t"
      "ds_read_b64_tr_b16 v[134:135], v226 offset:512\n\t"
      "ds_read_b64_tr_b16 v[136:137], v227\n\t"
      "ds_read_b64_tr_b16 v[138:139], v227 offset:512\n\t"
      "ds_read_b64_tr_b16 v[140:141], v228\n\t"
      "ds_read_b64_tr_b16 v[142:143], v228 offset:512\n\t"
      "s_add_u32 m0, s21, 57344\n\t"
      "s_nop 0\n\t"
      "global_load_lds_dwordx4 v224, s[6:7]\n\t"
      "global_load_lds_dwordx4 v221, s[6:7] offset:2048\n\t"
      "s_add_u32 m0, s21, 61440\n\t"
      "s_nop 0\n\t"
      "global_load_lds_dwordx4 v222, s[6:7]\n\t"
      "s_cbranch_vccnz WR10_%=\n\t"
      "global_load_lds_dwordx4 v223, s[6:7] offset:2048\n\t"
      "WR10_%=:\n\t"
      "s_sub_u32 s23, s23, 1\n\t"
      "s_cselect_b32 s48, -13, 1\n\t"
      "s_cselect_b32 s49, -1, 0\n\t"
      "s_mul_i32 s48, s48, 28672\n\t"
      "s_add_u32 s6, s6, s48\n\t"
      "s_addc_u32 s7, s7, s49\n\t"
      "s_cbranch_vccnz WW9_%=\n\t"
      "s_waitcnt vmcnt(5)\n\t"
      "s_branch WX9_%=\n\t"
      "WW9_%=:\n\t"
      "s_waitcnt vmcnt(4)\n\t"
      "WX9_%=:\n\t"
      "s_waitcnt lgkmcnt(0)\n\t"
      "s_barrier\n\t"
      "ds_read_b64_tr_b16 v[144:145], v225 offset:28672\n\t"
      "ds_read_b64_tr_b16 v[146:147], v225 offset:29184\n\t"
      "ds_read_b64_tr_b16 v[148:149], v226 offset:28672\n\t"
      "ds_read_b64_tr_b16 v[150:151], v226 offset:29184\n\t"
      "ds_read_b64_tr_b16 v[152:153], v227 offset:28672\n\t"
      "ds_read_b64_tr_b16 v[154:155], v227 offset:29184\n\t"
      "ds_read_b64_tr_b16 v[156:157], v228 offset:28672\n\t"
      "ds_read_b64_tr_b16 v[158:159], v228 offset:29184\n\t"
      "s_add_u32 m0, s21, 86016\n\t"
      "s_nop 0\n\t"
      "global_load_lds_dwordx4 v224, s[6:7]\n\t"
      "global_load_lds_dwordx4 v221, s[6:7] offset:2048\n\t"
      "s_add_u32 m0, s21, 90112\n\t"
      "s_nop 0\n\t"
      "global_load_lds_dwordx4 v222, s[6:7]\n\t"
      "s_cbranch_vccnz WR11_%=\n\t"
      "global_load_lds_dwordx4 v223, s[6:7] offset:2048\n\t"
      "WR11_%=:\n\t"
      "s_sub_u32 s23, s23, 1\n\t"
      "s_cselect_b32 s48, -13, 1\n\t"
      "s_cselect_b32 s49, -1, 0\n\t"
      "s_mul_i32 s48, s48, 28672\n\t"
      "s_add_u32 s6, s6, s48\n\t"
      "s_addc_u32 s7, s7, s49\n\t"
      "v_add_u32_e32 v225, 57344, v225\n\t"
      "v_add_u32_e32 v226, 57344, v226\n\t"
      "v_add_u32_e32 v227, 57344, v227\n\t"
      "v_add_u32_e32 v228, 57344, v228\n\t"
      "s_waitcnt lgkmcnt(0)\n\t"
      "s_barrier\n\t"
      "s_add_u32 m0, s21, 0\n\t"
      "s_nop 0\n\t"
      "global_load_lds_dwordx4 v224, s[6:7]\n\t"
      "global_load_lds_dwordx4 v221, s[6:7] offset:2048\n\t"
      "s_add_u32 m0, s21, 4096\n\t"
      "s_nop 0\n\t"
      "global_load_lds_dwordx4 v222, s[6:7]\n\t"
      "s_cbranch_vccnz WR12_%=\n\t"
      "global_load_lds_dwordx4 v223, s[6:7] offset:2048\n\t"
      "WR12_%=:\n\t"
      "s_sub_u32 s23, s23, 1\n\t"
      "s_cselect_b32 s48, -13, 1\n\t"
      "s_cselect_b32 s49, -1, 0\n\t"
      "s_mul_i32 s48, s48, 28672\n\t"
      "s_add_u32 s6, s6, s48\n\t"
      "s_addc_u32 s7, s7, s49\n\t"
      "s_add_u32 m0, s21, 28672\n\t"
      "s_nop 0\n\t"
      "global_load_lds_dwordx4 v224, s[6:7]\n\t"
      "global_load_lds_dwordx4 v221, s[6:7] offset:2048\n\t"
      "s_add_u32 m0, s21, 32768\n\t"
      "s_nop 0\n\t"
      "global_load_lds_dwordx4 v222, s[6:7]\n\t"
      "s_cbranch_vccnz WR13_%=\n\t"
      "global_load_lds_dwordx4 v223, s[6:7] offset:2048\n\t"
      "WR13_%=:\n\t"
      "s_waitcnt vmcnt(12)\n\t"
      "v_xor_b32_e32 v182, v182, v180\n\t"
      "v_xor_b32_e32 v183, v183, v181\n\t"
      "v_cmp_ne_u64_e32 vcc, s[12:13], v[182:183]\n\t"
      "s_cbranch_vccnz PWR_%=\n\t"
      "PWB_%=:\n\t"
      "v_readlane_b32 s48, v180, 0\n\t"
      "v_readlane_b32 s28, v180, 1\n\t"
      "v_readlane_b32 s29, v180, 2\n\t"
      "v_mov_b32_e32 v220, s12\n\t"
      "v_mov_b32_e32 v221, s13\n\t"
      "s_xor_b32 s10, s10, s48\n\t"
      "v_xor_b32_e32 v231, s28, v231\n\t"
      "v_xor_b32_e32 v237, s28, v237\n\t"
      "v_xor_b32_e32 v246, s28, v246\n\t"
      "v_xor_b32_e32 v247, s28, v247\n\t"
      "v_xor_b32_e32 v239, s29, v239\n\t"
      "s_waitcnt vmcnt(0)\n\t"
      "s_mov_b32 s51, 0\n\t"
      "s_branch QE_%=\n\t"
      ".p2align 6\n\t"
      "QL_%=:\n\t"
      "s_sleep 2\n\t"
      "s_add_u32 s51, s51, 1\n\t"
      "s_cmp_lt_u32 s51, 262144\n\t"
      "s_cbranch_scc0 QX_%=\n\t"
      "QE_%=:\n\t"
      "s_mov_b64 exec, 3\n\t"
      "global_load_dwordx2 v[188:189], v246, s[8:9] sc1\n\t"
      "s_waitcnt vmcnt(0)\n\t"
      "v_cmp_ne_u64_e32 vcc, s[12:13], v[188:189]\n\t"
      "s_mov_b64 exec, -1\n\t"
      "s_cbranch_vccnz QL_%=\n\t"
      "QX_%=:\n\t"
      "s_mov_b32 s51, 0\n\t"
      "s_branch OE_%=\n\t"
      "OL_%=:\n\t"
      "s_sleep 2\n\t"
      "s_add_u32 s51, s51, 1\n\t"
      "s_cmp_lt_u32 s51, 262144\n\t"
      "s_cbranch_scc0 OX_%=\n\t"
      "OE_%=:\n\t"
      "global_load_dwordx4 v[188:191], v236, s[10:11] sc1\n\t"
      "global_load_dwordx4 v[192:195], v236, s[10:11] offset:1024 sc1\n\t"
      "global_load_dword v198, v247, s[8:9] sc1\n\t"
      "s_waitcnt vmcnt(2)\n\t"
      "v_add3_u32 v196, v188, v189, v190\n\t"
      "v_add_u32_e32 v196, v196, v191\n\t"
      "s_waitcnt vmcnt(1)\n\t"
      "v_add3_u32 v197, v192, v193, v194\n\t"
      "v_add_u32_e32 v197, v197, v195\n\t"
      "s_nop 1\n\t"
      "v_permlane32_swap_b32_e32 v196, v197\n\t"
      "v_add_u32_e32 v196, v196, v197\n\t"
      "v_mov_b32_e32 v197, v196\n\t"
      "s_nop 1\n\t"
      "v_permlane16_swap_b32_e32 v196, v197\n\t"
      "v_add3_u32 v196, v196, v197, s12\n\t"
      "s_waitcnt vmcnt(0)\n\t"
      "v_cmp_ne_u32_e32 vcc, v196, v198\n\t"
      "s_cbranch_vccnz OL_%=\n\t"
      "OX_%=:\n\t"
      "s_mov_b64 vcc, s[46:47]\n\t"
      "ds_write_b64 v240, v[188:189]\n\t"
      "ds_write_b64 v242, v[192:193]\n\t"
      "s_mov_b64 exec, s[44:45]\n\t"
      "ds_write_b64 v241, v[190:191]\n\t"
      "ds_write_b64 v243, v[194:195]\n\t"
      "s_mov_b64 exec, -1\n\t"
      "s_waitcnt lgkmcnt(0)\n\t"
      "s_barrier\n\t"
      "ds_read_b128 v[176:179], v230\n\t"
      "ds_read_b128 v[180:183], v230 offset:64\n\t"
      "ds_read_b128 v[184:187], v230 offset:128\n\t"
      "s_waitcnt lgkmcnt(2)\n\t"
      "v_mfma_f32_16x16x32_bf16 v[160:163], v[176:179], v[0:3], v[160:163]\n\t"
      "v_mfma_f32_16x16x32_bf16 v[164:167], v[176:179], v[4:7], v[164:167]\n\t"
      "v_mfma_f32_16x16x32_bf16 v[168:171], v[176:179], v[8:11], v[168:171]\n\t"
      "s_cbranch_vccnz M0_%=\n\t"
      "v_mfma_f32_16x16x32_bf16 v[172:175], v[176:179], v[12:15], v[172:175]\n\t"
      "M0_%=:\n\t"
      "s_cbranch_vccz SN4_%=\n\t"
      "global_store_dwordx4 v231, v[248:251], s[14:15] sc1\n\t"
      "SN4_%=:\n\t"
      "ds_read_b128 v[176:179], v230 offset:192\n\t"
      "s_waitcnt lgkmcnt(2)\n\t"
      "v_mfma_f32_16x16x32_bf16 v[160:163], v[180:183], v[16:19], v[160:163]\n\t"
      "v_mfma_f32_16x16x32_bf16 v[164:167], v[180:183], v[20:23], v[164:167]\n\t"
      "v_mfma_f32_16x16x32_bf16 v[168:171], v[180:183], v[24:27], v[168:171]\n\t"
      "s_cbranch_vccnz M1_%=\n\t"
      "v_mfma_f32_16x16x32_bf16 v[172:175], v[180:183], v[28:31], v[172:175]\n\t"
      "M1_%=:\n\t"
      "s_mov_b64 exec, s[24:25]\n\t"
      "global_store_dwordx4 v237, v[248:251], s[14:15] sc1\n\t"
      "s_mov_b64 exec, -1\n\t"
      "ds_read_b128 v[180:183], v230 offset:256\n\t"
      "s_waitcnt lgkmcnt(2)\n\t"
      "v_mfma_f32_16x16x32_bf16 v[160:163], v[184:187], v[32:35], v[160:163]\n\t"
      "v_mfma_f32_16x16x32_bf16 v[164:167], v[184:187], v[36:39], v[164:167]\n\t"
      "v_mfma_f32_16x16x32_bf16 v[168:171], v[184:187], v[40:43], v[168:171]\n\t"
      "s_cbranch_vccnz M2_%=\n\t"
      "v_mfma_f32_16x16x32_bf16 v[172:175], v[184:187], v[44:47], v[172:175]\n\t"
      "M2_%=:\n\t"
      "ds_read_b64_tr_b16 v[188:189], v225\n\t"
      "ds_read_b64_tr_b16 v[190:191], v225 offset:512\n\t"
      "s_bitcmp1_b32 s20, 6\n\t"
      "s_cbranch_scc0 SG2_%=\n\t"
      "global_store_dwordx4 v236, v[248:251], s[16:17] sc1\n\t"
      "global_store_dwordx4 v235, v[248:251], s[16:17] sc1\n\t"
      "SG2_%=:\n\t"
      "ds_read_b128 v[184:187], v230 offset:320\n\t"
      "s_waitcnt lgkmcnt(4)\n\t"
      "v_mfma_f32_16x16x32_bf16 v[160:163], v[176:179], v[48:51], v[160:163]\n\t"
      "v_mfma_f32_16x16x32_bf16 v[164:167], v[176:179], v[52:55], v[164:167]\n\t"
      "v_mfma_f32_16x16x32_bf16 v[168:171], v[176:179], v[56:59], v[168:171]\n\t"
      "s_cbranch_vccnz M3_%=\n\t"
      "v_mfma_f32_16x16x32_bf16 v[172:175], v[176:179], v[60:63], v[172:175]\n\t"
      "M3_%=:\n\t"
      "ds_read_b64_tr_b16 v[192:193], v226\n\t"
      "ds_read_b64_tr_b16 v[194:195], v226 offset:512\n\t"
      "ds_read_b128 v[176:179], v230 offset:384\n\t"
      "s_waitcnt lgkmcnt(6)\n\t"
      "v_mfma_f32_16x16x32_bf16 v[160:163], v[180:183], v[64:67], v[160:163]\n\t"
      "v_mfma_f32_16x16x32_bf16 v[164:167], v[180:183], v[68:71], v[164:167]\n\t"
      "v_mfma_f32_16x16x32_bf16 v[168:171], v[180:183], v[72:75], v[168:171]\n\t"
      "s_cbranch_vccnz M4_%=\n\t"
      "v_mfma_f32_16x16x32_bf16 v[172:175], v[180:183], v[76:79], v[172:175]\n\t"
      "M4_%=:\n\t"
      "ds_read_b64_tr_b16 v[196:197], v227\n\t"
      "ds_read_b64_tr_b16 v[198:199], v227 offset:512\n\t"
      "ds_read_b128 v[180:183], v230 offset:448\n\t"
      "s_waitcnt lgkmcnt(6)\n\t"
      "v_mfma_f32_16x16x32_bf16 v[160:163], v[184:187], v[80:83], v[160:163]\n\t"
      "v_mfma_f32_16x16x32_bf16 v[164:167], v[184:187], v[84:87], v[164:167]\n\t"
      "v_mfma_f32_16x16x32_bf16 v[168:171], v[184:187], v[88:91], v[168:171]\n\t"
      "s_cbranch_vccnz M5_%=\n\t"
      "v_mfma_f32_16x16x32_bf16 v[172:175], v[184:187], v[92:95], v[172:175]\n\t"
      "M5_%=:\n\t"
      "ds_read_b64_tr_b16 v[200:201], v228\n\t"
      "ds_read_b64_tr_b16 v[202:203], v228 offset:512\n\t"
      "s_mov_b32 exec_lo, 0xffff\n\t"
      "s_mov_b32 exec_hi, 0x0\n\t"
      "s_mov_b32 m0, 163328\n\t"
      "s_nop 0\n\t"
      "global_load_lds_dword v238, s[4:5]\n\t"
      "s_mov_b64 exec, -1\n\t"
      "ds_read_b128 v[184:187], v230 offset:512\n\t"
      "s_waitcnt lgkmcnt(6)\n\t"
      "v_mfma_f32_16x16x32_bf16 v[160:163], v[176:179], v[96:99], v[160:163]\n\t"
      "v_mfma_f32_16x16x32_bf16 v[164:167], v[176:179], v[100:103], v[164:167]\n\t"
      "v_mfma_f32_16x16x32_bf16 v[168:171], v[176:179], v[104:107], v[168:171]\n\t"
      "s_cbranch_vccnz M6_%=\n\t"
      "v_mfma_f32_16x16x32_bf16 v[172:175], v[176:179], v[108:111], v[172:175]\n\t"
      "M6_%=:\n\t"
      "ds_read_b64_tr_b16 v[204:205], v225 offset:28672\n\t"
      "ds_read_b64_tr_b16 v[206:207], v225 offset:29184\n\t"
      "ds_read_b128 v[176:179], v230 offset:576\n\t"
      "s_waitcnt lgkmcnt(6)\n\t"
      "v_mfma_f32_16x16x32_bf16 v[160:163], v[180:183], v[112:115], v[160:163]\n\t"
      "v_mfma_f32_16x16x32_bf16 v[164:167], v[180:183], v[116:119], v[164:167]\n\t"
      "v_mfma_f32_16x16x32_bf16 v[168:171], v[180:183], v[120:123], v[168:171]\n\t"
      "s_cbranch_vccnz M7_%=\n\t"
      "v_mfma_f32_16x16x32_bf16 v[172:175], v[180:183], v[124:127], v[172:175]\n\t"
      "M7_%=:\n\t"
      "ds_read_b64_tr_b16 v[208:209], v226 offset:28672\n\t"
      "ds_read_b64_tr_b16 v[210:211], v226 offset:29184\n\t"
      "ds_read_b128 v[180:183], v230 offset:640\n\t"
      "s_waitcnt lgkmcnt(6)\n\t"
      "v_mfma_f32_16x16x32_bf16 v[160:163], v[184:187], v[128:131], v[160:163]\n\t"
      "v_mfma_f32_16x16x32_bf16 v[164:167], v[184:187], v[132:135], v[164:167]\n\t"
      "v_mfma_f32_16x16x32_bf16 v[168:171], v[184:187], v[136:139], v[168:171]\n\t"
      "s_cbranch_vccnz M8_%=\n\t"
      "v_mfma_f32_16x16x32_bf16 v[172:175], v[184:187], v[140:143], v[172:175]\n\t"
      "M8_%=:\n\t"
      "ds_read_b64_tr_b16 v[212:213], v227 offset:28672\n\t"
      "ds_read_b64_tr_b16 v[214:215], v227 offset:29184\n\t"
      "ds_read_b128 v[184:187], v230 offset:704\n\t"
      "s_waitcnt lgkmcnt(6)\n\t"
      "v_mfma_f32_16x16x32_bf16 v[160:163], v[176:179], v[144:147], v[160:163]\n\t"
      "v_mfma_f32_16x16x32_bf16 v[164:167], v[176:179], v[148:151], v[164:167]\n\t"
      "v_mfma_f32_16x16x32_bf16 v[168:171], v[176:179], v[152:155], v[168:171]\n\t"
      "s_cbranch_vccnz M9_%=\n\t"
      "v_mfma_f32_16x16x32_bf16 v[172:175], v[176:179], v[156:159], v[172:175]\n\t"
      "M9_%=:\n\t"
      "ds_read_b64_tr_b16 v[216:217], v228 offset:28672\n\t"
      "ds_read_b64_tr_b16 v[218:219], v228 offset:29184\n\t"
      "ds_read_b128 v[176:179], v230 offset:768\n\t"
      "s_waitcnt lgkmcnt(6)\n\t"
      "v_mfma_f32_16x16x32_bf16 v[160:163], v[180:183], v[188:191], v[160:163]\n\t"
      "v_add_u32_e32 v225, 4294909952, v225\n\t"
      "v_add_u32_e32 v226, 4294909952, v226\n\t"
      "v_add_u32_e32 v227, 4294909952, v227\n\t"
      "v_add_u32_e32 v228, 4294909952, v228\n\t"
      "ds_read_b64_tr_b16 v[188:189], v225\n\t"
      "ds_read_b64_tr_b16 v[190:191], v225 offset:512\n\t"
      "v_mfma_f32_16x16x32_bf16 v[164:167], v[180:183], v[192:195], v[164:167]\n\t"
      "ds_read_b64_tr_b16 v[192:193], v226\n\t"
      "ds_read_b64_tr_b16 v[194:195], v226 offset:512\n\t"
      "v_mfma_f32_16x16x32_bf16 v[168:171], v[180:183], v[196:199], v[168:171]\n\t"
      "ds_read_b64_tr_b16 v[196:197], v227\n\t"
      "ds_read_b64_tr_b16 v[198:199], v227 offset:512\n\t"
      "s_cbranch_vccnz M10_%=\n\t"
      "v_mfma_f32_16x16x32_bf16 v[172:175], v[180:183], v[200:203], v[172:175]\n\t"
      "M10_%=:\n\t"
      "ds_read_b64_tr_b16 v[200:201], v228\n\t"
      "ds_read_b64_tr_b16 v[202:203], v228 offset:512\n\t"
      "ds_read_b128 v[180:183], v230 offset:832\n\t"
      "s_waitcnt lgkmcnt(12)\n\t"
      "v_mfma_f32_16x16x32_bf16 v[160:163], v[184:187], v[204:207], v[160:163]\n\t"
      "ds_read_b64_tr_b16 v[204:205], v225 offset:28672\n\t"
      "ds_read_b64_tr_b16 v[206:207], v225 offset:29184\n\t"
      "v_mfma_f32_16x16x32_bf16 v[164:167], v[184:187], v[208:211], v[164:167]\n\t"
      "ds_read_b64_tr_b16 v[208:209], v226 offset:28672\n\t"
      "ds_read_b64_tr_b16 v[210:211], v226 offset:29184\n\t"
      "v_mfma_f32_16x16x32_bf16 v[168:171], v[184:187], v[212:215], v[168:171]\n\t"
      "ds_read_b64_tr_b16 v[212:213], v227 offset:28672\n\t"
      "ds_read_b64_tr_b16 v[214:215], v227 offset:29184\n\t"
      "s_waitcnt lgkmcnt(14)\n\t"
      "s_cbranch_vccnz M11_%=\n\t"
      "v_mfma_f32_16x16x32_bf16 v[172:175], v[184:187], v[216:219], v[172:175]\n\t"
      "M11_%=:\n\t"
      "ds_read_b64_tr_b16 v[216:217], v228 offset:28672\n\t"
      "ds_read_b64_tr_b16 v[218:219], v228 offset:29184\n\t"
      "s_waitcnt lgkmcnt(14)\n\t"
      "s_waitcnt lgkmcnt(14)\n\t"
      "v_mfma_f32_16x16x32_bf16 v[160:163], v[176:179], v[188:191], v[160:163]\n\t"
      "s_waitcnt lgkmcnt(13)\n\t"
      "v_mfma_f32_16x16x32_bf16 v[164:167], v[176:179], v[192:195], v[164:167]\n\t"
      "s_waitcnt lgkmcnt(11)\n\t"
      "v_mfma_f32_16x16x32_bf16 v[168:171], v[176:179], v[196:199], v[168:171]\n\t"
      "s_waitcnt lgkmcnt(9)\n\t"
      "s_cbranch_vccnz M12_%=\n\t"
      "v_mfma_f32_16x16x32_bf16 v[172:175], v[176:179], v[200:203], v[172:175]\n\t"
      "M12_%=:\n\t"
      "s_waitcnt lgkmcnt(8)\n\t"
      "s_waitcnt lgkmcnt(6)\n\t"
      "v_mfma_f32_16x16x32_bf16 v[160:163], v[180:183], v[204:207], v[160:163]\n\t"
      "s_waitcnt lgkmcnt(4)\n\t"
      "v_mfma_f32_16x16x32_bf16 v[164:167], v[180:183], v[208:211], v[164:167]\n\t"
      "s_waitcnt lgkmcnt(2)\n\t"
      "v_mfma_f32_16x16x32_bf16 v[168:171], v[180:183], v[212:215], v[168:171]\n\t"
      "s_waitcnt lgkmcnt(0)\n\t"
      "s_cbranch_vccnz M13_%=\n\t"
      "v_mfma_f32_16x16x32_bf16 v[172:175], v[180:183], v[216:219], v[172:175]\n\t"
      "M13_%=:\n\t"
      "s_nop 3\n\t"
      "v_pk_add_f32 v[188:189], v[160:161], v[164:165]\n\t"
      "v_pk_mul_f32 v[192:193], v[160:161], v[160:161]\n\t"
      "v_pk_add_f32 v[190:191], v[162:163], v[166:167]\n\t"
      "v_pk_mul_f32 v[194:195], v[162:163], v[162:163]\n\t"
      "v_pk_fma_f32 v[192:193], v[164:165], v[164:165], v[192:193]\n\t"
      "v_pk_fma_f32 v[194:195], v[166:167], v[166:167], v[194:195]\n\t"
      "v_pk_add_f32 v[188:189], v[188:189], v[168:169]\n\t"
      "v_pk_fma_f32 v[192:193], v[168:169], v[168:169], v[192:193]\n\t"
      "v_pk_add_f32 v[190:191], v[190:191], v[170:171]\n\t"
      "v_pk_fma_f32 v[194:195], v[170:171], v[170:171], v[194:195]\n\t"
      "v_pk_add_f32 v[188:189], v[188:189], v[172:173]\n\t"
      "v_pk_fma_f32 v[192:193], v[172:173], v[172:173], v[192:193]\n\t"
      "v_pk_add_f32 v[190:191], v[190:191], v[174:175]\n\t"
      "v_pk_fma_f32 v[194:195], v[174:175], v[174:175], v[194:195]\n\t"
      "v_add_f32_dpp v196, v188, v188 row_ror:8 row_mask:0xf bank_mask:0x3\n\t"
      "s_nop 1\n\t"
      "v_add_f32_dpp v196, v190, v190 row_ror:8 row_mask:0xf bank_mask:0xc\n\t"
      "v_add_f32_dpp v197, v189, v189 row_ror:8 row_mask:0xf bank_mask:0x3\n\t"
      "s_nop 1\n\t"
      "v_add_f32_dpp v197, v191, v191 row_ror:8 row_mask:0xf bank_mask:0xc\n\t"
      "v_add_f32_dpp v198, v192, v192 row_ror:8 row_mask:0xf bank_mask:0x3\n\t"
      "s_nop 1\n\t"
      "v_add_f32_dpp v198, v194, v194 row_ror:8 row_mask:0xf bank_mask:0xc\n\t"
      "v_add_f32_dpp v199, v193, v193 row_ror:8 row_mask:0xf bank_mask:0x3\n\t"
      "s_nop 1\n\t"
      "v_add_f32_dpp v199, v195, v195 row_ror:8 row_mask:0xf bank_mask:0xc\n\t"
      "v_add_f32_dpp v188, v196, v196 row_shl:4 row_mask:0xf bank_mask:0x5\n\t"
      "s_nop 1\n\t"
      "v_add_f32_dpp v188, v197, v197 row_shr:4 row_mask:0xf bank_mask:0xa\n\t"
      "v_add_f32_dpp v189, v198, v198 row_shl:4 row_mask:0xf bank_mask:0x5\n\t"
      "s_nop 1\n\t"
      "v_add_f32_dpp v189, v199, v199 row_shr:4 row_mask:0xf bank_mask:0xa\n\t"
      "v_add_f32_dpp v188, v188, v188 quad_perm:[1,0,3,2] row_mask:0xf bank_mask:0xf\n\t"
      "s_nop 0\n\t"
      "v_add_f32_dpp v189, v189, v189 quad_perm:[1,0,3,2] row_mask:0xf bank_mask:0xf\n\t"
      "v_add_f32_dpp v188, v188, v188 quad_perm:[2,3,0,1] row_mask:0xf bank_mask:0xf\n\t"
      "s_nop 0\n\t"
      "v_add_f32_dpp v189, v189, v189 quad_perm:[2,3,0,1] row_mask:0xf bank_mask:0xf\n\t"
      "ds_write_b64 v232, v[188:189]\n\t"
      "s_bitcmp1_b32 s20, 4\n\t"
      "s_cbranch_scc0 HI_%=\n\t"
      "s_mov_b64 exec, 255\n\t"
      "global_store_dwordx2 v239, v[220:221], s[8:9] sc1\n\t"
      "s_mov_b64 exec, -1\n\t"
      "HI_%=:\n\t"
      "s_waitcnt vmcnt(1)\n\t"
      "s_waitcnt lgkmcnt(0)\n\t"
      "s_barrier\n\t"
      "ds_read_b128 v[188:191], v233\n\t"
      "s_waitcnt lgkmcnt(0)\n\t"
      "v_pk_add_f32 v[188:189], v[188:189], v[190:191]\n\t"
      "s_nop 1\n\t"
      "v_add_f32_dpp v188, v188, v188 row_ror:4 row_mask:0xf bank_mask:0xf\n\t"
      "v_add_f32_dpp v189, v189, v189 row_ror:4 row_mask:0xf bank_mask:0xf\n\t"
      "s_nop 0\n\t"
      "v_add_f32_dpp v188, v188, v188 row_ror:8 row_mask:0xf bank_mask:0xf\n\t"
      "v_add_f32_dpp v189, v189, v189 row_ror:8 row_mask:0xf bank_mask:0xf\n\t"
      "v_mul_f32_e32 v188, 0x3b124925, v188\n\t"
      "v_mul_f32_e32 v189, 0x3b124925, v189\n\t"
      "v_fma_f32 v189, -v188, v188, v189\n\t"
      "v_max_f32_e32 v189, 0, v189\n\t"
      "v_add_f32_e32 v190, 0x3727c5ac, v189\n\t"
      "v_fmac_f32_e32 v189, 0x3727c5ac, v190\n\t"
      "v_rsq_f32_e32 v191, v189\n\t"
      "s_nop 0\n\t"
      "v_mul_f32_e64 v190, -v188, v191\n\t"
      "v_mov_b32_dpp v192, v191 quad_perm:[0,0,0,0] row_mask:0xf bank_mask:0xf\n\t"
      "s_nop 0\n\t"
      "v_mov_b32_dpp v196, v190 quad_perm:[0,0,0,0] row_mask:0xf bank_mask:0xf\n\t"
      "v_mov_b32_dpp v193, v191 quad_perm:[1,1,1,1] row_mask:0xf bank_mask:0xf\n\t"
      "v_mov_b32_dpp v197, v190 quad_perm:[1,1,1,1] row_mask:0xf bank_mask:0xf\n\t"
      "v_mov_b32_dpp v194, v191 quad_perm:[2,2,2,2] row_mask:0xf bank_mask:0xf\n\t"
      "v_mov_b32_dpp v198, v190 quad_perm:[2,2,2,2] row_mask:0xf bank_mask:0xf\n\t"
      "v_mov_b32_dpp v195, v191 quad_perm:[3,3,3,3] row_mask:0xf bank_mask:0xf\n\t"
      "v_mov_b32_dpp v199, v190 quad_perm:[3,3,3,3] row_mask:0xf bank_mask:0xf\n\t"
      "v_pk_fma_f32 v[160:161], v[160:161], v[192:193], v[196:197]\n\t"
      "v_pk_fma_f32 v[162:163], v[162:163], v[194:195], v[198:199]\n\t"
      "v_pk_fma_f32 v[164:165], v[164:165], v[192:193], v[196:197]\n\t"
      "v_pk_fma_f32 v[166:167], v[166:167], v[194:195], v[198:199]\n\t"
      "v_cvt_pk_bf16_f32 v212, v160, v161\n\t"
      "v_cvt_pk_bf16_f32 v213, v162, v163\n\t"
      "v_cvt_pk_bf16_f32 v214, v164, v165\n\t"
      "v_cvt_pk_bf16_f32 v215, v166, v167\n\t"
      "v_min_u32_e32 v212, -2, v212\n\t"
      "v_min_u32_e32 v213, -2, v213\n\t"
      "v_min_u32_e32 v214, -2, v214\n\t"
      "v_min_u32_e32 v215, -2, v215\n\t"
      "s_nop 0\n\t"
      "v_permlane16_swap_b32_e32 v212, v214\n\t"
      "v_permlane16_swap_b32_e32 v213, v215\n\t"
      "global_store_dwordx4 v231, v[212:215], s[14:15] sc1\n\t"
      "v_pk_fma_f32 v[168:169], v[168:169], v[192:193], v[196:197]\n\t"
      "v_pk_fma_f32 v[170:171], v[170:171], v[194:195], v[198:199]\n\t"
      "v_pk_fma_f32 v[172:173], v[172:173], v[192:193], v[196:197]\n\t"
      "v_pk_fma_f32 v[174:175], v[174:175], v[194:195], v[198:199]\n\t"
      "v_cvt_pk_bf16_f32 v216, v168, v169\n\t"
      "v_cvt_pk_bf16_f32 v217, v170, v171\n\t"
      "v_cvt_pk_bf16_f32 v218, v172, v173\n\t"
      "v_cvt_pk_bf16_f32 v219, v174, v175\n\t"
      "v_min_u32_e32 v216, -2, v216\n\t"
      "v_min_u32_e32 v217, -2, v217\n\t"
      "v_min_u32_e32 v218, -2, v218\n\t"
      "v_min_u32_e32 v219, -2, v219\n\t"
      "s_nop 0\n\t"
      "v_permlane16_swap_b32_e32 v216, v218\n\t"
      "v_permlane16_swap_b32_e32 v217, v219\n\t"
      "s_mov_b64 exec, s[24:25]\n\t"
      "global_store_dwordx4 v237, v[216:219], s[14:15] sc1\n\t"
      "s_mov_b64 exec, -1\n\t"
      "s_nop 1\n\t"
      "v_mov_b32_e32 v240, v188\n\t"
      "v_mul_f32_e32 v241, v189, v191\n\t"
      "v_add_u32_e32 v246, 65536, v234\n\t"
      "v_xor_b32_e32 v242, s12, v188\n\t"
      "v_xor_b32_e32 v243, s13, v241\n\t"
      "s_nop 1\n\t"
      "s_branch BFX_%=\n\t"
      "PWR_%=:\n\t"
      "s_mov_b32 s51, 0\n\t"
      "PWL_%=:\n\t"
      "s_add_u32 s51, s51, 1\n\t"
      "s_cmp_lt_u32 s51, 262144\n\t"
      "s_cbranch_scc0 PWX_%=\n\t"
      "s_sleep 1\n\t"
      "v_min_u32_e32 v184, 32, v236\n\t"
      "v_add_u32_e32 v184, s28, v184\n\t"
      "global_load_dwordx4 v[180:183], v184, s[8:9] sc1\n\t"
      "s_waitcnt vmcnt(0)\n\t"
      "v_xor_b32_e32 v182, v182, v180\n\t"
      "v_xor_b32_e32 v183, v183, v181\n\t"
      "v_cmp_ne_u64_e32 vcc, s[12:13], v[182:183]\n\t"
      "s_cbranch_vccnz PWL_%=\n\t"
      "PWX_%=:\n\t"
      "v_and_b32_e32 v180, 0x2000, v180\n\t"
      "s_nop 1\n\t"
      "s_branch PWB_%=\n\t"
      "BFX_%=:\n\t"
      : "+{s[4:19]}"(s), "+{s[20:27]}"(s2), "+{s[28:29]}"(par), "+{s30}"(lw), "+{v245}"(vn), "+{v247}"(vck), "+{v[224:239]}"(v), "+{v[240:243]}"(vo), "+{v246}"(vq)
      :
      : "memory", "vcc", "scc",
        "v0", "v1", "v2", "v3", "v4", "v5", "v6", "v7", "v8", "v9", "v10", "v11", "v12", "v13", "v14", "v15", "v16",
        "v17", "v18", "v19", "v20", "v21", "v22", "v23", "v24", "v25", "v26", "v27", "v28", "v29", "v30", "v31", "v32",
        "v33", "v34", "v35", "v36", "v37", "v38", "v39", "v40", "v41", "v42", "v43", "v44", "v45", "v46", "v47", "v48",
        "v49", "v50", "v51", "v52", "v53", "v54", "v55", "v56", "v57", "v58", "v59", "v60", "v61", "v62", "v63", "v64",
        "v65", "v66", "v67", "v68", "v69", "v70", "v71", "v72", "v73", "v74", "v75", "v76", "v77", "v78", "v79", "v80",
        "v81", "v82", "v83", "v84", "v85", "v86", "v87", "v88", "v89", "v90", "v91", "v92", "v93", "v94", "v95", "v96",
        "v97", "v98", "v99", "v100", "v101", "v102", "v103", "v104", "v105", "v106", "v107", "v108", "v109", "v110",
        "v111", "v112", "v113", "v114", "v115", "v116", "v117", "v118", "v119", "v120", "v121", "v122", "v123", "v124",
        "v125", "v126", "v127", "v128", "v129", "v130", "v131", "v132", "v133", "v134", "v135", "v136", "v137", "v138",
        "v139", "v140", "v141", "v142", "v143", "v144", "v145", "v146", "v147", "v148", "v149", "v150", "v151", "v152",
        "v153", "v154", "v155", "v156", "v157", "v158", "v159", "v160", "v161", "v162", "v163", "v164", "v165", "v166",
        "v167", "v168", "v169", "v170", "v171", "v172", "v173", "v174", "v175", "v176", "v177", "v178", "v179", "v180",
        "v181", "v182", "v183", "v184", "v185", "v186", "v187", "v188", "v189", "v190", "v191", "v192", "v193", "v194",
        "v195", "v196", "v197", "v198", "v199", "v200", "v201", "v202", "v203", "v204", "v205", "v206", "v207", "v208",
        "v209", "v210", "v211", "v212", "v213", "v214", "v215", "v216", "v217", "v218", "v219", "v220", "v221", "v222",
        "v223", "v248", "v249", "v250", "v251", "v252", "v253", "v254", "s40", "s41", "s42", "s43", "s44", "s45", "s46",
        "s47", "s48", "s49", "s50", "s51"
    );
    }
    PIN();
    LANE_BITS(__builtin_amdgcn_mbcnt_hi(~0u, __builtin_amdgcn_mbcnt_lo(~0u, 0u)));
    u32x8 v, s2;
    u32x16 s;
    v[0] = lane * 16;
    u64 kr = (u64)(6 - rn) | ((u64)rot << 32);
    v[1] = lr * 4096 + ((lc ^ swz_tr(lr)) << 4);
    v[2] = wave * 4096 + g * 1024 + q4 * 128 + (((2 * (wave & 3) + (p4 >> 1)) ^ swz_tr(8 * g + q4)) << 4) + (p4 & 1) * 8;
    v[3] = wave * 4096 + (wave & 3) * 1024 + ((lane * 16) ^ (g << 6));
    v[4] = (wave & 3) * 1024 + (((p4 * 16 + (lane >> 2)) * 16) ^ (p4 << 6));
    v[5] = (lane >> 2) * 896 + p4 * 8;
    v[6] = c * 2;
    const unsigned mh = (unsigned)par, mg = (unsigned)(par >> 32);
    v[7] = ST_H4 + xp * 16384 + ((side * 8192) ^ mg) + ((ib & 1) * 28 + ((7 * wave) >> 1) + p4) * 128;
    put(s, 0, (u64)(w2 + st * 128 + (wave * 224 + rn * 32) * 4096));
    put(s, 2, (u64)(ws + WS_G + (unit(blk0, wave, 0) ^ mg)));
    put(s, 4, (u64)(out + (64 * st + 16 * (wave & 3)) * 896 + dg * 128));
    unsigned od = (16 - dg) * 32;
    put(s, 6, (u64)(b2 + (64 * st + 16 * (wave & 3)) * 2));
    put(s, 8, (u64)(ws + WS_ST));
    s[12] = bits;
    s[13] = wave * G_WV;
    put(s2, 0, (u64)(ws + WS_HN + (unit(blk0, st >> 2, 0) ^ mh) + (2 * (st & 3) + ((wave >> 1) & 1)) * 1024));
    s2[2] = ST_FL + (64 * st + 16 * (wave & 3)) * 16;
    s2[3] = UNI(wave & 1 ? ~0u : 0u);
    put(s2, 4, (u64)(ws + WS_EP));
    asm volatile(
      "s_waitcnt lgkmcnt(0)\n\ts_nop 4\n\t"
      "s_lshr_b32 s31, s17, 2\n\t"
      "v_mov_b32_e32 v112, 163600\n\t"
      "ds_read_b128 v[112:115], v112\n\t"
      "v_xor_b32_e32 v248, 64, v241\n\t"
      "v_add_u32_e32 v249, 63488, v241\n\t"
      "v_add_u32_e32 v250, 95232, v248\n\t"
      "v_add_u32_e32 v248, 31744, v248\n\t"
      "v_xor_b32_e32 v251, 32, v242\n\t"
      "v_xor_b32_e32 v252, 64, v242\n\t"
      "v_xor_b32_e32 v253, 96, v242\n\t"
      "s_bitcmp1_b32 s16, 4\n\t"
      "s_cbranch_scc0 GW2_%=\n\t"
      "s_mov_b32 s48, 0xf000f\n\t"
      "s_mov_b32 s49, 0xf000f\n\t"
      "s_mov_b64 exec, s[48:49]\n\t"
      "global_store_dwordx4 v140, v[136:139], s[12:13] sc1\n\t"
      "s_mov_b64 exec, -1\n\t"
      "GW2_%=:\n\t"
      "s_add_u32 m0, s31, 66048\n\t"
      "s_nop 0\n\t"
      "global_load_lds_dwordx4 v241, s[4:5]\n\t"
      "global_load_lds_dwordx4 v248, s[4:5] offset:1024\n\t"
      "global_load_lds_dwordx4 v249, s[4:5] offset:2048\n\t"
      "global_load_lds_dwordx4 v250, s[4:5] offset:3072\n\t"
      "s_sub_u32 s28, s28, 1\n\t"
      "s_cselect_b32 s48, -6, 1\n\t"
      "s_cselect_b32 s49, -1, 0\n\t"
      "s_mul_i32 s48, s48, 131072\n\t"
      "s_add_u32 s4, s4, s48\n\t"
      "s_addc_u32 s5, s5, s49\n\t"
      "global_load_ushort v236, v246, s[10:11]\n\t"
      "s_waitcnt lgkmcnt(0)\n\t"
      "v_readfirstlane_b32 s14, v112\n\t"
      "v_readfirstlane_b32 s15, v113\n\t"
      "v_readfirstlane_b32 s26, v114\n\t"
      "v_readfirstlane_b32 s27, v115\n\t"
      "s_add_u32 s52, s6, 0\n\t"
      "s_addc_u32 s53, s7, 0\n\t"
      "s_add_u32 s54, s6, 131072\n\t"
      "s_addc_u32 s55, s7, 0\n\t"
      "s_add_u32 s56, s6, 262144\n\t"
      "s_addc_u32 s57, s7, 0\n\t"
      "s_add_u32 s58, s6, 393216\n\t"
      "s_addc_u32 s59, s7, 0\n\t"
      "s_add_u32 s60, s6, 524288\n\t"
      "s_addc_u32 s61, s7, 0\n\t"
      "s_add_u32 s62, s6, 655360\n\t"
      "s_addc_u32 s63, s7, 0\n\t"
      "s_add_u32 s64, s6, 786432\n\t"
      "s_addc_u32 s65, s7, 0\n\t"
      "s_add_u32 s48, s29, 0\n\t"
      "s_cmp_ge_u32 s48, 7\n\t"
      "s_cselect_b32 s49, 7, 0\n\t"
      "s_sub_u32 s48, s48, s49\n\t"
      "v_lshl_add_u32 v224, s48, 10, v240\n\t"
      "s_add_u32 s48, s29, 1\n\t"
      "s_cmp_ge_u32 s48, 7\n\t"
      "s_cselect_b32 s49, 7, 0\n\t"
      "s_sub_u32 s48, s48, s49\n\t"
      "v_lshl_add_u32 v225, s48, 10, v240\n\t"
      "s_add_u32 s48, s29, 2\n\t"
      "s_cmp_ge_u32 s48, 7\n\t"
      "s_cselect_b32 s49, 7, 0\n\t"
      "s_sub_u32 s48, s48, s49\n\t"
      "v_lshl_add_u32 v226, s48, 10, v240\n\t"
      "s_add_u32 s48, s29, 3\n\t"
      "s_cmp_ge_u32 s48, 7\n\t"
      "s_cselect_b32 s49, 7, 0\n\t"
      "s_sub_u32 s48, s48, s49\n\t"
      "v_lshl_add_u32 v227, s48, 10, v240\n\t"
      "s_add_u32 s48, s29, 4\n\t"
      "s_cmp_ge_u32 s48, 7\n\t"
      "s_cselect_b32 s49, 7, 0\n\t"
      "s_sub_u32 s48, s48, s49\n\t"
      "v_lshl_add_u32 v228, s48, 10, v240\n\t"
      "s_add_u32 s48, s29, 5\n\t"
      "s_cmp_ge_u32 s48, 7\n\t"
      "s_cselect_b32 s49, 7, 0\n\t"
      "s_sub_u32 s48, s48, s49\n\t"
      "v_lshl_add_u32 v229, s48, 10, v240\n\t"
      "s_add_u32 s48, s29, 6\n\t"
      "s_cmp_ge_u32 s48, 7\n\t"
      "s_cselect_b32 s49, 7, 0\n\t"
      "s_sub_u32 s48, s48, s49\n\t"
      "v_lshl_add_u32 v230, s48, 10, v240\n\t"
      "v_xor_b32_e32 v220, 1024, v243\n\t"
      "v_xor_b32_e32 v221, 2048, v243\n\t"
      "v_xor_b32_e32 v222, 3072, v243\n\t"
      "s_add_u32 s66, s8, s30\n\t"
      "s_addc_u32 s67, s9, 0\n\t"
      "v_lshrrev_b32_e32 v112, 4, v240\n\t"
      "v_and_b32_e32 v113, 15, v112\n\t"
      "v_lshrrev_b32_e32 v112, 4, v112\n\t"
      "s_lshr_b32 s48, s17, 10\n\t"
      "s_and_b32 s48, s48, 16\n\t"
      "v_lshlrev_b32_e32 v112, 3, v112\n\t"
      "v_sub_u32_e32 v112, v113, v112\n\t"
      "v_add_u32_e32 v112, s48, v112\n\t"
      "v_mov_b32_e32 v114, 0xffff\n\t"
      "v_mov_b32_e32 v115, 0xffff0000\n\t"
      "v_cmp_eq_u32_e32 vcc, 0, v112\n\t"
      "s_nop 1\n\t"
      "v_cndmask_b32_e32 v216, 0, v114, vcc\n\t"
      "v_cmp_eq_u32_e32 vcc, 1, v112\n\t"
      "s_nop 1\n\t"
      "v_cndmask_b32_e32 v216, v216, v115, vcc\n\t"
      "v_cmp_eq_u32_e32 vcc, 2, v112\n\t"
      "s_nop 1\n\t"
      "v_cndmask_b32_e32 v217, 0, v114, vcc\n\t"
      "v_cmp_eq_u32_e32 vcc, 3, v112\n\t"
      "s_nop 1\n\t"
      "v_cndmask_b32_e32 v217, v217, v115, vcc\n\t"
      "v_cmp_eq_u32_e32 vcc, 4, v112\n\t"
      "s_nop 1\n\t"
      "v_cndmask_b32_e32 v218, 0, v114, vcc\n\t"
      "v_cmp_eq_u32_e32 vcc, 5, v112\n\t"
      "s_nop 1\n\t"
      "v_cndmask_b32_e32 v218, v218, v115, vcc\n\t"
      "v_cmp_eq_u32_e32 vcc, 6, v112\n\t"
      "s_nop 1\n\t"
      "v_cndmask_b32_e32 v219, 0, v114, vcc\n\t"
      "v_cmp_eq_u32_e32 vcc, 7, v112\n\t"
      "s_nop 1\n\t"
      "v_cndmask_b32_e32 v219, v219, v115, vcc\n\t"
      "v_and_b32_e32 v116, 0xf0, v240\n\t"
      "v_add_u32_e32 v116, s22, v116\n\t"
      "v_mov_b64_e32 v[184:185], 0\n\t"
      "v_mov_b64_e32 v[186:187], 0\n\t"
      "v_mov_b64_e32 v[188:189], 0\n\t"
      "v_mov_b64_e32 v[190:191], 0\n\t"
      "v_mov_b64_e32 v[192:193], 0\n\t"
      "v_mov_b64_e32 v[194:195], 0\n\t"
      "v_mov_b64_e32 v[196:197], 0\n\t"
      "v_mov_b64_e32 v[198:199], 0\n\t"
      "v_mov_b64_e32 v[200:201], 0\n\t"
      "v_mov_b64_e32 v[202:203], 0\n\t"
      "v_mov_b64_e32 v[204:205], 0\n\t"
      "v_mov_b64_e32 v[206:207], 0\n\t"
      "v_mov_b64_e32 v[208:209], 0\n\t"
      "v_mov_b64_e32 v[210:211], 0\n\t"
      "s_waitcnt vmcnt(5)\n\t"
      "v_add_u32_e32 v242, 66048, v242\n\t"
      "v_add_u32_e32 v251, 66048, v251\n\t"
      "v_add_u32_e32 v252, 66048, v252\n\t"
      "v_add_u32_e32 v253, 66048, v253\n\t"
      "ds_read_b64_tr_b16 v[0:1], v242 offset:64512\n\t"
      "ds_read_b64_tr_b16 v[2:3], v242 offset:65024\n\t"
      "ds_read_b64_tr_b16 v[4:5], v251 offset:64512\n\t"
      "ds_read_b64_tr_b16 v[6:7], v251 offset:65024\n\t"
      "ds_read_b64_tr_b16 v[8:9], v252 offset:64512\n\t"
      "ds_read_b64_tr_b16 v[10:11], v252 offset:65024\n\t"
      "ds_read_b64_tr_b16 v[12:13], v253 offset:64512\n\t"
      "ds_read_b64_tr_b16 v[14:15], v253 offset:65024\n\t"
      "s_waitcnt vmcnt(1)\n\t"
      "s_add_u32 m0, s31, 0\n\t"
      "s_nop 0\n\t"
      "global_load_lds_dwordx4 v241, s[4:5]\n\t"
      "global_load_lds_dwordx4 v248, s[4:5] offset:1024\n\t"
      "global_load_lds_dwordx4 v249, s[4:5] offset:2048\n\t"
      "global_load_lds_dwordx4 v250, s[4:5] offset:3072\n\t"
      "s_sub_u32 s28, s28, 1\n\t"
      "s_cselect_b32 s48, -6, 1\n\t"
      "s_cselect_b32 s49, -1, 0\n\t"
      "s_mul_i32 s48, s48, 131072\n\t"
      "s_add_u32 s4, s4, s48\n\t"
      "s_addc_u32 s5, s5, s49\n\t"
      "ds_read_b64_tr_b16 v[16:17], v242\n\t"
      "ds_read_b64_tr_b16 v[18:19], v242 offset:512\n\t"
      "ds_read_b64_tr_b16 v[20:21], v251\n\t"
      "ds_read_b64_tr_b16 v[22:23], v251 offset:512\n\t"
      "ds_read_b64_tr_b16 v[24:25], v252\n\t"
      "ds_read_b64_tr_b16 v[26:27], v252 offset:512\n\t"
      "ds_read_b64_tr_b16 v[28:29], v253\n\t"
      "ds_read_b64_tr_b16 v[30:31], v253 offset:512\n\t"
      "s_waitcnt vmcnt(0)\n\t"
      "s_add_u32 m0, s31, 32768\n\t"
      "s_nop 0\n\t"
      "global_load_lds_dwordx4 v241, s[4:5]\n\t"
      "global_load_lds_dwordx4 v248, s[4:5] offset:1024\n\t"
      "global_load_lds_dwordx4 v249, s[4:5] offset:2048\n\t"
      "global_load_lds_dwordx4 v250, s[4:5] offset:3072\n\t"
      "s_sub_u32 s28, s28, 1\n\t"
      "s_cselect_b32 s48, -6, 1\n\t"
      "s_cselect_b32 s49, -1, 0\n\t"
      "s_mul_i32 s48, s48, 131072\n\t"
      "s_add_u32 s4, s4, s48\n\t"
      "s_addc_u32 s5, s5, s49\n\t"
      "v_add_u32_e32 v242, 4294901248, v242\n\t"
      "v_add_u32_e32 v251, 4294901248, v251\n\t"
      "v_add_u32_e32 v252, 4294901248, v252\n\t"
      "v_add_u32_e32 v253, 4294901248, v253\n\t"
      "ds_read_b64_tr_b16 v[32:33], v242\n\t"
      "ds_read_b64_tr_b16 v[34:35], v242 offset:512\n\t"
      "ds_read_b64_tr_b16 v[36:37], v251\n\t"
      "ds_read_b64_tr_b16 v[38:39], v251 offset:512\n\t"
      "ds_read_b64_tr_b16 v[40:41], v252\n\t"
      "ds_read_b64_tr_b16 v[42:43], v252 offset:512\n\t"
      "ds_read_b64_tr_b16 v[44:45], v253\n\t"
      "ds_read_b64_tr_b16 v[46:47], v253 offset:512\n\t"
      "s_waitcnt vmcnt(0)\n\t"
      "s_waitcnt lgkmcnt(14)\n\t"
      "s_add_u32 m0, s31, 130560\n\t"
      "s_nop 0\n\t"
      "global_load_lds_dwordx4 v241, s[4:5]\n\t"
      "global_load_lds_dwordx4 v248, s[4:5] offset:1024\n\t"
      "global_load_lds_dwordx4 v249, s[4:5] offset:2048\n\t"
      "global_load_lds_dwordx4 v250, s[4:5] offset:3072\n\t"
      "s_sub_u32 s28, s28, 1\n\t"
      "s_cselect_b32 s48, -6, 1\n\t"
      "s_cselect_b32 s49, -1, 0\n\t"
      "s_mul_i32 s48, s48, 131072\n\t"
      "s_add_u32 s4, s4, s48\n\t"
      "s_addc_u32 s5, s5, s49\n\t"
      "ds_read_b64_tr_b16 v[48:49], v242 offset:32768\n\t"
      "ds_read_b64_tr_b16 v[50:51], v242 offset:33280\n\t"
      "ds_read_b64_tr_b16 v[52:53], v251 offset:32768\n\t"
      "ds_read_b64_tr_b16 v[54:55], v251 offset:33280\n\t"
      "ds_read_b64_tr_b16 v[56:57], v252 offset:32768\n\t"
      "ds_read_b64_tr_b16 v[58:59], v252 offset:33280\n\t"
      "ds_read_b64_tr_b16 v[60:61], v253 offset:32768\n\t"
      "ds_read_b64_tr_b16 v[62:63], v253 offset:33280\n\t"
      "s_bitcmp1_b32 s16, 1\n\t"
      "s_cbranch_scc0 HWQ_%=\n\t"
      "global_load_dwordx4 v[112:115], v116, s[12:13] sc1\n\t"
      "HWQ_%=:\n\t"
      "s_waitcnt vmcnt(0)\n\t"
      "s_waitcnt lgkmcnt(14)\n\t"
      "s_add_u32 m0, s31, 66048\n\t"
      "s_nop 0\n\t"
      "global_load_lds_dwordx4 v241, s[4:5]\n\t"
      "global_load_lds_dwordx4 v248, s[4:5] offset:1024\n\t"
      "global_load_lds_dwordx4 v249, s[4:5] offset:2048\n\t"
      "global_load_lds_dwordx4 v250, s[4:5] offset:3072\n\t"
      "s_sub_u32 s28, s28, 1\n\t"
      "s_cselect_b32 s48, -6, 1\n\t"
      "s_cselect_b32 s49, -1, 0\n\t"
      "s_mul_i32 s48, s48, 131072\n\t"
      "s_add_u32 s4, s4, s48\n\t"
      "s_addc_u32 s5, s5, s49\n\t"
      "v_add_u32_e32 v242, 66048, v242\n\t"
      "v_add_u32_e32 v251, 66048, v251\n\t"
      "v_add_u32_e32 v252, 66048, v252\n\t"
      "v_add_u32_e32 v253, 66048, v253\n\t"
      "ds_read_b64_tr_b16 v[64:65], v242 offset:64512\n\t"
      "ds_read_b64_tr_b16 v[66:67], v242 offset:65024\n\t"
      "ds_read_b64_tr_b16 v[68:69], v251 offset:64512\n\t"
      "ds_read_b64_tr_b16 v[70:71], v251 offset:65024\n\t"
      "ds_read_b64_tr_b16 v[72:73], v252 offset:64512\n\t"
      "ds_read_b64_tr_b16 v[74:75], v252 offset:65024\n\t"
      "ds_read_b64_tr_b16 v[76:77], v253 offset:64512\n\t"
      "ds_read_b64_tr_b16 v[78:79], v253 offset:65024\n\t"
      "s_waitcnt vmcnt(0)\n\t"
      "s_waitcnt lgkmcnt(14)\n\t"
      "s_add_u32 m0, s31, 0\n\t"
      "s_nop 0\n\t"
      "global_load_lds_dwordx4 v241, s[4:5]\n\t"
      "global_load_lds_dwordx4 v248, s[4:5] offset:1024\n\t"
      "global_load_lds_dwordx4 v249, s[4:5] offset:2048\n\t"
      "global_load_lds_dwordx4 v250, s[4:5] offset:3072\n\t"
      "ds_read_b64_tr_b16 v[80:81], v242\n\t"
      "ds_read_b64_tr_b16 v[82:83], v242 offset:512\n\t"
      "ds_read_b64_tr_b16 v[84:85], v251\n\t"
      "ds_read_b64_tr_b16 v[86:87], v251 offset:512\n\t"
      "ds_read_b64_tr_b16 v[88:89], v252\n\t"
      "ds_read_b64_tr_b16 v[90:91], v252 offset:512\n\t"
      "ds_read_b64_tr_b16 v[92:93], v253\n\t"
      "ds_read_b64_tr_b16 v[94:95], v253 offset:512\n\t"
      "s_bitcmp1_b32 s16, 1\n\t"
      "s_cbranch_scc0 HWH_%=\n\t"
      "v_xor_b32_e32 v114, v114, v112\n\t"
      "v_xor_b32_e32 v115, v115, v113\n\t"
      "v_cmp_ne_u64_e32 vcc, s[14:15], v[114:115]\n\t"
      "s_cbranch_vccnz HWR_%=\n\t"
      "HWB_%=:\n\t"
      "s_mov_b32 exec_hi, s23\n\t"
      "s_not_b32 exec_lo, s23\n\t"
      "s_add_u32 s68, s20, 0\n\t"
      "s_addc_u32 s69, s21, 0\n\t"
      "global_load_dwordx4 v[184:187], v240, s[68:69] sc1\n\t"
      "s_add_u32 s68, s20, 131072\n\t"
      "s_addc_u32 s69, s21, 0\n\t"
      "global_load_dwordx4 v[188:191], v240, s[68:69] sc1\n\t"
      "s_add_u32 s68, s20, 262144\n\t"
      "s_addc_u32 s69, s21, 0\n\t"
      "global_load_dwordx4 v[192:195], v240, s[68:69] sc1\n\t"
      "s_add_u32 s68, s20, 393216\n\t"
      "s_addc_u32 s69, s21, 0\n\t"
      "global_load_dwordx4 v[196:199], v240, s[68:69] sc1\n\t"
      "s_add_u32 s68, s20, 524288\n\t"
      "s_addc_u32 s69, s21, 0\n\t"
      "global_load_dwordx4 v[200:203], v240, s[68:69] sc1\n\t"
      "s_add_u32 s68, s20, 655360\n\t"
      "s_addc_u32 s69, s21, 0\n\t"
      "global_load_dwordx4 v[204:207], v240, s[68:69] sc1\n\t"
      "s_add_u32 s68, s20, 786432\n\t"
      "s_addc_u32 s69, s21, 0\n\t"
      "global_load_dwordx4 v[208:211], v240, s[68:69] sc1\n\t"
      "s_mov_b64 exec, -1\n\t"
      "v_lshlrev_b32_e32 v236, 16, v236\n\t"
      "v_add_f32_e32 v236, v236, v112\n\t"
      "v_cvt_pk_bf16_f32 v114, v113, v113\n\t"
      "v_lshlrev_b32_e32 v115, 16, v114\n\t"
      "v_sub_f32_e32 v115, v113, v115\n\t"
      "v_cvt_pk_bf16_f32 v115, v115, v115\n\t"
      "v_mov_b32_e32 v237, v236\n\t"
      "v_mov_b32_e32 v238, v236\n\t"
      "v_mov_b32_e32 v239, v236\n\t"
      "v_and_b32_e32 v120, v216, v115\n\t"
      "v_and_b32_e32 v216, v216, v114\n\t"
      "v_and_b32_e32 v121, v217, v115\n\t"
      "v_and_b32_e32 v217, v217, v114\n\t"
      "v_and_b32_e32 v122, v218, v115\n\t"
      "v_and_b32_e32 v218, v218, v114\n\t"
      "v_and_b32_e32 v123, v219, v115\n\t"
      "v_and_b32_e32 v219, v219, v114\n\t"
      "HWH_%=:\n\t"
      "s_waitcnt vmcnt(0)\n\t"
      "v_add_u32_e32 v242, 4294901248, v242\n\t"
      "v_add_u32_e32 v251, 4294901248, v251\n\t"
      "v_add_u32_e32 v252, 4294901248, v252\n\t"
      "v_add_u32_e32 v253, 4294901248, v253\n\t"
      "ds_read_b64_tr_b16 v[96:97], v242\n\t"
      "ds_read_b64_tr_b16 v[98:99], v242 offset:512\n\t"
      "ds_read_b64_tr_b16 v[100:101], v251\n\t"
      "ds_read_b64_tr_b16 v[102:103], v251 offset:512\n\t"
      "ds_read_b64_tr_b16 v[104:105], v252\n\t"
      "ds_read_b64_tr_b16 v[106:107], v252 offset:512\n\t"
      "ds_read_b64_tr_b16 v[108:109], v253\n\t"
      "ds_read_b64_tr_b16 v[110:111], v253 offset:512\n\t"
      "s_mov_b64 exec, 15\n\t"
      "v_mov_b64_e32 v[232:233], 0\n\t"
      "global_atomic_or_x2 v[232:233], v247, v[232:233], s[12:13] sc0 sc1\n\t"
      "s_mov_b64 exec, -1\n\t"
      "s_bitcmp1_b32 s16, 1\n\t"
      "s_cbranch_scc0 HWE_%=\n\t"
      "s_waitcnt vmcnt(1)\n\t"
      "v_max3_u32 v234, v184, v185, v186\n\t"
      "v_max3_u32 v234, v234, v187, v188\n\t"
      "v_max3_u32 v234, v234, v189, v190\n\t"
      "v_max3_u32 v234, v234, v191, v192\n\t"
      "v_max3_u32 v234, v234, v193, v194\n\t"
      "v_max3_u32 v234, v234, v195, v196\n\t"
      "v_max3_u32 v234, v234, v197, v198\n\t"
      "v_max3_u32 v234, v234, v199, v200\n\t"
      "v_max3_u32 v234, v234, v201, v202\n\t"
      "v_max3_u32 v234, v234, v203, v204\n\t"
      "v_max3_u32 v234, v234, v205, v206\n\t"
      "v_max3_u32 v234, v234, v207, v208\n\t"
      "v_max3_u32 v234, v234, v209, v210\n\t"
      "v_max_u32_e32 v234, v234, v211\n\t"
      "v_cmp_eq_u32_e32 vcc, -1, v234\n\t"
      "s_cbranch_vccnz HXR_%=\n\t"
      "HXB_%=:\n\t"
      "v_mfma_f32_16x16x32_bf16 v[156:159], v[184:187], v[120:123], v[236:239]\n\t"
      "v_mfma_f32_16x16x32_bf16 v[160:163], v[188:191], v[120:123], v[236:239]\n\t"
      "v_mfma_f32_16x16x32_bf16 v[164:167], v[192:195], v[120:123], v[236:239]\n\t"
      "v_mfma_f32_16x16x32_bf16 v[168:171], v[196:199], v[120:123], v[236:239]\n\t"
      "v_mfma_f32_16x16x32_bf16 v[172:175], v[200:203], v[120:123], v[236:239]\n\t"
      "v_mfma_f32_16x16x32_bf16 v[176:179], v[204:207], v[120:123], v[236:239]\n\t"
      "v_mfma_f32_16x16x32_bf16 v[180:183], v[208:211], v[120:123], v[236:239]\n\t"
      "v_mfma_f32_16x16x32_bf16 v[156:159], v[184:187], v[216:219], v[156:159]\n\t"
      "v_mfma_f32_16x16x32_bf16 v[160:163], v[188:191], v[216:219], v[160:163]\n\t"
      "v_mfma_f32_16x16x32_bf16 v[164:167], v[192:195], v[216:219], v[164:167]\n\t"
      "v_mfma_f32_16x16x32_bf16 v[168:171], v[196:199], v[216:219], v[168:171]\n\t"
      "v_mfma_f32_16x16x32_bf16 v[172:175], v[200:203], v[216:219], v[172:175]\n\t"
      "v_mfma_f32_16x16x32_bf16 v[176:179], v[204:207], v[216:219], v[176:179]\n\t"
      "v_mfma_f32_16x16x32_bf16 v[180:183], v[208:211], v[216:219], v[180:183]\n\t"
      "HWE_%=:\n\t"
      "s_waitcnt lgkmcnt(0)\n\t"
      "s_barrier\n\t"
      "s_mov_b64 exec, 15\n\t"
      "s_waitcnt vmcnt(0)\n\t"
      "v_subrev_u32_e32 v232, s14, v232\n\t"
      "v_cmp_ne_u32_e64 s[48:49], s15, v233\n\t"
      "v_subrev_u32_e32 v232, 0, v232\n\t"
      "v_cmp_lt_u32_e32 vcc, 7, v232\n\t"
      "s_mov_b64 exec, -1\n\t"
      "s_or_b64 vcc, vcc, s[48:49]\n\t"
      "s_cbranch_vccz PRY_%=\n\t"
      "s_mov_b32 s51, 0\n\t"
      "s_branch PRE_%=\n\t"
      "PRL_%=:\n\t"
      "s_sleep 4\n\t"
      "s_add_u32 s51, s51, 1\n\t"
      "s_cmp_lt_u32 s51, 262144\n\t"
      "s_cbranch_scc0 PRX_%=\n\t"
      "PRE_%=:\n\t"
      "s_mov_b64 exec, 15\n\t"
      "v_mov_b64_e32 v[232:233], 0\n\t"
      "global_atomic_or_x2 v[232:233], v247, v[232:233], s[12:13] sc0 sc1\n\t"
      "s_waitcnt vmcnt(0)\n\t"
      "v_subrev_u32_e32 v232, s14, v232\n\t"
      "v_cmp_ne_u32_e64 s[48:49], s15, v233\n\t"
      "v_subrev_u32_e32 v232, 0, v232\n\t"
      "v_cmp_lt_u32_e32 vcc, 7, v232\n\t"
      "s_mov_b64 exec, -1\n\t"
      "s_or_b64 vcc, vcc, s[48:49]\n\t"
      "s_cbranch_vccnz PRL_%=\n\t"
      "PRX_%=:\n\t"
      "PRY_%=:\n\t"
      "global_load_dwordx4 v[128:131], v224, s[52:53] sc1\n\t"
      "global_load_dwordx4 v[132:135], v225, s[52:53] sc1\n\t"
      "global_load_dwordx4 v[136:139], v226, s[52:53] sc1\n\t"
      "global_load_dwordx4 v[140:143], v227, s[52:53] sc1\n\t"
      "global_load_dwordx4 v[144:147], v228, s[52:53] sc1\n\t"
      "global_load_dwordx4 v[148:151], v229, s[52:53] sc1\n\t"
      "global_load_dwordx4 v[152:155], v230, s[52:53] sc1\n\t"
      "s_cmp_ge_u32 s17, 65536\n\t"
      "s_cbranch_scc1 GQ_%=\n\t"
      ".p2align 6\n\t"
      "s_setprio 1\n\t"
      "s_waitcnt vmcnt(6)\n\t"
      "v_max3_u32 v234, v128, v129, v130\n\t"
      "v_max_u32_e32 v234, v234, v131\n\t"
      "v_cmp_eq_u32_e32 vcc, -1, v234\n\t"
      "s_cbranch_vccnz FP0R_%=\n\t"
      "FP0B_%=:\n\t"
      "v_mfma_f32_16x16x32_bf16 v[184:187], v[128:131], v[0:3], v[156:159]\n\t"
      "v_mfma_f32_16x16x32_bf16 v[188:191], v[128:131], v[4:7], 0\n\t"
      "v_mfma_f32_16x16x32_bf16 v[192:195], v[128:131], v[8:11], 0\n\t"
      "v_mfma_f32_16x16x32_bf16 v[196:199], v[128:131], v[12:15], 0\n\t"
      "global_load_dwordx4 v[128:131], v224, s[54:55] sc1\n\t"
      "s_waitcnt vmcnt(6)\n\t"
      "v_max3_u32 v234, v132, v133, v134\n\t"
      "v_max_u32_e32 v234, v234, v135\n\t"
      "v_cmp_eq_u32_e32 vcc, -1, v234\n\t"
      "s_cbranch_vccnz FP1R_%=\n\t"
      "FP1B_%=:\n\t"
      "v_mfma_f32_16x16x32_bf16 v[184:187], v[132:135], v[16:19], v[184:187]\n\t"
      "v_mfma_f32_16x16x32_bf16 v[188:191], v[132:135], v[20:23], v[188:191]\n\t"
      "v_mfma_f32_16x16x32_bf16 v[192:195], v[132:135], v[24:27], v[192:195]\n\t"
      "v_mfma_f32_16x16x32_bf16 v[196:199], v[132:135], v[28:31], v[196:199]\n\t"
      "global_load_dwordx4 v[132:135], v225, s[54:55] sc1\n\t"
      "s_waitcnt vmcnt(6)\n\t"
      "v_max3_u32 v234, v136, v137, v138\n\t"
      "v_max_u32_e32 v234, v234, v139\n\t"
      "v_cmp_eq_u32_e32 vcc, -1, v234\n\t"
      "s_cbranch_vccnz FP2R_%=\n\t"
      "FP2B_%=:\n\t"
      "v_mfma_f32_16x16x32_bf16 v[184:187], v[136:139], v[32:35], v[184:187]\n\t"
      "v_mfma_f32_16x16x32_bf16 v[188:191], v[136:139], v[36:39], v[188:191]\n\t"
      "v_mfma_f32_16x16x32_bf16 v[192:195], v[136:139], v[40:43], v[192:195]\n\t"
      "v_mfma_f32_16x16x32_bf16 v[196:199], v[136:139], v[44:47], v[196:199]\n\t"
      "global_load_dwordx4 v[136:139], v226, s[54:55] sc1\n\t"
      "s_waitcnt vmcnt(6)\n\t"
      "v_max3_u32 v234, v140, v141, v142\n\t"
      "v_max_u32_e32 v234, v234, v143\n\t"
      "v_cmp_eq_u32_e32 vcc, -1, v234\n\t"
      "s_cbranch_vccnz FP3R_%=\n\t"
      "FP3B_%=:\n\t"
      "v_mfma_f32_16x16x32_bf16 v[184:187], v[140:143], v[48:51], v[184:187]\n\t"
      "v_mfma_f32_16x16x32_bf16 v[188:191], v[140:143], v[52:55], v[188:191]\n\t"
      "v_mfma_f32_16x16x32_bf16 v[192:195], v[140:143], v[56:59], v[192:195]\n\t"
      "v_mfma_f32_16x16x32_bf16 v[196:199], v[140:143], v[60:63], v[196:199]\n\t"
      "global_load_dwordx4 v[140:143], v227, s[54:55] sc1\n\t"
      "s_waitcnt vmcnt(6)\n\t"
      "v_max3_u32 v234, v144, v145, v146\n\t"
      "v_max_u32_e32 v234, v234, v147\n\t"
      "v_cmp_eq_u32_e32 vcc, -1, v234\n\t"
      "s_cbranch_vccnz FP4R_%=\n\t"
      "FP4B_%=:\n\t"
      "v_mfma_f32_16x16x32_bf16 v[184:187], v[144:147], v[64:67], v[184:187]\n\t"
      "v_mfma_f32_16x16x32_bf16 v[188:191], v[144:147], v[68:71], v[188:191]\n\t"
      "v_mfma_f32_16x16x32_bf16 v[192:195], v[144:147], v[72:75], v[192:195]\n\t"
      "v_mfma_f32_16x16x32_bf16 v[196:199], v[144:147], v[76:79], v[196:199]\n\t"
      "global_load_dwordx4 v[144:147], v228, s[54:55] sc1\n\t"
      "s_waitcnt vmcnt(6)\n\t"
      "v_max3_u32 v234, v148, v149, v150\n\t"
      "v_max_u32_e32 v234, v234, v151\n\t"
      "v_cmp_eq_u32_e32 vcc, -1, v234\n\t"
      "s_cbranch_vccnz FP5R_%=\n\t"
      "FP5B_%=:\n\t"
      "v_mfma_f32_16x16x32_bf16 v[184:187], v[148:151], v[80:83], v[184:187]\n\t"
      "v_mfma_f32_16x16x32_bf16 v[188:191], v[148:151], v[84:87], v[188:191]\n\t"
      "v_mfma_f32_16x16x32_bf16 v[192:195], v[148:151], v[88:91], v[192:195]\n\t"
      "v_mfma_f32_16x16x32_bf16 v[196:199], v[148:151], v[92:95], v[196:199]\n\t"
      "global_load_dwordx4 v[148:151], v229, s[54:55] sc1\n\t"
      "s_waitcnt vmcnt(6)\n\t"
      "v_max3_u32 v234, v152, v153, v154\n\t"
      "v_max_u32_e32 v234, v234, v155\n\t"
      "v_cmp_eq_u32_e32 vcc, -1, v234\n\t"
      "s_cbranch_vccnz FP6R_%=\n\t"
      "FP6B_%=:\n\t"
      "v_mfma_f32_16x16x32_bf16 v[184:187], v[152:155], v[96:99], v[184:187]\n\t"
      "s_waitcnt vmcnt(5)\n\t"
      "v_cmp_eq_u32_e32 vcc, -1, v128\n\t"
      "v_mfma_f32_16x16x32_bf16 v[188:191], v[152:155], v[100:103], v[188:191]\n\t"
      "v_mfma_f32_16x16x32_bf16 v[192:195], v[152:155], v[104:107], v[192:195]\n\t"
      "s_cbranch_vccnz FP7R_%=\n\t"
      "FP7B_%=:\n\t"
      "v_mfma_f32_16x16x32_bf16 v[196:199], v[152:155], v[108:111], v[196:199]\n\t"
      "global_load_dwordx4 v[152:155], v230, s[54:55] sc1\n\t"
      "s_nop 0\n\t"
      "ds_write_b128 v243, v[184:187]\n\t"
      "s_nop 1\n\t"
      "ds_write_b128 v220, v[188:191]\n\t"
      "ds_write_b128 v221, v[192:195]\n\t"
      "s_nop 0\n\t"
      "ds_write_b128 v222, v[196:199]\n\t"
      "s_waitcnt lgkmcnt(0)\n\t"
      "s_barrier\n\t"
      "ds_read_b128 v[112:115], v244\n\t"
      "ds_read_b128 v[116:119], v244 offset:4096\n\t"
      "ds_read_b128 v[120:123], v244 offset:8192\n\t"
      "ds_read_b128 v[124:127], v244 offset:12288\n\t"
      "ds_read_b128 v[184:187], v244 offset:16384\n\t"
      "ds_read_b128 v[188:191], v244 offset:20480\n\t"
      "ds_read_b128 v[192:195], v244 offset:24576\n\t"
      "ds_read_b128 v[196:199], v244 offset:28672\n\t"
      "v_mfma_f32_16x16x32_bf16 v[200:203], v[128:131], v[0:3], v[160:163]\n\t"
      "s_waitcnt vmcnt(5)\n\t"
      "v_cmp_eq_u32_e32 vcc, -1, v132\n\t"
      "v_mfma_f32_16x16x32_bf16 v[204:207], v[128:131], v[4:7], 0\n\t"
      "s_waitcnt lgkmcnt(6)\n\t"
      "v_add_f32_e32 v112, v112, v116\n\t"
      "v_mfma_f32_16x16x32_bf16 v[208:211], v[128:131], v[8:11], 0\n\t"
      "s_cbranch_vccnz FP8R_%=\n\t"
      "FP8B_%=:\n\t"
      "v_add_f32_e32 v113, v113, v117\n\t"
      "v_add_f32_e32 v114, v114, v118\n\t"
      "v_mfma_f32_16x16x32_bf16 v[212:215], v[128:131], v[12:15], 0\n\t"
      "global_load_dwordx4 v[128:131], v224, s[56:57] sc1\n\t"
      "v_add_f32_e32 v115, v115, v119\n\t"
      "v_mfma_f32_16x16x32_bf16 v[200:203], v[132:135], v[16:19], v[200:203]\n\t"
      "s_waitcnt vmcnt(5)\n\t"
      "v_cmp_eq_u32_e32 vcc, -1, v136\n\t"
      "s_waitcnt lgkmcnt(4)\n\t"
      "v_add_f32_e32 v120, v120, v124\n\t"
      "v_mfma_f32_16x16x32_bf16 v[204:207], v[132:135], v[20:23], v[204:207]\n\t"
      "v_add_f32_e32 v121, v121, v125\n\t"
      "v_add_f32_e32 v122, v122, v126\n\t"
      "v_mfma_f32_16x16x32_bf16 v[208:211], v[132:135], v[24:27], v[208:211]\n\t"
      "s_cbranch_vccnz FP9R_%=\n\t"
      "FP9B_%=:\n\t"
      "v_add_f32_e32 v123, v123, v127\n\t"
      "s_waitcnt lgkmcnt(2)\n\t"
      "v_add_f32_e32 v184, v184, v188\n\t"
      "v_mfma_f32_16x16x32_bf16 v[212:215], v[132:135], v[28:31], v[212:215]\n\t"
      "global_load_dwordx4 v[132:135], v225, s[56:57] sc1\n\t"
      "v_add_f32_e32 v185, v185, v189\n\t"
      "v_mfma_f32_16x16x32_bf16 v[200:203], v[136:139], v[32:35], v[200:203]\n\t"
      "s_waitcnt vmcnt(5)\n\t"
      "v_cmp_eq_u32_e32 vcc, -1, v140\n\t"
      "v_add_f32_e32 v186, v186, v190\n\t"
      "v_mfma_f32_16x16x32_bf16 v[204:207], v[136:139], v[36:39], v[204:207]\n\t"
      "v_add_f32_e32 v187, v187, v191\n\t"
      "v_add_f32_e32 v112, v112, v120\n\t"
      "v_mfma_f32_16x16x32_bf16 v[208:211], v[136:139], v[40:43], v[208:211]\n\t"
      "s_cbranch_vccnz FP10R_%=\n\t"
      "FP10B_%=:\n\t"
      "v_add_f32_e32 v113, v113, v121\n\t"
      "v_add_f32_e32 v114, v114, v122\n\t"
      "v_mfma_f32_16x16x32_bf16 v[212:215], v[136:139], v[44:47], v[212:215]\n\t"
      "global_load_dwordx4 v[136:139], v226, s[56:57] sc1\n\t"
      "v_add_f32_e32 v115, v115, v123\n\t"
      "v_mfma_f32_16x16x32_bf16 v[200:203], v[140:143], v[48:51], v[200:203]\n\t"
      "s_waitcnt vmcnt(5)\n\t"
      "v_cmp_eq_u32_e32 vcc, -1, v144\n\t"
      "s_waitcnt lgkmcnt(0)\n\t"
      "v_add_f32_e32 v192, v192, v196\n\t"
      "v_mfma_f32_16x16x32_bf16 v[204:207], v[140:143], v[52:55], v[204:207]\n\t"
      "v_add_f32_e32 v193, v193, v197\n\t"
      "v_add_f32_e32 v194, v194, v198\n\t"
      "v_mfma_f32_16x16x32_bf16 v[208:211], v[140:143], v[56:59], v[208:211]\n\t"
      "s_cbranch_vccnz FP11R_%=\n\t"
      "FP11B_%=:\n\t"
      "v_add_f32_e32 v195, v195, v199\n\t"
      "v_add_f32_e32 v184, v184, v192\n\t"
      "v_mfma_f32_16x16x32_bf16 v[212:215], v[140:143], v[60:63], v[212:215]\n\t"
      "global_load_dwordx4 v[140:143], v227, s[56:57] sc1\n\t"
      "v_add_f32_e32 v185, v185, v193\n\t"
      "v_mfma_f32_16x16x32_bf16 v[200:203], v[144:147], v[64:67], v[200:203]\n\t"
      "s_waitcnt vmcnt(5)\n\t"
      "v_cmp_eq_u32_e32 vcc, -1, v148\n\t"
      "v_add_f32_e32 v186, v186, v194\n\t"
      "v_mfma_f32_16x16x32_bf16 v[204:207], v[144:147], v[68:71], v[204:207]\n\t"
      "v_add_f32_e32 v187, v187, v195\n\t"
      "v_add_f32_e32 v112, v112, v184\n\t"
      "v_mfma_f32_16x16x32_bf16 v[208:211], v[144:147], v[72:75], v[208:211]\n\t"
      "s_cbranch_vccnz FP12R_%=\n\t"
      "FP12B_%=:\n\t"
      "v_add_f32_e32 v113, v113, v185\n\t"
      "v_add_f32_e32 v114, v114, v186\n\t"
      "v_mfma_f32_16x16x32_bf16 v[212:215], v[144:147], v[76:79], v[212:215]\n\t"
      "global_load_dwordx4 v[144:147], v228, s[56:57] sc1\n\t"
      "v_add_f32_e32 v115, v115, v187\n\t"
      "v_mfma_f32_16x16x32_bf16 v[200:203], v[148:151], v[80:83], v[200:203]\n\t"
      "s_waitcnt vmcnt(5)\n\t"
      "v_cmp_eq_u32_e32 vcc, -1, v152\n\t"
      "v_cvt_pk_bf16_f32 v116, v112, v113\n\t"
      "v_mfma_f32_16x16x32_bf16 v[204:207], v[148:151], v[84:87], v[204:207]\n\t"
      "v_cvt_pk_bf16_f32 v117, v114, v115\n\t"
      "global_store_dwordx2 v245, v[116:117], s[8:9]\n\t"
      "v_mfma_f32_16x16x32_bf16 v[208:211], v[148:151], v[88:91], v[208:211]\n\t"
      "s_cbranch_vccnz FP13R_%=\n\t"
      "FP13B_%=:\n\t"
      "v_mfma_f32_16x16x32_bf16 v[212:215], v[148:151], v[92:95], v[212:215]\n\t"
      "global_load_dwordx4 v[148:151], v229, s[56:57] sc1\n\t"
      "v_mfma_f32_16x16x32_bf16 v[200:203], v[152:155], v[96:99], v[200:203]\n\t"
      "s_waitcnt vmcnt(6)\n\t"
      "v_cmp_eq_u32_e32 vcc, -1, v128\n\t"
      "v_mfma_f32_16x16x32_bf16 v[204:207], v[152:155], v[100:103], v[204:207]\n\t"
      "v_mfma_f32_16x16x32_bf16 v[208:211], v[152:155], v[104:107], v[208:211]\n\t"
      "s_cbranch_vccnz FP14R_%=\n\t"
      "FP14B_%=:\n\t"
      "v_mfma_f32_16x16x32_bf16 v[212:215], v[152:155], v[108:111], v[212:215]\n\t"
      "global_load_dwordx4 v[152:155], v230, s[56:57] sc1\n\t"
      "s_nop 0\n\t"
      "v_cmp_u_f32_e32 vcc, v200, v201\n\t"
      "v_cmp_u_f32_e64 s[48:49], v202, v203\n\t"
      "s_nop 0\n\t"
      "s_or_b64 vcc, vcc, s[48:49]\n\t"
      "s_cbranch_scc1 NP1R_%=\n\t"
      "NP1B_%=:\n\t"
      "ds_write_b128 v243, v[200:203] offset:32768\n\t"
      "ds_write_b128 v220, v[204:207] offset:32768\n\t"
      "ds_write_b128 v221, v[208:211] offset:32768\n\t"
      "ds_write_b128 v222, v[212:215] offset:32768\n\t"
      "s_waitcnt lgkmcnt(0)\n\t"
      "s_barrier\n\t"
      "ds_read_b128 v[112:115], v244 offset:32768\n\t"
      "ds_read_b128 v[116:119], v244 offset:36864\n\t"
      "ds_read_b128 v[120:123], v244 offset:40960\n\t"
      "ds_read_b128 v[124:127], v244 offset:45056\n\t"
      "ds_read_b128 v[200:203], v244 offset:49152\n\t"
      "ds_read_b128 v[204:207], v244 offset:53248\n\t"
      "ds_read_b128 v[208:211], v244 offset:57344\n\t"
      "ds_read_b128 v[212:215], v244 offset:61440\n\t"
      "v_mfma_f32_16x16x32_bf16 v[184:187], v[128:131], v[0:3], v[164:167]\n\t"
      "s_waitcnt vmcnt(6)\n\t"
      "v_cmp_eq_u32_e32 vcc, -1, v132\n\t"
      "v_mfma_f32_16x16x32_bf16 v[188:191], v[128:131], v[4:7], 0\n\t"
      "s_waitcnt lgkmcnt(6)\n\t"
      "v_add_f32_e32 v112, v112, v116\n\t"
      "v_mfma_f32_16x16x32_bf16 v[192:195], v[128:131], v[8:11], 0\n\t"
      "s_cbranch_vccnz FP15R_%=\n\t"
      "FP15B_%=:\n\t"
      "v_add_f32_e32 v113, v113, v117\n\t"
      "v_add_f32_e32 v114, v114, v118\n\t"
      "v_mfma_f32_16x16x32_bf16 v[196:199], v[128:131], v[12:15], 0\n\t"
      "global_load_dwordx4 v[128:131], v224, s[58:59] sc1\n\t"
      "v_add_f32_e32 v115, v115, v119\n\t"
      "v_mfma_f32_16x16x32_bf16 v[184:187], v[132:135], v[16:19], v[184:187]\n\t"
      "s_waitcnt vmcnt(6)\n\t"
      "v_cmp_eq_u32_e32 vcc, -1, v136\n\t"
      "s_waitcnt lgkmcnt(4)\n\t"
      "v_add_f32_e32 v120, v120, v124\n\t"
      "v_mfma_f32_16x16x32_bf16 v[188:191], v[132:135], v[20:23], v[188:191]\n\t"
      "v_add_f32_e32 v121, v121, v125\n\t"
      "v_add_f32_e32 v122, v122, v126\n\t"
      "v_mfma_f32_16x16x32_bf16 v[192:195], v[132:135], v[24:27], v[192:195]\n\t"
      "s_cbranch_vccnz FP16R_%=\n\t"
      "FP16B_%=:\n\t"
      "v_add_f32_e32 v123, v123, v127\n\t"
      "s_waitcnt lgkmcnt(2)\n\t"
      "v_add_f32_e32 v200, v200, v204\n\t"
      "v_mfma_f32_16x16x32_bf16 v[196:199], v[132:135], v[28:31], v[196:199]\n\t"
      "global_load_dwordx4 v[132:135], v225, s[58:59] sc1\n\t"
      "v_add_f32_e32 v201, v201, v205\n\t"
      "v_mfma_f32_16x16x32_bf16 v[184:187], v[136:139], v[32:35], v[184:187]\n\t"
      "s_waitcnt vmcnt(6)\n\t"
      "v_cmp_eq_u32_e32 vcc, -1, v140\n\t"
      "v_add_f32_e32 v202, v202, v206\n\t"
      "v_mfma_f32_16x16x32_bf16 v[188:191], v[136:139], v[36:39], v[188:191]\n\t"
      "v_add_f32_e32 v203, v203, v207\n\t"
      "v_add_f32_e32 v112, v112, v120\n\t"
      "v_mfma_f32_16x16x32_bf16 v[192:195], v[136:139], v[40:43], v[192:195]\n\t"
      "s_cbranch_vccnz FP17R_%=\n\t"
      "FP17B_%=:\n\t"
      "v_add_f32_e32 v113, v113, v121\n\t"
      "v_add_f32_e32 v114, v114, v122\n\t"
      "v_mfma_f32_16x16x32_bf16 v[196:199], v[136:139], v[44:47], v[196:199]\n\t"
      "global_load_dwordx4 v[136:139], v226, s[58:59] sc1\n\t"
      "v_add_f32_e32 v115, v115, v123\n\t"
      "v_mfma_f32_16x16x32_bf16 v[184:187], v[140:143], v[48:51], v[184:187]\n\t"
      "s_waitcnt vmcnt(6)\n\t"
      "v_cmp_eq_u32_e32 vcc, -1, v144\n\t"
      "s_waitcnt lgkmcnt(0)\n\t"
      "v_add_f32_e32 v208, v208, v212\n\t"
      "v_mfma_f32_16x16x32_bf16 v[188:191], v[140:143], v[52:55], v[188:191]\n\t"
      "v_add_f32_e32 v209, v209, v213\n\t"
      "v_add_f32_e32 v210, v210, v214\n\t"
      "v_mfma_f32_16x16x32_bf16 v[192:195], v[140:143], v[56:59], v[192:195]\n\t"
      "s_cbranch_vccnz FP18R_%=\n\t"
      "FP18B_%=:\n\t"
      "v_add_f32_e32 v211, v211, v215\n\t"
      "v_add_f32_e32 v200, v200, v208\n\t"
      "v_mfma_f32_16x16x32_bf16 v[196:199], v[140:143], v[60:63], v[196:199]\n\t"
      "global_load_dwordx4 v[140:143], v227, s[58:59] sc1\n\t"
      "v_add_f32_e32 v201, v201, v209\n\t"
      "v_mfma_f32_16x16x32_bf16 v[184:187], v[144:147], v[64:67], v[184:187]\n\t"
      "s_waitcnt vmcnt(5)\n\t"
      "v_cmp_eq_u32_e32 vcc, -1, v148\n\t"
      "v_add_f32_e32 v202, v202, v210\n\t"
      "v_mfma_f32_16x16x32_bf16 v[188:191], v[144:147], v[68:71], v[188:191]\n\t"
      "v_add_f32_e32 v203, v203, v211\n\t"
      "v_add_f32_e32 v112, v112, v200\n\t"
      "v_mfma_f32_16x16x32_bf16 v[192:195], v[144:147], v[72:75], v[192:195]\n\t"
      "s_cbranch_vccnz FP19R_%=\n\t"
      "FP19B_%=:\n\t"
      "v_add_f32_e32 v113, v113, v201\n\t"
      "v_add_f32_e32 v114, v114, v202\n\t"
      "v_mfma_f32_16x16x32_bf16 v[196:199], v[144:147], v[76:79], v[196:199]\n\t"
      "global_load_dwordx4 v[144:147], v228, s[58:59] sc1\n\t"
      "v_add_f32_e32 v115, v115, v203\n\t"
      "v_mfma_f32_16x16x32_bf16 v[184:187], v[148:151], v[80:83], v[184:187]\n\t"
      "s_waitcnt vmcnt(5)\n\t"
      "v_cmp_eq_u32_e32 vcc, -1, v152\n\t"
      "v_cvt_pk_bf16_f32 v116, v112, v113\n\t"
      "v_mfma_f32_16x16x32_bf16 v[188:191], v[148:151], v[84:87], v[188:191]\n\t"
      "v_cvt_pk_bf16_f32 v117, v114, v115\n\t"
      "global_store_dwordx2 v245, v[116:117], s[8:9] offset:32\n\t"
      "v_mfma_f32_16x16x32_bf16 v[192:195], v[148:151], v[88:91], v[192:195]\n\t"
      "s_cbranch_vccnz FP20R_%=\n\t"
      "FP20B_%=:\n\t"
      "v_mfma_f32_16x16x32_bf16 v[196:199], v[148:151], v[92:95], v[196:199]\n\t"
      "global_load_dwordx4 v[148:151], v229, s[58:59] sc1\n\t"
      "v_mfma_f32_16x16x32_bf16 v[184:187], v[152:155], v[96:99], v[184:187]\n\t"
      "s_waitcnt vmcnt(6)\n\t"
      "v_cmp_eq_u32_e32 vcc, -1, v128\n\t"
      "v_mfma_f32_16x16x32_bf16 v[188:191], v[152:155], v[100:103], v[188:191]\n\t"
      "v_mfma_f32_16x16x32_bf16 v[192:195], v[152:155], v[104:107], v[192:195]\n\t"
      "s_cbranch_vccnz FP21R_%=\n\t"
      "FP21B_%=:\n\t"
      "v_mfma_f32_16x16x32_bf16 v[196:199], v[152:155], v[108:111], v[196:199]\n\t"
      "global_load_dwordx4 v[152:155], v230, s[58:59] sc1\n\t"
      "s_nop 0\n\t"
      "v_cmp_u_f32_e32 vcc, v184, v185\n\t"
      "v_cmp_u_f32_e64 s[48:49], v186, v187\n\t"
      "s_nop 0\n\t"
      "s_or_b64 vcc, vcc, s[48:49]\n\t"
      "s_cbranch_scc1 NP2R_%=\n\t"
      "NP2B_%=:\n\t"
      "ds_write_b128 v243, v[184:187]\n\t"
      "ds_write_b128 v220, v[188:191]\n\t"
      "ds_write_b128 v221, v[192:195]\n\t"
      "ds_write_b128 v222, v[196:199]\n\t"
      "s_waitcnt lgkmcnt(0)\n\t"
      "s_barrier\n\t"
      "ds_read_b128 v[112:115], v244\n\t"
      "ds_read_b128 v[116:119], v244 offset:4096\n\t"
      "ds_read_b128 v[120:123], v244 offset:8192\n\t"
      "ds_read_b128 v[124:127], v244 offset:12288\n\t"
      "ds_read_b128 v[184:187], v244 offset:16384\n\t"
      "ds_read_b128 v[188:191], v244 offset:20480\n\t"
      "ds_read_b128 v[192:195], v244 offset:24576\n\t"
      "ds_read_b128 v[196:199], v244 offset:28672\n\t"
      "v_mfma_f32_16x16x32_bf16 v[200:203], v[128:131], v[0:3], v[168:171]\n\t"
      "s_waitcnt vmcnt(6)\n\t"
      "v_cmp_eq_u32_e32 vcc, -1, v132\n\t"
      "v_mfma_f32_16x16x32_bf16 v[204:207], v[128:131], v[4:7], 0\n\t"
      "s_waitcnt lgkmcnt(6)\n\t"
      "v_add_f32_e32 v112, v112, v116\n\t"
      "v_mfma_f32_16x16x32_bf16 v[208:211], v[128:131], v[8:11], 0\n\t"
      "s_cbranch_vccnz FP22R_%=\n\t"
      "FP22B_%=:\n\t"
      "v_add_f32_e32 v113, v113, v117\n\t"
      "v_add_f32_e32 v114, v114, v118\n\t"
      "v_mfma_f32_16x16x32_bf16 v[212:215], v[128:131], v[12:15], 0\n\t"
      "global_load_dwordx4 v[128:131], v224, s[60:61] sc1\n\t"
      "v_add_f32_e32 v115, v115, v119\n\t"
      "v_mfma_f32_16x16x32_bf16 v[200:203], v[132:135], v[16:19], v[200:203]\n\t"
      "s_waitcnt vmcnt(6)\n\t"
      "v_cmp_eq_u32_e32 vcc, -1, v136\n\t"
      "s_waitcnt lgkmcnt(4)\n\t"
      "v_add_f32_e32 v120, v120, v124\n\t"
      "v_mfma_f32_16x16x32_bf16 v[204:207], v[132:135], v[20:23], v[204:207]\n\t"
      "v_add_f32_e32 v121, v121, v125\n\t"
      "v_add_f32_e32 v122, v122, v126\n\t"
      "v_mfma_f32_16x16x32_bf16 v[208:211], v[132:135], v[24:27], v[208:211]\n\t"
      "s_cbranch_vccnz FP23R_%=\n\t"
      "FP23B_%=:\n\t"
      "v_add_f32_e32 v123, v123, v127\n\t"
      "s_waitcnt lgkmcnt(2)\n\t"
      "v_add_f32_e32 v184, v184, v188\n\t"
      "v_mfma_f32_16x16x32_bf16 v[212:215], v[132:135], v[28:31], v[212:215]\n\t"
      "global_load_dwordx4 v[132:135], v225, s[60:61] sc1\n\t"
      "v_add_f32_e32 v185, v185, v189\n\t"
      "v_mfma_f32_16x16x32_bf16 v[200:203], v[136:139], v[32:35], v[200:203]\n\t"
      "s_waitcnt vmcnt(6)\n\t"
      "v_cmp_eq_u32_e32 vcc, -1, v140\n\t"
      "v_add_f32_e32 v186, v186, v190\n\t"
      "v_mfma_f32_16x16x32_bf16 v[204:207], v[136:139], v[36:39], v[204:207]\n\t"
      "v_add_f32_e32 v187, v187, v191\n\t"
      "v_add_f32_e32 v112, v112, v120\n\t"
      "v_mfma_f32_16x16x32_bf16 v[208:211], v[136:139], v[40:43], v[208:211]\n\t"
      "s_cbranch_vccnz FP24R_%=\n\t"
      "FP24B_%=:\n\t"
      "v_add_f32_e32 v113, v113, v121\n\t"
      "v_add_f32_e32 v114, v114, v122\n\t"
      "v_mfma_f32_16x16x32_bf16 v[212:215], v[136:139], v[44:47], v[212:215]\n\t"
      "global_load_dwordx4 v[136:139], v226, s[60:61] sc1\n\t"
      "v_add_f32_e32 v115, v115, v123\n\t"
      "v_mfma_f32_16x16x32_bf16 v[200:203], v[140:143], v[48:51], v[200:203]\n\t"
      "s_waitcnt vmcnt(6)\n\t"
      "v_cmp_eq_u32_e32 vcc, -1, v144\n\t"
      "s_waitcnt lgkmcnt(0)\n\t"
      "v_add_f32_e32 v192, v192, v196\n\t"
      "v_mfma_f32_16x16x32_bf16 v[204:207], v[140:143], v[52:55], v[204:207]\n\t"
      "v_add_f32_e32 v193, v193, v197\n\t"
      "v_add_f32_e32 v194, v194, v198\n\t"
      "v_mfma_f32_16x16x32_bf16 v[208:211], v[140:143], v[56:59], v[208:211]\n\t"
      "s_cbranch_vccnz FP25R_%=\n\t"
      "FP25B_%=:\n\t"
      "v_add_f32_e32 v195, v195, v199\n\t"
      "v_add_f32_e32 v184, v184, v192\n\t"
      "v_mfma_f32_16x16x32_bf16 v[212:215], v[140:143], v[60:63], v[212:215]\n\t"
      "global_load_dwordx4 v[140:143], v227, s[60:61] sc1\n\t"
      "v_add_f32_e32 v185, v185, v193\n\t"
      "v_mfma_f32_16x16x32_bf16 v[200:203], v[144:147], v[64:67], v[200:203]\n\t"
      "s_waitcnt vmcnt(5)\n\t"
      "v_cmp_eq_u32_e32 vcc, -1, v148\n\t"
      "v_add_f32_e32 v186, v186, v194\n\t"
      "v_mfma_f32_16x16x32_bf16 v[204:207], v[144:147], v[68:71], v[204:207]\n\t"
      "v_add_f32_e32 v187, v187, v195\n\t"
      "v_add_f32_e32 v112, v112, v184\n\t"
      "v_mfma_f32_16x16x32_bf16 v[208:211], v[144:147], v[72:75], v[208:211]\n\t"
      "s_cbranch_vccnz FP26R_%=\n\t"
      "FP26B_%=:\n\t"
      "v_add_f32_e32 v113, v113, v185\n\t"
      "v_add_f32_e32 v114, v114, v186\n\t"
      "v_mfma_f32_16x16x32_bf16 v[212:215], v[144:147], v[76:79], v[212:215]\n\t"
      "global_load_dwordx4 v[144:147], v228, s[60:61] sc1\n\t"
      "v_add_f32_e32 v115, v115, v187\n\t"
      "v_mfma_f32_16x16x32_bf16 v[200:203], v[148:151], v[80:83], v[200:203]\n\t"
      "s_waitcnt vmcnt(5)\n\t"
      "v_cmp_eq_u32_e32 vcc, -1, v152\n\t"
      "v_cvt_pk_bf16_f32 v116, v112, v113\n\t"
      "v_mfma_f32_16x16x32_bf16 v[204:207], v[148:151], v[84:87], v[204:207]\n\t"
      "v_cvt_pk_bf16_f32 v117, v114, v115\n\t"
      "global_store_dwordx2 v245, v[116:117], s[66:67]\n\t"
      "v_mfma_f32_16x16x32_bf16 v[208:211], v[148:151], v[88:91], v[208:211]\n\t"
      "s_cbranch_vccnz FP27R_%=\n\t"
      "FP27B_%=:\n\t"
      "v_mfma_f32_16x16x32_bf16 v[212:215], v[148:151], v[92:95], v[212:215]\n\t"
      "global_load_dwordx4 v[148:151], v229, s[60:61] sc1\n\t"
      "v_mfma_f32_16x16x32_bf16 v[200:203], v[152:155], v[96:99], v[200:203]\n\t"
      "s_waitcnt vmcnt(6)\n\t"
      "v_cmp_eq_u32_e32 vcc, -1, v128\n\t"
      "v_mfma_f32_16x16x32_bf16 v[204:207], v[152:155], v[100:103], v[204:207]\n\t"
      "v_mfma_f32_16x16x32_bf16 v[208:211], v[152:155], v[104:107], v[208:211]\n\t"
      "s_cbranch_vccnz FP28R_%=\n\t"
      "FP28B_%=:\n\t"
      "v_mfma_f32_16x16x32_bf16 v[212:215], v[152:155], v[108:111], v[212:215]\n\t"
      "global_load_dwordx4 v[152:155], v230, s[60:61] sc1\n\t"
      "s_nop 0\n\t"
      "v_cmp_u_f32_e32 vcc, v200, v201\n\t"
      "v_cmp_u_f32_e64 s[48:49], v202, v203\n\t"
      "s_nop 0\n\t"
      "s_or_b64 vcc, vcc, s[48:49]\n\t"
      "s_cbranch_scc1 NP3R_%=\n\t"
      "NP3B_%=:\n\t"
      "ds_write_b128 v243, v[200:203] offset:32768\n\t"
      "ds_write_b128 v220, v[204:207] offset:32768\n\t"
      "ds_write_b128 v221, v[208:211] offset:32768\n\t"
      "ds_write_b128 v222, v[212:215] offset:32768\n\t"
      "s_waitcnt lgkmcnt(0)\n\t"
      "s_barrier\n\t"
      "ds_read_b128 v[112:115], v244 offset:32768\n\t"
      "ds_read_b128 v[116:119], v244 offset:36864\n\t"
      "ds_read_b128 v[120:123], v244 offset:40960\n\t"
      "ds_read_b128 v[124:127], v244 offset:45056\n\t"
      "ds_read_b128 v[200:203], v244 offset:49152\n\t"
      "ds_read_b128 v[204:207], v244 offset:53248\n\t"
      "ds_read_b128 v[208:211], v244 offset:57344\n\t"
      "ds_read_b128 v[212:215], v244 offset:61440\n\t"
      "v_mfma_f32_16x16x32_bf16 v[184:187], v[128:131], v[0:3], v[172:175]\n\t"
      "s_waitcnt vmcnt(6)\n\t"
      "v_cmp_eq_u32_e32 vcc, -1, v132\n\t"
      "v_mfma_f32_16x16x32_bf16 v[188:191], v[128:131], v[4:7], 0\n\t"
      "s_waitcnt lgkmcnt(6)\n\t"
      "v_add_f32_e32 v112, v112, v116\n\t"
      "v_mfma_f32_16x16x32_bf16 v[192:195], v[128:131], v[8:11], 0\n\t"
      "s_cbranch_vccnz FP29R_%=\n\t"
      "FP29B_%=:\n\t"
      "v_add_f32_e32 v113, v113, v117\n\t"
      "v_add_f32_e32 v114, v114, v118\n\t"
      "v_mfma_f32_16x16x32_bf16 v[196:199], v[128:131], v[12:15], 0\n\t"
      "global_load_dwordx4 v[128:131], v224, s[62:63] sc1\n\t"
      "v_add_f32_e32 v115, v115, v119\n\t"
      "v_mfma_f32_16x16x32_bf16 v[184:187], v[132:135], v[16:19], v[184:187]\n\t"
      "s_waitcnt vmcnt(6)\n\t"
      "v_cmp_eq_u32_e32 vcc, -1, v136\n\t"
      "s_waitcnt lgkmcnt(4)\n\t"
      "v_add_f32_e32 v120, v120, v124\n\t"
      "v_mfma_f32_16x16x32_bf16 v[188:191], v[132:135], v[20:23], v[188:191]\n\t"
      "v_add_f32_e32 v121, v121, v125\n\t"
      "v_add_f32_e32 v122, v122, v126\n\t"
      "v_mfma_f32_16x16x32_bf16 v[192:195], v[132:135], v[24:27], v[192:195]\n\t"
      "s_cbranch_vccnz FP30R_%=\n\t"
      "FP30B_%=:\n\t"
      "v_add_f32_e32 v123, v123, v127\n\t"
      "s_waitcnt lgkmcnt(2)\n\t"
      "v_add_f32_e32 v200, v200, v204\n\t"
      "v_mfma_f32_16x16x32_bf16 v[196:199], v[132:135], v[28:31], v[196:199]\n\t"
      "global_load_dwordx4 v[132:135], v225, s[62:63] sc1\n\t"
      "v_add_f32_e32 v201, v201, v205\n\t"
      "v_mfma_f32_16x16x32_bf16 v[184:187], v[136:139], v[32:35], v[184:187]\n\t"
      "s_waitcnt vmcnt(6)\n\t"
      "v_cmp_eq_u32_e32 vcc, -1, v140\n\t"
      "v_add_f32_e32 v202, v202, v206\n\t"
      "v_mfma_f32_16x16x32_bf16 v[188:191], v[136:139], v[36:39], v[188:191]\n\t"
      "v_add_f32_e32 v203, v203, v207\n\t"
      "v_add_f32_e32 v112, v112, v120\n\t"
      "v_mfma_f32_16x16x32_bf16 v[192:195], v[136:139], v[40:43], v[192:195]\n\t"
      "s_cbranch_vccnz FP31R_%=\n\t"
      "FP31B_%=:\n\t"
      "v_add_f32_e32 v113, v113, v121\n\t"
      "v_add_f32_e32 v114, v114, v122\n\t"
      "v_mfma_f32_16x16x32_bf16 v[196:199], v[136:139], v[44:47], v[196:199]\n\t"
      "global_load_dwordx4 v[136:139], v226, s[62:63] sc1\n\t"
      "v_add_f32_e32 v115, v115, v123\n\t"
      "v_mfma_f32_16x16x32_bf16 v[184:187], v[140:143], v[48:51], v[184:187]\n\t"
      "s_waitcnt vmcnt(6)\n\t"
      "v_cmp_eq_u32_e32 vcc, -1, v144\n\t"
      "s_waitcnt lgkmcnt(0)\n\t"
      "v_add_f32_e32 v208, v208, v212\n\t"
      "v_mfma_f32_16x16x32_bf16 v[188:191], v[140:143], v[52:55], v[188:191]\n\t"
      "v_add_f32_e32 v209, v209, v213\n\t"
      "v_add_f32_e32 v210, v210, v214\n\t"
      "v_mfma_f32_16x16x32_bf16 v[192:195], v[140:143], v[56:59], v[192:195]\n\t"
      "s_cbranch_vccnz FP32R_%=\n\t"
      "FP32B_%=:\n\t"
      "v_add_f32_e32 v211, v211, v215\n\t"
      "v_add_f32_e32 v200, v200, v208\n\t"
      "v_mfma_f32_16x16x32_bf16 v[196:199], v[140:143], v[60:63], v[196:199]\n\t"
      "global_load_dwordx4 v[140:143], v227, s[62:63] sc1\n\t"
      "v_add_f32_e32 v201, v201, v209\n\t"
      "v_mfma_f32_16x16x32_bf16 v[184:187], v[144:147], v[64:67], v[184:187]\n\t"
      "s_waitcnt vmcnt(5)\n\t"
      "v_cmp_eq_u32_e32 vcc, -1, v148\n\t"
      "v_add_f32_e32 v202, v202, v210\n\t"
      "v_mfma_f32_16x16x32_bf16 v[188:191], v[144:147], v[68:71], v[188:191]\n\t"
      "v_add_f32_e32 v203, v203, v211\n\t"
      "v_add_f32_e32 v112, v112, v200\n\t"
      "v_mfma_f32_16x16x32_bf16 v[192:195], v[144:147], v[72:75], v[192:195]\n\t"
      "s_cbranch_vccnz FP33R_%=\n\t"
      "FP33B_%=:\n\t"
      "v_add_f32_e32 v113, v113, v201\n\t"
      "v_add_f32_e32 v114, v114, v202\n\t"
      "v_mfma_f32_16x16x32_bf16 v[196:199], v[144:147], v[76:79], v[196:199]\n\t"
      "global_load_dwordx4 v[144:147], v228, s[62:63] sc1\n\t"
      "v_add_f32_e32 v115, v115, v203\n\t"
      "v_mfma_f32_16x16x32_bf16 v[184:187], v[148:151], v[80:83], v[184:187]\n\t"
      "s_waitcnt vmcnt(5)\n\t"
      "v_cmp_eq_u32_e32 vcc, -1, v152\n\t"
      "v_cvt_pk_bf16_f32 v116, v112, v113\n\t"
      "v_mfma_f32_16x16x32_bf16 v[188:191], v[148:151], v[84:87], v[188:191]\n\t"
      "v_cvt_pk_bf16_f32 v117, v114, v115\n\t"
      "global_store_dwordx2 v245, v[116:117], s[66:67] offset:32\n\t"
      "v_mfma_f32_16x16x32_bf16 v[192:195], v[148:151], v[88:91], v[192:195]\n\t"
      "s_cbranch_vccnz FP34R_%=\n\t"
      "FP34B_%=:\n\t"
      "v_mfma_f32_16x16x32_bf16 v[196:199], v[148:151], v[92:95], v[196:199]\n\t"
      "global_load_dwordx4 v[148:151], v229, s[62:63] sc1\n\t"
      "v_mfma_f32_16x16x32_bf16 v[184:187], v[152:155], v[96:99], v[184:187]\n\t"
      "s_waitcnt vmcnt(6)\n\t"
      "v_cmp_eq_u32_e32 vcc, -1, v128\n\t"
      "v_mfma_f32_16x16x32_bf16 v[188:191], v[152:155], v[100:103], v[188:191]\n\t"
      "v_mfma_f32_16x16x32_bf16 v[192:195], v[152:155], v[104:107], v[192:195]\n\t"
      "s_cbranch_vccnz FP35R_%=\n\t"
      "FP35B_%=:\n\t"
      "v_mfma_f32_16x16x32_bf16 v[196:199], v[152:155], v[108:111], v[196:199]\n\t"
      "global_load_dwordx4 v[152:155], v230, s[62:63] sc1\n\t"
      "s_nop 0\n\t"
      "v_cmp_u_f32_e32 vcc, v184, v185\n\t"
      "v_cmp_u_f32_e64 s[48:49], v186, v187\n\t"
      "s_nop 0\n\t"
      "s_or_b64 vcc, vcc, s[48:49]\n\t"
      "s_cbranch_scc1 NP4R_%=\n\t"
      "NP4B_%=:\n\t"
      "ds_write_b128 v243, v[184:187]\n\t"
      "ds_write_b128 v220, v[188:191]\n\t"
      "ds_write_b128 v221, v[192:195]\n\t"
      "ds_write_b128 v222, v[196:199]\n\t"
      "s_waitcnt lgkmcnt(0)\n\t"
      "s_barrier\n\t"
      "ds_read_b128 v[112:115], v244\n\t"
      "ds_read_b128 v[116:119], v244 offset:4096\n\t"
      "ds_read_b128 v[120:123], v244 offset:8192\n\t"
      "ds_read_b128 v[124:127], v244 offset:12288\n\t"
      "ds_read_b128 v[184:187], v244 offset:16384\n\t"
      "ds_read_b128 v[188:191], v244 offset:20480\n\t"
      "ds_read_b128 v[192:195], v244 offset:24576\n\t"
      "ds_read_b128 v[196:199], v244 offset:28672\n\t"
      "v_mfma_f32_16x16x32_bf16 v[200:203], v[128:131], v[0:3], v[176:179]\n\t"
      "s_waitcnt vmcnt(6)\n\t"
      "v_cmp_eq_u32_e32 vcc, -1, v132\n\t"
      "v_mfma_f32_16x16x32_bf16 v[204:207], v[128:131], v[4:7], 0\n\t"
      "s_waitcnt lgkmcnt(6)\n\t"
      "v_add_f32_e32 v112, v112, v116\n\t"
      "v_mfma_f32_16x16x32_bf16 v[208:211], v[128:131], v[8:11], 0\n\t"
      "s_cbranch_vccnz FP36R_%=\n\t"
      "FP36B_%=:\n\t"
      "v_add_f32_e32 v113, v113, v117\n\t"
      "v_add_f32_e32 v114, v114, v118\n\t"
      "v_mfma_f32_16x16x32_bf16 v[212:215], v[128:131], v[12:15], 0\n\t"
      "global_load_dwordx4 v[128:131], v224, s[64:65] sc1\n\t"
      "v_add_f32_e32 v115, v115, v119\n\t"
      "v_mfma_f32_16x16x32_bf16 v[200:203], v[132:135], v[16:19], v[200:203]\n\t"
      "s_waitcnt vmcnt(6)\n\t"
      "v_cmp_eq_u32_e32 vcc, -1, v136\n\t"
      "s_waitcnt lgkmcnt(4)\n\t"
      "v_add_f32_e32 v120, v120, v124\n\t"
      "v_mfma_f32_16x16x32_bf16 v[204:207], v[132:135], v[20:23], v[204:207]\n\t"
      "v_add_f32_e32 v121, v121, v125\n\t"
      "v_add_f32_e32 v122, v122, v126\n\t"
      "v_mfma_f32_16x16x32_bf16 v[208:211], v[132:135], v[24:27], v[208:211]\n\t"
      "s_cbranch_vccnz FP37R_%=\n\t"
      "FP37B_%=:\n\t"
      "v_add_f32_e32 v123, v123, v127\n\t"
      "s_waitcnt lgkmcnt(2)\n\t"
      "v_add_f32_e32 v184, v184, v188\n\t"
      "v_mfma_f32_16x16x32_bf16 v[212:215], v[132:135], v[28:31], v[212:215]\n\t"
      "global_load_dwordx4 v[132:135], v225, s[64:65] sc1\n\t"
      "v_add_f32_e32 v185, v185, v189\n\t"
      "v_mfma_f32_16x16x32_bf16 v[200:203], v[136:139], v[32:35], v[200:203]\n\t"
      "s_waitcnt vmcnt(6)\n\t"
      "v_cmp_eq_u32_e32 vcc, -1, v140\n\t"
      "v_add_f32_e32 v186, v186, v190\n\t"
      "v_mfma_f32_16x16x32_bf16 v[204:207], v[136:139], v[36:39], v[204:207]\n\t"
      "v_add_f32_e32 v187, v187, v191\n\t"
      "v_add_f32_e32 v112, v112, v120\n\t"
      "v_mfma_f32_16x16x32_bf16 v[208:211], v[136:139], v[40:43], v[208:211]\n\t"
      "s_cbranch_vccnz FP38R_%=\n\t"
      "FP38B_%=:\n\t"
      "v_add_f32_e32 v113, v113, v121\n\t"
      "v_add_f32_e32 v114, v114, v122\n\t"
      "v_mfma_f32_16x16x32_bf16 v[212:215], v[136:139], v[44:47], v[212:215]\n\t"
      "global_load_dwordx4 v[136:139], v226, s[64:65] sc1\n\t"
      "v_add_f32_e32 v115, v115, v123\n\t"
      "v_mfma_f32_16x16x32_bf16 v[200:203], v[140:143], v[48:51], v[200:203]\n\t"
      "s_waitcnt vmcnt(6)\n\t"
      "v_cmp_eq_u32_e32 vcc, -1, v144\n\t"
      "s_waitcnt lgkmcnt(0)\n\t"
      "v_add_f32_e32 v192, v192, v196\n\t"
      "v_mfma_f32_16x16x32_bf16 v[204:207], v[140:143], v[52:55], v[204:207]\n\t"
      "v_add_f32_e32 v193, v193, v197\n\t"
      "v_add_f32_e32 v194, v194, v198\n\t"
      "v_mfma_f32_16x16x32_bf16 v[208:211], v[140:143], v[56:59], v[208:211]\n\t"
      "s_cbranch_vccnz FP39R_%=\n\t"
      "FP39B_%=:\n\t"
      "v_add_f32_e32 v195, v195, v199\n\t"
      "v_add_f32_e32 v184, v184, v192\n\t"
      "v_mfma_f32_16x16x32_bf16 v[212:215], v[140:143], v[60:63], v[212:215]\n\t"
      "global_load_dwordx4 v[140:143], v227, s[64:65] sc1\n\t"
      "v_add_f32_e32 v185, v185, v193\n\t"
      "v_mfma_f32_16x16x32_bf16 v[200:203], v[144:147], v[64:67], v[200:203]\n\t"
      "s_waitcnt vmcnt(5)\n\t"
      "v_cmp_eq_u32_e32 vcc, -1, v148\n\t"
      "v_add_f32_e32 v186, v186, v194\n\t"
      "v_mfma_f32_16x16x32_bf16 v[204:207], v[144:147], v[68:71], v[204:207]\n\t"
      "v_add_f32_e32 v187, v187, v195\n\t"
      "v_add_f32_e32 v112, v112, v184\n\t"
      "v_mfma_f32_16x16x32_bf16 v[208:211], v[144:147], v[72:75], v[208:211]\n\t"
      "s_cbranch_vccnz FP40R_%=\n\t"
      "FP40B_%=:\n\t"
      "v_add_f32_e32 v113, v113, v185\n\t"
      "v_add_f32_e32 v114, v114, v186\n\t"
      "v_mfma_f32_16x16x32_bf16 v[212:215], v[144:147], v[76:79], v[212:215]\n\t"
      "global_load_dwordx4 v[144:147], v228, s[64:65] sc1\n\t"
      "v_add_f32_e32 v115, v115, v187\n\t"
      "v_mfma_f32_16x16x32_bf16 v[200:203], v[148:151], v[80:83], v[200:203]\n\t"
      "s_waitcnt vmcnt(5)\n\t"
      "v_cmp_eq_u32_e32 vcc, -1, v152\n\t"
      "v_cvt_pk_bf16_f32 v116, v112, v113\n\t"
      "v_mfma_f32_16x16x32_bf16 v[204:207], v[148:151], v[84:87], v[204:207]\n\t"
      "v_cvt_pk_bf16_f32 v117, v114, v115\n\t"
      "global_store_dwordx2 v245, v[116:117], s[8:9] offset:64\n\t"
      "v_mfma_f32_16x16x32_bf16 v[208:211], v[148:151], v[88:91], v[208:211]\n\t"
      "s_cbranch_vccnz FP41R_%=\n\t"
      "FP41B_%=:\n\t"
      "v_mfma_f32_16x16x32_bf16 v[212:215], v[148:151], v[92:95], v[212:215]\n\t"
      "global_load_dwordx4 v[148:151], v229, s[64:65] sc1\n\t"
      "v_mfma_f32_16x16x32_bf16 v[200:203], v[152:155], v[96:99], v[200:203]\n\t"
      "s_waitcnt vmcnt(6)\n\t"
      "v_cmp_eq_u32_e32 vcc, -1, v128\n\t"
      "v_mfma_f32_16x16x32_bf16 v[204:207], v[152:155], v[100:103], v[204:207]\n\t"
      "v_mfma_f32_16x16x32_bf16 v[208:211], v[152:155], v[104:107], v[208:211]\n\t"
      "s_cbranch_vccnz FP42R_%=\n\t"
      "FP42B_%=:\n\t"
      "v_mfma_f32_16x16x32_bf16 v[212:215], v[152:155], v[108:111], v[212:215]\n\t"
      "global_load_dwordx4 v[152:155], v230, s[64:65] sc1\n\t"
      "s_nop 0\n\t"
      "v_cmp_u_f32_e32 vcc, v200, v201\n\t"
      "v_cmp_u_f32_e64 s[48:49], v202, v203\n\t"
      "s_nop 0\n\t"
      "s_or_b64 vcc, vcc, s[48:49]\n\t"
      "s_cbranch_scc1 NP5R_%=\n\t"
      "NP5B_%=:\n\t"
      "ds_write_b128 v243, v[200:203] offset:32768\n\t"
      "ds_write_b128 v220, v[204:207] offset:32768\n\t"
      "ds_write_b128 v221, v[208:211] offset:32768\n\t"
      "ds_write_b128 v222, v[212:215] offset:32768\n\t"
      "s_waitcnt lgkmcnt(0)\n\t"
      "s_barrier\n\t"
      "ds_read_b128 v[112:115], v244 offset:32768\n\t"
      "ds_read_b128 v[116:119], v244 offset:36864\n\t"
      "ds_read_b128 v[120:123], v244 offset:40960\n\t"
      "ds_read_b128 v[124:127], v244 offset:45056\n\t"
      "ds_read_b128 v[200:203], v244 offset:49152\n\t"
      "ds_read_b128 v[204:207], v244 offset:53248\n\t"
      "ds_read_b128 v[208:211], v244 offset:57344\n\t"
      "ds_read_b128 v[212:215], v244 offset:61440\n\t"
      "v_mfma_f32_16x16x32_bf16 v[184:187], v[128:131], v[0:3], v[180:183]\n\t"
      "s_waitcnt vmcnt(6)\n\t"
      "v_cmp_eq_u32_e32 vcc, -1, v132\n\t"
      "v_mfma_f32_16x16x32_bf16 v[188:191], v[128:131], v[4:7], 0\n\t"
      "v_mfma_f32_16x16x32_bf16 v[192:195], v[128:131], v[8:11], 0\n\t"
      "s_cbranch_vccnz FP43R_%=\n\t"
      "FP43B_%=:\n\t"
      "v_mfma_f32_16x16x32_bf16 v[196:199], v[128:131], v[12:15], 0\n\t"
      "v_mfma_f32_16x16x32_bf16 v[184:187], v[132:135], v[16:19], v[184:187]\n\t"
      "s_waitcnt vmcnt(5)\n\t"
      "v_cmp_eq_u32_e32 vcc, -1, v136\n\t"
      "s_waitcnt lgkmcnt(6)\n\t"
      "v_add_f32_e32 v112, v112, v116\n\t"
      "v_mfma_f32_16x16x32_bf16 v[188:191], v[132:135], v[20:23], v[188:191]\n\t"
      "v_add_f32_e32 v113, v113, v117\n\t"
      "v_add_f32_e32 v114, v114, v118\n\t"
      "v_mfma_f32_16x16x32_bf16 v[192:195], v[132:135], v[24:27], v[192:195]\n\t"
      "s_cbranch_vccnz FP44R_%=\n\t"
      "FP44B_%=:\n\t"
      "v_add_f32_e32 v115, v115, v119\n\t"
      "s_waitcnt lgkmcnt(4)\n\t"
      "v_add_f32_e32 v120, v120, v124\n\t"
      "v_mfma_f32_16x16x32_bf16 v[196:199], v[132:135], v[28:31], v[196:199]\n\t"
      "v_add_f32_e32 v121, v121, v125\n\t"
      "v_add_f32_e32 v122, v122, v126\n\t"
      "v_mfma_f32_16x16x32_bf16 v[184:187], v[136:139], v[32:35], v[184:187]\n\t"
      "s_waitcnt vmcnt(4)\n\t"
      "v_cmp_eq_u32_e32 vcc, -1, v140\n\t"
      "v_add_f32_e32 v123, v123, v127\n\t"
      "v_mfma_f32_16x16x32_bf16 v[188:191], v[136:139], v[36:39], v[188:191]\n\t"
      "s_waitcnt lgkmcnt(2)\n\t"
      "v_add_f32_e32 v200, v200, v204\n\t"
      "v_add_f32_e32 v201, v201, v205\n\t"
      "v_mfma_f32_16x16x32_bf16 v[192:195], v[136:139], v[40:43], v[192:195]\n\t"
      "s_cbranch_vccnz FP45R_%=\n\t"
      "FP45B_%=:\n\t"
      "v_add_f32_e32 v202, v202, v206\n\t"
      "v_add_f32_e32 v203, v203, v207\n\t"
      "v_mfma_f32_16x16x32_bf16 v[196:199], v[136:139], v[44:47], v[196:199]\n\t"
      "v_add_f32_e32 v112, v112, v120\n\t"
      "v_add_f32_e32 v113, v113, v121\n\t"
      "v_mfma_f32_16x16x32_bf16 v[184:187], v[140:143], v[48:51], v[184:187]\n\t"
      "s_waitcnt vmcnt(3)\n\t"
      "v_cmp_eq_u32_e32 vcc, -1, v144\n\t"
      "v_add_f32_e32 v114, v114, v122\n\t"
      "v_mfma_f32_16x16x32_bf16 v[188:191], v[140:143], v[52:55], v[188:191]\n\t"
      "v_add_f32_e32 v115, v115, v123\n\t"
      "s_waitcnt lgkmcnt(0)\n\t"
      "v_add_f32_e32 v208, v208, v212\n\t"
      "v_mfma_f32_16x16x32_bf16 v[192:195], v[140:143], v[56:59], v[192:195]\n\t"
      "s_cbranch_vccnz FP46R_%=\n\t"
      "FP46B_%=:\n\t"
      "v_add_f32_e32 v209, v209, v213\n\t"
      "v_add_f32_e32 v210, v210, v214\n\t"
      "v_mfma_f32_16x16x32_bf16 v[196:199], v[140:143], v[60:63], v[196:199]\n\t"
      "v_add_f32_e32 v211, v211, v215\n\t"
      "v_add_f32_e32 v200, v200, v208\n\t"
      "v_mfma_f32_16x16x32_bf16 v[184:187], v[144:147], v[64:67], v[184:187]\n\t"
      "s_waitcnt vmcnt(1)\n\t"
      "v_cmp_eq_u32_e32 vcc, -1, v148\n\t"
      "v_add_f32_e32 v201, v201, v209\n\t"
      "v_mfma_f32_16x16x32_bf16 v[188:191], v[144:147], v[68:71], v[188:191]\n\t"
      "v_add_f32_e32 v202, v202, v210\n\t"
      "v_add_f32_e32 v203, v203, v211\n\t"
      "v_mfma_f32_16x16x32_bf16 v[192:195], v[144:147], v[72:75], v[192:195]\n\t"
      "s_cbranch_vccnz FP47R_%=\n\t"
      "FP47B_%=:\n\t"
      "v_add_f32_e32 v112, v112, v200\n\t"
      "v_add_f32_e32 v113, v113, v201\n\t"
      "v_mfma_f32_16x16x32_bf16 v[196:199], v[144:147], v[76:79], v[196:199]\n\t"
      "v_add_f32_e32 v114, v114, v202\n\t"
      "v_add_f32_e32 v115, v115, v203\n\t"
      "v_mfma_f32_16x16x32_bf16 v[184:187], v[148:151], v[80:83], v[184:187]\n\t"
      "s_waitcnt vmcnt(0)\n\t"
      "v_cmp_eq_u32_e32 vcc, -1, v152\n\t"
      "v_cvt_pk_bf16_f32 v116, v112, v113\n\t"
      "v_mfma_f32_16x16x32_bf16 v[188:191], v[148:151], v[84:87], v[188:191]\n\t"
      "v_cvt_pk_bf16_f32 v117, v114, v115\n\t"
      "global_store_dwordx2 v245, v[116:117], s[8:9] offset:96\n\t"
      "v_mfma_f32_16x16x32_bf16 v[192:195], v[148:151], v[88:91], v[192:195]\n\t"
      "s_cbranch_vccnz FP48R_%=\n\t"
      "FP48B_%=:\n\t"
      "v_mfma_f32_16x16x32_bf16 v[196:199], v[148:151], v[92:95], v[196:199]\n\t"
      "v_mfma_f32_16x16x32_bf16 v[184:187], v[152:155], v[96:99], v[184:187]\n\t"
      "v_mfma_f32_16x16x32_bf16 v[188:191], v[152:155], v[100:103], v[188:191]\n\t"
      "v_mfma_f32_16x16x32_bf16 v[192:195], v[152:155], v[104:107], v[192:195]\n\t"
      "v_mfma_f32_16x16x32_bf16 v[196:199], v[152:155], v[108:111], v[196:199]\n\t"
      "s_nop 4\n\t"
      "v_cmp_u_f32_e32 vcc, v184, v185\n\t"
      "v_cmp_u_f32_e64 s[48:49], v186, v187\n\t"
      "s_nop 0\n\t"
      "s_or_b64 vcc, vcc, s[48:49]\n\t"
      "s_cbranch_scc1 NP6R_%=\n\t"
      "NP6B_%=:\n\t"
      "ds_write_b128 v243, v[184:187]\n\t"
      "ds_write_b128 v220, v[188:191]\n\t"
      "ds_write_b128 v221, v[192:195]\n\t"
      "ds_write_b128 v222, v[196:199]\n\t"
      "s_waitcnt lgkmcnt(0)\n\t"
      "s_barrier\n\t"
      "ds_read_b128 v[112:115], v244\n\t"
      "ds_read_b128 v[116:119], v244 offset:4096\n\t"
      "ds_read_b128 v[120:123], v244 offset:8192\n\t"
      "ds_read_b128 v[124:127], v244 offset:12288\n\t"
      "ds_read_b128 v[184:187], v244 offset:16384\n\t"
      "ds_read_b128 v[188:191], v244 offset:20480\n\t"
      "ds_read_b128 v[192:195], v244 offset:24576\n\t"
      "ds_read_b128 v[196:199], v244 offset:28672\n\t"
      "s_waitcnt lgkmcnt(6)\n\t"
      "v_add_f32_e32 v112, v112, v116\n\t"
      "v_add_f32_e32 v113, v113, v117\n\t"
      "v_add_f32_e32 v114, v114, v118\n\t"
      "v_add_f32_e32 v115, v115, v119\n\t"
      "s_waitcnt lgkmcnt(4)\n\t"
      "v_add_f32_e32 v120, v120, v124\n\t"
      "v_add_f32_e32 v121, v121, v125\n\t"
      "v_add_f32_e32 v122, v122, v126\n\t"
      "v_add_f32_e32 v123, v123, v127\n\t"
      "s_waitcnt lgkmcnt(2)\n\t"
      "v_add_f32_e32 v184, v184, v188\n\t"
      "v_add_f32_e32 v185, v185, v189\n\t"
      "v_add_f32_e32 v186, v186, v190\n\t"
      "v_add_f32_e32 v187, v187, v191\n\t"
      "v_add_f32_e32 v112, v112, v120\n\t"
      "v_add_f32_e32 v113, v113, v121\n\t"
      "v_add_f32_e32 v114, v114, v122\n\t"
      "v_add_f32_e32 v115, v115, v123\n\t"
      "s_waitcnt lgkmcnt(0)\n\t"
      "v_add_f32_e32 v192, v192, v196\n\t"
      "v_add_f32_e32 v193, v193, v197\n\t"
      "v_add_f32_e32 v194, v194, v198\n\t"
      "v_add_f32_e32 v195, v195, v199\n\t"
      "v_add_f32_e32 v184, v184, v192\n\t"
      "v_add_f32_e32 v185, v185, v193\n\t"
      "v_add_f32_e32 v186, v186, v194\n\t"
      "v_add_f32_e32 v187, v187, v195\n\t"
      "v_add_f32_e32 v112, v112, v184\n\t"
      "v_add_f32_e32 v113, v113, v185\n\t"
      "v_add_f32_e32 v114, v114, v186\n\t"
      "v_add_f32_e32 v115, v115, v187\n\t"
      "v_cvt_pk_bf16_f32 v116, v112, v113\n\t"
      "v_cvt_pk_bf16_f32 v117, v114, v115\n\t"
      "global_store_dwordx2 v245, v[116:117], s[66:67] offset:64\n\t"
      "GX_%=:\n\t"
      "s_bitcmp1_b32 s16, 5\n\t"
      "s_cbranch_scc0 EP_%=\n\t"
      "v_mov_b32_e32 v0, s26\n\t"
      "v_mov_b32_e32 v1, s27\n\t"
      "v_mov_b32_e32 v2, 0\n\t"
      "s_mov_b64 exec, 1\n\t"
      "global_store_dwordx2 v2, v[0:1], s[24:25] sc1\n\t"
      "s_mov_b64 exec, -1\n\t"
      "EP_%=:\n\t"
      "s_endpgm\n\t"
      ".p2align 6\n\t"
      "GQ_%=:\n\t"
      "s_waitcnt vmcnt(6)\n\t"
      "v_max3_u32 v234, v128, v129, v130\n\t"
      "v_max_u32_e32 v234, v234, v131\n\t"
      "v_cmp_eq_u32_e32 vcc, -1, v234\n\t"
      "s_cbranch_vccnz FQ0R_%=\n\t"
      "FQ0B_%=:\n\t"
      "v_mfma_f32_16x16x32_bf16 v[184:187], v[128:131], v[0:3], 0\n\t"
      "v_mfma_f32_16x16x32_bf16 v[188:191], v[128:131], v[4:7], 0\n\t"
      "v_mfma_f32_16x16x32_bf16 v[192:195], v[128:131], v[8:11], 0\n\t"
      "v_mfma_f32_16x16x32_bf16 v[196:199], v[128:131], v[12:15], 0\n\t"
      "global_load_dwordx4 v[128:131], v224, s[54:55] sc1\n\t"
      "s_waitcnt vmcnt(6)\n\t"
      "v_max3_u32 v234, v132, v133, v134\n\t"
      "v_max_u32_e32 v234, v234, v135\n\t"
      "v_cmp_eq_u32_e32 vcc, -1, v234\n\t"
      "s_cbranch_vccnz FQ1R_%=\n\t"
      "FQ1B_%=:\n\t"
      "v_mfma_f32_16x16x32_bf16 v[184:187], v[132:135], v[16:19], v[184:187]\n\t"
      "v_mfma_f32_16x16x32_bf16 v[188:191], v[132:135], v[20:23], v[188:191]\n\t"
      "v_mfma_f32_16x16x32_bf16 v[192:195], v[132:135], v[24:27], v[192:195]\n\t"
      "v_mfma_f32_16x16x32_bf16 v[196:199], v[132:135], v[28:31], v[196:199]\n\t"
      "global_load_dwordx4 v[132:135], v225, s[54:55] sc1\n\t"
      "s_waitcnt vmcnt(6)\n\t"
      "v_max3_u32 v234, v136, v137, v138\n\t"
      "v_max_u32_e32 v234, v234, v139\n\t"
      "v_cmp_eq_u32_e32 vcc, -1, v234\n\t"
      "s_cbranch_vccnz FQ2R_%=\n\t"
      "FQ2B_%=:\n\t"
      "v_mfma_f32_16x16x32_bf16 v[184:187], v[136:139], v[32:35], v[184:187]\n\t"
      "v_mfma_f32_16x16x32_bf16 v[188:191], v[136:139], v[36:39], v[188:191]\n\t"
      "v_mfma_f32_16x16x32_bf16 v[192:195], v[136:139], v[40:43], v[192:195]\n\t"
      "v_mfma_f32_16x16x32_bf16 v[196:199], v[136:139], v[44:47], v[196:199]\n\t"
      "global_load_dwordx4 v[136:139], v226, s[54:55] sc1\n\t"
      "s_waitcnt vmcnt(6)\n\t"
      "v_max3_u32 v234, v140, v141, v142\n\t"
      "v_max_u32_e32 v234, v234, v143\n\t"
      "v_cmp_eq_u32_e32 vcc, -1, v234\n\t"
      "s_cbranch_vccnz FQ3R_%=\n\t"
      "FQ3B_%=:\n\t"
      "v_mfma_f32_16x16x32_bf16 v[184:187], v[140:143], v[48:51], v[184:187]\n\t"
      "v_mfma_f32_16x16x32_bf16 v[188:191], v[140:143], v[52:55], v[188:191]\n\t"
      "v_mfma_f32_16x16x32_bf16 v[192:195], v[140:143], v[56:59], v[192:195]\n\t"
      "v_mfma_f32_16x16x32_bf16 v[196:199], v[140:143], v[60:63], v[196:199]\n\t"
      "global_load_dwordx4 v[140:143], v227, s[54:55] sc1\n\t"
      "s_waitcnt vmcnt(6)\n\t"
      "v_max3_u32 v234, v144, v145, v146\n\t"
      "v_max_u32_e32 v234, v234, v147\n\t"
      "v_cmp_eq_u32_e32 vcc, -1, v234\n\t"
      "s_cbranch_vccnz FQ4R_%=\n\t"
      "FQ4B_%=:\n\t"
      "v_mfma_f32_16x16x32_bf16 v[184:187], v[144:147], v[64:67], v[184:187]\n\t"
      "v_mfma_f32_16x16x32_bf16 v[188:191], v[144:147], v[68:71], v[188:191]\n\t"
      "v_mfma_f32_16x16x32_bf16 v[192:195], v[144:147], v[72:75], v[192:195]\n\t"
      "v_mfma_f32_16x16x32_bf16 v[196:199], v[144:147], v[76:79], v[196:199]\n\t"
      "global_load_dwordx4 v[144:147], v228, s[54:55] sc1\n\t"
      "s_waitcnt vmcnt(6)\n\t"
      "v_max3_u32 v234, v148, v149, v150\n\t"
      "v_max_u32_e32 v234, v234, v151\n\t"
      "v_cmp_eq_u32_e32 vcc, -1, v234\n\t"
      "s_cbranch_vccnz FQ5R_%=\n\t"
      "FQ5B_%=:\n\t"
      "v_mfma_f32_16x16x32_bf16 v[184:187], v[148:151], v[80:83], v[184:187]\n\t"
      "v_mfma_f32_16x16x32_bf16 v[188:191], v[148:151], v[84:87], v[188:191]\n\t"
      "v_mfma_f32_16x16x32_bf16 v[192:195], v[148:151], v[88:91], v[192:195]\n\t"
      "v_mfma_f32_16x16x32_bf16 v[196:199], v[148:151], v[92:95], v[196:199]\n\t"
      "global_load_dwordx4 v[148:151], v229, s[54:55] sc1\n\t"
      "s_waitcnt vmcnt(6)\n\t"
      "v_max3_u32 v234, v152, v153, v154\n\t"
      "v_max_u32_e32 v234, v234, v155\n\t"
      "v_cmp_eq_u32_e32 vcc, -1, v234\n\t"
      "s_cbranch_vccnz FQ6R_%=\n\t"
      "FQ6B_%=:\n\t"
      "v_mfma_f32_16x16x32_bf16 v[184:187], v[152:155], v[96:99], v[184:187]\n\t"
      "s_waitcnt vmcnt(5)\n\t"
      "v_cmp_eq_u32_e32 vcc, -1, v128\n\t"
      "v_mfma_f32_16x16x32_bf16 v[188:191], v[152:155], v[100:103], v[188:191]\n\t"
      "v_mfma_f32_16x16x32_bf16 v[192:195], v[152:155], v[104:107], v[192:195]\n\t"
      "s_cbranch_vccnz FQ7R_%=\n\t"
      "FQ7B_%=:\n\t"
      "v_mfma_f32_16x16x32_bf16 v[196:199], v[152:155], v[108:111], v[196:199]\n\t"
      "global_load_dwordx4 v[152:155], v230, s[54:55] sc1\n\t"
      "s_nop 0\n\t"
      "ds_write_b128 v243, v[184:187]\n\t"
      "s_nop 1\n\t"
      "ds_write_b128 v220, v[188:191]\n\t"
      "ds_write_b128 v221, v[192:195]\n\t"
      "s_nop 0\n\t"
      "ds_write_b128 v222, v[196:199]\n\t"
      "v_mfma_f32_16x16x32_bf16 v[200:203], v[128:131], v[0:3], 0\n\t"
      "s_waitcnt vmcnt(5)\n\t"
      "v_cmp_eq_u32_e32 vcc, -1, v132\n\t"
      "v_mfma_f32_16x16x32_bf16 v[204:207], v[128:131], v[4:7], 0\n\t"
      "v_mfma_f32_16x16x32_bf16 v[208:211], v[128:131], v[8:11], 0\n\t"
      "s_cbranch_vccnz FQ8R_%=\n\t"
      "FQ8B_%=:\n\t"
      "v_mfma_f32_16x16x32_bf16 v[212:215], v[128:131], v[12:15], 0\n\t"
      "global_load_dwordx4 v[128:131], v224, s[56:57] sc1\n\t"
      "v_mfma_f32_16x16x32_bf16 v[200:203], v[132:135], v[16:19], v[200:203]\n\t"
      "s_waitcnt vmcnt(5)\n\t"
      "v_cmp_eq_u32_e32 vcc, -1, v136\n\t"
      "v_mfma_f32_16x16x32_bf16 v[204:207], v[132:135], v[20:23], v[204:207]\n\t"
      "v_mfma_f32_16x16x32_bf16 v[208:211], v[132:135], v[24:27], v[208:211]\n\t"
      "s_cbranch_vccnz FQ9R_%=\n\t"
      "FQ9B_%=:\n\t"
      "v_mfma_f32_16x16x32_bf16 v[212:215], v[132:135], v[28:31], v[212:215]\n\t"
      "global_load_dwordx4 v[132:135], v225, s[56:57] sc1\n\t"
      "v_mfma_f32_16x16x32_bf16 v[200:203], v[136:139], v[32:35], v[200:203]\n\t"
      "s_waitcnt vmcnt(5)\n\t"
      "v_cmp_eq_u32_e32 vcc, -1, v140\n\t"
      "v_mfma_f32_16x16x32_bf16 v[204:207], v[136:139], v[36:39], v[204:207]\n\t"
      "v_mfma_f32_16x16x32_bf16 v[208:211], v[136:139], v[40:43], v[208:211]\n\t"
      "s_cbranch_vccnz FQ10R_%=\n\t"
      "FQ10B_%=:\n\t"
      "v_mfma_f32_16x16x32_bf16 v[212:215], v[136:139], v[44:47], v[212:215]\n\t"
      "global_load_dwordx4 v[136:139], v226, s[56:57] sc1\n\t"
      "v_mfma_f32_16x16x32_bf16 v[200:203], v[140:143], v[48:51], v[200:203]\n\t"
      "s_waitcnt vmcnt(5)\n\t"
      "v_cmp_eq_u32_e32 vcc, -1, v144\n\t"
      "v_mfma_f32_16x16x32_bf16 v[204:207], v[140:143], v[52:55], v[204:207]\n\t"
      "v_mfma_f32_16x16x32_bf16 v[208:211], v[140:143], v[56:59], v[208:211]\n\t"
      "s_cbranch_vccnz FQ11R_%=\n\t"
      "FQ11B_%=:\n\t"
      "v_mfma_f32_16x16x32_bf16 v[212:215], v[140:143], v[60:63], v[212:215]\n\t"
      "global_load_dwordx4 v[140:143], v227, s[56:57] sc1\n\t"
      "v_mfma_f32_16x16x32_bf16 v[200:203], v[144:147], v[64:67], v[200:203]\n\t"
      "s_waitcnt vmcnt(5)\n\t"
      "v_cmp_eq_u32_e32 vcc, -1, v148\n\t"
      "v_mfma_f32_16x16x32_bf16 v[204:207], v[144:147], v[68:71], v[204:207]\n\t"
      "v_mfma_f32_16x16x32_bf16 v[208:211], v[144:147], v[72:75], v[208:211]\n\t"
      "s_cbranch_vccnz FQ12R_%=\n\t"
      "FQ12B_%=:\n\t"
      "v_mfma_f32_16x16x32_bf16 v[212:215], v[144:147], v[76:79], v[212:215]\n\t"
      "global_load_dwordx4 v[144:147], v228, s[56:57] sc1\n\t"
      "s_waitcnt lgkmcnt(0)\n\t"
      "s_barrier\n\t"
      "s_setprio 2\n\t"
      "v_mfma_f32_16x16x32_bf16 v[200:203], v[148:151], v[80:83], v[200:203]\n\t"
      "s_waitcnt vmcnt(5)\n\t"
      "v_cmp_eq_u32_e32 vcc, -1, v152\n\t"
      "v_mfma_f32_16x16x32_bf16 v[204:207], v[148:151], v[84:87], v[204:207]\n\t"
      "v_mfma_f32_16x16x32_bf16 v[208:211], v[148:151], v[88:91], v[208:211]\n\t"
      "s_cbranch_vccnz FQ13R_%=\n\t"
      "FQ13B_%=:\n\t"
      "v_mfma_f32_16x16x32_bf16 v[212:215], v[148:151], v[92:95], v[212:215]\n\t"
      "global_load_dwordx4 v[148:151], v229, s[56:57] sc1\n\t"
      "v_mfma_f32_16x16x32_bf16 v[200:203], v[152:155], v[96:99], v[200:203]\n\t"
      "s_waitcnt vmcnt(5)\n\t"
      "v_cmp_eq_u32_e32 vcc, -1, v128\n\t"
      "v_mfma_f32_16x16x32_bf16 v[204:207], v[152:155], v[100:103], v[204:207]\n\t"
      "v_mfma_f32_16x16x32_bf16 v[208:211], v[152:155], v[104:107], v[208:211]\n\t"
      "s_cbranch_vccnz FQ14R_%=\n\t"
      "FQ14B_%=:\n\t"
      "v_mfma_f32_16x16x32_bf16 v[212:215], v[152:155], v[108:111], v[212:215]\n\t"
      "global_load_dwordx4 v[152:155], v230, s[56:57] sc1\n\t"
      "s_setprio 0\n\t"
      "v_cmp_u_f32_e32 vcc, v200, v201\n\t"
      "v_cmp_u_f32_e64 s[48:49], v202, v203\n\t"
      "s_nop 0\n\t"
      "s_or_b64 vcc, vcc, s[48:49]\n\t"
      "s_cbranch_scc1 NQ1R_%=\n\t"
      "NQ1B_%=:\n\t"
      "ds_write_b128 v243, v[200:203] offset:32768\n\t"
      "ds_write_b128 v220, v[204:207] offset:32768\n\t"
      "ds_write_b128 v221, v[208:211] offset:32768\n\t"
      "ds_write_b128 v222, v[212:215] offset:32768\n\t"
      "v_mfma_f32_16x16x32_bf16 v[184:187], v[128:131], v[0:3], 0\n\t"
      "s_waitcnt vmcnt(5)\n\t"
      "v_cmp_eq_u32_e32 vcc, -1, v132\n\t"
      "v_mfma_f32_16x16x32_bf16 v[188:191], v[128:131], v[4:7], 0\n\t"
      "v_mfma_f32_16x16x32_bf16 v[192:195], v[128:131], v[8:11], 0\n\t"
      "s_cbranch_vccnz FQ15R_%=\n\t"
      "FQ15B_%=:\n\t"
      "v_mfma_f32_16x16x32_bf16 v[196:199], v[128:131], v[12:15], 0\n\t"
      "global_load_dwordx4 v[128:131], v224, s[58:59] sc1\n\t"
      "v_mfma_f32_16x16x32_bf16 v[184:187], v[132:135], v[16:19], v[184:187]\n\t"
      "s_waitcnt vmcnt(5)\n\t"
      "v_cmp_eq_u32_e32 vcc, -1, v136\n\t"
      "v_mfma_f32_16x16x32_bf16 v[188:191], v[132:135], v[20:23], v[188:191]\n\t"
      "v_mfma_f32_16x16x32_bf16 v[192:195], v[132:135], v[24:27], v[192:195]\n\t"
      "s_cbranch_vccnz FQ16R_%=\n\t"
      "FQ16B_%=:\n\t"
      "v_mfma_f32_16x16x32_bf16 v[196:199], v[132:135], v[28:31], v[196:199]\n\t"
      "global_load_dwordx4 v[132:135], v225, s[58:59] sc1\n\t"
      "v_mfma_f32_16x16x32_bf16 v[184:187], v[136:139], v[32:35], v[184:187]\n\t"
      "s_waitcnt vmcnt(5)\n\t"
      "v_cmp_eq_u32_e32 vcc, -1, v140\n\t"
      "v_mfma_f32_16x16x32_bf16 v[188:191], v[136:139], v[36:39], v[188:191]\n\t"
      "v_mfma_f32_16x16x32_bf16 v[192:195], v[136:139], v[40:43], v[192:195]\n\t"
      "s_cbranch_vccnz FQ17R_%=\n\t"
      "FQ17B_%=:\n\t"
      "v_mfma_f32_16x16x32_bf16 v[196:199], v[136:139], v[44:47], v[196:199]\n\t"
      "global_load_dwordx4 v[136:139], v226, s[58:59] sc1\n\t"
      "v_mfma_f32_16x16x32_bf16 v[184:187], v[140:143], v[48:51], v[184:187]\n\t"
      "s_waitcnt vmcnt(5)\n\t"
      "v_cmp_eq_u32_e32 vcc, -1, v144\n\t"
      "v_mfma_f32_16x16x32_bf16 v[188:191], v[140:143], v[52:55], v[188:191]\n\t"
      "v_mfma_f32_16x16x32_bf16 v[192:195], v[140:143], v[56:59], v[192:195]\n\t"
      "s_cbranch_vccnz FQ18R_%=\n\t"
      "FQ18B_%=:\n\t"
      "v_mfma_f32_16x16x32_bf16 v[196:199], v[140:143], v[60:63], v[196:199]\n\t"
      "global_load_dwordx4 v[140:143], v227, s[58:59] sc1\n\t"
      "v_mfma_f32_16x16x32_bf16 v[184:187], v[144:147], v[64:67], v[184:187]\n\t"
      "s_waitcnt vmcnt(5)\n\t"
      "v_cmp_eq_u32_e32 vcc, -1, v148\n\t"
      "v_mfma_f32_16x16x32_bf16 v[188:191], v[144:147], v[68:71], v[188:191]\n\t"
      "v_mfma_f32_16x16x32_bf16 v[192:195], v[144:147], v[72:75], v[192:195]\n\t"
      "s_cbranch_vccnz FQ19R_%=\n\t"
      "FQ19B_%=:\n\t"
      "v_mfma_f32_16x16x32_bf16 v[196:199], v[144:147], v[76:79], v[196:199]\n\t"
      "global_load_dwordx4 v[144:147], v228, s[58:59] sc1\n\t"
      "s_waitcnt lgkmcnt(0)\n\t"
      "s_barrier\n\t"
      "s_setprio 2\n\t"
      "v_mfma_f32_16x16x32_bf16 v[184:187], v[148:151], v[80:83], v[184:187]\n\t"
      "s_waitcnt vmcnt(5)\n\t"
      "v_cmp_eq_u32_e32 vcc, -1, v152\n\t"
      "v_mfma_f32_16x16x32_bf16 v[188:191], v[148:151], v[84:87], v[188:191]\n\t"
      "v_mfma_f32_16x16x32_bf16 v[192:195], v[148:151], v[88:91], v[192:195]\n\t"
      "s_cbranch_vccnz FQ20R_%=\n\t"
      "FQ20B_%=:\n\t"
      "v_mfma_f32_16x16x32_bf16 v[196:199], v[148:151], v[92:95], v[196:199]\n\t"
      "global_load_dwordx4 v[148:151], v229, s[58:59] sc1\n\t"
      "v_mfma_f32_16x16x32_bf16 v[184:187], v[152:155], v[96:99], v[184:187]\n\t"
      "s_waitcnt vmcnt(5)\n\t"
      "v_cmp_eq_u32_e32 vcc, -1, v128\n\t"
      "v_mfma_f32_16x16x32_bf16 v[188:191], v[152:155], v[100:103], v[188:191]\n\t"
      "v_mfma_f32_16x16x32_bf16 v[192:195], v[152:155], v[104:107], v[192:195]\n\t"
      "s_cbranch_vccnz FQ21R_%=\n\t"
      "FQ21B_%=:\n\t"
      "v_mfma_f32_16x16x32_bf16 v[196:199], v[152:155], v[108:111], v[196:199]\n\t"
      "global_load_dwordx4 v[152:155], v230, s[58:59] sc1\n\t"
      "s_setprio 0\n\t"
      "v_cmp_u_f32_e32 vcc, v184, v185\n\t"
      "v_cmp_u_f32_e64 s[48:49], v186, v187\n\t"
      "s_nop 0\n\t"
      "s_or_b64 vcc, vcc, s[48:49]\n\t"
      "s_cbranch_scc1 NQ2R_%=\n\t"
      "NQ2B_%=:\n\t"
      "ds_write_b128 v243, v[184:187]\n\t"
      "ds_write_b128 v220, v[188:191]\n\t"
      "ds_write_b128 v221, v[192:195]\n\t"
      "ds_write_b128 v222, v[196:199]\n\t"
      "v_mfma_f32_16x16x32_bf16 v[200:203], v[128:131], v[0:3], 0\n\t"
      "s_waitcnt vmcnt(5)\n\t"
      "v_cmp_eq_u32_e32 vcc, -1, v132\n\t"
      "v_mfma_f32_16x16x32_bf16 v[204:207], v[128:131], v[4:7], 0\n\t"
      "v_mfma_f32_16x16x32_bf16 v[208:211], v[128:131], v[8:11], 0\n\t"
      "s_cbranch_vccnz FQ22R_%=\n\t"
      "FQ22B_%=:\n\t"
      "v_mfma_f32_16x16x32_bf16 v[212:215], v[128:131], v[12:15], 0\n\t"
      "global_load_dwordx4 v[128:131], v224, s[60:61] sc1\n\t"
      "v_mfma_f32_16x16x32_bf16 v[200:203], v[132:135], v[16:19], v[200:203]\n\t"
      "s_waitcnt vmcnt(5)\n\t"
      "v_cmp_eq_u32_e32 vcc, -1, v136\n\t"
      "v_mfma_f32_16x16x32_bf16 v[204:207], v[132:135], v[20:23], v[204:207]\n\t"
      "v_mfma_f32_16x16x32_bf16 v[208:211], v[132:135], v[24:27], v[208:211]\n\t"
      "s_cbranch_vccnz FQ23R_%=\n\t"
      "FQ23B_%=:\n\t"
      "v_mfma_f32_16x16x32_bf16 v[212:215], v[132:135], v[28:31], v[212:215]\n\t"
      "global_load_dwordx4 v[132:135], v225, s[60:61] sc1\n\t"
      "v_mfma_f32_16x16x32_bf16 v[200:203], v[136:139], v[32:35], v[200:203]\n\t"
      "s_waitcnt vmcnt(5)\n\t"
      "v_cmp_eq_u32_e32 vcc, -1, v140\n\t"
      "v_mfma_f32_16x16x32_bf16 v[204:207], v[136:139], v[36:39], v[204:207]\n\t"
      "v_mfma_f32_16x16x32_bf16 v[208:211], v[136:139], v[40:43], v[208:211]\n\t"
      "s_cbranch_vccnz FQ24R_%=\n\t"
      "FQ24B_%=:\n\t"
      "v_mfma_f32_16x16x32_bf16 v[212:215], v[136:139], v[44:47], v[212:215]\n\t"
      "global_load_dwordx4 v[136:139], v226, s[60:61] sc1\n\t"
      "v_mfma_f32_16x16x32_bf16 v[200:203], v[140:143], v[48:51], v[200:203]\n\t"
      "s_waitcnt vmcnt(5)\n\t"
      "v_cmp_eq_u32_e32 vcc, -1, v144\n\t"
      "v_mfma_f32_16x16x32_bf16 v[204:207], v[140:143], v[52:55], v[204:207]\n\t"
      "v_mfma_f32_16x16x32_bf16 v[208:211], v[140:143], v[56:59], v[208:211]\n\t"
      "s_cbranch_vccnz FQ25R_%=\n\t"
      "FQ25B_%=:\n\t"
      "v_mfma_f32_16x16x32_bf16 v[212:215], v[140:143], v[60:63], v[212:215]\n\t"
      "global_load_dwordx4 v[140:143], v227, s[60:61] sc1\n\t"
      "v_mfma_f32_16x16x32_bf16 v[200:203], v[144:147], v[64:67], v[200:203]\n\t"
      "s_waitcnt vmcnt(5)\n\t"
      "v_cmp_eq_u32_e32 vcc, -1, v148\n\t"
      "v_mfma_f32_16x16x32_bf16 v[204:207], v[144:147], v[68:71], v[204:207]\n\t"
      "v_mfma_f32_16x16x32_bf16 v[208:211], v[144:147], v[72:75], v[208:211]\n\t"
      "s_cbranch_vccnz FQ26R_%=\n\t"
      "FQ26B_%=:\n\t"
      "v_mfma_f32_16x16x32_bf16 v[212:215], v[144:147], v[76:79], v[212:215]\n\t"
      "global_load_dwordx4 v[144:147], v228, s[60:61] sc1\n\t"
      "s_waitcnt lgkmcnt(0)\n\t"
      "s_barrier\n\t"
      "s_setprio 2\n\t"
      "v_mfma_f32_16x16x32_bf16 v[200:203], v[148:151], v[80:83], v[200:203]\n\t"
      "s_waitcnt vmcnt(5)\n\t"
      "v_cmp_eq_u32_e32 vcc, -1, v152\n\t"
      "v_mfma_f32_16x16x32_bf16 v[204:207], v[148:151], v[84:87], v[204:207]\n\t"
      "v_mfma_f32_16x16x32_bf16 v[208:211], v[148:151], v[88:91], v[208:211]\n\t"
      "s_cbranch_vccnz FQ27R_%=\n\t"
      "FQ27B_%=:\n\t"
      "v_mfma_f32_16x16x32_bf16 v[212:215], v[148:151], v[92:95], v[212:215]\n\t"
      "global_load_dwordx4 v[148:151], v229, s[60:61] sc1\n\t"
      "v_mfma_f32_16x16x32_bf16 v[200:203], v[152:155], v[96:99], v[200:203]\n\t"
      "s_waitcnt vmcnt(5)\n\t"
      "v_cmp_eq_u32_e32 vcc, -1, v128\n\t"
      "v_mfma_f32_16x16x32_bf16 v[204:207], v[152:155], v[100:103], v[204:207]\n\t"
      "v_mfma_f32_16x16x32_bf16 v[208:211], v[152:155], v[104:107], v[208:211]\n\t"
      "s_cbranch_vccnz FQ28R_%=\n\t"
      "FQ28B_%=:\n\t"
      "v_mfma_f32_16x16x32_bf16 v[212:215], v[152:155], v[108:111], v[212:215]\n\t"
      "global_load_dwordx4 v[152:155], v230, s[60:61] sc1\n\t"
      "s_setprio 0\n\t"
      "v_cmp_u_f32_e32 vcc, v200, v201\n\t"
      "v_cmp_u_f32_e64 s[48:49], v202, v203\n\t"
      "s_nop 0\n\t"
      "s_or_b64 vcc, vcc, s[48:49]\n\t"
      "s_cbranch_scc1 NQ3R_%=\n\t"
      "NQ3B_%=:\n\t"
      "ds_write_b128 v243, v[200:203] offset:32768\n\t"
      "ds_write_b128 v220, v[204:207] offset:32768\n\t"
      "ds_write_b128 v221, v[208:211] offset:32768\n\t"
      "ds_write_b128 v222, v[212:215] offset:32768\n\t"
      "v_mfma_f32_16x16x32_bf16 v[184:187], v[128:131], v[0:3], 0\n\t"
      "s_waitcnt vmcnt(5)\n\t"
      "v_cmp_eq_u32_e32 vcc, -1, v132\n\t"
      "v_mfma_f32_16x16x32_bf16 v[188:191], v[128:131], v[4:7], 0\n\t"
      "v_mfma_f32_16x16x32_bf16 v[192:195], v[128:131], v[8:11], 0\n\t"
      "s_cbranch_vccnz FQ29R_%=\n\t"
      "FQ29B_%=:\n\t"
      "v_mfma_f32_16x16x32_bf16 v[196:199], v[128:131], v[12:15], 0\n\t"
      "global_load_dwordx4 v[128:131], v224, s[62:63] sc1\n\t"
      "v_mfma_f32_16x16x32_bf16 v[184:187], v[132:135], v[16:19], v[184:187]\n\t"
      "s_waitcnt vmcnt(5)\n\t"
      "v_cmp_eq_u32_e32 vcc, -1, v136\n\t"
      "v_mfma_f32_16x16x32_bf16 v[188:191], v[132:135], v[20:23], v[188:191]\n\t"
      "v_mfma_f32_16x16x32_bf16 v[192:195], v[132:135], v[24:27], v[192:195]\n\t"
      "s_cbranch_vccnz FQ30R_%=\n\t"
      "FQ30B_%=:\n\t"
      "v_mfma_f32_16x16x32_bf16 v[196:199], v[132:135], v[28:31], v[196:199]\n\t"
      "global_load_dwordx4 v[132:135], v225, s[62:63] sc1\n\t"
      "v_mfma_f32_16x16x32_bf16 v[184:187], v[136:139], v[32:35], v[184:187]\n\t"
      "s_waitcnt vmcnt(5)\n\t"
      "v_cmp_eq_u32_e32 vcc, -1, v140\n\t"
      "v_mfma_f32_16x16x32_bf16 v[188:191], v[136:139], v[36:39], v[188:191]\n\t"
      "v_mfma_f32_16x16x32_bf16 v[192:195], v[136:139], v[40:43], v[192:195]\n\t"
      "s_cbranch_vccnz FQ31R_%=\n\t"
      "FQ31B_%=:\n\t"
      "v_mfma_f32_16x16x32_bf16 v[196:199], v[136:139], v[44:47], v[196:199]\n\t"
      "global_load_dwordx4 v[136:139], v226, s[62:63] sc1\n\t"
      "v_mfma_f32_16x16x32_bf16 v[184:187], v[140:143], v[48:51], v[184:187]\n\t"
      "s_waitcnt vmcnt(5)\n\t"
      "v_cmp_eq_u32_e32 vcc, -1, v144\n\t"
      "v_mfma_f32_16x16x32_bf16 v[188:191], v[140:143], v[52:55], v[188:191]\n\t"
      "v_mfma_f32_16x16x32_bf16 v[192:195], v[140:143], v[56:59], v[192:195]\n\t"
      "s_cbranch_vccnz FQ32R_%=\n\t"
      "FQ32B_%=:\n\t"
      "v_mfma_f32_16x16x32_bf16 v[196:199], v[140:143], v[60:63], v[196:199]\n\t"
      "global_load_dwordx4 v[140:143], v227, s[62:63] sc1\n\t"
      "v_mfma_f32_16x16x32_bf16 v[184:187], v[144:147], v[64:67], v[184:187]\n\t"
      "s_waitcnt vmcnt(5)\n\t"
      "v_cmp_eq_u32_e32 vcc, -1, v148\n\t"
      "v_mfma_f32_16x16x32_bf16 v[188:191], v[144:147], v[68:71], v[188:191]\n\t"
      "v_mfma_f32_16x16x32_bf16 v[192:195], v[144:147], v[72:75], v[192:195]\n\t"
      "s_cbranch_vccnz FQ33R_%=\n\t"
      "FQ33B_%=:\n\t"
      "v_mfma_f32_16x16x32_bf16 v[196:199], v[144:147], v[76:79], v[196:199]\n\t"
      "global_load_dwordx4 v[144:147], v228, s[62:63] sc1\n\t"
      "s_waitcnt lgkmcnt(0)\n\t"
      "s_barrier\n\t"
      "s_setprio 2\n\t"
      "v_mfma_f32_16x16x32_bf16 v[184:187], v[148:151], v[80:83], v[184:187]\n\t"
      "s_waitcnt vmcnt(5)\n\t"
      "v_cmp_eq_u32_e32 vcc, -1, v152\n\t"
      "v_mfma_f32_16x16x32_bf16 v[188:191], v[148:151], v[84:87], v[188:191]\n\t"
      "v_mfma_f32_16x16x32_bf16 v[192:195], v[148:151], v[88:91], v[192:195]\n\t"
      "s_cbranch_vccnz FQ34R_%=\n\t"
      "FQ34B_%=:\n\t"
      "v_mfma_f32_16x16x32_bf16 v[196:199], v[148:151], v[92:95], v[196:199]\n\t"
      "global_load_dwordx4 v[148:151], v229, s[62:63] sc1\n\t"
      "v_mfma_f32_16x16x32_bf16 v[184:187], v[152:155], v[96:99], v[184:187]\n\t"
      "s_waitcnt vmcnt(5)\n\t"
      "v_cmp_eq_u32_e32 vcc, -1, v128\n\t"
      "v_mfma_f32_16x16x32_bf16 v[188:191], v[152:155], v[100:103], v[188:191]\n\t"
      "v_mfma_f32_16x16x32_bf16 v[192:195], v[152:155], v[104:107], v[192:195]\n\t"
      "s_cbranch_vccnz FQ35R_%=\n\t"
      "FQ35B_%=:\n\t"
      "v_mfma_f32_16x16x32_bf16 v[196:199], v[152:155], v[108:111], v[196:199]\n\t"
      "global_load_dwordx4 v[152:155], v230, s[62:63] sc1\n\t"
      "s_setprio 0\n\t"
      "v_cmp_u_f32_e32 vcc, v184, v185\n\t"
      "v_cmp_u_f32_e64 s[48:49], v186, v187\n\t"
      "s_nop 0\n\t"
      "s_or_b64 vcc, vcc, s[48:49]\n\t"
      "s_cbranch_scc1 NQ4R_%=\n\t"
      "NQ4B_%=:\n\t"
      "ds_write_b128 v243, v[184:187]\n\t"
      "ds_write_b128 v220, v[188:191]\n\t"
      "ds_write_b128 v221, v[192:195]\n\t"
      "ds_write_b128 v222, v[196:199]\n\t"
      "v_mfma_f32_16x16x32_bf16 v[200:203], v[128:131], v[0:3], 0\n\t"
      "s_waitcnt vmcnt(5)\n\t"
      "v_cmp_eq_u32_e32 vcc, -1, v132\n\t"
      "v_mfma_f32_16x16x32_bf16 v[204:207], v[128:131], v[4:7], 0\n\t"
      "v_mfma_f32_16x16x32_bf16 v[208:211], v[128:131], v[8:11], 0\n\t"
      "s_cbranch_vccnz FQ36R_%=\n\t"
      "FQ36B_%=:\n\t"
      "v_mfma_f32_16x16x32_bf16 v[212:215], v[128:131], v[12:15], 0\n\t"
      "global_load_dwordx4 v[128:131], v224, s[64:65] sc1\n\t"
      "v_mfma_f32_16x16x32_bf16 v[200:203], v[132:135], v[16:19], v[200:203]\n\t"
      "s_waitcnt vmcnt(5)\n\t"
      "v_cmp_eq_u32_e32 vcc, -1, v136\n\t"
      "v_mfma_f32_16x16x32_bf16 v[204:207], v[132:135], v[20:23], v[204:207]\n\t"
      "v_mfma_f32_16x16x32_bf16 v[208:211], v[132:135], v[24:27], v[208:211]\n\t"
      "s_cbranch_vccnz FQ37R_%=\n\t"
      "FQ37B_%=:\n\t"
      "v_mfma_f32_16x16x32_bf16 v[212:215], v[132:135], v[28:31], v[212:215]\n\t"
      "global_load_dwordx4 v[132:135], v225, s[64:65] sc1\n\t"
      "v_mfma_f32_16x16x32_bf16 v[200:203], v[136:139], v[32:35], v[200:203]\n\t"
      "s_waitcnt vmcnt(5)\n\t"
      "v_cmp_eq_u32_e32 vcc, -1, v140\n\t"
      "v_mfma_f32_16x16x32_bf16 v[204:207], v[136:139], v[36:39], v[204:207]\n\t"
      "v_mfma_f32_16x16x32_bf16 v[208:211], v[136:139], v[40:43], v[208:211]\n\t"
      "s_cbranch_vccnz FQ38R_%=\n\t"
      "FQ38B_%=:\n\t"
      "v_mfma_f32_16x16x32_bf16 v[212:215], v[136:139], v[44:47], v[212:215]\n\t"
      "global_load_dwordx4 v[136:139], v226, s[64:65] sc1\n\t"
      "v_mfma_f32_16x16x32_bf16 v[200:203], v[140:143], v[48:51], v[200:203]\n\t"
      "s_waitcnt vmcnt(5)\n\t"
      "v_cmp_eq_u32_e32 vcc, -1, v144\n\t"
      "v_mfma_f32_16x16x32_bf16 v[204:207], v[140:143], v[52:55], v[204:207]\n\t"
      "v_mfma_f32_16x16x32_bf16 v[208:211], v[140:143], v[56:59], v[208:211]\n\t"
      "s_cbranch_vccnz FQ39R_%=\n\t"
      "FQ39B_%=:\n\t"
      "v_mfma_f32_16x16x32_bf16 v[212:215], v[140:143], v[60:63], v[212:215]\n\t"
      "global_load_dwordx4 v[140:143], v227, s[64:65] sc1\n\t"
      "v_mfma_f32_16x16x32_bf16 v[200:203], v[144:147], v[64:67], v[200:203]\n\t"
      "s_waitcnt vmcnt(5)\n\t"
      "v_cmp_eq_u32_e32 vcc, -1, v148\n\t"
      "v_mfma_f32_16x16x32_bf16 v[204:207], v[144:147], v[68:71], v[204:207]\n\t"
      "v_mfma_f32_16x16x32_bf16 v[208:211], v[144:147], v[72:75], v[208:211]\n\t"
      "s_cbranch_vccnz FQ40R_%=\n\t"
      "FQ40B_%=:\n\t"
      "v_mfma_f32_16x16x32_bf16 v[212:215], v[144:147], v[76:79], v[212:215]\n\t"
      "global_load_dwordx4 v[144:147], v228, s[64:65] sc1\n\t"
      "s_waitcnt lgkmcnt(0)\n\t"
      "s_barrier\n\t"
      "s_setprio 2\n\t"
      "v_mfma_f32_16x16x32_bf16 v[200:203], v[148:151], v[80:83], v[200:203]\n\t"
      "s_waitcnt vmcnt(5)\n\t"
      "v_cmp_eq_u32_e32 vcc, -1, v152\n\t"
      "v_mfma_f32_16x16x32_bf16 v[204:207], v[148:151], v[84:87], v[204:207]\n\t"
      "v_mfma_f32_16x16x32_bf16 v[208:211], v[148:151], v[88:91], v[208:211]\n\t"
      "s_cbranch_vccnz FQ41R_%=\n\t"
      "FQ41B_%=:\n\t"
      "v_mfma_f32_16x16x32_bf16 v[212:215], v[148:151], v[92:95], v[212:215]\n\t"
      "global_load_dwordx4 v[148:151], v229, s[64:65] sc1\n\t"
      "v_mfma_f32_16x16x32_bf16 v[200:203], v[152:155], v[96:99], v[200:203]\n\t"
      "s_waitcnt vmcnt(5)\n\t"
      "v_cmp_eq_u32_e32 vcc, -1, v128\n\t"
      "v_mfma_f32_16x16x32_bf16 v[204:207], v[152:155], v[100:103], v[204:207]\n\t"
      "v_mfma_f32_16x16x32_bf16 v[208:211], v[152:155], v[104:107], v[208:211]\n\t"
      "s_cbranch_vccnz FQ42R_%=\n\t"
      "FQ42B_%=:\n\t"
      "v_mfma_f32_16x16x32_bf16 v[212:215], v[152:155], v[108:111], v[212:215]\n\t"
      "global_load_dwordx4 v[152:155], v230, s[64:65] sc1\n\t"
      "s_setprio 0\n\t"
      "v_cmp_u_f32_e32 vcc, v200, v201\n\t"
      "v_cmp_u_f32_e64 s[48:49], v202, v203\n\t"
      "s_nop 0\n\t"
      "s_or_b64 vcc, vcc, s[48:49]\n\t"
      "s_cbranch_scc1 NQ5R_%=\n\t"
      "NQ5B_%=:\n\t"
      "ds_write_b128 v243, v[200:203] offset:32768\n\t"
      "ds_write_b128 v220, v[204:207] offset:32768\n\t"
      "ds_write_b128 v221, v[208:211] offset:32768\n\t"
      "ds_write_b128 v222, v[212:215] offset:32768\n\t"
      "v_mfma_f32_16x16x32_bf16 v[184:187], v[128:131], v[0:3], 0\n\t"
      "s_waitcnt vmcnt(5)\n\t"
      "v_cmp_eq_u32_e32 vcc, -1, v132\n\t"
      "v_mfma_f32_16x16x32_bf16 v[188:191], v[128:131], v[4:7], 0\n\t"
      "v_mfma_f32_16x16x32_bf16 v[192:195], v[128:131], v[8:11], 0\n\t"
      "s_cbranch_vccnz FQ43R_%=\n\t"
      "FQ43B_%=:\n\t"
      "v_mfma_f32_16x16x32_bf16 v[196:199], v[128:131], v[12:15], 0\n\t"
      "v_mfma_f32_16x16x32_bf16 v[184:187], v[132:135], v[16:19], v[184:187]\n\t"
      "s_waitcnt vmcnt(4)\n\t"
      "v_cmp_eq_u32_e32 vcc, -1, v136\n\t"
      "v_mfma_f32_16x16x32_bf16 v[188:191], v[132:135], v[20:23], v[188:191]\n\t"
      "v_mfma_f32_16x16x32_bf16 v[192:195], v[132:135], v[24:27], v[192:195]\n\t"
      "s_cbranch_vccnz FQ44R_%=\n\t"
      "FQ44B_%=:\n\t"
      "v_mfma_f32_16x16x32_bf16 v[196:199], v[132:135], v[28:31], v[196:199]\n\t"
      "v_mfma_f32_16x16x32_bf16 v[184:187], v[136:139], v[32:35], v[184:187]\n\t"
      "s_waitcnt vmcnt(3)\n\t"
      "v_cmp_eq_u32_e32 vcc, -1, v140\n\t"
      "v_mfma_f32_16x16x32_bf16 v[188:191], v[136:139], v[36:39], v[188:191]\n\t"
      "v_mfma_f32_16x16x32_bf16 v[192:195], v[136:139], v[40:43], v[192:195]\n\t"
      "s_cbranch_vccnz FQ45R_%=\n\t"
      "FQ45B_%=:\n\t"
      "v_mfma_f32_16x16x32_bf16 v[196:199], v[136:139], v[44:47], v[196:199]\n\t"
      "v_mfma_f32_16x16x32_bf16 v[184:187], v[140:143], v[48:51], v[184:187]\n\t"
      "s_waitcnt vmcnt(2)\n\t"
      "v_cmp_eq_u32_e32 vcc, -1, v144\n\t"
      "v_mfma_f32_16x16x32_bf16 v[188:191], v[140:143], v[52:55], v[188:191]\n\t"
      "v_mfma_f32_16x16x32_bf16 v[192:195], v[140:143], v[56:59], v[192:195]\n\t"
      "s_cbranch_vccnz FQ46R_%=\n\t"
      "FQ46B_%=:\n\t"
      "v_mfma_f32_16x16x32_bf16 v[196:199], v[140:143], v[60:63], v[196:199]\n\t"
      "v_mfma_f32_16x16x32_bf16 v[184:187], v[144:147], v[64:67], v[184:187]\n\t"
      "s_waitcnt vmcnt(1)\n\t"
      "v_cmp_eq_u32_e32 vcc, -1, v148\n\t"
      "v_mfma_f32_16x16x32_bf16 v[188:191], v[144:147], v[68:71], v[188:191]\n\t"
      "v_mfma_f32_16x16x32_bf16 v[192:195], v[144:147], v[72:75], v[192:195]\n\t"
      "s_cbranch_vccnz FQ47R_%=\n\t"
      "FQ47B_%=:\n\t"
      "v_mfma_f32_16x16x32_bf16 v[196:199], v[144:147], v[76:79], v[196:199]\n\t"
      "s_waitcnt lgkmcnt(0)\n\t"
      "s_barrier\n\t"
      "s_setprio 2\n\t"
      "v_mfma_f32_16x16x32_bf16 v[184:187], v[148:151], v[80:83], v[184:187]\n\t"
      "s_waitcnt vmcnt(0)\n\t"
      "v_cmp_eq_u32_e32 vcc, -1, v152\n\t"
      "v_mfma_f32_16x16x32_bf16 v[188:191], v[148:151], v[84:87], v[188:191]\n\t"
      "v_mfma_f32_16x16x32_bf16 v[192:195], v[148:151], v[88:91], v[192:195]\n\t"
      "s_cbranch_vccnz FQ48R_%=\n\t"
      "FQ48B_%=:\n\t"
      "v_mfma_f32_16x16x32_bf16 v[196:199], v[148:151], v[92:95], v[196:199]\n\t"
      "v_mfma_f32_16x16x32_bf16 v[184:187], v[152:155], v[96:99], v[184:187]\n\t"
      "v_mfma_f32_16x16x32_bf16 v[188:191], v[152:155], v[100:103], v[188:191]\n\t"
      "v_mfma_f32_16x16x32_bf16 v[192:195], v[152:155], v[104:107], v[192:195]\n\t"
      "v_mfma_f32_16x16x32_bf16 v[196:199], v[152:155], v[108:111], v[196:199]\n\t"
      "s_nop 4\n\t"
      "v_cmp_u_f32_e32 vcc, v184, v185\n\t"
      "v_cmp_u_f32_e64 s[48:49], v186, v187\n\t"
      "s_nop 0\n\t"
      "s_or_b64 vcc, vcc, s[48:49]\n\t"
      "s_cbranch_scc1 NQ6R_%=\n\t"
      "NQ6B_%=:\n\t"
      "ds_write_b128 v243, v[184:187]\n\t"
      "ds_write_b128 v220, v[188:191]\n\t"
      "ds_write_b128 v221, v[192:195]\n\t"
      "ds_write_b128 v222, v[196:199]\n\t"
      "s_waitcnt lgkmcnt(0)\n\t"
      "s_barrier\n\t"
      "s_branch GX_%=\n\t"
      "HWR_%=:\n\t"
      "s_mov_b32 s51, 0\n\t"
      "HWL_%=:\n\t"
      "s_add_u32 s51, s51, 1\n\t"
      "s_cmp_lt_u32 s51, 262144\n\t"
      "s_cbranch_scc0 HWX_%=\n\t"
      "s_sleep 2\n\t"
      "global_load_dwordx4 v[112:115], v116, s[12:13] sc1\n\t"
      "s_waitcnt vmcnt(0)\n\t"
      "v_xor_b32_e32 v114, v114, v112\n\t"
      "v_xor_b32_e32 v115, v115, v113\n\t"
      "v_cmp_ne_u64_e32 vcc, s[14:15], v[114:115]\n\t"
      "s_cbranch_vccnz HWL_%=\n\t"
      "HWX_%=:\n\t"
      "s_branch HWB_%=\n\t"
      "HXR_%=:\n\t"
      "s_mov_b32 s51, 0\n\t"
      "HXL_%=:\n\t"
      "s_add_u32 s51, s51, 1\n\t"
      "s_cmp_lt_u32 s51, 262144\n\t"
      "s_cbranch_scc0 HXX_%=\n\t"
      "s_sleep 2\n\t"
      "s_mov_b32 exec_hi, s23\n\t"
      "s_not_b32 exec_lo, s23\n\t"
      "s_add_u32 s68, s20, 0\n\t"
      "s_addc_u32 s69, s21, 0\n\t"
      "global_load_dwordx4 v[184:187], v240, s[68:69] sc1\n\t"
      "s_add_u32 s68, s20, 131072\n\t"
      "s_addc_u32 s69, s21, 0\n\t"
      "global_load_dwordx4 v[188:191], v240, s[68:69] sc1\n\t"
      "s_add_u32 s68, s20, 262144\n\t"
      "s_addc_u32 s69, s21, 0\n\t"
      "global_load_dwordx4 v[192:195], v240, s[68:69] sc1\n\t"
      "s_add_u32 s68, s20, 393216\n\t"
      "s_addc_u32 s69, s21, 0\n\t"
      "global_load_dwordx4 v[196:199], v240, s[68:69] sc1\n\t"
      "s_add_u32 s68, s20, 524288\n\t"
      "s_addc_u32 s69, s21, 0\n\t"
      "global_load_dwordx4 v[200:203], v240, s[68:69] sc1\n\t"
      "s_add_u32 s68, s20, 655360\n\t"
      "s_addc_u32 s69, s21, 0\n\t"
      "global_load_dwordx4 v[204:207], v240, s[68:69] sc1\n\t"
      "s_add_u32 s68, s20, 786432\n\t"
      "s_addc_u32 s69, s21, 0\n\t"
      "global_load_dwordx4 v[208:211], v240, s[68:69] sc1\n\t"
      "s_mov_b64 exec, -1\n\t"
      "s_waitcnt vmcnt(0)\n\t"
      "v_max3_u32 v234, v184, v185, v186\n\t"
      "v_max3_u32 v234, v234, v187, v188\n\t"
      "v_max3_u32 v234, v234, v189, v190\n\t"
      "v_max3_u32 v234, v234, v191, v192\n\t"
      "v_max3_u32 v234, v234, v193, v194\n\t"
      "v_max3_u32 v234, v234, v195, v196\n\t"
      "v_max3_u32 v234, v234, v197, v198\n\t"
      "v_max3_u32 v234, v234, v199, v200\n\t"
      "v_max3_u32 v234, v234, v201, v202\n\t"
      "v_max3_u32 v234, v234, v203, v204\n\t"
      "v_max3_u32 v234, v234, v205, v206\n\t"
      "v_max3_u32 v234, v234, v207, v208\n\t"
      "v_max3_u32 v234, v234, v209, v210\n\t"
      "v_max_u32_e32 v234, v234, v211\n\t"
      "v_cmp_eq_u32_e32 vcc, -1, v234\n\t"
      "s_cbranch_vccnz HXL_%=\n\t"
      "HXX_%=:\n\t"
      "s_nop 1\n\t"
      "s_branch HXB_%=\n\t"
      "FQ0R_%=:\n\t"
      "s_mov_b32 s51, 0\n\t"
      "FQ0L_%=:\n\t"
      "s_add_u32 s51, s51, 1\n\t"
      "s_cmp_lt_u32 s51, 262144\n\t"
      "s_cbranch_scc0 FQ0X_%=\n\t"
      "s_waitcnt vmcnt(0)\n\t"
      "s_sleep 1\n\t"
      "global_load_dwordx4 v[128:131], v224, s[52:53] sc1\n\t"
      "global_load_dwordx4 v[132:135], v225, s[52:53] sc1\n\t"
      "global_load_dwordx4 v[136:139], v226, s[52:53] sc1\n\t"
      "global_load_dwordx4 v[140:143], v227, s[52:53] sc1\n\t"
      "global_load_dwordx4 v[144:147], v228, s[52:53] sc1\n\t"
      "global_load_dwordx4 v[148:151], v229, s[52:53] sc1\n\t"
      "global_load_dwordx4 v[152:155], v230, s[52:53] sc1\n\t"
      "s_waitcnt vmcnt(6)\n\t"
      "v_max3_u32 v234, v128, v129, v130\n\t"
      "v_max_u32_e32 v234, v234, v131\n\t"
      "v_cmp_eq_u32_e32 vcc, -1, v234\n\t"
      "s_cbranch_vccnz FQ0L_%=\n\t"
      "FQ0X_%=:\n\t"
      "s_branch FQ0B_%=\n\t"
      "FQ1R_%=:\n\t"
      "s_mov_b32 s51, 0\n\t"
      "FQ1L_%=:\n\t"
      "s_add_u32 s51, s51, 1\n\t"
      "s_cmp_lt_u32 s51, 262144\n\t"
      "s_cbranch_scc0 FQ1X_%=\n\t"
      "s_waitcnt vmcnt(0)\n\t"
      "s_sleep 1\n\t"
      "global_load_dwordx4 v[132:135], v225, s[52:53] sc1\n\t"
      "global_load_dwordx4 v[136:139], v226, s[52:53] sc1\n\t"
      "global_load_dwordx4 v[140:143], v227, s[52:53] sc1\n\t"
      "global_load_dwordx4 v[144:147], v228, s[52:53] sc1\n\t"
      "global_load_dwordx4 v[148:151], v229, s[52:53] sc1\n\t"
      "global_load_dwordx4 v[152:155], v230, s[52:53] sc1\n\t"
      "global_load_dwordx4 v[128:131], v224, s[54:55] sc1\n\t"
      "s_waitcnt vmcnt(6)\n\t"
      "v_max3_u32 v234, v132, v133, v134\n\t"
      "v_max_u32_e32 v234, v234, v135\n\t"
      "v_cmp_eq_u32_e32 vcc, -1, v234\n\t"
      "s_cbranch_vccnz FQ1L_%=\n\t"
      "FQ1X_%=:\n\t"
      "s_branch FQ1B_%=\n\t"
      "FQ2R_%=:\n\t"
      "s_mov_b32 s51, 0\n\t"
      "FQ2L_%=:\n\t"
      "s_add_u32 s51, s51, 1\n\t"
      "s_cmp_lt_u32 s51, 262144\n\t"
      "s_cbranch_scc0 FQ2X_%=\n\t"
      "s_waitcnt vmcnt(0)\n\t"
      "s_sleep 1\n\t"
      "global_load_dwordx4 v[136:139], v226, s[52:53] sc1\n\t"
      "global_load_dwordx4 v[140:143], v227, s[52:53] sc1\n\t"
      "global_load_dwordx4 v[144:147], v228, s[52:53] sc1\n\t"
      "global_load_dwordx4 v[148:151], v229, s[52:53] sc1\n\t"
      "global_load_dwordx4 v[152:155], v230, s[52:53] sc1\n\t"
      "global_load_dwordx4 v[128:131], v224, s[54:55] sc1\n\t"
      "global_load_dwordx4 v[132:135], v225, s[54:55] sc1\n\t"
      "s_waitcnt vmcnt(6)\n\t"
      "v_max3_u32 v234, v136, v137, v138\n\t"
      "v_max_u32_e32 v234, v234, v139\n\t"
      "v_cmp_eq_u32_e32 vcc, -1, v234\n\t"
      "s_cbranch_vccnz FQ2L_%=\n\t"
      "FQ2X_%=:\n\t"
      "s_branch FQ2B_%=\n\t"
      "FQ3R_%=:\n\t"
      "s_mov_b32 s51, 0\n\t"
      "FQ3L_%=:\n\t"
      "s_add_u32 s51, s51, 1\n\t"
      "s_cmp_lt_u32 s51, 262144\n\t"
      "s_cbranch_scc0 FQ3X_%=\n\t"
      "s_waitcnt vmcnt(0)\n\t"
      "s_sleep 1\n\t"
      "global_load_dwordx4 v[140:143], v227, s[52:53] sc1\n\t"
      "global_load_dwordx4 v[144:147], v228, s[52:53] sc1\n\t"
      "global_load_dwordx4 v[148:151], v229, s[52:53] sc1\n\t"
      "global_load_dwordx4 v[152:155], v230, s[52:53] sc1\n\t"
      "global_load_dwordx4 v[128:131], v224, s[54:55] sc1\n\t"
      "global_load_dwordx4 v[132:135], v225, s[54:55] sc1\n\t"
      "global_load_dwordx4 v[136:139], v226, s[54:55] sc1\n\t"
      "s_waitcnt vmcnt(6)\n\t"
      "v_max3_u32 v234, v140, v141, v142\n\t"
      "v_max_u32_e32 v234, v234, v143\n\t"
      "v_cmp_eq_u32_e32 vcc, -1, v234\n\t"
      "s_cbranch_vccnz FQ3L_%=\n\t"
      "FQ3X_%=:\n\t"
      "s_branch FQ3B_%=\n\t"
      "FQ4R_%=:\n\t"
      "s_mov_b32 s51, 0\n\t"
      "FQ4L_%=:\n\t"
      "s_add_u32 s51, s51, 1\n\t"
      "s_cmp_lt_u32 s51, 262144\n\t"
      "s_cbranch_scc0 FQ4X_%=\n\t"
      "s_waitcnt vmcnt(0)\n\t"
      "s_sleep 1\n\t"
      "global_load_dwordx4 v[144:147], v228, s[52:53] sc1\n\t"
      "global_load_dwordx4 v[148:151], v229, s[52:53] sc1\n\t"
      "global_load_dwordx4 v[152:155], v230, s[52:53] sc1\n\t"
      "global_load_dwordx4 v[128:131], v224, s[54:55] sc1\n\t"
      "global_load_dwordx4 v[132:135], v225, s[54:55] sc1\n\t"
      "global_load_dwordx4 v[136:139], v226, s[54:55] sc1\n\t"
      "global_load_dwordx4 v[140:143], v227, s[54:55] sc1\n\t"
      "s_waitcnt vmcnt(6)\n\t"
      "v_max3_u32 v234, v144, v145, v146\n\t"
      "v_max_u32_e32 v234, v234, v147\n\t"
      "v_cmp_eq_u32_e32 vcc, -1, v234\n\t"
      "s_cbranch_vccnz FQ4L_%=\n\t"
      "FQ4X_%=:\n\t"
      "s_branch FQ4B_%=\n\t"
      "FQ5R_%=:\n\t"
      "s_mov_b32 s51, 0\n\t"
      "FQ5L_%=:\n\t"
      "s_add_u32 s51, s51, 1\n\t"
      "s_cmp_lt_u32 s51, 262144\n\t"
      "s_cbranch_scc0 FQ5X_%=\n\t"
      "s_waitcnt vmcnt(0)\n\t"
      "s_sleep 1\n\t"
      "global_load_dwordx4 v[148:151], v229, s[52:53] sc1\n\t"
      "global_load_dwordx4 v[152:155], v230, s[52:53] sc1\n\t"
      "global_load_dwordx4 v[128:131], v224, s[54:55] sc1\n\t"
      "global_load_dwordx4 v[132:135], v225, s[54:55] sc1\n\t"
      "global_load_dwordx4 v[136:139], v226, s[54:55] sc1\n\t"
      "global_load_dwordx4 v[140:143], v227, s[54:55] sc1\n\t"
      "global_load_dwordx4 v[144:147], v228, s[54:55] sc1\n\t"
      "s_waitcnt vmcnt(6)\n\t"
      "v_max3_u32 v234, v148, v149, v150\n\t"
      "v_max_u32_e32 v234, v234, v151\n\t"
      "v_cmp_eq_u32_e32 vcc, -1, v234\n\t"
      "s_cbranch_vccnz FQ5L_%=\n\t"
      "FQ5X_%=:\n\t"
      "s_branch FQ5B_%=\n\t"
      "FQ6R_%=:\n\t"
      "s_mov_b32 s51, 0\n\t"
      "FQ6L_%=:\n\t"
      "s_add_u32 s51, s51, 1\n\t"
      "s_cmp_lt_u32 s51, 262144\n\t"
      "s_cbranch_scc0 FQ6X_%=\n\t"
      "s_waitcnt vmcnt(0)\n\t"
      "s_sleep 1\n\t"
      "global_load_dwordx4 v[152:155], v230, s[52:53] sc1\n\t"
      "global_load_dwordx4 v[128:131], v224, s[54:55] sc1\n\t"
      "global_load_dwordx4 v[132:135], v225, s[54:55] sc1\n\t"
      "global_load_dwordx4 v[136:139], v226, s[54:55] sc1\n\t"
      "global_load_dwordx4 v[140:143], v227, s[54:55] sc1\n\t"
      "global_load_dwordx4 v[144:147], v228, s[54:55] sc1\n\t"
      "global_load_dwordx4 v[148:151], v229, s[54:55] sc1\n\t"
      "s_waitcnt vmcnt(6)\n\t"
      "v_max3_u32 v234, v152, v153, v154\n\t"
      "v_max_u32_e32 v234, v234, v155\n\t"
      "v_cmp_eq_u32_e32 vcc, -1, v234\n\t"
      "s_cbranch_vccnz FQ6L_%=\n\t"
      "FQ6X_%=:\n\t"
      "s_branch FQ6B_%=\n\t"
      "FQ7R_%=:\n\t"
      "s_mov_b32 s51, 0\n\t"
      "FQ7L_%=:\n\t"
      "s_add_u32 s51, s51, 1\n\t"
      "s_cmp_lt_u32 s51, 262144\n\t"
      "s_cbranch_scc0 FQ7X_%=\n\t"
      "s_waitcnt vmcnt(0)\n\t"
      "s_sleep 4\n\t"
      "global_load_dwordx4 v[128:131], v224, s[54:55] sc1\n\t"
      "global_load_dwordx4 v[132:135], v225, s[54:55] sc1\n\t"
      "global_load_dwordx4 v[136:139], v226, s[54:55] sc1\n\t"
      "global_load_dwordx4 v[140:143], v227, s[54:55] sc1\n\t"
      "global_load_dwordx4 v[144:147], v228, s[54:55] sc1\n\t"
      "global_load_dwordx4 v[148:151], v229, s[54:55] sc1\n\t"
      "s_waitcnt vmcnt(5)\n\t"
      "v_max3_u32 v234, v128, v129, v130\n\t"
      "v_max_u32_e32 v234, v234, v131\n\t"
      "v_cmp_eq_u32_e32 vcc, -1, v234\n\t"
      "s_cbranch_vccnz FQ7L_%=\n\t"
      "FQ7X_%=:\n\t"
      "s_branch FQ7B_%=\n\t"
      "FQ8R_%=:\n\t"
      "s_mov_b32 s51, 0\n\t"
      "FQ8L_%=:\n\t"
      "s_add_u32 s51, s51, 1\n\t"
      "s_cmp_lt_u32 s51, 262144\n\t"
      "s_cbranch_scc0 FQ8X_%=\n\t"
      "s_waitcnt vmcnt(0)\n\t"
      "s_sleep 4\n\t"
      "global_load_dwordx4 v[132:135], v225, s[54:55] sc1\n\t"
      "global_load_dwordx4 v[136:139], v226, s[54:55] sc1\n\t"
      "global_load_dwordx4 v[140:143], v227, s[54:55] sc1\n\t"
      "global_load_dwordx4 v[144:147], v228, s[54:55] sc1\n\t"
      "global_load_dwordx4 v[148:151], v229, s[54:55] sc1\n\t"
      "global_load_dwordx4 v[152:155], v230, s[54:55] sc1\n\t"
      "s_waitcnt vmcnt(5)\n\t"
      "v_max3_u32 v234, v132, v133, v134\n\t"
      "v_max_u32_e32 v234, v234, v135\n\t"
      "v_cmp_eq_u32_e32 vcc, -1, v234\n\t"
      "s_cbranch_vccnz FQ8L_%=\n\t"
      "FQ8X_%=:\n\t"
      "s_branch FQ8B_%=\n\t"
      "FQ9R_%=:\n\t"
      "s_mov_b32 s51, 0\n\t"
      "FQ9L_%=:\n\t"
      "s_add_u32 s51, s51, 1\n\t"
      "s_cmp_lt_u32 s51, 262144\n\t"
      "s_cbranch_scc0 FQ9X_%=\n\t"
      "s_waitcnt vmcnt(0)\n\t"
      "s_sleep 4\n\t"
      "global_load_dwordx4 v[136:139], v226, s[54:55] sc1\n\t"
      "global_load_dwordx4 v[140:143], v227, s[54:55] sc1\n\t"
      "global_load_dwordx4 v[144:147], v228, s[54:55] sc1\n\t"
      "global_load_dwordx4 v[148:151], v229, s[54:55] sc1\n\t"
      "global_load_dwordx4 v[152:155], v230, s[54:55] sc1\n\t"
      "global_load_dwordx4 v[128:131], v224, s[56:57] sc1\n\t"
      "s_waitcnt vmcnt(5)\n\t"
      "v_max3_u32 v234, v136, v137, v138\n\t"
      "v_max_u32_e32 v234, v234, v139\n\t"
      "v_cmp_eq_u32_e32 vcc, -1, v234\n\t"
      "s_cbranch_vccnz FQ9L_%=\n\t"
      "FQ9X_%=:\n\t"
      "s_branch FQ9B_%=\n\t"
      "FQ10R_%=:\n\t"
      "s_mov_b32 s51, 0\n\t"
      "FQ10L_%=:\n\t"
      "s_add_u32 s51, s51, 1\n\t"
      "s_cmp_lt_u32 s51, 262144\n\t"
      "s_cbranch_scc0 FQ10X_%=\n\t"
      "s_waitcnt vmcnt(0)\n\t"
      "s_sleep 4\n\t"
      "global_load_dwordx4 v[140:143], v227, s[54:55] sc1\n\t"
      "global_load_dwordx4 v[144:147], v228, s[54:55] sc1\n\t"
      "global_load_dwordx4 v[148:151], v229, s[54:55] sc1\n\t"
      "global_load_dwordx4 v[152:155], v230, s[54:55] sc1\n\t"
      "global_load_dwordx4 v[128:131], v224, s[56:57] sc1\n\t"
      "global_load_dwordx4 v[132:135], v225, s[56:57] sc1\n\t"
      "s_waitcnt vmcnt(5)\n\t"
      "v_max3_u32 v234, v140, v141, v142\n\t"
      "v_max_u32_e32 v234, v234, v143\n\t"
      "v_cmp_eq_u32_e32 vcc, -1, v234\n\t"
      "s_cbranch_vccnz FQ10L_%=\n\t"
      "FQ10X_%=:\n\t"
      "s_branch FQ10B_%=\n\t"
      "FQ11R_%=:\n\t"
      "s_mov_b32 s51, 0\n\t"
      "FQ11L_%=:\n\t"
      "s_add_u32 s51, s51, 1\n\t"
      "s_cmp_lt_u32 s51, 262144\n\t"
      "s_cbranch_scc0 FQ11X_%=\n\t"
      "s_waitcnt vmcnt(0)\n\t"
      "s_sleep 4\n\t"
      "global_load_dwordx4 v[144:147], v228, s[54:55] sc1\n\t"
      "global_load_dwordx4 v[148:151], v229, s[54:55] sc1\n\t"
      "global_load_dwordx4 v[152:155], v230, s[54:55] sc1\n\t"
      "global_load_dwordx4 v[128:131], v224, s[56:57] sc1\n\t"
      "global_load_dwordx4 v[132:135], v225, s[56:57] sc1\n\t"
      "global_load_dwordx4 v[136:139], v226, s[56:57] sc1\n\t"
      "s_waitcnt vmcnt(5)\n\t"
      "v_max3_u32 v234, v144, v145, v146\n\t"
      "v_max_u32_e32 v234, v234, v147\n\t"
      "v_cmp_eq_u32_e32 vcc, -1, v234\n\t"
      "s_cbranch_vccnz FQ11L_%=\n\t"
      "FQ11X_%=:\n\t"
      "s_branch FQ11B_%=\n\t"
      "FQ12R_%=:\n\t"
      "s_mov_b32 s51, 0\n\t"
      "FQ12L_%=:\n\t"
      "s_add_u32 s51, s51, 1\n\t"
      "s_cmp_lt_u32 s51, 262144\n\t"
      "s_cbranch_scc0 FQ12X_%=\n\t"
      "s_waitcnt vmcnt(0)\n\t"
      "s_sleep 4\n\t"
      "global_load_dwordx4 v[148:151], v229, s[54:55] sc1\n\t"
      "global_load_dwordx4 v[152:155], v230, s[54:55] sc1\n\t"
      "global_load_dwordx4 v[128:131], v224, s[56:57] sc1\n\t"
      "global_load_dwordx4 v[132:135], v225, s[56:57] sc1\n\t"
      "global_load_dwordx4 v[136:139], v226, s[56:57] sc1\n\t"
      "global_load_dwordx4 v[140:143], v227, s[56:57] sc1\n\t"
      "s_waitcnt vmcnt(5)\n\t"
      "v_max3_u32 v234, v148, v149, v150\n\t"
      "v_max_u32_e32 v234, v234, v151\n\t"
      "v_cmp_eq_u32_e32 vcc, -1, v234\n\t"
      "s_cbranch_vccnz FQ12L_%=\n\t"
      "FQ12X_%=:\n\t"
      "s_branch FQ12B_%=\n\t"
      "FQ13R_%=:\n\t"
      "s_mov_b32 s51, 0\n\t"
      "FQ13L_%=:\n\t"
      "s_add_u32 s51, s51, 1\n\t"
      "s_cmp_lt_u32 s51, 262144\n\t"
      "s_cbranch_scc0 FQ13X_%=\n\t"
      "s_waitcnt vmcnt(0)\n\t"
      "s_sleep 4\n\t"
      "global_load_dwordx4 v[152:155], v230, s[54:55] sc1\n\t"
      "global_load_dwordx4 v[128:131], v224, s[56:57] sc1\n\t"
      "global_load_dwordx4 v[132:135], v225, s[56:57] sc1\n\t"
      "global_load_dwordx4 v[136:139], v226, s[56:57] sc1\n\t"
      "global_load_dwordx4 v[140:143], v227, s[56:57] sc1\n\t"
      "global_load_dwordx4 v[144:147], v228, s[56:57] sc1\n\t"
      "s_waitcnt vmcnt(5)\n\t"
      "v_max3_u32 v234, v152, v153, v154\n\t"
      "v_max_u32_e32 v234, v234, v155\n\t"
      "v_cmp_eq_u32_e32 vcc, -1, v234\n\t"
      "s_cbranch_vccnz FQ13L_%=\n\t"
      "FQ13X_%=:\n\t"
      "s_branch FQ13B_%=\n\t"
      "FQ14R_%=:\n\t"
      "s_mov_b32 s51, 0\n\t"
      "FQ14L_%=:\n\t"
      "s_add_u32 s51, s51, 1\n\t"
      "s_cmp_lt_u32 s51, 262144\n\t"
      "s_cbranch_scc0 FQ14X_%=\n\t"
      "s_waitcnt vmcnt(0)\n\t"
      "s_sleep 4\n\t"
      "global_load_dwordx4 v[128:131], v224, s[56:57] sc1\n\t"
      "global_load_dwordx4 v[132:135], v225, s[56:57] sc1\n\t"
      "global_load_dwordx4 v[136:139], v226, s[56:57] sc1\n\t"
      "global_load_dwordx4 v[140:143], v227, s[56:57] sc1\n\t"
      "global_load_dwordx4 v[144:147], v228, s[56:57] sc1\n\t"
      "global_load_dwordx4 v[148:151], v229, s[56:57] sc1\n\t"
      "s_waitcnt vmcnt(5)\n\t"
      "v_max3_u32 v234, v128, v129, v130\n\t"
      "v_max_u32_e32 v234, v234, v131\n\t"
      "v_cmp_eq_u32_e32 vcc, -1, v234\n\t"
      "s_cbranch_vccnz FQ14L_%=\n\t"
      "FQ14X_%=:\n\t"
      "s_branch FQ14B_%=\n\t"
      "NQ1R_%=:\n\t"
      "s_mov_b32 s51, 0\n\t"
      "NQ1L_%=:\n\t"
      "s_add_u32 s51, s51, 1\n\t"
      "s_cmp_lt_u32 s51, 262144\n\t"
      "s_cbranch_scc0 NQ1X_%=\n\t"
      "s_waitcnt vmcnt(0)\n\t"
      "s_sleep 1\n\t"
      "global_load_dwordx4 v[128:131], v224, s[54:55] sc1\n\t"
      "global_load_dwordx4 v[132:135], v225, s[54:55] sc1\n\t"
      "global_load_dwordx4 v[136:139], v226, s[54:55] sc1\n\t"
      "global_load_dwordx4 v[140:143], v227, s[54:55] sc1\n\t"
      "global_load_dwordx4 v[144:147], v228, s[54:55] sc1\n\t"
      "global_load_dwordx4 v[148:151], v229, s[54:55] sc1\n\t"
      "global_load_dwordx4 v[152:155], v230, s[54:55] sc1\n\t"
      "s_waitcnt vmcnt(0)\n\t"
      "v_max3_u32 v234, v128, v129, v130\n\t"
      "v_max3_u32 v234, v234, v131, v132\n\t"
      "v_max3_u32 v234, v234, v133, v134\n\t"
      "v_max3_u32 v234, v234, v135, v136\n\t"
      "v_max3_u32 v234, v234, v137, v138\n\t"
      "v_max3_u32 v234, v234, v139, v140\n\t"
      "v_max3_u32 v234, v234, v141, v142\n\t"
      "v_max3_u32 v234, v234, v143, v144\n\t"
      "v_max3_u32 v234, v234, v145, v146\n\t"
      "v_max3_u32 v234, v234, v147, v148\n\t"
      "v_max3_u32 v234, v234, v149, v150\n\t"
      "v_max3_u32 v234, v234, v151, v152\n\t"
      "v_max3_u32 v234, v234, v153, v154\n\t"
      "v_max_u32_e32 v234, v234, v155\n\t"
      "v_cmp_eq_u32_e32 vcc, -1, v234\n\t"
      "s_cbranch_vccnz NQ1L_%=\n\t"
      "NQ1X_%=:\n\t"
      "v_mfma_f32_16x16x32_bf16 v[200:203], v[128:131], v[0:3], 0\n\t"
      "v_mfma_f32_16x16x32_bf16 v[204:207], v[128:131], v[4:7], 0\n\t"
      "v_mfma_f32_16x16x32_bf16 v[208:211], v[128:131], v[8:11], 0\n\t"
      "v_mfma_f32_16x16x32_bf16 v[212:215], v[128:131], v[12:15], 0\n\t"
      "v_mfma_f32_16x16x32_bf16 v[200:203], v[132:135], v[16:19], v[200:203]\n\t"
      "v_mfma_f32_16x16x32_bf16 v[204:207], v[132:135], v[20:23], v[204:207]\n\t"
      "v_mfma_f32_16x16x32_bf16 v[208:211], v[132:135], v[24:27], v[208:211]\n\t"
      "v_mfma_f32_16x16x32_bf16 v[212:215], v[132:135], v[28:31], v[212:215]\n\t"
      "v_mfma_f32_16x16x32_bf16 v[200:203], v[136:139], v[32:35], v[200:203]\n\t"
      "v_mfma_f32_16x16x32_bf16 v[204:207], v[136:139], v[36:39], v[204:207]\n\t"
      "v_mfma_f32_16x16x32_bf16 v[208:211], v[136:139], v[40:43], v[208:211]\n\t"
      "v_mfma_f32_16x16x32_bf16 v[212:215], v[136:139], v[44:47], v[212:215]\n\t"
      "v_mfma_f32_16x16x32_bf16 v[200:203], v[140:143], v[48:51], v[200:203]\n\t"
      "v_mfma_f32_16x16x32_bf16 v[204:207], v[140:143], v[52:55], v[204:207]\n\t"
      "v_mfma_f32_16x16x32_bf16 v[208:211], v[140:143], v[56:59], v[208:211]\n\t"
      "v_mfma_f32_16x16x32_bf16 v[212:215], v[140:143], v[60:63], v[212:215]\n\t"
      "v_mfma_f32_16x16x32_bf16 v[200:203], v[144:147], v[64:67], v[200:203]\n\t"
      "v_mfma_f32_16x16x32_bf16 v[204:207], v[144:147], v[68:71], v[204:207]\n\t"
      "v_mfma_f32_16x16x32_bf16 v[208:211], v[144:147], v[72:75], v[208:211]\n\t"
      "v_mfma_f32_16x16x32_bf16 v[212:215], v[144:147], v[76:79], v[212:215]\n\t"
      "v_mfma_f32_16x16x32_bf16 v[200:203], v[148:151], v[80:83], v[200:203]\n\t"
      "v_mfma_f32_16x16x32_bf16 v[204:207], v[148:151], v[84:87], v[204:207]\n\t"
      "v_mfma_f32_16x16x32_bf16 v[208:211], v[148:151], v[88:91], v[208:211]\n\t"
      "v_mfma_f32_16x16x32_bf16 v[212:215], v[148:151], v[92:95], v[212:215]\n\t"
      "v_mfma_f32_16x16x32_bf16 v[200:203], v[152:155], v[96:99], v[200:203]\n\t"
      "v_mfma_f32_16x16x32_bf16 v[204:207], v[152:155], v[100:103], v[204:207]\n\t"
      "v_mfma_f32_16x16x32_bf16 v[208:211], v[152:155], v[104:107], v[208:211]\n\t"
      "v_mfma_f32_16x16x32_bf16 v[212:215], v[152:155], v[108:111], v[212:215]\n\t"
      "global_load_dwordx4 v[128:131], v224, s[56:57] sc1\n\t"
      "global_load_dwordx4 v[132:135], v225, s[56:57] sc1\n\t"
      "global_load_dwordx4 v[136:139], v226, s[56:57] sc1\n\t"
      "global_load_dwordx4 v[140:143], v227, s[56:57] sc1\n\t"
      "global_load_dwordx4 v[144:147], v228, s[56:57] sc1\n\t"
      "global_load_dwordx4 v[148:151], v229, s[56:57] sc1\n\t"
      "global_load_dwordx4 v[152:155], v230, s[56:57] sc1\n\t"
      "s_waitcnt vmcnt(0)\n\t"
      "s_nop 7\n\t"
      "s_branch NQ1B_%=\n\t"
      "FQ15R_%=:\n\t"
      "s_mov_b32 s51, 0\n\t"
      "FQ15L_%=:\n\t"
      "s_add_u32 s51, s51, 1\n\t"
      "s_cmp_lt_u32 s51, 262144\n\t"
      "s_cbranch_scc0 FQ15X_%=\n\t"
      "s_waitcnt vmcnt(0)\n\t"
      "s_sleep 4\n\t"
      "global_load_dwordx4 v[132:135], v225, s[56:57] sc1\n\t"
      "global_load_dwordx4 v[136:139], v226, s[56:57] sc1\n\t"
      "global_load_dwordx4 v[140:143], v227, s[56:57] sc1\n\t"
      "global_load_dwordx4 v[144:147], v228, s[56:57] sc1\n\t"
      "global_load_dwordx4 v[148:151], v229, s[56:57] sc1\n\t"
      "global_load_dwordx4 v[152:155], v230, s[56:57] sc1\n\t"
      "s_waitcnt vmcnt(5)\n\t"
      "v_max3_u32 v234, v132, v133, v134\n\t"
      "v_max_u32_e32 v234, v234, v135\n\t"
      "v_cmp_eq_u32_e32 vcc, -1, v234\n\t"
      "s_cbranch_vccnz FQ15L_%=\n\t"
      "FQ15X_%=:\n\t"
      "s_branch FQ15B_%=\n\t"
      "FQ16R_%=:\n\t"
      "s_mov_b32 s51, 0\n\t"
      "FQ16L_%=:\n\t"
      "s_add_u32 s51, s51, 1\n\t"
      "s_cmp_lt_u32 s51, 262144\n\t"
      "s_cbranch_scc0 FQ16X_%=\n\t"
      "s_waitcnt vmcnt(0)\n\t"
      "s_sleep 4\n\t"
      "global_load_dwordx4 v[136:139], v226, s[56:57] sc1\n\t"
      "global_load_dwordx4 v[140:143], v227, s[56:57] sc1\n\t"
      "global_load_dwordx4 v[144:147], v228, s[56:57] sc1\n\t"
      "global_load_dwordx4 v[148:151], v229, s[56:57] sc1\n\t"
      "global_load_dwordx4 v[152:155], v230, s[56:57] sc1\n\t"
      "global_load_dwordx4 v[128:131], v224, s[58:59] sc1\n\t"
      "s_waitcnt vmcnt(5)\n\t"
      "v_max3_u32 v234, v136, v137, v138\n\t"
      "v_max_u32_e32 v234, v234, v139\n\t"
      "v_cmp_eq_u32_e32 vcc, -1, v234\n\t"
      "s_cbranch_vccnz FQ16L_%=\n\t"
      "FQ16X_%=:\n\t"
      "s_branch FQ16B_%=\n\t"
      "FQ17R_%=:\n\t"
      "s_mov_b32 s51, 0\n\t"
      "FQ17L_%=:\n\t"
      "s_add_u32 s51, s51, 1\n\t"
      "s_cmp_lt_u32 s51, 262144\n\t"
      "s_cbranch_scc0 FQ17X_%=\n\t"
      "s_waitcnt vmcnt(0)\n\t"
      "s_sleep 4\n\t"
      "global_load_dwordx4 v[140:143], v227, s[56:57] sc1\n\t"
      "global_load_dwordx4 v[144:147], v228, s[56:57] sc1\n\t"
      "global_load_dwordx4 v[148:151], v229, s[56:57] sc1\n\t"
      "global_load_dwordx4 v[152:155], v230, s[56:57] sc1\n\t"
      "global_load_dwordx4 v[128:131], v224, s[58:59] sc1\n\t"
      "global_load_dwordx4 v[132:135], v225, s[58:59] sc1\n\t"
      "s_waitcnt vmcnt(5)\n\t"
      "v_max3_u32 v234, v140, v141, v142\n\t"
      "v_max_u32_e32 v234, v234, v143\n\t"
      "v_cmp_eq_u32_e32 vcc, -1, v234\n\t"
      "s_cbranch_vccnz FQ17L_%=\n\t"
      "FQ17X_%=:\n\t"
      "s_branch FQ17B_%=\n\t"
      "FQ18R_%=:\n\t"
      "s_mov_b32 s51, 0\n\t"
      "FQ18L_%=:\n\t"
      "s_add_u32 s51, s51, 1\n\t"
      "s_cmp_lt_u32 s51, 262144\n\t"
      "s_cbranch_scc0 FQ18X_%=\n\t"
      "s_waitcnt vmcnt(0)\n\t"
      "s_sleep 4\n\t"
      "global_load_dwordx4 v[144:147], v228, s[56:57] sc1\n\t"
      "global_load_dwordx4 v[148:151], v229, s[56:57] sc1\n\t"
      "global_load_dwordx4 v[152:155], v230, s[56:57] sc1\n\t"
      "global_load_dwordx4 v[128:131], v224, s[58:59] sc1\n\t"
      "global_load_dwordx4 v[132:135], v225, s[58:59] sc1\n\t"
      "global_load_dwordx4 v[136:139], v226, s[58:59] sc1\n\t"
      "s_waitcnt vmcnt(5)\n\t"
      "v_max3_u32 v234, v144, v145, v146\n\t"
      "v_max_u32_e32 v234, v234, v147\n\t"
      "v_cmp_eq_u32_e32 vcc, -1, v234\n\t"
      "s_cbranch_vccnz FQ18L_%=\n\t"
      "FQ18X_%=:\n\t"
      "s_branch FQ18B_%=\n\t"
      "FQ19R_%=:\n\t"
      "s_mov_b32 s51, 0\n\t"
      "FQ19L_%=:\n\t"
      "s_add_u32 s51, s51, 1\n\t"
      "s_cmp_lt_u32 s51, 262144\n\t"
      "s_cbranch_scc0 FQ19X_%=\n\t"
      "s_waitcnt vmcnt(0)\n\t"
      "s_sleep 4\n\t"
      "global_load_dwordx4 v[148:151], v229, s[56:57] sc1\n\t"
      "global_load_dwordx4 v[152:155], v230, s[56:57] sc1\n\t"
      "global_load_dwordx4 v[128:131], v224, s[58:59] sc1\n\t"
      "global_load_dwordx4 v[132:135], v225, s[58:59] sc1\n\t"
      "global_load_dwordx4 v[136:139], v226, s[58:59] sc1\n\t"
      "global_load_dwordx4 v[140:143], v227, s[58:59] sc1\n\t"
      "s_waitcnt vmcnt(5)\n\t"
      "v_max3_u32 v234, v148, v149, v150\n\t"
      "v_max_u32_e32 v234, v234, v151\n\t"
      "v_cmp_eq_u32_e32 vcc, -1, v234\n\t"
      "s_cbranch_vccnz FQ19L_%=\n\t"
      "FQ19X_%=:\n\t"
      "s_branch FQ19B_%=\n\t"
      "FQ20R_%=:\n\t"
      "s_mov_b32 s51, 0\n\t"
      "FQ20L_%=:\n\t"
      "s_add_u32 s51, s51, 1\n\t"
      "s_cmp_lt_u32 s51, 262144\n\t"
      "s_cbranch_scc0 FQ20X_%=\n\t"
      "s_waitcnt vmcnt(0)\n\t"
      "s_sleep 4\n\t"
      "global_load_dwordx4 v[152:155], v230, s[56:57] sc1\n\t"
      "global_load_dwordx4 v[128:131], v224, s[58:59] sc1\n\t"
      "global_load_dwordx4 v[132:135], v225, s[58:59] sc1\n\t"
      "global_load_dwordx4 v[136:139], v226, s[58:59] sc1\n\t"
      "global_load_dwordx4 v[140:143], v227, s[58:59] sc1\n\t"
      "global_load_dwordx4 v[144:147], v228, s[58:59] sc1\n\t"
      "s_waitcnt vmcnt(5)\n\t"
      "v_max3_u32 v234, v152, v153, v154\n\t"
      "v_max_u32_e32 v234, v234, v155\n\t"
      "v_cmp_eq_u32_e32 vcc, -1, v234\n\t"
      "s_cbranch_vccnz FQ20L_%=\n\t"
      "FQ20X_%=:\n\t"
      "s_branch FQ20B_%=\n\t"
      "FQ21R_%=:\n\t"
      "s_mov_b32 s51, 0\n\t"
      "FQ21L_%=:\n\t"
      "s_add_u32 s51, s51, 1\n\t"
      "s_cmp_lt_u32 s51, 262144\n\t"
      "s_cbranch_scc0 FQ21X_%=\n\t"
      "s_waitcnt vmcnt(0)\n\t"
      "s_sleep 4\n\t"
      "global_load_dwordx4 v[128:131], v224, s[58:59] sc1\n\t"
      "global_load_dwordx4 v[132:135], v225, s[58:59] sc1\n\t"
      "global_load_dwordx4 v[136:139], v226, s[58:59] sc1\n\t"
      "global_load_dwordx4 v[140:143], v227, s[58:59] sc1\n\t"
      "global_load_dwordx4 v[144:147], v228, s[58:59] sc1\n\t"
      "global_load_dwordx4 v[148:151], v229, s[58:59] sc1\n\t"
      "s_waitcnt vmcnt(5)\n\t"
      "v_max3_u32 v234, v128, v129, v130\n\t"
      "v_max_u32_e32 v234, v234, v131\n\t"
      "v_cmp_eq_u32_e32 vcc, -1, v234\n\t"
      "s_cbranch_vccnz FQ21L_%=\n\t"
      "FQ21X_%=:\n\t"
      "s_branch FQ21B_%=\n\t"
      "NQ2R_%=:\n\t"
      "s_mov_b32 s51, 0\n\t"
      "NQ2L_%=:\n\t"
      "s_add_u32 s51, s51, 1\n\t"
      "s_cmp_lt_u32 s51, 262144\n\t"
      "s_cbranch_scc0 NQ2X_%=\n\t"
      "s_waitcnt vmcnt(0)\n\t"
      "s_sleep 1\n\t"
      "global_load_dwordx4 v[128:131], v224, s[56:57] sc1\n\t"
      "global_load_dwordx4 v[132:135], v225, s[56:57] sc1\n\t"
      "global_load_dwordx4 v[136:139], v226, s[56:57] sc1\n\t"
      "global_load_dwordx4 v[140:143], v227, s[56:57] sc1\n\t"
      "global_load_dwordx4 v[144:147], v228, s[56:57] sc1\n\t"
      "global_load_dwordx4 v[148:151], v229, s[56:57] sc1\n\t"
      "global_load_dwordx4 v[152:155], v230, s[56:57] sc1\n\t"
      "s_waitcnt vmcnt(0)\n\t"
      "v_max3_u32 v234, v128, v129, v130\n\t"
      "v_max3_u32 v234, v234, v131, v132\n\t"
      "v_max3_u32 v234, v234, v133, v134\n\t"
      "v_max3_u32 v234, v234, v135, v136\n\t"
      "v_max3_u32 v234, v234, v137, v138\n\t"
      "v_max3_u32 v234, v234, v139, v140\n\t"
      "v_max3_u32 v234, v234, v141, v142\n\t"
      "v_max3_u32 v234, v234, v143, v144\n\t"
      "v_max3_u32 v234, v234, v145, v146\n\t"
      "v_max3_u32 v234, v234, v147, v148\n\t"
      "v_max3_u32 v234, v234, v149, v150\n\t"
      "v_max3_u32 v234, v234, v151, v152\n\t"
      "v_max3_u32 v234, v234, v153, v154\n\t"
      "v_max_u32_e32 v234, v234, v155\n\t"
      "v_cmp_eq_u32_e32 vcc, -1, v234\n\t"
      "s_cbranch_vccnz NQ2L_%=\n\t"
      "NQ2X_%=:\n\t"
      "v_mfma_f32_16x16x32_bf16 v[184:187], v[128:131], v[0:3], 0\n\t"
      "v_mfma_f32_16x16x32_bf16 v[188:191], v[128:131], v[4:7], 0\n\t"
      "v_mfma_f32_16x16x32_bf16 v[192:195], v[128:131], v[8:11], 0\n\t"
      "v_mfma_f32_16x16x32_bf16 v[196:199], v[128:131], v[12:15], 0\n\t"
      "v_mfma_f32_16x16x32_bf16 v[184:187], v[132:135], v[16:19], v[184:187]\n\t"
      "v_mfma_f32_16x16x32_bf16 v[188:191], v[132:135], v[20:23], v[188:191]\n\t"
      "v_mfma_f32_16x16x32_bf16 v[192:195], v[132:135], v[24:27], v[192:195]\n\t"
      "v_mfma_f32_16x16x32_bf16 v[196:199], v[132:135], v[28:31], v[196:199]\n\t"
      "v_mfma_f32_16x16x32_bf16 v[184:187], v[136:139], v[32:35], v[184:187]\n\t"
      "v_mfma_f32_16x16x32_bf16 v[188:191], v[136:139], v[36:39], v[188:191]\n\t"
      "v_mfma_f32_16x16x32_bf16 v[192:195], v[136:139], v[40:43], v[192:195]\n\t"
      "v_mfma_f32_16x16x32_bf16 v[196:199], v[136:139], v[44:47], v[196:199]\n\t"
      "v_mfma_f32_16x16x32_bf16 v[184:187], v[140:143], v[48:51], v[184:187]\n\t"
      "v_mfma_f32_16x16x32_bf16 v[188:191], v[140:143], v[52:55], v[188:191]\n\t"
      "v_mfma_f32_16x16x32_bf16 v[192:195], v[140:143], v[56:59], v[192:195]\n\t"
      "v_mfma_f32_16x16x32_bf16 v[196:199], v[140:143], v[60:63], v[196:199]\n\t"
      "v_mfma_f32_16x16x32_bf16 v[184:187], v[144:147], v[64:67], v[184:187]\n\t"
      "v_mfma_f32_16x16x32_bf16 v[188:191], v[144:147], v[68:71], v[188:191]\n\t"
      "v_mfma_f32_16x16x32_bf16 v[192:195], v[144:147], v[72:75], v[192:195]\n\t"
      "v_mfma_f32_16x16x32_bf16 v[196:199], v[144:147], v[76:79], v[196:199]\n\t"
      "v_mfma_f32_16x16x32_bf16 v[184:187], v[148:151], v[80:83], v[184:187]\n\t"
      "v_mfma_f32_16x16x32_bf16 v[188:191], v[148:151], v[84:87], v[188:191]\n\t"
      "v_mfma_f32_16x16x32_bf16 v[192:195], v[148:151], v[88:91], v[192:195]\n\t"
      "v_mfma_f32_16x16x32_bf16 v[196:199], v[148:151], v[92:95], v[196:199]\n\t"
      "v_mfma_f32_16x16x32_bf16 v[184:187], v[152:155], v[96:99], v[184:187]\n\t"
      "v_mfma_f32_16x16x32_bf16 v[188:191], v[152:155], v[100:103], v[188:191]\n\t"
      "v_mfma_f32_16x16x32_bf16 v[192:195], v[152:155], v[104:107], v[192:195]\n\t"
      "v_mfma_f32_16x16x32_bf16 v[196:199], v[152:155], v[108:111], v[196:199]\n\t"
      "global_load_dwordx4 v[128:131], v224, s[58:59] sc1\n\t"
      "global_load_dwordx4 v[132:135], v225, s[58:59] sc1\n\t"
      "global_load_dwordx4 v[136:139], v226, s[58:59] sc1\n\t"
      "global_load_dwordx4 v[140:143], v227, s[58:59] sc1\n\t"
      "global_load_dwordx4 v[144:147], v228, s[58:59] sc1\n\t"
      "global_load_dwordx4 v[148:151], v229, s[58:59] sc1\n\t"
      "global_load_dwordx4 v[152:155], v230, s[58:59] sc1\n\t"
      "s_waitcnt vmcnt(0)\n\t"
      "s_nop 7\n\t"
      "s_branch NQ2B_%=\n\t"
      "FQ22R_%=:\n\t"
      "s_mov_b32 s51, 0\n\t"
      "FQ22L_%=:\n\t"
      "s_add_u32 s51, s51, 1\n\t"
      "s_cmp_lt_u32 s51, 262144\n\t"
      "s_cbranch_scc0 FQ22X_%=\n\t"
      "s_waitcnt vmcnt(0)\n\t"
      "s_sleep 4\n\t"
      "global_load_dwordx4 v[132:135], v225, s[58:59] sc1\n\t"
      "global_load_dwordx4 v[136:139], v226, s[58:59] sc1\n\t"
      "global_load_dwordx4 v[140:143], v227, s[58:59] sc1\n\t"
      "global_load_dwordx4 v[144:147], v228, s[58:59] sc1\n\t"
      "global_load_dwordx4 v[148:151], v229, s[58:59] sc1\n\t"
      "global_load_dwordx4 v[152:155], v230, s[58:59] sc1\n\t"
      "s_waitcnt vmcnt(5)\n\t"
      "v_max3_u32 v234, v132, v133, v134\n\t"
      "v_max_u32_e32 v234, v234, v135\n\t"
      "v_cmp_eq_u32_e32 vcc, -1, v234\n\t"
      "s_cbranch_vccnz FQ22L_%=\n\t"
      "FQ22X_%=:\n\t"
      "s_branch FQ22B_%=\n\t"
      "FQ23R_%=:\n\t"
      "s_mov_b32 s51, 0\n\t"
      "FQ23L_%=:\n\t"
      "s_add_u32 s51, s51, 1\n\t"
      "s_cmp_lt_u32 s51, 262144\n\t"
      "s_cbranch_scc0 FQ23X_%=\n\t"
      "s_waitcnt vmcnt(0)\n\t"
      "s_sleep 4\n\t"
      "global_load_dwordx4 v[136:139], v226, s[58:59] sc1\n\t"
      "global_load_dwordx4 v[140:143], v227, s[58:59] sc1\n\t"
      "global_load_dwordx4 v[144:147], v228, s[58:59] sc1\n\t"
      "global_load_dwordx4 v[148:151], v229, s[58:59] sc1\n\t"
      "global_load_dwordx4 v[152:155], v230, s[58:59] sc1\n\t"
      "global_load_dwordx4 v[128:131], v224, s[60:61] sc1\n\t"
      "s_waitcnt vmcnt(5)\n\t"
      "v_max3_u32 v234, v136, v137, v138\n\t"
      "v_max_u32_e32 v234, v234, v139\n\t"
      "v_cmp_eq_u32_e32 vcc, -1, v234\n\t"
      "s_cbranch_vccnz FQ23L_%=\n\t"
      "FQ23X_%=:\n\t"
      "s_branch FQ23B_%=\n\t"
      "FQ24R_%=:\n\t"
      "s_mov_b32 s51, 0\n\t"
      "FQ24L_%=:\n\t"
      "s_add_u32 s51, s51, 1\n\t"
      "s_cmp_lt_u32 s51, 262144\n\t"
      "s_cbranch_scc0 FQ24X_%=\n\t"
      "s_waitcnt vmcnt(0)\n\t"
      "s_sleep 4\n\t"
      "global_load_dwordx4 v[140:143], v227, s[58:59] sc1\n\t"
      "global_load_dwordx4 v[144:147], v228, s[58:59] sc1\n\t"
      "global_load_dwordx4 v[148:151], v229, s[58:59] sc1\n\t"
      "global_load_dwordx4 v[152:155], v230, s[58:59] sc1\n\t"
      "global_load_dwordx4 v[128:131], v224, s[60:61] sc1\n\t"
      "global_load_dwordx4 v[132:135], v225, s[60:61] sc1\n\t"
      "s_waitcnt vmcnt(5)\n\t"
      "v_max3_u32 v234, v140, v141, v142\n\t"
      "v_max_u32_e32 v234, v234, v143\n\t"
      "v_cmp_eq_u32_e32 vcc, -1, v234\n\t"
      "s_cbranch_vccnz FQ24L_%=\n\t"
      "FQ24X_%=:\n\t"
      "s_branch FQ24B_%=\n\t"
      "FQ25R_%=:\n\t"
      "s_mov_b32 s51, 0\n\t"
      "FQ25L_%=:\n\t"
      "s_add_u32 s51, s51, 1\n\t"
      "s_cmp_lt_u32 s51, 262144\n\t"
      "s_cbranch_scc0 FQ25X_%=\n\t"
      "s_waitcnt vmcnt(0)\n\t"
      "s_sleep 4\n\t"
      "global_load_dwordx4 v[144:147], v228, s[58:59] sc1\n\t"
      "global_load_dwordx4 v[148:151], v229, s[58:59] sc1\n\t"
      "global_load_dwordx4 v[152:155], v230, s[58:59] sc1\n\t"
      "global_load_dwordx4 v[128:131], v224, s[60:61] sc1\n\t"
      "global_load_dwordx4 v[132:135], v225, s[60:61] sc1\n\t"
      "global_load_dwordx4 v[136:139], v226, s[60:61] sc1\n\t"
      "s_waitcnt vmcnt(5)\n\t"
      "v_max3_u32 v234, v144, v145, v146\n\t"
      "v_max_u32_e32 v234, v234, v147\n\t"
      "v_cmp_eq_u32_e32 vcc, -1, v234\n\t"
      "s_cbranch_vccnz FQ25L_%=\n\t"
      "FQ25X_%=:\n\t"
      "s_branch FQ25B_%=\n\t"
      "FQ26R_%=:\n\t"
      "s_mov_b32 s51, 0\n\t"
      "FQ26L_%=:\n\t"
      "s_add_u32 s51, s51, 1\n\t"
      "s_cmp_lt_u32 s51, 262144\n\t"
      "s_cbranch_scc0 FQ26X_%=\n\t"
      "s_waitcnt vmcnt(0)\n\t"
      "s_sleep 4\n\t"
      "global_load_dwordx4 v[148:151], v229, s[58:59] sc1\n\t"
      "global_load_dwordx4 v[152:155], v230, s[58:59] sc1\n\t"
      "global_load_dwordx4 v[128:131], v224, s[60:61] sc1\n\t"
      "global_load_dwordx4 v[132:135], v225, s[60:61] sc1\n\t"
      "global_load_dwordx4 v[136:139], v226, s[60:61] sc1\n\t"
      "global_load_dwordx4 v[140:143], v227, s[60:61] sc1\n\t"
      "s_waitcnt vmcnt(5)\n\t"
      "v_max3_u32 v234, v148, v149, v150\n\t"
      "v_max_u32_e32 v234, v234, v151\n\t"
      "v_cmp_eq_u32_e32 vcc, -1, v234\n\t"
      "s_cbranch_vccnz FQ26L_%=\n\t"
      "FQ26X_%=:\n\t"
      "s_branch FQ26B_%=\n\t"
      "FQ27R_%=:\n\t"
      "s_mov_b32 s51, 0\n\t"
      "FQ27L_%=:\n\t"
      "s_add_u32 s51, s51, 1\n\t"
      "s_cmp_lt_u32 s51, 262144\n\t"
      "s_cbranch_scc0 FQ27X_%=\n\t"
      "s_waitcnt vmcnt(0)\n\t"
      "s_sleep 4\n\t"
      "global_load_dwordx4 v[152:155], v230, s[58:59] sc1\n\t"
      "global_load_dwordx4 v[128:131], v224, s[60:61] sc1\n\t"
      "global_load_dwordx4 v[132:135], v225, s[60:61] sc1\n\t"
      "global_load_dwordx4 v[136:139], v226, s[60:61] sc1\n\t"
      "global_load_dwordx4 v[140:143], v227, s[60:61] sc1\n\t"
      "global_load_dwordx4 v[144:147], v228, s[60:61] sc1\n\t"
      "s_waitcnt vmcnt(5)\n\t"
      "v_max3_u32 v234, v152, v153, v154\n\t"
      "v_max_u32_e32 v234, v234, v155\n\t"
      "v_cmp_eq_u32_e32 vcc, -1, v234\n\t"
      "s_cbranch_vccnz FQ27L_%=\n\t"
      "FQ27X_%=:\n\t"
      "s_branch FQ27B_%=\n\t"
      "FQ28R_%=:\n\t"
      "s_mov_b32 s51, 0\n\t"
      "FQ28L_%=:\n\t"
      "s_add_u32 s51, s51, 1\n\t"
      "s_cmp_lt_u32 s51, 262144\n\t"
      "s_cbranch_scc0 FQ28X_%=\n\t"
      "s_waitcnt vmcnt(0)\n\t"
      "s_sleep 4\n\t"
      "global_load_dwordx4 v[128:131], v224, s[60:61] sc1\n\t"
      "global_load_dwordx4 v[132:135], v225, s[60:61] sc1\n\t"
      "global_load_dwordx4 v[136:139], v226, s[60:61] sc1\n\t"
      "global_load_dwordx4 v[140:143], v227, s[60:61] sc1\n\t"
      "global_load_dwordx4 v[144:147], v228, s[60:61] sc1\n\t"
      "global_load_dwordx4 v[148:151], v229, s[60:61] sc1\n\t"
      "s_waitcnt vmcnt(5)\n\t"
      "v_max3_u32 v234, v128, v129, v130\n\t"
      "v_max_u32_e32 v234, v234, v131\n\t"
      "v_cmp_eq_u32_e32 vcc, -1, v234\n\t"
      "s_cbranch_vccnz FQ28L_%=\n\t"
      "FQ28X_%=:\n\t"
      "s_branch FQ28B_%=\n\t"
      "NQ3R_%=:\n\t"
      "s_mov_b32 s51, 0\n\t"
      "NQ3L_%=:\n\t"
      "s_add_u32 s51, s51, 1\n\t"
      "s_cmp_lt_u32 s51, 262144\n\t"
      "s_cbranch_scc0 NQ3X_%=\n\t"
      "s_waitcnt vmcnt(0)\n\t"
      "s_sleep 1\n\t"
      "global_load_dwordx4 v[128:131], v224, s[58:59] sc1\n\t"
      "global_load_dwordx4 v[132:135], v225, s[58:59] sc1\n\t"
      "global_load_dwordx4 v[136:139], v226, s[58:59] sc1\n\t"
      "global_load_dwordx4 v[140:143], v227, s[58:59] sc1\n\t"
      "global_load_dwordx4 v[144:147], v228, s[58:59] sc1\n\t"
      "global_load_dwordx4 v[148:151], v229, s[58:59] sc1\n\t"
      "global_load_dwordx4 v[152:155], v230, s[58:59] sc1\n\t"
      "s_waitcnt vmcnt(0)\n\t"
      "v_max3_u32 v234, v128, v129, v130\n\t"
      "v_max3_u32 v234, v234, v131, v132\n\t"
      "v_max3_u32 v234, v234, v133, v134\n\t"
      "v_max3_u32 v234, v234, v135, v136\n\t"
      "v_max3_u32 v234, v234, v137, v138\n\t"
      "v_max3_u32 v234, v234, v139, v140\n\t"
      "v_max3_u32 v234, v234, v141, v142\n\t"
      "v_max3_u32 v234, v234, v143, v144\n\t"
      "v_max3_u32 v234, v234, v145, v146\n\t"
      "v_max3_u32 v234, v234, v147, v148\n\t"
      "v_max3_u32 v234, v234, v149, v150\n\t"
      "v_max3_u32 v234, v234, v151, v152\n\t"
      "v_max3_u32 v234, v234, v153, v154\n\t"
      "v_max_u32_e32 v234, v234, v155\n\t"
      "v_cmp_eq_u32_e32 vcc, -1, v234\n\t"
      "s_cbranch_vccnz NQ3L_%=\n\t"
      "NQ3X_%=:\n\t"
      "v_mfma_f32_16x16x32_bf16 v[200:203], v[128:131], v[0:3], 0\n\t"
      "v_mfma_f32_16x16x32_bf16 v[204:207], v[128:131], v[4:7], 0\n\t"
      "v_mfma_f32_16x16x32_bf16 v[208:211], v[128:131], v[8:11], 0\n\t"
      "v_mfma_f32_16x16x32_bf16 v[212:215], v[128:131], v[12:15], 0\n\t"
      "v_mfma_f32_16x16x32_bf16 v[200:203], v[132:135], v[16:19], v[200:203]\n\t"
      "v_mfma_f32_16x16x32_bf16 v[204:207], v[132:135], v[20:23], v[204:207]\n\t"
      "v_mfma_f32_16x16x32_bf16 v[208:211], v[132:135], v[24:27], v[208:211]\n\t"
      "v_mfma_f32_16x16x32_bf16 v[212:215], v[132:135], v[28:31], v[212:215]\n\t"
      "v_mfma_f32_16x16x32_bf16 v[200:203], v[136:139], v[32:35], v[200:203]\n\t"
      "v_mfma_f32_16x16x32_bf16 v[204:207], v[136:139], v[36:39], v[204:207]\n\t"
      "v_mfma_f32_16x16x32_bf16 v[208:211], v[136:139], v[40:43], v[208:211]\n\t"
      "v_mfma_f32_16x16x32_bf16 v[212:215], v[136:139], v[44:47], v[212:215]\n\t"
      "v_mfma_f32_16x16x32_bf16 v[200:203], v[140:143], v[48:51], v[200:203]\n\t"
      "v_mfma_f32_16x16x32_bf16 v[204:207], v[140:143], v[52:55], v[204:207]\n\t"
      "v_mfma_f32_16x16x32_bf16 v[208:211], v[140:143], v[56:59], v[208:211]\n\t"
      "v_mfma_f32_16x16x32_bf16 v[212:215], v[140:143], v[60:63], v[212:215]\n\t"
      "v_mfma_f32_16x16x32_bf16 v[200:203], v[144:147], v[64:67], v[200:203]\n\t"
      "v_mfma_f32_16x16x32_bf16 v[204:207], v[144:147], v[68:71], v[204:207]\n\t"
      "v_mfma_f32_16x16x32_bf16 v[208:211], v[144:147], v[72:75], v[208:211]\n\t"
      "v_mfma_f32_16x16x32_bf16 v[212:215], v[144:147], v[76:79], v[212:215]\n\t"
      "v_mfma_f32_16x16x32_bf16 v[200:203], v[148:151], v[80:83], v[200:203]\n\t"
      "v_mfma_f32_16x16x32_bf16 v[204:207], v[148:151], v[84:87], v[204:207]\n\t"
      "v_mfma_f32_16x16x32_bf16 v[208:211], v[148:151], v[88:91], v[208:211]\n\t"
      "v_mfma_f32_16x16x32_bf16 v[212:215], v[148:151], v[92:95], v[212:215]\n\t"
      "v_mfma_f32_16x16x32_bf16 v[200:203], v[152:155], v[96:99], v[200:203]\n\t"
      "v_mfma_f32_16x16x32_bf16 v[204:207], v[152:155], v[100:103], v[204:207]\n\t"
      "v_mfma_f32_16x16x32_bf16 v[208:211], v[152:155], v[104:107], v[208:211]\n\t"
      "v_mfma_f32_16x16x32_bf16 v[212:215], v[152:155], v[108:111], v[212:215]\n\t"
      "global_load_dwordx4 v[128:131], v224, s[60:61] sc1\n\t"
      "global_load_dwordx4 v[132:135], v225, s[60:61] sc1\n\t"
      "global_load_dwordx4 v[136:139], v226, s[60:61] sc1\n\t"
      "global_load_dwordx4 v[140:143], v227, s[60:61] sc1\n\t"
      "global_load_dwordx4 v[144:147], v228, s[60:61] sc1\n\t"
      "global_load_dwordx4 v[148:151], v229, s[60:61] sc1\n\t"
      "global_load_dwordx4 v[152:155], v230, s[60:61] sc1\n\t"
      "s_waitcnt vmcnt(0)\n\t"
      "s_nop 7\n\t"
      "s_branch NQ3B_%=\n\t"
      "FQ29R_%=:\n\t"
      "s_mov_b32 s51, 0\n\t"
      "FQ29L_%=:\n\t"
      "s_add_u32 s51, s51, 1\n\t"
      "s_cmp_lt_u32 s51, 262144\n\t"
      "s_cbranch_scc0 FQ29X_%=\n\t"
      "s_waitcnt vmcnt(0)\n\t"
      "s_sleep 4\n\t"
      "global_load_dwordx4 v[132:135], v225, s[60:61] sc1\n\t"
      "global_load_dwordx4 v[136:139], v226, s[60:61] sc1\n\t"
      "global_load_dwordx4 v[140:143], v227, s[60:61] sc1\n\t"
      "global_load_dwordx4 v[144:147], v228, s[60:61] sc1\n\t"
      "global_load_dwordx4 v[148:151], v229, s[60:61] sc1\n\t"
      "global_load_dwordx4 v[152:155], v230, s[60:61] sc1\n\t"
      "s_waitcnt vmcnt(5)\n\t"
      "v_max3_u32 v234, v132, v133, v134\n\t"
      "v_max_u32_e32 v234, v234, v135\n\t"
      "v_cmp_eq_u32_e32 vcc, -1, v234\n\t"
      "s_cbranch_vccnz FQ29L_%=\n\t"
      "FQ29X_%=:\n\t"
      "s_branch FQ29B_%=\n\t"
      "FQ30R_%=:\n\t"
      "s_mov_b32 s51, 0\n\t"
      "FQ30L_%=:\n\t"
      "s_add_u32 s51, s51, 1\n\t"
      "s_cmp_lt_u32 s51, 262144\n\t"
      "s_cbranch_scc0 FQ30X_%=\n\t"
      "s_waitcnt vmcnt(0)\n\t"
      "s_sleep 4\n\t"
      "global_load_dwordx4 v[136:139], v226, s[60:61] sc1\n\t"
      "global_load_dwordx4 v[140:143], v227, s[60:61] sc1\n\t"
      "global_load_dwordx4 v[144:147], v228, s[60:61] sc1\n\t"
      "global_load_dwordx4 v[148:151], v229, s[60:61] sc1\n\t"
      "global_load_dwordx4 v[152:155], v230, s[60:61] sc1\n\t"
      "global_load_dwordx4 v[128:131], v224, s[62:63] sc1\n\t"
      "s_waitcnt vmcnt(5)\n\t"
      "v_max3_u32 v234, v136, v137, v138\n\t"
      "v_max_u32_e32 v234, v234, v139\n\t"
      "v_cmp_eq_u32_e32 vcc, -1, v234\n\t"
      "s_cbranch_vccnz FQ30L_%=\n\t"
      "FQ30X_%=:\n\t"
      "s_branch FQ30B_%=\n\t"
      "FQ31R_%=:\n\t"
      "s_mov_b32 s51, 0\n\t"
      "FQ31L_%=:\n\t"
      "s_add_u32 s51, s51, 1\n\t"
      "s_cmp_lt_u32 s51, 262144\n\t"
      "s_cbranch_scc0 FQ31X_%=\n\t"
      "s_waitcnt vmcnt(0)\n\t"
      "s_sleep 4\n\t"
      "global_load_dwordx4 v[140:143], v227, s[60:61] sc1\n\t"
      "global_load_dwordx4 v[144:147], v228, s[60:61] sc1\n\t"
      "global_load_dwordx4 v[148:151], v229, s[60:61] sc1\n\t"
      "global_load_dwordx4 v[152:155], v230, s[60:61] sc1\n\t"
      "global_load_dwordx4 v[128:131], v224, s[62:63] sc1\n\t"
      "global_load_dwordx4 v[132:135], v225, s[62:63] sc1\n\t"
      "s_waitcnt vmcnt(5)\n\t"
      "v_max3_u32 v234, v140, v141, v142\n\t"
      "v_max_u32_e32 v234, v234, v143\n\t"
      "v_cmp_eq_u32_e32 vcc, -1, v234\n\t"
      "s_cbranch_vccnz FQ31L_%=\n\t"
      "FQ31X_%=:\n\t"
      "s_branch FQ31B_%=\n\t"
      "FQ32R_%=:\n\t"
      "s_mov_b32 s51, 0\n\t"
      "FQ32L_%=:\n\t"
      "s_add_u32 s51, s51, 1\n\t"
      "s_cmp_lt_u32 s51, 262144\n\t"
      "s_cbranch_scc0 FQ32X_%=\n\t"
      "s_waitcnt vmcnt(0)\n\t"
      "s_sleep 4\n\t"
      "global_load_dwordx4 v[144:147], v228, s[60:61] sc1\n\t"
      "global_load_dwordx4 v[148:151], v229, s[60:61] sc1\n\t"
      "global_load_dwordx4 v[152:155], v230, s[60:61] sc1\n\t"
      "global_load_dwordx4 v[128:131], v224, s[62:63] sc1\n\t"
      "global_load_dwordx4 v[132:135], v225, s[62:63] sc1\n\t"
      "global_load_dwordx4 v[136:139], v226, s[62:63] sc1\n\t"
      "s_waitcnt vmcnt(5)\n\t"
      "v_max3_u32 v234, v144, v145, v146\n\t"
      "v_max_u32_e32 v234, v234, v147\n\t"
      "v_cmp_eq_u32_e32 vcc, -1, v234\n\t"
      "s_cbranch_vccnz FQ32L_%=\n\t"
      "FQ32X_%=:\n\t"
      "s_branch FQ32B_%=\n\t"
      "FQ33R_%=:\n\t"
      "s_mov_b32 s51, 0\n\t"
      "FQ33L_%=:\n\t"
      "s_add_u32 s51, s51, 1\n\t"
      "s_cmp_lt_u32 s51, 262144\n\t"
      "s_cbranch_scc0 FQ33X_%=\n\t"
      "s_waitcnt vmcnt(0)\n\t"
      "s_sleep 4\n\t"
      "global_load_dwordx4 v[148:151], v229, s[60:61] sc1\n\t"
      "global_load_dwordx4 v[152:155], v230, s[60:61] sc1\n\t"
      "global_load_dwordx4 v[128:131], v224, s[62:63] sc1\n\t"
      "global_load_dwordx4 v[132:135], v225, s[62:63] sc1\n\t"
      "global_load_dwordx4 v[136:139], v226, s[62:63] sc1\n\t"
      "global_load_dwordx4 v[140:143], v227, s[62:63] sc1\n\t"
      "s_waitcnt vmcnt(5)\n\t"
      "v_max3_u32 v234, v148, v149, v150\n\t"
      "v_max_u32_e32 v234, v234, v151\n\t"
      "v_cmp_eq_u32_e32 vcc, -1, v234\n\t"
      "s_cbranch_vccnz FQ33L_%=\n\t"
      "FQ33X_%=:\n\t"
      "s_branch FQ33B_%=\n\t"
      "FQ34R_%=:\n\t"
      "s_mov_b32 s51, 0\n\t"
      "FQ34L_%=:\n\t"
      "s_add_u32 s51, s51, 1\n\t"
      "s_cmp_lt_u32 s51, 262144\n\t"
      "s_cbranch_scc0 FQ34X_%=\n\t"
      "s_waitcnt vmcnt(0)\n\t"
      "s_sleep 4\n\t"
      "global_load_dwordx4 v[152:155], v230, s[60:61] sc1\n\t"
      "global_load_dwordx4 v[128:131], v224, s[62:63] sc1\n\t"
      "global_load_dwordx4 v[132:135], v225, s[62:63] sc1\n\t"
      "global_load_dwordx4 v[136:139], v226, s[62:63] sc1\n\t"
      "global_load_dwordx4 v[140:143], v227, s[62:63] sc1\n\t"
      "global_load_dwordx4 v[144:147], v228, s[62:63] sc1\n\t"
      "s_waitcnt vmcnt(5)\n\t"
      "v_max3_u32 v234, v152, v153, v154\n\t"
      "v_max_u32_e32 v234, v234, v155\n\t"
      "v_cmp_eq_u32_e32 vcc, -1, v234\n\t"
      "s_cbranch_vccnz FQ34L_%=\n\t"
      "FQ34X_%=:\n\t"
      "s_branch FQ34B_%=\n\t"
      "FQ35R_%=:\n\t"
      "s_mov_b32 s51, 0\n\t"
      "FQ35L_%=:\n\t"
      "s_add_u32 s51, s51, 1\n\t"
      "s_cmp_lt_u32 s51, 262144\n\t"
      "s_cbranch_scc0 FQ35X_%=\n\t"
      "s_waitcnt vmcnt(0)\n\t"
      "s_sleep 4\n\t"
      "global_load_dwordx4 v[128:131], v224, s[62:63] sc1\n\t"
      "global_load_dwordx4 v[132:135], v225, s[62:63] sc1\n\t"
      "global_load_dwordx4 v[136:139], v226, s[62:63] sc1\n\t"
      "global_load_dwordx4 v[140:143], v227, s[62:63] sc1\n\t"
      "global_load_dwordx4 v[144:147], v228, s[62:63] sc1\n\t"
      "global_load_dwordx4 v[148:151], v229, s[62:63] sc1\n\t"
      "s_waitcnt vmcnt(5)\n\t"
      "v_max3_u32 v234, v128, v129, v130\n\t"
      "v_max_u32_e32 v234, v234, v131\n\t"
      "v_cmp_eq_u32_e32 vcc, -1, v234\n\t"
      "s_cbranch_vccnz FQ35L_%=\n\t"
      "FQ35X_%=:\n\t"
      "s_branch FQ35B_%=\n\t"
      "NQ4R_%=:\n\t"
      "s_mov_b32 s51, 0\n\t"
      "NQ4L_%=:\n\t"
      "s_add_u32 s51, s51, 1\n\t"
      "s_cmp_lt_u32 s51, 262144\n\t"
      "s_cbranch_scc0 NQ4X_%=\n\t"
      "s_waitcnt vmcnt(0)\n\t"
      "s_sleep 1\n\t"
      "global_load_dwordx4 v[128:131], v224, s[60:61] sc1\n\t"
      "global_load_dwordx4 v[132:135], v225, s[60:61] sc1\n\t"
      "global_load_dwordx4 v[136:139], v226, s[60:61] sc1\n\t"
      "global_load_dwordx4 v[140:143], v227, s[60:61] sc1\n\t"
      "global_load_dwordx4 v[144:147], v228, s[60:61] sc1\n\t"
      "global_load_dwordx4 v[148:151], v229, s[60:61] sc1\n\t"
      "global_load_dwordx4 v[152:155], v230, s[60:61] sc1\n\t"
      "s_waitcnt vmcnt(0)\n\t"
      "v_max3_u32 v234, v128, v129, v130\n\t"
      "v_max3_u32 v234, v234, v131, v132\n\t"
      "v_max3_u32 v234, v234, v133, v134\n\t"
      "v_max3_u32 v234, v234, v135, v136\n\t"
      "v_max3_u32 v234, v234, v137, v138\n\t"
      "v_max3_u32 v234, v234, v139, v140\n\t"
      "v_max3_u32 v234, v234, v141, v142\n\t"
      "v_max3_u32 v234, v234, v143, v144\n\t"
      "v_max3_u32 v234, v234, v145, v146\n\t"
      "v_max3_u32 v234, v234, v147, v148\n\t"
      "v_max3_u32 v234, v234, v149, v150\n\t"
      "v_max3_u32 v234, v234, v151, v152\n\t"
      "v_max3_u32 v234, v234, v153, v154\n\t"
      "v_max_u32_e32 v234, v234, v155\n\t"
      "v_cmp_eq_u32_e32 vcc, -1, v234\n\t"
      "s_cbranch_vccnz NQ4L_%=\n\t"
      "NQ4X_%=:\n\t"
      "v_mfma_f32_16x16x32_bf16 v[184:187], v[128:131], v[0:3], 0\n\t"
      "v_mfma_f32_16x16x32_bf16 v[188:191], v[128:131], v[4:7], 0\n\t"
      "v_mfma_f32_16x16x32_bf16 v[192:195], v[128:131], v[8:11], 0\n\t"
      "v_mfma_f32_16x16x32_bf16 v[196:199], v[128:131], v[12:15], 0\n\t"
      "v_mfma_f32_16x16x32_bf16 v[184:187], v[132:135], v[16:19], v[184:187]\n\t"
      "v_mfma_f32_16x16x32_bf16 v[188:191], v[132:135], v[20:23], v[188:191]\n\t"
      "v_mfma_f32_16x16x32_bf16 v[192:195], v[132:135], v[24:27], v[192:195]\n\t"
      "v_mfma_f32_16x16x32_bf16 v[196:199], v[132:135], v[28:31], v[196:199]\n\t"
      "v_mfma_f32_16x16x32_bf16 v[184:187], v[136:139], v[32:35], v[184:187]\n\t"
      "v_mfma_f32_16x16x32_bf16 v[188:191], v[136:139], v[36:39], v[188:191]\n\t"
      "v_mfma_f32_16x16x32_bf16 v[192:195], v[136:139], v[40:43], v[192:195]\n\t"
      "v_mfma_f32_16x16x32_bf16 v[196:199], v[136:139], v[44:47], v[196:199]\n\t"
      "v_mfma_f32_16x16x32_bf16 v[184:187], v[140:143], v[48:51], v[184:187]\n\t"
      "v_mfma_f32_16x16x32_bf16 v[188:191], v[140:143], v[52:55], v[188:191]\n\t"
      "v_mfma_f32_16x16x32_bf16 v[192:195], v[140:143], v[56:59], v[192:195]\n\t"
      "v_mfma_f32_16x16x32_bf16 v[196:199], v[140:143], v[60:63], v[196:199]\n\t"
      "v_mfma_f32_16x16x32_bf16 v[184:187], v[144:147], v[64:67], v[184:187]\n\t"
      "v_mfma_f32_16x16x32_bf16 v[188:191], v[144:147], v[68:71], v[188:191]\n\t"
      "v_mfma_f32_16x16x32_bf16 v[192:195], v[144:147], v[72:75], v[192:195]\n\t"
      "v_mfma_f32_16x16x32_bf16 v[196:199], v[144:147], v[76:79], v[196:199]\n\t"
      "v_mfma_f32_16x16x32_bf16 v[184:187], v[148:151], v[80:83], v[184:187]\n\t"
      "v_mfma_f32_16x16x32_bf16 v[188:191], v[148:151], v[84:87], v[188:191]\n\t"
      "v_mfma_f32_16x16x32_bf16 v[192:195], v[148:151], v[88:91], v[192:195]\n\t"
      "v_mfma_f32_16x16x32_bf16 v[196:199], v[148:151], v[92:95], v[196:199]\n\t"
      "v_mfma_f32_16x16x32_bf16 v[184:187], v[152:155], v[96:99], v[184:187]\n\t"
      "v_mfma_f32_16x16x32_bf16 v[188:191], v[152:155], v[100:103], v[188:191]\n\t"
      "v_mfma_f32_16x16x32_bf16 v[192:195], v[152:155], v[104:107], v[192:195]\n\t"
      "v_mfma_f32_16x16x32_bf16 v[196:199], v[152:155], v[108:111], v[196:199]\n\t"
      "global_load_dwordx4 v[128:131], v224, s[62:63] sc1\n\t"
      "global_load_dwordx4 v[132:135], v225, s[62:63] sc1\n\t"
      "global_load_dwordx4 v[136:139], v226, s[62:63] sc1\n\t"
      "global_load_dwordx4 v[140:143], v227, s[62:63] sc1\n\t"
      "global_load_dwordx4 v[144:147], v228, s[62:63] sc1\n\t"
      "global_load_dwordx4 v[148:151], v229, s[62:63] sc1\n\t"
      "global_load_dwordx4 v[152:155], v230, s[62:63] sc1\n\t"
      "s_waitcnt vmcnt(0)\n\t"
      "s_nop 7\n\t"
      "s_branch NQ4B_%=\n\t"
      "FQ36R_%=:\n\t"
      "s_mov_b32 s51, 0\n\t"
      "FQ36L_%=:\n\t"
      "s_add_u32 s51, s51, 1\n\t"
      "s_cmp_lt_u32 s51, 262144\n\t"
      "s_cbranch_scc0 FQ36X_%=\n\t"
      "s_waitcnt vmcnt(0)\n\t"
      "s_sleep 4\n\t"
      "global_load_dwordx4 v[132:135], v225, s[62:63] sc1\n\t"
      "global_load_dwordx4 v[136:139], v226, s[62:63] sc1\n\t"
      "global_load_dwordx4 v[140:143], v227, s[62:63] sc1\n\t"
      "global_load_dwordx4 v[144:147], v228, s[62:63] sc1\n\t"
      "global_load_dwordx4 v[148:151], v229, s[62:63] sc1\n\t"
      "global_load_dwordx4 v[152:155], v230, s[62:63] sc1\n\t"
      "s_waitcnt vmcnt(5)\n\t"
      "v_max3_u32 v234, v132, v133, v134\n\t"
      "v_max_u32_e32 v234, v234, v135\n\t"
      "v_cmp_eq_u32_e32 vcc, -1, v234\n\t"
      "s_cbranch_vccnz FQ36L_%=\n\t"
      "FQ36X_%=:\n\t"
      "s_branch FQ36B_%=\n\t"
      "FQ37R_%=:\n\t"
      "s_mov_b32 s51, 0\n\t"
      "FQ37L_%=:\n\t"
      "s_add_u32 s51, s51, 1\n\t"
      "s_cmp_lt_u32 s51, 262144\n\t"
      "s_cbranch_scc0 FQ37X_%=\n\t"
      "s_waitcnt vmcnt(0)\n\t"
      "s_sleep 4\n\t"
      "global_load_dwordx4 v[136:139], v226, s[62:63] sc1\n\t"
      "global_load_dwordx4 v[140:143], v227, s[62:63] sc1\n\t"
      "global_load_dwordx4 v[144:147], v228, s[62:63] sc1\n\t"
      "global_load_dwordx4 v[148:151], v229, s[62:63] sc1\n\t"
      "global_load_dwordx4 v[152:155], v230, s[62:63] sc1\n\t"
      "global_load_dwordx4 v[128:131], v224, s[64:65] sc1\n\t"
      "s_waitcnt vmcnt(5)\n\t"
      "v_max3_u32 v234, v136, v137, v138\n\t"
      "v_max_u32_e32 v234, v234, v139\n\t"
      "v_cmp_eq_u32_e32 vcc, -1, v234\n\t"
      "s_cbranch_vccnz FQ37L_%=\n\t"
      "FQ37X_%=:\n\t"
      "s_branch FQ37B_%=\n\t"
      "FQ38R_%=:\n\t"
      "s_mov_b32 s51, 0\n\t"
      "FQ38L_%=:\n\t"
      "s_add_u32 s51, s51, 1\n\t"
      "s_cmp_lt_u32 s51, 262144\n\t"
      "s_cbranch_scc0 FQ38X_%=\n\t"
      "s_waitcnt vmcnt(0)\n\t"
      "s_sleep 4\n\t"
      "global_load_dwordx4 v[140:143], v227, s[62:63] sc1\n\t"
      "global_load_dwordx4 v[144:147], v228, s[62:63] sc1\n\t"
      "global_load_dwordx4 v[148:151], v229, s[62:63] sc1\n\t"
      "global_load_dwordx4 v[152:155], v230, s[62:63] sc1\n\t"
      "global_load_dwordx4 v[128:131], v224, s[64:65] sc1\n\t"
      "global_load_dwordx4 v[132:135], v225, s[64:65] sc1\n\t"
      "s_waitcnt vmcnt(5)\n\t"
      "v_max3_u32 v234, v140, v141, v142\n\t"
      "v_max_u32_e32 v234, v234, v143\n\t"
      "v_cmp_eq_u32_e32 vcc, -1, v234\n\t"
      "s_cbranch_vccnz FQ38L_%=\n\t"
      "FQ38X_%=:\n\t"
      "s_branch FQ38B_%=\n\t"
      "FQ39R_%=:\n\t"
      "s_mov_b32 s51, 0\n\t"
      "FQ39L_%=:\n\t"
      "s_add_u32 s51, s51, 1\n\t"
      "s_cmp_lt_u32 s51, 262144\n\t"
      "s_cbranch_scc0 FQ39X_%=\n\t"
      "s_waitcnt vmcnt(0)\n\t"
      "s_sleep 4\n\t"
      "global_load_dwordx4 v[144:147], v228, s[62:63] sc1\n\t"
      "global_load_dwordx4 v[148:151], v229, s[62:63] sc1\n\t"
      "global_load_dwordx4 v[152:155], v230, s[62:63] sc1\n\t"
      "global_load_dwordx4 v[128:131], v224, s[64:65] sc1\n\t"
      "global_load_dwordx4 v[132:135], v225, s[64:65] sc1\n\t"
      "global_load_dwordx4 v[136:139], v226, s[64:65] sc1\n\t"
      "s_waitcnt vmcnt(5)\n\t"
      "v_max3_u32 v234, v144, v145, v146\n\t"
      "v_max_u32_e32 v234, v234, v147\n\t"
      "v_cmp_eq_u32_e32 vcc, -1, v234\n\t"
      "s_cbranch_vccnz FQ39L_%=\n\t"
      "FQ39X_%=:\n\t"
      "s_branch FQ39B_%=\n\t"
      "FQ40R_%=:\n\t"
      "s_mov_b32 s51, 0\n\t"
      "FQ40L_%=:\n\t"
      "s_add_u32 s51, s51, 1\n\t"
      "s_cmp_lt_u32 s51, 262144\n\t"
      "s_cbranch_scc0 FQ40X_%=\n\t"
      "s_waitcnt vmcnt(0)\n\t"
      "s_sleep 4\n\t"
      "global_load_dwordx4 v[148:151], v229, s[62:63] sc1\n\t"
      "global_load_dwordx4 v[152:155], v230, s[62:63] sc1\n\t"
      "global_load_dwordx4 v[128:131], v224, s[64:65] sc1\n\t"
      "global_load_dwordx4 v[132:135], v225, s[64:65] sc1\n\t"
      "global_load_dwordx4 v[136:139], v226, s[64:65] sc1\n\t"
      "global_load_dwordx4 v[140:143], v227, s[64:65] sc1\n\t"
      "s_waitcnt vmcnt(5)\n\t"
      "v_max3_u32 v234, v148, v149, v150\n\t"
      "v_max_u32_e32 v234, v234, v151\n\t"
      "v_cmp_eq_u32_e32 vcc, -1, v234\n\t"
      "s_cbranch_vccnz FQ40L_%=\n\t"
      "FQ40X_%=:\n\t"
      "s_branch FQ40B_%=\n\t"
      "FQ41R_%=:\n\t"
      "s_mov_b32 s51, 0\n\t"
      "FQ41L_%=:\n\t"
      "s_add_u32 s51, s51, 1\n\t"
      "s_cmp_lt_u32 s51, 262144\n\t"
      "s_cbranch_scc0 FQ41X_%=\n\t"
      "s_waitcnt vmcnt(0)\n\t"
      "s_sleep 4\n\t"
      "global_load_dwordx4 v[152:155], v230, s[62:63] sc1\n\t"
      "global_load_dwordx4 v[128:131], v224, s[64:65] sc1\n\t"
      "global_load_dwordx4 v[132:135], v225, s[64:65] sc1\n\t"
      "global_load_dwordx4 v[136:139], v226, s[64:65] sc1\n\t"
      "global_load_dwordx4 v[140:143], v227, s[64:65] sc1\n\t"
      "global_load_dwordx4 v[144:147], v228, s[64:65] sc1\n\t"
      "s_waitcnt vmcnt(5)\n\t"
      "v_max3_u32 v234, v152, v153, v154\n\t"
      "v_max_u32_e32 v234, v234, v155\n\t"
      "v_cmp_eq_u32_e32 vcc, -1, v234\n\t"
      "s_cbranch_vccnz FQ41L_%=\n\t"
      "FQ41X_%=:\n\t"
      "s_branch FQ41B_%=\n\t"
      "FQ42R_%=:\n\t"
      "s_mov_b32 s51, 0\n\t"
      "FQ42L_%=:\n\t"
      "s_add_u32 s51, s51, 1\n\t"
      "s_cmp_lt_u32 s51, 262144\n\t"
      "s_cbranch_scc0 FQ42X_%=\n\t"
      "s_sleep 1\n\t"
      "s_mov_b64 exec, 15\n\t"
      "v_mov_b64_e32 v[232:233], 0\n\t"
      "global_atomic_or_x2 v[232:233], v247, v[232:233], s[12:13] sc0 sc1\n\t"
      "s_waitcnt vmcnt(0)\n\t"
      "v_subrev_u32_e32 v232, s14, v232\n\t"
      "v_cmp_ne_u32_e64 s[48:49], s15, v233\n\t"
      "v_subrev_u32_e32 v232, 6, v232\n\t"
      "v_cmp_lt_u32_e32 vcc, 7, v232\n\t"
      "s_mov_b64 exec, -1\n\t"
      "s_or_b64 vcc, vcc, s[48:49]\n\t"
      "s_cbranch_vccnz FQ42L_%=\n\t"
      "FQ42X_%=:\n\t"
      "FQ42DR_%=:\n\t"
      "s_mov_b32 s51, 0\n\t"
      "FQ42DL_%=:\n\t"
      "s_add_u32 s51, s51, 1\n\t"
      "s_cmp_lt_u32 s51, 262144\n\t"
      "s_cbranch_scc0 FQ42DX_%=\n\t"
      "s_waitcnt vmcnt(0)\n\t"
      "s_sleep 1\n\t"
      "global_load_dwordx4 v[128:131], v224, s[64:65] sc1\n\t"
      "global_load_dwordx4 v[132:135], v225, s[64:65] sc1\n\t"
      "global_load_dwordx4 v[136:139], v226, s[64:65] sc1\n\t"
      "global_load_dwordx4 v[140:143], v227, s[64:65] sc1\n\t"
      "global_load_dwordx4 v[144:147], v228, s[64:65] sc1\n\t"
      "global_load_dwordx4 v[148:151], v229, s[64:65] sc1\n\t"
      "s_waitcnt vmcnt(5)\n\t"
      "v_max3_u32 v234, v128, v129, v130\n\t"
      "v_max_u32_e32 v234, v234, v131\n\t"
      "v_cmp_eq_u32_e32 vcc, -1, v234\n\t"
      "s_cbranch_vccnz FQ42DL_%=\n\t"
      "FQ42DX_%=:\n\t"
      "s_branch FQ42B_%=\n\t"
      "NQ5R_%=:\n\t"
      "s_mov_b32 s51, 0\n\t"
      "NQ5L_%=:\n\t"
      "s_add_u32 s51, s51, 1\n\t"
      "s_cmp_lt_u32 s51, 262144\n\t"
      "s_cbranch_scc0 NQ5X_%=\n\t"
      "s_waitcnt vmcnt(0)\n\t"
      "s_sleep 1\n\t"
      "global_load_dwordx4 v[128:131], v224, s[62:63] sc1\n\t"
      "global_load_dwordx4 v[132:135], v225, s[62:63] sc1\n\t"
      "global_load_dwordx4 v[136:139], v226, s[62:63] sc1\n\t"
      "global_load_dwordx4 v[140:143], v227, s[62:63] sc1\n\t"
      "global_load_dwordx4 v[144:147], v228, s[62:63] sc1\n\t"
      "global_load_dwordx4 v[148:151], v229, s[62:63] sc1\n\t"
      "global_load_dwordx4 v[152:155], v230, s[62:63] sc1\n\t"
      "s_waitcnt vmcnt(0)\n\t"
      "v_max3_u32 v234, v128, v129, v130\n\t"
      "v_max3_u32 v234, v234, v131, v132\n\t"
      "v_max3_u32 v234, v234, v133, v134\n\t"
      "v_max3_u32 v234, v234, v135, v136\n\t"
      "v_max3_u32 v234, v234, v137, v138\n\t"
      "v_max3_u32 v234, v234, v139, v140\n\t"
      "v_max3_u32 v234, v234, v141, v142\n\t"
      "v_max3_u32 v234, v234, v143, v144\n\t"
      "v_max3_u32 v234, v234, v145, v146\n\t"
      "v_max3_u32 v234, v234, v147, v148\n\t"
      "v_max3_u32 v234, v234, v149, v150\n\t"
      "v_max3_u32 v234, v234, v151, v152\n\t"
      "v_max3_u32 v234, v234, v153, v154\n\t"
      "v_max_u32_e32 v234, v234, v155\n\t"
      "v_cmp_eq_u32_e32 vcc, -1, v234\n\t"
      "s_cbranch_vccnz NQ5L_%=\n\t"
      "NQ5X_%=:\n\t"
      "v_mfma_f32_16x16x32_bf16 v[200:203], v[128:131], v[0:3], 0\n\t"
      "v_mfma_f32_16x16x32_bf16 v[204:207], v[128:131], v[4:7], 0\n\t"
      "v_mfma_f32_16x16x32_bf16 v[208:211], v[128:131], v[8:11], 0\n\t"
      "v_mfma_f32_16x16x32_bf16 v[212:215], v[128:131], v[12:15], 0\n\t"
      "v_mfma_f32_16x16x32_bf16 v[200:203], v[132:135], v[16:19], v[200:203]\n\t"
      "v_mfma_f32_16x16x32_bf16 v[204:207], v[132:135], v[20:23], v[204:207]\n\t"
      "v_mfma_f32_16x16x32_bf16 v[208:211], v[132:135], v[24:27], v[208:211]\n\t"
      "v_mfma_f32_16x16x32_bf16 v[212:215], v[132:135], v[28:31], v[212:215]\n\t"
      "v_mfma_f32_16x16x32_bf16 v[200:203], v[136:139], v[32:35], v[200:203]\n\t"
      "v_mfma_f32_16x16x32_bf16 v[204:207], v[136:139], v[36:39], v[204:207]\n\t"
      "v_mfma_f32_16x16x32_bf16 v[208:211], v[136:139], v[40:43], v[208:211]\n\t"
      "v_mfma_f32_16x16x32_bf16 v[212:215], v[136:139], v[44:47], v[212:215]\n\t"
      "v_mfma_f32_16x16x32_bf16 v[200:203], v[140:143], v[48:51], v[200:203]\n\t"
      "v_mfma_f32_16x16x32_bf16 v[204:207], v[140:143], v[52:55], v[204:207]\n\t"
      "v_mfma_f32_16x16x32_bf16 v[208:211], v[140:143], v[56:59], v[208:211]\n\t"
      "v_mfma_f32_16x16x32_bf16 v[212:215], v[140:143], v[60:63], v[212:215]\n\t"
      "v_mfma_f32_16x16x32_bf16 v[200:203], v[144:147], v[64:67], v[200:203]\n\t"
      "v_mfma_f32_16x16x32_bf16 v[204:207], v[144:147], v[68:71], v[204:207]\n\t"
      "v_mfma_f32_16x16x32_bf16 v[208:211], v[144:147], v[72:75], v[208:211]\n\t"
      "v_mfma_f32_16x16x32_bf16 v[212:215], v[144:147], v[76:79], v[212:215]\n\t"
      "v_mfma_f32_16x16x32_bf16 v[200:203], v[148:151], v[80:83], v[200:203]\n\t"
      "v_mfma_f32_16x16x32_bf16 v[204:207], v[148:151], v[84:87], v[204:207]\n\t"
      "v_mfma_f32_16x16x32_bf16 v[208:211], v[148:151], v[88:91], v[208:211]\n\t"
      "v_mfma_f32_16x16x32_bf16 v[212:215], v[148:151], v[92:95], v[212:215]\n\t"
      "v_mfma_f32_16x16x32_bf16 v[200:203], v[152:155], v[96:99], v[200:203]\n\t"
      "v_mfma_f32_16x16x32_bf16 v[204:207], v[152:155], v[100:103], v[204:207]\n\t"
      "v_mfma_f32_16x16x32_bf16 v[208:211], v[152:155], v[104:107], v[208:211]\n\t"
      "v_mfma_f32_16x16x32_bf16 v[212:215], v[152:155], v[108:111], v[212:215]\n\t"
      "global_load_dwordx4 v[128:131], v224, s[64:65] sc1\n\t"
      "global_load_dwordx4 v[132:135], v225, s[64:65] sc1\n\t"
      "global_load_dwordx4 v[136:139], v226, s[64:65] sc1\n\t"
      "global_load_dwordx4 v[140:143], v227, s[64:65] sc1\n\t"
      "global_load_dwordx4 v[144:147], v228, s[64:65] sc1\n\t"
      "global_load_dwordx4 v[148:151], v229, s[64:65] sc1\n\t"
      "global_load_dwordx4 v[152:155], v230, s[64:65] sc1\n\t"
      "s_waitcnt vmcnt(0)\n\t"
      "s_nop 7\n\t"
      "s_branch NQ5B_%=\n\t"
      "FQ43R_%=:\n\t"
      "s_mov_b32 s51, 0\n\t"
      "FQ43L_%=:\n\t"
      "s_add_u32 s51, s51, 1\n\t"
      "s_cmp_lt_u32 s51, 262144\n\t"
      "s_cbranch_scc0 FQ43X_%=\n\t"
      "s_sleep 1\n\t"
      "s_mov_b64 exec, 15\n\t"
      "v_mov_b64_e32 v[232:233], 0\n\t"
      "global_atomic_or_x2 v[232:233], v247, v[232:233], s[12:13] sc0 sc1\n\t"
      "s_waitcnt vmcnt(0)\n\t"
      "v_subrev_u32_e32 v232, s14, v232\n\t"
      "v_cmp_ne_u32_e64 s[48:49], s15, v233\n\t"
      "v_subrev_u32_e32 v232, 6, v232\n\t"
      "v_cmp_lt_u32_e32 vcc, 7, v232\n\t"
      "s_mov_b64 exec, -1\n\t"
      "s_or_b64 vcc, vcc, s[48:49]\n\t"
      "s_cbranch_vccnz FQ43L_%=\n\t"
      "FQ43X_%=:\n\t"
      "FQ43DR_%=:\n\t"
      "s_mov_b32 s51, 0\n\t"
      "FQ43DL_%=:\n\t"
      "s_add_u32 s51, s51, 1\n\t"
      "s_cmp_lt_u32 s51, 262144\n\t"
      "s_cbranch_scc0 FQ43DX_%=\n\t"
      "s_waitcnt vmcnt(0)\n\t"
      "s_sleep 1\n\t"
      "global_load_dwordx4 v[132:135], v225, s[64:65] sc1\n\t"
      "global_load_dwordx4 v[136:139], v226, s[64:65] sc1\n\t"
      "global_load_dwordx4 v[140:143], v227, s[64:65] sc1\n\t"
      "global_load_dwordx4 v[144:147], v228, s[64:65] sc1\n\t"
      "global_load_dwordx4 v[148:151], v229, s[64:65] sc1\n\t"
      "global_load_dwordx4 v[152:155], v230, s[64:65] sc1\n\t"
      "s_waitcnt vmcnt(5)\n\t"
      "v_max3_u32 v234, v132, v133, v134\n\t"
      "v_max_u32_e32 v234, v234, v135\n\t"
      "v_cmp_eq_u32_e32 vcc, -1, v234\n\t"
      "s_cbranch_vccnz FQ43DL_%=\n\t"
      "FQ43DX_%=:\n\t"
      "s_branch FQ43B_%=\n\t"
      "FQ44R_%=:\n\t"
      "s_mov_b32 s51, 0\n\t"
      "FQ44L_%=:\n\t"
      "s_add_u32 s51, s51, 1\n\t"
      "s_cmp_lt_u32 s51, 262144\n\t"
      "s_cbranch_scc0 FQ44X_%=\n\t"
      "s_sleep 1\n\t"
      "s_mov_b64 exec, 15\n\t"
      "v_mov_b64_e32 v[232:233], 0\n\t"
      "global_atomic_or_x2 v[232:233], v247, v[232:233], s[12:13] sc0 sc1\n\t"
      "s_waitcnt vmcnt(0)\n\t"
      "v_subrev_u32_e32 v232, s14, v232\n\t"
      "v_cmp_ne_u32_e64 s[48:49], s15, v233\n\t"
      "v_subrev_u32_e32 v232, 6, v232\n\t"
      "v_cmp_lt_u32_e32 vcc, 7, v232\n\t"
      "s_mov_b64 exec, -1\n\t"
      "s_or_b64 vcc, vcc, s[48:49]\n\t"
      "s_cbranch_vccnz FQ44L_%=\n\t"
      "FQ44X_%=:\n\t"
      "FQ44DR_%=:\n\t"
      "s_mov_b32 s51, 0\n\t"
      "FQ44DL_%=:\n\t"
      "s_add_u32 s51, s51, 1\n\t"
      "s_cmp_lt_u32 s51, 262144\n\t"
      "s_cbranch_scc0 FQ44DX_%=\n\t"
      "s_waitcnt vmcnt(0)\n\t"
      "s_sleep 1\n\t"
      "global_load_dwordx4 v[136:139], v226, s[64:65] sc1\n\t"
      "global_load_dwordx4 v[140:143], v227, s[64:65] sc1\n\t"
      "global_load_dwordx4 v[144:147], v228, s[64:65] sc1\n\t"
      "global_load_dwordx4 v[148:151], v229, s[64:65] sc1\n\t"
      "global_load_dwordx4 v[152:155], v230, s[64:65] sc1\n\t"
      "s_waitcnt vmcnt(4)\n\t"
      "v_max3_u32 v234, v136, v137, v138\n\t"
      "v_max_u32_e32 v234, v234, v139\n\t"
      "v_cmp_eq_u32_e32 vcc, -1, v234\n\t"
      "s_cbranch_vccnz FQ44DL_%=\n\t"
      "FQ44DX_%=:\n\t"
      "s_branch FQ44B_%=\n\t"
      "FQ45R_%=:\n\t"
      "s_mov_b32 s51, 0\n\t"
      "FQ45L_%=:\n\t"
      "s_add_u32 s51, s51, 1\n\t"
      "s_cmp_lt_u32 s51, 262144\n\t"
      "s_cbranch_scc0 FQ45X_%=\n\t"
      "s_sleep 1\n\t"
      "s_mov_b64 exec, 15\n\t"
      "v_mov_b64_e32 v[232:233], 0\n\t"
      "global_atomic_or_x2 v[232:233], v247, v[232:233], s[12:13] sc0 sc1\n\t"
      "s_waitcnt vmcnt(0)\n\t"
      "v_subrev_u32_e32 v232, s14, v232\n\t"
      "v_cmp_ne_u32_e64 s[48:49], s15, v233\n\t"
      "v_subrev_u32_e32 v232, 6, v232\n\t"
      "v_cmp_lt_u32_e32 vcc, 7, v232\n\t"
      "s_mov_b64 exec, -1\n\t"
      "s_or_b64 vcc, vcc, s[48:49]\n\t"
      "s_cbranch_vccnz FQ45L_%=\n\t"
      "FQ45X_%=:\n\t"
      "FQ45DR_%=:\n\t"
      "s_mov_b32 s51, 0\n\t"
      "FQ45DL_%=:\n\t"
      "s_add_u32 s51, s51, 1\n\t"
      "s_cmp_lt_u32 s51, 262144\n\t"
      "s_cbranch_scc0 FQ45DX_%=\n\t"
      "s_waitcnt vmcnt(0)\n\t"
      "s_sleep 1\n\t"
      "global_load_dwordx4 v[140:143], v227, s[64:65] sc1\n\t"
      "global_load_dwordx4 v[144:147], v228, s[64:65] sc1\n\t"
      "global_load_dwordx4 v[148:151], v229, s[64:65] sc1\n\t"
      "global_load_dwordx4 v[152:155], v230, s[64:65] sc1\n\t"
      "s_waitcnt vmcnt(3)\n\t"
      "v_max3_u32 v234, v140, v141, v142\n\t"
      "v_max_u32_e32 v234, v234, v143\n\t"
      "v_cmp_eq_u32_e32 vcc, -1, v234\n\t"
      "s_cbranch_vccnz FQ45DL_%=\n\t"
      "FQ45DX_%=:\n\t"
      "s_branch FQ45B_%=\n\t"
      "FQ46R_%=:\n\t"
      "s_mov_b32 s51, 0\n\t"
      "FQ46L_%=:\n\t"
      "s_add_u32 s51, s51, 1\n\t"
      "s_cmp_lt_u32 s51, 262144\n\t"
      "s_cbranch_scc0 FQ46X_%=\n\t"
      "s_sleep 1\n\t"
      "s_mov_b64 exec, 15\n\t"
      "v_mov_b64_e32 v[232:233], 0\n\t"
      "global_atomic_or_x2 v[232:233], v247, v[232:233], s[12:13] sc0 sc1\n\t"
      "s_waitcnt vmcnt(0)\n\t"
      "v_subrev_u32_e32 v232, s14, v232\n\t"
      "v_cmp_ne_u32_e64 s[48:49], s15, v233\n\t"
      "v_subrev_u32_e32 v232, 6, v232\n\t"
      "v_cmp_lt_u32_e32 vcc, 7, v232\n\t"
      "s_mov_b64 exec, -1\n\t"
      "s_or_b64 vcc, vcc, s[48:49]\n\t"
      "s_cbranch_vccnz FQ46L_%=\n\t"
      "FQ46X_%=:\n\t"
      "FQ46DR_%=:\n\t"
      "s_mov_b32 s51, 0\n\t"
      "FQ46DL_%=:\n\t"
      "s_add_u32 s51, s51, 1\n\t"
      "s_cmp_lt_u32 s51, 262144\n\t"
      "s_cbranch_scc0 FQ46DX_%=\n\t"
      "s_waitcnt vmcnt(0)\n\t"
      "s_sleep 1\n\t"
      "global_load_dwordx4 v[144:147], v228, s[64:65] sc1\n\t"
      "global_load_dwordx4 v[148:151], v229, s[64:65] sc1\n\t"
      "global_load_dwordx4 v[152:155], v230, s[64:65] sc1\n\t"
      "s_waitcnt vmcnt(2)\n\t"
      "v_max3_u32 v234, v144, v145, v146\n\t"
      "v_max_u32_e32 v234, v234, v147\n\t"
      "v_cmp_eq_u32_e32 vcc, -1, v234\n\t"
      "s_cbranch_vccnz FQ46DL_%=\n\t"
      "FQ46DX_%=:\n\t"
      "s_branch FQ46B_%=\n\t"
      "FQ47R_%=:\n\t"
      "s_mov_b32 s51, 0\n\t"
      "FQ47L_%=:\n\t"
      "s_add_u32 s51, s51, 1\n\t"
      "s_cmp_lt_u32 s51, 262144\n\t"
      "s_cbranch_scc0 FQ47X_%=\n\t"
      "s_sleep 1\n\t"
      "s_mov_b64 exec, 15\n\t"
      "v_mov_b64_e32 v[232:233], 0\n\t"
      "global_atomic_or_x2 v[232:233], v247, v[232:233], s[12:13] sc0 sc1\n\t"
      "s_waitcnt vmcnt(0)\n\t"
      "v_subrev_u32_e32 v232, s14, v232\n\t"
      "v_cmp_ne_u32_e64 s[48:49], s15, v233\n\t"
      "v_subrev_u32_e32 v232, 6, v232\n\t"
      "v_cmp_lt_u32_e32 vcc, 7, v232\n\t"
      "s_mov_b64 exec, -1\n\t"
      "s_or_b64 vcc, vcc, s[48:49]\n\t"
      "s_cbranch_vccnz FQ47L_%=\n\t"
      "FQ47X_%=:\n\t"
      "FQ47DR_%=:\n\t"
      "s_mov_b32 s51, 0\n\t"
      "FQ47DL_%=:\n\t"
      "s_add_u32 s51, s51, 1\n\t"
      "s_cmp_lt_u32 s51, 262144\n\t"
      "s_cbranch_scc0 FQ47DX_%=\n\t"
      "s_waitcnt vmcnt(0)\n\t"
      "s_sleep 1\n\t"
      "global_load_dwordx4 v[148:151], v229, s[64:65] sc1\n\t"
      "global_load_dwordx4 v[152:155], v230, s[64:65] sc1\n\t"
      "s_waitcnt vmcnt(1)\n\t"
      "v_max3_u32 v234, v148, v149, v150\n\t"
      "v_max_u32_e32 v234, v234, v151\n\t"
      "v_cmp_eq_u32_e32 vcc, -1, v234\n\t"
      "s_cbranch_vccnz FQ47DL_%=\n\t"
      "FQ47DX_%=:\n\t"
      "s_branch FQ47B_%=\n\t"
      "FQ48R_%=:\n\t"
      "s_mov_b32 s51, 0\n\t"
      "FQ48L_%=:\n\t"
      "s_add_u32 s51, s51, 1\n\t"
      "s_cmp_lt_u32 s51, 262144\n\t"
      "s_cbranch_scc0 FQ48X_%=\n\t"
      "s_sleep 1\n\t"
      "s_mov_b64 exec, 15\n\t"
      "v_mov_b64_e32 v[232:233], 0\n\t"
      "global_atomic_or_x2 v[232:233], v247, v[232:233], s[12:13] sc0 sc1\n\t"
      "s_waitcnt vmcnt(0)\n\t"
      "v_subrev_u32_e32 v232, s14, v232\n\t"
      "v_cmp_ne_u32_e64 s[48:49], s15, v233\n\t"
      "v_subrev_u32_e32 v232, 6, v232\n\t"
      "v_cmp_lt_u32_e32 vcc, 7, v232\n\t"
      "s_mov_b64 exec, -1\n\t"
      "s_or_b64 vcc, vcc, s[48:49]\n\t"
      "s_cbranch_vccnz FQ48L_%=\n\t"
      "FQ48X_%=:\n\t"
      "FQ48DR_%=:\n\t"
      "s_mov_b32 s51, 0\n\t"
      "FQ48DL_%=:\n\t"
      "s_add_u32 s51, s51, 1\n\t"
      "s_cmp_lt_u32 s51, 262144\n\t"
      "s_cbranch_scc0 FQ48DX_%=\n\t"
      "s_waitcnt vmcnt(0)\n\t"
      "s_sleep 1\n\t"
      "global_load_dwordx4 v[152:155], v230, s[64:65] sc1\n\t"
      "s_waitcnt vmcnt(0)\n\t"
      "v_max3_u32 v234, v152, v153, v154\n\t"
      "v_max_u32_e32 v234, v234, v155\n\t"
      "v_cmp_eq_u32_e32 vcc, -1, v234\n\t"
      "s_cbranch_vccnz FQ48DL_%=\n\t"
      "FQ48DX_%=:\n\t"
      "s_branch FQ48B_%=\n\t"
      "NQ6R_%=:\n\t"
      "s_mov_b32 s51, 0\n\t"
      "NQ6L_%=:\n\t"
      "s_add_u32 s51, s51, 1\n\t"
      "s_cmp_lt_u32 s51, 262144\n\t"
      "s_cbranch_scc0 NQ6X_%=\n\t"
      "s_waitcnt vmcnt(0)\n\t"
      "s_sleep 1\n\t"
      "global_load_dwordx4 v[128:131], v224, s[64:65] sc1\n\t"
      "global_load_dwordx4 v[132:135], v225, s[64:65] sc1\n\t"
      "global_load_dwordx4 v[136:139], v226, s[64:65] sc1\n\t"
      "global_load_dwordx4 v[140:143], v227, s[64:65] sc1\n\t"
      "global_load_dwordx4 v[144:147], v228, s[64:65] sc1\n\t"
      "global_load_dwordx4 v[148:151], v229, s[64:65] sc1\n\t"
      "global_load_dwordx4 v[152:155], v230, s[64:65] sc1\n\t"
      "s_waitcnt vmcnt(0)\n\t"
      "v_max3_u32 v234, v128, v129, v130\n\t"
      "v_max3_u32 v234, v234, v131, v132\n\t"
      "v_max3_u32 v234, v234, v133, v134\n\t"
      "v_max3_u32 v234, v234, v135, v136\n\t"
      "v_max3_u32 v234, v234, v137, v138\n\t"
      "v_max3_u32 v234, v234, v139, v140\n\t"
      "v_max3_u32 v234, v234, v141, v142\n\t"
      "v_max3_u32 v234, v234, v143, v144\n\t"
      "v_max3_u32 v234, v234, v145, v146\n\t"
      "v_max3_u32 v234, v234, v147, v148\n\t"
      "v_max3_u32 v234, v234, v149, v150\n\t"
      "v_max3_u32 v234, v234, v151, v152\n\t"
      "v_max3_u32 v234, v234, v153, v154\n\t"
      "v_max_u32_e32 v234, v234, v155\n\t"
      "v_cmp_eq_u32_e32 vcc, -1, v234\n\t"
      "s_cbranch_vccnz NQ6L_%=\n\t"
      "NQ6X_%=:\n\t"
      "v_mfma_f32_16x16x32_bf16 v[184:187], v[128:131], v[0:3], 0\n\t"
      "v_mfma_f32_16x16x32_bf16 v[188:191], v[128:131], v[4:7], 0\n\t"
      "v_mfma_f32_16x16x32_bf16 v[192:195], v[128:131], v[8:11], 0\n\t"
      "v_mfma_f32_16x16x32_bf16 v[196:199], v[128:131], v[12:15], 0\n\t"
      "v_mfma_f32_16x16x32_bf16 v[184:187], v[132:135], v[16:19], v[184:187]\n\t"
      "v_mfma_f32_16x16x32_bf16 v[188:191], v[132:135], v[20:23], v[188:191]\n\t"
      "v_mfma_f32_16x16x32_bf16 v[192:195], v[132:135], v[24:27], v[192:195]\n\t"
      "v_mfma_f32_16x16x32_bf16 v[196:199], v[132:135], v[28:31], v[196:199]\n\t"
      "v_mfma_f32_16x16x32_bf16 v[184:187], v[136:139], v[32:35], v[184:187]\n\t"
      "v_mfma_f32_16x16x32_bf16 v[188:191], v[136:139], v[36:39], v[188:191]\n\t"
      "v_mfma_f32_16x16x32_bf16 v[192:195], v[136:139], v[40:43], v[192:195]\n\t"
      "v_mfma_f32_16x16x32_bf16 v[196:199], v[136:139], v[44:47], v[196:199]\n\t"
      "v_mfma_f32_16x16x32_bf16 v[184:187], v[140:143], v[48:51], v[184:187]\n\t"
      "v_mfma_f32_16x16x32_bf16 v[188:191], v[140:143], v[52:55], v[188:191]\n\t"
      "v_mfma_f32_16x16x32_bf16 v[192:195], v[140:143], v[56:59], v[192:195]\n\t"
      "v_mfma_f32_16x16x32_bf16 v[196:199], v[140:143], v[60:63], v[196:199]\n\t"
      "v_mfma_f32_16x16x32_bf16 v[184:187], v[144:147], v[64:67], v[184:187]\n\t"
      "v_mfma_f32_16x16x32_bf16 v[188:191], v[144:147], v[68:71], v[188:191]\n\t"
      "v_mfma_f32_16x16x32_bf16 v[192:195], v[144:147], v[72:75], v[192:195]\n\t"
      "v_mfma_f32_16x16x32_bf16 v[196:199], v[144:147], v[76:79], v[196:199]\n\t"
      "v_mfma_f32_16x16x32_bf16 v[184:187], v[148:151], v[80:83], v[184:187]\n\t"
      "v_mfma_f32_16x16x32_bf16 v[188:191], v[148:151], v[84:87], v[188:191]\n\t"
      "v_mfma_f32_16x16x32_bf16 v[192:195], v[148:151], v[88:91], v[192:195]\n\t"
      "v_mfma_f32_16x16x32_bf16 v[196:199], v[148:151], v[92:95], v[196:199]\n\t"
      "v_mfma_f32_16x16x32_bf16 v[184:187], v[152:155], v[96:99], v[184:187]\n\t"
      "v_mfma_f32_16x16x32_bf16 v[188:191], v[152:155], v[100:103], v[188:191]\n\t"
      "v_mfma_f32_16x16x32_bf16 v[192:195], v[152:155], v[104:107], v[192:195]\n\t"
      "v_mfma_f32_16x16x32_bf16 v[196:199], v[152:155], v[108:111], v[196:199]\n\t"
      "s_waitcnt vmcnt(0)\n\t"
      "s_nop 7\n\t"
      "s_branch NQ6B_%=\n\t"
      "FP0R_%=:\n\t"
      "s_mov_b32 s51, 0\n\t"
      "FP0L_%=:\n\t"
      "s_add_u32 s51, s51, 1\n\t"
      "s_cmp_lt_u32 s51, 262144\n\t"
      "s_cbranch_scc0 FP0X_%=\n\t"
      "s_waitcnt vmcnt(0)\n\t"
      "s_sleep 1\n\t"
      "global_load_dwordx4 v[128:131], v224, s[52:53] sc1\n\t"
      "global_load_dwordx4 v[132:135], v225, s[52:53] sc1\n\t"
      "global_load_dwordx4 v[136:139], v226, s[52:53] sc1\n\t"
      "global_load_dwordx4 v[140:143], v227, s[52:53] sc1\n\t"
      "global_load_dwordx4 v[144:147], v228, s[52:53] sc1\n\t"
      "global_load_dwordx4 v[148:151], v229, s[52:53] sc1\n\t"
      "global_load_dwordx4 v[152:155], v230, s[52:53] sc1\n\t"
      "s_waitcnt vmcnt(6)\n\t"
      "v_max3_u32 v234, v128, v129, v130\n\t"
      "v_max_u32_e32 v234, v234, v131\n\t"
      "v_cmp_eq_u32_e32 vcc, -1, v234\n\t"
      "s_cbranch_vccnz FP0L_%=\n\t"
      "FP0X_%=:\n\t"
      "s_branch FP0B_%=\n\t"
      "FP1R_%=:\n\t"
      "s_mov_b32 s51, 0\n\t"
      "FP1L_%=:\n\t"
      "s_add_u32 s51, s51, 1\n\t"
      "s_cmp_lt_u32 s51, 262144\n\t"
      "s_cbranch_scc0 FP1X_%=\n\t"
      "s_waitcnt vmcnt(0)\n\t"
      "s_sleep 1\n\t"
      "global_load_dwordx4 v[132:135], v225, s[52:53] sc1\n\t"
      "global_load_dwordx4 v[136:139], v226, s[52:53] sc1\n\t"
      "global_load_dwordx4 v[140:143], v227, s[52:53] sc1\n\t"
      "global_load_dwordx4 v[144:147], v228, s[52:53] sc1\n\t"
      "global_load_dwordx4 v[148:151], v229, s[52:53] sc1\n\t"
      "global_load_dwordx4 v[152:155], v230, s[52:53] sc1\n\t"
      "global_load_dwordx4 v[128:131], v224, s[54:55] sc1\n\t"
      "s_waitcnt vmcnt(6)\n\t"
      "v_max3_u32 v234, v132, v133, v134\n\t"
      "v_max_u32_e32 v234, v234, v135\n\t"
      "v_cmp_eq_u32_e32 vcc, -1, v234\n\t"
      "s_cbranch_vccnz FP1L_%=\n\t"
      "FP1X_%=:\n\t"
      "s_branch FP1B_%=\n\t"
      "FP2R_%=:\n\t"
      "s_mov_b32 s51, 0\n\t"
      "FP2L_%=:\n\t"
      "s_add_u32 s51, s51, 1\n\t"
      "s_cmp_lt_u32 s51, 262144\n\t"
      "s_cbranch_scc0 FP2X_%=\n\t"
      "s_waitcnt vmcnt(0)\n\t"
      "s_sleep 1\n\t"
      "global_load_dwordx4 v[136:139], v226, s[52:53] sc1\n\t"
      "global_load_dwordx4 v[140:143], v227, s[52:53] sc1\n\t"
      "global_load_dwordx4 v[144:147], v228, s[52:53] sc1\n\t"
      "global_load_dwordx4 v[148:151], v229, s[52:53] sc1\n\t"
      "global_load_dwordx4 v[152:155], v230, s[52:53] sc1\n\t"
      "global_load_dwordx4 v[128:131], v224, s[54:55] sc1\n\t"
      "global_load_dwordx4 v[132:135], v225, s[54:55] sc1\n\t"
      "s_waitcnt vmcnt(6)\n\t"
      "v_max3_u32 v234, v136, v137, v138\n\t"
      "v_max_u32_e32 v234, v234, v139\n\t"
      "v_cmp_eq_u32_e32 vcc, -1, v234\n\t"
      "s_cbranch_vccnz FP2L_%=\n\t"
      "FP2X_%=:\n\t"
      "s_branch FP2B_%=\n\t"
      "FP3R_%=:\n\t"
      "s_mov_b32 s51, 0\n\t"
      "FP3L_%=:\n\t"
      "s_add_u32 s51, s51, 1\n\t"
      "s_cmp_lt_u32 s51, 262144\n\t"
      "s_cbranch_scc0 FP3X_%=\n\t"
      "s_waitcnt vmcnt(0)\n\t"
      "s_sleep 1\n\t"
      "global_load_dwordx4 v[140:143], v227, s[52:53] sc1\n\t"
      "global_load_dwordx4 v[144:147], v228, s[52:53] sc1\n\t"
      "global_load_dwordx4 v[148:151], v229, s[52:53] sc1\n\t"
      "global_load_dwordx4 v[152:155], v230, s[52:53] sc1\n\t"
      "global_load_dwordx4 v[128:131], v224, s[54:55] sc1\n\t"
      "global_load_dwordx4 v[132:135], v225, s[54:55] sc1\n\t"
      "global_load_dwordx4 v[136:139], v226, s[54:55] sc1\n\t"
      "s_waitcnt vmcnt(6)\n\t"
      "v_max3_u32 v234, v140, v141, v142\n\t"
      "v_max_u32_e32 v234, v234, v143\n\t"
      "v_cmp_eq_u32_e32 vcc, -1, v234\n\t"
      "s_cbranch_vccnz FP3L_%=\n\t"
      "FP3X_%=:\n\t"
      "s_branch FP3B_%=\n\t"
      "FP4R_%=:\n\t"
      "s_mov_b32 s51, 0\n\t"
      "FP4L_%=:\n\t"
      "s_add_u32 s51, s51, 1\n\t"
      "s_cmp_lt_u32 s51, 262144\n\t"
      "s_cbranch_scc0 FP4X_%=\n\t"
      "s_waitcnt vmcnt(0)\n\t"
      "s_sleep 1\n\t"
      "global_load_dwordx4 v[144:147], v228, s[52:53] sc1\n\t"
      "global_load_dwordx4 v[148:151], v229, s[52:53] sc1\n\t"
      "global_load_dwordx4 v[152:155], v230, s[52:53] sc1\n\t"
      "global_load_dwordx4 v[128:131], v224, s[54:55] sc1\n\t"
      "global_load_dwordx4 v[132:135], v225, s[54:55] sc1\n\t"
      "global_load_dwordx4 v[136:139], v226, s[54:55] sc1\n\t"
      "global_load_dwordx4 v[140:143], v227, s[54:55] sc1\n\t"
      "s_waitcnt vmcnt(6)\n\t"
      "v_max3_u32 v234, v144, v145, v146\n\t"
      "v_max_u32_e32 v234, v234, v147\n\t"
      "v_cmp_eq_u32_e32 vcc, -1, v234\n\t"
      "s_cbranch_vccnz FP4L_%=\n\t"
      "FP4X_%=:\n\t"
      "s_branch FP4B_%=\n\t"
      "FP5R_%=:\n\t"
      "s_mov_b32 s51, 0\n\t"
      "FP5L_%=:\n\t"
      "s_add_u32 s51, s51, 1\n\t"
      "s_cmp_lt_u32 s51, 262144\n\t"
      "s_cbranch_scc0 FP5X_%=\n\t"
      "s_waitcnt vmcnt(0)\n\t"
      "s_sleep 1\n\t"
      "global_load_dwordx4 v[148:151], v229, s[52:53] sc1\n\t"
      "global_load_dwordx4 v[152:155], v230, s[52:53] sc1\n\t"
      "global_load_dwordx4 v[128:131], v224, s[54:55] sc1\n\t"
      "global_load_dwordx4 v[132:135], v225, s[54:55] sc1\n\t"
      "global_load_dwordx4 v[136:139], v226, s[54:55] sc1\n\t"
      "global_load_dwordx4 v[140:143], v227, s[54:55] sc1\n\t"
      "global_load_dwordx4 v[144:147], v228, s[54:55] sc1\n\t"
      "s_waitcnt vmcnt(6)\n\t"
      "v_max3_u32 v234, v148, v149, v150\n\t"
      "v_max_u32_e32 v234, v234, v151\n\t"
      "v_cmp_eq_u32_e32 vcc, -1, v234\n\t"
      "s_cbranch_vccnz FP5L_%=\n\t"
      "FP5X_%=:\n\t"
      "s_branch FP5B_%=\n\t"
      "FP6R_%=:\n\t"
      "s_mov_b32 s51, 0\n\t"
      "FP6L_%=:\n\t"
      "s_add_u32 s51, s51, 1\n\t"
      "s_cmp_lt_u32 s51, 262144\n\t"
      "s_cbranch_scc0 FP6X_%=\n\t"
      "s_waitcnt vmcnt(0)\n\t"
      "s_sleep 1\n\t"
      "global_load_dwordx4 v[152:155], v230, s[52:53] sc1\n\t"
      "global_load_dwordx4 v[128:131], v224, s[54:55] sc1\n\t"
      "global_load_dwordx4 v[132:135], v225, s[54:55] sc1\n\t"
      "global_load_dwordx4 v[136:139], v226, s[54:55] sc1\n\t"
      "global_load_dwordx4 v[140:143], v227, s[54:55] sc1\n\t"
      "global_load_dwordx4 v[144:147], v228, s[54:55] sc1\n\t"
      "global_load_dwordx4 v[148:151], v229, s[54:55] sc1\n\t"
      "s_waitcnt vmcnt(6)\n\t"
      "v_max3_u32 v234, v152, v153, v154\n\t"
      "v_max_u32_e32 v234, v234, v155\n\t"
      "v_cmp_eq_u32_e32 vcc, -1, v234\n\t"
      "s_cbranch_vccnz FP6L_%=\n\t"
      "FP6X_%=:\n\t"
      "s_branch FP6B_%=\n\t"
      "FP7R_%=:\n\t"
      "s_mov_b32 s51, 0\n\t"
      "FP7L_%=:\n\t"
      "s_add_u32 s51, s51, 1\n\t"
      "s_cmp_lt_u32 s51, 262144\n\t"
      "s_cbranch_scc0 FP7X_%=\n\t"
      "s_waitcnt vmcnt(0)\n\t"
      "s_sleep 4\n\t"
      "global_load_dwordx4 v[128:131], v224, s[54:55] sc1\n\t"
      "global_load_dwordx4 v[132:135], v225, s[54:55] sc1\n\t"
      "global_load_dwordx4 v[136:139], v226, s[54:55] sc1\n\t"
      "global_load_dwordx4 v[140:143], v227, s[54:55] sc1\n\t"
      "global_load_dwordx4 v[144:147], v228, s[54:55] sc1\n\t"
      "global_load_dwordx4 v[148:151], v229, s[54:55] sc1\n\t"
      "s_waitcnt vmcnt(5)\n\t"
      "v_max3_u32 v234, v128, v129, v130\n\t"
      "v_max_u32_e32 v234, v234, v131\n\t"
      "v_cmp_eq_u32_e32 vcc, -1, v234\n\t"
      "s_cbranch_vccnz FP7L_%=\n\t"
      "FP7X_%=:\n\t"
      "s_branch FP7B_%=\n\t"
      "FP8R_%=:\n\t"
      "s_mov_b32 s51, 0\n\t"
      "FP8L_%=:\n\t"
      "s_add_u32 s51, s51, 1\n\t"
      "s_cmp_lt_u32 s51, 262144\n\t"
      "s_cbranch_scc0 FP8X_%=\n\t"
      "s_waitcnt vmcnt(0)\n\t"
      "s_sleep 4\n\t"
      "global_load_dwordx4 v[132:135], v225, s[54:55] sc1\n\t"
      "global_load_dwordx4 v[136:139], v226, s[54:55] sc1\n\t"
      "global_load_dwordx4 v[140:143], v227, s[54:55] sc1\n\t"
      "global_load_dwordx4 v[144:147], v228, s[54:55] sc1\n\t"
      "global_load_dwordx4 v[148:151], v229, s[54:55] sc1\n\t"
      "global_load_dwordx4 v[152:155], v230, s[54:55] sc1\n\t"
      "s_waitcnt vmcnt(5)\n\t"
      "v_max3_u32 v234, v132, v133, v134\n\t"
      "v_max_u32_e32 v234, v234, v135\n\t"
      "v_cmp_eq_u32_e32 vcc, -1, v234\n\t"
      "s_cbranch_vccnz FP8L_%=\n\t"
      "FP8X_%=:\n\t"
      "s_branch FP8B_%=\n\t"
      "FP9R_%=:\n\t"
      "s_mov_b32 s51, 0\n\t"
      "FP9L_%=:\n\t"
      "s_add_u32 s51, s51, 1\n\t"
      "s_cmp_lt_u32 s51, 262144\n\t"
      "s_cbranch_scc0 FP9X_%=\n\t"
      "s_waitcnt vmcnt(0)\n\t"
      "s_sleep 4\n\t"
      "global_load_dwordx4 v[136:139], v226, s[54:55] sc1\n\t"
      "global_load_dwordx4 v[140:143], v227, s[54:55] sc1\n\t"
      "global_load_dwordx4 v[144:147], v228, s[54:55] sc1\n\t"
      "global_load_dwordx4 v[148:151], v229, s[54:55] sc1\n\t"
      "global_load_dwordx4 v[152:155], v230, s[54:55] sc1\n\t"
      "global_load_dwordx4 v[128:131], v224, s[56:57] sc1\n\t"
      "s_waitcnt vmcnt(5)\n\t"
      "v_max3_u32 v234, v136, v137, v138\n\t"
      "v_max_u32_e32 v234, v234, v139\n\t"
      "v_cmp_eq_u32_e32 vcc, -1, v234\n\t"
      "s_cbranch_vccnz FP9L_%=\n\t"
      "FP9X_%=:\n\t"
      "s_branch FP9B_%=\n\t"
      "FP10R_%=:\n\t"
      "s_mov_b32 s51, 0\n\t"
      "FP10L_%=:\n\t"
      "s_add_u32 s51, s51, 1\n\t"
      "s_cmp_lt_u32 s51, 262144\n\t"
      "s_cbranch_scc0 FP10X_%=\n\t"
      "s_waitcnt vmcnt(0)\n\t"
      "s_sleep 4\n\t"
      "global_load_dwordx4 v[140:143], v227, s[54:55] sc1\n\t"
      "global_load_dwordx4 v[144:147], v228, s[54:55] sc1\n\t"
      "global_load_dwordx4 v[148:151], v229, s[54:55] sc1\n\t"
      "global_load_dwordx4 v[152:155], v230, s[54:55] sc1\n\t"
      "global_load_dwordx4 v[128:131], v224, s[56:57] sc1\n\t"
      "global_load_dwordx4 v[132:135], v225, s[56:57] sc1\n\t"
      "s_waitcnt vmcnt(5)\n\t"
      "v_max3_u32 v234, v140, v141, v142\n\t"
      "v_max_u32_e32 v234, v234, v143\n\t"
      "v_cmp_eq_u32_e32 vcc, -1, v234\n\t"
      "s_cbranch_vccnz FP10L_%=\n\t"
      "FP10X_%=:\n\t"
      "s_branch FP10B_%=\n\t"
      "FP11R_%=:\n\t"
      "s_mov_b32 s51, 0\n\t"
      "FP11L_%=:\n\t"
      "s_add_u32 s51, s51, 1\n\t"
      "s_cmp_lt_u32 s51, 262144\n\t"
      "s_cbranch_scc0 FP11X_%=\n\t"
      "s_waitcnt vmcnt(0)\n\t"
      "s_sleep 4\n\t"
      "global_load_dwordx4 v[144:147], v228, s[54:55] sc1\n\t"
      "global_load_dwordx4 v[148:151], v229, s[54:55] sc1\n\t"
      "global_load_dwordx4 v[152:155], v230, s[54:55] sc1\n\t"
      "global_load_dwordx4 v[128:131], v224, s[56:57] sc1\n\t"
      "global_load_dwordx4 v[132:135], v225, s[56:57] sc1\n\t"
      "global_load_dwordx4 v[136:139], v226, s[56:57] sc1\n\t"
      "s_waitcnt vmcnt(5)\n\t"
      "v_max3_u32 v234, v144, v145, v146\n\t"
      "v_max_u32_e32 v234, v234, v147\n\t"
      "v_cmp_eq_u32_e32 vcc, -1, v234\n\t"
      "s_cbranch_vccnz FP11L_%=\n\t"
      "FP11X_%=:\n\t"
      "s_branch FP11B_%=\n\t"
      "FP12R_%=:\n\t"
      "s_mov_b32 s51, 0\n\t"
      "FP12L_%=:\n\t"
      "s_add_u32 s51, s51, 1\n\t"
      "s_cmp_lt_u32 s51, 262144\n\t"
      "s_cbranch_scc0 FP12X_%=\n\t"
      "s_waitcnt vmcnt(0)\n\t"
      "s_sleep 4\n\t"
      "global_load_dwordx4 v[148:151], v229, s[54:55] sc1\n\t"
      "global_load_dwordx4 v[152:155], v230, s[54:55] sc1\n\t"
      "global_load_dwordx4 v[128:131], v224, s[56:57] sc1\n\t"
      "global_load_dwordx4 v[132:135], v225, s[56:57] sc1\n\t"
      "global_load_dwordx4 v[136:139], v226, s[56:57] sc1\n\t"
      "global_load_dwordx4 v[140:143], v227, s[56:57] sc1\n\t"
      "s_waitcnt vmcnt(5)\n\t"
      "v_max3_u32 v234, v148, v149, v150\n\t"
      "v_max_u32_e32 v234, v234, v151\n\t"
      "v_cmp_eq_u32_e32 vcc, -1, v234\n\t"
      "s_cbranch_vccnz FP12L_%=\n\t"
      "FP12X_%=:\n\t"
      "s_branch FP12B_%=\n\t"
      "FP13R_%=:\n\t"
      "s_mov_b32 s51, 0\n\t"
      "FP13L_%=:\n\t"
      "s_add_u32 s51, s51, 1\n\t"
      "s_cmp_lt_u32 s51, 262144\n\t"
      "s_cbranch_scc0 FP13X_%=\n\t"
      "s_waitcnt vmcnt(0)\n\t"
      "s_sleep 4\n\t"
      "global_load_dwordx4 v[152:155], v230, s[54:55] sc1\n\t"
      "global_load_dwordx4 v[128:131], v224, s[56:57] sc1\n\t"
      "global_load_dwordx4 v[132:135], v225, s[56:57] sc1\n\t"
      "global_load_dwordx4 v[136:139], v226, s[56:57] sc1\n\t"
      "global_load_dwordx4 v[140:143], v227, s[56:57] sc1\n\t"
      "global_load_dwordx4 v[144:147], v228, s[56:57] sc1\n\t"
      "s_waitcnt vmcnt(5)\n\t"
      "v_max3_u32 v234, v152, v153, v154\n\t"
      "v_max_u32_e32 v234, v234, v155\n\t"
      "v_cmp_eq_u32_e32 vcc, -1, v234\n\t"
      "s_cbranch_vccnz FP13L_%=\n\t"
      "FP13X_%=:\n\t"
      "s_waitcnt vmcnt(0)\n\t"
      "s_branch FP13B_%=\n\t"
      "FP14R_%=:\n\t"
      "s_mov_b32 s51, 0\n\t"
      "FP14L_%=:\n\t"
      "s_add_u32 s51, s51, 1\n\t"
      "s_cmp_lt_u32 s51, 262144\n\t"
      "s_cbranch_scc0 FP14X_%=\n\t"
      "s_waitcnt vmcnt(0)\n\t"
      "s_sleep 4\n\t"
      "global_load_dwordx4 v[128:131], v224, s[56:57] sc1\n\t"
      "global_load_dwordx4 v[132:135], v225, s[56:57] sc1\n\t"
      "global_load_dwordx4 v[136:139], v226, s[56:57] sc1\n\t"
      "global_load_dwordx4 v[140:143], v227, s[56:57] sc1\n\t"
      "global_load_dwordx4 v[144:147], v228, s[56:57] sc1\n\t"
      "global_load_dwordx4 v[148:151], v229, s[56:57] sc1\n\t"
      "s_waitcnt vmcnt(5)\n\t"
      "v_max3_u32 v234, v128, v129, v130\n\t"
      "v_max_u32_e32 v234, v234, v131\n\t"
      "v_cmp_eq_u32_e32 vcc, -1, v234\n\t"
      "s_cbranch_vccnz FP14L_%=\n\t"
      "FP14X_%=:\n\t"
      "s_waitcnt vmcnt(0)\n\t"
      "s_branch FP14B_%=\n\t"
      "NP1R_%=:\n\t"
      "s_mov_b32 s51, 0\n\t"
      "NP1L_%=:\n\t"
      "s_add_u32 s51, s51, 1\n\t"
      "s_cmp_lt_u32 s51, 262144\n\t"
      "s_cbranch_scc0 NP1X_%=\n\t"
      "s_waitcnt vmcnt(0)\n\t"
      "s_sleep 1\n\t"
      "global_load_dwordx4 v[128:131], v224, s[54:55] sc1\n\t"
      "global_load_dwordx4 v[132:135], v225, s[54:55] sc1\n\t"
      "global_load_dwordx4 v[136:139], v226, s[54:55] sc1\n\t"
      "global_load_dwordx4 v[140:143], v227, s[54:55] sc1\n\t"
      "global_load_dwordx4 v[144:147], v228, s[54:55] sc1\n\t"
      "global_load_dwordx4 v[148:151], v229, s[54:55] sc1\n\t"
      "global_load_dwordx4 v[152:155], v230, s[54:55] sc1\n\t"
      "s_waitcnt vmcnt(0)\n\t"
      "v_max3_u32 v234, v128, v129, v130\n\t"
      "v_max3_u32 v234, v234, v131, v132\n\t"
      "v_max3_u32 v234, v234, v133, v134\n\t"
      "v_max3_u32 v234, v234, v135, v136\n\t"
      "v_max3_u32 v234, v234, v137, v138\n\t"
      "v_max3_u32 v234, v234, v139, v140\n\t"
      "v_max3_u32 v234, v234, v141, v142\n\t"
      "v_max3_u32 v234, v234, v143, v144\n\t"
      "v_max3_u32 v234, v234, v145, v146\n\t"
      "v_max3_u32 v234, v234, v147, v148\n\t"
      "v_max3_u32 v234, v234, v149, v150\n\t"
      "v_max3_u32 v234, v234, v151, v152\n\t"
      "v_max3_u32 v234, v234, v153, v154\n\t"
      "v_max_u32_e32 v234, v234, v155\n\t"
      "v_cmp_eq_u32_e32 vcc, -1, v234\n\t"
      "s_cbranch_vccnz NP1L_%=\n\t"
      "NP1X_%=:\n\t"
      "v_mfma_f32_16x16x32_bf16 v[200:203], v[128:131], v[0:3], v[160:163]\n\t"
      "v_mfma_f32_16x16x32_bf16 v[204:207], v[128:131], v[4:7], 0\n\t"
      "v_mfma_f32_16x16x32_bf16 v[208:211], v[128:131], v[8:11], 0\n\t"
      "v_mfma_f32_16x16x32_bf16 v[212:215], v[128:131], v[12:15], 0\n\t"
      "v_mfma_f32_16x16x32_bf16 v[200:203], v[132:135], v[16:19], v[200:203]\n\t"
      "v_mfma_f32_16x16x32_bf16 v[204:207], v[132:135], v[20:23], v[204:207]\n\t"
      "v_mfma_f32_16x16x32_bf16 v[208:211], v[132:135], v[24:27], v[208:211]\n\t"
      "v_mfma_f32_16x16x32_bf16 v[212:215], v[132:135], v[28:31], v[212:215]\n\t"
      "v_mfma_f32_16x16x32_bf16 v[200:203], v[136:139], v[32:35], v[200:203]\n\t"
      "v_mfma_f32_16x16x32_bf16 v[204:207], v[136:139], v[36:39], v[204:207]\n\t"
      "v_mfma_f32_16x16x32_bf16 v[208:211], v[136:139], v[40:43], v[208:211]\n\t"
      "v_mfma_f32_16x16x32_bf16 v[212:215], v[136:139], v[44:47], v[212:215]\n\t"
      "v_mfma_f32_16x16x32_bf16 v[200:203], v[140:143], v[48:51], v[200:203]\n\t"
      "v_mfma_f32_16x16x32_bf16 v[204:207], v[140:143], v[52:55], v[204:207]\n\t"
      "v_mfma_f32_16x16x32_bf16 v[208:211], v[140:143], v[56:59], v[208:211]\n\t"
      "v_mfma_f32_16x16x32_bf16 v[212:215], v[140:143], v[60:63], v[212:215]\n\t"
      "v_mfma_f32_16x16x32_bf16 v[200:203], v[144:147], v[64:67], v[200:203]\n\t"
      "v_mfma_f32_16x16x32_bf16 v[204:207], v[144:147], v[68:71], v[204:207]\n\t"
      "v_mfma_f32_16x16x32_bf16 v[208:211], v[144:147], v[72:75], v[208:211]\n\t"
      "v_mfma_f32_16x16x32_bf16 v[212:215], v[144:147], v[76:79], v[212:215]\n\t"
      "v_mfma_f32_16x16x32_bf16 v[200:203], v[148:151], v[80:83], v[200:203]\n\t"
      "v_mfma_f32_16x16x32_bf16 v[204:207], v[148:151], v[84:87], v[204:207]\n\t"
      "v_mfma_f32_16x16x32_bf16 v[208:211], v[148:151], v[88:91], v[208:211]\n\t"
      "v_mfma_f32_16x16x32_bf16 v[212:215], v[148:151], v[92:95], v[212:215]\n\t"
      "v_mfma_f32_16x16x32_bf16 v[200:203], v[152:155], v[96:99], v[200:203]\n\t"
      "v_mfma_f32_16x16x32_bf16 v[204:207], v[152:155], v[100:103], v[204:207]\n\t"
      "v_mfma_f32_16x16x32_bf16 v[208:211], v[152:155], v[104:107], v[208:211]\n\t"
      "v_mfma_f32_16x16x32_bf16 v[212:215], v[152:155], v[108:111], v[212:215]\n\t"
      "global_load_dwordx4 v[128:131], v224, s[56:57] sc1\n\t"
      "global_load_dwordx4 v[132:135], v225, s[56:57] sc1\n\t"
      "global_load_dwordx4 v[136:139], v226, s[56:57] sc1\n\t"
      "global_load_dwordx4 v[140:143], v227, s[56:57] sc1\n\t"
      "global_load_dwordx4 v[144:147], v228, s[56:57] sc1\n\t"
      "global_load_dwordx4 v[148:151], v229, s[56:57] sc1\n\t"
      "global_load_dwordx4 v[152:155], v230, s[56:57] sc1\n\t"
      "s_waitcnt vmcnt(0)\n\t"
      "s_nop 7\n\t"
      "s_branch NP1B_%=\n\t"
      "FP15R_%=:\n\t"
      "s_mov_b32 s51, 0\n\t"
      "FP15L_%=:\n\t"
      "s_add_u32 s51, s51, 1\n\t"
      "s_cmp_lt_u32 s51, 262144\n\t"
      "s_cbranch_scc0 FP15X_%=\n\t"
      "s_waitcnt vmcnt(0)\n\t"
      "s_sleep 4\n\t"
      "global_load_dwordx4 v[132:135], v225, s[56:57] sc1\n\t"
      "global_load_dwordx4 v[136:139], v226, s[56:57] sc1\n\t"
      "global_load_dwordx4 v[140:143], v227, s[56:57] sc1\n\t"
      "global_load_dwordx4 v[144:147], v228, s[56:57] sc1\n\t"
      "global_load_dwordx4 v[148:151], v229, s[56:57] sc1\n\t"
      "global_load_dwordx4 v[152:155], v230, s[56:57] sc1\n\t"
      "s_waitcnt vmcnt(5)\n\t"
      "v_max3_u32 v234, v132, v133, v134\n\t"
      "v_max_u32_e32 v234, v234, v135\n\t"
      "v_cmp_eq_u32_e32 vcc, -1, v234\n\t"
      "s_cbranch_vccnz FP15L_%=\n\t"
      "FP15X_%=:\n\t"
      "s_waitcnt vmcnt(0)\n\t"
      "s_branch FP15B_%=\n\t"
      "FP16R_%=:\n\t"
      "s_mov_b32 s51, 0\n\t"
      "FP16L_%=:\n\t"
      "s_add_u32 s51, s51, 1\n\t"
      "s_cmp_lt_u32 s51, 262144\n\t"
      "s_cbranch_scc0 FP16X_%=\n\t"
      "s_waitcnt vmcnt(0)\n\t"
      "s_sleep 4\n\t"
      "global_load_dwordx4 v[136:139], v226, s[56:57] sc1\n\t"
      "global_load_dwordx4 v[140:143], v227, s[56:57] sc1\n\t"
      "global_load_dwordx4 v[144:147], v228, s[56:57] sc1\n\t"
      "global_load_dwordx4 v[148:151], v229, s[56:57] sc1\n\t"
      "global_load_dwordx4 v[152:155], v230, s[56:57] sc1\n\t"
      "global_load_dwordx4 v[128:131], v224, s[58:59] sc1\n\t"
      "s_waitcnt vmcnt(5)\n\t"
      "v_max3_u32 v234, v136, v137, v138\n\t"
      "v_max_u32_e32 v234, v234, v139\n\t"
      "v_cmp_eq_u32_e32 vcc, -1, v234\n\t"
      "s_cbranch_vccnz FP16L_%=\n\t"
      "FP16X_%=:\n\t"
      "s_waitcnt vmcnt(0)\n\t"
      "s_branch FP16B_%=\n\t"
      "FP17R_%=:\n\t"
      "s_mov_b32 s51, 0\n\t"
      "FP17L_%=:\n\t"
      "s_add_u32 s51, s51, 1\n\t"
      "s_cmp_lt_u32 s51, 262144\n\t"
      "s_cbranch_scc0 FP17X_%=\n\t"
      "s_waitcnt vmcnt(0)\n\t"
      "s_sleep 4\n\t"
      "global_load_dwordx4 v[140:143], v227, s[56:57] sc1\n\t"
      "global_load_dwordx4 v[144:147], v228, s[56:57] sc1\n\t"
      "global_load_dwordx4 v[148:151], v229, s[56:57] sc1\n\t"
      "global_load_dwordx4 v[152:155], v230, s[56:57] sc1\n\t"
      "global_load_dwordx4 v[128:131], v224, s[58:59] sc1\n\t"
      "global_load_dwordx4 v[132:135], v225, s[58:59] sc1\n\t"
      "s_waitcnt vmcnt(5)\n\t"
      "v_max3_u32 v234, v140, v141, v142\n\t"
      "v_max_u32_e32 v234, v234, v143\n\t"
      "v_cmp_eq_u32_e32 vcc, -1, v234\n\t"
      "s_cbranch_vccnz FP17L_%=\n\t"
      "FP17X_%=:\n\t"
      "s_waitcnt vmcnt(0)\n\t"
      "s_branch FP17B_%=\n\t"
      "FP18R_%=:\n\t"
      "s_mov_b32 s51, 0\n\t"
      "FP18L_%=:\n\t"
      "s_add_u32 s51, s51, 1\n\t"
      "s_cmp_lt_u32 s51, 262144\n\t"
      "s_cbranch_scc0 FP18X_%=\n\t"
      "s_waitcnt vmcnt(0)\n\t"
      "s_sleep 4\n\t"
      "global_load_dwordx4 v[144:147], v228, s[56:57] sc1\n\t"
      "global_load_dwordx4 v[148:151], v229, s[56:57] sc1\n\t"
      "global_load_dwordx4 v[152:155], v230, s[56:57] sc1\n\t"
      "global_load_dwordx4 v[128:131], v224, s[58:59] sc1\n\t"
      "global_load_dwordx4 v[132:135], v225, s[58:59] sc1\n\t"
      "global_load_dwordx4 v[136:139], v226, s[58:59] sc1\n\t"
      "s_waitcnt vmcnt(5)\n\t"
      "v_max3_u32 v234, v144, v145, v146\n\t"
      "v_max_u32_e32 v234, v234, v147\n\t"
      "v_cmp_eq_u32_e32 vcc, -1, v234\n\t"
      "s_cbranch_vccnz FP18L_%=\n\t"
      "FP18X_%=:\n\t"
      "s_waitcnt vmcnt(0)\n\t"
      "s_branch FP18B_%=\n\t"
      "FP19R_%=:\n\t"
      "s_mov_b32 s51, 0\n\t"
      "FP19L_%=:\n\t"
      "s_add_u32 s51, s51, 1\n\t"
      "s_cmp_lt_u32 s51, 262144\n\t"
      "s_cbranch_scc0 FP19X_%=\n\t"
      "s_waitcnt vmcnt(0)\n\t"
      "s_sleep 4\n\t"
      "global_load_dwordx4 v[148:151], v229, s[56:57] sc1\n\t"
      "global_load_dwordx4 v[152:155], v230, s[56:57] sc1\n\t"
      "global_load_dwordx4 v[128:131], v224, s[58:59] sc1\n\t"
      "global_load_dwordx4 v[132:135], v225, s[58:59] sc1\n\t"
      "global_load_dwordx4 v[136:139], v226, s[58:59] sc1\n\t"
      "global_load_dwordx4 v[140:143], v227, s[58:59] sc1\n\t"
      "s_waitcnt vmcnt(5)\n\t"
      "v_max3_u32 v234, v148, v149, v150\n\t"
      "v_max_u32_e32 v234, v234, v151\n\t"
      "v_cmp_eq_u32_e32 vcc, -1, v234\n\t"
      "s_cbranch_vccnz FP19L_%=\n\t"
      "FP19X_%=:\n\t"
      "s_branch FP19B_%=\n\t"
      "FP20R_%=:\n\t"
      "s_mov_b32 s51, 0\n\t"
      "FP20L_%=:\n\t"
      "s_add_u32 s51, s51, 1\n\t"
      "s_cmp_lt_u32 s51, 262144\n\t"
      "s_cbranch_scc0 FP20X_%=\n\t"
      "s_waitcnt vmcnt(0)\n\t"
      "s_sleep 4\n\t"
      "global_load_dwordx4 v[152:155], v230, s[56:57] sc1\n\t"
      "global_load_dwordx4 v[128:131], v224, s[58:59] sc1\n\t"
      "global_load_dwordx4 v[132:135], v225, s[58:59] sc1\n\t"
      "global_load_dwordx4 v[136:139], v226, s[58:59] sc1\n\t"
      "global_load_dwordx4 v[140:143], v227, s[58:59] sc1\n\t"
      "global_load_dwordx4 v[144:147], v228, s[58:59] sc1\n\t"
      "s_waitcnt vmcnt(5)\n\t"
      "v_max3_u32 v234, v152, v153, v154\n\t"
      "v_max_u32_e32 v234, v234, v155\n\t"
      "v_cmp_eq_u32_e32 vcc, -1, v234\n\t"
      "s_cbranch_vccnz FP20L_%=\n\t"
      "FP20X_%=:\n\t"
      "s_waitcnt vmcnt(0)\n\t"
      "s_branch FP20B_%=\n\t"
      "FP21R_%=:\n\t"
      "s_mov_b32 s51, 0\n\t"
      "FP21L_%=:\n\t"
      "s_add_u32 s51, s51, 1\n\t"
      "s_cmp_lt_u32 s51, 262144\n\t"
      "s_cbranch_scc0 FP21X_%=\n\t"
      "s_waitcnt vmcnt(0)\n\t"
      "s_sleep 4\n\t"
      "global_load_dwordx4 v[128:131], v224, s[58:59] sc1\n\t"
      "global_load_dwordx4 v[132:135], v225, s[58:59] sc1\n\t"
      "global_load_dwordx4 v[136:139], v226, s[58:59] sc1\n\t"
      "global_load_dwordx4 v[140:143], v227, s[58:59] sc1\n\t"
      "global_load_dwordx4 v[144:147], v228, s[58:59] sc1\n\t"
      "global_load_dwordx4 v[148:151], v229, s[58:59] sc1\n\t"
      "s_waitcnt vmcnt(5)\n\t"
      "v_max3_u32 v234, v128, v129, v130\n\t"
      "v_max_u32_e32 v234, v234, v131\n\t"
      "v_cmp_eq_u32_e32 vcc, -1, v234\n\t"
      "s_cbranch_vccnz FP21L_%=\n\t"
      "FP21X_%=:\n\t"
      "s_waitcnt vmcnt(0)\n\t"
      "s_branch FP21B_%=\n\t"
      "NP2R_%=:\n\t"
      "s_mov_b32 s51, 0\n\t"
      "NP2L_%=:\n\t"
      "s_add_u32 s51, s51, 1\n\t"
      "s_cmp_lt_u32 s51, 262144\n\t"
      "s_cbranch_scc0 NP2X_%=\n\t"
      "s_waitcnt vmcnt(0)\n\t"
      "s_sleep 1\n\t"
      "global_load_dwordx4 v[128:131], v224, s[56:57] sc1\n\t"
      "global_load_dwordx4 v[132:135], v225, s[56:57] sc1\n\t"
      "global_load_dwordx4 v[136:139], v226, s[56:57] sc1\n\t"
      "global_load_dwordx4 v[140:143], v227, s[56:57] sc1\n\t"
      "global_load_dwordx4 v[144:147], v228, s[56:57] sc1\n\t"
      "global_load_dwordx4 v[148:151], v229, s[56:57] sc1\n\t"
      "global_load_dwordx4 v[152:155], v230, s[56:57] sc1\n\t"
      "s_waitcnt vmcnt(0)\n\t"
      "v_max3_u32 v234, v128, v129, v130\n\t"
      "v_max3_u32 v234, v234, v131, v132\n\t"
      "v_max3_u32 v234, v234, v133, v134\n\t"
      "v_max3_u32 v234, v234, v135, v136\n\t"
      "v_max3_u32 v234, v234, v137, v138\n\t"
      "v_max3_u32 v234, v234, v139, v140\n\t"
      "v_max3_u32 v234, v234, v141, v142\n\t"
      "v_max3_u32 v234, v234, v143, v144\n\t"
      "v_max3_u32 v234, v234, v145, v146\n\t"
      "v_max3_u32 v234, v234, v147, v148\n\t"
      "v_max3_u32 v234, v234, v149, v150\n\t"
      "v_max3_u32 v234, v234, v151, v152\n\t"
      "v_max3_u32 v234, v234, v153, v154\n\t"
      "v_max_u32_e32 v234, v234, v155\n\t"
      "v_cmp_eq_u32_e32 vcc, -1, v234\n\t"
      "s_cbranch_vccnz NP2L_%=\n\t"
      "NP2X_%=:\n\t"
      "v_mfma_f32_16x16x32_bf16 v[184:187], v[128:131], v[0:3], v[164:167]\n\t"
      "v_mfma_f32_16x16x32_bf16 v[188:191], v[128:131], v[4:7], 0\n\t"
      "v_mfma_f32_16x16x32_bf16 v[192:195], v[128:131], v[8:11], 0\n\t"
      "v_mfma_f32_16x16x32_bf16 v[196:199], v[128:131], v[12:15], 0\n\t"
      "v_mfma_f32_16x16x32_bf16 v[184:187], v[132:135], v[16:19], v[184:187]\n\t"
      "v_mfma_f32_16x16x32_bf16 v[188:191], v[132:135], v[20:23], v[188:191]\n\t"
      "v_mfma_f32_16x16x32_bf16 v[192:195], v[132:135], v[24:27], v[192:195]\n\t"
      "v_mfma_f32_16x16x32_bf16 v[196:199], v[132:135], v[28:31], v[196:199]\n\t"
      "v_mfma_f32_16x16x32_bf16 v[184:187], v[136:139], v[32:35], v[184:187]\n\t"
      "v_mfma_f32_16x16x32_bf16 v[188:191], v[136:139], v[36:39], v[188:191]\n\t"
      "v_mfma_f32_16x16x32_bf16 v[192:195], v[136:139], v[40:43], v[192:195]\n\t"
      "v_mfma_f32_16x16x32_bf16 v[196:199], v[136:139], v[44:47], v[196:199]\n\t"
      "v_mfma_f32_16x16x32_bf16 v[184:187], v[140:143], v[48:51], v[184:187]\n\t"
      "v_mfma_f32_16x16x32_bf16 v[188:191], v[140:143], v[52:55], v[188:191]\n\t"
      "v_mfma_f32_16x16x32_bf16 v[192:195], v[140:143], v[56:59], v[192:195]\n\t"
      "v_mfma_f32_16x16x32_bf16 v[196:199], v[140:143], v[60:63], v[196:199]\n\t"
      "v_mfma_f32_16x16x32_bf16 v[184:187], v[144:147], v[64:67], v[184:187]\n\t"
      "v_mfma_f32_16x16x32_bf16 v[188:191], v[144:147], v[68:71], v[188:191]\n\t"
      "v_mfma_f32_16x16x32_bf16 v[192:195], v[144:147], v[72:75], v[192:195]\n\t"
      "v_mfma_f32_16x16x32_bf16 v[196:199], v[144:147], v[76:79], v[196:199]\n\t"
      "v_mfma_f32_16x16x32_bf16 v[184:187], v[148:151], v[80:83], v[184:187]\n\t"
      "v_mfma_f32_16x16x32_bf16 v[188:191], v[148:151], v[84:87], v[188:191]\n\t"
      "v_mfma_f32_16x16x32_bf16 v[192:195], v[148:151], v[88:91], v[192:195]\n\t"
      "v_mfma_f32_16x16x32_bf16 v[196:199], v[148:151], v[92:95], v[196:199]\n\t"
      "v_mfma_f32_16x16x32_bf16 v[184:187], v[152:155], v[96:99], v[184:187]\n\t"
      "v_mfma_f32_16x16x32_bf16 v[188:191], v[152:155], v[100:103], v[188:191]\n\t"
      "v_mfma_f32_16x16x32_bf16 v[192:195], v[152:155], v[104:107], v[192:195]\n\t"
      "v_mfma_f32_16x16x32_bf16 v[196:199], v[152:155], v[108:111], v[196:199]\n\t"
      "global_load_dwordx4 v[128:131], v224, s[58:59] sc1\n\t"
      "global_load_dwordx4 v[132:135], v225, s[58:59] sc1\n\t"
      "global_load_dwordx4 v[136:139], v226, s[58:59] sc1\n\t"
      "global_load_dwordx4 v[140:143], v227, s[58:59] sc1\n\t"
      "global_load_dwordx4 v[144:147], v228, s[58:59] sc1\n\t"
      "global_load_dwordx4 v[148:151], v229, s[58:59] sc1\n\t"
      "global_load_dwordx4 v[152:155], v230, s[58:59] sc1\n\t"
      "s_waitcnt vmcnt(0)\n\t"
      "s_nop 7\n\t"
      "s_branch NP2B_%=\n\t"
      "FP22R_%=:\n\t"
      "s_mov_b32 s51, 0\n\t"
      "FP22L_%=:\n\t"
      "s_add_u32 s51, s51, 1\n\t"
      "s_cmp_lt_u32 s51, 262144\n\t"
      "s_cbranch_scc0 FP22X_%=\n\t"
      "s_waitcnt vmcnt(0)\n\t"
      "s_sleep 4\n\t"
      "global_load_dwordx4 v[132:135], v225, s[58:59] sc1\n\t"
      "global_load_dwordx4 v[136:139], v226, s[58:59] sc1\n\t"
      "global_load_dwordx4 v[140:143], v227, s[58:59] sc1\n\t"
      "global_load_dwordx4 v[144:147], v228, s[58:59] sc1\n\t"
      "global_load_dwordx4 v[148:151], v229, s[58:59] sc1\n\t"
      "global_load_dwordx4 v[152:155], v230, s[58:59] sc1\n\t"
      "s_waitcnt vmcnt(5)\n\t"
      "v_max3_u32 v234, v132, v133, v134\n\t"
      "v_max_u32_e32 v234, v234, v135\n\t"
      "v_cmp_eq_u32_e32 vcc, -1, v234\n\t"
      "s_cbranch_vccnz FP22L_%=\n\t"
      "FP22X_%=:\n\t"
      "s_waitcnt vmcnt(0)\n\t"
      "s_branch FP22B_%=\n\t"
      "FP23R_%=:\n\t"
      "s_mov_b32 s51, 0\n\t"
      "FP23L_%=:\n\t"
      "s_add_u32 s51, s51, 1\n\t"
      "s_cmp_lt_u32 s51, 262144\n\t"
      "s_cbranch_scc0 FP23X_%=\n\t"
      "s_waitcnt vmcnt(0)\n\t"
      "s_sleep 4\n\t"
      "global_load_dwordx4 v[136:139], v226, s[58:59] sc1\n\t"
      "global_load_dwordx4 v[140:143], v227, s[58:59] sc1\n\t"
      "global_load_dwordx4 v[144:147], v228, s[58:59] sc1\n\t"
      "global_load_dwordx4 v[148:151], v229, s[58:59] sc1\n\t"
      "global_load_dwordx4 v[152:155], v230, s[58:59] sc1\n\t"
      "global_load_dwordx4 v[128:131], v224, s[60:61] sc1\n\t"
      "s_waitcnt vmcnt(5)\n\t"
      "v_max3_u32 v234, v136, v137, v138\n\t"
      "v_max_u32_e32 v234, v234, v139\n\t"
      "v_cmp_eq_u32_e32 vcc, -1, v234\n\t"
      "s_cbranch_vccnz FP23L_%=\n\t"
      "FP23X_%=:\n\t"
      "s_waitcnt vmcnt(0)\n\t"
      "s_branch FP23B_%=\n\t"
      "FP24R_%=:\n\t"
      "s_mov_b32 s51, 0\n\t"
      "FP24L_%=:\n\t"
      "s_add_u32 s51, s51, 1\n\t"
      "s_cmp_lt_u32 s51, 262144\n\t"
      "s_cbranch_scc0 FP24X_%=\n\t"
      "s_waitcnt vmcnt(0)\n\t"
      "s_sleep 4\n\t"
      "global_load_dwordx4 v[140:143], v227, s[58:59] sc1\n\t"
      "global_load_dwordx4 v[144:147], v228, s[58:59] sc1\n\t"
      "global_load_dwordx4 v[148:151], v229, s[58:59] sc1\n\t"
      "global_load_dwordx4 v[152:155], v230, s[58:59] sc1\n\t"
      "global_load_dwordx4 v[128:131], v224, s[60:61] sc1\n\t"
      "global_load_dwordx4 v[132:135], v225, s[60:61] sc1\n\t"
      "s_waitcnt vmcnt(5)\n\t"
      "v_max3_u32 v234, v140, v141, v142\n\t"
      "v_max_u32_e32 v234, v234, v143\n\t"
      "v_cmp_eq_u32_e32 vcc, -1, v234\n\t"
      "s_cbranch_vccnz FP24L_%=\n\t"
      "FP24X_%=:\n\t"
      "s_waitcnt vmcnt(0)\n\t"
      "s_branch FP24B_%=\n\t"
      "FP25R_%=:\n\t"
      "s_mov_b32 s51, 0\n\t"
      "FP25L_%=:\n\t"
      "s_add_u32 s51, s51, 1\n\t"
      "s_cmp_lt_u32 s51, 262144\n\t"
      "s_cbranch_scc0 FP25X_%=\n\t"
      "s_waitcnt vmcnt(0)\n\t"
      "s_sleep 4\n\t"
      "global_load_dwordx4 v[144:147], v228, s[58:59] sc1\n\t"
      "global_load_dwordx4 v[148:151], v229, s[58:59] sc1\n\t"
      "global_load_dwordx4 v[152:155], v230, s[58:59] sc1\n\t"
      "global_load_dwordx4 v[128:131], v224, s[60:61] sc1\n\t"
      "global_load_dwordx4 v[132:135], v225, s[60:61] sc1\n\t"
      "global_load_dwordx4 v[136:139], v226, s[60:61] sc1\n\t"
      "s_waitcnt vmcnt(5)\n\t"
      "v_max3_u32 v234, v144, v145, v146\n\t"
      "v_max_u32_e32 v234, v234, v147\n\t"
      "v_cmp_eq_u32_e32 vcc, -1, v234\n\t"
      "s_cbranch_vccnz FP25L_%=\n\t"
      "FP25X_%=:\n\t"
      "s_waitcnt vmcnt(0)\n\t"
      "s_branch FP25B_%=\n\t"
      "FP26R_%=:\n\t"
      "s_mov_b32 s51, 0\n\t"
      "FP26L_%=:\n\t"
      "s_add_u32 s51, s51, 1\n\t"
      "s_cmp_lt_u32 s51, 262144\n\t"
      "s_cbranch_scc0 FP26X_%=\n\t"
      "s_waitcnt vmcnt(0)\n\t"
      "s_sleep 4\n\t"
      "global_load_dwordx4 v[148:151], v229, s[58:59] sc1\n\t"
      "global_load_dwordx4 v[152:155], v230, s[58:59] sc1\n\t"
      "global_load_dwordx4 v[128:131], v224, s[60:61] sc1\n\t"
      "global_load_dwordx4 v[132:135], v225, s[60:61] sc1\n\t"
      "global_load_dwordx4 v[136:139], v226, s[60:61] sc1\n\t"
      "global_load_dwordx4 v[140:143], v227, s[60:61] sc1\n\t"
      "s_waitcnt vmcnt(5)\n\t"
      "v_max3_u32 v234, v148, v149, v150\n\t"
      "v_max_u32_e32 v234, v234, v151\n\t"
      "v_cmp_eq_u32_e32 vcc, -1, v234\n\t"
      "s_cbranch_vccnz FP26L_%=\n\t"
      "FP26X_%=:\n\t"
      "s_branch FP26B_%=\n\t"
      "FP27R_%=:\n\t"
      "s_mov_b32 s51, 0\n\t"
      "FP27L_%=:\n\t"
      "s_add_u32 s51, s51, 1\n\t"
      "s_cmp_lt_u32 s51, 262144\n\t"
      "s_cbranch_scc0 FP27X_%=\n\t"
      "s_waitcnt vmcnt(0)\n\t"
      "s_sleep 4\n\t"
      "global_load_dwordx4 v[152:155], v230, s[58:59] sc1\n\t"
      "global_load_dwordx4 v[128:131], v224, s[60:61] sc1\n\t"
      "global_load_dwordx4 v[132:135], v225, s[60:61] sc1\n\t"
      "global_load_dwordx4 v[136:139], v226, s[60:61] sc1\n\t"
      "global_load_dwordx4 v[140:143], v227, s[60:61] sc1\n\t"
      "global_load_dwordx4 v[144:147], v228, s[60:61] sc1\n\t"
      "s_waitcnt vmcnt(5)\n\t"
      "v_max3_u32 v234, v152, v153, v154\n\t"
      "v_max_u32_e32 v234, v234, v155\n\t"
      "v_cmp_eq_u32_e32 vcc, -1, v234\n\t"
      "s_cbranch_vccnz FP27L_%=\n\t"
      "FP27X_%=:\n\t"
      "s_waitcnt vmcnt(0)\n\t"
      "s_branch FP27B_%=\n\t"
      "FP28R_%=:\n\t"
      "s_mov_b32 s51, 0\n\t"
      "FP28L_%=:\n\t"
      "s_add_u32 s51, s51, 1\n\t"
      "s_cmp_lt_u32 s51, 262144\n\t"
      "s_cbranch_scc0 FP28X_%=\n\t"
      "s_waitcnt vmcnt(0)\n\t"
      "s_sleep 4\n\t"
      "global_load_dwordx4 v[128:131], v224, s[60:61] sc1\n\t"
      "global_load_dwordx4 v[132:135], v225, s[60:61] sc1\n\t"
      "global_load_dwordx4 v[136:139], v226, s[60:61] sc1\n\t"
      "global_load_dwordx4 v[140:143], v227, s[60:61] sc1\n\t"
      "global_load_dwordx4 v[144:147], v228, s[60:61] sc1\n\t"
      "global_load_dwordx4 v[148:151], v229, s[60:61] sc1\n\t"
      "s_waitcnt vmcnt(5)\n\t"
      "v_max3_u32 v234, v128, v129, v130\n\t"
      "v_max_u32_e32 v234, v234, v131\n\t"
      "v_cmp_eq_u32_e32 vcc, -1, v234\n\t"
      "s_cbranch_vccnz FP28L_%=\n\t"
      "FP28X_%=:\n\t"
      "s_waitcnt vmcnt(0)\n\t"
      "s_branch FP28B_%=\n\t"
      "NP3R_%=:\n\t"
      "s_mov_b32 s51, 0\n\t"
      "NP3L_%=:\n\t"
      "s_add_u32 s51, s51, 1\n\t"
      "s_cmp_lt_u32 s51, 262144\n\t"
      "s_cbranch_scc0 NP3X_%=\n\t"
      "s_waitcnt vmcnt(0)\n\t"
      "s_sleep 1\n\t"
      "global_load_dwordx4 v[128:131], v224, s[58:59] sc1\n\t"
      "global_load_dwordx4 v[132:135], v225, s[58:59] sc1\n\t"
      "global_load_dwordx4 v[136:139], v226, s[58:59] sc1\n\t"
      "global_load_dwordx4 v[140:143], v227, s[58:59] sc1\n\t"
      "global_load_dwordx4 v[144:147], v228, s[58:59] sc1\n\t"
      "global_load_dwordx4 v[148:151], v229, s[58:59] sc1\n\t"
      "global_load_dwordx4 v[152:155], v230, s[58:59] sc1\n\t"
      "s_waitcnt vmcnt(0)\n\t"
      "v_max3_u32 v234, v128, v129, v130\n\t"
      "v_max3_u32 v234, v234, v131, v132\n\t"
      "v_max3_u32 v234, v234, v133, v134\n\t"
      "v_max3_u32 v234, v234, v135, v136\n\t"
      "v_max3_u32 v234, v234, v137, v138\n\t"
      "v_max3_u32 v234, v234, v139, v140\n\t"
      "v_max3_u32 v234, v234, v141, v142\n\t"
      "v_max3_u32 v234, v234, v143, v144\n\t"
      "v_max3_u32 v234, v234, v145, v146\n\t"
      "v_max3_u32 v234, v234, v147, v148\n\t"
      "v_max3_u32 v234, v234, v149, v150\n\t"
      "v_max3_u32 v234, v234, v151, v152\n\t"
      "v_max3_u32 v234, v234, v153, v154\n\t"
      "v_max_u32_e32 v234, v234, v155\n\t"
      "v_cmp_eq_u32_e32 vcc, -1, v234\n\t"
      "s_cbranch_vccnz NP3L_%=\n\t"
      "NP3X_%=:\n\t"
      "v_mfma_f32_16x16x32_bf16 v[200:203], v[128:131], v[0:3], v[168:171]\n\t"
      "v_mfma_f32_16x16x32_bf16 v[204:207], v[128:131], v[4:7], 0\n\t"
      "v_mfma_f32_16x16x32_bf16 v[208:211], v[128:131], v[8:11], 0\n\t"
      "v_mfma_f32_16x16x32_bf16 v[212:215], v[128:131], v[12:15], 0\n\t"
      "v_mfma_f32_16x16x32_bf16 v[200:203], v[132:135], v[16:19], v[200:203]\n\t"
      "v_mfma_f32_16x16x32_bf16 v[204:207], v[132:135], v[20:23], v[204:207]\n\t"
      "v_mfma_f32_16x16x32_bf16 v[208:211], v[132:135], v[24:27], v[208:211]\n\t"
      "v_mfma_f32_16x16x32_bf16 v[212:215], v[132:135], v[28:31], v[212:215]\n\t"
      "v_mfma_f32_16x16x32_bf16 v[200:203], v[136:139], v[32:35], v[200:203]\n\t"
      "v_mfma_f32_16x16x32_bf16 v[204:207], v[136:139], v[36:39], v[204:207]\n\t"
      "v_mfma_f32_16x16x32_bf16 v[208:211], v[136:139], v[40:43], v[208:211]\n\t"
      "v_mfma_f32_16x16x32_bf16 v[212:215], v[136:139], v[44:47], v[212:215]\n\t"
      "v_mfma_f32_16x16x32_bf16 v[200:203], v[140:143], v[48:51], v[200:203]\n\t"
      "v_mfma_f32_16x16x32_bf16 v[204:207], v[140:143], v[52:55], v[204:207]\n\t"
      "v_mfma_f32_16x16x32_bf16 v[208:211], v[140:143], v[56:59], v[208:211]\n\t"
      "v_mfma_f32_16x16x32_bf16 v[212:215], v[140:143], v[60:63], v[212:215]\n\t"
      "v_mfma_f32_16x16x32_bf16 v[200:203], v[144:147], v[64:67], v[200:203]\n\t"
      "v_mfma_f32_16x16x32_bf16 v[204:207], v[144:147], v[68:71], v[204:207]\n\t"
      "v_mfma_f32_16x16x32_bf16 v[208:211], v[144:147], v[72:75], v[208:211]\n\t"
      "v_mfma_f32_16x16x32_bf16 v[212:215], v[144:147], v[76:79], v[212:215]\n\t"
      "v_mfma_f32_16x16x32_bf16 v[200:203], v[148:151], v[80:83], v[200:203]\n\t"
      "v_mfma_f32_16x16x32_bf16 v[204:207], v[148:151], v[84:87], v[204:207]\n\t"
      "v_mfma_f32_16x16x32_bf16 v[208:211], v[148:151], v[88:91], v[208:211]\n\t"
      "v_mfma_f32_16x16x32_bf16 v[212:215], v[148:151], v[92:95], v[212:215]\n\t"
      "v_mfma_f32_16x16x32_bf16 v[200:203], v[152:155], v[96:99], v[200:203]\n\t"
      "v_mfma_f32_16x16x32_bf16 v[204:207], v[152:155], v[100:103], v[204:207]\n\t"
      "v_mfma_f32_16x16x32_bf16 v[208:211], v[152:155], v[104:107], v[208:211]\n\t"
      "v_mfma_f32_16x16x32_bf16 v[212:215], v[152:155], v[108:111], v[212:215]\n\t"
      "global_load_dwordx4 v[128:131], v224, s[60:61] sc1\n\t"
      "global_load_dwordx4 v[132:135], v225, s[60:61] sc1\n\t"
      "global_load_dwordx4 v[136:139], v226, s[60:61] sc1\n\t"
      "global_load_dwordx4 v[140:143], v227, s[60:61] sc1\n\t"
      "global_load_dwordx4 v[144:147], v228, s[60:61] sc1\n\t"
      "global_load_dwordx4 v[148:151], v229, s[60:61] sc1\n\t"
      "global_load_dwordx4 v[152:155], v230, s[60:61] sc1\n\t"
      "s_waitcnt vmcnt(0)\n\t"
      "s_nop 7\n\t"
      "s_branch NP3B_%=\n\t"
      "FP29R_%=:\n\t"
      "s_mov_b32 s51, 0\n\t"
      "FP29L_%=:\n\t"
      "s_add_u32 s51, s51, 1\n\t"
      "s_cmp_lt_u32 s51, 262144\n\t"
      "s_cbranch_scc0 FP29X_%=\n\t"
      "s_waitcnt vmcnt(0)\n\t"
      "s_sleep 4\n\t"
      "global_load_dwordx4 v[132:135], v225, s[60:61] sc1\n\t"
      "global_load_dwordx4 v[136:139], v226, s[60:61] sc1\n\t"
      "global_load_dwordx4 v[140:143], v227, s[60:61] sc1\n\t"
      "global_load_dwordx4 v[144:147], v228, s[60:61] sc1\n\t"
      "global_load_dwordx4 v[148:151], v229, s[60:61] sc1\n\t"
      "global_load_dwordx4 v[152:155], v230, s[60:61] sc1\n\t"
      "s_waitcnt vmcnt(5)\n\t"
      "v_max3_u32 v234, v132, v133, v134\n\t"
      "v_max_u32_e32 v234, v234, v135\n\t"
      "v_cmp_eq_u32_e32 vcc, -1, v234\n\t"
      "s_cbranch_vccnz FP29L_%=\n\t"
      "FP29X_%=:\n\t"
      "s_waitcnt vmcnt(0)\n\t"
      "s_branch FP29B_%=\n\t"
      "FP30R_%=:\n\t"
      "s_mov_b32 s51, 0\n\t"
      "FP30L_%=:\n\t"
      "s_add_u32 s51, s51, 1\n\t"
      "s_cmp_lt_u32 s51, 262144\n\t"
      "s_cbranch_scc0 FP30X_%=\n\t"
      "s_waitcnt vmcnt(0)\n\t"
      "s_sleep 4\n\t"
      "global_load_dwordx4 v[136:139], v226, s[60:61] sc1\n\t"
      "global_load_dwordx4 v[140:143], v227, s[60:61] sc1\n\t"
      "global_load_dwordx4 v[144:147], v228, s[60:61] sc1\n\t"
      "global_load_dwordx4 v[148:151], v229, s[60:61] sc1\n\t"
      "global_load_dwordx4 v[152:155], v230, s[60:61] sc1\n\t"
      "global_load_dwordx4 v[128:131], v224, s[62:63] sc1\n\t"
      "s_waitcnt vmcnt(5)\n\t"
      "v_max3_u32 v234, v136, v137, v138\n\t"
      "v_max_u32_e32 v234, v234, v139\n\t"
      "v_cmp_eq_u32_e32 vcc, -1, v234\n\t"
      "s_cbranch_vccnz FP30L_%=\n\t"
      "FP30X_%=:\n\t"
      "s_waitcnt vmcnt(0)\n\t"
      "s_branch FP30B_%=\n\t"
      "FP31R_%=:\n\t"
      "s_mov_b32 s51, 0\n\t"
      "FP31L_%=:\n\t"
      "s_add_u32 s51, s51, 1\n\t"
      "s_cmp_lt_u32 s51, 262144\n\t"
      "s_cbranch_scc0 FP31X_%=\n\t"
      "s_waitcnt vmcnt(0)\n\t"
      "s_sleep 4\n\t"
      "global_load_dwordx4 v[140:143], v227, s[60:61] sc1\n\t"
      "global_load_dwordx4 v[144:147], v228, s[60:61] sc1\n\t"
      "global_load_dwordx4 v[148:151], v229, s[60:61] sc1\n\t"
      "global_load_dwordx4 v[152:155], v230, s[60:61] sc1\n\t"
      "global_load_dwordx4 v[128:131], v224, s[62:63] sc1\n\t"
      "global_load_dwordx4 v[132:135], v225, s[62:63] sc1\n\t"
      "s_waitcnt vmcnt(5)\n\t"
      "v_max3_u32 v234, v140, v141, v142\n\t"
      "v_max_u32_e32 v234, v234, v143\n\t"
      "v_cmp_eq_u32_e32 vcc, -1, v234\n\t"
      "s_cbranch_vccnz FP31L_%=\n\t"
      "FP31X_%=:\n\t"
      "s_waitcnt vmcnt(0)\n\t"
      "s_branch FP31B_%=\n\t"
      "FP32R_%=:\n\t"
      "s_mov_b32 s51, 0\n\t"
      "FP32L_%=:\n\t"
      "s_add_u32 s51, s51, 1\n\t"
      "s_cmp_lt_u32 s51, 262144\n\t"
      "s_cbranch_scc0 FP32X_%=\n\t"
      "s_waitcnt vmcnt(0)\n\t"
      "s_sleep 4\n\t"
      "global_load_dwordx4 v[144:147], v228, s[60:61] sc1\n\t"
      "global_load_dwordx4 v[148:151], v229, s[60:61] sc1\n\t"
      "global_load_dwordx4 v[152:155], v230, s[60:61] sc1\n\t"
      "global_load_dwordx4 v[128:131], v224, s[62:63] sc1\n\t"
      "global_load_dwordx4 v[132:135], v225, s[62:63] sc1\n\t"
      "global_load_dwordx4 v[136:139], v226, s[62:63] sc1\n\t"
      "s_waitcnt vmcnt(5)\n\t"
      "v_max3_u32 v234, v144, v145, v146\n\t"
      "v_max_u32_e32 v234, v234, v147\n\t"
      "v_cmp_eq_u32_e32 vcc, -1, v234\n\t"
      "s_cbranch_vccnz FP32L_%=\n\t"
      "FP32X_%=:\n\t"
      "s_waitcnt vmcnt(0)\n\t"
      "s_branch FP32B_%=\n\t"
      "FP33R_%=:\n\t"
      "s_mov_b32 s51, 0\n\t"
      "FP33L_%=:\n\t"
      "s_add_u32 s51, s51, 1\n\t"
      "s_cmp_lt_u32 s51, 262144\n\t"
      "s_cbranch_scc0 FP33X_%=\n\t"
      "s_waitcnt vmcnt(0)\n\t"
      "s_sleep 4\n\t"
      "global_load_dwordx4 v[148:151], v229, s[60:61] sc1\n\t"
      "global_load_dwordx4 v[152:155], v230, s[60:61] sc1\n\t"
      "global_load_dwordx4 v[128:131], v224, s[62:63] sc1\n\t"
      "global_load_dwordx4 v[132:135], v225, s[62:63] sc1\n\t"
      "global_load_dwordx4 v[136:139], v226, s[62:63] sc1\n\t"
      "global_load_dwordx4 v[140:143], v227, s[62:63] sc1\n\t"
      "s_waitcnt vmcnt(5)\n\t"
      "v_max3_u32 v234, v148, v149, v150\n\t"
      "v_max_u32_e32 v234, v234, v151\n\t"
      "v_cmp_eq_u32_e32 vcc, -1, v234\n\t"
      "s_cbranch_vccnz FP33L_%=\n\t"
      "FP33X_%=:\n\t"
      "s_branch FP33B_%=\n\t"
      "FP34R_%=:\n\t"
      "s_mov_b32 s51, 0\n\t"
      "FP34L_%=:\n\t"
      "s_add_u32 s51, s51, 1\n\t"
      "s_cmp_lt_u32 s51, 262144\n\t"
      "s_cbranch_scc0 FP34X_%=\n\t"
      "s_waitcnt vmcnt(0)\n\t"
      "s_sleep 4\n\t"
      "global_load_dwordx4 v[152:155], v230, s[60:61] sc1\n\t"
      "global_load_dwordx4 v[128:131], v224, s[62:63] sc1\n\t"
      "global_load_dwordx4 v[132:135], v225, s[62:63] sc1\n\t"
      "global_load_dwordx4 v[136:139], v226, s[62:63] sc1\n\t"
      "global_load_dwordx4 v[140:143], v227, s[62:63] sc1\n\t"
      "global_load_dwordx4 v[144:147], v228, s[62:63] sc1\n\t"
      "s_waitcnt vmcnt(5)\n\t"
      "v_max3_u32 v234, v152, v153, v154\n\t"
      "v_max_u32_e32 v234, v234, v155\n\t"
      "v_cmp_eq_u32_e32 vcc, -1, v234\n\t"
      "s_cbranch_vccnz FP34L_%=\n\t"
      "FP34X_%=:\n\t"
      "s_waitcnt vmcnt(0)\n\t"
      "s_branch FP34B_%=\n\t"
      "FP35R_%=:\n\t"
      "s_mov_b32 s51, 0\n\t"
      "FP35L_%=:\n\t"
      "s_add_u32 s51, s51, 1\n\t"
      "s_cmp_lt_u32 s51, 262144\n\t"
      "s_cbranch_scc0 FP35X_%=\n\t"
      "s_waitcnt vmcnt(0)\n\t"
      "s_sleep 4\n\t"
      "global_load_dwordx4 v[128:131], v224, s[62:63] sc1\n\t"
      "global_load_dwordx4 v[132:135], v225, s[62:63] sc1\n\t"
      "global_load_dwordx4 v[136:139], v226, s[62:63] sc1\n\t"
      "global_load_dwordx4 v[140:143], v227, s[62:63] sc1\n\t"
      "global_load_dwordx4 v[144:147], v228, s[62:63] sc1\n\t"
      "global_load_dwordx4 v[148:151], v229, s[62:63] sc1\n\t"
      "s_waitcnt vmcnt(5)\n\t"
      "v_max3_u32 v234, v128, v129, v130\n\t"
      "v_max_u32_e32 v234, v234, v131\n\t"
      "v_cmp_eq_u32_e32 vcc, -1, v234\n\t"
      "s_cbranch_vccnz FP35L_%=\n\t"
      "FP35X_%=:\n\t"
      "s_waitcnt vmcnt(0)\n\t"
      "s_branch FP35B_%=\n\t"
      "NP4R_%=:\n\t"
      "s_mov_b32 s51, 0\n\t"
      "NP4L_%=:\n\t"
      "s_add_u32 s51, s51, 1\n\t"
      "s_cmp_lt_u32 s51, 262144\n\t"
      "s_cbranch_scc0 NP4X_%=\n\t"
      "s_waitcnt vmcnt(0)\n\t"
      "s_sleep 1\n\t"
      "global_load_dwordx4 v[128:131], v224, s[60:61] sc1\n\t"
      "global_load_dwordx4 v[132:135], v225, s[60:61] sc1\n\t"
      "global_load_dwordx4 v[136:139], v226, s[60:61] sc1\n\t"
      "global_load_dwordx4 v[140:143], v227, s[60:61] sc1\n\t"
      "global_load_dwordx4 v[144:147], v228, s[60:61] sc1\n\t"
      "global_load_dwordx4 v[148:151], v229, s[60:61] sc1\n\t"
      "global_load_dwordx4 v[152:155], v230, s[60:61] sc1\n\t"
      "s_waitcnt vmcnt(0)\n\t"
      "v_max3_u32 v234, v128, v129, v130\n\t"
      "v_max3_u32 v234, v234, v131, v132\n\t"
      "v_max3_u32 v234, v234, v133, v134\n\t"
      "v_max3_u32 v234, v234, v135, v136\n\t"
      "v_max3_u32 v234, v234, v137, v138\n\t"
      "v_max3_u32 v234, v234, v139, v140\n\t"
      "v_max3_u32 v234, v234, v141, v142\n\t"
      "v_max3_u32 v234, v234, v143, v144\n\t"
      "v_max3_u32 v234, v234, v145, v146\n\t"
      "v_max3_u32 v234, v234, v147, v148\n\t"
      "v_max3_u32 v234, v234, v149, v150\n\t"
      "v_max3_u32 v234, v234, v151, v152\n\t"
      "v_max3_u32 v234, v234, v153, v154\n\t"
      "v_max_u32_e32 v234, v234, v155\n\t"
      "v_cmp_eq_u32_e32 vcc, -1, v234\n\t"
      "s_cbranch_vccnz NP4L_%=\n\t"
      "NP4X_%=:\n\t"
      "v_mfma_f32_16x16x32_bf16 v[184:187], v[128:131], v[0:3], v[172:175]\n\t"
      "v_mfma_f32_16x16x32_bf16 v[188:191], v[128:131], v[4:7], 0\n\t"
      "v_mfma_f32_16x16x32_bf16 v[192:195], v[128:131], v[8:11], 0\n\t"
      "v_mfma_f32_16x16x32_bf16 v[196:199], v[128:131], v[12:15], 0\n\t"
      "v_mfma_f32_16x16x32_bf16 v[184:187], v[132:135], v[16:19], v[184:187]\n\t"
      "v_mfma_f32_16x16x32_bf16 v[188:191], v[132:135], v[20:23], v[188:191]\n\t"
      "v_mfma_f32_16x16x32_bf16 v[192:195], v[132:135], v[24:27], v[192:195]\n\t"
      "v_mfma_f32_16x16x32_bf16 v[196:199], v[132:135], v[28:31], v[196:199]\n\t"
      "v_mfma_f32_16x16x32_bf16 v[184:187], v[136:139], v[32:35], v[184:187]\n\t"
      "v_mfma_f32_16x16x32_bf16 v[188:191], v[136:139], v[36:39], v[188:191]\n\t"
      "v_mfma_f32_16x16x32_bf16 v[192:195], v[136:139], v[40:43], v[192:195]\n\t"
      "v_mfma_f32_16x16x32_bf16 v[196:199], v[136:139], v[44:47], v[196:199]\n\t"
      "v_mfma_f32_16x16x32_bf16 v[184:187], v[140:143], v[48:51], v[184:187]\n\t"
      "v_mfma_f32_16x16x32_bf16 v[188:191], v[140:143], v[52:55], v[188:191]\n\t"
      "v_mfma_f32_16x16x32_bf16 v[192:195], v[140:143], v[56:59], v[192:195]\n\t"
      "v_mfma_f32_16x16x32_bf16 v[196:199], v[140:143], v[60:63], v[196:199]\n\t"
      "v_mfma_f32_16x16x32_bf16 v[184:187], v[144:147], v[64:67], v[184:187]\n\t"
      "v_mfma_f32_16x16x32_bf16 v[188:191], v[144:147], v[68:71], v[188:191]\n\t"
      "v_mfma_f32_16x16x32_bf16 v[192:195], v[144:147], v[72:75], v[192:195]\n\t"
      "v_mfma_f32_16x16x32_bf16 v[196:199], v[144:147], v[76:79], v[196:199]\n\t"
      "v_mfma_f32_16x16x32_bf16 v[184:187], v[148:151], v[80:83], v[184:187]\n\t"
      "v_mfma_f32_16x16x32_bf16 v[188:191], v[148:151], v[84:87], v[188:191]\n\t"
      "v_mfma_f32_16x16x32_bf16 v[192:195], v[148:151], v[88:91], v[192:195]\n\t"
      "v_mfma_f32_16x16x32_bf16 v[196:199], v[148:151], v[92:95], v[196:199]\n\t"
      "v_mfma_f32_16x16x32_bf16 v[184:187], v[152:155], v[96:99], v[184:187]\n\t"
      "v_mfma_f32_16x16x32_bf16 v[188:191], v[152:155], v[100:103], v[188:191]\n\t"
      "v_mfma_f32_16x16x32_bf16 v[192:195], v[152:155], v[104:107], v[192:195]\n\t"
      "v_mfma_f32_16x16x32_bf16 v[196:199], v[152:155], v[108:111], v[196:199]\n\t"
      "global_load_dwordx4 v[128:131], v224, s[62:63] sc1\n\t"
      "global_load_dwordx4 v[132:135], v225, s[62:63] sc1\n\t"
      "global_load_dwordx4 v[136:139], v226, s[62:63] sc1\n\t"
      "global_load_dwordx4 v[140:143], v227, s[62:63] sc1\n\t"
      "global_load_dwordx4 v[144:147], v228, s[62:63] sc1\n\t"
      "global_load_dwordx4 v[148:151], v229, s[62:63] sc1\n\t"
      "global_load_dwordx4 v[152:155], v230, s[62:63] sc1\n\t"
      "s_waitcnt vmcnt(0)\n\t"
      "s_nop 7\n\t"
      "s_branch NP4B_%=\n\t"
      "FP36R_%=:\n\t"
      "s_mov_b32 s51, 0\n\t"
      "FP36L_%=:\n\t"
      "s_add_u32 s51, s51, 1\n\t"
      "s_cmp_lt_u32 s51, 262144\n\t"
      "s_cbranch_scc0 FP36X_%=\n\t"
      "s_waitcnt vmcnt(0)\n\t"
      "s_sleep 4\n\t"
      "global_load_dwordx4 v[132:135], v225, s[62:63] sc1\n\t"
      "global_load_dwordx4 v[136:139], v226, s[62:63] sc1\n\t"
      "global_load_dwordx4 v[140:143], v227, s[62:63] sc1\n\t"
      "global_load_dwordx4 v[144:147], v228, s[62:63] sc1\n\t"
      "global_load_dwordx4 v[148:151], v229, s[62:63] sc1\n\t"
      "global_load_dwordx4 v[152:155], v230, s[62:63] sc1\n\t"
      "s_waitcnt vmcnt(5)\n\t"
      "v_max3_u32 v234, v132, v133, v134\n\t"
      "v_max_u32_e32 v234, v234, v135\n\t"
      "v_cmp_eq_u32_e32 vcc, -1, v234\n\t"
      "s_cbranch_vccnz FP36L_%=\n\t"
      "FP36X_%=:\n\t"
      "s_waitcnt vmcnt(0)\n\t"
      "s_branch FP36B_%=\n\t"
      "FP37R_%=:\n\t"
      "s_mov_b32 s51, 0\n\t"
      "FP37L_%=:\n\t"
      "s_add_u32 s51, s51, 1\n\t"
      "s_cmp_lt_u32 s51, 262144\n\t"
      "s_cbranch_scc0 FP37X_%=\n\t"
      "s_waitcnt vmcnt(0)\n\t"
      "s_sleep 4\n\t"
      "global_load_dwordx4 v[136:139], v226, s[62:63] sc1\n\t"
      "global_load_dwordx4 v[140:143], v227, s[62:63] sc1\n\t"
      "global_load_dwordx4 v[144:147], v228, s[62:63] sc1\n\t"
      "global_load_dwordx4 v[148:151], v229, s[62:63] sc1\n\t"
      "global_load_dwordx4 v[152:155], v230, s[62:63] sc1\n\t"
      "global_load_dwordx4 v[128:131], v224, s[64:65] sc1\n\t"
      "s_waitcnt vmcnt(5)\n\t"
      "v_max3_u32 v234, v136, v137, v138\n\t"
      "v_max_u32_e32 v234, v234, v139\n\t"
      "v_cmp_eq_u32_e32 vcc, -1, v234\n\t"
      "s_cbranch_vccnz FP37L_%=\n\t"
      "FP37X_%=:\n\t"
      "s_waitcnt vmcnt(0)\n\t"
      "s_branch FP37B_%=\n\t"
      "FP38R_%=:\n\t"
      "s_mov_b32 s51, 0\n\t"
      "FP38L_%=:\n\t"
      "s_add_u32 s51, s51, 1\n\t"
      "s_cmp_lt_u32 s51, 262144\n\t"
      "s_cbranch_scc0 FP38X_%=\n\t"
      "s_waitcnt vmcnt(0)\n\t"
      "s_sleep 4\n\t"
      "global_load_dwordx4 v[140:143], v227, s[62:63] sc1\n\t"
      "global_load_dwordx4 v[144:147], v228, s[62:63] sc1\n\t"
      "global_load_dwordx4 v[148:151], v229, s[62:63] sc1\n\t"
      "global_load_dwordx4 v[152:155], v230, s[62:63] sc1\n\t"
      "global_load_dwordx4 v[128:131], v224, s[64:65] sc1\n\t"
      "global_load_dwordx4 v[132:135], v225, s[64:65] sc1\n\t"
      "s_waitcnt vmcnt(5)\n\t"
      "v_max3_u32 v234, v140, v141, v142\n\t"
      "v_max_u32_e32 v234, v234, v143\n\t"
      "v_cmp_eq_u32_e32 vcc, -1, v234\n\t"
      "s_cbranch_vccnz FP38L_%=\n\t"
      "FP38X_%=:\n\t"
      "s_waitcnt vmcnt(0)\n\t"
      "s_branch FP38B_%=\n\t"
      "FP39R_%=:\n\t"
      "s_mov_b32 s51, 0\n\t"
      "FP39L_%=:\n\t"
      "s_add_u32 s51, s51, 1\n\t"
      "s_cmp_lt_u32 s51, 262144\n\t"
      "s_cbranch_scc0 FP39X_%=\n\t"
      "s_waitcnt vmcnt(0)\n\t"
      "s_sleep 4\n\t"
      "global_load_dwordx4 v[144:147], v228, s[62:63] sc1\n\t"
      "global_load_dwordx4 v[148:151], v229, s[62:63] sc1\n\t"
      "global_load_dwordx4 v[152:155], v230, s[62:63] sc1\n\t"
      "global_load_dwordx4 v[128:131], v224, s[64:65] sc1\n\t"
      "global_load_dwordx4 v[132:135], v225, s[64:65] sc1\n\t"
      "global_load_dwordx4 v[136:139], v226, s[64:65] sc1\n\t"
      "s_waitcnt vmcnt(5)\n\t"
      "v_max3_u32 v234, v144, v145, v146\n\t"
      "v_max_u32_e32 v234, v234, v147\n\t"
      "v_cmp_eq_u32_e32 vcc, -1, v234\n\t"
      "s_cbranch_vccnz FP39L_%=\n\t"
      "FP39X_%=:\n\t"
      "s_waitcnt vmcnt(0)\n\t"
      "s_branch FP39B_%=\n\t"
      "FP40R_%=:\n\t"
      "s_mov_b32 s51, 0\n\t"
      "FP40L_%=:\n\t"
      "s_add_u32 s51, s51, 1\n\t"
      "s_cmp_lt_u32 s51, 262144\n\t"
      "s_cbranch_scc0 FP40X_%=\n\t"
      "s_waitcnt vmcnt(0)\n\t"
      "s_sleep 4\n\t"
      "global_load_dwordx4 v[148:151], v229, s[62:63] sc1\n\t"
      "global_load_dwordx4 v[152:155], v230, s[62:63] sc1\n\t"
      "global_load_dwordx4 v[128:131], v224, s[64:65] sc1\n\t"
      "global_load_dwordx4 v[132:135], v225, s[64:65] sc1\n\t"
      "global_load_dwordx4 v[136:139], v226, s[64:65] sc1\n\t"
      "global_load_dwordx4 v[140:143], v227, s[64:65] sc1\n\t"
      "s_waitcnt vmcnt(5)\n\t"
      "v_max3_u32 v234, v148, v149, v150\n\t"
      "v_max_u32_e32 v234, v234, v151\n\t"
      "v_cmp_eq_u32_e32 vcc, -1, v234\n\t"
      "s_cbranch_vccnz FP40L_%=\n\t"
      "FP40X_%=:\n\t"
      "s_branch FP40B_%=\n\t"
      "FP41R_%=:\n\t"
      "s_mov_b32 s51, 0\n\t"
      "FP41L_%=:\n\t"
      "s_add_u32 s51, s51, 1\n\t"
      "s_cmp_lt_u32 s51, 262144\n\t"
      "s_cbranch_scc0 FP41X_%=\n\t"
      "s_waitcnt vmcnt(0)\n\t"
      "s_sleep 4\n\t"
      "global_load_dwordx4 v[152:155], v230, s[62:63] sc1\n\t"
      "global_load_dwordx4 v[128:131], v224, s[64:65] sc1\n\t"
      "global_load_dwordx4 v[132:135], v225, s[64:65] sc1\n\t"
      "global_load_dwordx4 v[136:139], v226, s[64:65] sc1\n\t"
      "global_load_dwordx4 v[140:143], v227, s[64:65] sc1\n\t"
      "global_load_dwordx4 v[144:147], v228, s[64:65] sc1\n\t"
      "s_waitcnt vmcnt(5)\n\t"
      "v_max3_u32 v234, v152, v153, v154\n\t"
      "v_max_u32_e32 v234, v234, v155\n\t"
      "v_cmp_eq_u32_e32 vcc, -1, v234\n\t"
      "s_cbranch_vccnz FP41L_%=\n\t"
      "FP41X_%=:\n\t"
      "s_waitcnt vmcnt(0)\n\t"
      "s_branch FP41B_%=\n\t"
      "FP42R_%=:\n\t"
      "s_mov_b32 s51, 0\n\t"
      "FP42L_%=:\n\t"
      "s_add_u32 s51, s51, 1\n\t"
      "s_cmp_lt_u32 s51, 262144\n\t"
      "s_cbranch_scc0 FP42X_%=\n\t"
      "s_sleep 1\n\t"
      "s_mov_b64 exec, 15\n\t"
      "v_mov_b64_e32 v[232:233], 0\n\t"
      "global_atomic_or_x2 v[232:233], v247, v[232:233], s[12:13] sc0 sc1\n\t"
      "s_waitcnt vmcnt(0)\n\t"
      "v_subrev_u32_e32 v232, s14, v232\n\t"
      "v_cmp_ne_u32_e64 s[48:49], s15, v233\n\t"
      "v_subrev_u32_e32 v232, 6, v232\n\t"
      "v_cmp_lt_u32_e32 vcc, 7, v232\n\t"
      "s_mov_b64 exec, -1\n\t"
      "s_or_b64 vcc, vcc, s[48:49]\n\t"
      "s_cbranch_vccnz FP42L_%=\n\t"
      "FP42X_%=:\n\t"
      "FP42DR_%=:\n\t"
      "s_mov_b32 s51, 0\n\t"
      "FP42DL_%=:\n\t"
      "s_add_u32 s51, s51, 1\n\t"
      "s_cmp_lt_u32 s51, 262144\n\t"
      "s_cbranch_scc0 FP42DX_%=\n\t"
      "s_waitcnt vmcnt(0)\n\t"
      "s_sleep 1\n\t"
      "global_load_dwordx4 v[128:131], v224, s[64:65] sc1\n\t"
      "global_load_dwordx4 v[132:135], v225, s[64:65] sc1\n\t"
      "global_load_dwordx4 v[136:139], v226, s[64:65] sc1\n\t"
      "global_load_dwordx4 v[140:143], v227, s[64:65] sc1\n\t"
      "global_load_dwordx4 v[144:147], v228, s[64:65] sc1\n\t"
      "global_load_dwordx4 v[148:151], v229, s[64:65] sc1\n\t"
      "s_waitcnt vmcnt(5)\n\t"
      "v_max3_u32 v234, v128, v129, v130\n\t"
      "v_max_u32_e32 v234, v234, v131\n\t"
      "v_cmp_eq_u32_e32 vcc, -1, v234\n\t"
      "s_cbranch_vccnz FP42DL_%=\n\t"
      "FP42DX_%=:\n\t"
      "s_waitcnt vmcnt(0)\n\t"
      "s_branch FP42B_%=\n\t"
      "NP5R_%=:\n\t"
      "s_mov_b32 s51, 0\n\t"
      "NP5L_%=:\n\t"
      "s_add_u32 s51, s51, 1\n\t"
      "s_cmp_lt_u32 s51, 262144\n\t"
      "s_cbranch_scc0 NP5X_%=\n\t"
      "s_waitcnt vmcnt(0)\n\t"
      "s_sleep 1\n\t"
      "global_load_dwordx4 v[128:131], v224, s[62:63] sc1\n\t"
      "global_load_dwordx4 v[132:135], v225, s[62:63] sc1\n\t"
      "global_load_dwordx4 v[136:139], v226, s[62:63] sc1\n\t"
      "global_load_dwordx4 v[140:143], v227, s[62:63] sc1\n\t"
      "global_load_dwordx4 v[144:147], v228, s[62:63] sc1\n\t"
      "global_load_dwordx4 v[148:151], v229, s[62:63] sc1\n\t"
      "global_load_dwordx4 v[152:155], v230, s[62:63] sc1\n\t"
      "s_waitcnt vmcnt(0)\n\t"
      "v_max3_u32 v234, v128, v129, v130\n\t"
      "v_max3_u32 v234, v234, v131, v132\n\t"
      "v_max3_u32 v234, v234, v133, v134\n\t"
      "v_max3_u32 v234, v234, v135, v136\n\t"
      "v_max3_u32 v234, v234, v137, v138\n\t"
      "v_max3_u32 v234, v234, v139, v140\n\t"
      "v_max3_u32 v234, v234, v141, v142\n\t"
      "v_max3_u32 v234, v234, v143, v144\n\t"
      "v_max3_u32 v234, v234, v145, v146\n\t"
      "v_max3_u32 v234, v234, v147, v148\n\t"
      "v_max3_u32 v234, v234, v149, v150\n\t"
      "v_max3_u32 v234, v234, v151, v152\n\t"
      "v_max3_u32 v234, v234, v153, v154\n\t"
      "v_max_u32_e32 v234, v234, v155\n\t"
      "v_cmp_eq_u32_e32 vcc, -1, v234\n\t"
      "s_cbranch_vccnz NP5L_%=\n\t"
      "NP5X_%=:\n\t"
      "v_mfma_f32_16x16x32_bf16 v[200:203], v[128:131], v[0:3], v[176:179]\n\t"
      "v_mfma_f32_16x16x32_bf16 v[204:207], v[128:131], v[4:7], 0\n\t"
      "v_mfma_f32_16x16x32_bf16 v[208:211], v[128:131], v[8:11], 0\n\t"
      "v_mfma_f32_16x16x32_bf16 v[212:215], v[128:131], v[12:15], 0\n\t"
      "v_mfma_f32_16x16x32_bf16 v[200:203], v[132:135], v[16:19], v[200:203]\n\t"
      "v_mfma_f32_16x16x32_bf16 v[204:207], v[132:135], v[20:23], v[204:207]\n\t"
      "v_mfma_f32_16x16x32_bf16 v[208:211], v[132:135], v[24:27], v[208:211]\n\t"
      "v_mfma_f32_16x16x32_bf16 v[212:215], v[132:135], v[28:31], v[212:215]\n\t"
      "v_mfma_f32_16x16x32_bf16 v[200:203], v[136:139], v[32:35], v[200:203]\n\t"
      "v_mfma_f32_16x16x32_bf16 v[204:207], v[136:139], v[36:39], v[204:207]\n\t"
      "v_mfma_f32_16x16x32_bf16 v[208:211], v[136:139], v[40:43], v[208:211]\n\t"
      "v_mfma_f32_16x16x32_bf16 v[212:215], v[136:139], v[44:47], v[212:215]\n\t"
      "v_mfma_f32_16x16x32_bf16 v[200:203], v[140:143], v[48:51], v[200:203]\n\t"
      "v_mfma_f32_16x16x32_bf16 v[204:207], v[140:143], v[52:55], v[204:207]\n\t"
      "v_mfma_f32_16x16x32_bf16 v[208:211], v[140:143], v[56:59], v[208:211]\n\t"
      "v_mfma_f32_16x16x32_bf16 v[212:215], v[140:143], v[60:63], v[212:215]\n\t"
      "v_mfma_f32_16x16x32_bf16 v[200:203], v[144:147], v[64:67], v[200:203]\n\t"
      "v_mfma_f32_16x16x32_bf16 v[204:207], v[144:147], v[68:71], v[204:207]\n\t"
      "v_mfma_f32_16x16x32_bf16 v[208:211], v[144:147], v[72:75], v[208:211]\n\t"
      "v_mfma_f32_16x16x32_bf16 v[212:215], v[144:147], v[76:79], v[212:215]\n\t"
      "v_mfma_f32_16x16x32_bf16 v[200:203], v[148:151], v[80:83], v[200:203]\n\t"
      "v_mfma_f32_16x16x32_bf16 v[204:207], v[148:151], v[84:87], v[204:207]\n\t"
      "v_mfma_f32_16x16x32_bf16 v[208:211], v[148:151], v[88:91], v[208:211]\n\t"
      "v_mfma_f32_16x16x32_bf16 v[212:215], v[148:151], v[92:95], v[212:215]\n\t"
      "v_mfma_f32_16x16x32_bf16 v[200:203], v[152:155], v[96:99], v[200:203]\n\t"
      "v_mfma_f32_16x16x32_bf16 v[204:207], v[152:155], v[100:103], v[204:207]\n\t"
      "v_mfma_f32_16x16x32_bf16 v[208:211], v[152:155], v[104:107], v[208:211]\n\t"
      "v_mfma_f32_16x16x32_bf16 v[212:215], v[152:155], v[108:111], v[212:215]\n\t"
      "global_load_dwordx4 v[128:131], v224, s[64:65] sc1\n\t"
      "global_load_dwordx4 v[132:135], v225, s[64:65] sc1\n\t"
      "global_load_dwordx4 v[136:139], v226, s[64:65] sc1\n\t"
      "global_load_dwordx4 v[140:143], v227, s[64:65] sc1\n\t"
      "global_load_dwordx4 v[144:147], v228, s[64:65] sc1\n\t"
      "global_load_dwordx4 v[148:151], v229, s[64:65] sc1\n\t"
      "global_load_dwordx4 v[152:155], v230, s[64:65] sc1\n\t"
      "s_waitcnt vmcnt(0)\n\t"
      "s_nop 7\n\t"
      "s_branch NP5B_%=\n\t"
      "FP43R_%=:\n\t"
      "s_mov_b32 s51, 0\n\t"
      "FP43L_%=:\n\t"
      "s_add_u32 s51, s51, 1\n\t"
      "s_cmp_lt_u32 s51, 262144\n\t"
      "s_cbranch_scc0 FP43X_%=\n\t"
      "s_sleep 1\n\t"
      "s_mov_b64 exec, 15\n\t"
      "v_mov_b64_e32 v[232:233], 0\n\t"
      "global_atomic_or_x2 v[232:233], v247, v[232:233], s[12:13] sc0 sc1\n\t"
      "s_waitcnt vmcnt(0)\n\t"
      "v_subrev_u32_e32 v232, s14, v232\n\t"
      "v_cmp_ne_u32_e64 s[48:49], s15, v233\n\t"
      "v_subrev_u32_e32 v232, 6, v232\n\t"
      "v_cmp_lt_u32_e32 vcc, 7, v232\n\t"
      "s_mov_b64 exec, -1\n\t"
      "s_or_b64 vcc, vcc, s[48:49]\n\t"
      "s_cbranch_vccnz FP43L_%=\n\t"
      "FP43X_%=:\n\t"
      "FP43DR_%=:\n\t"
      "s_mov_b32 s51, 0\n\t"
      "FP43DL_%=:\n\t"
      "s_add_u32 s51, s51, 1\n\t"
      "s_cmp_lt_u32 s51, 262144\n\t"
      "s_cbranch_scc0 FP43DX_%=\n\t"
      "s_waitcnt vmcnt(0)\n\t"
      "s_sleep 1\n\t"
      "global_load_dwordx4 v[132:135], v225, s[64:65] sc1\n\t"
      "global_load_dwordx4 v[136:139], v226, s[64:65] sc1\n\t"
      "global_load_dwordx4 v[140:143], v227, s[64:65] sc1\n\t"
      "global_load_dwordx4 v[144:147], v228, s[64:65] sc1\n\t"
      "global_load_dwordx4 v[148:151], v229, s[64:65] sc1\n\t"
      "global_load_dwordx4 v[152:155], v230, s[64:65] sc1\n\t"
      "s_waitcnt vmcnt(5)\n\t"
      "v_max3_u32 v234, v132, v133, v134\n\t"
      "v_max_u32_e32 v234, v234, v135\n\t"
      "v_cmp_eq_u32_e32 vcc, -1, v234\n\t"
      "s_cbranch_vccnz FP43DL_%=\n\t"
      "FP43DX_%=:\n\t"
      "s_waitcnt vmcnt(0)\n\t"
      "s_branch FP43B_%=\n\t"
      "FP44R_%=:\n\t"
      "s_mov_b32 s51, 0\n\t"
      "FP44L_%=:\n\t"
      "s_add_u32 s51, s51, 1\n\t"
      "s_cmp_lt_u32 s51, 262144\n\t"
      "s_cbranch_scc0 FP44X_%=\n\t"
      "s_sleep 1\n\t"
      "s_mov_b64 exec, 15\n\t"
      "v_mov_b64_e32 v[232:233], 0\n\t"
      "global_atomic_or_x2 v[232:233], v247, v[232:233], s[12:13] sc0 sc1\n\t"
      "s_waitcnt vmcnt(0)\n\t"
      "v_subrev_u32_e32 v232, s14, v232\n\t"
      "v_cmp_ne_u32_e64 s[48:49], s15, v233\n\t"
      "v_subrev_u32_e32 v232, 6, v232\n\t"
      "v_cmp_lt_u32_e32 vcc, 7, v232\n\t"
      "s_mov_b64 exec, -1\n\t"
      "s_or_b64 vcc, vcc, s[48:49]\n\t"
      "s_cbranch_vccnz FP44L_%=\n\t"
      "FP44X_%=:\n\t"
      "FP44DR_%=:\n\t"
      "s_mov_b32 s51, 0\n\t"
      "FP44DL_%=:\n\t"
      "s_add_u32 s51, s51, 1\n\t"
      "s_cmp_lt_u32 s51, 262144\n\t"
      "s_cbranch_scc0 FP44DX_%=\n\t"
      "s_waitcnt vmcnt(0)\n\t"
      "s_sleep 1\n\t"
      "global_load_dwordx4 v[136:139], v226, s[64:65] sc1\n\t"
      "global_load_dwordx4 v[140:143], v227, s[64:65] sc1\n\t"
      "global_load_dwordx4 v[144:147], v228, s[64:65] sc1\n\t"
      "global_load_dwordx4 v[148:151], v229, s[64:65] sc1\n\t"
      "global_load_dwordx4 v[152:155], v230, s[64:65] sc1\n\t"
      "s_waitcnt vmcnt(4)\n\t"
      "v_max3_u32 v234, v136, v137, v138\n\t"
      "v_max_u32_e32 v234, v234, v139\n\t"
      "v_cmp_eq_u32_e32 vcc, -1, v234\n\t"
      "s_cbranch_vccnz FP44DL_%=\n\t"
      "FP44DX_%=:\n\t"
      "s_waitcnt vmcnt(0)\n\t"
      "s_branch FP44B_%=\n\t"
      "FP45R_%=:\n\t"
      "s_mov_b32 s51, 0\n\t"
      "FP45L_%=:\n\t"
      "s_add_u32 s51, s51, 1\n\t"
      "s_cmp_lt_u32 s51, 262144\n\t"
      "s_cbranch_scc0 FP45X_%=\n\t"
      "s_sleep 1\n\t"
      "s_mov_b64 exec, 15\n\t"
      "v_mov_b64_e32 v[232:233], 0\n\t"
      "global_atomic_or_x2 v[232:233], v247, v[232:233], s[12:13] sc0 sc1\n\t"
      "s_waitcnt vmcnt(0)\n\t"
      "v_subrev_u32_e32 v232, s14, v232\n\t"
      "v_cmp_ne_u32_e64 s[48:49], s15, v233\n\t"
      "v_subrev_u32_e32 v232, 6, v232\n\t"
      "v_cmp_lt_u32_e32 vcc, 7, v232\n\t"
      "s_mov_b64 exec, -1\n\t"
      "s_or_b64 vcc, vcc, s[48:49]\n\t"
      "s_cbranch_vccnz FP45L_%=\n\t"
      "FP45X_%=:\n\t"
      "FP45DR_%=:\n\t"
      "s_mov_b32 s51, 0\n\t"
      "FP45DL_%=:\n\t"
      "s_add_u32 s51, s51, 1\n\t"
      "s_cmp_lt_u32 s51, 262144\n\t"
      "s_cbranch_scc0 FP45DX_%=\n\t"
      "s_waitcnt vmcnt(0)\n\t"
      "s_sleep 1\n\t"
      "global_load_dwordx4 v[140:143], v227, s[64:65] sc1\n\t"
      "global_load_dwordx4 v[144:147], v228, s[64:65] sc1\n\t"
      "global_load_dwordx4 v[148:151], v229, s[64:65] sc1\n\t"
      "global_load_dwordx4 v[152:155], v230, s[64:65] sc1\n\t"
      "s_waitcnt vmcnt(3)\n\t"
      "v_max3_u32 v234, v140, v141, v142\n\t"
      "v_max_u32_e32 v234, v234, v143\n\t"
      "v_cmp_eq_u32_e32 vcc, -1, v234\n\t"
      "s_cbranch_vccnz FP45DL_%=\n\t"
      "FP45DX_%=:\n\t"
      "s_waitcnt vmcnt(0)\n\t"
      "s_branch FP45B_%=\n\t"
      "FP46R_%=:\n\t"
      "s_mov_b32 s51, 0\n\t"
      "FP46L_%=:\n\t"
      "s_add_u32 s51, s51, 1\n\t"
      "s_cmp_lt_u32 s51, 262144\n\t"
      "s_cbranch_scc0 FP46X_%=\n\t"
      "s_sleep 1\n\t"
      "s_mov_b64 exec, 15\n\t"
      "v_mov_b64_e32 v[232:233], 0\n\t"
      "global_atomic_or_x2 v[232:233], v247, v[232:233], s[12:13] sc0 sc1\n\t"
      "s_waitcnt vmcnt(0)\n\t"
      "v_subrev_u32_e32 v232, s14, v232\n\t"
      "v_cmp_ne_u32_e64 s[48:49], s15, v233\n\t"
      "v_subrev_u32_e32 v232, 6, v232\n\t"
      "v_cmp_lt_u32_e32 vcc, 7, v232\n\t"
      "s_mov_b64 exec, -1\n\t"
      "s_or_b64 vcc, vcc, s[48:49]\n\t"
      "s_cbranch_vccnz FP46L_%=\n\t"
      "FP46X_%=:\n\t"
      "FP46DR_%=:\n\t"
      "s_mov_b32 s51, 0\n\t"
      "FP46DL_%=:\n\t"
      "s_add_u32 s51, s51, 1\n\t"
      "s_cmp_lt_u32 s51, 262144\n\t"
      "s_cbranch_scc0 FP46DX_%=\n\t"
      "s_waitcnt vmcnt(0)\n\t"
      "s_sleep 1\n\t"
      "global_load_dwordx4 v[144:147], v228, s[64:65] sc1\n\t"
      "global_load_dwordx4 v[148:151], v229, s[64:65] sc1\n\t"
      "global_load_dwordx4 v[152:155], v230, s[64:65] sc1\n\t"
      "s_waitcnt vmcnt(2)\n\t"
      "v_max3_u32 v234, v144, v145, v146\n\t"
      "v_max_u32_e32 v234, v234, v147\n\t"
      "v_cmp_eq_u32_e32 vcc, -1, v234\n\t"
      "s_cbranch_vccnz FP46DL_%=\n\t"
      "FP46DX_%=:\n\t"
      "s_waitcnt vmcnt(0)\n\t"
      "s_branch FP46B_%=\n\t"
      "FP47R_%=:\n\t"
      "s_mov_b32 s51, 0\n\t"
      "FP47L_%=:\n\t"
      "s_add_u32 s51, s51, 1\n\t"
      "s_cmp_lt_u32 s51, 262144\n\t"
      "s_cbranch_scc0 FP47X_%=\n\t"
      "s_sleep 1\n\t"
      "s_mov_b64 exec, 15\n\t"
      "v_mov_b64_e32 v[232:233], 0\n\t"
      "global_atomic_or_x2 v[232:233], v247, v[232:233], s[12:13] sc0 sc1\n\t"
      "s_waitcnt vmcnt(0)\n\t"
      "v_subrev_u32_e32 v232, s14, v232\n\t"
      "v_cmp_ne_u32_e64 s[48:49], s15, v233\n\t"
      "v_subrev_u32_e32 v232, 6, v232\n\t"
      "v_cmp_lt_u32_e32 vcc, 7, v232\n\t"
      "s_mov_b64 exec, -1\n\t"
      "s_or_b64 vcc, vcc, s[48:49]\n\t"
      "s_cbranch_vccnz FP47L_%=\n\t"
      "FP47X_%=:\n\t"
      "FP47DR_%=:\n\t"
      "s_mov_b32 s51, 0\n\t"
      "FP47DL_%=:\n\t"
      "s_add_u32 s51, s51, 1\n\t"
      "s_cmp_lt_u32 s51, 262144\n\t"
      "s_cbranch_scc0 FP47DX_%=\n\t"
      "s_waitcnt vmcnt(0)\n\t"
      "s_sleep 1\n\t"
      "global_load_dwordx4 v[148:151], v229, s[64:65] sc1\n\t"
      "global_load_dwordx4 v[152:155], v230, s[64:65] sc1\n\t"
      "s_waitcnt vmcnt(1)\n\t"
      "v_max3_u32 v234, v148, v149, v150\n\t"
      "v_max_u32_e32 v234, v234, v151\n\t"
      "v_cmp_eq_u32_e32 vcc, -1, v234\n\t"
      "s_cbranch_vccnz FP47DL_%=\n\t"
      "FP47DX_%=:\n\t"
      "s_branch FP47B_%=\n\t"
      "FP48R_%=:\n\t"
      "s_mov_b32 s51, 0\n\t"
      "FP48L_%=:\n\t"
      "s_add_u32 s51, s51, 1\n\t"
      "s_cmp_lt_u32 s51, 262144\n\t"
      "s_cbranch_scc0 FP48X_%=\n\t"
      "s_sleep 1\n\t"
      "s_mov_b64 exec, 15\n\t"
      "v_mov_b64_e32 v[232:233], 0\n\t"
      "global_atomic_or_x2 v[232:233], v247, v[232:233], s[12:13] sc0 sc1\n\t"
      "s_waitcnt vmcnt(0)\n\t"
      "v_subrev_u32_e32 v232, s14, v232\n\t"
      "v_cmp_ne_u32_e64 s[48:49], s15, v233\n\t"
      "v_subrev_u32_e32 v232, 6, v232\n\t"
      "v_cmp_lt_u32_e32 vcc, 7, v232\n\t"
      "s_mov_b64 exec, -1\n\t"
      "s_or_b64 vcc, vcc, s[48:49]\n\t"
      "s_cbranch_vccnz FP48L_%=\n\t"
      "FP48X_%=:\n\t"
      "FP48DR_%=:\n\t"
      "s_mov_b32 s51, 0\n\t"
      "FP48DL_%=:\n\t"
      "s_add_u32 s51, s51, 1\n\t"
      "s_cmp_lt_u32 s51, 262144\n\t"
      "s_cbranch_scc0 FP48DX_%=\n\t"
      "s_waitcnt vmcnt(0)\n\t"
      "s_sleep 1\n\t"
      "global_load_dwordx4 v[152:155], v230, s[64:65] sc1\n\t"
      "s_waitcnt vmcnt(0)\n\t"
      "v_max3_u32 v234, v152, v153, v154\n\t"
      "v_max_u32_e32 v234, v234, v155\n\t"
      "v_cmp_eq_u32_e32 vcc, -1, v234\n\t"
      "s_cbranch_vccnz FP48DL_%=\n\t"
      "FP48DX_%=:\n\t"
      "s_waitcnt vmcnt(0)\n\t"
      "s_branch FP48B_%=\n\t"
      "NP6R_%=:\n\t"
      "s_mov_b32 s51, 0\n\t"
      "NP6L_%=:\n\t"
      "s_add_u32 s51, s51, 1\n\t"
      "s_cmp_lt_u32 s51, 262144\n\t"
      "s_cbranch_scc0 NP6X_%=\n\t"
      "s_waitcnt vmcnt(0)\n\t"
      "s_sleep 1\n\t"
      "global_load_dwordx4 v[128:131], v224, s[64:65] sc1\n\t"
      "global_load_dwordx4 v[132:135], v225, s[64:65] sc1\n\t"
      "global_load_dwordx4 v[136:139], v226, s[64:65] sc1\n\t"
      "global_load_dwordx4 v[140:143], v227, s[64:65] sc1\n\t"
      "global_load_dwordx4 v[144:147], v228, s[64:65] sc1\n\t"
      "global_load_dwordx4 v[148:151], v229, s[64:65] sc1\n\t"
      "global_load_dwordx4 v[152:155], v230, s[64:65] sc1\n\t"
      "s_waitcnt vmcnt(0)\n\t"
      "v_max3_u32 v234, v128, v129, v130\n\t"
      "v_max3_u32 v234, v234, v131, v132\n\t"
      "v_max3_u32 v234, v234, v133, v134\n\t"
      "v_max3_u32 v234, v234, v135, v136\n\t"
      "v_max3_u32 v234, v234, v137, v138\n\t"
      "v_max3_u32 v234, v234, v139, v140\n\t"
      "v_max3_u32 v234, v234, v141, v142\n\t"
      "v_max3_u32 v234, v234, v143, v144\n\t"
      "v_max3_u32 v234, v234, v145, v146\n\t"
      "v_max3_u32 v234, v234, v147, v148\n\t"
      "v_max3_u32 v234, v234, v149, v150\n\t"
      "v_max3_u32 v234, v234, v151, v152\n\t"
      "v_max3_u32 v234, v234, v153, v154\n\t"
      "v_max_u32_e32 v234, v234, v155\n\t"
      "v_cmp_eq_u32_e32 vcc, -1, v234\n\t"
      "s_cbranch_vccnz NP6L_%=\n\t"
      "NP6X_%=:\n\t"
      "v_mfma_f32_16x16x32_bf16 v[184:187], v[128:131], v[0:3], v[180:183]\n\t"
      "v_mfma_f32_16x16x32_bf16 v[188:191], v[128:131], v[4:7], 0\n\t"
      "v_mfma_f32_16x16x32_bf16 v[192:195], v[128:131], v[8:11], 0\n\t"
      "v_mfma_f32_16x16x32_bf16 v[196:199], v[128:131], v[12:15], 0\n\t"
      "v_mfma_f32_16x16x32_bf16 v[184:187], v[132:135], v[16:19], v[184:187]\n\t"
      "v_mfma_f32_16x16x32_bf16 v[188:191], v[132:135], v[20:23], v[188:191]\n\t"
      "v_mfma_f32_16x16x32_bf16 v[192:195], v[132:135], v[24:27], v[192:195]\n\t"
      "v_mfma_f32_16x16x32_bf16 v[196:199], v[132:135], v[28:31], v[196:199]\n\t"
      "v_mfma_f32_16x16x32_bf16 v[184:187], v[136:139], v[32:35], v[184:187]\n\t"
      "v_mfma_f32_16x16x32_bf16 v[188:191], v[136:139], v[36:39], v[188:191]\n\t"
      "v_mfma_f32_16x16x32_bf16 v[192:195], v[136:139], v[40:43], v[192:195]\n\t"
      "v_mfma_f32_16x16x32_bf16 v[196:199], v[136:139], v[44:47], v[196:199]\n\t"
      "v_mfma_f32_16x16x32_bf16 v[184:187], v[140:143], v[48:51], v[184:187]\n\t"
      "v_mfma_f32_16x16x32_bf16 v[188:191], v[140:143], v[52:55], v[188:191]\n\t"
      "v_mfma_f32_16x16x32_bf16 v[192:195], v[140:143], v[56:59], v[192:195]\n\t"
      "v_mfma_f32_16x16x32_bf16 v[196:199], v[140:143], v[60:63], v[196:199]\n\t"
      "v_mfma_f32_16x16x32_bf16 v[184:187], v[144:147], v[64:67], v[184:187]\n\t"
      "v_mfma_f32_16x16x32_bf16 v[188:191], v[144:147], v[68:71], v[188:191]\n\t"
      "v_mfma_f32_16x16x32_bf16 v[192:195], v[144:147], v[72:75], v[192:195]\n\t"
      "v_mfma_f32_16x16x32_bf16 v[196:199], v[144:147], v[76:79], v[196:199]\n\t"
      "v_mfma_f32_16x16x32_bf16 v[184:187], v[148:151], v[80:83], v[184:187]\n\t"
      "v_mfma_f32_16x16x32_bf16 v[188:191], v[148:151], v[84:87], v[188:191]\n\t"
      "v_mfma_f32_16x16x32_bf16 v[192:195], v[148:151], v[88:91], v[192:195]\n\t"
      "v_mfma_f32_16x16x32_bf16 v[196:199], v[148:151], v[92:95], v[196:199]\n\t"
      "v_mfma_f32_16x16x32_bf16 v[184:187], v[152:155], v[96:99], v[184:187]\n\t"
      "v_mfma_f32_16x16x32_bf16 v[188:191], v[152:155], v[100:103], v[188:191]\n\t"
      "v_mfma_f32_16x16x32_bf16 v[192:195], v[152:155], v[104:107], v[192:195]\n\t"
      "v_mfma_f32_16x16x32_bf16 v[196:199], v[152:155], v[108:111], v[196:199]\n\t"
      "s_waitcnt vmcnt(0)\n\t"
      "s_nop 7\n\t"
      "s_branch NP6B_%=\n\t"
      : "+{s[4:19]}"(s), "+{s[20:27]}"(s2), "+{s[28:29]}"(kr), "+{s30}"(od), "+{v[240:247]}"(v), "+{v[136:139]}"(vo), "+{v140}"(vq)
      :
      : "memory", "vcc", "scc",
        "v0", "v1", "v2", "v3", "v4", "v5", "v6", "v7", "v8", "v9", "v10", "v11", "v12", "v13", "v14", "v15", "v16",
        "v17", "v18", "v19", "v20", "v21", "v22", "v23", "v24", "v25", "v26", "v27", "v28", "v29", "v30", "v31", "v32",
        "v33", "v34", "v35", "v36", "v37", "v38", "v39", "v40", "v41", "v42", "v43", "v44", "v45", "v46", "v47", "v48",
        "v49", "v50", "v51", "v52", "v53", "v54", "v55", "v56", "v57", "v58", "v59", "v60", "v61", "v62", "v63", "v64",
        "v65", "v66", "v67", "v68", "v69", "v70", "v71", "v72", "v73", "v74", "v75", "v76", "v77", "v78", "v79", "v80",
        "v81", "v82", "v83", "v84", "v85", "v86", "v87", "v88", "v89", "v90", "v91", "v92", "v93", "v94", "v95", "v96",
        "v97", "v98", "v99", "v100", "v101", "v102", "v103", "v104", "v105", "v106", "v107", "v108", "v109", "v110",
        "v111", "v112", "v113", "v114", "v115", "v116", "v117", "v118", "v119", "v120", "v121", "v122", "v123", "v124",
        "v125", "v126", "v127", "v128", "v129", "v130", "v131", "v132", "v133", "v134", "v135", "v141", "v142", "v143",
        "v144", "v145", "v146", "v147", "v148", "v149", "v150", "v151", "v152", "v153", "v154", "v155", "v156", "v157",
        "v158", "v159", "v160", "v161", "v162", "v163", "v164", "v165", "v166", "v167", "v168", "v169", "v170", "v171",
        "v172", "v173", "v174", "v175", "v176", "v177", "v178", "v179", "v180", "v181", "v182", "v183", "v184", "v185",
        "v186", "v187", "v188", "v189", "v190", "v191", "v192", "v193", "v194", "v195", "v196", "v197", "v198", "v199",
        "v200", "v201", "v202", "v203", "v204", "v205", "v206", "v207", "v208", "v209", "v210", "v211", "v212", "v213",
        "v214", "v215", "v216", "v217", "v218", "v219", "v220", "v221", "v222", "v223", "v224", "v225", "v226", "v227",
        "v228", "v229", "v230", "v231", "v232", "v233", "v234", "v235", "v236", "v237", "v238", "v239", "v248", "v249",
        "v250", "v251", "v252", "v253", "s31", "s44", "s45", "s46", "s47", "s48", "s49", "s50", "s51", "s52", "s53",
        "s54", "s55", "s56", "s57", "s58", "s59", "s60", "s61", "s62", "s63", "s64", "s65", "s66", "s67", "s68", "s69",
        "s70"
    );
    __builtin_amdgcn_endpgm();
  }
}

extern "C" void kernel_launch(void* const* d_in, const int* in_sizes, int n_in, void* d_out, int out_size,
                              void* d_ws, size_t ws_size, hipStream_t stream) {
  (void)hipFuncSetAttribute((const void*)fused_block, hipFuncAttributeMaxDynamicSharedMemorySize, LDS_BYTES);
  fused_block<<<256, 512, LDS_BYTES, stream>>>((const char*)d_in[0], (const char*)d_in[1], (const char*)d_in[2],
                                               (const char*)d_in[4], (const char*)d_in[5], (const char*)d_in[6],
                                               (const char*)d_in[7], (char*)d_out, (char*)d_ws);
}
```

```cpp
#include <hip/hip_runtime.h>
#include <stdint.h>

#define WS_ST 0
#define WS_HN 2097152
#define WS_O 4194304
#define WS_G 6291456
#define ST_FL 65536
#define ST_PW 114688
#define WS_EP 131072
#define PROBE 1835008
#define ST_CK 3981312
#define ST_H2 4112384
#define ST_H3 8175616
#define ST_H4 8306688
#define A_UNIT 5120
#define A_HALO 40960
#define A_SLOT 43008
#define A_CS 131072
#define A_ST 132096
#define A_EX 142336
#define A_FL 154624
#define A_MK 154752
#define B_UNIT 7168
#define B_SLOT 57344
#define B_X 114688
#define B_OP 928
#define B_PS 129536
#define B_WX 130560
#define B_WY 66048
#define G_WV 16384
#define A_W0 49152
#define A_WD 40960
#define G_RED 0
#define G_RED2 32768
#define LDS_KEY 163600
#define LDS_SINK 163328
#define LDS_BYTES 163840
#define XO 2
#define M44 0
#define KROT 14
#define KMUL 1
#define HP4 1
#define W2E 1
#define R4 0
#define SP3 1

typedef unsigned short u16;
typedef unsigned long long u64;
typedef __attribute__((ext_vector_type(4))) unsigned int u32x4;
typedef __attribute__((ext_vector_type(8))) unsigned int u32x8;
typedef __attribute__((ext_vector_type(16))) unsigned int u32x16;
#define UNI(x) (unsigned)__builtin_amdgcn_readfirstlane((int)(x))
#define PIN() __builtin_amdgcn_sched_barrier(0)

__device__ __forceinline__ int swz_tr(int row) { return (((row >> 1) & 1) | (((row >> 3) & 1) << 1)) << 1; }
__device__ __forceinline__ int swz_row(int row) { return (row >> 1) & 7; }
__device__ __forceinline__ int unit(int blk, int slice, int par) { return (((blk < 14 ? blk : blk - 14) * 8 + slice) * 2 + ((blk < 14 ? 0 : 1) ^ par)) * 8192; }
__device__ __forceinline__ int part(int n, int side) { return (n >> 13) * 16384 + side * 8192 + (n & 8191); }
template <class T>
__device__ __forceinline__ void put(T& s, int i, u64 p) { s[i] = (unsigned)p; s[i + 1] = (unsigned)(p >> 32); }
template <class T>
__device__ __forceinline__ void put2(T& s, int i, unsigned lo, unsigned hi) { s[i] = UNI(lo); s[i + 1] = UNI(hi); }

__global__ __launch_bounds__(512, 2) void fused_block(const char* __restrict__ x, const char* __restrict__ wqkv,
                                                   const char* __restrict__ wo, const char* __restrict__ w1,
                                                   const char* __restrict__ b1, const char* __restrict__ w2,
                                                   const char* __restrict__ b2, char* __restrict__ out,
                                                   char* __restrict__ ws0) {
  u32x8 f;
  asm volatile(
      "s_load_dwordx4 s[4:7], s[0:1], 0x0\n\t"
      "v_readfirstlane_b32 s3, v0\n\t"
      "s_getpc_b64 s[40:41]\n\t"
      "s_lshr_b32 s12, s2, 3\n\t"
      "s_or_b32 s13, s3, s12\n\t"
      "s_and_b32 s13, s13, 0x1d0\n\t"
      "s_cmp_lg_u32 s13, 16\n\t"
      "s_cbranch_scc1 ET2_%=\n\t"
      "s_and_b32 s12, s12, 7\n\t"
      "s_mov_b32 m0, 163328\n\t"
      "v_lshl_add_u32 v3, s12, 6, v0\n\t"
      "v_min_u32_e32 v3, 507, v3\n\t"
      "v_lshlrev_b32_e32 v3, 7, v3\n\t"
      "global_load_lds_dword v3, s[40:41]\n\t"
      "ET2_%=:\n\t"
      "v_lshrrev_b32_e32 v1, 3, v0\n\t"
      "v_and_b32_e32 v2, 7, v0\n\t"
      "s_and_b32 s17, s2, 7\n\t"
      "s_lshr_b32 s18, s2, 3\n\t"
      "s_lshr_b32 s3, s3, 6\n\t"
      "s_cmpk_gt_u32 s2, 0x7f\n\t"
      "s_cbranch_scc1 EB_%=\n\t"
      "v_and_b32_e32 v3, 2, v1\n\t"
      "v_lshrrev_b32_e32 v4, 1, v1\n\t"
      "s_lshr_b32 s19, s18, 3\n\t"
      "s_lshl1_add_u32 s19, s17, s19\n\t"
      "v_and_or_b32 v3, v4, 4, v3\n\t"
      "v_xor_b32_e32 v3, v3, v2\n\t"
      "v_min_u32_e32 v3, 6, v3\n\t"
      "v_mul_u32_u24_e32 v5, 2688, v1\n\t"
      "v_lshl_add_u32 v3, v3, 4, v5\n\t"
      "v_add_u32_e32 v195, 2048, v3\n\t"
      "v_add_u32_e32 v196, 1920, v3\n\t"
      "v_add_u32_e32 v197, 5888, v3\n\t"
      "s_mul_i32 s12, s19, 114688\n\t"
      "v_and_b32_e32 v5, 0x38, v1\n\t"
      "v_add_u32_e32 v5, v5, v1\n\t"
      "v_mul_u32_u24_e32 v5, 896, v5\n\t"
      "s_and_b32 s13, s18, 7\n\t"
      "s_mul_i32 s13, s13, 112\n\t"
      "v_and_b32_e32 v6, 3, v4\n\t"
      "v_xor_b32_e32 v6, v6, v2\n\t"
      "v_lshl_add_u32 v5, v6, 4, v5\n\t"
      "s_sub_u32 s12, s12, 15360\n\t"
      "s_ashr_i32 s14, s12, 31\n\t"
      "s_sub_u32 s13, s13, 4096\n\t"
      "v_add_u32_e32 v192, 15360, v5\n\t"
      "v_xor_b32_e32 v5, 64, v5\n\t"
      "v_add_u32_e32 v193, 21504, v5\n\t"
      "s_mul_i32 s9, s3, 5120\n\t"
      "s_waitcnt lgkmcnt(0)\n\t"
      "s_add_u32 s4, s4, s12\n\t"
      "s_addc_u32 s5, s5, s14\n\t"
      "s_add_u32 s6, s6, s13\n\t"
      "s_addc_u32 s7, s7, -1\n\t"
      "s_add_u32 m0, s9, 0\n\t"
      "s_nop 0\n\t"
      "global_load_lds_dwordx4 v195, s[6:7] offset:2048\n\t"
      "global_load_lds_dwordx4 v192, s[4:5]\n\t"
      "global_load_lds_dwordx4 v193, s[4:5] offset:1024\n\t"
      "global_load_lds_dwordx4 v196, s[6:7] offset:3072\n\t"
      "s_add_u32 m0, s9, 4096\n\t"
      "s_nop 0\n\t"
      "global_load_lds_dwordx4 v197, s[6:7]\n\t"
      "s_sub_u32 s3, s3, 4\n\t"
      "s_cbranch_scc1 EX_%=\n\t"
      "s_cmp_eq_u32 s19, 0\n\t"
      "s_cselect_b32 s13, 16, 0\n\t"
      "s_lshl2_add_u32 s13, s3, s13\n\t"
      "v_and_or_b32 v3, v1, 3, s13\n\t"
      "v_bfe_u32 v4, v3, 1, 3\n\t"
      "v_mul_u32_u24_e32 v3, 896, v3\n\t"
      "s_cmp_eq_u32 s3, 0\n\t"
      "s_cselect_b32 s10, 0xffffff00, -1\n\t"
      "s_mov_b32 s11, 0\n\t"
      "v_xor_b32_e32 v4, v4, v2\n\t"
      "v_lshl_add_u32 v3, v4, 4, v3\n\t"
      "v_add_u32_e32 v194, 1024, v3\n\t"
      "s_lshl_b32 s12, s3, 9\n\t"
      "s_add_u32 s12, s12, 40960\n\t"
      "s_add_u32 m0, s12, 0\n\t"
      "s_mov_b64 exec, s[10:11]\n\t"
      "global_load_lds_dwordx4 v194, s[4:5]\n\t"
      "s_mov_b64 exec, -1\n\t"
      "s_sub_u32 s40, s4, 57344\n\t"
      "s_subb_u32 s41, s5, 0\n\t"
      "s_sub_u32 s42, s6, 86016\n\t"
      "s_subb_u32 s43, s7, 0\n\t"
      "s_add_u32 s40, s40, 128\n\t"
      "s_addc_u32 s41, s41, 0\n\t"
      "s_add_u32 s42, s42, 172032\n\t"
      "s_addc_u32 s43, s43, 0\n\t"
      "s_add_u32 m0, s9, 22528\n\t"
      "s_nop 0\n\t"
      "global_load_lds_dwordx4 v195, s[42:43] offset:2048\n\t"
      "global_load_lds_dwordx4 v192, s[40:41]\n\t"
      "global_load_lds_dwordx4 v193, s[40:41] offset:1024\n\t"
      "global_load_lds_dwordx4 v196, s[42:43] offset:3072\n\t"
      "s_add_u32 m0, s9, 26624\n\t"
      "s_nop 0\n\t"
      "global_load_lds_dwordx4 v197, s[42:43]\n\t"
      "s_branch EX_%=\n\t"
      "EB_%=:\n\t"
      "v_and_b32_e32 v3, 63, v0\n\t"
      "v_lshlrev_b32_e32 v3, 4, v3\n\t"
      "s_sub_u32 s18, s18, 16\n\t"
      "s_min_u32 s16, s3, 6\n\t"
      "s_lshl4_add_u32 s15, s17, s18\n\t"
      "s_mul_i32 s15, s15, 14336\n\t"
      "s_lshl_b32 s12, s16, 7\n\t"
      "s_add_u32 s15, s15, s12\n\t"
      "v_mul_u32_u24_e32 v6, 56, v3\n\t"
      "v_add_u32_e32 v6, s15, v6\n\t"
      "s_add_u32 s19, s17, 2\n\t"
      "s_and_b32 s19, s19, 3\n\t"
      "s_and_b32 s12, s17, 4\n\t"
      "s_or_b32 s19, s19, s12\n\t"
      "s_lshl4_add_u32 s19, s19, s18\n\t"
      "s_lshl3_add_u32 s12, s19, s3\n\t"
      "s_lshl_b32 s12, s12, 11\n\t"
      "s_mul_i32 s19, s19, 14336\n\t"
      "s_lshl_b32 s16, s16, 11\n\t"
      "s_add_u32 s19, s19, s16\n\t"
      "s_add_u32 s8, s16, 114688\n\t"
      "v_add_u32_e32 v1, s19, v3\n\t"
      "s_lshl4_add_u32 s16, s3, s18\n\t"
      "s_mul_i32 s13, s16, 6784\n\t"
      "s_add_u32 s13, s13, 344064\n\t"
      "s_mul_i32 s14, s16, 2688\n\t"
      "s_add_u32 s14, s14, 0\n\t"
      "v_min_u32_e32 v4, 832, v3\n\t"
      "v_lshl_add_u32 v4, v4, 3, s13\n\t"
      "v_min_u32_e32 v4, 1204096, v4\n\t"
      "v_min_u32_e32 v5, 320, v3\n\t"
      "v_lshl_add_u32 v5, v5, 3, s14\n\t"
      "s_mov_b32 m0, 163328\n\t"
      "s_waitcnt lgkmcnt(0)\n\t"
      "s_mov_b32 m0, s8\n\t"
      "s_nop 0\n\t"
      "global_load_lds_dwordx4 v1, s[4:5]\n\t"
      "global_load_lds_dwordx4 v1, s[4:5] offset:1024\n\t"
      "s_mov_b32 m0, 163328\n\t"
      "s_nop 0\n\t"
      "global_load_lds_dword v5, s[6:7]\n\t"
      "s_barrier\n\t"
      "global_load_lds_dword v4, s[6:7]\n\t"
      "s_mov_b64 exec, 0xffff\n\t"
      "global_load_lds_dword v6, s[4:5]\n\t"
      "s_mov_b64 exec, -1\n\t"
      "EX_%=:\n\t"
      : "={v[192:199]}"(f)
      : "{s[0:1]}"(__builtin_amdgcn_kernarg_segment_ptr()), "{s2}"(blockIdx.x), "{v0}"(threadIdx.x)
      : "memory", "scc", "s3", "s4", "s5", "s6", "s7", "s8", "s9", "s10", "s11", "s12", "s13", "s14", "s15", "s16", "s17", "s18", "s19", "v1", "v2",
        "v3", "v4", "v5", "v6", "s40", "s41", "s42", "s43", "s48", "s49", "s50"
      );
  PIN();
  char* const ws = ws0 + (-(unsigned)(uintptr_t)ws0 & ((2u << 20) - 1));
  const int wave = __builtin_amdgcn_readfirstlane(threadIdx.x >> 6);
  const int xcd = blockIdx.x & 7, idx = blockIdx.x >> 3, side = xcd >> 2, xp = xcd & 3;
#define LANE_BITS(l) \
  const int lane = (l), g = lane >> 4, c = lane & 15, q4 = c >> 2, p4 = lane & 3, lr = lane >> 3, lc = lane & 7

  if (__builtin_expect(idx < 16, 1)) {
    const int rt = M44 ? 2 * (xcd & 6) + (idx >> 2) : 2 * xcd + (idx >> 3), hp = M44 ? (idx & 3) ^ (3 + (xcd & 1)) : idx & 7, hh = (wave >> 1) & 1, half = wave & 1;
    const unsigned sta = A_ST + half * 1024;
    if (wave < 4) {
      LANE_BITS(threadIdx.x & 63);
      u32x16 v;
      u32x8 s;
      v[0] = 4 * half * A_UNIT + c * 128 + ((g ^ swz_row(c)) << 4);
      v[1] = A_HALO + c * 128 + ((g ^ swz_row(c)) << 4);
#pragma unroll
      for (int j = 0; j < 2; ++j) {
        const int by = 56 * hh + 32 * j + 8 * p4;
        v[2 + j] = g * A_UNIT + 2048 + q4 * 128 + (((by >> 4) ^ swz_tr(8 * g + q4)) << 4) + (by & 8);
      }
      const unsigned csa = A_CS + hh * 384, exa = A_EX + hh * 3 * 2048;
      v[4] = csa + g * 16;
      v[5] = csa + c * 4;
      v[6] = v[4] + 64 * half;
      v[7] = v[5] + 64 * half;
      v[8] = A_FL + (wave ^ 1) * 8;
      v[9] = sta + g * 16;
      v[10] = sta + c * 4;
      const int key = 4 * g + (c & 1);
      v[11] = sta + 384 + (key <= c ? 128 : 64) + key * 4;
      unsigned vk1 = sta + 384 + (key + 2 <= c ? 128 : 64) + key * 4 + 8;
      v[12] = exa + half * 2048 + lane * 16;
      v[13] = exa + half * 2048 + lane * 8;
      v[14] = exa + lane * 16 + 8 * half;
      v[15] = lane * 16;
      put(s, 0, (u64)(ws + WS_O + part(((8 * (rt & 7) + 4 * half) * 16 + 2 * hp + hh) * 1024, 0)));
      s[2] = half;
      s[3] = UNI(rt == 0 && half == 0 ? 0xff800000u : 0u);
      s[4] = exa + 1024 + 512 * half;
      s[5] = A_FL + 32 + 4 * half;
      put(s, 6, (u64)(ws + WS_ST));
      unsigned hint = ST_H2 + part(((((rt & 7) * 8 + hp) * 2 + half) * 2 + hh) * 64, side);
      unsigned chk = ST_CK + part((((rt & 7) * 2 + half) * 16 + 2 * hp + hh) * 256, side);
    asm volatile(
      "s_waitcnt lgkmcnt(0)\n\ts_nop 4\n\t"
      "v_xor_b32_e32 v248, 64, v224\n\t"
      "v_xor_b32_e32 v249, 64, v225\n\t"
      "s_mov_b32 s36, 0xaaaaaaaa\n\t"
      "s_mov_b32 s37, 0xaaaaaaaa\n\t"
      "s_mov_b32 s38, 0xfff0ffff\n\t"
      "s_mov_b32 s39, 0xf000ff00\n\t"
      "s_mov_b32 s40, 0xffc0fffc\n\t"
      "s_mov_b32 s41, 0xc000fc00\n\t"
      "s_mov_b32 s42, 0x0\n\t"
      "s_mov_b32 s43, 0xffff0000\n\t"
      "s_mov_b32 s44, 0x10001\n\t"
      "s_mov_b32 s45, 0x10001\n\t"
      "v_mov_b32_e32 v216, 0xffff\n\t"
      "v_mov_b32_e32 v217, 0xffff0000\n\t"
      "v_cndmask_b32_e64 v216, v216, v217, s[36:37]\n\t"
      "v_cndmask_b32_e64 v240, v216, 0, s[38:39]\n\t"
      "v_cndmask_b32_e64 v241, v216, 0, s[40:41]\n\t"
      "v_cndmask_b32_e64 v242, 0, v216, s[38:39]\n\t"
      "v_cndmask_b32_e64 v243, 0, v216, s[40:41]\n\t"
      "v_mov_b32_e32 v244, s7\n\t"
      "v_mov_b32_e32 v245, s7\n\t"
      "v_mov_b32_e32 v246, s7\n\t"
      "v_mov_b32_e32 v247, s7\n\t"
      "v_mov_b64_e32 v[0:1], 0\n\t"
      "v_mov_b64_e32 v[2:3], 0\n\t"
      "v_mov_b64_e32 v[4:5], 0\n\t"
      "v_mov_b64_e32 v[6:7], 0\n\t"
      "v_mov_b64_e32 v[8:9], 0\n\t"
      "v_mov_b64_e32 v[10:11], 0\n\t"
      "v_mov_b64_e32 v[12:13], 0\n\t"
      "v_mov_b64_e32 v[14:15], 0\n\t"
      "v_mov_b64_e32 v[16:17], 0\n\t"
      "v_mov_b64_e32 v[18:19], 0\n\t"
      "v_mov_b64_e32 v[20:21], 0\n\t"
      "v_mov_b64_e32 v[22:23], 0\n\t"
      "v_mov_b64_e32 v[24:25], 0\n\t"
      "v_mov_b64_e32 v[26:27], 0\n\t"
      "v_mov_b64_e32 v[28:29], 0\n\t"
      "v_mov_b64_e32 v[30:31], 0\n\t"
      "v_mov_b64_e32 v[32:33], 0\n\t"
      "v_mov_b64_e32 v[34:35], 0\n\t"
      "v_mov_b64_e32 v[36:37], 0\n\t"
      "v_mov_b64_e32 v[38:39], 0\n\t"
      "v_mov_b64_e32 v[40:41], 0\n\t"
      "v_mov_b64_e32 v[42:43], 0\n\t"
      "v_mov_b64_e32 v[44:45], 0\n\t"
      "v_mov_b64_e32 v[46:47], 0\n\t"
      "v_mov_b64_e32 v[48:49], 0\n\t"
      "v_mov_b64_e32 v[50:51], 0\n\t"
      "v_mov_b64_e32 v[52:53], 0\n\t"
      "v_mov_b64_e32 v[54:55], 0\n\t"
      "v_mov_b64_e32 v[56:57], 0\n\t"
      "v_mov_b64_e32 v[58:59], 0\n\t"
      "v_mov_b64_e32 v[60:61], 0\n\t"
      "v_mov_b64_e32 v[62:63], 0\n\t"
      "v_mov_b64_e32 v[64:65], 0\n\t"
      "v_mov_b64_e32 v[66:67], 0\n\t"
      "v_mov_b64_e32 v[68:69], 0\n\t"
      "v_mov_b64_e32 v[70:71], 0\n\t"
      "v_mov_b64_e32 v[72:73], 0\n\t"
      "v_mov_b64_e32 v[74:75], 0\n\t"
      "v_mov_b64_e32 v[76:77], 0\n\t"
      "v_mov_b64_e32 v[78:79], 0\n\t"
      "v_mov_b64_e32 v[80:81], 0\n\t"
      "v_mov_b64_e32 v[82:83], 0\n\t"
      "v_mov_b64_e32 v[84:85], 0\n\t"
      "v_mov_b64_e32 v[86:87], 0\n\t"
      "v_mov_b64_e32 v[88:89], 0\n\t"
      "v_mov_b64_e32 v[90:91], 0\n\t"
      "v_mov_b64_e32 v[92:93], 0\n\t"
      "v_mov_b64_e32 v[94:95], 0\n\t"
      "v_mov_b64_e32 v[96:97], 0\n\t"
      "v_mov_b64_e32 v[98:99], 0\n\t"
      "v_mov_b64_e32 v[100:101], 0\n\t"
      "v_mov_b64_e32 v[102:103], 0\n\t"
      "v_mov_b64_e32 v[104:105], 0\n\t"
      "v_mov_b64_e32 v[106:107], 0\n\t"
      "s_mov_b32 s47, 6\n\t"
      "s_cmp_eq_u32 s6, 0\n\t"
      "s_cbranch_scc0 LOOP1_%=\n\t"
      "s_waitcnt vmcnt(0)\n\t"
      "s_barrier\n\t"
      "ds_read_b64_tr_b16 v[108:109], v226\n\t"
      "ds_read_b64_tr_b16 v[110:111], v226 offset:512\n\t"
      "ds_read_b128 v[132:135], v224\n\t"
      "ds_read_b128 v[136:139], v224 offset:5120\n\t"
      "ds_read_b128 v[140:143], v224 offset:10240\n\t"
      "ds_read_b128 v[144:147], v224 offset:15360\n\t"
      "ds_read_b64_tr_b16 v[112:113], v227\n\t"
      "ds_read_b64_tr_b16 v[114:115], v227 offset:512\n\t"
      "ds_read_b128 v[148:151], v225\n\t"
      "ds_read_b64_tr_b16 v[116:117], v226 offset:1024\n\t"
      "ds_read_b64_tr_b16 v[118:119], v226 offset:1536\n\t"
      "ds_read_b64_tr_b16 v[120:121], v227 offset:1024\n\t"
      "ds_read_b64_tr_b16 v[122:123], v227 offset:1536\n\t"
      "ds_read_b64_tr_b16 v[124:125], v226 offset:2048\n\t"
      "ds_read_b64_tr_b16 v[126:127], v226 offset:2560\n\t"
      "ds_read_b64_tr_b16 v[128:129], v227 offset:2048\n\t"
      "ds_read_b64_tr_b16 v[130:131], v227 offset:2560\n\t"
      "s_branch EVEN0_%=\n\t"
      "s_waitcnt lgkmcnt(0)\n\t"
      ".p2align 6\n\t"
      "ODD0_%=:\n\t"
      "s_waitcnt lgkmcnt(0)\n\t"
      "v_mfma_f32_16x16x32_bf16 v[0:3], v[152:155], v[176:179], v[0:3]\n\t"
      "v_add_u32_e32 v224, s46, v224\n\t"
      "v_add_u32_e32 v226, s46, v226\n\t"
      "v_mfma_f32_16x16x32_bf16 v[24:27], v[152:155], v[180:183], v[24:27]\n\t"
      "v_add_u32_e32 v227, s46, v227\n\t"
      "v_add_u32_e32 v225, s46, v225\n\t"
      "v_mfma_f32_16x16x32_bf16 v[48:51], v[152:155], v[184:187], v[48:51]\n\t"
      "s_barrier\n\t"
      "ds_read_b64_tr_b16 v[108:109], v226\n\t"
      "v_mfma_f32_16x16x32_bf16 v[72:75], v[152:155], v[188:191], v[72:75]\n\t"
      "ds_read_b64_tr_b16 v[110:111], v226 offset:512\n\t"
      "v_mfma_f32_16x16x32_bf16 v[4:7], v[156:159], v[176:179], v[4:7]\n\t"
      "ds_read_b128 v[132:135], v224\n\t"
      "v_mfma_f32_16x16x32_bf16 v[28:31], v[156:159], v[180:183], v[28:31]\n\t"
      "ds_read_b128 v[136:139], v224 offset:5120\n\t"
      "v_mfma_f32_16x16x32_bf16 v[52:55], v[156:159], v[184:187], v[52:55]\n\t"
      "ds_read_b128 v[140:143], v224 offset:10240\n\t"
      "v_mfma_f32_16x16x32_bf16 v[76:79], v[156:159], v[188:191], v[76:79]\n\t"
      "ds_read_b128 v[144:147], v224 offset:15360\n\t"
      "v_mfma_f32_16x16x32_bf16 v[96:99], v[152:155], v[192:195], v[96:99]\n\t"
      "v_mfma_f32_16x16x32_bf16 v[8:11], v[160:163], v[176:179], v[8:11]\n\t"
      "ds_read_b64_tr_b16 v[112:113], v227\n\t"
      "v_mfma_f32_16x16x32_bf16 v[32:35], v[160:163], v[180:183], v[32:35]\n\t"
      "ds_read_b64_tr_b16 v[114:115], v227 offset:512\n\t"
      "v_mfma_f32_16x16x32_bf16 v[56:59], v[160:163], v[184:187], v[56:59]\n\t"
      "ds_read_b128 v[148:151], v225\n\t"
      "v_mfma_f32_16x16x32_bf16 v[80:83], v[160:163], v[188:191], v[80:83]\n\t"
      "ds_read_b64_tr_b16 v[116:117], v226 offset:1024\n\t"
      "v_mfma_f32_16x16x32_bf16 v[12:15], v[164:167], v[176:179], v[12:15]\n\t"
      "ds_read_b64_tr_b16 v[118:119], v226 offset:1536\n\t"
      "v_mfma_f32_16x16x32_bf16 v[36:39], v[164:167], v[180:183], v[36:39]\n\t"
      "ds_read_b64_tr_b16 v[120:121], v227 offset:1024\n\t"
      "v_mfma_f32_16x16x32_bf16 v[60:63], v[164:167], v[184:187], v[60:63]\n\t"
      "v_mfma_f32_16x16x32_bf16 v[84:87], v[164:167], v[188:191], v[84:87]\n\t"
      "ds_read_b64_tr_b16 v[122:123], v227 offset:1536\n\t"
      "v_mfma_f32_16x16x32_bf16 v[100:103], v[160:163], v[192:195], v[100:103]\n\t"
      "ds_read_b64_tr_b16 v[124:125], v226 offset:2048\n\t"
      "v_mfma_f32_16x16x32_bf16 v[16:19], v[176:179], v[168:171], v[16:19]\n\t"
      "ds_read_b64_tr_b16 v[126:127], v226 offset:2560\n\t"
      "v_mfma_f32_16x16x32_bf16 v[40:43], v[180:183], v[168:171], v[40:43]\n\t"
      "ds_read_b64_tr_b16 v[128:129], v227 offset:2048\n\t"
      "v_mfma_f32_16x16x32_bf16 v[64:67], v[184:187], v[168:171], v[64:67]\n\t"
      "ds_read_b64_tr_b16 v[130:131], v227 offset:2560\n\t"
      "v_mfma_f32_16x16x32_bf16 v[88:91], v[188:191], v[168:171], v[88:91]\n\t"
      "v_mfma_f32_16x16x32_bf16 v[20:23], v[176:179], v[172:175], v[20:23]\n\t"
      "v_mfma_f32_16x16x32_bf16 v[44:47], v[180:183], v[172:175], v[44:47]\n\t"
      "v_add_u32_e32 v248, s46, v248\n\t"
      "v_add_u32_e32 v249, s46, v249\n\t"
      "v_mfma_f32_16x16x32_bf16 v[68:71], v[184:187], v[172:175], v[68:71]\n\t"
      "v_mfma_f32_16x16x32_bf16 v[92:95], v[188:191], v[172:175], v[92:95]\n\t"
      "v_mfma_f32_16x16x32_bf16 v[104:107], v[192:195], v[168:171], v[104:107]\n\t"
      "EVEN0_%=:\n\t"
      "s_waitcnt lgkmcnt(0)\n\t"
      "v_mfma_f32_16x16x32_bf16 v[0:3], v[108:111], v[132:135], v[0:3]\n\t"
      "ds_read_b64_tr_b16 v[152:153], v226 offset:20480\n\t"
      "v_mfma_f32_16x16x32_bf16 v[24:27], v[108:111], v[136:139], v[24:27]\n\t"
      "ds_read_b64_tr_b16 v[154:155], v226 offset:20992\n\t"
      "v_mfma_f32_16x16x32_bf16 v[48:51], v[108:111], v[140:143], v[48:51]\n\t"
      "ds_read_b128 v[176:179], v248\n\t"
      "v_mfma_f32_16x16x32_bf16 v[72:75], v[108:111], v[144:147], v[72:75]\n\t"
      "ds_read_b128 v[180:183], v248 offset:5120\n\t"
      "v_mfma_f32_16x16x32_bf16 v[4:7], v[112:115], v[132:135], v[4:7]\n\t"
      "v_mfma_f32_16x16x32_bf16 v[28:31], v[112:115], v[136:139], v[28:31]\n\t"
      "ds_read_b128 v[184:187], v248 offset:10240\n\t"
      "v_mfma_f32_16x16x32_bf16 v[52:55], v[112:115], v[140:143], v[52:55]\n\t"
      "ds_read_b128 v[188:191], v248 offset:15360\n\t"
      "v_mfma_f32_16x16x32_bf16 v[76:79], v[112:115], v[144:147], v[76:79]\n\t"
      "ds_read_b64_tr_b16 v[156:157], v227 offset:20480\n\t"
      "v_mfma_f32_16x16x32_bf16 v[96:99], v[108:111], v[148:151], v[96:99]\n\t"
      "v_mfma_f32_16x16x32_bf16 v[8:11], v[116:119], v[132:135], v[8:11]\n\t"
      "ds_read_b64_tr_b16 v[158:159], v227 offset:20992\n\t"
      "v_mfma_f32_16x16x32_bf16 v[32:35], v[116:119], v[136:139], v[32:35]\n\t"
      "ds_read_b128 v[192:195], v249\n\t"
      "v_mfma_f32_16x16x32_bf16 v[56:59], v[116:119], v[140:143], v[56:59]\n\t"
      "ds_read_b64_tr_b16 v[160:161], v226 offset:21504\n\t"
      "v_mfma_f32_16x16x32_bf16 v[80:83], v[116:119], v[144:147], v[80:83]\n\t"
      "ds_read_b64_tr_b16 v[162:163], v226 offset:22016\n\t"
      "v_mfma_f32_16x16x32_bf16 v[12:15], v[120:123], v[132:135], v[12:15]\n\t"
      "v_mfma_f32_16x16x32_bf16 v[36:39], v[120:123], v[136:139], v[36:39]\n\t"
      "ds_read_b64_tr_b16 v[164:165], v227 offset:21504\n\t"
      "v_mfma_f32_16x16x32_bf16 v[60:63], v[120:123], v[140:143], v[60:63]\n\t"
      "ds_read_b64_tr_b16 v[166:167], v227 offset:22016\n\t"
      "v_mfma_f32_16x16x32_bf16 v[84:87], v[120:123], v[144:147], v[84:87]\n\t"
      "ds_read_b64_tr_b16 v[168:169], v226 offset:22528\n\t"
      "v_mfma_f32_16x16x32_bf16 v[100:103], v[116:119], v[148:151], v[100:103]\n\t"
      "v_mfma_f32_16x16x32_bf16 v[16:19], v[132:135], v[124:127], v[16:19]\n\t"
      "ds_read_b64_tr_b16 v[170:171], v226 offset:23040\n\t"
      "v_mfma_f32_16x16x32_bf16 v[40:43], v[136:139], v[124:127], v[40:43]\n\t"
      "ds_read_b64_tr_b16 v[172:173], v227 offset:22528\n\t"
      "v_mfma_f32_16x16x32_bf16 v[64:67], v[140:143], v[124:127], v[64:67]\n\t"
      "ds_read_b64_tr_b16 v[174:175], v227 offset:23040\n\t"
      "s_cmp_eq_u32 s47, 4\n\t"
      "s_cselect_b32 s46, -2, 1\n\t"
      "v_mfma_f32_16x16x32_bf16 v[88:91], v[144:147], v[124:127], v[88:91]\n\t"
      "s_cmp_eq_u32 s47, 1\n\t"
      "s_cselect_b32 s46, -2, s46\n\t"
      "v_mfma_f32_16x16x32_bf16 v[20:23], v[132:135], v[128:131], v[20:23]\n\t"
      "s_mul_i32 s46, s46, 43008\n\t"
      "v_mfma_f32_16x16x32_bf16 v[44:47], v[136:139], v[128:131], v[44:47]\n\t"
      "v_mfma_f32_16x16x32_bf16 v[68:71], v[140:143], v[128:131], v[68:71]\n\t"
      "v_mfma_f32_16x16x32_bf16 v[92:95], v[144:147], v[128:131], v[92:95]\n\t"
      "s_sub_u32 s47, s47, 1\n\t"
      "v_mfma_f32_16x16x32_bf16 v[104:107], v[148:151], v[124:127], v[104:107]\n\t"
      "s_cbranch_scc0 ODD0_%=\n\t"
      "s_waitcnt lgkmcnt(0)\n\t"
      "v_mfma_f32_16x16x32_bf16 v[96:99], v[152:155], v[192:195], v[96:99]\n\t"
      "v_mov_b32_e32 v212, s9\n\t"
      "ds_read_b32 v212, v212\n\t"
      "ds_read2_b32 v[128:129], v234 offset0:32 offset1:32\n\t"
      "v_mfma_f32_16x16x32_bf16 v[100:103], v[160:163], v[192:195], v[100:103]\n\t"
      "ds_read2_b32 v[130:131], v234 offset0:48 offset1:48\n\t"
      "v_mfma_f32_16x16x32_bf16 v[104:107], v[192:195], v[168:171], v[104:107]\n\t"
      "ds_read2_b32 v[132:133], v234 offset0:64 offset1:64\n\t"
      "ds_read2_b32 v[134:135], v234 offset0:80 offset1:80\n\t"
      "v_mfma_f32_16x16x32_bf16 v[80:83], v[160:163], v[188:191], v[80:83]\n\t"
      "ds_read2_b32 v[248:249], v234 offset0:0 offset1:0\n\t"
      "v_mfma_f32_16x16x32_bf16 v[84:87], v[164:167], v[188:191], v[84:87]\n\t"
      "ds_read_b128 v[196:199], v233\n\t"
      "ds_read_b128 v[136:139], v233 offset:128\n\t"
      "v_mfma_f32_16x16x32_bf16 v[88:91], v[188:191], v[168:171], v[88:91]\n\t"
      "ds_read_b128 v[140:143], v233 offset:192\n\t"
      "v_mfma_f32_16x16x32_bf16 v[92:95], v[188:191], v[172:175], v[92:95]\n\t"
      "ds_read_b128 v[144:147], v233 offset:256\n\t"
      "ds_read_b128 v[148:151], v233 offset:320\n\t"
      "v_mfma_f32_16x16x32_bf16 v[56:59], v[160:163], v[184:187], v[56:59]\n\t"
      "s_mov_b64 exec, s[44:45]\n\t"
      "ds_write_b128 v230, v[96:99]\n\t"
      "ds_write_b128 v230, v[100:103] offset:128\n\t"
      "s_mov_b64 exec, 0xffff\n\t"
      "ds_write_b32 v231, v104 offset:256\n\t"
      "v_xor_b32_e32 v227, 8, v232\n\t"
      "ds_write_b32 v227, v227\n\t"
      "s_mov_b64 exec, -1\n\t"
      "v_mfma_f32_16x16x32_bf16 v[60:63], v[164:167], v[184:187], v[60:63]\n\t"
      "v_mfma_f32_16x16x32_bf16 v[64:67], v[184:187], v[168:171], v[64:67]\n\t"
      "v_mfma_f32_16x16x32_bf16 v[68:71], v[184:187], v[172:175], v[68:71]\n\t"
      "s_waitcnt lgkmcnt(4)\n\t"
      "v_mfma_f32_16x16x32_bf16 v[32:35], v[160:163], v[180:183], v[32:35]\n\t"
      "ds_read_b32 v226, v232\n\t"
      "ds_read_b128 v[116:119], v228 offset:128\n\t"
      "v_mfma_f32_16x16x32_bf16 v[36:39], v[164:167], v[180:183], v[36:39]\n\t"
      "ds_read_b128 v[120:123], v228 offset:192\n\t"
      "ds_read2_b32 v[250:251], v229 offset0:64 offset1:64\n\t"
      "v_mfma_f32_16x16x32_bf16 v[40:43], v[180:183], v[168:171], v[40:43]\n\t"
      "ds_read2_b32 v[252:253], v229 offset0:80 offset1:80\n\t"
      "ds_read_b128 v[124:127], v230 offset:128\n\t"
      "v_mfma_f32_16x16x32_bf16 v[44:47], v[180:183], v[172:175], v[44:47]\n\t"
      "ds_read2_b32 v[224:225], v231 offset0:64 offset1:64\n\t"
      "ds_read_b128 v[108:111], v228\n\t"
      "v_mfma_f32_16x16x32_bf16 v[8:11], v[160:163], v[176:179], v[8:11]\n\t"
      "ds_read_b128 v[112:115], v228 offset:64\n\t"
      "ds_read2_b32 v[204:205], v235 offset0:0 offset1:16\n\t"
      "v_mfma_f32_16x16x32_bf16 v[12:15], v[164:167], v[176:179], v[12:15]\n\t"
      "ds_read2_b32 v[206:207], v235 offset0:32 offset1:48\n\t"
      "ds_read2_b32 v[208:209], v254 offset0:0 offset1:16\n\t"
      "v_mfma_f32_16x16x32_bf16 v[16:19], v[176:179], v[168:171], v[16:19]\n\t"
      "ds_read2_b32 v[210:211], v254 offset0:32 offset1:48\n\t"
      "ds_read2_b32 v[200:201], v234 offset0:192 offset1:208\n\t"
      "v_mfma_f32_16x16x32_bf16 v[20:23], v[176:179], v[172:175], v[20:23]\n\t"
      "ds_read2_b32 v[202:203], v234 offset0:224 offset1:240\n\t"
      "LOOPX_%=:\n\t"
      "s_waitcnt lgkmcnt(14)\n\t"
      "v_min_u32_e32 v226, v226, v212\n\t"
      "v_cmp_eq_u32_e32 vcc, 0, v226\n\t"
      "s_cbranch_vccnz XAR_%=\n\t"
      "XAB_%=:\n\t"
      "s_waitcnt lgkmcnt(12)\n\t"
      "v_pk_fma_f32 v[80:81], v[134:135], v[116:117], v[80:81]\n\t"
      "v_pk_fma_f32 v[82:83], v[134:135], v[118:119], v[82:83]\n\t"
      "v_pk_fma_f32 v[84:85], v[134:135], v[120:121], v[84:85]\n\t"
      "v_pk_fma_f32 v[86:87], v[134:135], v[122:123], v[86:87]\n\t"
      "v_mfma_f32_16x16x32_bf16 v[72:75], v[152:155], v[188:191], v[72:75]\n\t"
      "v_cvt_pk_bf16_f32 v80, v80, v81\n\t"
      "v_cvt_pk_bf16_f32 v81, v82, v83\n\t"
      "v_cvt_pk_bf16_f32 v82, v84, v85\n\t"
      "v_cvt_pk_bf16_f32 v83, v86, v87\n\t"
      "v_mfma_f32_16x16x32_bf16 v[76:79], v[156:159], v[188:191], v[76:79]\n\t"
      "ds_write_b128 v236, v[80:83] offset:2048\n\t"
      "s_waitcnt lgkmcnt(11)\n\t"
      "v_pk_fma_f32 v[88:89], v[148:149], v[250:251], v[88:89]\n\t"
      "v_pk_fma_f32 v[90:91], v[150:151], v[250:251], v[90:91]\n\t"
      "v_pk_fma_f32 v[92:93], v[148:149], v[252:253], v[92:93]\n\t"
      "v_pk_fma_f32 v[94:95], v[150:151], v[252:253], v[94:95]\n\t"
      "v_mfma_f32_16x16x32_bf16 v[48:51], v[152:155], v[184:187], v[48:51]\n\t"
      "v_cvt_pk_bf16_f32 v216, v88, v89\n\t"
      "v_cvt_pk_bf16_f32 v217, v90, v91\n\t"
      "v_cvt_pk_bf16_f32 v218, v92, v93\n\t"
      "v_cvt_pk_bf16_f32 v219, v94, v95\n\t"
      "v_mfma_f32_16x16x32_bf16 v[52:55], v[156:159], v[184:187], v[52:55]\n\t"
      "ds_write_b64 v237, v[216:217] offset:3072\n\t"
      "ds_write_b64 v237, v[218:219] offset:3584\n\t"
      "s_waitcnt lgkmcnt(11)\n\t"
      "v_pk_fma_f32 v[100:101], v[248:249], v[124:125], v[100:101]\n\t"
      "v_pk_fma_f32 v[104:105], v[196:197], v[224:225], v[104:105]\n\t"
      "v_pk_fma_f32 v[102:103], v[248:249], v[126:127], v[102:103]\n\t"
      "v_pk_fma_f32 v[106:107], v[198:199], v[224:225], v[106:107]\n\t"
      "v_mfma_f32_16x16x32_bf16 v[24:27], v[152:155], v[180:183], v[24:27]\n\t"
      "v_cvt_pk_bf16_f32 v220, v100, v101\n\t"
      "v_cvt_pk_bf16_f32 v221, v102, v103\n\t"
      "v_cvt_pk_bf16_f32 v222, v104, v105\n\t"
      "v_cvt_pk_bf16_f32 v223, v106, v107\n\t"
      "v_mfma_f32_16x16x32_bf16 v[28:31], v[156:159], v[180:183], v[28:31]\n\t"
      "ds_write_b64 v238, v[220:221]\n\t"
      "v_lshrrev_b32_e32 v96, 1, v239\n\t"
      "v_add_u32_e32 v96, s8, v96\n\t"
      "ds_write_b64 v96, v[222:223]\n\t"
      "v_xor_b32_e32 v227, 8, v232\n\t"
      "ds_write_b32 v227, v227 offset:4\n\t"
      "v_pk_fma_f32 v[56:57], v[132:133], v[116:117], v[56:57]\n\t"
      "v_pk_fma_f32 v[58:59], v[132:133], v[118:119], v[58:59]\n\t"
      "v_pk_fma_f32 v[60:61], v[132:133], v[120:121], v[60:61]\n\t"
      "v_pk_fma_f32 v[62:63], v[132:133], v[122:123], v[62:63]\n\t"
      "v_mfma_f32_16x16x32_bf16 v[0:3], v[152:155], v[176:179], v[0:3]\n\t"
      "v_cvt_pk_bf16_f32 v56, v56, v57\n\t"
      "v_cvt_pk_bf16_f32 v57, v58, v59\n\t"
      "v_cvt_pk_bf16_f32 v58, v60, v61\n\t"
      "v_cvt_pk_bf16_f32 v59, v62, v63\n\t"
      "v_mfma_f32_16x16x32_bf16 v[4:7], v[156:159], v[176:179], v[4:7]\n\t"
      "v_mov_b32_e32 v164, v216\n\t"
      "v_mov_b32_e32 v165, v217\n\t"
      "v_mov_b32_e32 v174, v218\n\t"
      "v_mov_b32_e32 v175, v219\n\t"
      "s_waitcnt lgkmcnt(12)\n\t"
      "v_pk_fma_f32 v[72:73], v[134:135], v[108:109], v[72:73]\n\t"
      "v_pk_fma_f32 v[74:75], v[134:135], v[110:111], v[74:75]\n\t"
      "v_pk_fma_f32 v[76:77], v[134:135], v[112:113], v[76:77]\n\t"
      "v_pk_fma_f32 v[78:79], v[134:135], v[114:115], v[78:79]\n\t"
      "v_cvt_pk_bf16_f32 v72, v72, v73\n\t"
      "v_cvt_pk_bf16_f32 v73, v74, v75\n\t"
      "v_cvt_pk_bf16_f32 v74, v76, v77\n\t"
      "v_cvt_pk_bf16_f32 v75, v78, v79\n\t"
      "v_cndmask_b32_e64 v74, v74, 0, s[42:43]\n\t"
      "v_cndmask_b32_e64 v75, v75, 0, s[42:43]\n\t"
      "s_nop 1\n\t"
      "v_mfma_f32_16x16x32_bf16 v[76:79], v[56:59], v[72:75], 0\n\t"
      "v_mfma_f32_16x16x32_bf16 v[84:87], v[80:83], v[72:75], 0\n\t"
      "v_pk_fma_f32 v[32:33], v[130:131], v[116:117], v[32:33]\n\t"
      "v_pk_fma_f32 v[34:35], v[130:131], v[118:119], v[34:35]\n\t"
      "v_pk_fma_f32 v[36:37], v[130:131], v[120:121], v[36:37]\n\t"
      "v_pk_fma_f32 v[38:39], v[130:131], v[122:123], v[38:39]\n\t"
      "v_cvt_pk_bf16_f32 v32, v32, v33\n\t"
      "v_cvt_pk_bf16_f32 v33, v34, v35\n\t"
      "v_cvt_pk_bf16_f32 v34, v36, v37\n\t"
      "v_cvt_pk_bf16_f32 v35, v38, v39\n\t"
      "v_pk_fma_f32 v[48:49], v[132:133], v[108:109], v[48:49]\n\t"
      "v_pk_fma_f32 v[50:51], v[132:133], v[110:111], v[50:51]\n\t"
      "v_pk_fma_f32 v[52:53], v[132:133], v[112:113], v[52:53]\n\t"
      "v_pk_fma_f32 v[54:55], v[132:133], v[114:115], v[54:55]\n\t"
      "v_cvt_pk_bf16_f32 v48, v48, v49\n\t"
      "v_cvt_pk_bf16_f32 v49, v50, v51\n\t"
      "v_cvt_pk_bf16_f32 v50, v52, v53\n\t"
      "v_cvt_pk_bf16_f32 v51, v54, v55\n\t"
      "v_cndmask_b32_e64 v50, v50, 0, s[42:43]\n\t"
      "v_cndmask_b32_e64 v51, v51, 0, s[42:43]\n\t"
      "v_pk_fma_f32 v[64:65], v[144:145], v[250:251], v[64:65]\n\t"
      "v_pk_fma_f32 v[66:67], v[146:147], v[250:251], v[66:67]\n\t"
      "v_pk_fma_f32 v[68:69], v[144:145], v[252:253], v[68:69]\n\t"
      "v_pk_fma_f32 v[70:71], v[146:147], v[252:253], v[70:71]\n\t"
      "v_cvt_pk_bf16_f32 v162, v64, v65\n\t"
      "v_cvt_pk_bf16_f32 v163, v66, v67\n\t"
      "v_cvt_pk_bf16_f32 v172, v68, v69\n\t"
      "v_cvt_pk_bf16_f32 v173, v70, v71\n\t"
      "ds_read_b32 v226, v232 offset:4\n\t"
      "ds_read_b128 v[152:155], v236\n\t"
      "ds_read_b64 v[156:157], v237 offset:1024\n\t"
      "ds_read_b64 v[166:167], v237 offset:1536\n\t"
      "v_mfma_f32_16x16x32_bf16 v[52:55], v[32:35], v[48:51], 0\n\t"
      "v_mfma_f32_16x16x32_bf16 v[60:63], v[56:59], v[48:51], 0\n\t"
      "v_pk_fma_f32 v[8:9], v[128:129], v[116:117], v[8:9]\n\t"
      "v_pk_fma_f32 v[10:11], v[128:129], v[118:119], v[10:11]\n\t"
      "v_pk_fma_f32 v[12:13], v[128:129], v[120:121], v[12:13]\n\t"
      "v_pk_fma_f32 v[14:15], v[128:129], v[122:123], v[14:15]\n\t"
      "v_cvt_pk_bf16_f32 v8, v8, v9\n\t"
      "v_cvt_pk_bf16_f32 v9, v10, v11\n\t"
      "v_cvt_pk_bf16_f32 v10, v12, v13\n\t"
      "v_cvt_pk_bf16_f32 v11, v14, v15\n\t"
      "v_pk_fma_f32 v[24:25], v[130:131], v[108:109], v[24:25]\n\t"
      "v_pk_fma_f32 v[26:27], v[130:131], v[110:111], v[26:27]\n\t"
      "v_pk_fma_f32 v[28:29], v[130:131], v[112:113], v[28:29]\n\t"
      "v_pk_fma_f32 v[30:31], v[130:131], v[114:115], v[30:31]\n\t"
      "v_cvt_pk_bf16_f32 v24, v24, v25\n\t"
      "v_cvt_pk_bf16_f32 v25, v26, v27\n\t"
      "v_cvt_pk_bf16_f32 v26, v28, v29\n\t"
      "v_cvt_pk_bf16_f32 v27, v30, v31\n\t"
      "v_cndmask_b32_e64 v26, v26, 0, s[42:43]\n\t"
      "v_cndmask_b32_e64 v27, v27, 0, s[42:43]\n\t"
      "v_pk_fma_f32 v[40:41], v[140:141], v[250:251], v[40:41]\n\t"
      "v_pk_fma_f32 v[42:43], v[142:143], v[250:251], v[42:43]\n\t"
      "v_pk_fma_f32 v[44:45], v[140:141], v[252:253], v[44:45]\n\t"
      "v_pk_fma_f32 v[46:47], v[142:143], v[252:253], v[46:47]\n\t"
      "v_cvt_pk_bf16_f32 v160, v40, v41\n\t"
      "v_cvt_pk_bf16_f32 v161, v42, v43\n\t"
      "v_cvt_pk_bf16_f32 v170, v44, v45\n\t"
      "v_cvt_pk_bf16_f32 v171, v46, v47\n\t"
      "v_mfma_f32_16x16x32_bf16 v[28:31], v[8:11], v[24:27], 0\n\t"
      "v_mfma_f32_16x16x32_bf16 v[36:39], v[32:35], v[24:27], 0\n\t"
      "v_pk_fma_f32 v[0:1], v[128:129], v[108:109], v[0:1]\n\t"
      "v_pk_fma_f32 v[2:3], v[128:129], v[110:111], v[2:3]\n\t"
      "v_pk_fma_f32 v[4:5], v[128:129], v[112:113], v[4:5]\n\t"
      "v_pk_fma_f32 v[6:7], v[128:129], v[114:115], v[6:7]\n\t"
      "v_cvt_pk_bf16_f32 v0, v0, v1\n\t"
      "v_cvt_pk_bf16_f32 v1, v2, v3\n\t"
      "v_cvt_pk_bf16_f32 v2, v4, v5\n\t"
      "v_cvt_pk_bf16_f32 v3, v6, v7\n\t"
      "v_cndmask_b32_e64 v2, v2, 0, s[42:43]\n\t"
      "v_cndmask_b32_e64 v3, v3, 0, s[42:43]\n\t"
      "v_pk_fma_f32 v[16:17], v[136:137], v[250:251], v[16:17]\n\t"
      "v_pk_fma_f32 v[18:19], v[138:139], v[250:251], v[18:19]\n\t"
      "v_pk_fma_f32 v[20:21], v[136:137], v[252:253], v[20:21]\n\t"
      "v_pk_fma_f32 v[22:23], v[138:139], v[252:253], v[22:23]\n\t"
      "v_cvt_pk_bf16_f32 v158, v16, v17\n\t"
      "v_cvt_pk_bf16_f32 v159, v18, v19\n\t"
      "v_cvt_pk_bf16_f32 v168, v20, v21\n\t"
      "v_cvt_pk_bf16_f32 v169, v22, v23\n\t"
      "s_waitcnt lgkmcnt(3)\n\t"
      "v_cmp_eq_u32_e32 vcc, 0, v226\n\t"
      "s_cbranch_vccnz XBR_%=\n\t"
      "XBB_%=:\n\t"
      "s_waitcnt lgkmcnt(2)\n\t"
      "v_mfma_f32_16x16x32_bf16 v[4:7], v[152:155], v[0:3], v[244:247]\n\t"
      "v_mfma_f32_16x16x32_bf16 v[12:15], v[8:11], v[0:3], 0\n\t"
      "v_mov_b32_e32 v252, 154752\n\t"
      "ds_read_b64 v[250:251], v252 offset:8848\n\t"
      "ds_read_b64 v[196:197], v252\n\t"
      "s_and_b32 s46, s12, 0x2000\n\t"
      "v_cndmask_b32_e64 v76, v76, v77, s[36:37]\n\t"
      "v_cndmask_b32_e64 v84, v84, v85, s[36:37]\n\t"
      "v_cndmask_b32_e64 v78, v78, v79, s[36:37]\n\t"
      "v_cndmask_b32_e64 v86, v86, v87, s[36:37]\n\t"
      "v_cndmask_b32_e64 v179, v76, v84, s[38:39]\n\t"
      "v_cndmask_b32_e64 v183, v78, v86, s[40:41]\n\t"
      "v_cndmask_b32_e64 v52, v52, v53, s[36:37]\n\t"
      "v_cndmask_b32_e64 v60, v60, v61, s[36:37]\n\t"
      "v_cndmask_b32_e64 v54, v54, v55, s[36:37]\n\t"
      "v_cndmask_b32_e64 v62, v62, v63, s[36:37]\n\t"
      "v_cndmask_b32_e64 v178, v52, v60, s[38:39]\n\t"
      "v_cndmask_b32_e64 v182, v54, v62, s[40:41]\n\t"
      "v_pk_mul_f32 v[178:179], v[178:179], v[206:207]\n\t"
      "v_pk_mul_f32 v[182:183], v[182:183], v[210:211]\n\t"
      "v_max_f32_e32 v219, v179, v183\n\t"
      "v_max_f32_e32 v218, v178, v182\n\t"
      "v_cndmask_b32_e64 v28, v28, v29, s[36:37]\n\t"
      "v_cndmask_b32_e64 v36, v36, v37, s[36:37]\n\t"
      "v_cndmask_b32_e64 v30, v30, v31, s[36:37]\n\t"
      "v_cndmask_b32_e64 v38, v38, v39, s[36:37]\n\t"
      "v_cndmask_b32_e64 v177, v28, v36, s[38:39]\n\t"
      "v_cndmask_b32_e64 v181, v30, v38, s[40:41]\n\t"
      "v_cndmask_b32_e64 v4, v4, v5, s[36:37]\n\t"
      "v_cndmask_b32_e64 v12, v12, v13, s[36:37]\n\t"
      "v_cndmask_b32_e64 v6, v6, v7, s[36:37]\n\t"
      "v_cndmask_b32_e64 v14, v14, v15, s[36:37]\n\t"
      "v_cndmask_b32_e64 v176, v4, v12, s[38:39]\n\t"
      "v_cndmask_b32_e64 v180, v6, v14, s[40:41]\n\t"
      "v_pk_mul_f32 v[176:177], v[176:177], v[204:205]\n\t"
      "v_pk_mul_f32 v[180:181], v[180:181], v[208:209]\n\t"
      "v_max_f32_e32 v217, v177, v181\n\t"
      "v_max_f32_e32 v216, v176, v180\n\t"
      "v_permlane16_swap_b32_e32 v218, v219\n\t"
      "s_nop 0\n\t"
      "v_permlane16_swap_b32_e32 v216, v217\n\t"
      "v_max_f32_e32 v218, v218, v219\n\t"
      "v_max_f32_e32 v216, v216, v217\n\t"
      "v_mov_b32_e32 v219, v218\n\t"
      "v_mov_b32_e32 v217, v216\n\t"
      "s_nop 0\n\t"
      "v_permlane32_swap_b32_e32 v218, v219\n\t"
      "v_permlane32_swap_b32_e32 v216, v217\n\t"
      "v_max_f32_e32 v218, v218, v219\n\t"
      "v_max_f32_e32 v216, v216, v217\n\t"
      "v_mov_b32_e32 v219, v218\n\t"
      "v_mov_b32_e32 v217, v216\n\t"
      "s_nop 0\n\t"
      "v_permlane16_swap_b32_e32 v218, v219\n\t"
      "v_permlane16_swap_b32_e32 v216, v217\n\t"
      "v_pk_add_f32 v[178:179], v[178:179], v[218:219] neg_lo:[0,1] neg_hi:[0,1]\n\t"
      "v_pk_add_f32 v[182:183], v[182:183], v[218:219] neg_lo:[0,1] neg_hi:[0,1]\n\t"
      "v_pk_add_f32 v[176:177], v[176:177], v[216:217] neg_lo:[0,1] neg_hi:[0,1]\n\t"
      "v_pk_add_f32 v[180:181], v[180:181], v[216:217] neg_lo:[0,1] neg_hi:[0,1]\n\t"
      "v_pk_mul_f32 v[178:179], v[178:179], v[202:203]\n\t"
      "v_pk_mul_f32 v[182:183], v[182:183], v[202:203]\n\t"
      "v_pk_mul_f32 v[176:177], v[176:177], v[200:201]\n\t"
      "v_pk_mul_f32 v[180:181], v[180:181], v[200:201]\n\t"
      "v_exp_f32_e32 v179, v179\n\t"
      "v_exp_f32_e32 v183, v183\n\t"
      "v_exp_f32_e32 v178, v178\n\t"
      "v_exp_f32_e32 v182, v182\n\t"
      "v_exp_f32_e32 v177, v177\n\t"
      "v_exp_f32_e32 v181, v181\n\t"
      "v_exp_f32_e32 v176, v176\n\t"
      "v_exp_f32_e32 v180, v180\n\t"
      "s_waitcnt lgkmcnt(0)\n\t"
      "v_xor_b32_e32 v253, s12, v197\n\t"
      "s_mov_b64 exec, 1\n\t"
      "global_store_dwordx2 v253, v[250:251], s[10:11] sc1\n\t"
      "s_mov_b64 exec, -1\n\t"
      "v_pk_add_f32 v[218:219], v[178:179], v[182:183]\n\t"
      "v_pk_mul_f32 v[178:179], v[178:179], v[206:207]\n\t"
      "v_pk_mul_f32 v[182:183], v[182:183], v[210:211]\n\t"
      "v_cvt_pk_bf16_f32 v179, v179, v179\n\t"
      "v_cvt_pk_bf16_f32 v183, v183, v183\n\t"
      "v_and_b32_e32 v72, v240, v179\n\t"
      "v_and_b32_e32 v73, v241, v183\n\t"
      "v_and_b32_e32 v74, v242, v179\n\t"
      "v_and_b32_e32 v75, v243, v183\n\t"
      "v_cvt_pk_bf16_f32 v178, v178, v178\n\t"
      "v_cvt_pk_bf16_f32 v182, v182, v182\n\t"
      "v_and_b32_e32 v48, v240, v178\n\t"
      "v_and_b32_e32 v49, v241, v182\n\t"
      "v_and_b32_e32 v50, v242, v178\n\t"
      "v_and_b32_e32 v51, v243, v182\n\t"
      "v_mfma_f32_16x16x32_bf16 v[88:91], v[162:165], v[72:75], 0\n\t"
      "v_mfma_f32_16x16x32_bf16 v[92:95], v[172:175], v[72:75], 0\n\t"
      "v_pk_add_f32 v[216:217], v[176:177], v[180:181]\n\t"
      "v_pk_mul_f32 v[176:177], v[176:177], v[204:205]\n\t"
      "v_pk_mul_f32 v[180:181], v[180:181], v[208:209]\n\t"
      "v_cvt_pk_bf16_f32 v177, v177, v177\n\t"
      "v_cvt_pk_bf16_f32 v181, v181, v181\n\t"
      "v_and_b32_e32 v24, v240, v177\n\t"
      "v_and_b32_e32 v25, v241, v181\n\t"
      "v_and_b32_e32 v26, v242, v177\n\t"
      "v_and_b32_e32 v27, v243, v181\n\t"
      "v_mfma_f32_16x16x32_bf16 v[64:67], v[160:163], v[48:51], 0\n\t"
      "v_mfma_f32_16x16x32_bf16 v[68:71], v[170:173], v[48:51], 0\n\t"
      "v_cvt_pk_bf16_f32 v176, v176, v176\n\t"
      "v_cvt_pk_bf16_f32 v180, v180, v180\n\t"
      "v_and_b32_e32 v0, v240, v176\n\t"
      "v_and_b32_e32 v1, v241, v180\n\t"
      "v_and_b32_e32 v2, v242, v176\n\t"
      "v_and_b32_e32 v3, v243, v180\n\t"
      "v_mfma_f32_16x16x32_bf16 v[40:43], v[158:161], v[24:27], 0\n\t"
      "v_mfma_f32_16x16x32_bf16 v[44:47], v[168:171], v[24:27], 0\n\t"
      "s_add_u32 s48, s4, 98304\n\t"
      "s_addc_u32 s49, s5, 0\n\t"
      "v_mfma_f32_16x16x32_bf16 v[16:19], v[156:159], v[0:3], 0\n\t"
      "v_mfma_f32_16x16x32_bf16 v[20:23], v[166:169], v[0:3], 0\n\t"
      "v_permlane16_swap_b32_e32 v218, v219\n\t"
      "v_permlane16_swap_b32_e32 v216, v217\n\t"
      "v_add_f32_e32 v218, v218, v219\n\t"
      "v_add_f32_e32 v216, v216, v217\n\t"
      "v_mov_b32_e32 v219, v218\n\t"
      "v_mov_b32_e32 v217, v216\n\t"
      "s_nop 0\n\t"
      "v_permlane32_swap_b32_e32 v218, v219\n\t"
      "v_permlane32_swap_b32_e32 v216, v217\n\t"
      "v_add_f32_e32 v218, v218, v219\n\t"
      "v_add_f32_e32 v216, v216, v217\n\t"
      "v_mov_b32_e32 v219, v218\n\t"
      "v_mov_b32_e32 v217, v216\n\t"
      "s_nop 0\n\t"
      "v_permlane16_swap_b32_e32 v218, v219\n\t"
      "v_permlane16_swap_b32_e32 v216, v217\n\t"
      "v_rcp_f32_e32 v190, v219\n\t"
      "v_rcp_f32_e32 v188, v218\n\t"
      "v_rcp_f32_e32 v186, v217\n\t"
      "v_rcp_f32_e32 v184, v216\n\t"
      "v_mov_b32_e32 v191, v190\n\t"
      "v_mov_b32_e32 v189, v188\n\t"
      "v_mov_b32_e32 v187, v186\n\t"
      "v_mov_b32_e32 v185, v184\n\t"
      "v_lshrrev_b32_e32 v252, 2, v239\n\t"
      "v_xad_u32 v252, s13, v197, v252\n\t"
      "v_xad_u32 v196, s46, v196, v239\n\t"
      "v_pk_mul_f32 v[88:89], v[190:191], v[88:89]\n\t"
      "v_pk_mul_f32 v[90:91], v[190:191], v[90:91]\n\t"
      "v_pk_mul_f32 v[92:93], v[190:191], v[92:93]\n\t"
      "v_pk_mul_f32 v[94:95], v[190:191], v[94:95]\n\t"
      "v_cvt_pk_bf16_f32 v80, v88, v89\n\t"
      "v_cvt_pk_bf16_f32 v81, v90, v91\n\t"
      "v_cvt_pk_bf16_f32 v82, v92, v93\n\t"
      "v_cvt_pk_bf16_f32 v83, v94, v95\n\t"
      "global_store_dwordx4 v196, v[80:83], s[48:49] sc1\n\t"
      "s_sub_u32 s48, s48, 32768\n\t"
      "s_subb_u32 s49, s49, 0\n\t"
      "v_add3_u32 v219, v80, v81, v82\n\t"
      "v_add_u32_e32 v219, v219, v83\n\t"
      "v_pk_mul_f32 v[64:65], v[188:189], v[64:65]\n\t"
      "v_pk_mul_f32 v[66:67], v[188:189], v[66:67]\n\t"
      "v_pk_mul_f32 v[68:69], v[188:189], v[68:69]\n\t"
      "v_pk_mul_f32 v[70:71], v[188:189], v[70:71]\n\t"
      "v_cvt_pk_bf16_f32 v56, v64, v65\n\t"
      "v_cvt_pk_bf16_f32 v57, v66, v67\n\t"
      "v_cvt_pk_bf16_f32 v58, v68, v69\n\t"
      "v_cvt_pk_bf16_f32 v59, v70, v71\n\t"
      "global_store_dwordx4 v196, v[56:59], s[48:49] sc1\n\t"
      "s_sub_u32 s48, s48, 32768\n\t"
      "s_subb_u32 s49, s49, 0\n\t"
      "v_add3_u32 v218, v56, v57, v58\n\t"
      "v_add_u32_e32 v218, v218, v59\n\t"
      "v_pk_mul_f32 v[40:41], v[186:187], v[40:41]\n\t"
      "v_pk_mul_f32 v[42:43], v[186:187], v[42:43]\n\t"
      "v_pk_mul_f32 v[44:45], v[186:187], v[44:45]\n\t"
      "v_pk_mul_f32 v[46:47], v[186:187], v[46:47]\n\t"
      "v_cvt_pk_bf16_f32 v32, v40, v41\n\t"
      "v_cvt_pk_bf16_f32 v33, v42, v43\n\t"
      "v_cvt_pk_bf16_f32 v34, v44, v45\n\t"
      "v_cvt_pk_bf16_f32 v35, v46, v47\n\t"
      "global_store_dwordx4 v196, v[32:35], s[48:49] sc1\n\t"
      "s_sub_u32 s48, s48, 32768\n\t"
      "s_subb_u32 s49, s49, 0\n\t"
      "v_add3_u32 v217, v32, v33, v34\n\t"
      "v_add_u32_e32 v217, v217, v35\n\t"
      "s_nop 1\n\t"
      "v_permlane32_swap_b32_e32 v217, v219\n\t"
      "v_add_u32_e32 v217, v217, v219\n\t"
      "v_pk_mul_f32 v[16:17], v[184:185], v[16:17]\n\t"
      "v_pk_mul_f32 v[18:19], v[184:185], v[18:19]\n\t"
      "v_pk_mul_f32 v[20:21], v[184:185], v[20:21]\n\t"
      "v_pk_mul_f32 v[22:23], v[184:185], v[22:23]\n\t"
      "v_cvt_pk_bf16_f32 v8, v16, v17\n\t"
      "v_cvt_pk_bf16_f32 v9, v18, v19\n\t"
      "v_cvt_pk_bf16_f32 v10, v20, v21\n\t"
      "v_cvt_pk_bf16_f32 v11, v22, v23\n\t"
      "global_store_dwordx4 v196, v[8:11], s[48:49] sc1\n\t"
      "v_add3_u32 v216, v8, v9, v10\n\t"
      "v_add_u32_e32 v216, v216, v11\n\t"
      "s_nop 1\n\t"
      "v_permlane32_swap_b32_e32 v216, v218\n\t"
      "v_add_u32_e32 v216, v216, v218\n\t"
      "s_nop 1\n\t"
      "v_permlane16_swap_b32_e32 v216, v217\n\t"
      "v_add3_u32 v216, v216, v217, v250\n\t"
      "global_store_dword v252, v216, s[10:11] sc1\n\t"
      "s_barrier\n\t"
      "s_nop 1\n\t"
      "s_branch CEND_%=\n\t"
      "LOOP1_%=:\n\t"
      "s_waitcnt vmcnt(0)\n\t"
      "s_barrier\n\t"
      "ds_read_b64_tr_b16 v[108:109], v226\n\t"
      "ds_read_b64_tr_b16 v[110:111], v226 offset:512\n\t"
      "ds_read_b128 v[132:135], v224\n\t"
      "ds_read_b128 v[136:139], v224 offset:5120\n\t"
      "ds_read_b128 v[140:143], v224 offset:10240\n\t"
      "ds_read_b128 v[144:147], v224 offset:15360\n\t"
      "ds_read_b64_tr_b16 v[112:113], v227\n\t"
      "ds_read_b64_tr_b16 v[114:115], v227 offset:512\n\t"
      "ds_read_b128 v[148:151], v225\n\t"
      "ds_read_b64_tr_b16 v[116:117], v226 offset:1024\n\t"
      "ds_read_b64_tr_b16 v[118:119], v226 offset:1536\n\t"
      "ds_read_b64_tr_b16 v[120:121], v227 offset:1024\n\t"
      "ds_read_b64_tr_b16 v[122:123], v227 offset:1536\n\t"
      "ds_read_b64_tr_b16 v[124:125], v226 offset:2048\n\t"
      "ds_read_b64_tr_b16 v[126:127], v226 offset:2560\n\t"
      "ds_read_b64_tr_b16 v[128:129], v227 offset:2048\n\t"
      "ds_read_b64_tr_b16 v[130:131], v227 offset:2560\n\t"
      "s_branch EVEN1_%=\n\t"
      "s_waitcnt lgkmcnt(0)\n\t"
      ".p2align 6\n\t"
      "ODD1_%=:\n\t"
      "s_waitcnt lgkmcnt(0)\n\t"
      "v_mfma_f32_16x16x32_bf16 v[0:3], v[152:155], v[176:179], v[0:3]\n\t"
      "v_add_u32_e32 v224, s46, v224\n\t"
      "v_add_u32_e32 v226, s46, v226\n\t"
      "v_mfma_f32_16x16x32_bf16 v[24:27], v[152:155], v[180:183], v[24:27]\n\t"
      "v_add_u32_e32 v227, s46, v227\n\t"
      "v_add_u32_e32 v225, s46, v225\n\t"
      "v_mfma_f32_16x16x32_bf16 v[48:51], v[152:155], v[184:187], v[48:51]\n\t"
      "s_barrier\n\t"
      "ds_read_b64_tr_b16 v[108:109], v226\n\t"
      "v_mfma_f32_16x16x32_bf16 v[72:75], v[152:155], v[188:191], v[72:75]\n\t"
      "ds_read_b64_tr_b16 v[110:111], v226 offset:512\n\t"
      "v_mfma_f32_16x16x32_bf16 v[4:7], v[156:159], v[176:179], v[4:7]\n\t"
      "ds_read_b128 v[132:135], v224\n\t"
      "v_mfma_f32_16x16x32_bf16 v[28:31], v[156:159], v[180:183], v[28:31]\n\t"
      "ds_read_b128 v[136:139], v224 offset:5120\n\t"
      "v_mfma_f32_16x16x32_bf16 v[52:55], v[156:159], v[184:187], v[52:55]\n\t"
      "ds_read_b128 v[140:143], v224 offset:10240\n\t"
      "v_mfma_f32_16x16x32_bf16 v[76:79], v[156:159], v[188:191], v[76:79]\n\t"
      "ds_read_b128 v[144:147], v224 offset:15360\n\t"
      "v_mfma_f32_16x16x32_bf16 v[96:99], v[156:159], v[192:195], v[96:99]\n\t"
      "v_mfma_f32_16x16x32_bf16 v[8:11], v[160:163], v[176:179], v[8:11]\n\t"
      "ds_read_b64_tr_b16 v[112:113], v227\n\t"
      "v_mfma_f32_16x16x32_bf16 v[32:35], v[160:163], v[180:183], v[32:35]\n\t"
      "ds_read_b64_tr_b16 v[114:115], v227 offset:512\n\t"
      "v_mfma_f32_16x16x32_bf16 v[56:59], v[160:163], v[184:187], v[56:59]\n\t"
      "ds_read_b128 v[148:151], v225\n\t"
      "v_mfma_f32_16x16x32_bf16 v[80:83], v[160:163], v[188:191], v[80:83]\n\t"
      "ds_read_b64_tr_b16 v[116:117], v226 offset:1024\n\t"
      "v_mfma_f32_16x16x32_bf16 v[12:15], v[164:167], v[176:179], v[12:15]\n\t"
      "ds_read_b64_tr_b16 v[118:119], v226 offset:1536\n\t"
      "v_mfma_f32_16x16x32_bf16 v[36:39], v[164:167], v[180:183], v[36:39]\n\t"
      "ds_read_b64_tr_b16 v[120:121], v227 offset:1024\n\t"
      "v_mfma_f32_16x16x32_bf16 v[60:63], v[164:167], v[184:187], v[60:63]\n\t"
      "v_mfma_f32_16x16x32_bf16 v[84:87], v[164:167], v[188:191], v[84:87]\n\t"
      "ds_read_b64_tr_b16 v[122:123], v227 offset:1536\n\t"
      "v_mfma_f32_16x16x32_bf16 v[100:103], v[164:167], v[192:195], v[100:103]\n\t"
      "ds_read_b64_tr_b16 v[124:125], v226 offset:2048\n\t"
      "v_mfma_f32_16x16x32_bf16 v[16:19], v[176:179], v[168:171], v[16:19]\n\t"
      "ds_read_b64_tr_b16 v[126:127], v226 offset:2560\n\t"
      "v_mfma_f32_16x16x32_bf16 v[40:43], v[180:183], v[168:171], v[40:43]\n\t"
      "ds_read_b64_tr_b16 v[128:129], v227 offset:2048\n\t"
      "v_mfma_f32_16x16x32_bf16 v[64:67], v[184:187], v[168:171], v[64:67]\n\t"
      "ds_read_b64_tr_b16 v[130:131], v227 offset:2560\n\t"
      "v_mfma_f32_16x16x32_bf16 v[88:91], v[188:191], v[168:171], v[88:91]\n\t"
      "v_mfma_f32_16x16x32_bf16 v[20:23], v[176:179], v[172:175], v[20:23]\n\t"
      "v_mfma_f32_16x16x32_bf16 v[44:47], v[180:183], v[172:175], v[44:47]\n\t"
      "v_add_u32_e32 v248, s46, v248\n\t"
      "v_add_u32_e32 v249, s46, v249\n\t"
      "v_mfma_f32_16x16x32_bf16 v[68:71], v[184:187], v[172:175], v[68:71]\n\t"
      "v_mfma_f32_16x16x32_bf16 v[92:95], v[188:191], v[172:175], v[92:95]\n\t"
      "v_mfma_f32_16x16x32_bf16 v[104:107], v[192:195], v[172:175], v[104:107]\n\t"
      "EVEN1_%=:\n\t"
      "s_waitcnt lgkmcnt(0)\n\t"
      "v_mfma_f32_16x16x32_bf16 v[0:3], v[108:111], v[132:135], v[0:3]\n\t"
      "ds_read_b64_tr_b16 v[152:153], v226 offset:20480\n\t"
      "v_mfma_f32_16x16x32_bf16 v[24:27], v[108:111], v[136:139], v[24:27]\n\t"
      "ds_read_b64_tr_b16 v[154:155], v226 offset:20992\n\t"
      "v_mfma_f32_16x16x32_bf16 v[48:51], v[108:111], v[140:143], v[48:51]\n\t"
      "ds_read_b128 v[176:179], v248\n\t"
      "v_mfma_f32_16x16x32_bf16 v[72:75], v[108:111], v[144:147], v[72:75]\n\t"
      "ds_read_b128 v[180:183], v248 offset:5120\n\t"
      "v_mfma_f32_16x16x32_bf16 v[4:7], v[112:115], v[132:135], v[4:7]\n\t"
      "v_mfma_f32_16x16x32_bf16 v[28:31], v[112:115], v[136:139], v[28:31]\n\t"
      "ds_read_b128 v[184:187], v248 offset:10240\n\t"
      "v_mfma_f32_16x16x32_bf16 v[52:55], v[112:115], v[140:143], v[52:55]\n\t"
      "ds_read_b128 v[188:191], v248 offset:15360\n\t"
      "v_mfma_f32_16x16x32_bf16 v[76:79], v[112:115], v[144:147], v[76:79]\n\t"
      "ds_read_b64_tr_b16 v[156:157], v227 offset:20480\n\t"
      "v_mfma_f32_16x16x32_bf16 v[96:99], v[112:115], v[148:151], v[96:99]\n\t"
      "v_mfma_f32_16x16x32_bf16 v[8:11], v[116:119], v[132:135], v[8:11]\n\t"
      "ds_read_b64_tr_b16 v[158:159], v227 offset:20992\n\t"
      "v_mfma_f32_16x16x32_bf16 v[32:35], v[116:119], v[136:139], v[32:35]\n\t"
      "ds_read_b128 v[192:195], v249\n\t"
      "v_mfma_f32_16x16x32_bf16 v[56:59], v[116:119], v[140:143], v[56:59]\n\t"
      "ds_read_b64_tr_b16 v[160:161], v226 offset:21504\n\t"
      "v_mfma_f32_16x16x32_bf16 v[80:83], v[116:119], v[144:147], v[80:83]\n\t"
      "ds_read_b64_tr_b16 v[162:163], v226 offset:22016\n\t"
      "v_mfma_f32_16x16x32_bf16 v[12:15], v[120:123], v[132:135], v[12:15]\n\t"
      "v_mfma_f32_16x16x32_bf16 v[36:39], v[120:123], v[136:139], v[36:39]\n\t"
      "ds_read_b64_tr_b16 v[164:165], v227 offset:21504\n\t"
      "v_mfma_f32_16x16x32_bf16 v[60:63], v[120:123], v[140:143], v[60:63]\n\t"
      "ds_read_b64_tr_b16 v[166:167], v227 offset:22016\n\t"
      "v_mfma_f32_16x16x32_bf16 v[84:87], v[120:123], v[144:147], v[84:87]\n\t"
      "ds_read_b64_tr_b16 v[168:169], v226 offset:22528\n\t"
      "v_mfma_f32_16x16x32_bf16 v[100:103], v[120:123], v[148:151], v[100:103]\n\t"
      "v_mfma_f32_16x16x32_bf16 v[16:19], v[132:135], v[124:127], v[16:19]\n\t"
      "ds_read_b64_tr_b16 v[170:171], v226 offset:23040\n\t"
      "v_mfma_f32_16x16x32_bf16 v[40:43], v[136:139], v[124:127], v[40:43]\n\t"
      "ds_read_b64_tr_b16 v[172:173], v227 offset:22528\n\t"
      "v_mfma_f32_16x16x32_bf16 v[64:67], v[140:143], v[124:127], v[64:67]\n\t"
      "ds_read_b64_tr_b16 v[174:175], v227 offset:23040\n\t"
      "s_cmp_eq_u32 s47, 4\n\t"
      "s_cselect_b32 s46, -2, 1\n\t"
      "v_mfma_f32_16x16x32_bf16 v[88:91], v[144:147], v[124:127], v[88:91]\n\t"
      "s_cmp_eq_u32 s47, 1\n\t"
      "s_cselect_b32 s46, -2, s46\n\t"
      "v_mfma_f32_16x16x32_bf16 v[20:23], v[132:135], v[128:131], v[20:23]\n\t"
      "s_mul_i32 s46, s46, 43008\n\t"
      "v_mfma_f32_16x16x32_bf16 v[44:47], v[136:139], v[128:131], v[44:47]\n\t"
      "v_mfma_f32_16x16x32_bf16 v[68:71], v[140:143], v[128:131], v[68:71]\n\t"
      "v_mfma_f32_16x16x32_bf16 v[92:95], v[144:147], v[128:131], v[92:95]\n\t"
      "s_sub_u32 s47, s47, 1\n\t"
      "v_mfma_f32_16x16x32_bf16 v[104:107], v[148:151], v[128:131], v[104:107]\n\t"
      "s_cbranch_scc0 ODD1_%=\n\t"
      "s_waitcnt lgkmcnt(0)\n\t"
      "v_mfma_f32_16x16x32_bf16 v[96:99], v[156:159], v[192:195], v[96:99]\n\t"
      "v_mov_b32_e32 v212, s9\n\t"
      "ds_read_b32 v212, v212\n\t"
      "ds_read2_b32 v[128:129], v234 offset0:32 offset1:32\n\t"
      "v_mfma_f32_16x16x32_bf16 v[100:103], v[164:167], v[192:195], v[100:103]\n\t"
      "ds_read2_b32 v[130:131], v234 offset0:48 offset1:48\n\t"
      "v_mfma_f32_16x16x32_bf16 v[104:107], v[192:195], v[172:175], v[104:107]\n\t"
      "ds_read2_b32 v[132:133], v234 offset0:64 offset1:64\n\t"
      "ds_read2_b32 v[134:135], v234 offset0:80 offset1:80\n\t"
      "v_mfma_f32_16x16x32_bf16 v[80:83], v[160:163], v[188:191], v[80:83]\n\t"
      "ds_read2_b32 v[248:249], v234 offset0:0 offset1:0\n\t"
      "v_mfma_f32_16x16x32_bf16 v[84:87], v[164:167], v[188:191], v[84:87]\n\t"
      "ds_read_b128 v[196:199], v233\n\t"
      "ds_read_b128 v[136:139], v233 offset:128\n\t"
      "v_mfma_f32_16x16x32_bf16 v[88:91], v[188:191], v[168:171], v[88:91]\n\t"
      "ds_read_b128 v[140:143], v233 offset:192\n\t"
      "v_mfma_f32_16x16x32_bf16 v[92:95], v[188:191], v[172:175], v[92:95]\n\t"
      "ds_read_b128 v[144:147], v233 offset:256\n\t"
      "ds_read_b128 v[148:151], v233 offset:320\n\t"
      "v_mfma_f32_16x16x32_bf16 v[56:59], v[160:163], v[184:187], v[56:59]\n\t"
      "s_mov_b64 exec, s[44:45]\n\t"
      "ds_write_b128 v230, v[96:99]\n\t"
      "ds_write_b128 v230, v[100:103] offset:128\n\t"
      "s_mov_b64 exec, 0xffff\n\t"
      "ds_write_b32 v231, v104 offset:256\n\t"
      "v_xor_b32_e32 v227, 8, v232\n\t"
      "ds_write_b32 v227, v227\n\t"
      "s_mov_b64 exec, -1\n\t"
      "v_mfma_f32_16x16x32_bf16 v[60:63], v[164:167], v[184:187], v[60:63]\n\t"
      "v_mfma_f32_16x16x32_bf16 v[64:67], v[184:187], v[168:171], v[64:67]\n\t"
      "v_mfma_f32_16x16x32_bf16 v[68:71], v[184:187], v[172:175], v[68:71]\n\t"
      "s_waitcnt lgkmcnt(4)\n\t"
      "v_mfma_f32_16x16x32_bf16 v[32:35], v[160:163], v[180:183], v[32:35]\n\t"
      "ds_read_b32 v226, v232\n\t"
      "ds_read_b128 v[116:119], v228 offset:128\n\t"
      "v_mfma_f32_16x16x32_bf16 v[36:39], v[164:167], v[180:183], v[36:39]\n\t"
      "ds_read_b128 v[120:123], v228 offset:192\n\t"
      "ds_read2_b32 v[250:251], v229 offset0:64 offset1:64\n\t"
      "v_mfma_f32_16x16x32_bf16 v[40:43], v[180:183], v[168:171], v[40:43]\n\t"
      "ds_read2_b32 v[252:253], v229 offset0:80 offset1:80\n\t"
      "ds_read_b128 v[124:127], v230 offset:128\n\t"
      "v_mfma_f32_16x16x32_bf16 v[44:47], v[180:183], v[172:175], v[44:47]\n\t"
      "ds_read2_b32 v[224:225], v231 offset0:64 offset1:64\n\t"
      "ds_read_b128 v[108:111], v228\n\t"
      "v_mfma_f32_16x16x32_bf16 v[8:11], v[160:163], v[176:179], v[8:11]\n\t"
      "ds_read_b128 v[112:115], v228 offset:64\n\t"
      "ds_read2_b32 v[204:205], v235 offset0:0 offset1:16\n\t"
      "v_mfma_f32_16x16x32_bf16 v[12:15], v[164:167], v[176:179], v[12:15]\n\t"
      "ds_read2_b32 v[206:207], v235 offset0:32 offset1:48\n\t"
      "ds_read2_b32 v[208:209], v254 offset0:0 offset1:16\n\t"
      "v_mfma_f32_16x16x32_bf16 v[16:19], v[176:179], v[168:171], v[16:19]\n\t"
      "ds_read2_b32 v[210:211], v254 offset0:32 offset1:48\n\t"
      "ds_read2_b32 v[200:201], v234 offset0:192 offset1:208\n\t"
      "v_mfma_f32_16x16x32_bf16 v[20:23], v[176:179], v[172:175], v[20:23]\n\t"
      "ds_read2_b32 v[202:203], v234 offset0:224 offset1:240\n\t"
      "s_branch LOOPX_%=\n\t"
      "XAR_%=:\n\t"
      "s_mov_b32 s51, 0\n\t"
      "XAL_%=:\n\t"
      "s_add_u32 s51, s51, 1\n\t"
      "s_cmp_lt_u32 s51, 262144\n\t"
      "s_cbranch_scc0 XAX_%=\n\t"
      "s_waitcnt lgkmcnt(0)\n\t"
      "v_mov_b32_e32 v212, s9\n\t"
      "ds_read_b32 v212, v212\n\t"
      "ds_read2_b32 v[128:129], v234 offset0:32 offset1:32\n\t"
      "ds_read2_b32 v[130:131], v234 offset0:48 offset1:48\n\t"
      "ds_read2_b32 v[132:133], v234 offset0:64 offset1:64\n\t"
      "ds_read2_b32 v[134:135], v234 offset0:80 offset1:80\n\t"
      "ds_read2_b32 v[248:249], v234 offset0:0 offset1:0\n\t"
      "ds_read_b128 v[196:199], v233\n\t"
      "ds_read_b128 v[136:139], v233 offset:128\n\t"
      "ds_read_b128 v[140:143], v233 offset:192\n\t"
      "ds_read_b128 v[144:147], v233 offset:256\n\t"
      "ds_read_b128 v[148:151], v233 offset:320\n\t"
      "s_waitcnt lgkmcnt(0)\n\t"
      "ds_read_b32 v226, v232\n\t"
      "ds_read_b128 v[116:119], v228 offset:128\n\t"
      "ds_read_b128 v[120:123], v228 offset:192\n\t"
      "ds_read2_b32 v[250:251], v229 offset0:64 offset1:64\n\t"
      "ds_read2_b32 v[252:253], v229 offset0:80 offset1:80\n\t"
      "ds_read_b128 v[124:127], v230 offset:128\n\t"
      "ds_read2_b32 v[224:225], v231 offset0:64 offset1:64\n\t"
      "ds_read_b128 v[108:111], v228\n\t"
      "ds_read_b128 v[112:115], v228 offset:64\n\t"
      "ds_read2_b32 v[204:205], v235 offset0:0 offset1:16\n\t"
      "ds_read2_b32 v[206:207], v235 offset0:32 offset1:48\n\t"
      "ds_read2_b32 v[208:209], v254 offset0:0 offset1:16\n\t"
      "ds_read2_b32 v[210:211], v254 offset0:32 offset1:48\n\t"
      "ds_read2_b32 v[200:201], v234 offset0:192 offset1:208\n\t"
      "ds_read2_b32 v[202:203], v234 offset0:224 offset1:240\n\t"
      "s_waitcnt lgkmcnt(0)\n\t"
      "v_min_u32_e32 v226, v226, v212\n\t"
      "v_cmp_eq_u32_e32 vcc, 0, v226\n\t"
      "s_cbranch_vccnz XAL_%=\n\t"
      "XAX_%=:\n\t"
      "s_branch XAB_%=\n\t"
      "XBR_%=:\n\t"
      "s_mov_b32 s51, 0\n\t"
      "XBL_%=:\n\t"
      "s_add_u32 s51, s51, 1\n\t"
      "s_cmp_lt_u32 s51, 262144\n\t"
      "s_cbranch_scc0 XBX_%=\n\t"
      "s_waitcnt lgkmcnt(0)\n\t"
      "ds_read_b32 v226, v232 offset:4\n\t"
      "ds_read_b128 v[152:155], v236\n\t"
      "ds_read_b64 v[156:157], v237 offset:1024\n\t"
      "ds_read_b64 v[166:167], v237 offset:1536\n\t"
      "s_waitcnt lgkmcnt(0)\n\t"
      "v_cmp_eq_u32_e32 vcc, 0, v226\n\t"
      "s_cbranch_vccnz XBL_%=\n\t"
      "XBX_%=:\n\t"
      "s_branch XBB_%=\n\t"
      "CEND_%=:\n\t"
      : "+{s[4:11]}"(s), "+{s12}"(hint), "+{s13}"(chk), "+{v[224:239]}"(v), "+{v254}"(vk1)
      :
      : "memory", "vcc", "scc",
        "v0", "v1", "v2", "v3", "v4", "v5", "v6", "v7", "v8", "v9", "v10", "v11", "v12", "v13", "v14", "v15", "v16",
        "v17", "v18", "v19", "v20", "v21", "v22", "v23", "v24", "v25", "v26", "v27", "v28", "v29", "v30", "v31", "v32",
        "v33", "v34", "v35", "v36", "v37", "v38", "v39", "v40", "v41", "v42", "v43", "v44", "v45", "v46", "v47", "v48",
        "v49", "v50", "v51", "v52", "v53", "v54", "v55", "v56", "v57", "v58", "v59", "v60", "v61", "v62", "v63", "v64",
        "v65", "v66", "v67", "v68", "v69", "v70", "v71", "v72", "v73", "v74", "v75", "v76", "v77", "v78", "v79", "v80",
        "v81", "v82", "v83", "v84", "v85", "v86", "v87", "v88", "v89", "v90", "v91", "v92", "v93", "v94", "v95", "v96",
        "v97", "v98", "v99", "v100", "v101", "v102", "v103", "v104", "v105", "v106", "v107", "v108", "v109", "v110",
        "v111", "v112", "v113", "v114", "v115", "v116", "v117", "v118", "v119", "v120", "v121", "v122", "v123", "v124",
        "v125", "v126", "v127", "v128", "v129", "v130", "v131", "v132", "v133", "v134", "v135", "v136", "v137", "v138",
        "v139", "v140", "v141", "v142", "v143", "v144", "v145", "v146", "v147", "v148", "v149", "v150", "v151", "v152",
        "v153", "v154", "v155", "v156", "v157", "v158", "v159", "v160", "v161", "v162", "v163", "v164", "v165", "v166",
        "v167", "v168", "v169", "v170", "v171", "v172", "v173", "v174", "v175", "v176", "v177", "v178", "v179", "v180",
        "v181", "v182", "v183", "v184", "v185", "v186", "v187", "v188", "v189", "v190", "v191", "v192", "v193", "v194",
        "v195", "v196", "v197", "v198", "v199", "v200", "v201", "v202", "v203", "v204", "v205", "v206", "v207", "v208",
        "v209", "v210", "v211", "v212", "v213", "v214", "v215", "v216", "v217", "v218", "v219", "v220", "v221", "v222",
        "v223", "v240", "v241", "v242", "v243", "v244", "v245", "v246", "v247", "v248", "v249", "v250", "v251", "v252",
        "v253", "s36", "s37", "s38", "s39", "s40", "s41", "s42", "s43", "s44", "s45", "s46", "s47", "s48", "s49", "s50",
        "s51", "s52", "s53", "s54", "s55"
    );
    } else {
      LANE_BITS(threadIdx.x & 63);
      u32x8 v;
      u32x16 s;
      v[0] = ((8 * rt + 4 * half + g) * 16 + c) * 16;
      const int hi = -(lane >> 5), m3 = min(c, 2);
      v[1] = (((max(8 * rt + ((g & 1) ? 0 : 4 * half) - 1, 0) * 16 + c) * 16) & ~hi) | ((ST_PW + xcd * 128 + m3 * 16) & hi);
      v[2] = sta + 128 + lane * 4;
      v[3] = ((sta + ((g & 1) ? 0 : 64) + c * 4) & ~hi) | ((A_MK + m3 * 4) & hi);
      v[4] = lr * A_SLOT + A_HALO + lc * 16;
      v[6] = (side * 1024 + (idx * 4 + wave - 4) * 8 + lr) * 3584 + (7 * xp + min(lc, 6)) * 128;
      v[7] = v[6] + (1 - 2 * side) * 1024 * 3584;
      u64 tw = (u64)w1;
      const int n = idx * 4 + wave - 4;
      u64 pf = (u64)(n < 14 ? b1 + xp * 896 + n * 64 : n < 30 ? b2 + xp * 1024 + (n - 14) * 64 : n < 38 ? w1 + (n - 30) * 1000000
                : n < 46 ? w2 + (n - 38) * 1000000 : n < 48 ? out + (n - 46) * 1800000 : ws + (n & 1) * (4 << 20) + (4 << 20) - 4096);
      s[4] = UNI(idx == 15 && xcd < 3 && wave == 4 ? (xcd == 0 ? WS_O : xcd == 1 ? WS_HN + 16 : WS_G + 32) + PROBE : 0);
      v[5] = lane * (8192 + 640);
      put(s, 0, (u64)(x + (rt * 128 * 896 + 128) - (16 * 896 + 1024)));
      put(s, 2, (u64)(wqkv + (hp * 112 + 64 * 2688) - 4096));
      s[5] = wave * A_UNIT;
      put2(s, 6, wave > 4 ? 0xffffffffu : 0xffffff00u, 0u);
      s[8] = A_HALO + (wave - 4) * 512;
      s[9] = A_FL + 32 + 4 * half;
      put(s, 10, (u64)(ws + WS_ST));
    asm volatile(
      "s_waitcnt lgkmcnt(0)\n\ts_nop 4\n\t"
      "s_load_dwordx2 s[18:19], s[14:15], 0x20000\n\t"
      "s_sub_u32 s40, s4, 57344\n\t"
      "s_subb_u32 s41, s5, 0\n\t"
      "s_sub_u32 s42, s6, 86016\n\t"
      "s_subb_u32 s43, s7, 0\n\t"
      "s_setprio 2\n\t"
      "v_mov_b32_e32 v0, 0x3f803f80\n\t"
      "v_mov_b32_e32 v1, 0x3f803f80\n\t"
      "v_mov_b32_e32 v2, 0x3f803f80\n\t"
      "v_mov_b32_e32 v3, 0x3f803f80\n\t"
      "s_mov_b64 exec, 0xffffff\n\t"
      "ds_write_b128 v204, v[0:3]\n\t"
      "s_mov_b64 exec, -1\n\t"
      "v_mov_b32_e32 v32, 154624\n\t"
      "v_mov_b32_e32 v33, s13\n\t"
      "v_mov_b32_e32 v36, 0\n\t"
      "v_mov_b32_e32 v37, 0\n\t"
      "v_mov_b32_e32 v38, 0\n\t"
      "v_mov_b32_e32 v39, 0\n\t"
      "ds_write_b128 v32, v[36:39]\n\t"
      "ds_write_b128 v32, v[36:39] offset:16\n\t"
      "ds_write_b128 v32, v[36:39] offset:32\n\t"
      "s_cmp_eq_u32 s8, 0\n\t"
      "s_cbranch_scc1 PRt_%=\n\t"
      "s_add_u32 s52, s14, s8\n\t"
      "s_addc_u32 s53, s15, 0\n\t"
      "s_mov_b32 s54, 0\n\t"
      "s_mov_b64 s[56:57], -1\n\t"
      "s_load_dword s50, s[52:53], 0x1f80\n\t"
      "PRt_%=:\n\t"
      "s_cmp_eq_u32 s8, 0\n\t"
      "s_cbranch_scc1 PRu_%=\n\t"
      "s_mov_b64 exec, 3\n\t"
      "global_store_dword v205, v0, s[52:53] sc1\n\t"
      "s_mov_b64 exec, -1\n\t"
      "PRu_%=:\n\t"
      "s_add_u32 m0, s9, 43008\n\t"
      "s_nop 0\n\t"
      "global_load_lds_dwordx4 v195, s[6:7] offset:2048\n\t"
      "global_load_lds_dwordx4 v192, s[4:5]\n\t"
      "global_load_lds_dwordx4 v193, s[4:5] offset:1024\n\t"
      "global_load_lds_dwordx4 v196, s[6:7] offset:3072\n\t"
      "s_add_u32 m0, s9, 47104\n\t"
      "s_nop 0\n\t"
      "global_load_lds_dwordx4 v197, s[6:7]\n\t"
      "s_add_u32 m0, s12, 43008\n\t"
      "s_mov_b64 exec, s[10:11]\n\t"
      "global_load_lds_dwordx4 v194, s[4:5]\n\t"
      "s_mov_b64 exec, -1\n\t"
      "s_waitcnt lgkmcnt(0)\n\t"
      "s_add_u32 s18, s18, 1\n\t"
      "s_addc_u32 s19, s19, 0\n\t"
      "s_mul_i32 s48, s18, 0x9e3779b9\n\t"
      "s_mul_hi_u32 s17, s18, 0x7f4a7c15\n\t"
      "s_add_u32 s17, s17, s48\n\t"
      "s_mul_i32 s48, s19, 0x7f4a7c15\n\t"
      "s_add_u32 s17, s17, s48\n\t"
      "s_mul_i32 s16, s18, 0x7f4a7c15\n\t"
      "s_add_u32 s16, s16, 0xd192ed03\n\t"
      "s_addc_u32 s17, s17, 0xd1b54a32\n\t"
      "v_mov_b32_e32 v12, s16\n\t"
      "v_mov_b32_e32 v13, s17\n\t"
      "v_mov_b32_e32 v14, s18\n\t"
      "v_mov_b32_e32 v15, s19\n\t"
      "v_mov_b32_e32 v16, 163600\n\t"
      "s_mov_b64 exec, 1\n\t"
      "ds_write_b128 v16, v[12:15]\n\t"
      "s_mov_b64 exec, -1\n\t"
      "s_and_b32 s47, s8, 48\n\t"
      "s_add_u32 s47, s47, 114688\n\t"
      "v_mbcnt_lo_u32_b32 v28, -1, 0\n\t"
      "v_mov_b32_e32 v25, 0\n\t"
      "v_mov_b32_e32 v26, s16\n\t"
      "v_mov_b32_e32 v27, s17\n\t"
      "v_lshl_add_u32 v28, v28, 7, s47\n\t"
      "s_waitcnt vmcnt(11) lgkmcnt(0)\n\t"
      "s_barrier\n\t"
      "s_cmp_eq_u32 s8, 0\n\t"
      "s_cbranch_scc1 PRC0_%=\n\t"
      "s_mov_b32 s55, 8832\n\t"
      "s_call_b64 s[44:45], PRB_%=\n\t"
      "PRC0_%=:\n\t"
      "s_add_u32 s4, s4, 128\n\t"
      "s_addc_u32 s5, s5, 0\n\t"
      "s_add_u32 s6, s6, 172032\n\t"
      "s_addc_u32 s7, s7, 0\n\t"
      "s_add_u32 s40, s40, 128\n\t"
      "s_addc_u32 s41, s41, 0\n\t"
      "s_add_u32 s42, s42, 172032\n\t"
      "s_addc_u32 s43, s43, 0\n\t"
      "s_add_u32 m0, s9, 65536\n\t"
      "s_nop 0\n\t"
      "global_load_lds_dwordx4 v195, s[42:43] offset:2048\n\t"
      "global_load_lds_dwordx4 v192, s[40:41]\n\t"
      "global_load_lds_dwordx4 v193, s[40:41] offset:1024\n\t"
      "global_load_lds_dwordx4 v196, s[42:43] offset:3072\n\t"
      "s_add_u32 m0, s9, 69632\n\t"
      "s_nop 0\n\t"
      "global_load_lds_dwordx4 v197, s[42:43]\n\t"
      "s_add_u32 m0, s9, 86016\n\t"
      "s_nop 0\n\t"
      "global_load_lds_dwordx4 v195, s[6:7] offset:2048\n\t"
      "global_load_lds_dwordx4 v192, s[4:5]\n\t"
      "global_load_lds_dwordx4 v193, s[4:5] offset:1024\n\t"
      "global_load_lds_dwordx4 v196, s[6:7] offset:3072\n\t"
      "s_add_u32 m0, s9, 90112\n\t"
      "s_nop 0\n\t"
      "global_load_lds_dwordx4 v197, s[6:7]\n\t"
      "s_add_u32 m0, s12, 86016\n\t"
      "s_mov_b64 exec, s[10:11]\n\t"
      "global_load_lds_dwordx4 v194, s[4:5]\n\t"
      "s_mov_b64 exec, -1\n\t"
      "s_waitcnt vmcnt(11)\n\t"
      "s_barrier\n\t"
      "s_cmp_eq_u32 s8, 0\n\t"
      "s_cbranch_scc1 PRC1_%=\n\t"
      "s_mov_b32 s55, 17664\n\t"
      "s_call_b64 s[44:45], PRB_%=\n\t"
      "PRC1_%=:\n\t"
      "s_add_u32 s4, s4, 128\n\t"
      "s_addc_u32 s5, s5, 0\n\t"
      "s_add_u32 s6, s6, 172032\n\t"
      "s_addc_u32 s7, s7, 0\n\t"
      "s_add_u32 s40, s40, 128\n\t"
      "s_addc_u32 s41, s41, 0\n\t"
      "s_add_u32 s42, s42, 172032\n\t"
      "s_addc_u32 s43, s43, 0\n\t"
      "s_add_u32 m0, s9, 4294946816\n\t"
      "s_nop 0\n\t"
      "global_load_lds_dwordx4 v195, s[42:43] offset:2048\n\t"
      "global_load_lds_dwordx4 v192, s[40:41]\n\t"
      "global_load_lds_dwordx4 v193, s[40:41] offset:1024\n\t"
      "global_load_lds_dwordx4 v196, s[42:43] offset:3072\n\t"
      "s_add_u32 m0, s9, 4294950912\n\t"
      "s_nop 0\n\t"
      "global_load_lds_dwordx4 v197, s[42:43]\n\t"
      "s_add_u32 m0, s9, 0\n\t"
      "s_nop 0\n\t"
      "global_load_lds_dwordx4 v195, s[6:7] offset:2048\n\t"
      "global_load_lds_dwordx4 v192, s[4:5]\n\t"
      "global_load_lds_dwordx4 v193, s[4:5] offset:1024\n\t"
      "global_load_lds_dwordx4 v196, s[6:7] offset:3072\n\t"
      "s_add_u32 m0, s9, 4096\n\t"
      "s_nop 0\n\t"
      "global_load_lds_dwordx4 v197, s[6:7]\n\t"
      "s_add_u32 m0, s12, 0\n\t"
      "s_mov_b64 exec, s[10:11]\n\t"
      "global_load_lds_dwordx4 v194, s[4:5]\n\t"
      "s_mov_b64 exec, -1\n\t"
      "s_cmp_eq_u32 s8, 0\n\t"
      "s_cbranch_scc1 PRS_%=\n\t"
      "s_cmp_lt_u32 s57, s56\n\t"
      "s_cselect_b32 s46, 0x2000, 0\n\t"
      "v_mov_b32_e32 v24, s46\n\t"
      "v_xor_b32_e32 v26, s46, v26\n\t"
      "s_mov_b64 exec, 0xff\n\t"
      "global_store_dwordx4 v28, v[24:27], s[14:15] sc1\n\t"
      "s_mov_b64 exec, -1\n\t"
      "PRS_%=:\n\t"
      "s_waitcnt vmcnt(11)\n\t"
      "s_barrier\n\t"
      "s_add_u32 s4, s4, 128\n\t"
      "s_addc_u32 s5, s5, 0\n\t"
      "s_add_u32 s6, s6, 172032\n\t"
      "s_addc_u32 s7, s7, 0\n\t"
      "s_add_u32 s40, s40, 128\n\t"
      "s_addc_u32 s41, s41, 0\n\t"
      "s_add_u32 s42, s42, 172032\n\t"
      "s_addc_u32 s43, s43, 0\n\t"
      "s_add_u32 m0, s9, 22528\n\t"
      "s_nop 0\n\t"
      "global_load_lds_dwordx4 v195, s[42:43] offset:2048\n\t"
      "global_load_lds_dwordx4 v192, s[40:41]\n\t"
      "global_load_lds_dwordx4 v193, s[40:41] offset:1024\n\t"
      "global_load_lds_dwordx4 v196, s[42:43] offset:3072\n\t"
      "s_add_u32 m0, s9, 26624\n\t"
      "s_nop 0\n\t"
      "global_load_lds_dwordx4 v197, s[42:43]\n\t"
      "s_add_u32 m0, s9, 43008\n\t"
      "s_nop 0\n\t"
      "global_load_lds_dwordx4 v195, s[6:7] offset:2048\n\t"
      "global_load_lds_dwordx4 v192, s[4:5]\n\t"
      "global_load_lds_dwordx4 v193, s[4:5] offset:1024\n\t"
      "global_load_lds_dwordx4 v196, s[6:7] offset:3072\n\t"
      "s_add_u32 m0, s9, 47104\n\t"
      "s_nop 0\n\t"
      "global_load_lds_dwordx4 v197, s[6:7]\n\t"
      "s_add_u32 m0, s12, 43008\n\t"
      "s_mov_b64 exec, s[10:11]\n\t"
      "global_load_lds_dwordx4 v194, s[4:5]\n\t"
      "s_mov_b64 exec, -1\n\t"
      "s_load_dword s50, s[22:23], 0x0\n\t"
      "s_waitcnt vmcnt(11)\n\t"
      "s_barrier\n\t"
      "s_add_u32 s4, s4, 128\n\t"
      "s_addc_u32 s5, s5, 0\n\t"
      "s_add_u32 s6, s6, 172032\n\t"
      "s_addc_u32 s7, s7, 0\n\t"
      "s_add_u32 s40, s40, 128\n\t"
      "s_addc_u32 s41, s41, 0\n\t"
      "s_add_u32 s42, s42, 172032\n\t"
      "s_addc_u32 s43, s43, 0\n\t"
      "s_add_u32 m0, s9, 65536\n\t"
      "s_nop 0\n\t"
      "global_load_lds_dwordx4 v195, s[42:43] offset:2048\n\t"
      "global_load_lds_dwordx4 v192, s[40:41]\n\t"
      "global_load_lds_dwordx4 v193, s[40:41] offset:1024\n\t"
      "global_load_lds_dwordx4 v196, s[42:43] offset:3072\n\t"
      "s_add_u32 m0, s9, 69632\n\t"
      "s_nop 0\n\t"
      "global_load_lds_dwordx4 v197, s[42:43]\n\t"
      "s_add_u32 m0, s9, 86016\n\t"
      "s_nop 0\n\t"
      "global_load_lds_dwordx4 v195, s[6:7] offset:2048\n\t"
      "global_load_lds_dwordx4 v192, s[4:5]\n\t"
      "global_load_lds_dwordx4 v193, s[4:5] offset:1024\n\t"
      "global_load_lds_dwordx4 v196, s[6:7] offset:3072\n\t"
      "s_add_u32 m0, s9, 90112\n\t"
      "s_nop 0\n\t"
      "global_load_lds_dwordx4 v197, s[6:7]\n\t"
      "s_add_u32 m0, s12, 86016\n\t"
      "s_mov_b64 exec, s[10:11]\n\t"
      "global_load_lds_dwordx4 v194, s[4:5]\n\t"
      "s_mov_b64 exec, -1\n\t"
      "s_waitcnt vmcnt(11)\n\t"
      "s_barrier\n\t"
      "s_add_u32 s4, s4, 128\n\t"
      "s_addc_u32 s5, s5, 0\n\t"
      "s_add_u32 s6, s6, 172032\n\t"
      "s_addc_u32 s7, s7, 0\n\t"
      "s_add_u32 s40, s40, 128\n\t"
      "s_addc_u32 s41, s41, 0\n\t"
      "s_add_u32 s42, s42, 172032\n\t"
      "s_addc_u32 s43, s43, 0\n\t"
      "s_add_u32 m0, s9, 4294946816\n\t"
      "s_nop 0\n\t"
      "global_load_lds_dwordx4 v195, s[42:43] offset:2048\n\t"
      "global_load_lds_dwordx4 v192, s[40:41]\n\t"
      "global_load_lds_dwordx4 v193, s[40:41] offset:1024\n\t"
      "global_load_lds_dwordx4 v196, s[42:43] offset:3072\n\t"
      "s_add_u32 m0, s9, 4294950912\n\t"
      "s_nop 0\n\t"
      "global_load_lds_dwordx4 v197, s[42:43]\n\t"
      "s_add_u32 m0, s9, 0\n\t"
      "s_nop 0\n\t"
      "global_load_lds_dwordx4 v195, s[6:7] offset:2048\n\t"
      "global_load_lds_dwordx4 v192, s[4:5]\n\t"
      "global_load_lds_dwordx4 v193, s[4:5] offset:1024\n\t"
      "global_load_lds_dwordx4 v196, s[6:7] offset:3072\n\t"
      "s_add_u32 m0, s9, 4096\n\t"
      "s_nop 0\n\t"
      "global_load_lds_dwordx4 v197, s[6:7]\n\t"
      "s_add_u32 m0, s12, 0\n\t"
      "s_mov_b64 exec, s[10:11]\n\t"
      "global_load_lds_dwordx4 v194, s[4:5]\n\t"
      "s_mov_b64 exec, -1\n\t"
      "global_load_dwordx4 v[4:7], v200, s[14:15] sc1\n\t"
      "global_load_dwordx4 v[8:11], v201, s[14:15] sc1\n\t"
      "s_mov_b32 m0, 163328\n\t"
      "s_add_u32 s44, s20, 0\n\t"
      "s_addc_u32 s45, s21, 0\n\t"
      "global_load_lds_dword v206, s[44:45]\n\t"
      "s_waitcnt vmcnt(14)\n\t"
      "s_barrier\n\t"
      "s_mov_b32 m0, 163328\n\t"
      "s_add_u32 s44, s20, 1835008\n\t"
      "s_addc_u32 s45, s21, 0\n\t"
      "global_load_lds_dword v206, s[44:45]\n\t"
      "s_waitcnt vmcnt(4)\n\t"
      "s_barrier\n\t"
      "s_waitcnt vmcnt(2)\n\t"
      "v_xor_b32_e32 v6, v6, v4\n\t"
      "v_xor_b32_e32 v7, v7, v5\n\t"
      "v_xor_b32_e32 v10, v10, v8\n\t"
      "v_xor_b32_e32 v11, v11, v9\n\t"
      "v_cmp_ne_u64_e64 s[48:49], s[16:17], v[6:7]\n\t"
      "v_cmp_ne_u64_e32 vcc, s[16:17], v[10:11]\n\t"
      "s_nop 0\n\t"
      "s_or_b64 vcc, vcc, s[48:49]\n\t"
      "s_cbranch_scc1 GRR_%=\n\t"
      "GRB_%=:\n\t"
      "v_mul_f32_e32 v6, 0x3e8b97f6, v5\n\t"
      "ds_write2_b32 v202, v4, v5 offset0:0 offset1:96\n\t"
      "ds_write2_b32 v203, v8, v9 offset0:0 offset1:96\n\t"
      "ds_write_b32 v202, v6 offset:640\n\t"
      "s_mov_b64 exec, 1\n\t"
      "ds_write_b32 v33, v33\n\t"
      "s_mov_b64 exec, -1\n\t"
      "s_setprio 0\n\t"
      "s_mov_b32 m0, 163328\n\t"
      "s_add_u32 s44, s20, 0\n\t"
      "s_addc_u32 s45, s21, 0\n\t"
      "global_load_lds_dword v207, s[44:45]\n\t"
      "s_mov_b32 m0, 163328\n\t"
      "s_add_u32 s44, s20, 1835008\n\t"
      "s_addc_u32 s45, s21, 0\n\t"
      "global_load_lds_dword v207, s[44:45]\n\t"
      "s_waitcnt lgkmcnt(0)\n\t"
      "s_branch HEND_%=\n\t"
      ".p2align 6\n\t"
      "PRB_%=:\n\t"
      "s_waitcnt lgkmcnt(0)\n\t"
      "s_memtime s[58:59]\n\t"
      "s_waitcnt lgkmcnt(0)\n\t"
      "s_load_dword s50, s[52:53], s54\n\t"
      "s_waitcnt lgkmcnt(0)\n\t"
      "s_memtime s[46:47]\n\t"
      "s_add_u32 s54, s54, 8832\n\t"
      "s_waitcnt lgkmcnt(0)\n\t"
      "s_sub_u32 s46, s46, s58\n\t"
      "s_min_u32 s46, s56, s46\n\t"
      "s_mov_b32 s56, s57\n\t"
      "s_mov_b32 s57, s46\n\t"
      "s_cmp_lt_u32 s54, s55\n\t"
      "s_cbranch_scc1 PRB_%=\n\t"
      "s_setpc_b64 s[44:45]\n\t"
      "GRR_%=:\n\t"
      "s_mov_b32 s51, 0\n\t"
      "GRL_%=:\n\t"
      "s_add_u32 s51, s51, 1\n\t"
      "s_cmp_lt_u32 s51, 262144\n\t"
      "s_cbranch_scc0 GRX_%=\n\t"
      "s_sleep 2\n\t"
      "global_load_dwordx4 v[4:7], v200, s[14:15] sc1\n\t"
      "global_load_dwordx4 v[8:11], v201, s[14:15] sc1\n\t"
      "s_waitcnt vmcnt(0)\n\t"
      "v_xor_b32_e32 v6, v6, v4\n\t"
      "v_xor_b32_e32 v7, v7, v5\n\t"
      "v_xor_b32_e32 v10, v10, v8\n\t"
      "v_xor_b32_e32 v11, v11, v9\n\t"
      "v_cmp_ne_u64_e64 s[48:49], s[16:17], v[6:7]\n\t"
      "v_cmp_ne_u64_e32 vcc, s[16:17], v[10:11]\n\t"
      "s_nop 0\n\t"
      "s_or_b64 vcc, vcc, s[48:49]\n\t"
      "s_cbranch_scc1 GRL_%=\n\t"
      "GRX_%=:\n\t"
      "s_mov_b32 exec_lo, 0\n\t"
      "v_and_b32_e32 v8, 0x2000, v8\n\t"
      "s_mov_b32 exec_lo, -1\n\t"
      "s_branch GRB_%=\n\t"
      "HEND_%=:\n\t"
      : "+{s[4:19]}"(s), "+{s[20:21]}"(tw), "+{s[22:23]}"(pf), "+{v[192:199]}"(f), "+{v[200:207]}"(v)
      :
      : "memory", "vcc", "scc",
        "v0", "v1", "v2", "v3", "v4", "v5", "v6", "v7", "v8", "v9", "v10", "v11", "v12", "v13", "v14", "v15", "v16",
        "v17", "v18", "v19", "v20", "v21", "v22", "v23", "v24", "v25", "v26", "v27", "v28", "v29", "v30", "v31", "v32",
        "v33", "v34", "v35", "v36", "v37", "v38", "v39", "s40", "s41", "s42", "s43", "s44", "s45", "s46", "s47", "s48",
        "s49", "s50", "s51", "s52", "s53", "s54", "s55", "s56", "s57", "s58", "s59"
    );
    }
    PIN();
    LANE_BITS(__builtin_amdgcn_mbcnt_hi(~0u, __builtin_amdgcn_mbcnt_lo(~0u, 0u)));
    const bool has3 = idx < 14;
    const int ft = has3 ? 7 * xp + (idx >> 1) : 0, blk0 = 7 * (2 * side + (idx & 1));
    u32x8 v;
    u32x16 s;
    v[0] = lane * 16;
    const int rot = 4 * (idx & 1), kg = 2 * ft + ((wave >> 1) & 1);
    u64 kr = (u64)(7 - rot) | ((u64)rot << 32);
    v[1] = has3 ? lr * 3584 + ((lc ^ swz_tr(lr)) << 4) : SP3 ? (min(lane, 55) + 96 * (idx & 1)) * 4096 + 384 + xp * 1024 : (lr + 8 * side) * 4096 + lc * 128 + xp * 1024;
    v[2] = wave * G_WV + g * 1024 + q4 * 128 + (((2 * (wave & 3) + (p4 >> 1)) ^ swz_tr(8 * g + q4)) << 4) + (p4 & 1) * 8;
    v[3] = wave * 4096 + (wave & 3) * 1024 + ((lane * 16) ^ ((g & 1) << 7));
    const int gg = 2 * (lane >> 5) + (lane & 1);
    v[4] = (wave & 3) * 1024 + (((gg * 16 + ((lane >> 1) & 15)) * 16) ^ ((gg & 1) << 7));
    v[5] = (wave & 1) * 512 + lane * 8;
    v[6] = g * 8;
    v[7] = ST_H3 + part(xp * 16384 + (wave * 16 + c) * 128, side);
    put(s, 0, has3 ? (u64)(w1 + ft * 128 + (wave * 256 + rot * 32) * 3584) : (u64)(w2 + (wave * 224 + (idx & 1) * 16) * 4096));
    put(s, 2, (u64)(ws + WS_HN + unit(blk0, wave, 0)));
    put(s, 4, (u64)(ws + WS_G + unit(blk0, kg / 7, 0) + kg % 7 * 1024));
    put(s, 6, (u64)(b1 + (64 * ft + 16 * (wave & 3)) * 2));
    put(s, 8, (u64)(ws + WS_ST));
    unsigned hint = ST_H4 + side * 8192 + ((idx & 1) * 28 + ft) * 128;
    s[12] = UNI((has3 ? 4 : 0) |  (wave == 4 * side + ((xp + XO) & 3) ? 16 : 0));
    s[13] = wave * G_WV;
    asm volatile(
      "s_waitcnt lgkmcnt(0)\n\ts_nop 4\n\t"
      "s_bitcmp0_b32 s16, 2\n\t"
      "s_cbranch_scc1 ASP_%=\n\t"
      "v_mov_b32_e32 v216, 163600\n\t"
      "ds_read_b128 v[216:219], v216\n\t"
      "v_mov_b32_e32 v220, 154756\n\t"
      "ds_read2_b32 v[220:221], v220 offset0:0 offset1:1\n\t"
      "v_xor_b32_e32 v248, 64, v241\n\t"
      "v_add_u32_e32 v249, 55296, v241\n\t"
      "v_add_u32_e32 v250, 82944, v248\n\t"
      "v_add_u32_e32 v248, 27648, v248\n\t"
      "v_xor_b32_e32 v251, 32, v242\n\t"
      "v_xor_b32_e32 v252, 64, v242\n\t"
      "v_xor_b32_e32 v253, 96, v242\n\t"
      "s_cmp_ge_u32 s17, 65536\n\t"
      "s_cbranch_scc0 BOH_%=\n\t"
      "s_barrier\n\t"
      "BOH_%=:\n\t"
      "s_add_u32 m0, s17, 0\n\t"
      "s_nop 0\n\t"
      "global_load_lds_dwordx4 v241, s[4:5]\n\t"
      "global_load_lds_dwordx4 v248, s[4:5] offset:1024\n\t"
      "global_load_lds_dwordx4 v249, s[4:5] offset:2048\n\t"
      "global_load_lds_dwordx4 v250, s[4:5] offset:3072\n\t"
      "s_sub_u32 s20, s20, 1\n\t"
      "s_cselect_b32 s48, -7, 1\n\t"
      "s_cselect_b32 s49, -1, 0\n\t"
      "s_mul_i32 s48, s48, 114688\n\t"
      "s_add_u32 s4, s4, s48\n\t"
      "s_addc_u32 s5, s5, s49\n\t"
      "global_load_dwordx2 v[238:239], v246, s[10:11]\n\t"
      "s_waitcnt lgkmcnt(0)\n\t"
      "v_readfirstlane_b32 s14, v216\n\t"
      "v_readfirstlane_b32 s15, v217\n\t"
      "v_readfirstlane_b32 s46, v218\n\t"
      "v_readfirstlane_b32 s47, v219\n\t"
      "v_readfirstlane_b32 s48, v220\n\t"
      "v_readfirstlane_b32 s49, v221\n\t"
      "v_xor_b32_e32 v247, v247, v221\n\t"
      "s_nop 0\n\t"
      "s_xor_b32 s6, s6, s48\n\t"
      "s_xor_b32 s8, s8, s49\n\t"
      "s_xor_b32 s22, s22, s49\n\t"
      "s_add_u32 s52, s6, 0\n\t"
      "s_addc_u32 s53, s7, 0\n\t"
      "s_add_u32 s54, s6, 131072\n\t"
      "s_addc_u32 s55, s7, 0\n\t"
      "s_add_u32 s56, s6, 262144\n\t"
      "s_addc_u32 s57, s7, 0\n\t"
      "s_add_u32 s58, s6, 393216\n\t"
      "s_addc_u32 s59, s7, 0\n\t"
      "s_add_u32 s60, s6, 524288\n\t"
      "s_addc_u32 s61, s7, 0\n\t"
      "s_add_u32 s62, s6, 655360\n\t"
      "s_addc_u32 s63, s7, 0\n\t"
      "s_add_u32 s64, s6, 786432\n\t"
      "s_addc_u32 s65, s7, 0\n\t"
      "s_add_u32 s48, s21, 0\n\t"
      "s_cmp_ge_u32 s48, 8\n\t"
      "s_cselect_b32 s49, 8, 0\n\t"
      "s_sub_u32 s48, s48, s49\n\t"
      "v_lshl_add_u32 v176, s48, 10, v240\n\t"
      "s_add_u32 s48, s21, 1\n\t"
      "s_cmp_ge_u32 s48, 8\n\t"
      "s_cselect_b32 s49, 8, 0\n\t"
      "s_sub_u32 s48, s48, s49\n\t"
      "v_lshl_add_u32 v177, s48, 10, v240\n\t"
      "s_add_u32 s48, s21, 2\n\t"
      "s_cmp_ge_u32 s48, 8\n\t"
      "s_cselect_b32 s49, 8, 0\n\t"
      "s_sub_u32 s48, s48, s49\n\t"
      "v_lshl_add_u32 v178, s48, 10, v240\n\t"
      "s_add_u32 s48, s21, 3\n\t"
      "s_cmp_ge_u32 s48, 8\n\t"
      "s_cselect_b32 s49, 8, 0\n\t"
      "s_sub_u32 s48, s48, s49\n\t"
      "v_lshl_add_u32 v179, s48, 10, v240\n\t"
      "s_add_u32 s48, s21, 4\n\t"
      "s_cmp_ge_u32 s48, 8\n\t"
      "s_cselect_b32 s49, 8, 0\n\t"
      "s_sub_u32 s48, s48, s49\n\t"
      "v_lshl_add_u32 v180, s48, 10, v240\n\t"
      "s_add_u32 s48, s21, 5\n\t"
      "s_cmp_ge_u32 s48, 8\n\t"
      "s_cselect_b32 s49, 8, 0\n\t"
      "s_sub_u32 s48, s48, s49\n\t"
      "v_lshl_add_u32 v181, s48, 10, v240\n\t"
      "s_add_u32 s48, s21, 6\n\t"
      "s_cmp_ge_u32 s48, 8\n\t"
      "s_cselect_b32 s49, 8, 0\n\t"
      "s_sub_u32 s48, s48, s49\n\t"
      "v_lshl_add_u32 v182, s48, 10, v240\n\t"
      "s_add_u32 s48, s21, 7\n\t"
      "s_cmp_ge_u32 s48, 8\n\t"
      "s_cselect_b32 s49, 8, 0\n\t"
      "s_sub_u32 s48, s48, s49\n\t"
      "v_lshl_add_u32 v183, s48, 10, v240\n\t"
      "s_add_u32 m0, s17, 4096\n\t"
      "s_nop 0\n\t"
      "global_load_lds_dwordx4 v241, s[4:5]\n\t"
      "global_load_lds_dwordx4 v248, s[4:5] offset:1024\n\t"
      "global_load_lds_dwordx4 v249, s[4:5] offset:2048\n\t"
      "global_load_lds_dwordx4 v250, s[4:5] offset:3072\n\t"
      "s_sub_u32 s20, s20, 1\n\t"
      "s_cselect_b32 s48, -7, 1\n\t"
      "s_cselect_b32 s49, -1, 0\n\t"
      "s_mul_i32 s48, s48, 114688\n\t"
      "s_add_u32 s4, s4, s48\n\t"
      "s_addc_u32 s5, s5, s49\n\t"
      "s_waitcnt vmcnt(5)\n\t"
      "s_add_u32 m0, s17, 8192\n\t"
      "s_nop 0\n\t"
      "global_load_lds_dwordx4 v241, s[4:5]\n\t"
      "global_load_lds_dwordx4 v248, s[4:5] offset:1024\n\t"
      "global_load_lds_dwordx4 v249, s[4:5] offset:2048\n\t"
      "global_load_lds_dwordx4 v250, s[4:5] offset:3072\n\t"
      "s_sub_u32 s20, s20, 1\n\t"
      "s_cselect_b32 s48, -7, 1\n\t"
      "s_cselect_b32 s49, -1, 0\n\t"
      "s_mul_i32 s48, s48, 114688\n\t"
      "s_add_u32 s4, s4, s48\n\t"
      "s_addc_u32 s5, s5, s49\n\t"
      "ds_read_b64_tr_b16 v[0:1], v242\n\t"
      "ds_read_b64_tr_b16 v[2:3], v242 offset:512\n\t"
      "ds_read_b64_tr_b16 v[4:5], v251\n\t"
      "ds_read_b64_tr_b16 v[6:7], v251 offset:512\n\t"
      "ds_read_b64_tr_b16 v[8:9], v252\n\t"
      "ds_read_b64_tr_b16 v[10:11], v252 offset:512\n\t"
      "ds_read_b64_tr_b16 v[12:13], v253\n\t"
      "ds_read_b64_tr_b16 v[14:15], v253 offset:512\n\t"
      "s_waitcnt vmcnt(4)\n\t"
      "s_add_u32 m0, s17, 12288\n\t"
      "s_nop 0\n\t"
      "global_load_lds_dwordx4 v241, s[4:5]\n\t"
      "global_load_lds_dwordx4 v248, s[4:5] offset:1024\n\t"
      "global_load_lds_dwordx4 v249, s[4:5] offset:2048\n\t"
      "global_load_lds_dwordx4 v250, s[4:5] offset:3072\n\t"
      "s_sub_u32 s20, s20, 1\n\t"
      "s_cselect_b32 s48, -7, 1\n\t"
      "s_cselect_b32 s49, -1, 0\n\t"
      "s_mul_i32 s48, s48, 114688\n\t"
      "s_add_u32 s4, s4, s48\n\t"
      "s_addc_u32 s5, s5, s49\n\t"
      "ds_read_b64_tr_b16 v[16:17], v242 offset:4096\n\t"
      "ds_read_b64_tr_b16 v[18:19], v242 offset:4608\n\t"
      "ds_read_b64_tr_b16 v[20:21], v251 offset:4096\n\t"
      "ds_read_b64_tr_b16 v[22:23], v251 offset:4608\n\t"
      "ds_read_b64_tr_b16 v[24:25], v252 offset:4096\n\t"
      "ds_read_b64_tr_b16 v[26:27], v252 offset:4608\n\t"
      "ds_read_b64_tr_b16 v[28:29], v253 offset:4096\n\t"
      "ds_read_b64_tr_b16 v[30:31], v253 offset:4608\n\t"
      "v_xor_b32_e32 v164, 1024, v243\n\t"
      "v_xor_b32_e32 v165, 2048, v243\n\t"
      "v_xor_b32_e32 v166, 3072, v243\n\t"
      "v_lshlrev_b32_e32 v236, 16, v238\n\t"
      "v_and_b32_e32 v237, 0xffff0000, v238\n\t"
      "v_lshlrev_b32_e32 v238, 16, v239\n\t"
      "v_and_b32_e32 v239, 0xffff0000, v239\n\t"
      "v_mov_b32_e32 v235, 0xbdd2d3e8\n\t"
      "s_waitcnt vmcnt(4)\n\t"
      "s_waitcnt lgkmcnt(8)\n\t"
      "s_add_u32 m0, s17, 0\n\t"
      "s_nop 0\n\t"
      "global_load_lds_dwordx4 v241, s[4:5]\n\t"
      "global_load_lds_dwordx4 v248, s[4:5] offset:1024\n\t"
      "global_load_lds_dwordx4 v249, s[4:5] offset:2048\n\t"
      "global_load_lds_dwordx4 v250, s[4:5] offset:3072\n\t"
      "s_sub_u32 s20, s20, 1\n\t"
      "s_cselect_b32 s48, -7, 1\n\t"
      "s_cselect_b32 s49, -1, 0\n\t"
      "s_mul_i32 s48, s48, 114688\n\t"
      "s_add_u32 s4, s4, s48\n\t"
      "s_addc_u32 s5, s5, s49\n\t"
      "ds_read_b64_tr_b16 v[32:33], v242 offset:8192\n\t"
      "ds_read_b64_tr_b16 v[34:35], v242 offset:8704\n\t"
      "ds_read_b64_tr_b16 v[36:37], v251 offset:8192\n\t"
      "ds_read_b64_tr_b16 v[38:39], v251 offset:8704\n\t"
      "ds_read_b64_tr_b16 v[40:41], v252 offset:8192\n\t"
      "ds_read_b64_tr_b16 v[42:43], v252 offset:8704\n\t"
      "ds_read_b64_tr_b16 v[44:45], v253 offset:8192\n\t"
      "ds_read_b64_tr_b16 v[46:47], v253 offset:8704\n\t"
      "s_waitcnt vmcnt(4)\n\t"
      "s_waitcnt lgkmcnt(8)\n\t"
      "s_add_u32 m0, s17, 4096\n\t"
      "s_nop 0\n\t"
      "global_load_lds_dwordx4 v241, s[4:5]\n\t"
      "global_load_lds_dwordx4 v248, s[4:5] offset:1024\n\t"
      "global_load_lds_dwordx4 v249, s[4:5] offset:2048\n\t"
      "global_load_lds_dwordx4 v250, s[4:5] offset:3072\n\t"
      "s_sub_u32 s20, s20, 1\n\t"
      "s_cselect_b32 s48, -7, 1\n\t"
      "s_cselect_b32 s49, -1, 0\n\t"
      "s_mul_i32 s48, s48, 114688\n\t"
      "s_add_u32 s4, s4, s48\n\t"
      "s_addc_u32 s5, s5, s49\n\t"
      "ds_read_b64_tr_b16 v[48:49], v242 offset:12288\n\t"
      "ds_read_b64_tr_b16 v[50:51], v242 offset:12800\n\t"
      "ds_read_b64_tr_b16 v[52:53], v251 offset:12288\n\t"
      "ds_read_b64_tr_b16 v[54:55], v251 offset:12800\n\t"
      "ds_read_b64_tr_b16 v[56:57], v252 offset:12288\n\t"
      "ds_read_b64_tr_b16 v[58:59], v252 offset:12800\n\t"
      "ds_read_b64_tr_b16 v[60:61], v253 offset:12288\n\t"
      "ds_read_b64_tr_b16 v[62:63], v253 offset:12800\n\t"
      "s_waitcnt vmcnt(4)\n\t"
      "s_waitcnt lgkmcnt(8)\n\t"
      "s_add_u32 m0, s17, 8192\n\t"
      "s_nop 0\n\t"
      "global_load_lds_dwordx4 v241, s[4:5]\n\t"
      "global_load_lds_dwordx4 v248, s[4:5] offset:1024\n\t"
      "global_load_lds_dwordx4 v249, s[4:5] offset:2048\n\t"
      "global_load_lds_dwordx4 v250, s[4:5] offset:3072\n\t"
      "s_sub_u32 s20, s20, 1\n\t"
      "s_cselect_b32 s48, -7, 1\n\t"
      "s_cselect_b32 s49, -1, 0\n\t"
      "s_mul_i32 s48, s48, 114688\n\t"
      "s_add_u32 s4, s4, s48\n\t"
      "s_addc_u32 s5, s5, s49\n\t"
      "ds_read_b64_tr_b16 v[64:65], v242\n\t"
      "ds_read_b64_tr_b16 v[66:67], v242 offset:512\n\t"
      "ds_read_b64_tr_b16 v[68:69], v251\n\t"
      "ds_read_b64_tr_b16 v[70:71], v251 offset:512\n\t"
      "ds_read_b64_tr_b16 v[72:73], v252\n\t"
      "ds_read_b64_tr_b16 v[74:75], v252 offset:512\n\t"
      "ds_read_b64_tr_b16 v[76:77], v253\n\t"
      "ds_read_b64_tr_b16 v[78:79], v253 offset:512\n\t"
      "s_waitcnt vmcnt(4)\n\t"
      "s_waitcnt lgkmcnt(8)\n\t"
      "s_add_u32 m0, s17, 12288\n\t"
      "s_nop 0\n\t"
      "global_load_lds_dwordx4 v241, s[4:5]\n\t"
      "global_load_lds_dwordx4 v248, s[4:5] offset:1024\n\t"
      "global_load_lds_dwordx4 v249, s[4:5] offset:2048\n\t"
      "global_load_lds_dwordx4 v250, s[4:5] offset:3072\n\t"
      "ds_read_b64_tr_b16 v[80:81], v242 offset:4096\n\t"
      "ds_read_b64_tr_b16 v[82:83], v242 offset:4608\n\t"
      "ds_read_b64_tr_b16 v[84:85], v251 offset:4096\n\t"
      "ds_read_b64_tr_b16 v[86:87], v251 offset:4608\n\t"
      "ds_read_b64_tr_b16 v[88:89], v252 offset:4096\n\t"
      "ds_read_b64_tr_b16 v[90:91], v252 offset:4608\n\t"
      "ds_read_b64_tr_b16 v[92:93], v253 offset:4096\n\t"
      "ds_read_b64_tr_b16 v[94:95], v253 offset:4608\n\t"
      "s_waitcnt vmcnt(4)\n\t"
      "ds_read_b64_tr_b16 v[96:97], v242 offset:8192\n\t"
      "ds_read_b64_tr_b16 v[98:99], v242 offset:8704\n\t"
      "ds_read_b64_tr_b16 v[100:101], v251 offset:8192\n\t"
      "ds_read_b64_tr_b16 v[102:103], v251 offset:8704\n\t"
      "ds_read_b64_tr_b16 v[104:105], v252 offset:8192\n\t"
      "ds_read_b64_tr_b16 v[106:107], v252 offset:8704\n\t"
      "ds_read_b64_tr_b16 v[108:109], v253 offset:8192\n\t"
      "ds_read_b64_tr_b16 v[110:111], v253 offset:8704\n\t"
      "s_waitcnt vmcnt(0)\n\t"
      "ds_read_b64_tr_b16 v[112:113], v242 offset:12288\n\t"
      "ds_read_b64_tr_b16 v[114:115], v242 offset:12800\n\t"
      "ds_read_b64_tr_b16 v[116:117], v251 offset:12288\n\t"
      "ds_read_b64_tr_b16 v[118:119], v251 offset:12800\n\t"
      "ds_read_b64_tr_b16 v[120:121], v252 offset:12288\n\t"
      "ds_read_b64_tr_b16 v[122:123], v252 offset:12800\n\t"
      "ds_read_b64_tr_b16 v[124:125], v253 offset:12288\n\t"
      "ds_read_b64_tr_b16 v[126:127], v253 offset:12800\n\t"
      "s_waitcnt lgkmcnt(0)\n\t"
      "s_barrier\n\t"
      "s_mov_b32 s51, 0\n\t"
      "s_branch PRE_%=\n\t"
      "PRL_%=:\n\t"
      "s_sleep 4\n\t"
      "s_add_u32 s51, s51, 1\n\t"
      "s_cmp_lt_u32 s51, 262144\n\t"
      "s_cbranch_scc0 PRX_%=\n\t"
      "PRE_%=:\n\t"
      "s_mov_b64 exec, 65535\n\t"
      "s_bitcmp1_b32 s16, 4\n\t"
      "s_cbranch_scc1 PM_%=\n\t"
      "global_load_dwordx2 v[232:233], v247, s[12:13] sc1\n\t"
      "s_waitcnt vmcnt(0)\n\t"
      "PM_%=:\n\t"
      "s_cbranch_scc0 PN_%=\n\t"
      "v_mov_b64_e32 v[232:233], 0\n\t"
      "global_atomic_or_x2 v[232:233], v247, v[232:233], s[12:13] sc0 sc1\n\t"
      "s_waitcnt vmcnt(0)\n\t"
      "PN_%=:\n\t"
      "v_subrev_u32_e32 v232, s14, v232\n\t"
      "v_cmp_ne_u32_e64 s[48:49], s15, v233\n\t"
      "v_subrev_u32_e32 v232, 0, v232\n\t"
      "v_cmp_lt_u32_e32 vcc, 7, v232\n\t"
      "s_mov_b64 exec, -1\n\t"
      "s_or_b64 vcc, vcc, s[48:49]\n\t"
      "s_cbranch_vccnz PRL_%=\n\t"
      "PRX_%=:\n\t"
      "PRY_%=:\n\t"
      "global_load_dwordx4 v[128:131], v176, s[52:53] sc1\n\t"
      "global_load_dwordx4 v[132:135], v177, s[52:53] sc1\n\t"
      "global_load_dwordx4 v[136:139], v178, s[52:53] sc1\n\t"
      "global_load_dwordx4 v[140:143], v179, s[52:53] sc1\n\t"
      "global_load_dwordx4 v[144:147], v180, s[52:53] sc1\n\t"
      "global_load_dwordx4 v[148:151], v181, s[52:53] sc1\n\t"
      "global_load_dwordx4 v[152:155], v182, s[52:53] sc1\n\t"
      "global_load_dwordx4 v[156:159], v183, s[52:53] sc1\n\t"
      "s_cmp_ge_u32 s17, 65536\n\t"
      "s_cbranch_scc1 GQ_%=\n\t"
      ".p2align 6\n\t"
      "s_setprio 1\n\t"
      "v_mov_b32_e32 v232, s14\n\t"
      "v_mov_b32_e32 v233, s15\n\t"
      "v_lshl_add_u32 v247, v240, 10, s22\n\t"
      "s_waitcnt vmcnt(7)\n\t"
      "v_max3_u32 v234, v128, v129, v130\n\t"
      "v_max_u32_e32 v234, v234, v131\n\t"
      "v_cmp_eq_u32_e32 vcc, -1, v234\n\t"
      "s_cbranch_vccnz FP0R_%=\n\t"
      "FP0B_%=:\n\t"
      "v_mfma_f32_16x16x32_bf16 v[184:187], v[0:3], v[128:131], 0\n\t"
      "v_mfma_f32_16x16x32_bf16 v[188:191], v[4:7], v[128:131], 0\n\t"
      "v_mfma_f32_16x16x32_bf16 v[192:195], v[8:11], v[128:131], 0\n\t"
      "v_mfma_f32_16x16x32_bf16 v[196:199], v[12:15], v[128:131], 0\n\t"
      "global_load_dwordx4 v[128:131], v176, s[54:55] sc1\n\t"
      "s_waitcnt vmcnt(7)\n\t"
      "v_max3_u32 v234, v132, v133, v134\n\t"
      "v_max_u32_e32 v234, v234, v135\n\t"
      "v_cmp_eq_u32_e32 vcc, -1, v234\n\t"
      "s_cbranch_vccnz FP1R_%=\n\t"
      "FP1B_%=:\n\t"
      "v_mfma_f32_16x16x32_bf16 v[184:187], v[16:19], v[132:135], v[184:187]\n\t"
      "v_mfma_f32_16x16x32_bf16 v[188:191], v[20:23], v[132:135], v[188:191]\n\t"
      "v_mfma_f32_16x16x32_bf16 v[192:195], v[24:27], v[132:135], v[192:195]\n\t"
      "v_mfma_f32_16x16x32_bf16 v[196:199], v[28:31], v[132:135], v[196:199]\n\t"
      "global_load_dwordx4 v[132:135], v177, s[54:55] sc1\n\t"
      "s_waitcnt vmcnt(7)\n\t"
      "v_max3_u32 v234, v136, v137, v138\n\t"
      "v_max_u32_e32 v234, v234, v139\n\t"
      "v_cmp_eq_u32_e32 vcc, -1, v234\n\t"
      "s_cbranch_vccnz FP2R_%=\n\t"
      "FP2B_%=:\n\t"
      "v_mfma_f32_16x16x32_bf16 v[184:187], v[32:35], v[136:139], v[184:187]\n\t"
      "v_mfma_f32_16x16x32_bf16 v[188:191], v[36:39], v[136:139], v[188:191]\n\t"
      "v_mfma_f32_16x16x32_bf16 v[192:195], v[40:43], v[136:139], v[192:195]\n\t"
      "v_mfma_f32_16x16x32_bf16 v[196:199], v[44:47], v[136:139], v[196:199]\n\t"
      "global_load_dwordx4 v[136:139], v178, s[54:55] sc1\n\t"
      "s_waitcnt vmcnt(7)\n\t"
      "v_max3_u32 v234, v140, v141, v142\n\t"
      "v_max_u32_e32 v234, v234, v143\n\t"
      "v_cmp_eq_u32_e32 vcc, -1, v234\n\t"
      "s_cbranch_vccnz FP3R_%=\n\t"
      "FP3B_%=:\n\t"
      "v_mfma_f32_16x16x32_bf16 v[184:187], v[48:51], v[140:143], v[184:187]\n\t"
      "v_mfma_f32_16x16x32_bf16 v[188:191], v[52:55], v[140:143], v[188:191]\n\t"
      "v_mfma_f32_16x16x32_bf16 v[192:195], v[56:59], v[140:143], v[192:195]\n\t"
      "v_mfma_f32_16x16x32_bf16 v[196:199], v[60:63], v[140:143], v[196:199]\n\t"
      "global_load_dwordx4 v[140:143], v179, s[54:55] sc1\n\t"
      "s_waitcnt vmcnt(7)\n\t"
      "v_max3_u32 v234, v144, v145, v146\n\t"
      "v_max_u32_e32 v234, v234, v147\n\t"
      "v_cmp_eq_u32_e32 vcc, -1, v234\n\t"
      "s_cbranch_vccnz FP4R_%=\n\t"
      "FP4B_%=:\n\t"
      "v_mfma_f32_16x16x32_bf16 v[184:187], v[64:67], v[144:147], v[184:187]\n\t"
      "v_mfma_f32_16x16x32_bf16 v[188:191], v[68:71], v[144:147], v[188:191]\n\t"
      "v_mfma_f32_16x16x32_bf16 v[192:195], v[72:75], v[144:147], v[192:195]\n\t"
      "v_mfma_f32_16x16x32_bf16 v[196:199], v[76:79], v[144:147], v[196:199]\n\t"
      "global_load_dwordx4 v[144:147], v180, s[54:55] sc1\n\t"
      "s_waitcnt vmcnt(7)\n\t"
      "v_max3_u32 v234, v148, v149, v150\n\t"
      "v_max_u32_e32 v234, v234, v151\n\t"
      "v_cmp_eq_u32_e32 vcc, -1, v234\n\t"
      "s_cbranch_vccnz FP5R_%=\n\t"
      "FP5B_%=:\n\t"
      "v_mfma_f32_16x16x32_bf16 v[184:187], v[80:83], v[148:151], v[184:187]\n\t"
      "v_mfma_f32_16x16x32_bf16 v[188:191], v[84:87], v[148:151], v[188:191]\n\t"
      "v_mfma_f32_16x16x32_bf16 v[192:195], v[88:91], v[148:151], v[192:195]\n\t"
      "v_mfma_f32_16x16x32_bf16 v[196:199], v[92:95], v[148:151], v[196:199]\n\t"
      "global_load_dwordx4 v[148:151], v181, s[54:55] sc1\n\t"
      "s_waitcnt vmcnt(7)\n\t"
      "v_max3_u32 v234, v152, v153, v154\n\t"
      "v_max_u32_e32 v234, v234, v155\n\t"
      "v_cmp_eq_u32_e32 vcc, -1, v234\n\t"
      "s_cbranch_vccnz FP6R_%=\n\t"
      "FP6B_%=:\n\t"
      "v_mfma_f32_16x16x32_bf16 v[184:187], v[96:99], v[152:155], v[184:187]\n\t"
      "s_waitcnt vmcnt(6)\n\t"
      "v_max3_u32 v234, v156, v157, v158\n\t"
      "v_max_u32_e32 v234, v234, v159\n\t"
      "v_cmp_eq_u32_e32 vcc, -1, v234\n\t"
      "s_cbranch_vccnz FP7R_%=\n\t"
      "FP7B_%=:\n\t"
      "v_mfma_f32_16x16x32_bf16 v[184:187], v[112:115], v[156:159], v[184:187]\n\t"
      "v_mfma_f32_16x16x32_bf16 v[188:191], v[100:103], v[152:155], v[188:191]\n\t"
      "v_mfma_f32_16x16x32_bf16 v[188:191], v[116:119], v[156:159], v[188:191]\n\t"
      "v_mfma_f32_16x16x32_bf16 v[192:195], v[104:107], v[152:155], v[192:195]\n\t"
      "v_mfma_f32_16x16x32_bf16 v[192:195], v[120:123], v[156:159], v[192:195]\n\t"
      "s_nop 3\n\t"
      "ds_write_b128 v243, v[184:187]\n\t"
      "v_mfma_f32_16x16x32_bf16 v[196:199], v[108:111], v[152:155], v[196:199]\n\t"
      "global_load_dwordx4 v[152:155], v182, s[54:55] sc1\n\t"
      "v_mfma_f32_16x16x32_bf16 v[196:199], v[124:127], v[156:159], v[196:199]\n\t"
      "global_load_dwordx4 v[156:159], v183, s[54:55] sc1\n\t"
      "ds_write_b128 v164, v[188:191]\n\t"
      "ds_write_b128 v165, v[192:195]\n\t"
      "s_nop 4\n\t"
      "ds_write_b128 v166, v[196:199]\n\t"
      "s_waitcnt lgkmcnt(0)\n\t"
      "s_barrier\n\t"
      "ds_read_b128 v[216:219], v244\n\t"
      "ds_read_b128 v[220:223], v244 offset:4096\n\t"
      "ds_read_b128 v[224:227], v244 offset:8192\n\t"
      "ds_read_b128 v[228:231], v244 offset:12288\n\t"
      "ds_read_b128 v[184:187], v244 offset:16384\n\t"
      "ds_read_b128 v[188:191], v244 offset:20480\n\t"
      "ds_read_b128 v[192:195], v244 offset:24576\n\t"
      "ds_read_b128 v[196:199], v244 offset:28672\n\t"
      "s_waitcnt lgkmcnt(6)\n\t"
      "v_add_f32_e32 v216, v216, v220\n\t"
      "v_add_f32_e32 v217, v217, v221\n\t"
      "v_add_f32_e32 v218, v218, v222\n\t"
      "v_add_f32_e32 v219, v219, v223\n\t"
      "s_waitcnt lgkmcnt(4)\n\t"
      "v_add_f32_e32 v224, v224, v228\n\t"
      "v_add_f32_e32 v225, v225, v229\n\t"
      "v_add_f32_e32 v226, v226, v230\n\t"
      "v_add_f32_e32 v227, v227, v231\n\t"
      "s_waitcnt lgkmcnt(2)\n\t"
      "v_add_f32_e32 v184, v184, v188\n\t"
      "v_add_f32_e32 v185, v185, v189\n\t"
      "v_add_f32_e32 v186, v186, v190\n\t"
      "v_add_f32_e32 v187, v187, v191\n\t"
      "v_add_f32_e32 v216, v216, v224\n\t"
      "v_add_f32_e32 v217, v217, v225\n\t"
      "v_add_f32_e32 v218, v218, v226\n\t"
      "v_add_f32_e32 v219, v219, v227\n\t"
      "s_waitcnt lgkmcnt(0)\n\t"
      "v_add_f32_e32 v192, v192, v196\n\t"
      "v_add_f32_e32 v193, v193, v197\n\t"
      "v_add_f32_e32 v194, v194, v198\n\t"
      "v_add_f32_e32 v195, v195, v199\n\t"
      "v_add_f32_e32 v184, v184, v192\n\t"
      "v_add_f32_e32 v185, v185, v193\n\t"
      "v_add_f32_e32 v186, v186, v194\n\t"
      "v_add_f32_e32 v187, v187, v195\n\t"
      "v_add_f32_e32 v216, v216, v184\n\t"
      "v_add_f32_e32 v217, v217, v185\n\t"
      "s_waitcnt vmcnt(7)\n\t"
      "v_mfma_f32_16x16x32_bf16 v[200:203], v[0:3], v[128:131], 0\n\t"
      "v_add_f32_e32 v218, v218, v186\n\t"
      "v_add_f32_e32 v219, v219, v187\n\t"
      "v_mfma_f32_16x16x32_bf16 v[204:207], v[4:7], v[128:131], 0\n\t"
      "v_mul_f32_e32 v220, v216, v216\n\t"
      "v_mul_f32_e32 v221, v217, v217\n\t"
      "v_mfma_f32_16x16x32_bf16 v[208:211], v[8:11], v[128:131], 0\n\t"
      "v_mul_f32_e32 v222, v218, v218\n\t"
      "v_mul_f32_e32 v223, v219, v219\n\t"
      "v_mfma_f32_16x16x32_bf16 v[212:215], v[12:15], v[128:131], 0\n\t"
      "global_load_dwordx4 v[128:131], v176, s[56:57] sc1\n\t"
      "s_waitcnt vmcnt(7)\n\t"
      "v_mfma_f32_16x16x32_bf16 v[200:203], v[16:19], v[132:135], v[200:203]\n\t"
      "v_fmaak_f32 v220, v235, v220, 0xc0135761\n\t"
      "v_fmaak_f32 v221, v235, v221, 0xc0135761\n\t"
      "v_mfma_f32_16x16x32_bf16 v[204:207], v[20:23], v[132:135], v[204:207]\n\t"
      "v_fmaak_f32 v222, v235, v222, 0xc0135761\n\t"
      "v_fmaak_f32 v223, v235, v223, 0xc0135761\n\t"
      "v_mfma_f32_16x16x32_bf16 v[208:211], v[24:27], v[132:135], v[208:211]\n\t"
      "v_mul_f32_e32 v220, v216, v220\n\t"
      "v_mul_f32_e32 v221, v217, v221\n\t"
      "v_mfma_f32_16x16x32_bf16 v[212:215], v[28:31], v[132:135], v[212:215]\n\t"
      "global_load_dwordx4 v[132:135], v177, s[56:57] sc1\n\t"
      "s_waitcnt vmcnt(7)\n\t"
      "v_mfma_f32_16x16x32_bf16 v[200:203], v[32:35], v[136:139], v[200:203]\n\t"
      "v_mul_f32_e32 v222, v218, v222\n\t"
      "v_mul_f32_e32 v223, v219, v223\n\t"
      "v_mfma_f32_16x16x32_bf16 v[204:207], v[36:39], v[136:139], v[204:207]\n\t"
      "v_exp_f32_e32 v220, v220\n\t"
      "v_mfma_f32_16x16x32_bf16 v[208:211], v[40:43], v[136:139], v[208:211]\n\t"
      "v_exp_f32_e32 v221, v221\n\t"
      "v_mfma_f32_16x16x32_bf16 v[212:215], v[44:47], v[136:139], v[212:215]\n\t"
      "global_load_dwordx4 v[136:139], v178, s[56:57] sc1\n\t"
      "s_waitcnt vmcnt(7)\n\t"
      "v_mfma_f32_16x16x32_bf16 v[200:203], v[48:51], v[140:143], v[200:203]\n\t"
      "v_exp_f32_e32 v222, v222\n\t"
      "v_mfma_f32_16x16x32_bf16 v[204:207], v[52:55], v[140:143], v[204:207]\n\t"
      "v_exp_f32_e32 v223, v223\n\t"
      "v_mfma_f32_16x16x32_bf16 v[208:211], v[56:59], v[140:143], v[208:211]\n\t"
      "v_add_f32_e32 v220, 1.0, v220\n\t"
      "v_add_f32_e32 v221, 1.0, v221\n\t"
      "v_mfma_f32_16x16x32_bf16 v[212:215], v[60:63], v[140:143], v[212:215]\n\t"
      "global_load_dwordx4 v[140:143], v179, s[56:57] sc1\n\t"
      "s_waitcnt vmcnt(7)\n\t"
      "v_mfma_f32_16x16x32_bf16 v[200:203], v[64:67], v[144:147], v[200:203]\n\t"
      "v_add_f32_e32 v222, 1.0, v222\n\t"
      "v_add_f32_e32 v223, 1.0, v223\n\t"
      "v_mfma_f32_16x16x32_bf16 v[204:207], v[68:71], v[144:147], v[204:207]\n\t"
      "v_rcp_f32_e32 v220, v220\n\t"
      "v_mfma_f32_16x16x32_bf16 v[208:211], v[72:75], v[144:147], v[208:211]\n\t"
      "v_rcp_f32_e32 v221, v221\n\t"
      "v_mfma_f32_16x16x32_bf16 v[212:215], v[76:79], v[144:147], v[212:215]\n\t"
      "global_load_dwordx4 v[144:147], v180, s[56:57] sc1\n\t"
      "s_waitcnt vmcnt(7)\n\t"
      "v_mfma_f32_16x16x32_bf16 v[200:203], v[80:83], v[148:151], v[200:203]\n\t"
      "v_rcp_f32_e32 v222, v222\n\t"
      "v_mfma_f32_16x16x32_bf16 v[204:207], v[84:87], v[148:151], v[204:207]\n\t"
      "v_rcp_f32_e32 v223, v223\n\t"
      "v_mfma_f32_16x16x32_bf16 v[208:211], v[88:91], v[148:151], v[208:211]\n\t"
      "v_mul_f32_e32 v216, v216, v220\n\t"
      "v_mfma_f32_16x16x32_bf16 v[212:215], v[92:95], v[148:151], v[212:215]\n\t"
      "global_load_dwordx4 v[148:151], v181, s[56:57] sc1\n\t"
      "s_waitcnt vmcnt(7)\n\t"
      "v_mfma_f32_16x16x32_bf16 v[200:203], v[96:99], v[152:155], v[200:203]\n\t"
      "v_mul_f32_e32 v217, v217, v221\n\t"
      "v_mul_f32_e32 v218, v218, v222\n\t"
      "s_waitcnt vmcnt(6)\n\t"
      "v_mfma_f32_16x16x32_bf16 v[200:203], v[112:115], v[156:159], v[200:203]\n\t"
      "v_mul_f32_e32 v219, v219, v223\n\t"
      "v_cvt_pk_bf16_f32 v220, v216, v217\n\t"
      "v_mfma_f32_16x16x32_bf16 v[204:207], v[100:103], v[152:155], v[204:207]\n\t"
      "v_cvt_pk_bf16_f32 v221, v218, v219\n\t"
      "v_min_u32_e32 v220, -2, v220\n\t"
      "v_mfma_f32_16x16x32_bf16 v[204:207], v[116:119], v[156:159], v[204:207]\n\t"
      "s_nop 1\n\t"
      "v_cmp_u_f32_e32 vcc, v200, v200\n\t"
      "v_mfma_f32_16x16x32_bf16 v[208:211], v[104:107], v[152:155], v[208:211]\n\t"
      "v_min_u32_e32 v221, -2, v221\n\t"
      "global_store_dwordx2 v245, v[220:221], s[8:9] sc1\n\t"
      "s_add_u32 s8, s8, 131072\n\t"
      "s_addc_u32 s9, s9, 0\n\t"
      "v_mfma_f32_16x16x32_bf16 v[208:211], v[120:123], v[156:159], v[208:211]\n\t"
      "s_cbranch_vccnz NP1R_%=\n\t"
      "NP1B_%=:\n\t"
      "ds_write_b128 v243, v[200:203] offset:32768\n\t"
      "v_mfma_f32_16x16x32_bf16 v[212:215], v[108:111], v[152:155], v[212:215]\n\t"
      "global_load_dwordx4 v[152:155], v182, s[56:57] sc1\n\t"
      "v_mfma_f32_16x16x32_bf16 v[212:215], v[124:127], v[156:159], v[212:215]\n\t"
      "global_load_dwordx4 v[156:159], v183, s[56:57] sc1\n\t"
      "ds_write_b128 v164, v[204:207] offset:32768\n\t"
      "s_nop 0\n\t"
      "ds_write_b128 v165, v[208:211] offset:32768\n\t"
      "s_nop 3\n\t"
      "ds_write_b128 v166, v[212:215] offset:32768\n\t"
      "s_waitcnt lgkmcnt(0)\n\t"
      "s_barrier\n\t"
      "ds_read_b128 v[216:219], v244 offset:32768\n\t"
      "ds_read_b128 v[220:223], v244 offset:36864\n\t"
      "ds_read_b128 v[224:227], v244 offset:40960\n\t"
      "ds_read_b128 v[228:231], v244 offset:45056\n\t"
      "ds_read_b128 v[200:203], v244 offset:49152\n\t"
      "ds_read_b128 v[204:207], v244 offset:53248\n\t"
      "ds_read_b128 v[208:211], v244 offset:57344\n\t"
      "ds_read_b128 v[212:215], v244 offset:61440\n\t"
      "s_waitcnt lgkmcnt(6)\n\t"
      "v_add_f32_e32 v216, v216, v220\n\t"
      "v_add_f32_e32 v217, v217, v221\n\t"
      "v_add_f32_e32 v218, v218, v222\n\t"
      "v_add_f32_e32 v219, v219, v223\n\t"
      "s_waitcnt lgkmcnt(4)\n\t"
      "v_add_f32_e32 v224, v224, v228\n\t"
      "v_add_f32_e32 v225, v225, v229\n\t"
      "v_add_f32_e32 v226, v226, v230\n\t"
      "v_add_f32_e32 v227, v227, v231\n\t"
      "s_waitcnt lgkmcnt(2)\n\t"
      "v_add_f32_e32 v200, v200, v204\n\t"
      "v_add_f32_e32 v201, v201, v205\n\t"
      "v_add_f32_e32 v202, v202, v206\n\t"
      "v_add_f32_e32 v203, v203, v207\n\t"
      "v_add_f32_e32 v216, v216, v224\n\t"
      "v_add_f32_e32 v217, v217, v225\n\t"
      "v_add_f32_e32 v218, v218, v226\n\t"
      "v_add_f32_e32 v219, v219, v227\n\t"
      "s_waitcnt lgkmcnt(0)\n\t"
      "v_add_f32_e32 v208, v208, v212\n\t"
      "v_add_f32_e32 v209, v209, v213\n\t"
      "v_add_f32_e32 v210, v210, v214\n\t"
      "v_add_f32_e32 v211, v211, v215\n\t"
      "v_add_f32_e32 v200, v200, v208\n\t"
      "v_add_f32_e32 v201, v201, v209\n\t"
      "v_add_f32_e32 v202, v202, v210\n\t"
      "v_add_f32_e32 v203, v203, v211\n\t"
      "v_add_f32_e32 v216, v216, v200\n\t"
      "v_add_f32_e32 v217, v217, v201\n\t"
      "s_waitcnt vmcnt(8)\n\t"
      "v_mfma_f32_16x16x32_bf16 v[184:187], v[0:3], v[128:131], 0\n\t"
      "v_add_f32_e32 v218, v218, v202\n\t"
      "v_add_f32_e32 v219, v219, v203\n\t"
      "s_cmp_eq_u32 s17, 0\n\t"
      "s_cbranch_scc0 HI0_%=\n\t"
      "s_mov_b64 exec, 0xf\n\t"
      "global_store_dwordx2 v247, v[232:233], s[12:13] sc1\n\t"
      "s_mov_b64 exec, -1\n\t"
      "s_nop 0\n\t"
      "v_add_u32_e32 v232, 6, v232\n\t"
      "HI0_%=:\n\t"
      "v_mfma_f32_16x16x32_bf16 v[188:191], v[4:7], v[128:131], 0\n\t"
      "v_mul_f32_e32 v220, v216, v216\n\t"
      "v_mul_f32_e32 v221, v217, v217\n\t"
      "v_mfma_f32_16x16x32_bf16 v[192:195], v[8:11], v[128:131], 0\n\t"
      "v_mul_f32_e32 v222, v218, v218\n\t"
      "v_mul_f32_e32 v223, v219, v219\n\t"
      "v_mfma_f32_16x16x32_bf16 v[196:199], v[12:15], v[128:131], 0\n\t"
      "global_load_dwordx4 v[128:131], v176, s[58:59] sc1\n\t"
      "s_waitcnt vmcnt(8)\n\t"
      "v_mfma_f32_16x16x32_bf16 v[184:187], v[16:19], v[132:135], v[184:187]\n\t"
      "v_fmaak_f32 v220, v235, v220, 0xc0135761\n\t"
      "v_fmaak_f32 v221, v235, v221, 0xc0135761\n\t"
      "v_mfma_f32_16x16x32_bf16 v[188:191], v[20:23], v[132:135], v[188:191]\n\t"
      "v_fmaak_f32 v222, v235, v222, 0xc0135761\n\t"
      "v_fmaak_f32 v223, v235, v223, 0xc0135761\n\t"
      "v_mfma_f32_16x16x32_bf16 v[192:195], v[24:27], v[132:135], v[192:195]\n\t"
      "v_mul_f32_e32 v220, v216, v220\n\t"
      "v_mul_f32_e32 v221, v217, v221\n\t"
      "v_mfma_f32_16x16x32_bf16 v[196:199], v[28:31], v[132:135], v[196:199]\n\t"
      "global_load_dwordx4 v[132:135], v177, s[58:59] sc1\n\t"
      "s_waitcnt vmcnt(8)\n\t"
      "v_mfma_f32_16x16x32_bf16 v[184:187], v[32:35], v[136:139], v[184:187]\n\t"
      "v_mul_f32_e32 v222, v218, v222\n\t"
      "v_mul_f32_e32 v223, v219, v223\n\t"
      "v_mfma_f32_16x16x32_bf16 v[188:191], v[36:39], v[136:139], v[188:191]\n\t"
      "v_exp_f32_e32 v220, v220\n\t"
      "v_mfma_f32_16x16x32_bf16 v[192:195], v[40:43], v[136:139], v[192:195]\n\t"
      "v_exp_f32_e32 v221, v221\n\t"
      "v_mfma_f32_16x16x32_bf16 v[196:199], v[44:47], v[136:139], v[196:199]\n\t"
      "global_load_dwordx4 v[136:139], v178, s[58:59] sc1\n\t"
      "s_waitcnt vmcnt(8)\n\t"
      "v_mfma_f32_16x16x32_bf16 v[184:187], v[48:51], v[140:143], v[184:187]\n\t"
      "v_exp_f32_e32 v222, v222\n\t"
      "v_mfma_f32_16x16x32_bf16 v[188:191], v[52:55], v[140:143], v[188:191]\n\t"
      "v_exp_f32_e32 v223, v223\n\t"
      "v_mfma_f32_16x16x32_bf16 v[192:195], v[56:59], v[140:143], v[192:195]\n\t"
      "v_add_f32_e32 v220, 1.0, v220\n\t"
      "v_add_f32_e32 v221, 1.0, v221\n\t"
      "v_mfma_f32_16x16x32_bf16 v[196:199], v[60:63], v[140:143], v[196:199]\n\t"
      "global_load_dwordx4 v[140:143], v179, s[58:59] sc1\n\t"
      "s_waitcnt vmcnt(8)\n\t"
      "v_mfma_f32_16x16x32_bf16 v[184:187], v[64:67], v[144:147], v[184:187]\n\t"
      "v_add_f32_e32 v222, 1.0, v222\n\t"
      "v_add_f32_e32 v223, 1.0, v223\n\t"
      "v_mfma_f32_16x16x32_bf16 v[188:191], v[68:71], v[144:147], v[188:191]\n\t"
      "v_rcp_f32_e32 v220, v220\n\t"
      "v_mfma_f32_16x16x32_bf16 v[192:195], v[72:75], v[144:147], v[192:195]\n\t"
      "v_rcp_f32_e32 v221, v221\n\t"
      "v_mfma_f32_16x16x32_bf16 v[196:199], v[76:79], v[144:147], v[196:199]\n\t"
      "global_load_dwordx4 v[144:147], v180, s[58:59] sc1\n\t"
      "s_waitcnt vmcnt(8)\n\t"
      "v_mfma_f32_16x16x32_bf16 v[184:187], v[80:83], v[148:151], v[184:187]\n\t"
      "v_rcp_f32_e32 v222, v222\n\t"
      "v_mfma_f32_16x16x32_bf16 v[188:191], v[84:87], v[148:151], v[188:191]\n\t"
      "v_rcp_f32_e32 v223, v223\n\t"
      "v_mfma_f32_16x16x32_bf16 v[192:195], v[88:91], v[148:151], v[192:195]\n\t"
      "v_mul_f32_e32 v216, v216, v220\n\t"
      "v_mfma_f32_16x16x32_bf16 v[196:199], v[92:95], v[148:151], v[196:199]\n\t"
      "global_load_dwordx4 v[148:151], v181, s[58:59] sc1\n\t"
      "s_waitcnt vmcnt(7)\n\t"
      "v_mfma_f32_16x16x32_bf16 v[184:187], v[96:99], v[152:155], v[184:187]\n\t"
      "v_mul_f32_e32 v217, v217, v221\n\t"
      "v_mul_f32_e32 v218, v218, v222\n\t"
      "s_waitcnt vmcnt(6)\n\t"
      "v_mfma_f32_16x16x32_bf16 v[184:187], v[112:115], v[156:159], v[184:187]\n\t"
      "v_mul_f32_e32 v219, v219, v223\n\t"
      "v_cvt_pk_bf16_f32 v220, v216, v217\n\t"
      "v_mfma_f32_16x16x32_bf16 v[188:191], v[100:103], v[152:155], v[188:191]\n\t"
      "v_cvt_pk_bf16_f32 v221, v218, v219\n\t"
      "v_min_u32_e32 v220, -2, v220\n\t"
      "v_mfma_f32_16x16x32_bf16 v[188:191], v[116:119], v[156:159], v[188:191]\n\t"
      "s_nop 1\n\t"
      "v_cmp_u_f32_e32 vcc, v184, v184\n\t"
      "v_mfma_f32_16x16x32_bf16 v[192:195], v[104:107], v[152:155], v[192:195]\n\t"
      "v_min_u32_e32 v221, -2, v221\n\t"
      "global_store_dwordx2 v245, v[220:221], s[8:9] sc1\n\t"
      "s_add_u32 s8, s8, 131072\n\t"
      "s_addc_u32 s9, s9, 0\n\t"
      "v_mfma_f32_16x16x32_bf16 v[192:195], v[120:123], v[156:159], v[192:195]\n\t"
      "s_cbranch_vccnz NP2R_%=\n\t"
      "NP2B_%=:\n\t"
      "ds_write_b128 v243, v[184:187]\n\t"
      "v_mfma_f32_16x16x32_bf16 v[196:199], v[108:111], v[152:155], v[196:199]\n\t"
      "global_load_dwordx4 v[152:155], v182, s[58:59] sc1\n\t"
      "v_mfma_f32_16x16x32_bf16 v[196:199], v[124:127], v[156:159], v[196:199]\n\t"
      "global_load_dwordx4 v[156:159], v183, s[58:59] sc1\n\t"
      "ds_write_b128 v164, v[188:191]\n\t"
      "s_nop 0\n\t"
      "ds_write_b128 v165, v[192:195]\n\t"
      "s_nop 3\n\t"
      "ds_write_b128 v166, v[196:199]\n\t"
      "s_waitcnt lgkmcnt(0)\n\t"
      "s_barrier\n\t"
      "ds_read_b128 v[216:219], v244\n\t"
      "ds_read_b128 v[220:223], v244 offset:4096\n\t"
      "ds_read_b128 v[224:227], v244 offset:8192\n\t"
      "ds_read_b128 v[228:231], v244 offset:12288\n\t"
      "ds_read_b128 v[184:187], v244 offset:16384\n\t"
      "ds_read_b128 v[188:191], v244 offset:20480\n\t"
      "ds_read_b128 v[192:195], v244 offset:24576\n\t"
      "ds_read_b128 v[196:199], v244 offset:28672\n\t"
      "s_waitcnt lgkmcnt(6)\n\t"
      "v_add_f32_e32 v216, v216, v220\n\t"
      "v_add_f32_e32 v217, v217, v221\n\t"
      "v_add_f32_e32 v218, v218, v222\n\t"
      "v_add_f32_e32 v219, v219, v223\n\t"
      "s_waitcnt lgkmcnt(4)\n\t"
      "v_add_f32_e32 v224, v224, v228\n\t"
      "v_add_f32_e32 v225, v225, v229\n\t"
      "v_add_f32_e32 v226, v226, v230\n\t"
      "v_add_f32_e32 v227, v227, v231\n\t"
      "s_waitcnt lgkmcnt(2)\n\t"
      "v_add_f32_e32 v184, v184, v188\n\t"
      "v_add_f32_e32 v185, v185, v189\n\t"
      "v_add_f32_e32 v186, v186, v190\n\t"
      "v_add_f32_e32 v187, v187, v191\n\t"
      "v_add_f32_e32 v216, v216, v224\n\t"
      "v_add_f32_e32 v217, v217, v225\n\t"
      "v_add_f32_e32 v218, v218, v226\n\t"
      "v_add_f32_e32 v219, v219, v227\n\t"
      "s_waitcnt lgkmcnt(0)\n\t"
      "v_add_f32_e32 v192, v192, v196\n\t"
      "v_add_f32_e32 v193, v193, v197\n\t"
      "v_add_f32_e32 v194, v194, v198\n\t"
      "v_add_f32_e32 v195, v195, v199\n\t"
      "v_add_f32_e32 v184, v184, v192\n\t"
      "v_add_f32_e32 v185, v185, v193\n\t"
      "v_add_f32_e32 v186, v186, v194\n\t"
      "v_add_f32_e32 v187, v187, v195\n\t"
      "v_add_f32_e32 v216, v216, v184\n\t"
      "v_add_f32_e32 v217, v217, v185\n\t"
      "s_waitcnt vmcnt(8)\n\t"
      "v_mfma_f32_16x16x32_bf16 v[200:203], v[0:3], v[128:131], 0\n\t"
      "v_add_f32_e32 v218, v218, v186\n\t"
      "v_add_f32_e32 v219, v219, v187\n\t"
      "v_mfma_f32_16x16x32_bf16 v[204:207], v[4:7], v[128:131], 0\n\t"
      "v_mul_f32_e32 v220, v216, v216\n\t"
      "v_mul_f32_e32 v221, v217, v217\n\t"
      "v_mfma_f32_16x16x32_bf16 v[208:211], v[8:11], v[128:131], 0\n\t"
      "v_mul_f32_e32 v222, v218, v218\n\t"
      "v_mul_f32_e32 v223, v219, v219\n\t"
      "v_mfma_f32_16x16x32_bf16 v[212:215], v[12:15], v[128:131], 0\n\t"
      "global_load_dwordx4 v[128:131], v176, s[60:61] sc1\n\t"
      "s_waitcnt vmcnt(8)\n\t"
      "v_mfma_f32_16x16x32_bf16 v[200:203], v[16:19], v[132:135], v[200:203]\n\t"
      "v_fmaak_f32 v220, v235, v220, 0xc0135761\n\t"
      "v_fmaak_f32 v221, v235, v221, 0xc0135761\n\t"
      "v_mfma_f32_16x16x32_bf16 v[204:207], v[20:23], v[132:135], v[204:207]\n\t"
      "v_fmaak_f32 v222, v235, v222, 0xc0135761\n\t"
      "v_fmaak_f32 v223, v235, v223, 0xc0135761\n\t"
      "v_mfma_f32_16x16x32_bf16 v[208:211], v[24:27], v[132:135], v[208:211]\n\t"
      "v_mul_f32_e32 v220, v216, v220\n\t"
      "v_mul_f32_e32 v221, v217, v221\n\t"
      "v_mfma_f32_16x16x32_bf16 v[212:215], v[28:31], v[132:135], v[212:215]\n\t"
      "global_load_dwordx4 v[132:135], v177, s[60:61] sc1\n\t"
      "s_waitcnt vmcnt(8)\n\t"
      "v_mfma_f32_16x16x32_bf16 v[200:203], v[32:35], v[136:139], v[200:203]\n\t"
      "v_mul_f32_e32 v222, v218, v222\n\t"
      "v_mul_f32_e32 v223, v219, v223\n\t"
      "v_mfma_f32_16x16x32_bf16 v[204:207], v[36:39], v[136:139], v[204:207]\n\t"
      "v_exp_f32_e32 v220, v220\n\t"
      "v_mfma_f32_16x16x32_bf16 v[208:211], v[40:43], v[136:139], v[208:211]\n\t"
      "v_exp_f32_e32 v221, v221\n\t"
      "v_mfma_f32_16x16x32_bf16 v[212:215], v[44:47], v[136:139], v[212:215]\n\t"
      "global_load_dwordx4 v[136:139], v178, s[60:61] sc1\n\t"
      "s_waitcnt vmcnt(8)\n\t"
      "v_mfma_f32_16x16x32_bf16 v[200:203], v[48:51], v[140:143], v[200:203]\n\t"
      "v_exp_f32_e32 v222, v222\n\t"
      "v_mfma_f32_16x16x32_bf16 v[204:207], v[52:55], v[140:143], v[204:207]\n\t"
      "v_exp_f32_e32 v223, v223\n\t"
      "v_mfma_f32_16x16x32_bf16 v[208:211], v[56:59], v[140:143], v[208:211]\n\t"
      "v_add_f32_e32 v220, 1.0, v220\n\t"
      "v_add_f32_e32 v221, 1.0, v221\n\t"
      "v_mfma_f32_16x16x32_bf16 v[212:215], v[60:63], v[140:143], v[212:215]\n\t"
      "global_load_dwordx4 v[140:143], v179, s[60:61] sc1\n\t"
      "s_waitcnt vmcnt(8)\n\t"
      "v_mfma_f32_16x16x32_bf16 v[200:203], v[64:67], v[144:147], v[200:203]\n\t"
      "v_add_f32_e32 v222, 1.0, v222\n\t"
      "v_add_f32_e32 v223, 1.0, v223\n\t"
      "v_mfma_f32_16x16x32_bf16 v[204:207], v[68:71], v[144:147], v[204:207]\n\t"
      "v_rcp_f32_e32 v220, v220\n\t"
      "v_mfma_f32_16x16x32_bf16 v[208:211], v[72:75], v[144:147], v[208:211]\n\t"
      "v_rcp_f32_e32 v221, v221\n\t"
      "v_mfma_f32_16x16x32_bf16 v[212:215], v[76:79], v[144:147], v[212:215]\n\t"
      "global_load_dwordx4 v[144:147], v180, s[60:61] sc1\n\t"
      "s_waitcnt vmcnt(8)\n\t"
      "v_mfma_f32_16x16x32_bf16 v[200:203], v[80:83], v[148:151], v[200:203]\n\t"
      "v_rcp_f32_e32 v222, v222\n\t"
      "v_mfma_f32_16x16x32_bf16 v[204:207], v[84:87], v[148:151], v[204:207]\n\t"
      "v_rcp_f32_e32 v223, v223\n\t"
      "v_mfma_f32_16x16x32_bf16 v[208:211], v[88:91], v[148:151], v[208:211]\n\t"
      "v_mul_f32_e32 v216, v216, v220\n\t"
      "v_mfma_f32_16x16x32_bf16 v[212:215], v[92:95], v[148:151], v[212:215]\n\t"
      "global_load_dwordx4 v[148:151], v181, s[60:61] sc1\n\t"
      "s_waitcnt vmcnt(7)\n\t"
      "v_mfma_f32_16x16x32_bf16 v[200:203], v[96:99], v[152:155], v[200:203]\n\t"
      "v_mul_f32_e32 v217, v217, v221\n\t"
      "v_mul_f32_e32 v218, v218, v222\n\t"
      "s_waitcnt vmcnt(6)\n\t"
      "v_mfma_f32_16x16x32_bf16 v[200:203], v[112:115], v[156:159], v[200:203]\n\t"
      "v_mul_f32_e32 v219, v219, v223\n\t"
      "v_cvt_pk_bf16_f32 v220, v216, v217\n\t"
      "v_mfma_f32_16x16x32_bf16 v[204:207], v[100:103], v[152:155], v[204:207]\n\t"
      "v_cvt_pk_bf16_f32 v221, v218, v219\n\t"
      "v_min_u32_e32 v220, -2, v220\n\t"
      "v_mfma_f32_16x16x32_bf16 v[204:207], v[116:119], v[156:159], v[204:207]\n\t"
      "s_nop 1\n\t"
      "v_cmp_u_f32_e32 vcc, v200, v200\n\t"
      "v_mfma_f32_16x16x32_bf16 v[208:211], v[104:107], v[152:155], v[208:211]\n\t"
      "v_min_u32_e32 v221, -2, v221\n\t"
      "global_store_dwordx2 v245, v[220:221], s[8:9] sc1\n\t"
      "s_add_u32 s8, s8, 131072\n\t"
      "s_addc_u32 s9, s9, 0\n\t"
      "v_mfma_f32_16x16x32_bf16 v[208:211], v[120:123], v[156:159], v[208:211]\n\t"
      "s_cbranch_vccnz NP3R_%=\n\t"
      "NP3B_%=:\n\t"
      "ds_write_b128 v243, v[200:203] offset:32768\n\t"
      "v_mfma_f32_16x16x32_bf16 v[212:215], v[108:111], v[152:155], v[212:215]\n\t"
      "global_load_dwordx4 v[152:155], v182, s[60:61] sc1\n\t"
      "v_mfma_f32_16x16x32_bf16 v[212:215], v[124:127], v[156:159], v[212:215]\n\t"
      "global_load_dwordx4 v[156:159], v183, s[60:61] sc1\n\t"
      "ds_write_b128 v164, v[204:207] offset:32768\n\t"
      "s_nop 0\n\t"
      "ds_write_b128 v165, v[208:211] offset:32768\n\t"
      "s_nop 3\n\t"
      "ds_write_b128 v166, v[212:215] offset:32768\n\t"
      "s_waitcnt lgkmcnt(0)\n\t"
      "s_barrier\n\t"
      "ds_read_b128 v[216:219], v244 offset:32768\n\t"
      "ds_read_b128 v[220:223], v244 offset:36864\n\t"
      "ds_read_b128 v[224:227], v244 offset:40960\n\t"
      "ds_read_b128 v[228:231], v244 offset:45056\n\t"
      "ds_read_b128 v[200:203], v244 offset:49152\n\t"
      "ds_read_b128 v[204:207], v244 offset:53248\n\t"
      "ds_read_b128 v[208:211], v244 offset:57344\n\t"
      "ds_read_b128 v[212:215], v244 offset:61440\n\t"
      "s_waitcnt lgkmcnt(6)\n\t"
      "v_add_f32_e32 v216, v216, v220\n\t"
      "v_add_f32_e32 v217, v217, v221\n\t"
      "v_add_f32_e32 v218, v218, v222\n\t"
      "v_add_f32_e32 v219, v219, v223\n\t"
      "s_waitcnt lgkmcnt(4)\n\t"
      "v_add_f32_e32 v224, v224, v228\n\t"
      "v_add_f32_e32 v225, v225, v229\n\t"
      "v_add_f32_e32 v226, v226, v230\n\t"
      "v_add_f32_e32 v227, v227, v231\n\t"
      "s_waitcnt lgkmcnt(2)\n\t"
      "v_add_f32_e32 v200, v200, v204\n\t"
      "v_add_f32_e32 v201, v201, v205\n\t"
      "v_add_f32_e32 v202, v202, v206\n\t"
      "v_add_f32_e32 v203, v203, v207\n\t"
      "v_add_f32_e32 v216, v216, v224\n\t"
      "v_add_f32_e32 v217, v217, v225\n\t"
      "v_add_f32_e32 v218, v218, v226\n\t"
      "v_add_f32_e32 v219, v219, v227\n\t"
      "s_waitcnt lgkmcnt(0)\n\t"
      "v_add_f32_e32 v208, v208, v212\n\t"
      "v_add_f32_e32 v209, v209, v213\n\t"
      "v_add_f32_e32 v210, v210, v214\n\t"
      "v_add_f32_e32 v211, v211, v215\n\t"
      "v_add_f32_e32 v200, v200, v208\n\t"
      "v_add_f32_e32 v201, v201, v209\n\t"
      "v_add_f32_e32 v202, v202, v210\n\t"
      "v_add_f32_e32 v203, v203, v211\n\t"
      "v_add_f32_e32 v216, v216, v200\n\t"
      "v_add_f32_e32 v217, v217, v201\n\t"
      "s_waitcnt vmcnt(8)\n\t"
      "v_mfma_f32_16x16x32_bf16 v[184:187], v[0:3], v[128:131], 0\n\t"
      "v_add_f32_e32 v218, v218, v202\n\t"
      "v_add_f32_e32 v219, v219, v203\n\t"
      "v_mfma_f32_16x16x32_bf16 v[188:191], v[4:7], v[128:131], 0\n\t"
      "v_mul_f32_e32 v220, v216, v216\n\t"
      "v_mul_f32_e32 v221, v217, v217\n\t"
      "v_mfma_f32_16x16x32_bf16 v[192:195], v[8:11], v[128:131], 0\n\t"
      "v_mul_f32_e32 v222, v218, v218\n\t"
      "v_mul_f32_e32 v223, v219, v219\n\t"
      "v_mfma_f32_16x16x32_bf16 v[196:199], v[12:15], v[128:131], 0\n\t"
      "global_load_dwordx4 v[128:131], v176, s[62:63] sc1\n\t"
      "s_waitcnt vmcnt(8)\n\t"
      "v_mfma_f32_16x16x32_bf16 v[184:187], v[16:19], v[132:135], v[184:187]\n\t"
      "v_fmaak_f32 v220, v235, v220, 0xc0135761\n\t"
      "v_fmaak_f32 v221, v235, v221, 0xc0135761\n\t"
      "v_mfma_f32_16x16x32_bf16 v[188:191], v[20:23], v[132:135], v[188:191]\n\t"
      "v_fmaak_f32 v222, v235, v222, 0xc0135761\n\t"
      "v_fmaak_f32 v223, v235, v223, 0xc0135761\n\t"
      "v_mfma_f32_16x16x32_bf16 v[192:195], v[24:27], v[132:135], v[192:195]\n\t"
      "v_mul_f32_e32 v220, v216, v220\n\t"
      "v_mul_f32_e32 v221, v217, v221\n\t"
      "v_mfma_f32_16x16x32_bf16 v[196:199], v[28:31], v[132:135], v[196:199]\n\t"
      "global_load_dwordx4 v[132:135], v177, s[62:63] sc1\n\t"
      "s_waitcnt vmcnt(8)\n\t"
      "v_mfma_f32_16x16x32_bf16 v[184:187], v[32:35], v[136:139], v[184:187]\n\t"
      "v_mul_f32_e32 v222, v218, v222\n\t"
      "v_mul_f32_e32 v223, v219, v223\n\t"
      "v_mfma_f32_16x16x32_bf16 v[188:191], v[36:39], v[136:139], v[188:191]\n\t"
      "v_exp_f32_e32 v220, v220\n\t"
      "v_mfma_f32_16x16x32_bf16 v[192:195], v[40:43], v[136:139], v[192:195]\n\t"
      "v_exp_f32_e32 v221, v221\n\t"
      "v_mfma_f32_16x16x32_bf16 v[196:199], v[44:47], v[136:139], v[196:199]\n\t"
      "global_load_dwordx4 v[136:139], v178, s[62:63] sc1\n\t"
      "s_waitcnt vmcnt(8)\n\t"
      "v_mfma_f32_16x16x32_bf16 v[184:187], v[48:51], v[140:143], v[184:187]\n\t"
      "v_exp_f32_e32 v222, v222\n\t"
      "v_mfma_f32_16x16x32_bf16 v[188:191], v[52:55], v[140:143], v[188:191]\n\t"
      "v_exp_f32_e32 v223, v223\n\t"
      "v_mfma_f32_16x16x32_bf16 v[192:195], v[56:59], v[140:143], v[192:195]\n\t"
      "v_add_f32_e32 v220, 1.0, v220\n\t"
      "v_add_f32_e32 v221, 1.0, v221\n\t"
      "v_mfma_f32_16x16x32_bf16 v[196:199], v[60:63], v[140:143], v[196:199]\n\t"
      "global_load_dwordx4 v[140:143], v179, s[62:63] sc1\n\t"
      "s_waitcnt vmcnt(8)\n\t"
      "v_mfma_f32_16x16x32_bf16 v[184:187], v[64:67], v[144:147], v[184:187]\n\t"
      "v_add_f32_e32 v222, 1.0, v222\n\t"
      "v_add_f32_e32 v223, 1.0, v223\n\t"
      "v_mfma_f32_16x16x32_bf16 v[188:191], v[68:71], v[144:147], v[188:191]\n\t"
      "v_rcp_f32_e32 v220, v220\n\t"
      "v_mfma_f32_16x16x32_bf16 v[192:195], v[72:75], v[144:147], v[192:195]\n\t"
      "v_rcp_f32_e32 v221, v221\n\t"
      "v_mfma_f32_16x16x32_bf16 v[196:199], v[76:79], v[144:147], v[196:199]\n\t"
      "global_load_dwordx4 v[144:147], v180, s[62:63] sc1\n\t"
      "s_waitcnt vmcnt(8)\n\t"
      "v_mfma_f32_16x16x32_bf16 v[184:187], v[80:83], v[148:151], v[184:187]\n\t"
      "v_rcp_f32_e32 v222, v222\n\t"
      "v_mfma_f32_16x16x32_bf16 v[188:191], v[84:87], v[148:151], v[188:191]\n\t"
      "v_rcp_f32_e32 v223, v223\n\t"
      "v_mfma_f32_16x16x32_bf16 v[192:195], v[88:91], v[148:151], v[192:195]\n\t"
      "v_mul_f32_e32 v216, v216, v220\n\t"
      "v_mfma_f32_16x16x32_bf16 v[196:199], v[92:95], v[148:151], v[196:199]\n\t"
      "global_load_dwordx4 v[148:151], v181, s[62:63] sc1\n\t"
      "s_waitcnt vmcnt(7)\n\t"
      "v_mfma_f32_16x16x32_bf16 v[184:187], v[96:99], v[152:155], v[184:187]\n\t"
      "v_mul_f32_e32 v217, v217, v221\n\t"
      "v_mul_f32_e32 v218, v218, v222\n\t"
      "s_waitcnt vmcnt(6)\n\t"
      "v_mfma_f32_16x16x32_bf16 v[184:187], v[112:115], v[156:159], v[184:187]\n\t"
      "v_mul_f32_e32 v219, v219, v223\n\t"
      "v_cvt_pk_bf16_f32 v220, v216, v217\n\t"
      "v_mfma_f32_16x16x32_bf16 v[188:191], v[100:103], v[152:155], v[188:191]\n\t"
      "v_cvt_pk_bf16_f32 v221, v218, v219\n\t"
      "v_min_u32_e32 v220, -2, v220\n\t"
      "v_mfma_f32_16x16x32_bf16 v[188:191], v[116:119], v[156:159], v[188:191]\n\t"
      "s_nop 1\n\t"
      "v_cmp_u_f32_e32 vcc, v184, v184\n\t"
      "v_mfma_f32_16x16x32_bf16 v[192:195], v[104:107], v[152:155], v[192:195]\n\t"
      "v_min_u32_e32 v221, -2, v221\n\t"
      "global_store_dwordx2 v245, v[220:221], s[8:9] sc1\n\t"
      "s_add_u32 s8, s8, 131072\n\t"
      "s_addc_u32 s9, s9, 0\n\t"
      "v_mfma_f32_16x16x32_bf16 v[192:195], v[120:123], v[156:159], v[192:195]\n\t"
      "s_cbranch_vccnz NP4R_%=\n\t"
      "NP4B_%=:\n\t"
      "ds_write_b128 v243, v[184:187]\n\t"
      "v_mfma_f32_16x16x32_bf16 v[196:199], v[108:111], v[152:155], v[196:199]\n\t"
      "global_load_dwordx4 v[152:155], v182, s[62:63] sc1\n\t"
      "v_mfma_f32_16x16x32_bf16 v[196:199], v[124:127], v[156:159], v[196:199]\n\t"
      "global_load_dwordx4 v[156:159], v183, s[62:63] sc1\n\t"
      "ds_write_b128 v164, v[188:191]\n\t"
      "s_nop 0\n\t"
      "ds_write_b128 v165, v[192:195]\n\t"
      "s_nop 3\n\t"
      "ds_write_b128 v166, v[196:199]\n\t"
      "s_waitcnt lgkmcnt(0)\n\t"
      "s_barrier\n\t"
      "ds_read_b128 v[216:219], v244\n\t"
      "ds_read_b128 v[220:223], v244 offset:4096\n\t"
      "ds_read_b128 v[224:227], v244 offset:8192\n\t"
      "ds_read_b128 v[228:231], v244 offset:12288\n\t"
      "ds_read_b128 v[184:187], v244 offset:16384\n\t"
      "ds_read_b128 v[188:191], v244 offset:20480\n\t"
      "ds_read_b128 v[192:195], v244 offset:24576\n\t"
      "ds_read_b128 v[196:199], v244 offset:28672\n\t"
      "s_waitcnt lgkmcnt(6)\n\t"
      "v_add_f32_e32 v216, v216, v220\n\t"
      "v_add_f32_e32 v217, v217, v221\n\t"
      "v_add_f32_e32 v218, v218, v222\n\t"
      "v_add_f32_e32 v219, v219, v223\n\t"
      "s_waitcnt lgkmcnt(4)\n\t"
      "v_add_f32_e32 v224, v224, v228\n\t"
      "v_add_f32_e32 v225, v225, v229\n\t"
      "v_add_f32_e32 v226, v226, v230\n\t"
      "v_add_f32_e32 v227, v227, v231\n\t"
      "s_waitcnt lgkmcnt(2)\n\t"
      "v_add_f32_e32 v184, v184, v188\n\t"
      "v_add_f32_e32 v185, v185, v189\n\t"
      "v_add_f32_e32 v186, v186, v190\n\t"
      "v_add_f32_e32 v187, v187, v191\n\t"
      "v_add_f32_e32 v216, v216, v224\n\t"
      "v_add_f32_e32 v217, v217, v225\n\t"
      "v_add_f32_e32 v218, v218, v226\n\t"
      "v_add_f32_e32 v219, v219, v227\n\t"
      "s_waitcnt lgkmcnt(0)\n\t"
      "v_add_f32_e32 v192, v192, v196\n\t"
      "v_add_f32_e32 v193, v193, v197\n\t"
      "v_add_f32_e32 v194, v194, v198\n\t"
      "v_add_f32_e32 v195, v195, v199\n\t"
      "v_add_f32_e32 v184, v184, v192\n\t"
      "v_add_f32_e32 v185, v185, v193\n\t"
      "v_add_f32_e32 v186, v186, v194\n\t"
      "v_add_f32_e32 v187, v187, v195\n\t"
      "v_add_f32_e32 v216, v216, v184\n\t"
      "v_add_f32_e32 v217, v217, v185\n\t"
      "s_waitcnt vmcnt(8)\n\t"
      "v_mfma_f32_16x16x32_bf16 v[200:203], v[0:3], v[128:131], 0\n\t"
      "v_add_f32_e32 v218, v218, v186\n\t"
      "v_add_f32_e32 v219, v219, v187\n\t"
      "v_mfma_f32_16x16x32_bf16 v[204:207], v[4:7], v[128:131], 0\n\t"
      "v_mul_f32_e32 v220, v216, v216\n\t"
      "v_mul_f32_e32 v221, v217, v217\n\t"
      "v_mfma_f32_16x16x32_bf16 v[208:211], v[8:11], v[128:131], 0\n\t"
      "v_mul_f32_e32 v222, v218, v218\n\t"
      "v_mul_f32_e32 v223, v219, v219\n\t"
      "v_mfma_f32_16x16x32_bf16 v[212:215], v[12:15], v[128:131], 0\n\t"
      "global_load_dwordx4 v[128:131], v176, s[64:65] sc1\n\t"
      "s_waitcnt vmcnt(8)\n\t"
      "v_mfma_f32_16x16x32_bf16 v[200:203], v[16:19], v[132:135], v[200:203]\n\t"
      "v_fmaak_f32 v220, v235, v220, 0xc0135761\n\t"
      "v_fmaak_f32 v221, v235, v221, 0xc0135761\n\t"
      "v_mfma_f32_16x16x32_bf16 v[204:207], v[20:23], v[132:135], v[204:207]\n\t"
      "v_fmaak_f32 v222, v235, v222, 0xc0135761\n\t"
      "v_fmaak_f32 v223, v235, v223, 0xc0135761\n\t"
      "v_mfma_f32_16x16x32_bf16 v[208:211], v[24:27], v[132:135], v[208:211]\n\t"
      "v_mul_f32_e32 v220, v216, v220\n\t"
      "v_mul_f32_e32 v221, v217, v221\n\t"
      "v_mfma_f32_16x16x32_bf16 v[212:215], v[28:31], v[132:135], v[212:215]\n\t"
      "global_load_dwordx4 v[132:135], v177, s[64:65] sc1\n\t"
      "s_waitcnt vmcnt(8)\n\t"
      "v_mfma_f32_16x16x32_bf16 v[200:203], v[32:35], v[136:139], v[200:203]\n\t"
      "v_mul_f32_e32 v222, v218, v222\n\t"
      "v_mul_f32_e32 v223, v219, v223\n\t"
      "v_mfma_f32_16x16x32_bf16 v[204:207], v[36:39], v[136:139], v[204:207]\n\t"
      "v_exp_f32_e32 v220, v220\n\t"
      "v_mfma_f32_16x16x32_bf16 v[208:211], v[40:43], v[136:139], v[208:211]\n\t"
      "v_exp_f32_e32 v221, v221\n\t"
      "v_mfma_f32_16x16x32_bf16 v[212:215], v[44:47], v[136:139], v[212:215]\n\t"
      "global_load_dwordx4 v[136:139], v178, s[64:65] sc1\n\t"
      "s_waitcnt vmcnt(8)\n\t"
      "v_mfma_f32_16x16x32_bf16 v[200:203], v[48:51], v[140:143], v[200:203]\n\t"
      "v_exp_f32_e32 v222, v222\n\t"
      "v_mfma_f32_16x16x32_bf16 v[204:207], v[52:55], v[140:143], v[204:207]\n\t"
      "v_exp_f32_e32 v223, v223\n\t"
      "v_mfma_f32_16x16x32_bf16 v[208:211], v[56:59], v[140:143], v[208:211]\n\t"
      "v_add_f32_e32 v220, 1.0, v220\n\t"
      "v_add_f32_e32 v221, 1.0, v221\n\t"
      "v_mfma_f32_16x16x32_bf16 v[212:215], v[60:63], v[140:143], v[212:215]\n\t"
      "global_load_dwordx4 v[140:143], v179, s[64:65] sc1\n\t"
      "s_waitcnt vmcnt(8)\n\t"
      "v_mfma_f32_16x16x32_bf16 v[200:203], v[64:67], v[144:147], v[200:203]\n\t"
      "v_add_f32_e32 v222, 1.0, v222\n\t"
      "v_add_f32_e32 v223, 1.0, v223\n\t"
      "v_mfma_f32_16x16x32_bf16 v[204:207], v[68:71], v[144:147], v[204:207]\n\t"
      "v_rcp_f32_e32 v220, v220\n\t"
      "v_mfma_f32_16x16x32_bf16 v[208:211], v[72:75], v[144:147], v[208:211]\n\t"
      "v_rcp_f32_e32 v221, v221\n\t"
      "v_mfma_f32_16x16x32_bf16 v[212:215], v[76:79], v[144:147], v[212:215]\n\t"
      "global_load_dwordx4 v[144:147], v180, s[64:65] sc1\n\t"
      "s_waitcnt vmcnt(8)\n\t"
      "v_mfma_f32_16x16x32_bf16 v[200:203], v[80:83], v[148:151], v[200:203]\n\t"
      "v_rcp_f32_e32 v222, v222\n\t"
      "v_mfma_f32_16x16x32_bf16 v[204:207], v[84:87], v[148:151], v[204:207]\n\t"
      "v_rcp_f32_e32 v223, v223\n\t"
      "v_mfma_f32_16x16x32_bf16 v[208:211], v[88:91], v[148:151], v[208:211]\n\t"
      "v_mul_f32_e32 v216, v216, v220\n\t"
      "v_mfma_f32_16x16x32_bf16 v[212:215], v[92:95], v[148:151], v[212:215]\n\t"
      "global_load_dwordx4 v[148:151], v181, s[64:65] sc1\n\t"
      "s_waitcnt vmcnt(7)\n\t"
      "v_mfma_f32_16x16x32_bf16 v[200:203], v[96:99], v[152:155], v[200:203]\n\t"
      "v_mul_f32_e32 v217, v217, v221\n\t"
      "v_mul_f32_e32 v218, v218, v222\n\t"
      "s_waitcnt vmcnt(6)\n\t"
      "v_mfma_f32_16x16x32_bf16 v[200:203], v[112:115], v[156:159], v[200:203]\n\t"
      "v_mul_f32_e32 v219, v219, v223\n\t"
      "v_cvt_pk_bf16_f32 v220, v216, v217\n\t"
      "v_mfma_f32_16x16x32_bf16 v[204:207], v[100:103], v[152:155], v[204:207]\n\t"
      "v_cvt_pk_bf16_f32 v221, v218, v219\n\t"
      "v_min_u32_e32 v220, -2, v220\n\t"
      "v_mfma_f32_16x16x32_bf16 v[204:207], v[116:119], v[156:159], v[204:207]\n\t"
      "s_nop 1\n\t"
      "v_cmp_u_f32_e32 vcc, v200, v200\n\t"
      "v_mfma_f32_16x16x32_bf16 v[208:211], v[104:107], v[152:155], v[208:211]\n\t"
      "v_min_u32_e32 v221, -2, v221\n\t"
      "global_store_dwordx2 v245, v[220:221], s[8:9] sc1\n\t"
      "s_add_u32 s8, s8, 131072\n\t"
      "s_addc_u32 s9, s9, 0\n\t"
      "v_mfma_f32_16x16x32_bf16 v[208:211], v[120:123], v[156:159], v[208:211]\n\t"
      "s_cbranch_vccnz NP5R_%=\n\t"
      "NP5B_%=:\n\t"
      "ds_write_b128 v243, v[200:203] offset:32768\n\t"
      "v_mfma_f32_16x16x32_bf16 v[212:215], v[108:111], v[152:155], v[212:215]\n\t"
      "global_load_dwordx4 v[152:155], v182, s[64:65] sc1\n\t"
      "v_mfma_f32_16x16x32_bf16 v[212:215], v[124:127], v[156:159], v[212:215]\n\t"
      "global_load_dwordx4 v[156:159], v183, s[64:65] sc1\n\t"
      "ds_write_b128 v164, v[204:207] offset:32768\n\t"
      "s_nop 0\n\t"
      "ds_write_b128 v165, v[208:211] offset:32768\n\t"
      "s_nop 3\n\t"
      "ds_write_b128 v166, v[212:215] offset:32768\n\t"
      "s_waitcnt lgkmcnt(0)\n\t"
      "s_barrier\n\t"
      "ds_read_b128 v[216:219], v244 offset:32768\n\t"
      "ds_read_b128 v[220:223], v244 offset:36864\n\t"
      "ds_read_b128 v[224:227], v244 offset:40960\n\t"
      "ds_read_b128 v[228:231], v244 offset:45056\n\t"
      "ds_read_b128 v[200:203], v244 offset:49152\n\t"
      "ds_read_b128 v[204:207], v244 offset:53248\n\t"
      "ds_read_b128 v[208:211], v244 offset:57344\n\t"
      "ds_read_b128 v[212:215], v244 offset:61440\n\t"
      "s_waitcnt lgkmcnt(6)\n\t"
      "v_add_f32_e32 v216, v216, v220\n\t"
      "v_add_f32_e32 v217, v217, v221\n\t"
      "v_add_f32_e32 v218, v218, v222\n\t"
      "v_add_f32_e32 v219, v219, v223\n\t"
      "s_waitcnt lgkmcnt(4)\n\t"
      "v_add_f32_e32 v224, v224, v228\n\t"
      "v_add_f32_e32 v225, v225, v229\n\t"
      "v_add_f32_e32 v226, v226, v230\n\t"
      "v_add_f32_e32 v227, v227, v231\n\t"
      "s_waitcnt lgkmcnt(2)\n\t"
      "v_add_f32_e32 v200, v200, v204\n\t"
      "v_add_f32_e32 v201, v201, v205\n\t"
      "v_add_f32_e32 v202, v202, v206\n\t"
      "v_add_f32_e32 v203, v203, v207\n\t"
      "s_waitcnt vmcnt(8)\n\t"
      "v_mfma_f32_16x16x32_bf16 v[184:187], v[0:3], v[128:131], 0\n\t"
      "v_add_f32_e32 v216, v216, v224\n\t"
      "v_add_f32_e32 v217, v217, v225\n\t"
      "v_mfma_f32_16x16x32_bf16 v[188:191], v[4:7], v[128:131], 0\n\t"
      "v_add_f32_e32 v218, v218, v226\n\t"
      "v_add_f32_e32 v219, v219, v227\n\t"
      "v_mfma_f32_16x16x32_bf16 v[192:195], v[8:11], v[128:131], 0\n\t"
      "s_waitcnt lgkmcnt(0)\n\t"
      "v_add_f32_e32 v208, v208, v212\n\t"
      "v_add_f32_e32 v209, v209, v213\n\t"
      "v_mfma_f32_16x16x32_bf16 v[196:199], v[12:15], v[128:131], 0\n\t"
      "v_add_f32_e32 v210, v210, v214\n\t"
      "v_add_f32_e32 v211, v211, v215\n\t"
      "s_waitcnt vmcnt(7)\n\t"
      "v_mfma_f32_16x16x32_bf16 v[184:187], v[16:19], v[132:135], v[184:187]\n\t"
      "v_add_f32_e32 v200, v200, v208\n\t"
      "v_add_f32_e32 v201, v201, v209\n\t"
      "v_mfma_f32_16x16x32_bf16 v[188:191], v[20:23], v[132:135], v[188:191]\n\t"
      "v_add_f32_e32 v202, v202, v210\n\t"
      "v_add_f32_e32 v203, v203, v211\n\t"
      "v_mfma_f32_16x16x32_bf16 v[192:195], v[24:27], v[132:135], v[192:195]\n\t"
      "v_add_f32_e32 v216, v216, v200\n\t"
      "v_add_f32_e32 v217, v217, v201\n\t"
      "v_mfma_f32_16x16x32_bf16 v[196:199], v[28:31], v[132:135], v[196:199]\n\t"
      "v_add_f32_e32 v218, v218, v202\n\t"
      "v_add_f32_e32 v219, v219, v203\n\t"
      "s_waitcnt vmcnt(6)\n\t"
      "v_mfma_f32_16x16x32_bf16 v[184:187], v[32:35], v[136:139], v[184:187]\n\t"
      "v_mul_f32_e32 v220, v216, v216\n\t"
      "v_mul_f32_e32 v221, v217, v217\n\t"
      "v_mfma_f32_16x16x32_bf16 v[188:191], v[36:39], v[136:139], v[188:191]\n\t"
      "v_mul_f32_e32 v222, v218, v218\n\t"
      "v_mul_f32_e32 v223, v219, v219\n\t"
      "v_mfma_f32_16x16x32_bf16 v[192:195], v[40:43], v[136:139], v[192:195]\n\t"
      "v_fmaak_f32 v220, v235, v220, 0xc0135761\n\t"
      "v_fmaak_f32 v221, v235, v221, 0xc0135761\n\t"
      "v_mfma_f32_16x16x32_bf16 v[196:199], v[44:47], v[136:139], v[196:199]\n\t"
      "v_fmaak_f32 v222, v235, v222, 0xc0135761\n\t"
      "v_fmaak_f32 v223, v235, v223, 0xc0135761\n\t"
      "s_waitcnt vmcnt(5)\n\t"
      "v_mfma_f32_16x16x32_bf16 v[184:187], v[48:51], v[140:143], v[184:187]\n\t"
      "v_mul_f32_e32 v220, v216, v220\n\t"
      "v_mul_f32_e32 v221, v217, v221\n\t"
      "v_mfma_f32_16x16x32_bf16 v[188:191], v[52:55], v[140:143], v[188:191]\n\t"
      "v_mul_f32_e32 v222, v218, v222\n\t"
      "v_mul_f32_e32 v223, v219, v223\n\t"
      "v_mfma_f32_16x16x32_bf16 v[192:195], v[56:59], v[140:143], v[192:195]\n\t"
      "v_exp_f32_e32 v220, v220\n\t"
      "v_mfma_f32_16x16x32_bf16 v[196:199], v[60:63], v[140:143], v[196:199]\n\t"
      "v_exp_f32_e32 v221, v221\n\t"
      "s_waitcnt vmcnt(4)\n\t"
      "v_mfma_f32_16x16x32_bf16 v[184:187], v[64:67], v[144:147], v[184:187]\n\t"
      "v_exp_f32_e32 v222, v222\n\t"
      "v_mfma_f32_16x16x32_bf16 v[188:191], v[68:71], v[144:147], v[188:191]\n\t"
      "v_exp_f32_e32 v223, v223\n\t"
      "v_mfma_f32_16x16x32_bf16 v[192:195], v[72:75], v[144:147], v[192:195]\n\t"
      "v_add_f32_e32 v220, 1.0, v220\n\t"
      "v_add_f32_e32 v221, 1.0, v221\n\t"
      "v_mfma_f32_16x16x32_bf16 v[196:199], v[76:79], v[144:147], v[196:199]\n\t"
      "v_add_f32_e32 v222, 1.0, v222\n\t"
      "v_add_f32_e32 v223, 1.0, v223\n\t"
      "s_waitcnt vmcnt(3)\n\t"
      "v_mfma_f32_16x16x32_bf16 v[184:187], v[80:83], v[148:151], v[184:187]\n\t"
      "v_rcp_f32_e32 v220, v220\n\t"
      "v_mfma_f32_16x16x32_bf16 v[188:191], v[84:87], v[148:151], v[188:191]\n\t"
      "v_rcp_f32_e32 v221, v221\n\t"
      "v_mfma_f32_16x16x32_bf16 v[192:195], v[88:91], v[148:151], v[192:195]\n\t"
      "v_rcp_f32_e32 v222, v222\n\t"
      "v_mfma_f32_16x16x32_bf16 v[196:199], v[92:95], v[148:151], v[196:199]\n\t"
      "v_rcp_f32_e32 v223, v223\n\t"
      "s_waitcnt vmcnt(1)\n\t"
      "v_mfma_f32_16x16x32_bf16 v[184:187], v[96:99], v[152:155], v[184:187]\n\t"
      "v_mul_f32_e32 v216, v216, v220\n\t"
      "s_waitcnt vmcnt(0)\n\t"
      "v_mfma_f32_16x16x32_bf16 v[184:187], v[112:115], v[156:159], v[184:187]\n\t"
      "v_mul_f32_e32 v217, v217, v221\n\t"
      "v_mul_f32_e32 v218, v218, v222\n\t"
      "v_mfma_f32_16x16x32_bf16 v[188:191], v[100:103], v[152:155], v[188:191]\n\t"
      "v_mul_f32_e32 v219, v219, v223\n\t"
      "v_cvt_pk_bf16_f32 v220, v216, v217\n\t"
      "v_mfma_f32_16x16x32_bf16 v[188:191], v[116:119], v[156:159], v[188:191]\n\t"
      "s_nop 1\n\t"
      "v_cmp_u_f32_e32 vcc, v184, v184\n\t"
      "v_cvt_pk_bf16_f32 v221, v218, v219\n\t"
      "v_min_u32_e32 v220, -2, v220\n\t"
      "v_mfma_f32_16x16x32_bf16 v[192:195], v[104:107], v[152:155], v[192:195]\n\t"
      "v_min_u32_e32 v221, -2, v221\n\t"
      "global_store_dwordx2 v245, v[220:221], s[8:9] sc1\n\t"
      "s_add_u32 s8, s8, 131072\n\t"
      "s_addc_u32 s9, s9, 0\n\t"
      "v_mfma_f32_16x16x32_bf16 v[192:195], v[120:123], v[156:159], v[192:195]\n\t"
      "s_cbranch_vccnz NP6R_%=\n\t"
      "NP6B_%=:\n\t"
      "ds_write_b128 v243, v[184:187]\n\t"
      "v_mfma_f32_16x16x32_bf16 v[196:199], v[108:111], v[152:155], v[196:199]\n\t"
      "v_mfma_f32_16x16x32_bf16 v[196:199], v[124:127], v[156:159], v[196:199]\n\t"
      "ds_write_b128 v164, v[188:191]\n\t"
      "s_nop 2\n\t"
      "ds_write_b128 v165, v[192:195]\n\t"
      "s_nop 2\n\t"
      "ds_write_b128 v166, v[196:199]\n\t"
      "s_waitcnt lgkmcnt(0)\n\t"
      "s_barrier\n\t"
      "ds_read_b128 v[216:219], v244\n\t"
      "ds_read_b128 v[220:223], v244 offset:4096\n\t"
      "ds_read_b128 v[224:227], v244 offset:8192\n\t"
      "ds_read_b128 v[228:231], v244 offset:12288\n\t"
      "ds_read_b128 v[184:187], v244 offset:16384\n\t"
      "ds_read_b128 v[188:191], v244 offset:20480\n\t"
      "ds_read_b128 v[192:195], v244 offset:24576\n\t"
      "ds_read_b128 v[196:199], v244 offset:28672\n\t"
      "s_waitcnt lgkmcnt(6)\n\t"
      "v_add_f32_e32 v216, v216, v220\n\t"
      "v_add_f32_e32 v217, v217, v221\n\t"
      "v_add_f32_e32 v218, v218, v222\n\t"
      "v_add_f32_e32 v219, v219, v223\n\t"
      "s_waitcnt lgkmcnt(4)\n\t"
      "v_add_f32_e32 v224, v224, v228\n\t"
      "v_add_f32_e32 v225, v225, v229\n\t"
      "v_add_f32_e32 v226, v226, v230\n\t"
      "v_add_f32_e32 v227, v227, v231\n\t"
      "s_waitcnt lgkmcnt(2)\n\t"
      "v_add_f32_e32 v184, v184, v188\n\t"
      "v_add_f32_e32 v185, v185, v189\n\t"
      "v_add_f32_e32 v186, v186, v190\n\t"
      "v_add_f32_e32 v187, v187, v191\n\t"
      "v_add_f32_e32 v216, v216, v224\n\t"
      "v_add_f32_e32 v217, v217, v225\n\t"
      "v_add_f32_e32 v218, v218, v226\n\t"
      "v_add_f32_e32 v219, v219, v227\n\t"
      "s_waitcnt lgkmcnt(0)\n\t"
      "v_add_f32_e32 v192, v192, v196\n\t"
      "v_add_f32_e32 v193, v193, v197\n\t"
      "v_add_f32_e32 v194, v194, v198\n\t"
      "v_add_f32_e32 v195, v195, v199\n\t"
      "v_add_f32_e32 v184, v184, v192\n\t"
      "v_add_f32_e32 v185, v185, v193\n\t"
      "v_add_f32_e32 v186, v186, v194\n\t"
      "v_add_f32_e32 v187, v187, v195\n\t"
      "v_add_f32_e32 v216, v216, v184\n\t"
      "v_add_f32_e32 v217, v217, v185\n\t"
      "v_add_f32_e32 v218, v218, v186\n\t"
      "v_add_f32_e32 v219, v219, v187\n\t"
      "v_mul_f32_e32 v220, v216, v216\n\t"
      "v_mul_f32_e32 v221, v217, v217\n\t"
      "v_mul_f32_e32 v222, v218, v218\n\t"
      "v_mul_f32_e32 v223, v219, v219\n\t"
      "v_fmaak_f32 v220, v235, v220, 0xc0135761\n\t"
      "v_fmaak_f32 v221, v235, v221, 0xc0135761\n\t"
      "v_fmaak_f32 v222, v235, v222, 0xc0135761\n\t"
      "v_fmaak_f32 v223, v235, v223, 0xc0135761\n\t"
      "v_mul_f32_e32 v220, v216, v220\n\t"
      "v_mul_f32_e32 v221, v217, v221\n\t"
      "v_mul_f32_e32 v222, v218, v222\n\t"
      "v_mul_f32_e32 v223, v219, v223\n\t"
      "v_exp_f32_e32 v220, v220\n\t"
      "v_exp_f32_e32 v221, v221\n\t"
      "v_exp_f32_e32 v222, v222\n\t"
      "v_exp_f32_e32 v223, v223\n\t"
      "v_add_f32_e32 v220, 1.0, v220\n\t"
      "v_add_f32_e32 v221, 1.0, v221\n\t"
      "v_add_f32_e32 v222, 1.0, v222\n\t"
      "v_add_f32_e32 v223, 1.0, v223\n\t"
      "v_rcp_f32_e32 v220, v220\n\t"
      "v_rcp_f32_e32 v221, v221\n\t"
      "v_rcp_f32_e32 v222, v222\n\t"
      "v_rcp_f32_e32 v223, v223\n\t"
      "v_mul_f32_e32 v216, v216, v220\n\t"
      "v_mul_f32_e32 v217, v217, v221\n\t"
      "v_mul_f32_e32 v218, v218, v222\n\t"
      "v_mul_f32_e32 v219, v219, v223\n\t"
      "v_cvt_pk_bf16_f32 v220, v216, v217\n\t"
      "v_cvt_pk_bf16_f32 v221, v218, v219\n\t"
      "v_min_u32_e32 v220, -2, v220\n\t"
      "v_min_u32_e32 v221, -2, v221\n\t"
      "s_cmp_eq_u32 s17, 0\n\t"
      "s_cbranch_scc0 HI6_%=\n\t"
      "s_mov_b64 exec, 0xf\n\t"
      "global_store_dwordx2 v247, v[232:233], s[12:13] sc1\n\t"
      "s_mov_b64 exec, -1\n\t"
      "HI6_%=:\n\t"
      "global_store_dwordx2 v245, v[220:221], s[8:9] sc1\n\t"
      "GX_%=:\n\t"
      "AEND_%=:\n\t"
      "s_endpgm\n\t"
      ".p2align 6\n\t"
      "GQ_%=:\n\t"
      "s_waitcnt vmcnt(7)\n\t"
      "v_max3_u32 v234, v128, v129, v130\n\t"
      "v_max_u32_e32 v234, v234, v131\n\t"
      "v_cmp_eq_u32_e32 vcc, -1, v234\n\t"
      "s_cbranch_vccnz FQ0R_%=\n\t"
      "FQ0B_%=:\n\t"
      "v_mfma_f32_16x16x32_bf16 v[184:187], v[0:3], v[128:131], v[236:239]\n\t"
      "v_mfma_f32_16x16x32_bf16 v[188:191], v[4:7], v[128:131], 0\n\t"
      "v_mfma_f32_16x16x32_bf16 v[192:195], v[8:11], v[128:131], 0\n\t"
      "v_mfma_f32_16x16x32_bf16 v[196:199], v[12:15], v[128:131], 0\n\t"
      "global_load_dwordx4 v[128:131], v176, s[54:55] sc1\n\t"
      "s_waitcnt vmcnt(7)\n\t"
      "v_max3_u32 v234, v132, v133, v134\n\t"
      "v_max_u32_e32 v234, v234, v135\n\t"
      "v_cmp_eq_u32_e32 vcc, -1, v234\n\t"
      "s_cbranch_vccnz FQ1R_%=\n\t"
      "FQ1B_%=:\n\t"
      "v_mfma_f32_16x16x32_bf16 v[184:187], v[16:19], v[132:135], v[184:187]\n\t"
      "v_mfma_f32_16x16x32_bf16 v[188:191], v[20:23], v[132:135], v[188:191]\n\t"
      "v_mfma_f32_16x16x32_bf16 v[192:195], v[24:27], v[132:135], v[192:195]\n\t"
      "v_mfma_f32_16x16x32_bf16 v[196:199], v[28:31], v[132:135], v[196:199]\n\t"
      "global_load_dwordx4 v[132:135], v177, s[54:55] sc1\n\t"
      "s_waitcnt vmcnt(7)\n\t"
      "v_max3_u32 v234, v136, v137, v138\n\t"
      "v_max_u32_e32 v234, v234, v139\n\t"
      "v_cmp_eq_u32_e32 vcc, -1, v234\n\t"
      "s_cbranch_vccnz FQ2R_%=\n\t"
      "FQ2B_%=:\n\t"
      "v_mfma_f32_16x16x32_bf16 v[184:187], v[32:35], v[136:139], v[184:187]\n\t"
      "v_mfma_f32_16x16x32_bf16 v[188:191], v[36:39], v[136:139], v[188:191]\n\t"
      "v_mfma_f32_16x16x32_bf16 v[192:195], v[40:43], v[136:139], v[192:195]\n\t"
      "v_mfma_f32_16x16x32_bf16 v[196:199], v[44:47], v[136:139], v[196:199]\n\t"
      "global_load_dwordx4 v[136:139], v178, s[54:55] sc1\n\t"
      "s_waitcnt vmcnt(7)\n\t"
      "v_max3_u32 v234, v140, v141, v142\n\t"
      "v_max_u32_e32 v234, v234, v143\n\t"
      "v_cmp_eq_u32_e32 vcc, -1, v234\n\t"
      "s_cbranch_vccnz FQ3R_%=\n\t"
      "FQ3B_%=:\n\t"
      "v_mfma_f32_16x16x32_bf16 v[184:187], v[48:51], v[140:143], v[184:187]\n\t"
      "v_mfma_f32_16x16x32_bf16 v[188:191], v[52:55], v[140:143], v[188:191]\n\t"
      "v_mfma_f32_16x16x32_bf16 v[192:195], v[56:59], v[140:143], v[192:195]\n\t"
      "v_mfma_f32_16x16x32_bf16 v[196:199], v[60:63], v[140:143], v[196:199]\n\t"
      "global_load_dwordx4 v[140:143], v179, s[54:55] sc1\n\t"
      "s_waitcnt vmcnt(7)\n\t"
      "v_max3_u32 v234, v144, v145, v146\n\t"
      "v_max_u32_e32 v234, v234, v147\n\t"
      "v_cmp_eq_u32_e32 vcc, -1, v234\n\t"
      "s_cbranch_vccnz FQ4R_%=\n\t"
      "FQ4B_%=:\n\t"
      "v_mfma_f32_16x16x32_bf16 v[184:187], v[64:67], v[144:147], v[184:187]\n\t"
      "v_mfma_f32_16x16x32_bf16 v[188:191], v[68:71], v[144:147], v[188:191]\n\t"
      "v_mfma_f32_16x16x32_bf16 v[192:195], v[72:75], v[144:147], v[192:195]\n\t"
      "v_mfma_f32_16x16x32_bf16 v[196:199], v[76:79], v[144:147], v[196:199]\n\t"
      "global_load_dwordx4 v[144:147], v180, s[54:55] sc1\n\t"
      "s_waitcnt vmcnt(7)\n\t"
      "v_max3_u32 v234, v148, v149, v150\n\t"
      "v_max_u32_e32 v234, v234, v151\n\t"
      "v_cmp_eq_u32_e32 vcc, -1, v234\n\t"
      "s_cbranch_vccnz FQ5R_%=\n\t"
      "FQ5B_%=:\n\t"
      "v_mfma_f32_16x16x32_bf16 v[184:187], v[80:83], v[148:151], v[184:187]\n\t"
      "v_mfma_f32_16x16x32_bf16 v[188:191], v[84:87], v[148:151], v[188:191]\n\t"
      "v_mfma_f32_16x16x32_bf16 v[192:195], v[88:91], v[148:151], v[192:195]\n\t"
      "v_mfma_f32_16x16x32_bf16 v[196:199], v[92:95], v[148:151], v[196:199]\n\t"
      "global_load_dwordx4 v[148:151], v181, s[54:55] sc1\n\t"
      "s_waitcnt vmcnt(7)\n\t"
      "v_max3_u32 v234, v152, v153, v154\n\t"
      "v_max_u32_e32 v234, v234, v155\n\t"
      "v_cmp_eq_u32_e32 vcc, -1, v234\n\t"
      "s_cbranch_vccnz FQ6R_%=\n\t"
      "FQ6B_%=:\n\t"
      "v_mfma_f32_16x16x32_bf16 v[184:187], v[96:99], v[152:155], v[184:187]\n\t"
      "v_mfma_f32_16x16x32_bf16 v[188:191], v[100:103], v[152:155], v[188:191]\n\t"
      "v_mfma_f32_16x16x32_bf16 v[192:195], v[104:107], v[152:155], v[192:195]\n\t"
      "v_mfma_f32_16x16x32_bf16 v[196:199], v[108:111], v[152:155], v[196:199]\n\t"
      "global_load_dwordx4 v[152:155], v182, s[54:55] sc1\n\t"
      "s_waitcnt vmcnt(7)\n\t"
      "v_max3_u32 v234, v156, v157, v158\n\t"
      "v_max_u32_e32 v234, v234, v159\n\t"
      "v_cmp_eq_u32_e32 vcc, -1, v234\n\t"
      "s_cbranch_vccnz FQ7R_%=\n\t"
      "FQ7B_%=:\n\t"
      "v_mfma_f32_16x16x32_bf16 v[184:187], v[112:115], v[156:159], v[184:187]\n\t"
      "v_mfma_f32_16x16x32_bf16 v[188:191], v[116:119], v[156:159], v[188:191]\n\t"
      "v_mfma_f32_16x16x32_bf16 v[192:195], v[120:123], v[156:159], v[192:195]\n\t"
      "v_mfma_f32_16x16x32_bf16 v[196:199], v[124:127], v[156:159], v[196:199]\n\t"
      "global_load_dwordx4 v[156:159], v183, s[54:55] sc1\n\t"
      "s_nop 3\n\t"
      "ds_write_b128 v243, v[184:187]\n\t"
      "ds_write_b128 v164, v[188:191]\n\t"
      "ds_write_b128 v165, v[192:195]\n\t"
      "ds_write_b128 v166, v[196:199]\n\t"
      "s_waitcnt vmcnt(7)\n\t"
      "v_mfma_f32_16x16x32_bf16 v[200:203], v[0:3], v[128:131], v[236:239]\n\t"
      "v_mfma_f32_16x16x32_bf16 v[204:207], v[4:7], v[128:131], 0\n\t"
      "v_mfma_f32_16x16x32_bf16 v[208:211], v[8:11], v[128:131], 0\n\t"
      "v_mfma_f32_16x16x32_bf16 v[212:215], v[12:15], v[128:131], 0\n\t"
      "global_load_dwordx4 v[128:131], v176, s[56:57] sc1\n\t"
      "s_waitcnt vmcnt(7)\n\t"
      "v_mfma_f32_16x16x32_bf16 v[200:203], v[16:19], v[132:135], v[200:203]\n\t"
      "v_mfma_f32_16x16x32_bf16 v[204:207], v[20:23], v[132:135], v[204:207]\n\t"
      "v_mfma_f32_16x16x32_bf16 v[208:211], v[24:27], v[132:135], v[208:211]\n\t"
      "v_mfma_f32_16x16x32_bf16 v[212:215], v[28:31], v[132:135], v[212:215]\n\t"
      "global_load_dwordx4 v[132:135], v177, s[56:57] sc1\n\t"
      "s_waitcnt vmcnt(7)\n\t"
      "v_mfma_f32_16x16x32_bf16 v[200:203], v[32:35], v[136:139], v[200:203]\n\t"
      "v_mfma_f32_16x16x32_bf16 v[204:207], v[36:39], v[136:139], v[204:207]\n\t"
      "v_mfma_f32_16x16x32_bf16 v[208:211], v[40:43], v[136:139], v[208:211]\n\t"
      "v_mfma_f32_16x16x32_bf16 v[212:215], v[44:47], v[136:139], v[212:215]\n\t"
      "global_load_dwordx4 v[136:139], v178, s[56:57] sc1\n\t"
      "s_waitcnt vmcnt(7)\n\t"
      "v_mfma_f32_16x16x32_bf16 v[200:203], v[48:51], v[140:143], v[200:203]\n\t"
      "v_mfma_f32_16x16x32_bf16 v[204:207], v[52:55], v[140:143], v[204:207]\n\t"
      "v_mfma_f32_16x16x32_bf16 v[208:211], v[56:59], v[140:143], v[208:211]\n\t"
      "v_mfma_f32_16x16x32_bf16 v[212:215], v[60:63], v[140:143], v[212:215]\n\t"
      "global_load_dwordx4 v[140:143], v179, s[56:57] sc1\n\t"
      "s_waitcnt vmcnt(7)\n\t"
      "v_mfma_f32_16x16x32_bf16 v[200:203], v[64:67], v[144:147], v[200:203]\n\t"
      "v_mfma_f32_16x16x32_bf16 v[204:207], v[68:71], v[144:147], v[204:207]\n\t"
      "v_mfma_f32_16x16x32_bf16 v[208:211], v[72:75], v[144:147], v[208:211]\n\t"
      "v_mfma_f32_16x16x32_bf16 v[212:215], v[76:79], v[144:147], v[212:215]\n\t"
      "global_load_dwordx4 v[144:147], v180, s[56:57] sc1\n\t"
      "s_waitcnt vmcnt(7)\n\t"
      "v_mfma_f32_16x16x32_bf16 v[200:203], v[80:83], v[148:151], v[200:203]\n\t"
      "v_mfma_f32_16x16x32_bf16 v[204:207], v[84:87], v[148:151], v[204:207]\n\t"
      "s_waitcnt lgkmcnt(0)\n\t"
      "s_barrier\n\t"
      "s_setprio 2\n\t"
      "v_mfma_f32_16x16x32_bf16 v[208:211], v[88:91], v[148:151], v[208:211]\n\t"
      "v_mfma_f32_16x16x32_bf16 v[212:215], v[92:95], v[148:151], v[212:215]\n\t"
      "global_load_dwordx4 v[148:151], v181, s[56:57] sc1\n\t"
      "s_waitcnt vmcnt(7)\n\t"
      "v_mfma_f32_16x16x32_bf16 v[200:203], v[96:99], v[152:155], v[200:203]\n\t"
      "v_mfma_f32_16x16x32_bf16 v[204:207], v[100:103], v[152:155], v[204:207]\n\t"
      "v_mfma_f32_16x16x32_bf16 v[208:211], v[104:107], v[152:155], v[208:211]\n\t"
      "v_mfma_f32_16x16x32_bf16 v[212:215], v[108:111], v[152:155], v[212:215]\n\t"
      "global_load_dwordx4 v[152:155], v182, s[56:57] sc1\n\t"
      "s_waitcnt vmcnt(7)\n\t"
      "v_mfma_f32_16x16x32_bf16 v[200:203], v[112:115], v[156:159], v[200:203]\n\t"
      "v_mfma_f32_16x16x32_bf16 v[204:207], v[116:119], v[156:159], v[204:207]\n\t"
      "v_mfma_f32_16x16x32_bf16 v[208:211], v[120:123], v[156:159], v[208:211]\n\t"
      "v_mfma_f32_16x16x32_bf16 v[212:215], v[124:127], v[156:159], v[212:215]\n\t"
      "global_load_dwordx4 v[156:159], v183, s[56:57] sc1\n\t"
      "s_setprio 0\n\t"
      "s_nop 2\n\t"
      "v_cmp_u_f32_e32 vcc, v200, v200\n\t"
      "s_cbranch_vccnz NQ1R_%=\n\t"
      "NQ1B_%=:\n\t"
      "ds_write_b128 v243, v[200:203] offset:32768\n\t"
      "ds_write_b128 v164, v[204:207] offset:32768\n\t"
      "ds_write_b128 v165, v[208:211] offset:32768\n\t"
      "ds_write_b128 v166, v[212:215] offset:32768\n\t"
      "s_waitcnt vmcnt(7)\n\t"
      "v_mfma_f32_16x16x32_bf16 v[184:187], v[0:3], v[128:131], v[236:239]\n\t"
      "v_mfma_f32_16x16x32_bf16 v[188:191], v[4:7], v[128:131], 0\n\t"
      "v_mfma_f32_16x16x32_bf16 v[192:195], v[8:11], v[128:131], 0\n\t"
      "v_mfma_f32_16x16x32_bf16 v[196:199], v[12:15], v[128:131], 0\n\t"
      "global_load_dwordx4 v[128:131], v176, s[58:59] sc1\n\t"
      "s_waitcnt vmcnt(7)\n\t"
      "v_mfma_f32_16x16x32_bf16 v[184:187], v[16:19], v[132:135], v[184:187]\n\t"
      "v_mfma_f32_16x16x32_bf16 v[188:191], v[20:23], v[132:135], v[188:191]\n\t"
      "v_mfma_f32_16x16x32_bf16 v[192:195], v[24:27], v[132:135], v[192:195]\n\t"
      "v_mfma_f32_16x16x32_bf16 v[196:199], v[28:31], v[132:135], v[196:199]\n\t"
      "global_load_dwordx4 v[132:135], v177, s[58:59] sc1\n\t"
      "s_waitcnt vmcnt(7)\n\t"
      "v_mfma_f32_16x16x32_bf16 v[184:187], v[32:35], v[136:139], v[184:187]\n\t"
      "v_mfma_f32_16x16x32_bf16 v[188:191], v[36:39], v[136:139], v[188:191]\n\t"
      "v_mfma_f32_16x16x32_bf16 v[192:195], v[40:43], v[136:139], v[192:195]\n\t"
      "v_mfma_f32_16x16x32_bf16 v[196:199], v[44:47], v[136:139], v[196:199]\n\t"
      "global_load_dwordx4 v[136:139], v178, s[58:59] sc1\n\t"
      "s_waitcnt vmcnt(7)\n\t"
      "v_mfma_f32_16x16x32_bf16 v[184:187], v[48:51], v[140:143], v[184:187]\n\t"
      "v_mfma_f32_16x16x32_bf16 v[188:191], v[52:55], v[140:143], v[188:191]\n\t"
      "v_mfma_f32_16x16x32_bf16 v[192:195], v[56:59], v[140:143], v[192:195]\n\t"
      "v_mfma_f32_16x16x32_bf16 v[196:199], v[60:63], v[140:143], v[196:199]\n\t"
      "global_load_dwordx4 v[140:143], v179, s[58:59] sc1\n\t"
      "s_waitcnt vmcnt(7)\n\t"
      "v_mfma_f32_16x16x32_bf16 v[184:187], v[64:67], v[144:147], v[184:187]\n\t"
      "v_mfma_f32_16x16x32_bf16 v[188:191], v[68:71], v[144:147], v[188:191]\n\t"
      "v_mfma_f32_16x16x32_bf16 v[192:195], v[72:75], v[144:147], v[192:195]\n\t"
      "v_mfma_f32_16x16x32_bf16 v[196:199], v[76:79], v[144:147], v[196:199]\n\t"
      "global_load_dwordx4 v[144:147], v180, s[58:59] sc1\n\t"
      "s_waitcnt vmcnt(7)\n\t"
      "v_mfma_f32_16x16x32_bf16 v[184:187], v[80:83], v[148:151], v[184:187]\n\t"
      "v_mfma_f32_16x16x32_bf16 v[188:191], v[84:87], v[148:151], v[188:191]\n\t"
      "s_waitcnt lgkmcnt(0)\n\t"
      "s_barrier\n\t"
      "s_setprio 2\n\t"
      "v_mfma_f32_16x16x32_bf16 v[192:195], v[88:91], v[148:151], v[192:195]\n\t"
      "v_mfma_f32_16x16x32_bf16 v[196:199], v[92:95], v[148:151], v[196:199]\n\t"
      "global_load_dwordx4 v[148:151], v181, s[58:59] sc1\n\t"
      "s_waitcnt vmcnt(7)\n\t"
      "v_mfma_f32_16x16x32_bf16 v[184:187], v[96:99], v[152:155], v[184:187]\n\t"
      "v_mfma_f32_16x16x32_bf16 v[188:191], v[100:103], v[152:155], v[188:191]\n\t"
      "v_mfma_f32_16x16x32_bf16 v[192:195], v[104:107], v[152:155], v[192:195]\n\t"
      "v_mfma_f32_16x16x32_bf16 v[196:199], v[108:111], v[152:155], v[196:199]\n\t"
      "global_load_dwordx4 v[152:155], v182, s[58:59] sc1\n\t"
      "s_waitcnt vmcnt(7)\n\t"
      "v_mfma_f32_16x16x32_bf16 v[184:187], v[112:115], v[156:159], v[184:187]\n\t"
      "v_mfma_f32_16x16x32_bf16 v[188:191], v[116:119], v[156:159], v[188:191]\n\t"
      "v_mfma_f32_16x16x32_bf16 v[192:195], v[120:123], v[156:159], v[192:195]\n\t"
      "v_mfma_f32_16x16x32_bf16 v[196:199], v[124:127], v[156:159], v[196:199]\n\t"
      "global_load_dwordx4 v[156:159], v183, s[58:59] sc1\n\t"
      "s_setprio 0\n\t"
      "s_nop 2\n\t"
      "v_cmp_u_f32_e32 vcc, v184, v184\n\t"
      "s_cbranch_vccnz NQ2R_%=\n\t"
      "NQ2B_%=:\n\t"
      "ds_write_b128 v243, v[184:187]\n\t"
      "ds_write_b128 v164, v[188:191]\n\t"
      "ds_write_b128 v165, v[192:195]\n\t"
      "ds_write_b128 v166, v[196:199]\n\t"
      "s_waitcnt vmcnt(7)\n\t"
      "v_mfma_f32_16x16x32_bf16 v[200:203], v[0:3], v[128:131], v[236:239]\n\t"
      "v_mfma_f32_16x16x32_bf16 v[204:207], v[4:7], v[128:131], 0\n\t"
      "v_mfma_f32_16x16x32_bf16 v[208:211], v[8:11], v[128:131], 0\n\t"
      "v_mfma_f32_16x16x32_bf16 v[212:215], v[12:15], v[128:131], 0\n\t"
      "global_load_dwordx4 v[128:131], v176, s[60:61] sc1\n\t"
      "s_waitcnt vmcnt(7)\n\t"
      "v_mfma_f32_16x16x32_bf16 v[200:203], v[16:19], v[132:135], v[200:203]\n\t"
      "v_mfma_f32_16x16x32_bf16 v[204:207], v[20:23], v[132:135], v[204:207]\n\t"
      "v_mfma_f32_16x16x32_bf16 v[208:211], v[24:27], v[132:135], v[208:211]\n\t"
      "v_mfma_f32_16x16x32_bf16 v[212:215], v[28:31], v[132:135], v[212:215]\n\t"
      "global_load_dwordx4 v[132:135], v177, s[60:61] sc1\n\t"
      "s_waitcnt vmcnt(7)\n\t"
      "v_mfma_f32_16x16x32_bf16 v[200:203], v[32:35], v[136:139], v[200:203]\n\t"
      "v_mfma_f32_16x16x32_bf16 v[204:207], v[36:39], v[136:139], v[204:207]\n\t"
      "v_mfma_f32_16x16x32_bf16 v[208:211], v[40:43], v[136:139], v[208:211]\n\t"
      "v_mfma_f32_16x16x32_bf16 v[212:215], v[44:47], v[136:139], v[212:215]\n\t"
      "global_load_dwordx4 v[136:139], v178, s[60:61] sc1\n\t"
      "s_waitcnt vmcnt(7)\n\t"
      "v_mfma_f32_16x16x32_bf16 v[200:203], v[48:51], v[140:143], v[200:203]\n\t"
      "v_mfma_f32_16x16x32_bf16 v[204:207], v[52:55], v[140:143], v[204:207]\n\t"
      "v_mfma_f32_16x16x32_bf16 v[208:211], v[56:59], v[140:143], v[208:211]\n\t"
      "v_mfma_f32_16x16x32_bf16 v[212:215], v[60:63], v[140:143], v[212:215]\n\t"
      "global_load_dwordx4 v[140:143], v179, s[60:61] sc1\n\t"
      "s_waitcnt vmcnt(7)\n\t"
      "v_mfma_f32_16x16x32_bf16 v[200:203], v[64:67], v[144:147], v[200:203]\n\t"
      "v_mfma_f32_16x16x32_bf16 v[204:207], v[68:71], v[144:147], v[204:207]\n\t"
      "v_mfma_f32_16x16x32_bf16 v[208:211], v[72:75], v[144:147], v[208:211]\n\t"
      "v_mfma_f32_16x16x32_bf16 v[212:215], v[76:79], v[144:147], v[212:215]\n\t"
      "global_load_dwordx4 v[144:147], v180, s[60:61] sc1\n\t"
      "s_waitcnt vmcnt(7)\n\t"
      "v_mfma_f32_16x16x32_bf16 v[200:203], v[80:83], v[148:151], v[200:203]\n\t"
      "v_mfma_f32_16x16x32_bf16 v[204:207], v[84:87], v[148:151], v[204:207]\n\t"
      "s_waitcnt lgkmcnt(0)\n\t"
      "s_barrier\n\t"
      "s_setprio 2\n\t"
      "v_mfma_f32_16x16x32_bf16 v[208:211], v[88:91], v[148:151], v[208:211]\n\t"
      "v_mfma_f32_16x16x32_bf16 v[212:215], v[92:95], v[148:151], v[212:215]\n\t"
      "global_load_dwordx4 v[148:151], v181, s[60:61] sc1\n\t"
      "s_waitcnt vmcnt(7)\n\t"
      "v_mfma_f32_16x16x32_bf16 v[200:203], v[96:99], v[152:155], v[200:203]\n\t"
      "v_mfma_f32_16x16x32_bf16 v[204:207], v[100:103], v[152:155], v[204:207]\n\t"
      "v_mfma_f32_16x16x32_bf16 v[208:211], v[104:107], v[152:155], v[208:211]\n\t"
      "v_mfma_f32_16x16x32_bf16 v[212:215], v[108:111], v[152:155], v[212:215]\n\t"
      "global_load_dwordx4 v[152:155], v182, s[60:61] sc1\n\t"
      "s_waitcnt vmcnt(7)\n\t"
      "v_mfma_f32_16x16x32_bf16 v[200:203], v[112:115], v[156:159], v[200:203]\n\t"
      "v_mfma_f32_16x16x32_bf16 v[204:207], v[116:119], v[156:159], v[204:207]\n\t"
      "v_mfma_f32_16x16x32_bf16 v[208:211], v[120:123], v[156:159], v[208:211]\n\t"
      "v_mfma_f32_16x16x32_bf16 v[212:215], v[124:127], v[156:159], v[212:215]\n\t"
      "global_load_dwordx4 v[156:159], v183, s[60:61] sc1\n\t"
      "s_setprio 0\n\t"
      "s_nop 2\n\t"
      "v_cmp_u_f32_e32 vcc, v200, v200\n\t"
      "s_cbranch_vccnz NQ3R_%=\n\t"
      "NQ3B_%=:\n\t"
      "ds_write_b128 v243, v[200:203] offset:32768\n\t"
      "ds_write_b128 v164, v[204:207] offset:32768\n\t"
      "ds_write_b128 v165, v[208:211] offset:32768\n\t"
      "ds_write_b128 v166, v[212:215] offset:32768\n\t"
      "s_waitcnt vmcnt(7)\n\t"
      "v_mfma_f32_16x16x32_bf16 v[184:187], v[0:3], v[128:131], v[236:239]\n\t"
      "v_mfma_f32_16x16x32_bf16 v[188:191], v[4:7], v[128:131], 0\n\t"
      "v_mfma_f32_16x16x32_bf16 v[192:195], v[8:11], v[128:131], 0\n\t"
      "v_mfma_f32_16x16x32_bf16 v[196:199], v[12:15], v[128:131], 0\n\t"
      "global_load_dwordx4 v[128:131], v176, s[62:63] sc1\n\t"
      "s_waitcnt vmcnt(7)\n\t"
      "v_mfma_f32_16x16x32_bf16 v[184:187], v[16:19], v[132:135], v[184:187]\n\t"
      "v_mfma_f32_16x16x32_bf16 v[188:191], v[20:23], v[132:135], v[188:191]\n\t"
      "v_mfma_f32_16x16x32_bf16 v[192:195], v[24:27], v[132:135], v[192:195]\n\t"
      "v_mfma_f32_16x16x32_bf16 v[196:199], v[28:31], v[132:135], v[196:199]\n\t"
      "global_load_dwordx4 v[132:135], v177, s[62:63] sc1\n\t"
      "s_waitcnt vmcnt(7)\n\t"
      "v_mfma_f32_16x16x32_bf16 v[184:187], v[32:35], v[136:139], v[184:187]\n\t"
      "v_mfma_f32_16x16x32_bf16 v[188:191], v[36:39], v[136:139], v[188:191]\n\t"
      "v_mfma_f32_16x16x32_bf16 v[192:195], v[40:43], v[136:139], v[192:195]\n\t"
      "v_mfma_f32_16x16x32_bf16 v[196:199], v[44:47], v[136:139], v[196:199]\n\t"
      "global_load_dwordx4 v[136:139], v178, s[62:63] sc1\n\t"
      "s_waitcnt vmcnt(7)\n\t"
      "v_mfma_f32_16x16x32_bf16 v[184:187], v[48:51], v[140:143], v[184:187]\n\t"
      "v_mfma_f32_16x16x32_bf16 v[188:191], v[52:55], v[140:143], v[188:191]\n\t"
      "v_mfma_f32_16x16x32_bf16 v[192:195], v[56:59], v[140:143], v[192:195]\n\t"
      "v_mfma_f32_16x16x32_bf16 v[196:199], v[60:63], v[140:143], v[196:199]\n\t"
      "global_load_dwordx4 v[140:143], v179, s[62:63] sc1\n\t"
      "s_waitcnt vmcnt(7)\n\t"
      "v_mfma_f32_16x16x32_bf16 v[184:187], v[64:67], v[144:147], v[184:187]\n\t"
      "v_mfma_f32_16x16x32_bf16 v[188:191], v[68:71], v[144:147], v[188:191]\n\t"
      "v_mfma_f32_16x16x32_bf16 v[192:195], v[72:75], v[144:147], v[192:195]\n\t"
      "v_mfma_f32_16x16x32_bf16 v[196:199], v[76:79], v[144:147], v[196:199]\n\t"
      "global_load_dwordx4 v[144:147], v180, s[62:63] sc1\n\t"
      "s_waitcnt vmcnt(7)\n\t"
      "v_mfma_f32_16x16x32_bf16 v[184:187], v[80:83], v[148:151], v[184:187]\n\t"
      "v_mfma_f32_16x16x32_bf16 v[188:191], v[84:87], v[148:151], v[188:191]\n\t"
      "s_waitcnt lgkmcnt(0)\n\t"
      "s_barrier\n\t"
      "s_setprio 2\n\t"
      "v_mfma_f32_16x16x32_bf16 v[192:195], v[88:91], v[148:151], v[192:195]\n\t"
      "v_mfma_f32_16x16x32_bf16 v[196:199], v[92:95], v[148:151], v[196:199]\n\t"
      "global_load_dwordx4 v[148:151], v181, s[62:63] sc1\n\t"
      "s_waitcnt vmcnt(7)\n\t"
      "v_mfma_f32_16x16x32_bf16 v[184:187], v[96:99], v[152:155], v[184:187]\n\t"
      "v_mfma_f32_16x16x32_bf16 v[188:191], v[100:103], v[152:155], v[188:191]\n\t"
      "v_mfma_f32_16x16x32_bf16 v[192:195], v[104:107], v[152:155], v[192:195]\n\t"
      "v_mfma_f32_16x16x32_bf16 v[196:199], v[108:111], v[152:155], v[196:199]\n\t"
      "global_load_dwordx4 v[152:155], v182, s[62:63] sc1\n\t"
      "s_waitcnt vmcnt(7)\n\t"
      "v_mfma_f32_16x16x32_bf16 v[184:187], v[112:115], v[156:159], v[184:187]\n\t"
      "v_mfma_f32_16x16x32_bf16 v[188:191], v[116:119], v[156:159], v[188:191]\n\t"
      "v_mfma_f32_16x16x32_bf16 v[192:195], v[120:123], v[156:159], v[192:195]\n\t"
      "v_mfma_f32_16x16x32_bf16 v[196:199], v[124:127], v[156:159], v[196:199]\n\t"
      "global_load_dwordx4 v[156:159], v183, s[62:63] sc1\n\t"
      "s_setprio 0\n\t"
      "s_nop 2\n\t"
      "v_cmp_u_f32_e32 vcc, v184, v184\n\t"
      "s_cbranch_vccnz NQ4R_%=\n\t"
      "NQ4B_%=:\n\t"
      "ds_write_b128 v243, v[184:187]\n\t"
      "ds_write_b128 v164, v[188:191]\n\t"
      "ds_write_b128 v165, v[192:195]\n\t"
      "ds_write_b128 v166, v[196:199]\n\t"
      "s_waitcnt vmcnt(7)\n\t"
      "v_mfma_f32_16x16x32_bf16 v[200:203], v[0:3], v[128:131], v[236:239]\n\t"
      "v_mfma_f32_16x16x32_bf16 v[204:207], v[4:7], v[128:131], 0\n\t"
      "v_mfma_f32_16x16x32_bf16 v[208:211], v[8:11], v[128:131], 0\n\t"
      "v_mfma_f32_16x16x32_bf16 v[212:215], v[12:15], v[128:131], 0\n\t"
      "global_load_dwordx4 v[128:131], v176, s[64:65] sc1\n\t"
      "s_waitcnt vmcnt(7)\n\t"
      "v_mfma_f32_16x16x32_bf16 v[200:203], v[16:19], v[132:135], v[200:203]\n\t"
      "v_mfma_f32_16x16x32_bf16 v[204:207], v[20:23], v[132:135], v[204:207]\n\t"
      "v_mfma_f32_16x16x32_bf16 v[208:211], v[24:27], v[132:135], v[208:211]\n\t"
      "v_mfma_f32_16x16x32_bf16 v[212:215], v[28:31], v[132:135], v[212:215]\n\t"
      "global_load_dwordx4 v[132:135], v177, s[64:65] sc1\n\t"
      "s_waitcnt vmcnt(7)\n\t"
      "v_mfma_f32_16x16x32_bf16 v[200:203], v[32:35], v[136:139], v[200:203]\n\t"
      "v_mfma_f32_16x16x32_bf16 v[204:207], v[36:39], v[136:139], v[204:207]\n\t"
      "v_mfma_f32_16x16x32_bf16 v[208:211], v[40:43], v[136:139], v[208:211]\n\t"
      "v_mfma_f32_16x16x32_bf16 v[212:215], v[44:47], v[136:139], v[212:215]\n\t"
      "global_load_dwordx4 v[136:139], v178, s[64:65] sc1\n\t"
      "s_waitcnt vmcnt(7)\n\t"
      "v_mfma_f32_16x16x32_bf16 v[200:203], v[48:51], v[140:143], v[200:203]\n\t"
      "v_mfma_f32_16x16x32_bf16 v[204:207], v[52:55], v[140:143], v[204:207]\n\t"
      "v_mfma_f32_16x16x32_bf16 v[208:211], v[56:59], v[140:143], v[208:211]\n\t"
      "v_mfma_f32_16x16x32_bf16 v[212:215], v[60:63], v[140:143], v[212:215]\n\t"
      "global_load_dwordx4 v[140:143], v179, s[64:65] sc1\n\t"
      "s_waitcnt vmcnt(7)\n\t"
      "v_mfma_f32_16x16x32_bf16 v[200:203], v[64:67], v[144:147], v[200:203]\n\t"
      "v_mfma_f32_16x16x32_bf16 v[204:207], v[68:71], v[144:147], v[204:207]\n\t"
      "v_mfma_f32_16x16x32_bf16 v[208:211], v[72:75], v[144:147], v[208:211]\n\t"
      "v_mfma_f32_16x16x32_bf16 v[212:215], v[76:79], v[144:147], v[212:215]\n\t"
      "global_load_dwordx4 v[144:147], v180, s[64:65] sc1\n\t"
      "s_waitcnt vmcnt(7)\n\t"
      "v_mfma_f32_16x16x32_bf16 v[200:203], v[80:83], v[148:151], v[200:203]\n\t"
      "v_mfma_f32_16x16x32_bf16 v[204:207], v[84:87], v[148:151], v[204:207]\n\t"
      "s_waitcnt lgkmcnt(0)\n\t"
      "s_barrier\n\t"
      "s_setprio 2\n\t"
      "v_mfma_f32_16x16x32_bf16 v[208:211], v[88:91], v[148:151], v[208:211]\n\t"
      "v_mfma_f32_16x16x32_bf16 v[212:215], v[92:95], v[148:151], v[212:215]\n\t"
      "global_load_dwordx4 v[148:151], v181, s[64:65] sc1\n\t"
      "s_waitcnt vmcnt(7)\n\t"
      "v_mfma_f32_16x16x32_bf16 v[200:203], v[96:99], v[152:155], v[200:203]\n\t"
      "v_mfma_f32_16x16x32_bf16 v[204:207], v[100:103], v[152:155], v[204:207]\n\t"
      "v_mfma_f32_16x16x32_bf16 v[208:211], v[104:107], v[152:155], v[208:211]\n\t"
      "v_mfma_f32_16x16x32_bf16 v[212:215], v[108:111], v[152:155], v[212:215]\n\t"
      "global_load_dwordx4 v[152:155], v182, s[64:65] sc1\n\t"
      "s_waitcnt vmcnt(7)\n\t"
      "v_mfma_f32_16x16x32_bf16 v[200:203], v[112:115], v[156:159], v[200:203]\n\t"
      "v_mfma_f32_16x16x32_bf16 v[204:207], v[116:119], v[156:159], v[204:207]\n\t"
      "v_mfma_f32_16x16x32_bf16 v[208:211], v[120:123], v[156:159], v[208:211]\n\t"
      "v_mfma_f32_16x16x32_bf16 v[212:215], v[124:127], v[156:159], v[212:215]\n\t"
      "global_load_dwordx4 v[156:159], v183, s[64:65] sc1\n\t"
      "s_setprio 0\n\t"
      "s_nop 2\n\t"
      "v_cmp_u_f32_e32 vcc, v200, v200\n\t"
      "s_cbranch_vccnz NQ5R_%=\n\t"
      "NQ5B_%=:\n\t"
      "ds_write_b128 v243, v[200:203] offset:32768\n\t"
      "ds_write_b128 v164, v[204:207] offset:32768\n\t"
      "ds_write_b128 v165, v[208:211] offset:32768\n\t"
      "ds_write_b128 v166, v[212:215] offset:32768\n\t"
      "s_waitcnt vmcnt(7)\n\t"
      "v_mfma_f32_16x16x32_bf16 v[184:187], v[0:3], v[128:131], v[236:239]\n\t"
      "v_mfma_f32_16x16x32_bf16 v[188:191], v[4:7], v[128:131], 0\n\t"
      "v_mfma_f32_16x16x32_bf16 v[192:195], v[8:11], v[128:131], 0\n\t"
      "v_mfma_f32_16x16x32_bf16 v[196:199], v[12:15], v[128:131], 0\n\t"
      "s_waitcnt vmcnt(6)\n\t"
      "v_mfma_f32_16x16x32_bf16 v[184:187], v[16:19], v[132:135], v[184:187]\n\t"
      "v_mfma_f32_16x16x32_bf16 v[188:191], v[20:23], v[132:135], v[188:191]\n\t"
      "v_mfma_f32_16x16x32_bf16 v[192:195], v[24:27], v[132:135], v[192:195]\n\t"
      "v_mfma_f32_16x16x32_bf16 v[196:199], v[28:31], v[132:135], v[196:199]\n\t"
      "s_waitcnt vmcnt(5)\n\t"
      "v_mfma_f32_16x16x32_bf16 v[184:187], v[32:35], v[136:139], v[184:187]\n\t"
      "v_mfma_f32_16x16x32_bf16 v[188:191], v[36:39], v[136:139], v[188:191]\n\t"
      "v_mfma_f32_16x16x32_bf16 v[192:195], v[40:43], v[136:139], v[192:195]\n\t"
      "v_mfma_f32_16x16x32_bf16 v[196:199], v[44:47], v[136:139], v[196:199]\n\t"
      "s_waitcnt vmcnt(4)\n\t"
      "v_mfma_f32_16x16x32_bf16 v[184:187], v[48:51], v[140:143], v[184:187]\n\t"
      "v_mfma_f32_16x16x32_bf16 v[188:191], v[52:55], v[140:143], v[188:191]\n\t"
      "v_mfma_f32_16x16x32_bf16 v[192:195], v[56:59], v[140:143], v[192:195]\n\t"
      "v_mfma_f32_16x16x32_bf16 v[196:199], v[60:63], v[140:143], v[196:199]\n\t"
      "s_waitcnt vmcnt(3)\n\t"
      "v_mfma_f32_16x16x32_bf16 v[184:187], v[64:67], v[144:147], v[184:187]\n\t"
      "v_mfma_f32_16x16x32_bf16 v[188:191], v[68:71], v[144:147], v[188:191]\n\t"
      "v_mfma_f32_16x16x32_bf16 v[192:195], v[72:75], v[144:147], v[192:195]\n\t"
      "v_mfma_f32_16x16x32_bf16 v[196:199], v[76:79], v[144:147], v[196:199]\n\t"
      "s_waitcnt vmcnt(2)\n\t"
      "v_mfma_f32_16x16x32_bf16 v[184:187], v[80:83], v[148:151], v[184:187]\n\t"
      "v_mfma_f32_16x16x32_bf16 v[188:191], v[84:87], v[148:151], v[188:191]\n\t"
      "s_waitcnt lgkmcnt(0)\n\t"
      "s_barrier\n\t"
      "s_setprio 2\n\t"
      "v_mfma_f32_16x16x32_bf16 v[192:195], v[88:91], v[148:151], v[192:195]\n\t"
      "v_mfma_f32_16x16x32_bf16 v[196:199], v[92:95], v[148:151], v[196:199]\n\t"
      "s_waitcnt vmcnt(1)\n\t"
      "v_mfma_f32_16x16x32_bf16 v[184:187], v[96:99], v[152:155], v[184:187]\n\t"
      "v_mfma_f32_16x16x32_bf16 v[188:191], v[100:103], v[152:155], v[188:191]\n\t"
      "v_mfma_f32_16x16x32_bf16 v[192:195], v[104:107], v[152:155], v[192:195]\n\t"
      "v_mfma_f32_16x16x32_bf16 v[196:199], v[108:111], v[152:155], v[196:199]\n\t"
      "s_waitcnt vmcnt(0)\n\t"
      "v_mfma_f32_16x16x32_bf16 v[184:187], v[112:115], v[156:159], v[184:187]\n\t"
      "v_mfma_f32_16x16x32_bf16 v[188:191], v[116:119], v[156:159], v[188:191]\n\t"
      "v_mfma_f32_16x16x32_bf16 v[192:195], v[120:123], v[156:159], v[192:195]\n\t"
      "v_mfma_f32_16x16x32_bf16 v[196:199], v[124:127], v[156:159], v[196:199]\n\t"
      "s_nop 4\n\t"
      "v_cmp_u_f32_e32 vcc, v184, v184\n\t"
      "s_cbranch_vccnz NQ6R_%=\n\t"
      "NQ6B_%=:\n\t"
      "ds_write_b128 v243, v[184:187]\n\t"
      "ds_write_b128 v164, v[188:191]\n\t"
      "ds_write_b128 v165, v[192:195]\n\t"
      "ds_write_b128 v166, v[196:199]\n\t"
      "s_waitcnt lgkmcnt(0)\n\t"
      "s_barrier\n\t"
      "s_branch GX_%=\n\t"
      "ASP_%=:\n\t"
      "s_mov_b32 m0, 163328\n\t"
      "s_cmp_ge_u32 s17, 65536\n\t"
      "s_cbranch_scc0 BOT_%=\n\t"
      "s_barrier\n\t"
      "BOT_%=:\n\t"
      "global_load_lds_dword v241, s[4:5]\n\t"
      "s_add_u32 s4, s4, 229376\n\t"
      "s_addc_u32 s5, s5, 0\n\t"
      "global_load_lds_dword v241, s[4:5]\n\t"
      "s_add_u32 s4, s4, 229376\n\t"
      "s_addc_u32 s5, s5, 0\n\t"
      "s_waitcnt vmcnt(0)\n\t"
      "s_sleep 1\n\t"
      "s_getpc_b64 s[44:45]\n\t"
      "s_add_u32 s44, s44, _Z11fused_blockPKcS0_S0_S0_S0_S0_S0_PcS1_@rel32@lo+4\n\t"
      "s_addc_u32 s45, s45, _Z11fused_blockPKcS0_S0_S0_S0_S0_S0_PcS1_@rel32@hi+12\n\t"
      "s_lshr_b32 s46, s17, 8\n\t"
      "v_lshrrev_b32_e32 v0, 4, v240\n\t"
      "v_add_u32_e32 v0, s46, v0\n\t"
      "v_min_u32_e32 v0, 507, v0\n\t"
      "v_lshlrev_b32_e32 v0, 7, v0\n\t"
      "global_load_lds_dword v0, s[44:45] sc1\n\t"
      "s_branch AEND_%=\n\t"
      "FQ0R_%=:\n\t"
      "s_mov_b32 s51, 0\n\t"
      "FQ0L_%=:\n\t"
      "s_add_u32 s51, s51, 1\n\t"
      "s_cmp_lt_u32 s51, 262144\n\t"
      "s_cbranch_scc0 FQ0X_%=\n\t"
      "s_waitcnt vmcnt(0)\n\t"
      "s_sleep 1\n\t"
      "global_load_dwordx4 v[128:131], v176, s[52:53] sc1\n\t"
      "global_load_dwordx4 v[132:135], v177, s[52:53] sc1\n\t"
      "global_load_dwordx4 v[136:139], v178, s[52:53] sc1\n\t"
      "global_load_dwordx4 v[140:143], v179, s[52:53] sc1\n\t"
      "global_load_dwordx4 v[144:147], v180, s[52:53] sc1\n\t"
      "global_load_dwordx4 v[148:151], v181, s[52:53] sc1\n\t"
      "global_load_dwordx4 v[152:155], v182, s[52:53] sc1\n\t"
      "global_load_dwordx4 v[156:159], v183, s[52:53] sc1\n\t"
      "s_waitcnt vmcnt(7)\n\t"
      "v_max3_u32 v234, v128, v129, v130\n\t"
      "v_max_u32_e32 v234, v234, v131\n\t"
      "v_cmp_eq_u32_e32 vcc, -1, v234\n\t"
      "s_cbranch_vccnz FQ0L_%=\n\t"
      "FQ0X_%=:\n\t"
      "s_branch FQ0B_%=\n\t"
      "FQ1R_%=:\n\t"
      "s_mov_b32 s51, 0\n\t"
      "FQ1L_%=:\n\t"
      "s_add_u32 s51, s51, 1\n\t"
      "s_cmp_lt_u32 s51, 262144\n\t"
      "s_cbranch_scc0 FQ1X_%=\n\t"
      "s_waitcnt vmcnt(0)\n\t"
      "s_sleep 1\n\t"
      "global_load_dwordx4 v[132:135], v177, s[52:53] sc1\n\t"
      "global_load_dwordx4 v[136:139], v178, s[52:53] sc1\n\t"
      "global_load_dwordx4 v[140:143], v179, s[52:53] sc1\n\t"
      "global_load_dwordx4 v[144:147], v180, s[52:53] sc1\n\t"
      "global_load_dwordx4 v[148:151], v181, s[52:53] sc1\n\t"
      "global_load_dwordx4 v[152:155], v182, s[52:53] sc1\n\t"
      "global_load_dwordx4 v[156:159], v183, s[52:53] sc1\n\t"
      "global_load_dwordx4 v[128:131], v176, s[54:55] sc1\n\t"
      "s_waitcnt vmcnt(7)\n\t"
      "v_max3_u32 v234, v132, v133, v134\n\t"
      "v_max_u32_e32 v234, v234, v135\n\t"
      "v_cmp_eq_u32_e32 vcc, -1, v234\n\t"
      "s_cbranch_vccnz FQ1L_%=\n\t"
      "FQ1X_%=:\n\t"
      "s_branch FQ1B_%=\n\t"
      "FQ2R_%=:\n\t"
      "s_mov_b32 s51, 0\n\t"
      "FQ2L_%=:\n\t"
      "s_add_u32 s51, s51, 1\n\t"
      "s_cmp_lt_u32 s51, 262144\n\t"
      "s_cbranch_scc0 FQ2X_%=\n\t"
      "s_waitcnt vmcnt(0)\n\t"
      "s_sleep 1\n\t"
      "global_load_dwordx4 v[136:139], v178, s[52:53] sc1\n\t"
      "global_load_dwordx4 v[140:143], v179, s[52:53] sc1\n\t"
      "global_load_dwordx4 v[144:147], v180, s[52:53] sc1\n\t"
      "global_load_dwordx4 v[148:151], v181, s[52:53] sc1\n\t"
      "global_load_dwordx4 v[152:155], v182, s[52:53] sc1\n\t"
      "global_load_dwordx4 v[156:159], v183, s[52:53] sc1\n\t"
      "global_load_dwordx4 v[128:131], v176, s[54:55] sc1\n\t"
      "global_load_dwordx4 v[132:135], v177, s[54:55] sc1\n\t"
      "s_waitcnt vmcnt(7)\n\t"
      "v_max3_u32 v234, v136, v137, v138\n\t"
      "v_max_u32_e32 v234, v234, v139\n\t"
      "v_cmp_eq_u32_e32 vcc, -1, v234\n\t"
      "s_cbranch_vccnz FQ2L_%=\n\t"
      "FQ2X_%=:\n\t"
      "s_branch FQ2B_%=\n\t"
      "FQ3R_%=:\n\t"
      "s_mov_b32 s51, 0\n\t"
      "FQ3L_%=:\n\t"
      "s_add_u32 s51, s51, 1\n\t"
      "s_cmp_lt_u32 s51, 262144\n\t"
      "s_cbranch_scc0 FQ3X_%=\n\t"
      "s_waitcnt vmcnt(0)\n\t"
      "s_sleep 1\n\t"
      "global_load_dwordx4 v[140:143], v179, s[52:53] sc1\n\t"
      "global_load_dwordx4 v[144:147], v180, s[52:53] sc1\n\t"
      "global_load_dwordx4 v[148:151], v181, s[52:53] sc1\n\t"
      "global_load_dwordx4 v[152:155], v182, s[52:53] sc1\n\t"
      "global_load_dwordx4 v[156:159], v183, s[52:53] sc1\n\t"
      "global_load_dwordx4 v[128:131], v176, s[54:55] sc1\n\t"
      "global_load_dwordx4 v[132:135], v177, s[54:55] sc1\n\t"
      "global_load_dwordx4 v[136:139], v178, s[54:55] sc1\n\t"
      "s_waitcnt vmcnt(7)\n\t"
      "v_max3_u32 v234, v140, v141, v142\n\t"
      "v_max_u32_e32 v234, v234, v143\n\t"
      "v_cmp_eq_u32_e32 vcc, -1, v234\n\t"
      "s_cbranch_vccnz FQ3L_%=\n\t"
      "FQ3X_%=:\n\t"
      "s_branch FQ3B_%=\n\t"
      "FQ4R_%=:\n\t"
      "s_mov_b32 s51, 0\n\t"
      "FQ4L_%=:\n\t"
      "s_add_u32 s51, s51, 1\n\t"
      "s_cmp_lt_u32 s51, 262144\n\t"
      "s_cbranch_scc0 FQ4X_%=\n\t"
      "s_waitcnt vmcnt(0)\n\t"
      "s_sleep 1\n\t"
      "global_load_dwordx4 v[144:147], v180, s[52:53] sc1\n\t"
      "global_load_dwordx4 v[148:151], v181, s[52:53] sc1\n\t"
      "global_load_dwordx4 v[152:155], v182, s[52:53] sc1\n\t"
      "global_load_dwordx4 v[156:159], v183, s[52:53] sc1\n\t"
      "global_load_dwordx4 v[128:131], v176, s[54:55] sc1\n\t"
      "global_load_dwordx4 v[132:135], v177, s[54:55] sc1\n\t"
      "global_load_dwordx4 v[136:139], v178, s[54:55] sc1\n\t"
      "global_load_dwordx4 v[140:143], v179, s[54:55] sc1\n\t"
      "s_waitcnt vmcnt(7)\n\t"
      "v_max3_u32 v234, v144, v145, v146\n\t"
      "v_max_u32_e32 v234, v234, v147\n\t"
      "v_cmp_eq_u32_e32 vcc, -1, v234\n\t"
      "s_cbranch_vccnz FQ4L_%=\n\t"
      "FQ4X_%=:\n\t"
      "s_branch FQ4B_%=\n\t"
      "FQ5R_%=:\n\t"
      "s_mov_b32 s51, 0\n\t"
      "FQ5L_%=:\n\t"
      "s_add_u32 s51, s51, 1\n\t"
      "s_cmp_lt_u32 s51, 262144\n\t"
      "s_cbranch_scc0 FQ5X_%=\n\t"
      "s_waitcnt vmcnt(0)\n\t"
      "s_sleep 1\n\t"
      "global_load_dwordx4 v[148:151], v181, s[52:53] sc1\n\t"
      "global_load_dwordx4 v[152:155], v182, s[52:53] sc1\n\t"
      "global_load_dwordx4 v[156:159], v183, s[52:53] sc1\n\t"
      "global_load_dwordx4 v[128:131], v176, s[54:55] sc1\n\t"
      "global_load_dwordx4 v[132:135], v177, s[54:55] sc1\n\t"
      "global_load_dwordx4 v[136:139], v178, s[54:55] sc1\n\t"
      "global_load_dwordx4 v[140:143], v179, s[54:55] sc1\n\t"
      "global_load_dwordx4 v[144:147], v180, s[54:55] sc1\n\t"
      "s_waitcnt vmcnt(7)\n\t"
      "v_max3_u32 v234, v148, v149, v150\n\t"
      "v_max_u32_e32 v234, v234, v151\n\t"
      "v_cmp_eq_u32_e32 vcc, -1, v234\n\t"
      "s_cbranch_vccnz FQ5L_%=\n\t"
      "FQ5X_%=:\n\t"
      "s_branch FQ5B_%=\n\t"
      "FQ6R_%=:\n\t"
      "s_mov_b32 s51, 0\n\t"
      "FQ6L_%=:\n\t"
      "s_add_u32 s51, s51, 1\n\t"
      "s_cmp_lt_u32 s51, 262144\n\t"
      "s_cbranch_scc0 FQ6X_%=\n\t"
      "s_waitcnt vmcnt(0)\n\t"
      "s_sleep 1\n\t"
      "global_load_dwordx4 v[152:155], v182, s[52:53] sc1\n\t"
      "global_load_dwordx4 v[156:159], v183, s[52:53] sc1\n\t"
      "global_load_dwordx4 v[128:131], v176, s[54:55] sc1\n\t"
      "global_load_dwordx4 v[132:135], v177, s[54:55] sc1\n\t"
      "global_load_dwordx4 v[136:139], v178, s[54:55] sc1\n\t"
      "global_load_dwordx4 v[140:143], v179, s[54:55] sc1\n\t"
      "global_load_dwordx4 v[144:147], v180, s[54:55] sc1\n\t"
      "global_load_dwordx4 v[148:151], v181, s[54:55] sc1\n\t"
      "s_waitcnt vmcnt(7)\n\t"
      "v_max3_u32 v234, v152, v153, v154\n\t"
      "v_max_u32_e32 v234, v234, v155\n\t"
      "v_cmp_eq_u32_e32 vcc, -1, v234\n\t"
      "s_cbranch_vccnz FQ6L_%=\n\t"
      "FQ6X_%=:\n\t"
      "s_branch FQ6B_%=\n\t"
      "FQ7R_%=:\n\t"
      "s_mov_b32 s51, 0\n\t"
      "FQ7L_%=:\n\t"
      "s_add_u32 s51, s51, 1\n\t"
      "s_cmp_lt_u32 s51, 262144\n\t"
      "s_cbranch_scc0 FQ7X_%=\n\t"
      "s_waitcnt vmcnt(0)\n\t"
      "s_sleep 1\n\t"
      "global_load_dwordx4 v[156:159], v183, s[52:53] sc1\n\t"
      "global_load_dwordx4 v[128:131], v176, s[54:55] sc1\n\t"
      "global_load_dwordx4 v[132:135], v177, s[54:55] sc1\n\t"
      "global_load_dwordx4 v[136:139], v178, s[54:55] sc1\n\t"
      "global_load_dwordx4 v[140:143], v179, s[54:55] sc1\n\t"
      "global_load_dwordx4 v[144:147], v180, s[54:55] sc1\n\t"
      "global_load_dwordx4 v[148:151], v181, s[54:55] sc1\n\t"
      "global_load_dwordx4 v[152:155], v182, s[54:55] sc1\n\t"
      "s_waitcnt vmcnt(7)\n\t"
      "v_max3_u32 v234, v156, v157, v158\n\t"
      "v_max_u32_e32 v234, v234, v159\n\t"
      "v_cmp_eq_u32_e32 vcc, -1, v234\n\t"
      "s_cbranch_vccnz FQ7L_%=\n\t"
      "FQ7X_%=:\n\t"
      "s_branch FQ7B_%=\n\t"
      "NQ1R_%=:\n\t"
      "s_mov_b32 s51, 0\n\t"
      "NQ1L_%=:\n\t"
      "s_add_u32 s51, s51, 1\n\t"
      "s_cmp_lt_u32 s51, 262144\n\t"
      "s_cbranch_scc0 NQ1X_%=\n\t"
      "s_waitcnt vmcnt(0)\n\t"
      "s_sleep 1\n\t"
      "global_load_dwordx4 v[128:131], v176, s[54:55] sc1\n\t"
      "global_load_dwordx4 v[132:135], v177, s[54:55] sc1\n\t"
      "global_load_dwordx4 v[136:139], v178, s[54:55] sc1\n\t"
      "global_load_dwordx4 v[140:143], v179, s[54:55] sc1\n\t"
      "global_load_dwordx4 v[144:147], v180, s[54:55] sc1\n\t"
      "global_load_dwordx4 v[148:151], v181, s[54:55] sc1\n\t"
      "global_load_dwordx4 v[152:155], v182, s[54:55] sc1\n\t"
      "global_load_dwordx4 v[156:159], v183, s[54:55] sc1\n\t"
      "s_waitcnt vmcnt(0)\n\t"
      "v_max3_u32 v234, v128, v129, v130\n\t"
      "v_max3_u32 v234, v234, v131, v132\n\t"
      "v_max3_u32 v234, v234, v133, v134\n\t"
      "v_max3_u32 v234, v234, v135, v136\n\t"
      "v_max3_u32 v234, v234, v137, v138\n\t"
      "v_max3_u32 v234, v234, v139, v140\n\t"
      "v_max3_u32 v234, v234, v141, v142\n\t"
      "v_max3_u32 v234, v234, v143, v144\n\t"
      "v_max3_u32 v234, v234, v145, v146\n\t"
      "v_max3_u32 v234, v234, v147, v148\n\t"
      "v_max3_u32 v234, v234, v149, v150\n\t"
      "v_max3_u32 v234, v234, v151, v152\n\t"
      "v_max3_u32 v234, v234, v153, v154\n\t"
      "v_max3_u32 v234, v234, v155, v156\n\t"
      "v_max3_u32 v234, v234, v157, v158\n\t"
      "v_max_u32_e32 v234, v234, v159\n\t"
      "v_cmp_eq_u32_e32 vcc, -1, v234\n\t"
      "s_cbranch_vccnz NQ1L_%=\n\t"
      "NQ1X_%=:\n\t"
      "v_mfma_f32_16x16x32_bf16 v[200:203], v[0:3], v[128:131], v[236:239]\n\t"
      "v_mfma_f32_16x16x32_bf16 v[204:207], v[4:7], v[128:131], 0\n\t"
      "v_mfma_f32_16x16x32_bf16 v[208:211], v[8:11], v[128:131], 0\n\t"
      "v_mfma_f32_16x16x32_bf16 v[212:215], v[12:15], v[128:131], 0\n\t"
      "v_mfma_f32_16x16x32_bf16 v[200:203], v[16:19], v[132:135], v[200:203]\n\t"
      "v_mfma_f32_16x16x32_bf16 v[204:207], v[20:23], v[132:135], v[204:207]\n\t"
      "v_mfma_f32_16x16x32_bf16 v[208:211], v[24:27], v[132:135], v[208:211]\n\t"
      "v_mfma_f32_16x16x32_bf16 v[212:215], v[28:31], v[132:135], v[212:215]\n\t"
      "v_mfma_f32_16x16x32_bf16 v[200:203], v[32:35], v[136:139], v[200:203]\n\t"
      "v_mfma_f32_16x16x32_bf16 v[204:207], v[36:39], v[136:139], v[204:207]\n\t"
      "v_mfma_f32_16x16x32_bf16 v[208:211], v[40:43], v[136:139], v[208:211]\n\t"
      "v_mfma_f32_16x16x32_bf16 v[212:215], v[44:47], v[136:139], v[212:215]\n\t"
      "v_mfma_f32_16x16x32_bf16 v[200:203], v[48:51], v[140:143], v[200:203]\n\t"
      "v_mfma_f32_16x16x32_bf16 v[204:207], v[52:55], v[140:143], v[204:207]\n\t"
      "v_mfma_f32_16x16x32_bf16 v[208:211], v[56:59], v[140:143], v[208:211]\n\t"
      "v_mfma_f32_16x16x32_bf16 v[212:215], v[60:63], v[140:143], v[212:215]\n\t"
      "v_mfma_f32_16x16x32_bf16 v[200:203], v[64:67], v[144:147], v[200:203]\n\t"
      "v_mfma_f32_16x16x32_bf16 v[204:207], v[68:71], v[144:147], v[204:207]\n\t"
      "v_mfma_f32_16x16x32_bf16 v[208:211], v[72:75], v[144:147], v[208:211]\n\t"
      "v_mfma_f32_16x16x32_bf16 v[212:215], v[76:79], v[144:147], v[212:215]\n\t"
      "v_mfma_f32_16x16x32_bf16 v[200:203], v[80:83], v[148:151], v[200:203]\n\t"
      "v_mfma_f32_16x16x32_bf16 v[204:207], v[84:87], v[148:151], v[204:207]\n\t"
      "v_mfma_f32_16x16x32_bf16 v[208:211], v[88:91], v[148:151], v[208:211]\n\t"
      "v_mfma_f32_16x16x32_bf16 v[212:215], v[92:95], v[148:151], v[212:215]\n\t"
      "v_mfma_f32_16x16x32_bf16 v[200:203], v[96:99], v[152:155], v[200:203]\n\t"
      "v_mfma_f32_16x16x32_bf16 v[204:207], v[100:103], v[152:155], v[204:207]\n\t"
      "v_mfma_f32_16x16x32_bf16 v[208:211], v[104:107], v[152:155], v[208:211]\n\t"
      "v_mfma_f32_16x16x32_bf16 v[212:215], v[108:111], v[152:155], v[212:215]\n\t"
      "v_mfma_f32_16x16x32_bf16 v[200:203], v[112:115], v[156:159], v[200:203]\n\t"
      "v_mfma_f32_16x16x32_bf16 v[204:207], v[116:119], v[156:159], v[204:207]\n\t"
      "v_mfma_f32_16x16x32_bf16 v[208:211], v[120:123], v[156:159], v[208:211]\n\t"
      "v_mfma_f32_16x16x32_bf16 v[212:215], v[124:127], v[156:159], v[212:215]\n\t"
      "global_load_dwordx4 v[128:131], v176, s[56:57] sc1\n\t"
      "global_load_dwordx4 v[132:135], v177, s[56:57] sc1\n\t"
      "global_load_dwordx4 v[136:139], v178, s[56:57] sc1\n\t"
      "global_load_dwordx4 v[140:143], v179, s[56:57] sc1\n\t"
      "global_load_dwordx4 v[144:147], v180, s[56:57] sc1\n\t"
      "global_load_dwordx4 v[148:151], v181, s[56:57] sc1\n\t"
      "global_load_dwordx4 v[152:155], v182, s[56:57] sc1\n\t"
      "global_load_dwordx4 v[156:159], v183, s[56:57] sc1\n\t"
      "s_waitcnt vmcnt(0)\n\t"
      "s_nop 7\n\t"
      "s_branch NQ1B_%=\n\t"
      "NQ2R_%=:\n\t"
      "s_mov_b32 s51, 0\n\t"
      "NQ2L_%=:\n\t"
      "s_add_u32 s51, s51, 1\n\t"
      "s_cmp_lt_u32 s51, 262144\n\t"
      "s_cbranch_scc0 NQ2X_%=\n\t"
      "s_waitcnt vmcnt(0)\n\t"
      "s_sleep 1\n\t"
      "global_load_dwordx4 v[128:131], v176, s[56:57] sc1\n\t"
      "global_load_dwordx4 v[132:135], v177, s[56:57] sc1\n\t"
      "global_load_dwordx4 v[136:139], v178, s[56:57] sc1\n\t"
      "global_load_dwordx4 v[140:143], v179, s[56:57] sc1\n\t"
      "global_load_dwordx4 v[144:147], v180, s[56:57] sc1\n\t"
      "global_load_dwordx4 v[148:151], v181, s[56:57] sc1\n\t"
      "global_load_dwordx4 v[152:155], v182, s[56:57] sc1\n\t"
      "global_load_dwordx4 v[156:159], v183, s[56:57] sc1\n\t"
      "s_waitcnt vmcnt(0)\n\t"
      "v_max3_u32 v234, v128, v129, v130\n\t"
      "v_max3_u32 v234, v234, v131, v132\n\t"
      "v_max3_u32 v234, v234, v133, v134\n\t"
      "v_max3_u32 v234, v234, v135, v136\n\t"
      "v_max3_u32 v234, v234, v137, v138\n\t"
      "v_max3_u32 v234, v234, v139, v140\n\t"
      "v_max3_u32 v234, v234, v141, v142\n\t"
      "v_max3_u32 v234, v234, v143, v144\n\t"
      "v_max3_u32 v234, v234, v145, v146\n\t"
      "v_max3_u32 v234, v234, v147, v148\n\t"
      "v_max3_u32 v234, v234, v149, v150\n\t"
      "v_max3_u32 v234, v234, v151, v152\n\t"
      "v_max3_u32 v234, v234, v153, v154\n\t"
      "v_max3_u32 v234, v234, v155, v156\n\t"
      "v_max3_u32 v234, v234, v157, v158\n\t"
      "v_max_u32_e32 v234, v234, v159\n\t"
      "v_cmp_eq_u32_e32 vcc, -1, v234\n\t"
      "s_cbranch_vccnz NQ2L_%=\n\t"
      "NQ2X_%=:\n\t"
      "v_mfma_f32_16x16x32_bf16 v[184:187], v[0:3], v[128:131], v[236:239]\n\t"
      "v_mfma_f32_16x16x32_bf16 v[188:191], v[4:7], v[128:131], 0\n\t"
      "v_mfma_f32_16x16x32_bf16 v[192:195], v[8:11], v[128:131], 0\n\t"
      "v_mfma_f32_16x16x32_bf16 v[196:199], v[12:15], v[128:131], 0\n\t"
      "v_mfma_f32_16x16x32_bf16 v[184:187], v[16:19], v[132:135], v[184:187]\n\t"
      "v_mfma_f32_16x16x32_bf16 v[188:191], v[20:23], v[132:135], v[188:191]\n\t"
      "v_mfma_f32_16x16x32_bf16 v[192:195], v[24:27], v[132:135], v[192:195]\n\t"
      "v_mfma_f32_16x16x32_bf16 v[196:199], v[28:31], v[132:135], v[196:199]\n\t"
      "v_mfma_f32_16x16x32_bf16 v[184:187], v[32:35], v[136:139], v[184:187]\n\t"
      "v_mfma_f32_16x16x32_bf16 v[188:191], v[36:39], v[136:139], v[188:191]\n\t"
      "v_mfma_f32_16x16x32_bf16 v[192:195], v[40:43], v[136:139], v[192:195]\n\t"
      "v_mfma_f32_16x16x32_bf16 v[196:199], v[44:47], v[136:139], v[196:199]\n\t"
      "v_mfma_f32_16x16x32_bf16 v[184:187], v[48:51], v[140:143], v[184:187]\n\t"
      "v_mfma_f32_16x16x32_bf16 v[188:191], v[52:55], v[140:143], v[188:191]\n\t"
      "v_mfma_f32_16x16x32_bf16 v[192:195], v[56:59], v[140:143], v[192:195]\n\t"
      "v_mfma_f32_16x16x32_bf16 v[196:199], v[60:63], v[140:143], v[196:199]\n\t"
      "v_mfma_f32_16x16x32_bf16 v[184:187], v[64:67], v[144:147], v[184:187]\n\t"
      "v_mfma_f32_16x16x32_bf16 v[188:191], v[68:71], v[144:147], v[188:191]\n\t"
      "v_mfma_f32_16x16x32_bf16 v[192:195], v[72:75], v[144:147], v[192:195]\n\t"
      "v_mfma_f32_16x16x32_bf16 v[196:199], v[76:79], v[144:147], v[196:199]\n\t"
      "v_mfma_f32_16x16x32_bf16 v[184:187], v[80:83], v[148:151], v[184:187]\n\t"
      "v_mfma_f32_16x16x32_bf16 v[188:191], v[84:87], v[148:151], v[188:191]\n\t"
      "v_mfma_f32_16x16x32_bf16 v[192:195], v[88:91], v[148:151], v[192:195]\n\t"
      "v_mfma_f32_16x16x32_bf16 v[196:199], v[92:95], v[148:151], v[196:199]\n\t"
      "v_mfma_f32_16x16x32_bf16 v[184:187], v[96:99], v[152:155], v[184:187]\n\t"
      "v_mfma_f32_16x16x32_bf16 v[188:191], v[100:103], v[152:155], v[188:191]\n\t"
      "v_mfma_f32_16x16x32_bf16 v[192:195], v[104:107], v[152:155], v[192:195]\n\t"
      "v_mfma_f32_16x16x32_bf16 v[196:199], v[108:111], v[152:155], v[196:199]\n\t"
      "v_mfma_f32_16x16x32_bf16 v[184:187], v[112:115], v[156:159], v[184:187]\n\t"
      "v_mfma_f32_16x16x32_bf16 v[188:191], v[116:119], v[156:159], v[188:191]\n\t"
      "v_mfma_f32_16x16x32_bf16 v[192:195], v[120:123], v[156:159], v[192:195]\n\t"
      "v_mfma_f32_16x16x32_bf16 v[196:199], v[124:127], v[156:159], v[196:199]\n\t"
      "global_load_dwordx4 v[128:131], v176, s[58:59] sc1\n\t"
      "global_load_dwordx4 v[132:135], v177, s[58:59] sc1\n\t"
      "global_load_dwordx4 v[136:139], v178, s[58:59] sc1\n\t"
      "global_load_dwordx4 v[140:143], v179, s[58:59] sc1\n\t"
      "global_load_dwordx4 v[144:147], v180, s[58:59] sc1\n\t"
      "global_load_dwordx4 v[148:151], v181, s[58:59] sc1\n\t"
      "global_load_dwordx4 v[152:155], v182, s[58:59] sc1\n\t"
      "global_load_dwordx4 v[156:159], v183, s[58:59] sc1\n\t"
      "s_waitcnt vmcnt(0)\n\t"
      "s_nop 7\n\t"
      "s_branch NQ2B_%=\n\t"
      "NQ3R_%=:\n\t"
      "s_mov_b32 s51, 0\n\t"
      "NQ3L_%=:\n\t"
      "s_add_u32 s51, s51, 1\n\t"
      "s_cmp_lt_u32 s51, 262144\n\t"
      "s_cbranch_scc0 NQ3X_%=\n\t"
      "s_waitcnt vmcnt(0)\n\t"
      "s_sleep 1\n\t"
      "global_load_dwordx4 v[128:131], v176, s[58:59] sc1\n\t"
      "global_load_dwordx4 v[132:135], v177, s[58:59] sc1\n\t"
      "global_load_dwordx4 v[136:139], v178, s[58:59] sc1\n\t"
      "global_load_dwordx4 v[140:143], v179, s[58:59] sc1\n\t"
      "global_load_dwordx4 v[144:147], v180, s[58:59] sc1\n\t"
      "global_load_dwordx4 v[148:151], v181, s[58:59] sc1\n\t"
      "global_load_dwordx4 v[152:155], v182, s[58:59] sc1\n\t"
      "global_load_dwordx4 v[156:159], v183, s[58:59] sc1\n\t"
      "s_waitcnt vmcnt(0)\n\t"
      "v_max3_u32 v234, v128, v129, v130\n\t"
      "v_max3_u32 v234, v234, v131, v132\n\t"
      "v_max3_u32 v234, v234, v133, v134\n\t"
      "v_max3_u32 v234, v234, v135, v136\n\t"
      "v_max3_u32 v234, v234, v137, v138\n\t"
      "v_max3_u32 v234, v234, v139, v140\n\t"
      "v_max3_u32 v234, v234, v141, v142\n\t"
      "v_max3_u32 v234, v234, v143, v144\n\t"
      "v_max3_u32 v234, v234, v145, v146\n\t"
      "v_max3_u32 v234, v234, v147, v148\n\t"
      "v_max3_u32 v234, v234, v149, v150\n\t"
      "v_max3_u32 v234, v234, v151, v152\n\t"
      "v_max3_u32 v234, v234, v153, v154\n\t"
      "v_max3_u32 v234, v234, v155, v156\n\t"
      "v_max3_u32 v234, v234, v157, v158\n\t"
      "v_max_u32_e32 v234, v234, v159\n\t"
      "v_cmp_eq_u32_e32 vcc, -1, v234\n\t"
      "s_cbranch_vccnz NQ3L_%=\n\t"
      "NQ3X_%=:\n\t"
      "v_mfma_f32_16x16x32_bf16 v[200:203], v[0:3], v[128:131], v[236:239]\n\t"
      "v_mfma_f32_16x16x32_bf16 v[204:207], v[4:7], v[128:131], 0\n\t"
      "v_mfma_f32_16x16x32_bf16 v[208:211], v[8:11], v[128:131], 0\n\t"
      "v_mfma_f32_16x16x32_bf16 v[212:215], v[12:15], v[128:131], 0\n\t"
      "v_mfma_f32_16x16x32_bf16 v[200:203], v[16:19], v[132:135], v[200:203]\n\t"
      "v_mfma_f32_16x16x32_bf16 v[204:207], v[20:23], v[132:135], v[204:207]\n\t"
      "v_mfma_f32_16x16x32_bf16 v[208:211], v[24:27], v[132:135], v[208:211]\n\t"
      "v_mfma_f32_16x16x32_bf16 v[212:215], v[28:31], v[132:135], v[212:215]\n\t"
      "v_mfma_f32_16x16x32_bf16 v[200:203], v[32:35], v[136:139], v[200:203]\n\t"
      "v_mfma_f32_16x16x32_bf16 v[204:207], v[36:39], v[136:139], v[204:207]\n\t"
      "v_mfma_f32_16x16x32_bf16 v[208:211], v[40:43], v[136:139], v[208:211]\n\t"
      "v_mfma_f32_16x16x32_bf16 v[212:215], v[44:47], v[136:139], v[212:215]\n\t"
      "v_mfma_f32_16x16x32_bf16 v[200:203], v[48:51], v[140:143], v[200:203]\n\t"
      "v_mfma_f32_16x16x32_bf16 v[204:207], v[52:55], v[140:143], v[204:207]\n\t"
      "v_mfma_f32_16x16x32_bf16 v[208:211], v[56:59], v[140:143], v[208:211]\n\t"
      "v_mfma_f32_16x16x32_bf16 v[212:215], v[60:63], v[140:143], v[212:215]\n\t"
      "v_mfma_f32_16x16x32_bf16 v[200:203], v[64:67], v[144:147], v[200:203]\n\t"
      "v_mfma_f32_16x16x32_bf16 v[204:207], v[68:71], v[144:147], v[204:207]\n\t"
      "v_mfma_f32_16x16x32_bf16 v[208:211], v[72:75], v[144:147], v[208:211]\n\t"
      "v_mfma_f32_16x16x32_bf16 v[212:215], v[76:79], v[144:147], v[212:215]\n\t"
      "v_mfma_f32_16x16x32_bf16 v[200:203], v[80:83], v[148:151], v[200:203]\n\t"
      "v_mfma_f32_16x16x32_bf16 v[204:207], v[84:87], v[148:151], v[204:207]\n\t"
      "v_mfma_f32_16x16x32_bf16 v[208:211], v[88:91], v[148:151], v[208:211]\n\t"
      "v_mfma_f32_16x16x32_bf16 v[212:215], v[92:95], v[148:151], v[212:215]\n\t"
      "v_mfma_f32_16x16x32_bf16 v[200:203], v[96:99], v[152:155], v[200:203]\n\t"
      "v_mfma_f32_16x16x32_bf16 v[204:207], v[100:103], v[152:155], v[204:207]\n\t"
      "v_mfma_f32_16x16x32_bf16 v[208:211], v[104:107], v[152:155], v[208:211]\n\t"
      "v_mfma_f32_16x16x32_bf16 v[212:215], v[108:111], v[152:155], v[212:215]\n\t"
      "v_mfma_f32_16x16x32_bf16 v[200:203], v[112:115], v[156:159], v[200:203]\n\t"
      "v_mfma_f32_16x16x32_bf16 v[204:207], v[116:119], v[156:159], v[204:207]\n\t"
      "v_mfma_f32_16x16x32_bf16 v[208:211], v[120:123], v[156:159], v[208:211]\n\t"
      "v_mfma_f32_16x16x32_bf16 v[212:215], v[124:127], v[156:159], v[212:215]\n\t"
      "global_load_dwordx4 v[128:131], v176, s[60:61] sc1\n\t"
      "global_load_dwordx4 v[132:135], v177, s[60:61] sc1\n\t"
      "global_load_dwordx4 v[136:139], v178, s[60:61] sc1\n\t"
      "global_load_dwordx4 v[140:143], v179, s[60:61] sc1\n\t"
      "global_load_dwordx4 v[144:147], v180, s[60:61] sc1\n\t"
      "global_load_dwordx4 v[148:151], v181, s[60:61] sc1\n\t"
      "global_load_dwordx4 v[152:155], v182, s[60:61] sc1\n\t"
      "global_load_dwordx4 v[156:159], v183, s[60:61] sc1\n\t"
      "s_waitcnt vmcnt(0)\n\t"
      "s_nop 7\n\t"
      "s_branch NQ3B_%=\n\t"
      "NQ4R_%=:\n\t"
      "s_mov_b32 s51, 0\n\t"
      "NQ4L_%=:\n\t"
      "s_add_u32 s51, s51, 1\n\t"
      "s_cmp_lt_u32 s51, 262144\n\t"
      "s_cbranch_scc0 NQ4X_%=\n\t"
      "s_waitcnt vmcnt(0)\n\t"
      "s_sleep 1\n\t"
      "global_load_dwordx4 v[128:131], v176, s[60:61] sc1\n\t"
      "global_load_dwordx4 v[132:135], v177, s[60:61] sc1\n\t"
      "global_load_dwordx4 v[136:139], v178, s[60:61] sc1\n\t"
      "global_load_dwordx4 v[140:143], v179, s[60:61] sc1\n\t"
      "global_load_dwordx4 v[144:147], v180, s[60:61] sc1\n\t"
      "global_load_dwordx4 v[148:151], v181, s[60:61] sc1\n\t"
      "global_load_dwordx4 v[152:155], v182, s[60:61] sc1\n\t"
      "global_load_dwordx4 v[156:159], v183, s[60:61] sc1\n\t"
      "s_waitcnt vmcnt(0)\n\t"
      "v_max3_u32 v234, v128, v129, v130\n\t"
      "v_max3_u32 v234, v234, v131, v132\n\t"
      "v_max3_u32 v234, v234, v133, v134\n\t"
      "v_max3_u32 v234, v234, v135, v136\n\t"
      "v_max3_u32 v234, v234, v137, v138\n\t"
      "v_max3_u32 v234, v234, v139, v140\n\t"
      "v_max3_u32 v234, v234, v141, v142\n\t"
      "v_max3_u32 v234, v234, v143, v144\n\t"
      "v_max3_u32 v234, v234, v145, v146\n\t"
      "v_max3_u32 v234, v234, v147, v148\n\t"
      "v_max3_u32 v234, v234, v149, v150\n\t"
      "v_max3_u32 v234, v234, v151, v152\n\t"
      "v_max3_u32 v234, v234, v153, v154\n\t"
      "v_max3_u32 v234, v234, v155, v156\n\t"
      "v_max3_u32 v234, v234, v157, v158\n\t"
      "v_max_u32_e32 v234, v234, v159\n\t"
      "v_cmp_eq_u32_e32 vcc, -1, v234\n\t"
      "s_cbranch_vccnz NQ4L_%=\n\t"
      "NQ4X_%=:\n\t"
      "v_mfma_f32_16x16x32_bf16 v[184:187], v[0:3], v[128:131], v[236:239]\n\t"
      "v_mfma_f32_16x16x32_bf16 v[188:191], v[4:7], v[128:131], 0\n\t"
      "v_mfma_f32_16x16x32_bf16 v[192:195], v[8:11], v[128:131], 0\n\t"
      "v_mfma_f32_16x16x32_bf16 v[196:199], v[12:15], v[128:131], 0\n\t"
      "v_mfma_f32_16x16x32_bf16 v[184:187], v[16:19], v[132:135], v[184:187]\n\t"
      "v_mfma_f32_16x16x32_bf16 v[188:191], v[20:23], v[132:135], v[188:191]\n\t"
      "v_mfma_f32_16x16x32_bf16 v[192:195], v[24:27], v[132:135], v[192:195]\n\t"
      "v_mfma_f32_16x16x32_bf16 v[196:199], v[28:31], v[132:135], v[196:199]\n\t"
      "v_mfma_f32_16x16x32_bf16 v[184:187], v[32:35], v[136:139], v[184:187]\n\t"
      "v_mfma_f32_16x16x32_bf16 v[188:191], v[36:39], v[136:139], v[188:191]\n\t"
      "v_mfma_f32_16x16x32_bf16 v[192:195], v[40:43], v[136:139], v[192:195]\n\t"
      "v_mfma_f32_16x16x32_bf16 v[196:199], v[44:47], v[136:139], v[196:199]\n\t"
      "v_mfma_f32_16x16x32_bf16 v[184:187], v[48:51], v[140:143], v[184:187]\n\t"
      "v_mfma_f32_16x16x32_bf16 v[188:191], v[52:55], v[140:143], v[188:191]\n\t"
      "v_mfma_f32_16x16x32_bf16 v[192:195], v[56:59], v[140:143], v[192:195]\n\t"
      "v_mfma_f32_16x16x32_bf16 v[196:199], v[60:63], v[140:143], v[196:199]\n\t"
      "v_mfma_f32_16x16x32_bf16 v[184:187], v[64:67], v[144:147], v[184:187]\n\t"
      "v_mfma_f32_16x16x32_bf16 v[188:191], v[68:71], v[144:147], v[188:191]\n\t"
      "v_mfma_f32_16x16x32_bf16 v[192:195], v[72:75], v[144:147], v[192:195]\n\t"
      "v_mfma_f32_16x16x32_bf16 v[196:199], v[76:79], v[144:147], v[196:199]\n\t"
      "v_mfma_f32_16x16x32_bf16 v[184:187], v[80:83], v[148:151], v[184:187]\n\t"
      "v_mfma_f32_16x16x32_bf16 v[188:191], v[84:87], v[148:151], v[188:191]\n\t"
      "v_mfma_f32_16x16x32_bf16 v[192:195], v[88:91], v[148:151], v[192:195]\n\t"
      "v_mfma_f32_16x16x32_bf16 v[196:199], v[92:95], v[148:151], v[196:199]\n\t"
      "v_mfma_f32_16x16x32_bf16 v[184:187], v[96:99], v[152:155], v[184:187]\n\t"
      "v_mfma_f32_16x16x32_bf16 v[188:191], v[100:103], v[152:155], v[188:191]\n\t"
      "v_mfma_f32_16x16x32_bf16 v[192:195], v[104:107], v[152:155], v[192:195]\n\t"
      "v_mfma_f32_16x16x32_bf16 v[196:199], v[108:111], v[152:155], v[196:199]\n\t"
      "v_mfma_f32_16x16x32_bf16 v[184:187], v[112:115], v[156:159], v[184:187]\n\t"
      "v_mfma_f32_16x16x32_bf16 v[188:191], v[116:119], v[156:159], v[188:191]\n\t"
      "v_mfma_f32_16x16x32_bf16 v[192:195], v[120:123], v[156:159], v[192:195]\n\t"
      "v_mfma_f32_16x16x32_bf16 v[196:199], v[124:127], v[156:159], v[196:199]\n\t"
      "global_load_dwordx4 v[128:131], v176, s[62:63] sc1\n\t"
      "global_load_dwordx4 v[132:135], v177, s[62:63] sc1\n\t"
      "global_load_dwordx4 v[136:139], v178, s[62:63] sc1\n\t"
      "global_load_dwordx4 v[140:143], v179, s[62:63] sc1\n\t"
      "global_load_dwordx4 v[144:147], v180, s[62:63] sc1\n\t"
      "global_load_dwordx4 v[148:151], v181, s[62:63] sc1\n\t"
      "global_load_dwordx4 v[152:155], v182, s[62:63] sc1\n\t"
      "global_load_dwordx4 v[156:159], v183, s[62:63] sc1\n\t"
      "s_waitcnt vmcnt(0)\n\t"
      "s_nop 7\n\t"
      "s_branch NQ4B_%=\n\t"
      "NQ5R_%=:\n\t"
      "s_mov_b32 s51, 0\n\t"
      "NQ5L_%=:\n\t"
      "s_add_u32 s51, s51, 1\n\t"
      "s_cmp_lt_u32 s51, 262144\n\t"
      "s_cbranch_scc0 NQ5X_%=\n\t"
      "s_waitcnt vmcnt(0)\n\t"
      "s_sleep 1\n\t"
      "global_load_dwordx4 v[128:131], v176, s[62:63] sc1\n\t"
      "global_load_dwordx4 v[132:135], v177, s[62:63] sc1\n\t"
      "global_load_dwordx4 v[136:139], v178, s[62:63] sc1\n\t"
      "global_load_dwordx4 v[140:143], v179, s[62:63] sc1\n\t"
      "global_load_dwordx4 v[144:147], v180, s[62:63] sc1\n\t"
      "global_load_dwordx4 v[148:151], v181, s[62:63] sc1\n\t"
      "global_load_dwordx4 v[152:155], v182, s[62:63] sc1\n\t"
      "global_load_dwordx4 v[156:159], v183, s[62:63] sc1\n\t"
      "s_waitcnt vmcnt(0)\n\t"
      "v_max3_u32 v234, v128, v129, v130\n\t"
      "v_max3_u32 v234, v234, v131, v132\n\t"
      "v_max3_u32 v234, v234, v133, v134\n\t"
      "v_max3_u32 v234, v234, v135, v136\n\t"
      "v_max3_u32 v234, v234, v137, v138\n\t"
      "v_max3_u32 v234, v234, v139, v140\n\t"
      "v_max3_u32 v234, v234, v141, v142\n\t"
      "v_max3_u32 v234, v234, v143, v144\n\t"
      "v_max3_u32 v234, v234, v145, v146\n\t"
      "v_max3_u32 v234, v234, v147, v148\n\t"
      "v_max3_u32 v234, v234, v149, v150\n\t"
      "v_max3_u32 v234, v234, v151, v152\n\t"
      "v_max3_u32 v234, v234, v153, v154\n\t"
      "v_max3_u32 v234, v234, v155, v156\n\t"
      "v_max3_u32 v234, v234, v157, v158\n\t"
      "v_max_u32_e32 v234, v234, v159\n\t"
      "v_cmp_eq_u32_e32 vcc, -1, v234\n\t"
      "s_cbranch_vccnz NQ5L_%=\n\t"
      "NQ5X_%=:\n\t"
      "v_mfma_f32_16x16x32_bf16 v[200:203], v[0:3], v[128:131], v[236:239]\n\t"
      "v_mfma_f32_16x16x32_bf16 v[204:207], v[4:7], v[128:131], 0\n\t"
      "v_mfma_f32_16x16x32_bf16 v[208:211], v[8:11], v[128:131], 0\n\t"
      "v_mfma_f32_16x16x32_bf16 v[212:215], v[12:15], v[128:131], 0\n\t"
      "v_mfma_f32_16x16x32_bf16 v[200:203], v[16:19], v[132:135], v[200:203]\n\t"
      "v_mfma_f32_16x16x32_bf16 v[204:207], v[20:23], v[132:135], v[204:207]\n\t"
      "v_mfma_f32_16x16x32_bf16 v[208:211], v[24:27], v[132:135], v[208:211]\n\t"
      "v_mfma_f32_16x16x32_bf16 v[212:215], v[28:31], v[132:135], v[212:215]\n\t"
      "v_mfma_f32_16x16x32_bf16 v[200:203], v[32:35], v[136:139], v[200:203]\n\t"
      "v_mfma_f32_16x16x32_bf16 v[204:207], v[36:39], v[136:139], v[204:207]\n\t"
      "v_mfma_f32_16x16x32_bf16 v[208:211], v[40:43], v[136:139], v[208:211]\n\t"
      "v_mfma_f32_16x16x32_bf16 v[212:215], v[44:47], v[136:139], v[212:215]\n\t"
      "v_mfma_f32_16x16x32_bf16 v[200:203], v[48:51], v[140:143], v[200:203]\n\t"
      "v_mfma_f32_16x16x32_bf16 v[204:207], v[52:55], v[140:143], v[204:207]\n\t"
      "v_mfma_f32_16x16x32_bf16 v[208:211], v[56:59], v[140:143], v[208:211]\n\t"
      "v_mfma_f32_16x16x32_bf16 v[212:215], v[60:63], v[140:143], v[212:215]\n\t"
      "v_mfma_f32_16x16x32_bf16 v[200:203], v[64:67], v[144:147], v[200:203]\n\t"
      "v_mfma_f32_16x16x32_bf16 v[204:207], v[68:71], v[144:147], v[204:207]\n\t"
      "v_mfma_f32_16x16x32_bf16 v[208:211], v[72:75], v[144:147], v[208:211]\n\t"
      "v_mfma_f32_16x16x32_bf16 v[212:215], v[76:79], v[144:147], v[212:215]\n\t"
      "v_mfma_f32_16x16x32_bf16 v[200:203], v[80:83], v[148:151], v[200:203]\n\t"
      "v_mfma_f32_16x16x32_bf16 v[204:207], v[84:87], v[148:151], v[204:207]\n\t"
      "v_mfma_f32_16x16x32_bf16 v[208:211], v[88:91], v[148:151], v[208:211]\n\t"
      "v_mfma_f32_16x16x32_bf16 v[212:215], v[92:95], v[148:151], v[212:215]\n\t"
      "v_mfma_f32_16x16x32_bf16 v[200:203], v[96:99], v[152:155], v[200:203]\n\t"
      "v_mfma_f32_16x16x32_bf16 v[204:207], v[100:103], v[152:155], v[204:207]\n\t"
      "v_mfma_f32_16x16x32_bf16 v[208:211], v[104:107], v[152:155], v[208:211]\n\t"
      "v_mfma_f32_16x16x32_bf16 v[212:215], v[108:111], v[152:155], v[212:215]\n\t"
      "v_mfma_f32_16x16x32_bf16 v[200:203], v[112:115], v[156:159], v[200:203]\n\t"
      "v_mfma_f32_16x16x32_bf16 v[204:207], v[116:119], v[156:159], v[204:207]\n\t"
      "v_mfma_f32_16x16x32_bf16 v[208:211], v[120:123], v[156:159], v[208:211]\n\t"
      "v_mfma_f32_16x16x32_bf16 v[212:215], v[124:127], v[156:159], v[212:215]\n\t"
      "global_load_dwordx4 v[128:131], v176, s[64:65] sc1\n\t"
      "global_load_dwordx4 v[132:135], v177, s[64:65] sc1\n\t"
      "global_load_dwordx4 v[136:139], v178, s[64:65] sc1\n\t"
      "global_load_dwordx4 v[140:143], v179, s[64:65] sc1\n\t"
      "global_load_dwordx4 v[144:147], v180, s[64:65] sc1\n\t"
      "global_load_dwordx4 v[148:151], v181, s[64:65] sc1\n\t"
      "global_load_dwordx4 v[152:155], v182, s[64:65] sc1\n\t"
      "global_load_dwordx4 v[156:159], v183, s[64:65] sc1\n\t"
      "s_waitcnt vmcnt(0)\n\t"
      "s_nop 7\n\t"
      "s_branch NQ5B_%=\n\t"
      "NQ6R_%=:\n\t"
      "s_mov_b32 s51, 0\n\t"
      "NQ6L_%=:\n\t"
      "s_add_u32 s51, s51, 1\n\t"
      "s_cmp_lt_u32 s51, 262144\n\t"
      "s_cbranch_scc0 NQ6X_%=\n\t"
      "s_waitcnt vmcnt(0)\n\t"
      "s_sleep 1\n\t"
      "global_load_dwordx4 v[128:131], v176, s[64:65] sc1\n\t"
      "global_load_dwordx4 v[132:135], v177, s[64:65] sc1\n\t"
      "global_load_dwordx4 v[136:139], v178, s[64:65] sc1\n\t"
      "global_load_dwordx4 v[140:143], v179, s[64:65] sc1\n\t"
      "global_load_dwordx4 v[144:147], v180, s[64:65] sc1\n\t"
      "global_load_dwordx4 v[148:151], v181, s[64:65] sc1\n\t"
      "global_load_dwordx4 v[152:155], v182, s[64:65] sc1\n\t"
      "global_load_dwordx4 v[156:159], v183, s[64:65] sc1\n\t"
      "s_waitcnt vmcnt(0)\n\t"
      "v_max3_u32 v234, v128, v129, v130\n\t"
      "v_max3_u32 v234, v234, v131, v132\n\t"
      "v_max3_u32 v234, v234, v133, v134\n\t"
      "v_max3_u32 v234, v234, v135, v136\n\t"
      "v_max3_u32 v234, v234, v137, v138\n\t"
      "v_max3_u32 v234, v234, v139, v140\n\t"
      "v_max3_u32 v234, v234, v141, v142\n\t"
      "v_max3_u32 v234, v234, v143, v144\n\t"
      "v_max3_u32 v234, v234, v145, v146\n\t"
      "v_max3_u32 v234, v234, v147, v148\n\t"
      "v_max3_u32 v234, v234, v149, v150\n\t"
      "v_max3_u32 v234, v234, v151, v152\n\t"
      "v_max3_u32 v234, v234, v153, v154\n\t"
      "v_max3_u32 v234, v234, v155, v156\n\t"
      "v_max3_u32 v234, v234, v157, v158\n\t"
      "v_max_u32_e32 v234, v234, v159\n\t"
      "v_cmp_eq_u32_e32 vcc, -1, v234\n\t"
      "s_cbranch_vccnz NQ6L_%=\n\t"
      "NQ6X_%=:\n\t"
      "v_mfma_f32_16x16x32_bf16 v[184:187], v[0:3], v[128:131], v[236:239]\n\t"
      "v_mfma_f32_16x16x32_bf16 v[188:191], v[4:7], v[128:131], 0\n\t"
      "v_mfma_f32_16x16x32_bf16 v[192:195], v[8:11], v[128:131], 0\n\t"
      "v_mfma_f32_16x16x32_bf16 v[196:199], v[12:15], v[128:131], 0\n\t"
      "v_mfma_f32_16x16x32_bf16 v[184:187], v[16:19], v[132:135], v[184:187]\n\t"
      "v_mfma_f32_16x16x32_bf16 v[188:191], v[20:23], v[132:135], v[188:191]\n\t"
      "v_mfma_f32_16x16x32_bf16 v[192:195], v[24:27], v[132:135], v[192:195]\n\t"
      "v_mfma_f32_16x16x32_bf16 v[196:199], v[28:31], v[132:135], v[196:199]\n\t"
      "v_mfma_f32_16x16x32_bf16 v[184:187], v[32:35], v[136:139], v[184:187]\n\t"
      "v_mfma_f32_16x16x32_bf16 v[188:191], v[36:39], v[136:139], v[188:191]\n\t"
      "v_mfma_f32_16x16x32_bf16 v[192:195], v[40:43], v[136:139], v[192:195]\n\t"
      "v_mfma_f32_16x16x32_bf16 v[196:199], v[44:47], v[136:139], v[196:199]\n\t"
      "v_mfma_f32_16x16x32_bf16 v[184:187], v[48:51], v[140:143], v[184:187]\n\t"
      "v_mfma_f32_16x16x32_bf16 v[188:191], v[52:55], v[140:143], v[188:191]\n\t"
      "v_mfma_f32_16x16x32_bf16 v[192:195], v[56:59], v[140:143], v[192:195]\n\t"
      "v_mfma_f32_16x16x32_bf16 v[196:199], v[60:63], v[140:143], v[196:199]\n\t"
      "v_mfma_f32_16x16x32_bf16 v[184:187], v[64:67], v[144:147], v[184:187]\n\t"
      "v_mfma_f32_16x16x32_bf16 v[188:191], v[68:71], v[144:147], v[188:191]\n\t"
      "v_mfma_f32_16x16x32_bf16 v[192:195], v[72:75], v[144:147], v[192:195]\n\t"
      "v_mfma_f32_16x16x32_bf16 v[196:199], v[76:79], v[144:147], v[196:199]\n\t"
      "v_mfma_f32_16x16x32_bf16 v[184:187], v[80:83], v[148:151], v[184:187]\n\t"
      "v_mfma_f32_16x16x32_bf16 v[188:191], v[84:87], v[148:151], v[188:191]\n\t"
      "v_mfma_f32_16x16x32_bf16 v[192:195], v[88:91], v[148:151], v[192:195]\n\t"
      "v_mfma_f32_16x16x32_bf16 v[196:199], v[92:95], v[148:151], v[196:199]\n\t"
      "v_mfma_f32_16x16x32_bf16 v[184:187], v[96:99], v[152:155], v[184:187]\n\t"
      "v_mfma_f32_16x16x32_bf16 v[188:191], v[100:103], v[152:155], v[188:191]\n\t"
      "v_mfma_f32_16x16x32_bf16 v[192:195], v[104:107], v[152:155], v[192:195]\n\t"
      "v_mfma_f32_16x16x32_bf16 v[196:199], v[108:111], v[152:155], v[196:199]\n\t"
      "v_mfma_f32_16x16x32_bf16 v[184:187], v[112:115], v[156:159], v[184:187]\n\t"
      "v_mfma_f32_16x16x32_bf16 v[188:191], v[116:119], v[156:159], v[188:191]\n\t"
      "v_mfma_f32_16x16x32_bf16 v[192:195], v[120:123], v[156:159], v[192:195]\n\t"
      "v_mfma_f32_16x16x32_bf16 v[196:199], v[124:127], v[156:159], v[196:199]\n\t"
      "s_waitcnt vmcnt(0)\n\t"
      "s_nop 7\n\t"
      "s_branch NQ6B_%=\n\t"
      "FP0R_%=:\n\t"
      "s_mov_b32 s51, 0\n\t"
      "FP0L_%=:\n\t"
      "s_add_u32 s51, s51, 1\n\t"
      "s_cmp_lt_u32 s51, 262144\n\t"
      "s_cbranch_scc0 FP0X_%=\n\t"
      "s_waitcnt vmcnt(0)\n\t"
      "s_sleep 1\n\t"
      "global_load_dwordx4 v[128:131], v176, s[52:53] sc1\n\t"
      "global_load_dwordx4 v[132:135], v177, s[52:53] sc1\n\t"
      "global_load_dwordx4 v[136:139], v178, s[52:53] sc1\n\t"
      "global_load_dwordx4 v[140:143], v179, s[52:53] sc1\n\t"
      "global_load_dwordx4 v[144:147], v180, s[52:53] sc1\n\t"
      "global_load_dwordx4 v[148:151], v181, s[52:53] sc1\n\t"
      "global_load_dwordx4 v[152:155], v182, s[52:53] sc1\n\t"
      "global_load_dwordx4 v[156:159], v183, s[52:53] sc1\n\t"
      "s_waitcnt vmcnt(7)\n\t"
      "v_max3_u32 v234, v128, v129, v130\n\t"
      "v_max_u32_e32 v234, v234, v131\n\t"
      "v_cmp_eq_u32_e32 vcc, -1, v234\n\t"
      "s_cbranch_vccnz FP0L_%=\n\t"
      "FP0X_%=:\n\t"
      "s_branch FP0B_%=\n\t"
      "FP1R_%=:\n\t"
      "s_mov_b32 s51, 0\n\t"
      "FP1L_%=:\n\t"
      "s_add_u32 s51, s51, 1\n\t"
      "s_cmp_lt_u32 s51, 262144\n\t"
      "s_cbranch_scc0 FP1X_%=\n\t"
      "s_waitcnt vmcnt(0)\n\t"
      "s_sleep 1\n\t"
      "global_load_dwordx4 v[132:135], v177, s[52:53] sc1\n\t"
      "global_load_dwordx4 v[136:139], v178, s[52:53] sc1\n\t"
      "global_load_dwordx4 v[140:143], v179, s[52:53] sc1\n\t"
      "global_load_dwordx4 v[144:147], v180, s[52:53] sc1\n\t"
      "global_load_dwordx4 v[148:151], v181, s[52:53] sc1\n\t"
      "global_load_dwordx4 v[152:155], v182, s[52:53] sc1\n\t"
      "global_load_dwordx4 v[156:159], v183, s[52:53] sc1\n\t"
      "global_load_dwordx4 v[128:131], v176, s[54:55] sc1\n\t"
      "s_waitcnt vmcnt(7)\n\t"
      "v_max3_u32 v234, v132, v133, v134\n\t"
      "v_max_u32_e32 v234, v234, v135\n\t"
      "v_cmp_eq_u32_e32 vcc, -1, v234\n\t"
      "s_cbranch_vccnz FP1L_%=\n\t"
      "FP1X_%=:\n\t"
      "s_branch FP1B_%=\n\t"
      "FP2R_%=:\n\t"
      "s_mov_b32 s51, 0\n\t"
      "FP2L_%=:\n\t"
      "s_add_u32 s51, s51, 1\n\t"
      "s_cmp_lt_u32 s51, 262144\n\t"
      "s_cbranch_scc0 FP2X_%=\n\t"
      "s_waitcnt vmcnt(0)\n\t"
      "s_sleep 1\n\t"
      "global_load_dwordx4 v[136:139], v178, s[52:53] sc1\n\t"
      "global_load_dwordx4 v[140:143], v179, s[52:53] sc1\n\t"
      "global_load_dwordx4 v[144:147], v180, s[52:53] sc1\n\t"
      "global_load_dwordx4 v[148:151], v181, s[52:53] sc1\n\t"
      "global_load_dwordx4 v[152:155], v182, s[52:53] sc1\n\t"
      "global_load_dwordx4 v[156:159], v183, s[52:53] sc1\n\t"
      "global_load_dwordx4 v[128:131], v176, s[54:55] sc1\n\t"
      "global_load_dwordx4 v[132:135], v177, s[54:55] sc1\n\t"
      "s_waitcnt vmcnt(7)\n\t"
      "v_max3_u32 v234, v136, v137, v138\n\t"
      "v_max_u32_e32 v234, v234, v139\n\t"
      "v_cmp_eq_u32_e32 vcc, -1, v234\n\t"
      "s_cbranch_vccnz FP2L_%=\n\t"
      "FP2X_%=:\n\t"
      "s_branch FP2B_%=\n\t"
      "FP3R_%=:\n\t"
      "s_mov_b32 s51, 0\n\t"
      "FP3L_%=:\n\t"
      "s_add_u32 s51, s51, 1\n\t"
      "s_cmp_lt_u32 s51, 262144\n\t"
      "s_cbranch_scc0 FP3X_%=\n\t"
      "s_waitcnt vmcnt(0)\n\t"
      "s_sleep 1\n\t"
      "global_load_dwordx4 v[140:143], v179, s[52:53] sc1\n\t"
      "global_load_dwordx4 v[144:147], v180, s[52:53] sc1\n\t"
      "global_load_dwordx4 v[148:151], v181, s[52:53] sc1\n\t"
      "global_load_dwordx4 v[152:155], v182, s[52:53] sc1\n\t"
      "global_load_dwordx4 v[156:159], v183, s[52:53] sc1\n\t"
      "global_load_dwordx4 v[128:131], v176, s[54:55] sc1\n\t"
      "global_load_dwordx4 v[132:135], v177, s[54:55] sc1\n\t"
      "global_load_dwordx4 v[136:139], v178, s[54:55] sc1\n\t"
      "s_waitcnt vmcnt(7)\n\t"
      "v_max3_u32 v234, v140, v141, v142\n\t"
      "v_max_u32_e32 v234, v234, v143\n\t"
      "v_cmp_eq_u32_e32 vcc, -1, v234\n\t"
      "s_cbranch_vccnz FP3L_%=\n\t"
      "FP3X_%=:\n\t"
      "s_branch FP3B_%=\n\t"
      "FP4R_%=:\n\t"
      "s_mov_b32 s51, 0\n\t"
      "FP4L_%=:\n\t"
      "s_add_u32 s51, s51, 1\n\t"
      "s_cmp_lt_u32 s51, 262144\n\t"
      "s_cbranch_scc0 FP4X_%=\n\t"
      "s_waitcnt vmcnt(0)\n\t"
      "s_sleep 1\n\t"
      "global_load_dwordx4 v[144:147], v180, s[52:53] sc1\n\t"
      "global_load_dwordx4 v[148:151], v181, s[52:53] sc1\n\t"
      "global_load_dwordx4 v[152:155], v182, s[52:53] sc1\n\t"
      "global_load_dwordx4 v[156:159], v183, s[52:53] sc1\n\t"
      "global_load_dwordx4 v[128:131], v176, s[54:55] sc1\n\t"
      "global_load_dwordx4 v[132:135], v177, s[54:55] sc1\n\t"
      "global_load_dwordx4 v[136:139], v178, s[54:55] sc1\n\t"
      "global_load_dwordx4 v[140:143], v179, s[54:55] sc1\n\t"
      "s_waitcnt vmcnt(7)\n\t"
      "v_max3_u32 v234, v144, v145, v146\n\t"
      "v_max_u32_e32 v234, v234, v147\n\t"
      "v_cmp_eq_u32_e32 vcc, -1, v234\n\t"
      "s_cbranch_vccnz FP4L_%=\n\t"
      "FP4X_%=:\n\t"
      "s_branch FP4B_%=\n\t"
      "FP5R_%=:\n\t"
      "s_mov_b32 s51, 0\n\t"
      "FP5L_%=:\n\t"
      "s_add_u32 s51, s51, 1\n\t"
      "s_cmp_lt_u32 s51, 262144\n\t"
      "s_cbranch_scc0 FP5X_%=\n\t"
      "s_waitcnt vmcnt(0)\n\t"
      "s_sleep 1\n\t"
      "global_load_dwordx4 v[148:151], v181, s[52:53] sc1\n\t"
      "global_load_dwordx4 v[152:155], v182, s[52:53] sc1\n\t"
      "global_load_dwordx4 v[156:159], v183, s[52:53] sc1\n\t"
      "global_load_dwordx4 v[128:131], v176, s[54:55] sc1\n\t"
      "global_load_dwordx4 v[132:135], v177, s[54:55] sc1\n\t"
      "global_load_dwordx4 v[136:139], v178, s[54:55] sc1\n\t"
      "global_load_dwordx4 v[140:143], v179, s[54:55] sc1\n\t"
      "global_load_dwordx4 v[144:147], v180, s[54:55] sc1\n\t"
      "s_waitcnt vmcnt(7)\n\t"
      "v_max3_u32 v234, v148, v149, v150\n\t"
      "v_max_u32_e32 v234, v234, v151\n\t"
      "v_cmp_eq_u32_e32 vcc, -1, v234\n\t"
      "s_cbranch_vccnz FP5L_%=\n\t"
      "FP5X_%=:\n\t"
      "s_branch FP5B_%=\n\t"
      "FP6R_%=:\n\t"
      "s_mov_b32 s51, 0\n\t"
      "FP6L_%=:\n\t"
      "s_add_u32 s51, s51, 1\n\t"
      "s_cmp_lt_u32 s51, 262144\n\t"
      "s_cbranch_scc0 FP6X_%=\n\t"
      "s_waitcnt vmcnt(0)\n\t"
      "s_sleep 1\n\t"
      "global_load_dwordx4 v[152:155], v182, s[52:53] sc1\n\t"
      "global_load_dwordx4 v[156:159], v183, s[52:53] sc1\n\t"
      "global_load_dwordx4 v[128:131], v176, s[54:55] sc1\n\t"
      "global_load_dwordx4 v[132:135], v177, s[54:55] sc1\n\t"
      "global_load_dwordx4 v[136:139], v178, s[54:55] sc1\n\t"
      "global_load_dwordx4 v[140:143], v179, s[54:55] sc1\n\t"
      "global_load_dwordx4 v[144:147], v180, s[54:55] sc1\n\t"
      "global_load_dwordx4 v[148:151], v181, s[54:55] sc1\n\t"
      "s_waitcnt vmcnt(7)\n\t"
      "v_max3_u32 v234, v152, v153, v154\n\t"
      "v_max_u32_e32 v234, v234, v155\n\t"
      "v_cmp_eq_u32_e32 vcc, -1, v234\n\t"
      "s_cbranch_vccnz FP6L_%=\n\t"
      "FP6X_%=:\n\t"
      "s_branch FP6B_%=\n\t"
      "FP7R_%=:\n\t"
      "s_mov_b32 s51, 0\n\t"
      "FP7L_%=:\n\t"
      "s_add_u32 s51, s51, 1\n\t"
      "s_cmp_lt_u32 s51, 262144\n\t"
      "s_cbranch_scc0 FP7X_%=\n\t"
      "s_waitcnt vmcnt(0)\n\t"
      "s_sleep 1\n\t"
      "global_load_dwordx4 v[156:159], v183, s[52:53] sc1\n\t"
      "global_load_dwordx4 v[128:131], v176, s[54:55] sc1\n\t"
      "global_load_dwordx4 v[132:135], v177, s[54:55] sc1\n\t"
      "global_load_dwordx4 v[136:139], v178, s[54:55] sc1\n\t"
      "global_load_dwordx4 v[140:143], v179, s[54:55] sc1\n\t"
      "global_load_dwordx4 v[144:147], v180, s[54:55] sc1\n\t"
      "global_load_dwordx4 v[148:151], v181, s[54:55] sc1\n\t"
      "s_waitcnt vmcnt(6)\n\t"
      "v_max3_u32 v234, v156, v157, v158\n\t"
      "v_max_u32_e32 v234, v234, v159\n\t"
      "v_cmp_eq_u32_e32 vcc, -1, v234\n\t"
      "s_cbranch_vccnz FP7L_%=\n\t"
      "FP7X_%=:\n\t"
      "s_branch FP7B_%=\n\t"
      "NP1R_%=:\n\t"
      "s_mov_b32 s51, 0\n\t"
      "NP1L_%=:\n\t"
      "s_add_u32 s51, s51, 1\n\t"
      "s_cmp_lt_u32 s51, 262144\n\t"
      "s_cbranch_scc0 NP1X_%=\n\t"
      "s_waitcnt vmcnt(0)\n\t"
      "s_sleep 1\n\t"
      "global_load_dwordx4 v[128:131], v176, s[54:55] sc1\n\t"
      "global_load_dwordx4 v[132:135], v177, s[54:55] sc1\n\t"
      "global_load_dwordx4 v[136:139], v178, s[54:55] sc1\n\t"
      "global_load_dwordx4 v[140:143], v179, s[54:55] sc1\n\t"
      "global_load_dwordx4 v[144:147], v180, s[54:55] sc1\n\t"
      "global_load_dwordx4 v[148:151], v181, s[54:55] sc1\n\t"
      "global_load_dwordx4 v[152:155], v182, s[54:55] sc1\n\t"
      "global_load_dwordx4 v[156:159], v183, s[54:55] sc1\n\t"
      "s_waitcnt vmcnt(0)\n\t"
      "v_max3_u32 v234, v128, v129, v130\n\t"
      "v_max3_u32 v234, v234, v131, v132\n\t"
      "v_max3_u32 v234, v234, v133, v134\n\t"
      "v_max3_u32 v234, v234, v135, v136\n\t"
      "v_max3_u32 v234, v234, v137, v138\n\t"
      "v_max3_u32 v234, v234, v139, v140\n\t"
      "v_max3_u32 v234, v234, v141, v142\n\t"
      "v_max3_u32 v234, v234, v143, v144\n\t"
      "v_max3_u32 v234, v234, v145, v146\n\t"
      "v_max3_u32 v234, v234, v147, v148\n\t"
      "v_max3_u32 v234, v234, v149, v150\n\t"
      "v_max3_u32 v234, v234, v151, v152\n\t"
      "v_max3_u32 v234, v234, v153, v154\n\t"
      "v_max3_u32 v234, v234, v155, v156\n\t"
      "v_max3_u32 v234, v234, v157, v158\n\t"
      "v_max_u32_e32 v234, v234, v159\n\t"
      "v_cmp_eq_u32_e32 vcc, -1, v234\n\t"
      "s_cbranch_vccnz NP1L_%=\n\t"
      "NP1X_%=:\n\t"
      "v_mfma_f32_16x16x32_bf16 v[200:203], v[0:3], v[128:131], 0\n\t"
      "v_mfma_f32_16x16x32_bf16 v[204:207], v[4:7], v[128:131], 0\n\t"
      "v_mfma_f32_16x16x32_bf16 v[208:211], v[8:11], v[128:131], 0\n\t"
      "v_mfma_f32_16x16x32_bf16 v[212:215], v[12:15], v[128:131], 0\n\t"
      "v_mfma_f32_16x16x32_bf16 v[200:203], v[16:19], v[132:135], v[200:203]\n\t"
      "v_mfma_f32_16x16x32_bf16 v[204:207], v[20:23], v[132:135], v[204:207]\n\t"
      "v_mfma_f32_16x16x32_bf16 v[208:211], v[24:27], v[132:135], v[208:211]\n\t"
      "v_mfma_f32_16x16x32_bf16 v[212:215], v[28:31], v[132:135], v[212:215]\n\t"
      "v_mfma_f32_16x16x32_bf16 v[200:203], v[32:35], v[136:139], v[200:203]\n\t"
      "v_mfma_f32_16x16x32_bf16 v[204:207], v[36:39], v[136:139], v[204:207]\n\t"
      "v_mfma_f32_16x16x32_bf16 v[208:211], v[40:43], v[136:139], v[208:211]\n\t"
      "v_mfma_f32_16x16x32_bf16 v[212:215], v[44:47], v[136:139], v[212:215]\n\t"
      "v_mfma_f32_16x16x32_bf16 v[200:203], v[48:51], v[140:143], v[200:203]\n\t"
      "v_mfma_f32_16x16x32_bf16 v[204:207], v[52:55], v[140:143], v[204:207]\n\t"
      "v_mfma_f32_16x16x32_bf16 v[208:211], v[56:59], v[140:143], v[208:211]\n\t"
      "v_mfma_f32_16x16x32_bf16 v[212:215], v[60:63], v[140:143], v[212:215]\n\t"
      "v_mfma_f32_16x16x32_bf16 v[200:203], v[64:67], v[144:147], v[200:203]\n\t"
      "v_mfma_f32_16x16x32_bf16 v[204:207], v[68:71], v[144:147], v[204:207]\n\t"
      "v_mfma_f32_16x16x32_bf16 v[208:211], v[72:75], v[144:147], v[208:211]\n\t"
      "v_mfma_f32_16x16x32_bf16 v[212:215], v[76:79], v[144:147], v[212:215]\n\t"
      "v_mfma_f32_16x16x32_bf16 v[200:203], v[80:83], v[148:151], v[200:203]\n\t"
      "v_mfma_f32_16x16x32_bf16 v[204:207], v[84:87], v[148:151], v[204:207]\n\t"
      "v_mfma_f32_16x16x32_bf16 v[208:211], v[88:91], v[148:151], v[208:211]\n\t"
      "v_mfma_f32_16x16x32_bf16 v[212:215], v[92:95], v[148:151], v[212:215]\n\t"
      "v_mfma_f32_16x16x32_bf16 v[200:203], v[96:99], v[152:155], v[200:203]\n\t"
      "v_mfma_f32_16x16x32_bf16 v[204:207], v[100:103], v[152:155], v[204:207]\n\t"
      "v_mfma_f32_16x16x32_bf16 v[208:211], v[104:107], v[152:155], v[208:211]\n\t"
      "v_mfma_f32_16x16x32_bf16 v[200:203], v[112:115], v[156:159], v[200:203]\n\t"
      "v_mfma_f32_16x16x32_bf16 v[204:207], v[116:119], v[156:159], v[204:207]\n\t"
      "v_mfma_f32_16x16x32_bf16 v[208:211], v[120:123], v[156:159], v[208:211]\n\t"
      "global_load_dwordx4 v[128:131], v176, s[56:57] sc1\n\t"
      "global_load_dwordx4 v[132:135], v177, s[56:57] sc1\n\t"
      "global_load_dwordx4 v[136:139], v178, s[56:57] sc1\n\t"
      "global_load_dwordx4 v[140:143], v179, s[56:57] sc1\n\t"
      "global_load_dwordx4 v[144:147], v180, s[56:57] sc1\n\t"
      "global_load_dwordx4 v[148:151], v181, s[56:57] sc1\n\t"
      "s_waitcnt vmcnt(0)\n\t"
      "s_nop 7\n\t"
      "s_branch NP1B_%=\n\t"
      "NP2R_%=:\n\t"
      "s_mov_b32 s51, 0\n\t"
      "NP2L_%=:\n\t"
      "s_add_u32 s51, s51, 1\n\t"
      "s_cmp_lt_u32 s51, 262144\n\t"
      "s_cbranch_scc0 NP2X_%=\n\t"
      "s_waitcnt vmcnt(0)\n\t"
      "s_sleep 1\n\t"
      "global_load_dwordx4 v[128:131], v176, s[56:57] sc1\n\t"
      "global_load_dwordx4 v[132:135], v177, s[56:57] sc1\n\t"
      "global_load_dwordx4 v[136:139], v178, s[56:57] sc1\n\t"
      "global_load_dwordx4 v[140:143], v179, s[56:57] sc1\n\t"
      "global_load_dwordx4 v[144:147], v180, s[56:57] sc1\n\t"
      "global_load_dwordx4 v[148:151], v181, s[56:57] sc1\n\t"
      "global_load_dwordx4 v[152:155], v182, s[56:57] sc1\n\t"
      "global_load_dwordx4 v[156:159], v183, s[56:57] sc1\n\t"
      "s_waitcnt vmcnt(0)\n\t"
      "v_max3_u32 v234, v128, v129, v130\n\t"
      "v_max3_u32 v234, v234, v131, v132\n\t"
      "v_max3_u32 v234, v234, v133, v134\n\t"
      "v_max3_u32 v234, v234, v135, v136\n\t"
      "v_max3_u32 v234, v234, v137, v138\n\t"
      "v_max3_u32 v234, v234, v139, v140\n\t"
      "v_max3_u32 v234, v234, v141, v142\n\t"
      "v_max3_u32 v234, v234, v143, v144\n\t"
      "v_max3_u32 v234, v234, v145, v146\n\t"
      "v_max3_u32 v234, v234, v147, v148\n\t"
      "v_max3_u32 v234, v234, v149, v150\n\t"
      "v_max3_u32 v234, v234, v151, v152\n\t"
      "v_max3_u32 v234, v234, v153, v154\n\t"
      "v_max3_u32 v234, v234, v155, v156\n\t"
      "v_max3_u32 v234, v234, v157, v158\n\t"
      "v_max_u32_e32 v234, v234, v159\n\t"
      "v_cmp_eq_u32_e32 vcc, -1, v234\n\t"
      "s_cbranch_vccnz NP2L_%=\n\t"
      "NP2X_%=:\n\t"
      "v_mfma_f32_16x16x32_bf16 v[184:187], v[0:3], v[128:131], 0\n\t"
      "v_mfma_f32_16x16x32_bf16 v[188:191], v[4:7], v[128:131], 0\n\t"
      "v_mfma_f32_16x16x32_bf16 v[192:195], v[8:11], v[128:131], 0\n\t"
      "v_mfma_f32_16x16x32_bf16 v[196:199], v[12:15], v[128:131], 0\n\t"
      "v_mfma_f32_16x16x32_bf16 v[184:187], v[16:19], v[132:135], v[184:187]\n\t"
      "v_mfma_f32_16x16x32_bf16 v[188:191], v[20:23], v[132:135], v[188:191]\n\t"
      "v_mfma_f32_16x16x32_bf16 v[192:195], v[24:27], v[132:135], v[192:195]\n\t"
      "v_mfma_f32_16x16x32_bf16 v[196:199], v[28:31], v[132:135], v[196:199]\n\t"
      "v_mfma_f32_16x16x32_bf16 v[184:187], v[32:35], v[136:139], v[184:187]\n\t"
      "v_mfma_f32_16x16x32_bf16 v[188:191], v[36:39], v[136:139], v[188:191]\n\t"
      "v_mfma_f32_16x16x32_bf16 v[192:195], v[40:43], v[136:139], v[192:195]\n\t"
      "v_mfma_f32_16x16x32_bf16 v[196:199], v[44:47], v[136:139], v[196:199]\n\t"
      "v_mfma_f32_16x16x32_bf16 v[184:187], v[48:51], v[140:143], v[184:187]\n\t"
      "v_mfma_f32_16x16x32_bf16 v[188:191], v[52:55], v[140:143], v[188:191]\n\t"
      "v_mfma_f32_16x16x32_bf16 v[192:195], v[56:59], v[140:143], v[192:195]\n\t"
      "v_mfma_f32_16x16x32_bf16 v[196:199], v[60:63], v[140:143], v[196:199]\n\t"
      "v_mfma_f32_16x16x32_bf16 v[184:187], v[64:67], v[144:147], v[184:187]\n\t"
      "v_mfma_f32_16x16x32_bf16 v[188:191], v[68:71], v[144:147], v[188:191]\n\t"
      "v_mfma_f32_16x16x32_bf16 v[192:195], v[72:75], v[144:147], v[192:195]\n\t"
      "v_mfma_f32_16x16x32_bf16 v[196:199], v[76:79], v[144:147], v[196:199]\n\t"
      "v_mfma_f32_16x16x32_bf16 v[184:187], v[80:83], v[148:151], v[184:187]\n\t"
      "v_mfma_f32_16x16x32_bf16 v[188:191], v[84:87], v[148:151], v[188:191]\n\t"
      "v_mfma_f32_16x16x32_bf16 v[192:195], v[88:91], v[148:151], v[192:195]\n\t"
      "v_mfma_f32_16x16x32_bf16 v[196:199], v[92:95], v[148:151], v[196:199]\n\t"
      "v_mfma_f32_16x16x32_bf16 v[184:187], v[96:99], v[152:155], v[184:187]\n\t"
      "v_mfma_f32_16x16x32_bf16 v[188:191], v[100:103], v[152:155], v[188:191]\n\t"
      "v_mfma_f32_16x16x32_bf16 v[192:195], v[104:107], v[152:155], v[192:195]\n\t"
      "v_mfma_f32_16x16x32_bf16 v[184:187], v[112:115], v[156:159], v[184:187]\n\t"
      "v_mfma_f32_16x16x32_bf16 v[188:191], v[116:119], v[156:159], v[188:191]\n\t"
      "v_mfma_f32_16x16x32_bf16 v[192:195], v[120:123], v[156:159], v[192:195]\n\t"
      "global_load_dwordx4 v[128:131], v176, s[58:59] sc1\n\t"
      "global_load_dwordx4 v[132:135], v177, s[58:59] sc1\n\t"
      "global_load_dwordx4 v[136:139], v178, s[58:59] sc1\n\t"
      "global_load_dwordx4 v[140:143], v179, s[58:59] sc1\n\t"
      "global_load_dwordx4 v[144:147], v180, s[58:59] sc1\n\t"
      "global_load_dwordx4 v[148:151], v181, s[58:59] sc1\n\t"
      "s_waitcnt vmcnt(0)\n\t"
      "s_nop 7\n\t"
      "s_branch NP2B_%=\n\t"
      "NP3R_%=:\n\t"
      "s_mov_b32 s51, 0\n\t"
      "NP3L_%=:\n\t"
      "s_add_u32 s51, s51, 1\n\t"
      "s_cmp_lt_u32 s51, 262144\n\t"
      "s_cbranch_scc0 NP3X_%=\n\t"
      "s_waitcnt vmcnt(0)\n\t"
      "s_sleep 1\n\t"
      "global_load_dwordx4 v[128:131], v176, s[58:59] sc1\n\t"
      "global_load_dwordx4 v[132:135], v177, s[58:59] sc1\n\t"
      "global_load_dwordx4 v[136:139], v178, s[58:59] sc1\n\t"
      "global_load_dwordx4 v[140:143], v179, s[58:59] sc1\n\t"
      "global_load_dwordx4 v[144:147], v180, s[58:59] sc1\n\t"
      "global_load_dwordx4 v[148:151], v181, s[58:59] sc1\n\t"
      "global_load_dwordx4 v[152:155], v182, s[58:59] sc1\n\t"
      "global_load_dwordx4 v[156:159], v183, s[58:59] sc1\n\t"
      "s_waitcnt vmcnt(0)\n\t"
      "v_max3_u32 v234, v128, v129, v130\n\t"
      "v_max3_u32 v234, v234, v131, v132\n\t"
      "v_max3_u32 v234, v234, v133, v134\n\t"
      "v_max3_u32 v234, v234, v135, v136\n\t"
      "v_max3_u32 v234, v234, v137, v138\n\t"
      "v_max3_u32 v234, v234, v139, v140\n\t"
      "v_max3_u32 v234, v234, v141, v142\n\t"
      "v_max3_u32 v234, v234, v143, v144\n\t"
      "v_max3_u32 v234, v234, v145, v146\n\t"
      "v_max3_u32 v234, v234, v147, v148\n\t"
      "v_max3_u32 v234, v234, v149, v150\n\t"
      "v_max3_u32 v234, v234, v151, v152\n\t"
      "v_max3_u32 v234, v234, v153, v154\n\t"
      "v_max3_u32 v234, v234, v155, v156\n\t"
      "v_max3_u32 v234, v234, v157, v158\n\t"
      "v_max_u32_e32 v234, v234, v159\n\t"
      "v_cmp_eq_u32_e32 vcc, -1, v234\n\t"
      "s_cbranch_vccnz NP3L_%=\n\t"
      "NP3X_%=:\n\t"
      "v_mfma_f32_16x16x32_bf16 v[200:203], v[0:3], v[128:131], 0\n\t"
      "v_mfma_f32_16x16x32_bf16 v[204:207], v[4:7], v[128:131], 0\n\t"
      "v_mfma_f32_16x16x32_bf16 v[208:211], v[8:11], v[128:131], 0\n\t"
      "v_mfma_f32_16x16x32_bf16 v[212:215], v[12:15], v[128:131], 0\n\t"
      "v_mfma_f32_16x16x32_bf16 v[200:203], v[16:19], v[132:135], v[200:203]\n\t"
      "v_mfma_f32_16x16x32_bf16 v[204:207], v[20:23], v[132:135], v[204:207]\n\t"
      "v_mfma_f32_16x16x32_bf16 v[208:211], v[24:27], v[132:135], v[208:211]\n\t"
      "v_mfma_f32_16x16x32_bf16 v[212:215], v[28:31], v[132:135], v[212:215]\n\t"
      "v_mfma_f32_16x16x32_bf16 v[200:203], v[32:35], v[136:139], v[200:203]\n\t"
      "v_mfma_f32_16x16x32_bf16 v[204:207], v[36:39], v[136:139], v[204:207]\n\t"
      "v_mfma_f32_16x16x32_bf16 v[208:211], v[40:43], v[136:139], v[208:211]\n\t"
      "v_mfma_f32_16x16x32_bf16 v[212:215], v[44:47], v[136:139], v[212:215]\n\t"
      "v_mfma_f32_16x16x32_bf16 v[200:203], v[48:51], v[140:143], v[200:203]\n\t"
      "v_mfma_f32_16x16x32_bf16 v[204:207], v[52:55], v[140:143], v[204:207]\n\t"
      "v_mfma_f32_16x16x32_bf16 v[208:211], v[56:59], v[140:143], v[208:211]\n\t"
      "v_mfma_f32_16x16x32_bf16 v[212:215], v[60:63], v[140:143], v[212:215]\n\t"
      "v_mfma_f32_16x16x32_bf16 v[200:203], v[64:67], v[144:147], v[200:203]\n\t"
      "v_mfma_f32_16x16x32_bf16 v[204:207], v[68:71], v[144:147], v[204:207]\n\t"
      "v_mfma_f32_16x16x32_bf16 v[208:211], v[72:75], v[144:147], v[208:211]\n\t"
      "v_mfma_f32_16x16x32_bf16 v[212:215], v[76:79], v[144:147], v[212:215]\n\t"
      "v_mfma_f32_16x16x32_bf16 v[200:203], v[80:83], v[148:151], v[200:203]\n\t"
      "v_mfma_f32_16x16x32_bf16 v[204:207], v[84:87], v[148:151], v[204:207]\n\t"
      "v_mfma_f32_16x16x32_bf16 v[208:211], v[88:91], v[148:151], v[208:211]\n\t"
      "v_mfma_f32_16x16x32_bf16 v[212:215], v[92:95], v[148:151], v[212:215]\n\t"
      "v_mfma_f32_16x16x32_bf16 v[200:203], v[96:99], v[152:155], v[200:203]\n\t"
      "v_mfma_f32_16x16x32_bf16 v[204:207], v[100:103], v[152:155], v[204:207]\n\t"
      "v_mfma_f32_16x16x32_bf16 v[208:211], v[104:107], v[152:155], v[208:211]\n\t"
      "v_mfma_f32_16x16x32_bf16 v[200:203], v[112:115], v[156:159], v[200:203]\n\t"
      "v_mfma_f32_16x16x32_bf16 v[204:207], v[116:119], v[156:159], v[204:207]\n\t"
      "v_mfma_f32_16x16x32_bf16 v[208:211], v[120:123], v[156:159], v[208:211]\n\t"
      "global_load_dwordx4 v[128:131], v176, s[60:61] sc1\n\t"
      "global_load_dwordx4 v[132:135], v177, s[60:61] sc1\n\t"
      "global_load_dwordx4 v[136:139], v178, s[60:61] sc1\n\t"
      "global_load_dwordx4 v[140:143], v179, s[60:61] sc1\n\t"
      "global_load_dwordx4 v[144:147], v180, s[60:61] sc1\n\t"
      "global_load_dwordx4 v[148:151], v181, s[60:61] sc1\n\t"
      "s_waitcnt vmcnt(0)\n\t"
      "s_nop 7\n\t"
      "s_branch NP3B_%=\n\t"
      "NP4R_%=:\n\t"
      "s_mov_b32 s51, 0\n\t"
      "NP4L_%=:\n\t"
      "s_add_u32 s51, s51, 1\n\t"
      "s_cmp_lt_u32 s51, 262144\n\t"
      "s_cbranch_scc0 NP4X_%=\n\t"
      "s_waitcnt vmcnt(0)\n\t"
      "s_sleep 1\n\t"
      "global_load_dwordx4 v[128:131], v176, s[60:61] sc1\n\t"
      "global_load_dwordx4 v[132:135], v177, s[60:61] sc1\n\t"
      "global_load_dwordx4 v[136:139], v178, s[60:61] sc1\n\t"
      "global_load_dwordx4 v[140:143], v179, s[60:61] sc1\n\t"
      "global_load_dwordx4 v[144:147], v180, s[60:61] sc1\n\t"
      "global_load_dwordx4 v[148:151], v181, s[60:61] sc1\n\t"
      "global_load_dwordx4 v[152:155], v182, s[60:61] sc1\n\t"
      "global_load_dwordx4 v[156:159], v183, s[60:61] sc1\n\t"
      "s_waitcnt vmcnt(0)\n\t"
      "v_max3_u32 v234, v128, v129, v130\n\t"
      "v_max3_u32 v234, v234, v131, v132\n\t"
      "v_max3_u32 v234, v234, v133, v134\n\t"
      "v_max3_u32 v234, v234, v135, v136\n\t"
      "v_max3_u32 v234, v234, v137, v138\n\t"
      "v_max3_u32 v234, v234, v139, v140\n\t"
      "v_max3_u32 v234, v234, v141, v142\n\t"
      "v_max3_u32 v234, v234, v143, v144\n\t"
      "v_max3_u32 v234, v234, v145, v146\n\t"
      "v_max3_u32 v234, v234, v147, v148\n\t"
      "v_max3_u32 v234, v234, v149, v150\n\t"
      "v_max3_u32 v234, v234, v151, v152\n\t"
      "v_max3_u32 v234, v234, v153, v154\n\t"
      "v_max3_u32 v234, v234, v155, v156\n\t"
      "v_max3_u32 v234, v234, v157, v158\n\t"
      "v_max_u32_e32 v234, v234, v159\n\t"
      "v_cmp_eq_u32_e32 vcc, -1, v234\n\t"
      "s_cbranch_vccnz NP4L_%=\n\t"
      "NP4X_%=:\n\t"
      "v_mfma_f32_16x16x32_bf16 v[184:187], v[0:3], v[128:131], 0\n\t"
      "v_mfma_f32_16x16x32_bf16 v[188:191], v[4:7], v[128:131], 0\n\t"
      "v_mfma_f32_16x16x32_bf16 v[192:195], v[8:11], v[128:131], 0\n\t"
      "v_mfma_f32_16x16x32_bf16 v[196:199], v[12:15], v[128:131], 0\n\t"
      "v_mfma_f32_16x16x32_bf16 v[184:187], v[16:19], v[132:135], v[184:187]\n\t"
      "v_mfma_f32_16x16x32_bf16 v[188:191], v[20:23], v[132:135], v[188:191]\n\t"
      "v_mfma_f32_16x16x32_bf16 v[192:195], v[24:27], v[132:135], v[192:195]\n\t"
      "v_mfma_f32_16x16x32_bf16 v[196:199], v[28:31], v[132:135], v[196:199]\n\t"
      "v_mfma_f32_16x16x32_bf16 v[184:187], v[32:35], v[136:139], v[184:187]\n\t"
      "v_mfma_f32_16x16x32_bf16 v[188:191], v[36:39], v[136:139], v[188:191]\n\t"
      "v_mfma_f32_16x16x32_bf16 v[192:195], v[40:43], v[136:139], v[192:195]\n\t"
      "v_mfma_f32_16x16x32_bf16 v[196:199], v[44:47], v[136:139], v[196:199]\n\t"
      "v_mfma_f32_16x16x32_bf16 v[184:187], v[48:51], v[140:143], v[184:187]\n\t"
      "v_mfma_f32_16x16x32_bf16 v[188:191], v[52:55], v[140:143], v[188:191]\n\t"
      "v_mfma_f32_16x16x32_bf16 v[192:195], v[56:59], v[140:143], v[192:195]\n\t"
      "v_mfma_f32_16x16x32_bf16 v[196:199], v[60:63], v[140:143], v[196:199]\n\t"
      "v_mfma_f32_16x16x32_bf16 v[184:187], v[64:67], v[144:147], v[184:187]\n\t"
      "v_mfma_f32_16x16x32_bf16 v[188:191], v[68:71], v[144:147], v[188:191]\n\t"
      "v_mfma_f32_16x16x32_bf16 v[192:195], v[72:75], v[144:147], v[192:195]\n\t"
      "v_mfma_f32_16x16x32_bf16 v[196:199], v[76:79], v[144:147], v[196:199]\n\t"
      "v_mfma_f32_16x16x32_bf16 v[184:187], v[80:83], v[148:151], v[184:187]\n\t"
      "v_mfma_f32_16x16x32_bf16 v[188:191], v[84:87], v[148:151], v[188:191]\n\t"
      "v_mfma_f32_16x16x32_bf16 v[192:195], v[88:91], v[148:151], v[192:195]\n\t"
      "v_mfma_f32_16x16x32_bf16 v[196:199], v[92:95], v[148:151], v[196:199]\n\t"
      "v_mfma_f32_16x16x32_bf16 v[184:187], v[96:99], v[152:155], v[184:187]\n\t"
      "v_mfma_f32_16x16x32_bf16 v[188:191], v[100:103], v[152:155], v[188:191]\n\t"
      "v_mfma_f32_16x16x32_bf16 v[192:195], v[104:107], v[152:155], v[192:195]\n\t"
      "v_mfma_f32_16x16x32_bf16 v[184:187], v[112:115], v[156:159], v[184:187]\n\t"
      "v_mfma_f32_16x16x32_bf16 v[188:191], v[116:119], v[156:159], v[188:191]\n\t"
      "v_mfma_f32_16x16x32_bf16 v[192:195], v[120:123], v[156:159], v[192:195]\n\t"
      "global_load_dwordx4 v[128:131], v176, s[62:63] sc1\n\t"
      "global_load_dwordx4 v[132:135], v177, s[62:63] sc1\n\t"
      "global_load_dwordx4 v[136:139], v178, s[62:63] sc1\n\t"
      "global_load_dwordx4 v[140:143], v179, s[62:63] sc1\n\t"
      "global_load_dwordx4 v[144:147], v180, s[62:63] sc1\n\t"
      "global_load_dwordx4 v[148:151], v181, s[62:63] sc1\n\t"
      "s_waitcnt vmcnt(0)\n\t"
      "s_nop 7\n\t"
      "s_branch NP4B_%=\n\t"
      "NP5R_%=:\n\t"
      "s_mov_b32 s51, 0\n\t"
      "NP5L_%=:\n\t"
      "s_add_u32 s51, s51, 1\n\t"
      "s_cmp_lt_u32 s51, 262144\n\t"
      "s_cbranch_scc0 NP5X_%=\n\t"
      "s_waitcnt vmcnt(0)\n\t"
      "s_sleep 1\n\t"
      "global_load_dwordx4 v[128:131], v176, s[62:63] sc1\n\t"
      "global_load_dwordx4 v[132:135], v177, s[62:63] sc1\n\t"
      "global_load_dwordx4 v[136:139], v178, s[62:63] sc1\n\t"
      "global_load_dwordx4 v[140:143], v179, s[62:63] sc1\n\t"
      "global_load_dwordx4 v[144:147], v180, s[62:63] sc1\n\t"
      "global_load_dwordx4 v[148:151], v181, s[62:63] sc1\n\t"
      "global_load_dwordx4 v[152:155], v182, s[62:63] sc1\n\t"
      "global_load_dwordx4 v[156:159], v183, s[62:63] sc1\n\t"
      "s_waitcnt vmcnt(0)\n\t"
      "v_max3_u32 v234, v128, v129, v130\n\t"
      "v_max3_u32 v234, v234, v131, v132\n\t"
      "v_max3_u32 v234, v234, v133, v134\n\t"
      "v_max3_u32 v234, v234, v135, v136\n\t"
      "v_max3_u32 v234, v234, v137, v138\n\t"
      "v_max3_u32 v234, v234, v139, v140\n\t"
      "v_max3_u32 v234, v234, v141, v142\n\t"
      "v_max3_u32 v234, v234, v143, v144\n\t"
      "v_max3_u32 v234, v234, v145, v146\n\t"
      "v_max3_u32 v234, v234, v147, v148\n\t"
      "v_max3_u32 v234, v234, v149, v150\n\t"
      "v_max3_u32 v234, v234, v151, v152\n\t"
      "v_max3_u32 v234, v234, v153, v154\n\t"
      "v_max3_u32 v234, v234, v155, v156\n\t"
      "v_max3_u32 v234, v234, v157, v158\n\t"
      "v_max_u32_e32 v234, v234, v159\n\t"
      "v_cmp_eq_u32_e32 vcc, -1, v234\n\t"
      "s_cbranch_vccnz NP5L_%=\n\t"
      "NP5X_%=:\n\t"
      "v_mfma_f32_16x16x32_bf16 v[200:203], v[0:3], v[128:131], 0\n\t"
      "v_mfma_f32_16x16x32_bf16 v[204:207], v[4:7], v[128:131], 0\n\t"
      "v_mfma_f32_16x16x32_bf16 v[208:211], v[8:11], v[128:131], 0\n\t"
      "v_mfma_f32_16x16x32_bf16 v[212:215], v[12:15], v[128:131], 0\n\t"
      "v_mfma_f32_16x16x32_bf16 v[200:203], v[16:19], v[132:135], v[200:203]\n\t"
      "v_mfma_f32_16x16x32_bf16 v[204:207], v[20:23], v[132:135], v[204:207]\n\t"
      "v_mfma_f32_16x16x32_bf16 v[208:211], v[24:27], v[132:135], v[208:211]\n\t"
      "v_mfma_f32_16x16x32_bf16 v[212:215], v[28:31], v[132:135], v[212:215]\n\t"
      "v_mfma_f32_16x16x32_bf16 v[200:203], v[32:35], v[136:139], v[200:203]\n\t"
      "v_mfma_f32_16x16x32_bf16 v[204:207], v[36:39], v[136:139], v[204:207]\n\t"
      "v_mfma_f32_16x16x32_bf16 v[208:211], v[40:43], v[136:139], v[208:211]\n\t"
      "v_mfma_f32_16x16x32_bf16 v[212:215], v[44:47], v[136:139], v[212:215]\n\t"
      "v_mfma_f32_16x16x32_bf16 v[200:203], v[48:51], v[140:143], v[200:203]\n\t"
      "v_mfma_f32_16x16x32_bf16 v[204:207], v[52:55], v[140:143], v[204:207]\n\t"
      "v_mfma_f32_16x16x32_bf16 v[208:211], v[56:59], v[140:143], v[208:211]\n\t"
      "v_mfma_f32_16x16x32_bf16 v[212:215], v[60:63], v[140:143], v[212:215]\n\t"
      "v_mfma_f32_16x16x32_bf16 v[200:203], v[64:67], v[144:147], v[200:203]\n\t"
      "v_mfma_f32_16x16x32_bf16 v[204:207], v[68:71], v[144:147], v[204:207]\n\t"
      "v_mfma_f32_16x16x32_bf16 v[208:211], v[72:75], v[144:147], v[208:211]\n\t"
      "v_mfma_f32_16x16x32_bf16 v[212:215], v[76:79], v[144:147], v[212:215]\n\t"
      "v_mfma_f32_16x16x32_bf16 v[200:203], v[80:83], v[148:151], v[200:203]\n\t"
      "v_mfma_f32_16x16x32_bf16 v[204:207], v[84:87], v[148:151], v[204:207]\n\t"
      "v_mfma_f32_16x16x32_bf16 v[208:211], v[88:91], v[148:151], v[208:211]\n\t"
      "v_mfma_f32_16x16x32_bf16 v[212:215], v[92:95], v[148:151], v[212:215]\n\t"
      "v_mfma_f32_16x16x32_bf16 v[200:203], v[96:99], v[152:155], v[200:203]\n\t"
      "v_mfma_f32_16x16x32_bf16 v[204:207], v[100:103], v[152:155], v[204:207]\n\t"
      "v_mfma_f32_16x16x32_bf16 v[208:211], v[104:107], v[152:155], v[208:211]\n\t"
      "v_mfma_f32_16x16x32_bf16 v[200:203], v[112:115], v[156:159], v[200:203]\n\t"
      "v_mfma_f32_16x16x32_bf16 v[204:207], v[116:119], v[156:159], v[204:207]\n\t"
      "v_mfma_f32_16x16x32_bf16 v[208:211], v[120:123], v[156:159], v[208:211]\n\t"
      "global_load_dwordx4 v[128:131], v176, s[64:65] sc1\n\t"
      "global_load_dwordx4 v[132:135], v177, s[64:65] sc1\n\t"
      "global_load_dwordx4 v[136:139], v178, s[64:65] sc1\n\t"
      "global_load_dwordx4 v[140:143], v179, s[64:65] sc1\n\t"
      "global_load_dwordx4 v[144:147], v180, s[64:65] sc1\n\t"
      "global_load_dwordx4 v[148:151], v181, s[64:65] sc1\n\t"
      "s_waitcnt vmcnt(0)\n\t"
      "s_nop 7\n\t"
      "s_branch NP5B_%=\n\t"
      "NP6R_%=:\n\t"
      "s_mov_b32 s51, 0\n\t"
      "NP6L_%=:\n\t"
      "s_add_u32 s51, s51, 1\n\t"
      "s_cmp_lt_u32 s51, 262144\n\t"
      "s_cbranch_scc0 NP6X_%=\n\t"
      "s_waitcnt vmcnt(0)\n\t"
      "s_sleep 1\n\t"
      "global_load_dwordx4 v[128:131], v176, s[64:65] sc1\n\t"
      "global_load_dwordx4 v[132:135], v177, s[64:65] sc1\n\t"
      "global_load_dwordx4 v[136:139], v178, s[64:65] sc1\n\t"
      "global_load_dwordx4 v[140:143], v179, s[64:65] sc1\n\t"
      "global_load_dwordx4 v[144:147], v180, s[64:65] sc1\n\t"
      "global_load_dwordx4 v[148:151], v181, s[64:65] sc1\n\t"
      "global_load_dwordx4 v[152:155], v182, s[64:65] sc1\n\t"
      "global_load_dwordx4 v[156:159], v183, s[64:65] sc1\n\t"
      "s_waitcnt vmcnt(0)\n\t"
      "v_max3_u32 v234, v128, v129, v130\n\t"
      "v_max3_u32 v234, v234, v131, v132\n\t"
      "v_max3_u32 v234, v234, v133, v134\n\t"
      "v_max3_u32 v234, v234, v135, v136\n\t"
      "v_max3_u32 v234, v234, v137, v138\n\t"
      "v_max3_u32 v234, v234, v139, v140\n\t"
      "v_max3_u32 v234, v234, v141, v142\n\t"
      "v_max3_u32 v234, v234, v143, v144\n\t"
      "v_max3_u32 v234, v234, v145, v146\n\t"
      "v_max3_u32 v234, v234, v147, v148\n\t"
      "v_max3_u32 v234, v234, v149, v150\n\t"
      "v_max3_u32 v234, v234, v151, v152\n\t"
      "v_max3_u32 v234, v234, v153, v154\n\t"
      "v_max3_u32 v234, v234, v155, v156\n\t"
      "v_max3_u32 v234, v234, v157, v158\n\t"
      "v_max_u32_e32 v234, v234, v159\n\t"
      "v_cmp_eq_u32_e32 vcc, -1, v234\n\t"
      "s_cbranch_vccnz NP6L_%=\n\t"
      "NP6X_%=:\n\t"
      "v_mfma_f32_16x16x32_bf16 v[184:187], v[0:3], v[128:131], 0\n\t"
      "v_mfma_f32_16x16x32_bf16 v[188:191], v[4:7], v[128:131], 0\n\t"
      "v_mfma_f32_16x16x32_bf16 v[192:195], v[8:11], v[128:131], 0\n\t"
      "v_mfma_f32_16x16x32_bf16 v[196:199], v[12:15], v[128:131], 0\n\t"
      "v_mfma_f32_16x16x32_bf16 v[184:187], v[16:19], v[132:135], v[184:187]\n\t"
      "v_mfma_f32_16x16x32_bf16 v[188:191], v[20:23], v[132:135], v[188:191]\n\t"
      "v_mfma_f32_16x16x32_bf16 v[192:195], v[24:27], v[132:135], v[192:195]\n\t"
      "v_mfma_f32_16x16x32_bf16 v[196:199], v[28:31], v[132:135], v[196:199]\n\t"
      "v_mfma_f32_16x16x32_bf16 v[184:187], v[32:35], v[136:139], v[184:187]\n\t"
      "v_mfma_f32_16x16x32_bf16 v[188:191], v[36:39], v[136:139], v[188:191]\n\t"
      "v_mfma_f32_16x16x32_bf16 v[192:195], v[40:43], v[136:139], v[192:195]\n\t"
      "v_mfma_f32_16x16x32_bf16 v[196:199], v[44:47], v[136:139], v[196:199]\n\t"
      "v_mfma_f32_16x16x32_bf16 v[184:187], v[48:51], v[140:143], v[184:187]\n\t"
      "v_mfma_f32_16x16x32_bf16 v[188:191], v[52:55], v[140:143], v[188:191]\n\t"
      "v_mfma_f32_16x16x32_bf16 v[192:195], v[56:59], v[140:143], v[192:195]\n\t"
      "v_mfma_f32_16x16x32_bf16 v[196:199], v[60:63], v[140:143], v[196:199]\n\t"
      "v_mfma_f32_16x16x32_bf16 v[184:187], v[64:67], v[144:147], v[184:187]\n\t"
      "v_mfma_f32_16x16x32_bf16 v[188:191], v[68:71], v[144:147], v[188:191]\n\t"
      "v_mfma_f32_16x16x32_bf16 v[192:195], v[72:75], v[144:147], v[192:195]\n\t"
      "v_mfma_f32_16x16x32_bf16 v[196:199], v[76:79], v[144:147], v[196:199]\n\t"
      "v_mfma_f32_16x16x32_bf16 v[184:187], v[80:83], v[148:151], v[184:187]\n\t"
      "v_mfma_f32_16x16x32_bf16 v[188:191], v[84:87], v[148:151], v[188:191]\n\t"
      "v_mfma_f32_16x16x32_bf16 v[192:195], v[88:91], v[148:151], v[192:195]\n\t"
      "v_mfma_f32_16x16x32_bf16 v[196:199], v[92:95], v[148:151], v[196:199]\n\t"
      "v_mfma_f32_16x16x32_bf16 v[184:187], v[96:99], v[152:155], v[184:187]\n\t"
      "v_mfma_f32_16x16x32_bf16 v[188:191], v[100:103], v[152:155], v[188:191]\n\t"
      "v_mfma_f32_16x16x32_bf16 v[192:195], v[104:107], v[152:155], v[192:195]\n\t"
      "v_mfma_f32_16x16x32_bf16 v[184:187], v[112:115], v[156:159], v[184:187]\n\t"
      "v_mfma_f32_16x16x32_bf16 v[188:191], v[116:119], v[156:159], v[188:191]\n\t"
      "v_mfma_f32_16x16x32_bf16 v[192:195], v[120:123], v[156:159], v[192:195]\n\t"
      "s_waitcnt vmcnt(0)\n\t"
      "s_nop 7\n\t"
      "s_branch NP6B_%=\n\t"
      : "+{s[4:19]}"(s), "+{s[20:21]}"(kr), "+{s22}"(hint), "+{v[240:247]}"(v)
      :
      : "memory", "vcc", "scc",
        "v0", "v1", "v2", "v3", "v4", "v5", "v6", "v7", "v8", "v9", "v10", "v11", "v12", "v13", "v14", "v15", "v16",
        "v17", "v18", "v19", "v20", "v21", "v22", "v23", "v24", "v25", "v26", "v27", "v28", "v29", "v30", "v31", "v32",
        "v33", "v34", "v35", "v36", "v37", "v38", "v39", "v40", "v41", "v42", "v43", "v44", "v45", "v46", "v47", "v48",
        "v49", "v50", "v51", "v52", "v53", "v54", "v55", "v56", "v57", "v58", "v59", "v60", "v61", "v62", "v63", "v64",
        "v65", "v66", "v67", "v68", "v69", "v70", "v71", "v72", "v73", "v74", "v75", "v76", "v77", "v78", "v79", "v80",
        "v81", "v82", "v83", "v84", "v85", "v86", "v87", "v88", "v89", "v90", "v91", "v92", "v93", "v94", "v95", "v96",
        "v97", "v98", "v99", "v100", "v101", "v102", "v103", "v104", "v105", "v106", "v107", "v108", "v109", "v110",
        "v111", "v112", "v113", "v114", "v115", "v116", "v117", "v118", "v119", "v120", "v121", "v122", "v123", "v124",
        "v125", "v126", "v127", "v128", "v129", "v130", "v131", "v132", "v133", "v134", "v135", "v136", "v137", "v138",
        "v139", "v140", "v141", "v142", "v143", "v144", "v145", "v146", "v147", "v148", "v149", "v150", "v151", "v152",
        "v153", "v154", "v155", "v156", "v157", "v158", "v159", "v160", "v161", "v162", "v163", "v164", "v165", "v166",
        "v167", "v168", "v169", "v170", "v171", "v172", "v173", "v174", "v175", "v176", "v177", "v178", "v179", "v180",
        "v181", "v182", "v183", "v184", "v185", "v186", "v187", "v188", "v189", "v190", "v191", "v192", "v193", "v194",
        "v195", "v196", "v197", "v198", "v199", "v200", "v201", "v202", "v203", "v204", "v205", "v206", "v207", "v208",
        "v209", "v210", "v211", "v212", "v213", "v214", "v215", "v216", "v217", "v218", "v219", "v220", "v221", "v222",
        "v223", "v224", "v225", "v226", "v227", "v228", "v229", "v230", "v231", "v232", "v233", "v234", "v235", "v236",
        "v237", "v238", "v239", "v248", "v249", "v250", "v251", "v252", "v253", "s44", "s45", "s46", "s47", "s48",
        "s49", "s50", "s51", "s52", "s53", "s54", "s55", "s56", "s57", "s58", "s59", "s60", "s61", "s62", "s63", "s64",
        "s65", "s66", "s67", "s68", "s69", "s70"
    );
    __builtin_amdgcn_endpgm();
  } else {
    const int ib = idx - 16, t = 16 * (4 * side + ((xp + XO) & 3)) + ib;
    const int st = 8 * xp + (ib >> 1), dg = 2 * side + (ib & 1), blk0 = 7 * dg;
    const int rot = R4 == 0 ? 4 * (ib & 1) + 2 * side - (ib & side & 1) : R4 == 1 ? 3 + 2 * side : R4 == 2 ? 3 : (3 * (ib & 1) + (ib >> 1) + 2 * side) % 6, rn = (rot + W2E) % 7;
    const unsigned bits = UNI((wave >= 4 ? 1 : 0) | ((wave >= 4) != HP4 ? 2 : 0) | (wave == 0 ? 16 : 0) |
                              (t == 0 && wave == 0 ? 32 : 0) | (wave < 7 && t < 112 ? 64 : 0));
    u32x4 vo;
    unsigned vq;
    u64 par = ST_PW + xcd * 128;
    {
      LANE_BITS(threadIdx.x & 63);
      const int cb0 = wave < 4 ? 4 * wave : 4 + 3 * wave;
      const int k0 = (ib + 2 * xcd) % KROT * KMUL;
      u32x16 v, s;
      u32x8 s2;
      v[0] = (8 * (wave & 3) + lr) * 896 + (wave >> 2) * 128 + ((lc ^ swz_tr(8 * wave + lr)) << 4) + 4096;
#pragma unroll
      for (int b = 0; b < 4; ++b) {
        const int cb = min(cb0 + b, 27);
        v[1 + b] = g * B_UNIT + (cb >> 2) * 1024 + q4 * 128 + (((2 * (cb & 3) + (p4 >> 1)) ^ swz_tr(8 * g + q4)) << 4) + (p4 & 1) * 8;
        const int by = 112 * wave + 56 * (b >> 1) + 32 * (b & 1) + 8 * g - 64 * k0;
        vo[b] = B_X + c * B_OP + (by < 0 ? by + 896 : by);
      }
      v[5] = B_X + (4 * g + q4) * 896 + cb0 * 32 + p4 * 8;
      v[6] = B_X + c * B_OP + 16 * g;
      v[7] = unit(7 * (wave & 3) + (wave >> 2) * 2, t >> 4, 0) + (g & 1) * 131072 + (t & 15) * 512 + (g >> 1) * 256 + c * 16;
      v[13] = v[7] + 4 * 131072;
      v[8] = B_PS + (4 * g + q4) * 64 + wave * 8;
      v[9] = B_PS + (4 * g + p4) * 64 + q4 * 16;
      v[10] = (t * 16 + 4 * g + p4) * 16;
      v[11] = lane * 16 + ((2 * (t >> 2) + 1) % 7 ? 1024 : 16384 - 6144);
      v[12] = lane * 16;
      v[14] = (wave * 224 + 96 + 64 * (side ^ (lane >> 5)) + ib * 4 + (lr & 3)) * 4096 + lc * 128 + xp * 1024;
      v[15] = ST_H3 + part((lc & 3) * 16384 + t * 128, lc >> 2);
      put(s, 0, (u64)w2);
      put(s, 2, (u64)(wo + k0 * 32 * 896 - 4096));
      put(s, 4, (u64)(ws + WS_ST));
      put(s, 6, (u64)(ws + WS_O + part(((t & 63) * 16 + 2 * wave) * 1024, side)));
      put(s, 10, (u64)(ws + WS_HN));
      put(s, 12, (u64)(ws + WS_G + unit(7 * (t & 3), 2 * (t >> 2) / 7, 0) + wave * 131072 + 2 * (t >> 2) % 7 * 1024));
      put(s, 14, (u64)(w2 + st * 128 + (wave * 224 + rot * 32) * 4096));
      unsigned vn = lr * 4096 + ((lc ^ swz_tr(lr)) << 4), lw = wave * 4096;
      vq = ST_H2 + part((((((t >> 3) & 7) * 8 + wave) * 2 + ((t >> 2) & 1)) * 2) * 64, side) + (lane & 1) * 64;
      unsigned vck = ST_CK + part((((t >> 2) & 15) * 16 + 2 * wave) * 256, side) + ((g >> 1) * 4 + (t & 3)) * 64 + c * 4;
      s2[0] = bits;
      s2[1] = (wave & 3) * B_UNIT + (wave >> 2) * 1024;
      s2[2] = UNI(wave < 4 ? ~0u : 0u);
      s2[3] = 13 - k0;
      put2(s2, 4, wave < 4 ? ~0u : 0xffffu, wave < 4 ? ~0u : 0xffffu);
    asm volatile(
      "s_waitcnt lgkmcnt(0)\n\ts_nop 4\n\t"
      "s_load_dwordx2 s[26:27], s[8:9], 0x20000\n\t"
      "v_mov_b32_e32 v248, -1\n\t"
      "v_mov_b32_e32 v249, -1\n\t"
      "v_mov_b32_e32 v250, -1\n\t"
      "v_mov_b32_e32 v251, -1\n\t"
      "v_add_u32_e32 v221, 4294965504, v224\n\t"
      "v_add_u32_e32 v222, 512, v224\n\t"
      "v_add_u32_e32 v223, 4294966016, v224\n\t"
      "s_bitcmp1_b32 s20, 0\n\t"
      "s_cselect_b64 s[46:47], -1, 0\n\t"
      "s_mov_b64 vcc, s[46:47]\n\t"
      "s_mov_b32 s44, 0xffffffff\n\t"
      "s_mov_b32 s45, 0xffff\n\t"
      "s_waitcnt vmcnt(3)\n\t"
      "s_barrier\n\t"
      "ds_read_b64_tr_b16 v[204:205], v229\n\t"
      "ds_read_b64_tr_b16 v[206:207], v229 offset:32\n\t"
      "ds_read_b64_tr_b16 v[208:209], v229 offset:64\n\t"
      "ds_read_b64_tr_b16 v[210:211], v229 offset:96\n\t"
      "s_waitcnt lgkmcnt(0)\n\t"
      "s_add_u32 s26, s26, 1\n\t"
      "s_addc_u32 s27, s27, 0\n\t"
      "s_mul_i32 s48, s26, 0x9e3779b9\n\t"
      "s_mul_hi_u32 s13, s26, 0x7f4a7c15\n\t"
      "s_add_u32 s13, s13, s48\n\t"
      "s_mul_i32 s48, s27, 0x7f4a7c15\n\t"
      "s_add_u32 s13, s13, s48\n\t"
      "s_mul_i32 s12, s26, 0x7f4a7c15\n\t"
      "s_add_u32 s12, s12, 0xd192ed03\n\t"
      "s_addc_u32 s13, s13, 0xd1b54a32\n\t"
      "v_mov_b32_e32 v188, s12\n\t"
      "v_mov_b32_e32 v189, s13\n\t"
      "v_mov_b32_e32 v190, s26\n\t"
      "v_mov_b32_e32 v191, s27\n\t"
      "v_mov_b32_e32 v192, 163600\n\t"
      "s_mov_b64 exec, 1\n\t"
      "ds_write_b128 v192, v[188:191]\n\t"
      "s_mov_b64 exec, -1\n\t"
      "v_and_b32_e32 v210, s22, v210\n\t"
      "v_and_b32_e32 v211, s22, v211\n\t"
      "v_lshlrev_b32_e32 v160, 16, v204\n\t"
      "v_and_b32_e32 v161, 0xffff0000, v204\n\t"
      "v_lshlrev_b32_e32 v162, 16, v205\n\t"
      "v_and_b32_e32 v163, 0xffff0000, v205\n\t"
      "v_lshlrev_b32_e32 v164, 16, v206\n\t"
      "v_and_b32_e32 v165, 0xffff0000, v206\n\t"
      "v_lshlrev_b32_e32 v166, 16, v207\n\t"
      "v_and_b32_e32 v167, 0xffff0000, v207\n\t"
      "v_lshlrev_b32_e32 v168, 16, v208\n\t"
      "v_and_b32_e32 v169, 0xffff0000, v208\n\t"
      "v_lshlrev_b32_e32 v170, 16, v209\n\t"
      "v_and_b32_e32 v171, 0xffff0000, v209\n\t"
      "v_lshlrev_b32_e32 v172, 16, v210\n\t"
      "v_and_b32_e32 v173, 0xffff0000, v210\n\t"
      "v_lshlrev_b32_e32 v174, 16, v211\n\t"
      "v_and_b32_e32 v175, 0xffff0000, v211\n\t"
      "v_pk_add_f32 v[188:189], v[160:161], v[164:165]\n\t"
      "v_pk_mul_f32 v[192:193], v[160:161], v[160:161]\n\t"
      "v_pk_add_f32 v[190:191], v[162:163], v[166:167]\n\t"
      "v_pk_mul_f32 v[194:195], v[162:163], v[162:163]\n\t"
      "v_pk_fma_f32 v[192:193], v[164:165], v[164:165], v[192:193]\n\t"
      "v_pk_fma_f32 v[194:195], v[166:167], v[166:167], v[194:195]\n\t"
      "v_pk_add_f32 v[188:189], v[188:189], v[168:169]\n\t"
      "v_pk_fma_f32 v[192:193], v[168:169], v[168:169], v[192:193]\n\t"
      "v_pk_add_f32 v[190:191], v[190:191], v[170:171]\n\t"
      "v_pk_fma_f32 v[194:195], v[170:171], v[170:171], v[194:195]\n\t"
      "s_cbranch_vccnz SB0_%=\n\t"
      "v_pk_add_f32 v[188:189], v[188:189], v[172:173]\n\t"
      "v_pk_fma_f32 v[192:193], v[172:173], v[172:173], v[192:193]\n\t"
      "v_pk_add_f32 v[190:191], v[190:191], v[174:175]\n\t"
      "v_pk_fma_f32 v[194:195], v[174:175], v[174:175], v[194:195]\n\t"
      "SB0_%=:\n\t"
      "v_add_f32_dpp v196, v188, v188 row_ror:8 row_mask:0xf bank_mask:0x3\n\t"
      "v_add_f32_dpp v197, v189, v189 row_ror:8 row_mask:0xf bank_mask:0x3\n\t"
      "v_add_f32_dpp v198, v192, v192 row_ror:8 row_mask:0xf bank_mask:0x3\n\t"
      "v_add_f32_dpp v199, v193, v193 row_ror:8 row_mask:0xf bank_mask:0x3\n\t"
      "v_add_f32_dpp v196, v190, v190 row_ror:8 row_mask:0xf bank_mask:0xc\n\t"
      "v_add_f32_dpp v197, v191, v191 row_ror:8 row_mask:0xf bank_mask:0xc\n\t"
      "v_add_f32_dpp v198, v194, v194 row_ror:8 row_mask:0xf bank_mask:0xc\n\t"
      "v_add_f32_dpp v199, v195, v195 row_ror:8 row_mask:0xf bank_mask:0xc\n\t"
      "v_add_f32_dpp v188, v196, v196 row_shl:4 row_mask:0xf bank_mask:0x5\n\t"
      "v_add_f32_dpp v189, v198, v198 row_shl:4 row_mask:0xf bank_mask:0x5\n\t"
      "s_nop 0\n\t"
      "v_add_f32_dpp v188, v197, v197 row_shr:4 row_mask:0xf bank_mask:0xa\n\t"
      "v_add_f32_dpp v189, v199, v199 row_shr:4 row_mask:0xf bank_mask:0xa\n\t"
      "s_nop 0\n\t"
      "v_add_f32_dpp v188, v188, v188 quad_perm:[1,0,3,2] row_mask:0xf bank_mask:0xf\n\t"
      "v_add_f32_dpp v189, v189, v189 quad_perm:[1,0,3,2] row_mask:0xf bank_mask:0xf\n\t"
      "s_nop 0\n\t"
      "v_add_f32_dpp v188, v188, v188 quad_perm:[2,3,0,1] row_mask:0xf bank_mask:0xf\n\t"
      "v_add_f32_dpp v189, v189, v189 quad_perm:[2,3,0,1] row_mask:0xf bank_mask:0xf\n\t"
      "ds_write_b64 v232, v[188:189]\n\t"
      "s_add_u32 m0, s21, 0\n\t"
      "s_nop 0\n\t"
      "global_load_lds_dwordx4 v224, s[6:7]\n\t"
      "global_load_lds_dwordx4 v221, s[6:7] offset:2048\n\t"
      "s_add_u32 m0, s21, 4096\n\t"
      "s_nop 0\n\t"
      "global_load_lds_dwordx4 v222, s[6:7]\n\t"
      "s_cbranch_vccnz WR0_%=\n\t"
      "global_load_lds_dwordx4 v223, s[6:7] offset:2048\n\t"
      "WR0_%=:\n\t"
      "s_sub_u32 s23, s23, 1\n\t"
      "s_cselect_b32 s48, -13, 1\n\t"
      "s_cselect_b32 s49, -1, 0\n\t"
      "s_mul_i32 s48, s48, 28672\n\t"
      "s_add_u32 s6, s6, s48\n\t"
      "s_addc_u32 s7, s7, s49\n\t"
      "s_add_u32 m0, s21, 28672\n\t"
      "s_nop 0\n\t"
      "global_load_lds_dwordx4 v224, s[6:7]\n\t"
      "global_load_lds_dwordx4 v221, s[6:7] offset:2048\n\t"
      "s_add_u32 m0, s21, 32768\n\t"
      "s_nop 0\n\t"
      "global_load_lds_dwordx4 v222, s[6:7]\n\t"
      "s_cbranch_vccnz WR1_%=\n\t"
      "global_load_lds_dwordx4 v223, s[6:7] offset:2048\n\t"
      "WR1_%=:\n\t"
      "s_sub_u32 s23, s23, 1\n\t"
      "s_cselect_b32 s48, -13, 1\n\t"
      "s_cselect_b32 s49, -1, 0\n\t"
      "s_mul_i32 s48, s48, 28672\n\t"
      "s_add_u32 s6, s6, s48\n\t"
      "s_addc_u32 s7, s7, s49\n\t"
      "s_waitcnt lgkmcnt(0)\n\t"
      "s_barrier\n\t"
      "ds_read_b128 v[188:191], v233\n\t"
      "s_waitcnt lgkmcnt(0)\n\t"
      "v_pk_add_f32 v[188:189], v[188:189], v[190:191]\n\t"
      "s_nop 1\n\t"
      "v_add_f32_dpp v188, v188, v188 row_ror:4 row_mask:0xf bank_mask:0xf\n\t"
      "v_add_f32_dpp v189, v189, v189 row_ror:4 row_mask:0xf bank_mask:0xf\n\t"
      "s_nop 0\n\t"
      "v_add_f32_dpp v188, v188, v188 row_ror:8 row_mask:0xf bank_mask:0xf\n\t"
      "v_add_f32_dpp v189, v189, v189 row_ror:8 row_mask:0xf bank_mask:0xf\n\t"
      "v_mul_f32_e32 v188, 0x3b124925, v188\n\t"
      "v_mul_f32_e32 v189, 0x3b124925, v189\n\t"
      "v_fma_f32 v189, -v188, v188, v189\n\t"
      "v_max_f32_e32 v189, 0, v189\n\t"
      "s_bitcmp1_b32 s20, 4\n\t"
      "s_cbranch_scc0 GW_%=\n\t"
      "v_add_f32_e32 v189, 0x3727c5ac, v189\n\t"
      "v_rsq_f32_e32 v189, v189\n\t"
      "v_xor_b32_e32 v188, 0x80000000, v188\n\t"
      "v_xor_b32_e32 v190, s12, v188\n\t"
      "v_xor_b32_e32 v191, s13, v189\n\t"
      "s_mov_b32 s48, 0xf000f\n\t"
      "s_mov_b32 s49, 0xf000f\n\t"
      "s_mov_b64 exec, s[48:49]\n\t"
      "global_store_dwordx4 v234, v[188:191], s[8:9] sc1\n\t"
      "s_mov_b64 exec, -1\n\t"
      "GW_%=:\n\t"
      "v_xor_b32_e32 v176, 64, v245\n\t"
      "v_add_u32_e32 v177, 63488, v245\n\t"
      "v_add_u32_e32 v178, 95232, v176\n\t"
      "v_add_u32_e32 v176, 31744, v176\n\t"
      "s_add_u32 m0, s30, 130560\n\t"
      "s_nop 0\n\t"
      "global_load_lds_dwordx4 v245, s[18:19]\n\t"
      "global_load_lds_dwordx4 v176, s[18:19] offset:1024\n\t"
      "global_load_lds_dwordx4 v177, s[18:19] offset:2048\n\t"
      "global_load_lds_dwordx4 v178, s[18:19] offset:3072\n\t"
      "s_add_u32 s18, s18, 131072\n\t"
      "s_addc_u32 s19, s19, 0\n\t"
      "s_cbranch_vccnz SN3_%=\n\t"
      "global_store_dwordx4 v231, v[248:251], s[14:15] sc1\n\t"
      "SN3_%=:\n\t"
      "s_cbranch_vccnz WW0_%=\n\t"
      "s_waitcnt vmcnt(8)\n\t"
      "s_branch WX0_%=\n\t"
      "WW0_%=:\n\t"
      "s_waitcnt vmcnt(7)\n\t"
      "WX0_%=:\n\t"
      "s_waitcnt lgkmcnt(0)\n\t"
      "s_barrier\n\t"
      "ds_read_b64_tr_b16 v[0:1], v225\n\t"
      "ds_read_b64_tr_b16 v[2:3], v225 offset:512\n\t"
      "ds_read_b64_tr_b16 v[4:5], v226\n\t"
      "ds_read_b64_tr_b16 v[6:7], v226 offset:512\n\t"
      "ds_read_b64_tr_b16 v[8:9], v227\n\t"
      "ds_read_b64_tr_b16 v[10:11], v227 offset:512\n\t"
      "ds_read_b64_tr_b16 v[12:13], v228\n\t"
      "ds_read_b64_tr_b16 v[14:15], v228 offset:512\n\t"
      "s_add_u32 m0, s21, 57344\n\t"
      "s_nop 0\n\t"
      "global_load_lds_dwordx4 v224, s[6:7]\n\t"
      "global_load_lds_dwordx4 v221, s[6:7] offset:2048\n\t"
      "s_add_u32 m0, s21, 61440\n\t"
      "s_nop 0\n\t"
      "global_load_lds_dwordx4 v222, s[6:7]\n\t"
      "s_cbranch_vccnz WR2_%=\n\t"
      "global_load_lds_dwordx4 v223, s[6:7] offset:2048\n\t"
      "WR2_%=:\n\t"
      "s_sub_u32 s23, s23, 1\n\t"
      "s_cselect_b32 s48, -13, 1\n\t"
      "s_cselect_b32 s49, -1, 0\n\t"
      "s_mul_i32 s48, s48, 28672\n\t"
      "s_add_u32 s6, s6, s48\n\t"
      "s_addc_u32 s7, s7, s49\n\t"
      "s_cbranch_vccnz WW1_%=\n\t"
      "s_waitcnt vmcnt(8)\n\t"
      "s_branch WX1_%=\n\t"
      "WW1_%=:\n\t"
      "s_waitcnt vmcnt(7)\n\t"
      "WX1_%=:\n\t"
      "s_waitcnt lgkmcnt(0)\n\t"
      "s_barrier\n\t"
      "ds_read_b64_tr_b16 v[16:17], v225 offset:28672\n\t"
      "ds_read_b64_tr_b16 v[18:19], v225 offset:29184\n\t"
      "ds_read_b64_tr_b16 v[20:21], v226 offset:28672\n\t"
      "ds_read_b64_tr_b16 v[22:23], v226 offset:29184\n\t"
      "ds_read_b64_tr_b16 v[24:25], v227 offset:28672\n\t"
      "ds_read_b64_tr_b16 v[26:27], v227 offset:29184\n\t"
      "ds_read_b64_tr_b16 v[28:29], v228 offset:28672\n\t"
      "ds_read_b64_tr_b16 v[30:31], v228 offset:29184\n\t"
      "s_add_u32 m0, s21, 86016\n\t"
      "s_nop 0\n\t"
      "global_load_lds_dwordx4 v224, s[6:7]\n\t"
      "global_load_lds_dwordx4 v221, s[6:7] offset:2048\n\t"
      "s_add_u32 m0, s21, 90112\n\t"
      "s_nop 0\n\t"
      "global_load_lds_dwordx4 v222, s[6:7]\n\t"
      "s_cbranch_vccnz WR3_%=\n\t"
      "global_load_lds_dwordx4 v223, s[6:7] offset:2048\n\t"
      "WR3_%=:\n\t"
      "s_sub_u32 s23, s23, 1\n\t"
      "s_cselect_b32 s48, -13, 1\n\t"
      "s_cselect_b32 s49, -1, 0\n\t"
      "s_mul_i32 s48, s48, 28672\n\t"
      "s_add_u32 s6, s6, s48\n\t"
      "s_addc_u32 s7, s7, s49\n\t"
      "v_add_u32_e32 v225, 57344, v225\n\t"
      "v_add_u32_e32 v226, 57344, v226\n\t"
      "v_add_u32_e32 v227, 57344, v227\n\t"
      "v_add_u32_e32 v228, 57344, v228\n\t"
      "s_cbranch_vccnz WW2_%=\n\t"
      "s_waitcnt vmcnt(4)\n\t"
      "s_branch WX2_%=\n\t"
      "WW2_%=:\n\t"
      "s_waitcnt vmcnt(3)\n\t"
      "WX2_%=:\n\t"
      "s_waitcnt lgkmcnt(0)\n\t"
      "s_barrier\n\t"
      "ds_read_b64_tr_b16 v[32:33], v225\n\t"
      "ds_read_b64_tr_b16 v[34:35], v225 offset:512\n\t"
      "ds_read_b64_tr_b16 v[36:37], v226\n\t"
      "ds_read_b64_tr_b16 v[38:39], v226 offset:512\n\t"
      "ds_read_b64_tr_b16 v[40:41], v227\n\t"
      "ds_read_b64_tr_b16 v[42:43], v227 offset:512\n\t"
      "ds_read_b64_tr_b16 v[44:45], v228\n\t"
      "ds_read_b64_tr_b16 v[46:47], v228 offset:512\n\t"
      "s_add_u32 m0, s21, 0\n\t"
      "s_nop 0\n\t"
      "global_load_lds_dwordx4 v224, s[6:7]\n\t"
      "global_load_lds_dwordx4 v221, s[6:7] offset:2048\n\t"
      "s_add_u32 m0, s21, 4096\n\t"
      "s_nop 0\n\t"
      "global_load_lds_dwordx4 v222, s[6:7]\n\t"
      "s_cbranch_vccnz WR4_%=\n\t"
      "global_load_lds_dwordx4 v223, s[6:7] offset:2048\n\t"
      "WR4_%=:\n\t"
      "s_sub_u32 s23, s23, 1\n\t"
      "s_cselect_b32 s48, -13, 1\n\t"
      "s_cselect_b32 s49, -1, 0\n\t"
      "s_mul_i32 s48, s48, 28672\n\t"
      "s_add_u32 s6, s6, s48\n\t"
      "s_addc_u32 s7, s7, s49\n\t"
      "s_cbranch_vccnz WW3_%=\n\t"
      "s_waitcnt vmcnt(4)\n\t"
      "s_branch WX3_%=\n\t"
      "WW3_%=:\n\t"
      "s_waitcnt vmcnt(3)\n\t"
      "WX3_%=:\n\t"
      "s_waitcnt lgkmcnt(0)\n\t"
      "s_barrier\n\t"
      "ds_read_b64_tr_b16 v[48:49], v225 offset:28672\n\t"
      "ds_read_b64_tr_b16 v[50:51], v225 offset:29184\n\t"
      "ds_read_b64_tr_b16 v[52:53], v226 offset:28672\n\t"
      "ds_read_b64_tr_b16 v[54:55], v226 offset:29184\n\t"
      "ds_read_b64_tr_b16 v[56:57], v227 offset:28672\n\t"
      "ds_read_b64_tr_b16 v[58:59], v227 offset:29184\n\t"
      "ds_read_b64_tr_b16 v[60:61], v228 offset:28672\n\t"
      "ds_read_b64_tr_b16 v[62:63], v228 offset:29184\n\t"
      "s_add_u32 m0, s21, 28672\n\t"
      "s_nop 0\n\t"
      "global_load_lds_dwordx4 v224, s[6:7]\n\t"
      "global_load_lds_dwordx4 v221, s[6:7] offset:2048\n\t"
      "s_add_u32 m0, s21, 32768\n\t"
      "s_nop 0\n\t"
      "global_load_lds_dwordx4 v222, s[6:7]\n\t"
      "s_cbranch_vccnz WR5_%=\n\t"
      "global_load_lds_dwordx4 v223, s[6:7] offset:2048\n\t"
      "WR5_%=:\n\t"
      "s_sub_u32 s23, s23, 1\n\t"
      "s_cselect_b32 s48, -13, 1\n\t"
      "s_cselect_b32 s49, -1, 0\n\t"
      "s_mul_i32 s48, s48, 28672\n\t"
      "s_add_u32 s6, s6, s48\n\t"
      "s_addc_u32 s7, s7, s49\n\t"
      "v_add_u32_e32 v225, 4294909952, v225\n\t"
      "v_add_u32_e32 v226, 4294909952, v226\n\t"
      "v_add_u32_e32 v227, 4294909952, v227\n\t"
      "v_add_u32_e32 v228, 4294909952, v228\n\t"
      "s_cbranch_vccnz WW4_%=\n\t"
      "s_waitcnt vmcnt(4)\n\t"
      "s_branch WX4_%=\n\t"
      "WW4_%=:\n\t"
      "s_waitcnt vmcnt(3)\n\t"
      "WX4_%=:\n\t"
      "s_waitcnt lgkmcnt(0)\n\t"
      "s_barrier\n\t"
      "ds_read_b64_tr_b16 v[64:65], v225\n\t"
      "ds_read_b64_tr_b16 v[66:67], v225 offset:512\n\t"
      "ds_read_b64_tr_b16 v[68:69], v226\n\t"
      "ds_read_b64_tr_b16 v[70:71], v226 offset:512\n\t"
      "ds_read_b64_tr_b16 v[72:73], v227\n\t"
      "ds_read_b64_tr_b16 v[74:75], v227 offset:512\n\t"
      "ds_read_b64_tr_b16 v[76:77], v228\n\t"
      "ds_read_b64_tr_b16 v[78:79], v228 offset:512\n\t"
      "s_add_u32 m0, s21, 57344\n\t"
      "s_nop 0\n\t"
      "global_load_lds_dwordx4 v224, s[6:7]\n\t"
      "global_load_lds_dwordx4 v221, s[6:7] offset:2048\n\t"
      "s_add_u32 m0, s21, 61440\n\t"
      "s_nop 0\n\t"
      "global_load_lds_dwordx4 v222, s[6:7]\n\t"
      "s_cbranch_vccnz WR6_%=\n\t"
      "global_load_lds_dwordx4 v223, s[6:7] offset:2048\n\t"
      "WR6_%=:\n\t"
      "s_sub_u32 s23, s23, 1\n\t"
      "s_cselect_b32 s48, -13, 1\n\t"
      "s_cselect_b32 s49, -1, 0\n\t"
      "s_mul_i32 s48, s48, 28672\n\t"
      "s_add_u32 s6, s6, s48\n\t"
      "s_addc_u32 s7, s7, s49\n\t"
      "s_cbranch_vccnz WW5_%=\n\t"
      "s_waitcnt vmcnt(4)\n\t"
      "s_branch WX5_%=\n\t"
      "WW5_%=:\n\t"
      "s_waitcnt vmcnt(3)\n\t"
      "WX5_%=:\n\t"
      "s_waitcnt lgkmcnt(0)\n\t"
      "s_barrier\n\t"
      "ds_read_b64_tr_b16 v[80:81], v225 offset:28672\n\t"
      "ds_read_b64_tr_b16 v[82:83], v225 offset:29184\n\t"
      "ds_read_b64_tr_b16 v[84:85], v226 offset:28672\n\t"
      "ds_read_b64_tr_b16 v[86:87], v226 offset:29184\n\t"
      "ds_read_b64_tr_b16 v[88:89], v227 offset:28672\n\t"
      "ds_read_b64_tr_b16 v[90:91], v227 offset:29184\n\t"
      "ds_read_b64_tr_b16 v[92:93], v228 offset:28672\n\t"
      "ds_read_b64_tr_b16 v[94:95], v228 offset:29184\n\t"
      "s_add_u32 m0, s21, 86016\n\t"
      "s_nop 0\n\t"
      "global_load_lds_dwordx4 v224, s[6:7]\n\t"
      "global_load_lds_dwordx4 v221, s[6:7] offset:2048\n\t"
      "s_add_u32 m0, s21, 90112\n\t"
      "s_nop 0\n\t"
      "global_load_lds_dwordx4 v222, s[6:7]\n\t"
      "s_cbranch_vccnz WR7_%=\n\t"
      "global_load_lds_dwordx4 v223, s[6:7] offset:2048\n\t"
      "WR7_%=:\n\t"
      "s_sub_u32 s23, s23, 1\n\t"
      "s_cselect_b32 s48, -13, 1\n\t"
      "s_cselect_b32 s49, -1, 0\n\t"
      "s_mul_i32 s48, s48, 28672\n\t"
      "s_add_u32 s6, s6, s48\n\t"
      "s_addc_u32 s7, s7, s49\n\t"
      "v_add_u32_e32 v225, 57344, v225\n\t"
      "v_add_u32_e32 v226, 57344, v226\n\t"
      "v_add_u32_e32 v227, 57344, v227\n\t"
      "v_add_u32_e32 v228, 57344, v228\n\t"
      "s_cbranch_vccnz WW6_%=\n\t"
      "s_waitcnt vmcnt(4)\n\t"
      "s_branch WX6_%=\n\t"
      "WW6_%=:\n\t"
      "s_waitcnt vmcnt(3)\n\t"
      "WX6_%=:\n\t"
      "s_waitcnt lgkmcnt(0)\n\t"
      "s_barrier\n\t"
      "ds_read_b64_tr_b16 v[96:97], v225\n\t"
      "ds_read_b64_tr_b16 v[98:99], v225 offset:512\n\t"
      "ds_read_b64_tr_b16 v[100:101], v226\n\t"
      "ds_read_b64_tr_b16 v[102:103], v226 offset:512\n\t"
      "ds_read_b64_tr_b16 v[104:105], v227\n\t"
      "ds_read_b64_tr_b16 v[106:107], v227 offset:512\n\t"
      "ds_read_b64_tr_b16 v[108:109], v228\n\t"
      "ds_read_b64_tr_b16 v[110:111], v228 offset:512\n\t"
      "s_add_u32 m0, s21, 0\n\t"
      "s_nop 0\n\t"
      "global_load_lds_dwordx4 v224, s[6:7]\n\t"
      "global_load_lds_dwordx4 v221, s[6:7] offset:2048\n\t"
      "s_add_u32 m0, s21, 4096\n\t"
      "s_nop 0\n\t"
      "global_load_lds_dwordx4 v222, s[6:7]\n\t"
      "s_cbranch_vccnz WR8_%=\n\t"
      "global_load_lds_dwordx4 v223, s[6:7] offset:2048\n\t"
      "WR8_%=:\n\t"
      "s_sub_u32 s23, s23, 1\n\t"
      "s_cselect_b32 s48, -13, 1\n\t"
      "s_cselect_b32 s49, -1, 0\n\t"
      "s_mul_i32 s48, s48, 28672\n\t"
      "s_add_u32 s6, s6, s48\n\t"
      "s_addc_u32 s7, s7, s49\n\t"
      "s_cbranch_vccnz WW7_%=\n\t"
      "s_waitcnt vmcnt(4)\n\t"
      "s_branch WX7_%=\n\t"
      "WW7_%=:\n\t"
      "s_waitcnt vmcnt(3)\n\t"
      "WX7_%=:\n\t"
      "s_waitcnt lgkmcnt(0)\n\t"
      "s_barrier\n\t"
      "ds_read_b64_tr_b16 v[112:113], v225 offset:28672\n\t"
      "ds_read_b64_tr_b16 v[114:115], v225 offset:29184\n\t"
      "ds_read_b64_tr_b16 v[116:117], v226 offset:28672\n\t"
      "ds_read_b64_tr_b16 v[118:119], v226 offset:29184\n\t"
      "ds_read_b64_tr_b16 v[120:121], v227 offset:28672\n\t"
      "ds_read_b64_tr_b16 v[122:123], v227 offset:29184\n\t"
      "ds_read_b64_tr_b16 v[124:125], v228 offset:28672\n\t"
      "ds_read_b64_tr_b16 v[126:127], v228 offset:29184\n\t"
      "s_add_u32 m0, s21, 28672\n\t"
      "s_nop 0\n\t"
      "global_load_lds_dwordx4 v224, s[6:7]\n\t"
      "global_load_lds_dwordx4 v221, s[6:7] offset:2048\n\t"
      "s_add_u32 m0, s21, 32768\n\t"
      "s_nop 0\n\t"
      "global_load_lds_dwordx4 v222, s[6:7]\n\t"
      "s_cbranch_vccnz WR9_%=\n\t"
      "global_load_lds_dwordx4 v223, s[6:7] offset:2048\n\t"
      "WR9_%=:\n\t"
      "s_sub_u32 s23, s23, 1\n\t"
      "s_cselect_b32 s48, -13, 1\n\t"
      "s_cselect_b32 s49, -1, 0\n\t"
      "s_mul_i32 s48, s48, 28672\n\t"
      "s_add_u32 s6, s6, s48\n\t"
      "s_addc_u32 s7, s7, s49\n\t"
      "v_add_u32_e32 v225, 4294909952, v225\n\t"
      "v_add_u32_e32 v226, 4294909952, v226\n\t"
      "v_add_u32_e32 v227, 4294909952, v227\n\t"
      "v_add_u32_e32 v228, 4294909952, v228\n\t"
      "s_cbranch_vccnz WW8_%=\n\t"
      "s_waitcnt vmcnt(4)\n\t"
      "s_branch WX8_%=\n\t"
      "WW8_%=:\n\t"
      "s_waitcnt vmcnt(3)\n\t"
      "WX8_%=:\n\t"
      "s_waitcnt lgkmcnt(0)\n\t"
      "s_barrier\n\t"
      "ds_read_b64_tr_b16 v[128:129], v225\n\t"
      "ds_read_b64_tr_b16 v[130:131], v225 offset:512\n\t"
      "ds_read_b64_tr_b16 v[132:133], v226\n\t"
      "ds_read_b64_tr_b16 v[134:135], v226 offset:512\n\t"
      "ds_read_b64_tr_b16 v[136:137], v227\n\t"
      "ds_read_b64_tr_b16 v[138:139], v227 offset:512\n\t"
      "ds_read_b64_tr_b16 v[140:141], v228\n\t"
      "ds_read_b64_tr_b16 v[142:143], v228 offset:512\n\t"
      "s_add_u32 m0, s21, 57344\n\t"
      "s_nop 0\n\t"
      "global_load_lds_dwordx4 v224, s[6:7]\n\t"
      "global_load_lds_dwordx4 v221, s[6:7] offset:2048\n\t"
      "s_add_u32 m0, s21, 61440\n\t"
      "s_nop 0\n\t"
      "global_load_lds_dwordx4 v222, s[6:7]\n\t"
      "s_cbranch_vccnz WR10_%=\n\t"
      "global_load_lds_dwordx4 v223, s[6:7] offset:2048\n\t"
      "WR10_%=:\n\t"
      "s_sub_u32 s23, s23, 1\n\t"
      "s_cselect_b32 s48, -13, 1\n\t"
      "s_cselect_b32 s49, -1, 0\n\t"
      "s_mul_i32 s48, s48, 28672\n\t"
      "s_add_u32 s6, s6, s48\n\t"
      "s_addc_u32 s7, s7, s49\n\t"
      "s_cbranch_vccnz WW9_%=\n\t"
      "s_waitcnt vmcnt(4)\n\t"
      "s_branch WX9_%=\n\t"
      "WW9_%=:\n\t"
      "s_waitcnt vmcnt(3)\n\t"
      "WX9_%=:\n\t"
      "s_waitcnt lgkmcnt(0)\n\t"
      "s_barrier\n\t"
      "ds_read_b64_tr_b16 v[144:145], v225 offset:28672\n\t"
      "ds_read_b64_tr_b16 v[146:147], v225 offset:29184\n\t"
      "ds_read_b64_tr_b16 v[148:149], v226 offset:28672\n\t"
      "ds_read_b64_tr_b16 v[150:151], v226 offset:29184\n\t"
      "ds_read_b64_tr_b16 v[152:153], v227 offset:28672\n\t"
      "ds_read_b64_tr_b16 v[154:155], v227 offset:29184\n\t"
      "ds_read_b64_tr_b16 v[156:157], v228 offset:28672\n\t"
      "ds_read_b64_tr_b16 v[158:159], v228 offset:29184\n\t"
      "s_add_u32 m0, s21, 86016\n\t"
      "s_nop 0\n\t"
      "global_load_lds_dwordx4 v224, s[6:7]\n\t"
      "global_load_lds_dwordx4 v221, s[6:7] offset:2048\n\t"
      "s_add_u32 m0, s21, 90112\n\t"
      "s_nop 0\n\t"
      "global_load_lds_dwordx4 v222, s[6:7]\n\t"
      "s_cbranch_vccnz WR11_%=\n\t"
      "global_load_lds_dwordx4 v223, s[6:7] offset:2048\n\t"
      "WR11_%=:\n\t"
      "s_sub_u32 s23, s23, 1\n\t"
      "s_cselect_b32 s48, -13, 1\n\t"
      "s_cselect_b32 s49, -1, 0\n\t"
      "s_mul_i32 s48, s48, 28672\n\t"
      "s_add_u32 s6, s6, s48\n\t"
      "s_addc_u32 s7, s7, s49\n\t"
      "v_add_u32_e32 v225, 57344, v225\n\t"
      "v_add_u32_e32 v226, 57344, v226\n\t"
      "v_add_u32_e32 v227, 57344, v227\n\t"
      "v_add_u32_e32 v228, 57344, v228\n\t"
      "v_min_u32_e32 v184, 32, v236\n\t"
      "v_add_u32_e32 v184, s28, v184\n\t"
      "global_load_dwordx4 v[180:183], v184, s[8:9] sc1\n\t"
      "s_cbranch_vccnz WW10_%=\n\t"
      "s_waitcnt vmcnt(5)\n\t"
      "s_branch WX10_%=\n\t"
      "WW10_%=:\n\t"
      "s_waitcnt vmcnt(4)\n\t"
      "WX10_%=:\n\t"
      "s_waitcnt lgkmcnt(0)\n\t"
      "s_barrier\n\t"
      "ds_read_b64_tr_b16 v[188:189], v225\n\t"
      "ds_read_b64_tr_b16 v[190:191], v225 offset:512\n\t"
      "ds_read_b64_tr_b16 v[192:193], v226\n\t"
      "ds_read_b64_tr_b16 v[194:195], v226 offset:512\n\t"
      "ds_read_b64_tr_b16 v[196:197], v227\n\t"
      "ds_read_b64_tr_b16 v[198:199], v227 offset:512\n\t"
      "ds_read_b64_tr_b16 v[200:201], v228\n\t"
      "ds_read_b64_tr_b16 v[202:203], v228 offset:512\n\t"
      "s_add_u32 m0, s21, 0\n\t"
      "s_nop 0\n\t"
      "global_load_lds_dwordx4 v224, s[6:7]\n\t"
      "global_load_lds_dwordx4 v221, s[6:7] offset:2048\n\t"
      "s_add_u32 m0, s21, 4096\n\t"
      "s_nop 0\n\t"
      "global_load_lds_dwordx4 v222, s[6:7]\n\t"
      "s_cbranch_vccnz WR12_%=\n\t"
      "global_load_lds_dwordx4 v223, s[6:7] offset:2048\n\t"
      "WR12_%=:\n\t"
      "s_sub_u32 s23, s23, 1\n\t"
      "s_cselect_b32 s48, -13, 1\n\t"
      "s_cselect_b32 s49, -1, 0\n\t"
      "s_mul_i32 s48, s48, 28672\n\t"
      "s_add_u32 s6, s6, s48\n\t"
      "s_addc_u32 s7, s7, s49\n\t"
      "s_cbranch_vccnz WW11_%=\n\t"
      "s_waitcnt vmcnt(5)\n\t"
      "s_branch WX11_%=\n\t"
      "WW11_%=:\n\t"
      "s_waitcnt vmcnt(4)\n\t"
      "WX11_%=:\n\t"
      "s_waitcnt lgkmcnt(0)\n\t"
      "s_barrier\n\t"
      "ds_read_b64_tr_b16 v[204:205], v225 offset:28672\n\t"
      "ds_read_b64_tr_b16 v[206:207], v225 offset:29184\n\t"
      "ds_read_b64_tr_b16 v[208:209], v226 offset:28672\n\t"
      "ds_read_b64_tr_b16 v[210:211], v226 offset:29184\n\t"
      "ds_read_b64_tr_b16 v[212:213], v227 offset:28672\n\t"
      "ds_read_b64_tr_b16 v[214:215], v227 offset:29184\n\t"
      "ds_read_b64_tr_b16 v[216:217], v228 offset:28672\n\t"
      "ds_read_b64_tr_b16 v[218:219], v228 offset:29184\n\t"
      "s_add_u32 m0, s21, 28672\n\t"
      "s_nop 0\n\t"
      "global_load_lds_dwordx4 v224, s[6:7]\n\t"
      "global_load_lds_dwordx4 v221, s[6:7] offset:2048\n\t"
      "s_add_u32 m0, s21, 32768\n\t"
      "s_nop 0\n\t"
      "global_load_lds_dwordx4 v222, s[6:7]\n\t"
      "s_cbranch_vccnz WR13_%=\n\t"
      "global_load_lds_dwordx4 v223, s[6:7] offset:2048\n\t"
      "WR13_%=:\n\t"
      "v_add_u32_e32 v225, 4294909952, v225\n\t"
      "v_add_u32_e32 v226, 4294909952, v226\n\t"
      "v_add_u32_e32 v227, 4294909952, v227\n\t"
      "v_add_u32_e32 v228, 4294909952, v228\n\t"
      "s_waitcnt vmcnt(6)\n\t"
      "v_xor_b32_e32 v182, v182, v180\n\t"
      "v_xor_b32_e32 v183, v183, v181\n\t"
      "v_cmp_ne_u64_e32 vcc, s[12:13], v[182:183]\n\t"
      "s_cbranch_vccnz PWR_%=\n\t"
      "PWB_%=:\n\t"
      "v_readlane_b32 s48, v180, 0\n\t"
      "v_readlane_b32 s28, v180, 1\n\t"
      "v_readlane_b32 s29, v180, 2\n\t"
      "v_mov_b32_e32 v220, s12\n\t"
      "v_mov_b32_e32 v221, s13\n\t"
      "s_xor_b32 s10, s10, s48\n\t"
      "v_xor_b32_e32 v231, s28, v231\n\t"
      "v_xor_b32_e32 v237, s28, v237\n\t"
      "v_xor_b32_e32 v246, s28, v246\n\t"
      "v_xor_b32_e32 v247, s28, v247\n\t"
      "v_xor_b32_e32 v239, s29, v239\n\t"
      "s_waitcnt lgkmcnt(0)\n\t"
      "s_waitcnt vmcnt(0)\n\t"
      "s_mov_b32 s51, 0\n\t"
      "s_branch QE_%=\n\t"
      ".p2align 6\n\t"
      "QL_%=:\n\t"
      "s_sleep 2\n\t"
      "s_add_u32 s51, s51, 1\n\t"
      "s_cmp_lt_u32 s51, 262144\n\t"
      "s_cbranch_scc0 QX_%=\n\t"
      "QE_%=:\n\t"
      "s_mov_b64 exec, 3\n\t"
      "global_load_dwordx2 v[176:177], v246, s[8:9] sc1\n\t"
      "s_waitcnt vmcnt(0)\n\t"
      "v_cmp_ne_u64_e32 vcc, s[12:13], v[176:177]\n\t"
      "s_mov_b64 exec, -1\n\t"
      "s_cbranch_vccnz QL_%=\n\t"
      "QX_%=:\n\t"
      "s_mov_b32 s51, 0\n\t"
      "s_branch OE_%=\n\t"
      "OL_%=:\n\t"
      "s_sleep 2\n\t"
      "s_add_u32 s51, s51, 1\n\t"
      "s_cmp_lt_u32 s51, 262144\n\t"
      "s_cbranch_scc0 OX_%=\n\t"
      "OE_%=:\n\t"
      "global_load_dwordx4 v[176:179], v236, s[10:11] sc1\n\t"
      "global_load_dwordx4 v[180:183], v236, s[10:11] offset:1024 sc1\n\t"
      "global_load_dword v186, v247, s[8:9] sc1\n\t"
      "s_waitcnt vmcnt(2)\n\t"
      "ds_write_b64 v240, v[176:177]\n\t"
      "s_mov_b64 exec, s[44:45]\n\t"
      "ds_write_b64 v241, v[178:179]\n\t"
      "s_mov_b64 exec, -1\n\t"
      "v_add3_u32 v184, v176, v177, v178\n\t"
      "v_add_u32_e32 v184, v184, v179\n\t"
      "s_waitcnt vmcnt(1)\n\t"
      "ds_write_b64 v242, v[180:181]\n\t"
      "s_mov_b64 exec, s[44:45]\n\t"
      "ds_write_b64 v243, v[182:183]\n\t"
      "s_mov_b64 exec, -1\n\t"
      "v_add3_u32 v185, v180, v181, v182\n\t"
      "v_add_u32_e32 v185, v185, v183\n\t"
      "s_nop 1\n\t"
      "v_permlane32_swap_b32_e32 v184, v185\n\t"
      "v_add_u32_e32 v184, v184, v185\n\t"
      "v_mov_b32_e32 v185, v184\n\t"
      "s_nop 1\n\t"
      "v_permlane16_swap_b32_e32 v184, v185\n\t"
      "v_add3_u32 v184, v184, v185, s12\n\t"
      "s_waitcnt vmcnt(0) lgkmcnt(0)\n\t"
      "v_cmp_ne_u32_e32 vcc, v184, v186\n\t"
      "s_cbranch_vccnz OL_%=\n\t"
      "OX_%=:\n\t"
      "s_mov_b64 vcc, s[46:47]\n\t"
      "s_waitcnt lgkmcnt(0)\n\t"
      "s_barrier\n\t"
      "ds_read_b128 v[176:179], v230\n\t"
      "ds_read_b128 v[180:183], v230 offset:64\n\t"
      "ds_read_b128 v[184:187], v230 offset:128\n\t"
      "s_waitcnt lgkmcnt(2)\n\t"
      "v_mfma_f32_16x16x32_bf16 v[160:163], v[176:179], v[0:3], v[160:163]\n\t"
      "v_mfma_f32_16x16x32_bf16 v[164:167], v[176:179], v[4:7], v[164:167]\n\t"
      "v_mfma_f32_16x16x32_bf16 v[168:171], v[176:179], v[8:11], v[168:171]\n\t"
      "s_cbranch_vccnz M0_%=\n\t"
      "v_mfma_f32_16x16x32_bf16 v[172:175], v[176:179], v[12:15], v[172:175]\n\t"
      "M0_%=:\n\t"
      "s_cbranch_vccz SN4_%=\n\t"
      "global_store_dwordx4 v231, v[248:251], s[14:15] sc1\n\t"
      "SN4_%=:\n\t"
      "ds_read_b128 v[176:179], v230 offset:192\n\t"
      "s_waitcnt lgkmcnt(2)\n\t"
      "v_mfma_f32_16x16x32_bf16 v[160:163], v[180:183], v[16:19], v[160:163]\n\t"
      "v_mfma_f32_16x16x32_bf16 v[164:167], v[180:183], v[20:23], v[164:167]\n\t"
      "v_mfma_f32_16x16x32_bf16 v[168:171], v[180:183], v[24:27], v[168:171]\n\t"
      "s_cbranch_vccnz M1_%=\n\t"
      "v_mfma_f32_16x16x32_bf16 v[172:175], v[180:183], v[28:31], v[172:175]\n\t"
      "M1_%=:\n\t"
      "s_mov_b64 exec, s[24:25]\n\t"
      "global_store_dwordx4 v237, v[248:251], s[14:15] sc1\n\t"
      "s_mov_b64 exec, -1\n\t"
      "ds_read_b128 v[180:183], v230 offset:256\n\t"
      "s_waitcnt lgkmcnt(2)\n\t"
      "v_mfma_f32_16x16x32_bf16 v[160:163], v[184:187], v[32:35], v[160:163]\n\t"
      "v_mfma_f32_16x16x32_bf16 v[164:167], v[184:187], v[36:39], v[164:167]\n\t"
      "v_mfma_f32_16x16x32_bf16 v[168:171], v[184:187], v[40:43], v[168:171]\n\t"
      "s_cbranch_vccnz M2_%=\n\t"
      "v_mfma_f32_16x16x32_bf16 v[172:175], v[184:187], v[44:47], v[172:175]\n\t"
      "M2_%=:\n\t"
      "ds_read_b64_tr_b16 v[0:1], v225\n\t"
      "ds_read_b64_tr_b16 v[2:3], v225 offset:512\n\t"
      "s_bitcmp1_b32 s20, 6\n\t"
      "s_cbranch_scc0 SG2_%=\n\t"
      "global_store_dwordx4 v236, v[248:251], s[16:17] sc1\n\t"
      "global_store_dwordx4 v235, v[248:251], s[16:17] sc1\n\t"
      "SG2_%=:\n\t"
      "ds_read_b128 v[184:187], v230 offset:320\n\t"
      "s_waitcnt lgkmcnt(4)\n\t"
      "v_mfma_f32_16x16x32_bf16 v[160:163], v[176:179], v[48:51], v[160:163]\n\t"
      "v_mfma_f32_16x16x32_bf16 v[164:167], v[176:179], v[52:55], v[164:167]\n\t"
      "v_mfma_f32_16x16x32_bf16 v[168:171], v[176:179], v[56:59], v[168:171]\n\t"
      "s_cbranch_vccnz M3_%=\n\t"
      "v_mfma_f32_16x16x32_bf16 v[172:175], v[176:179], v[60:63], v[172:175]\n\t"
      "M3_%=:\n\t"
      "ds_read_b64_tr_b16 v[4:5], v226\n\t"
      "ds_read_b64_tr_b16 v[6:7], v226 offset:512\n\t"
      "ds_read_b128 v[176:179], v230 offset:384\n\t"
      "s_waitcnt lgkmcnt(6)\n\t"
      "v_mfma_f32_16x16x32_bf16 v[160:163], v[180:183], v[64:67], v[160:163]\n\t"
      "v_mfma_f32_16x16x32_bf16 v[164:167], v[180:183], v[68:71], v[164:167]\n\t"
      "v_mfma_f32_16x16x32_bf16 v[168:171], v[180:183], v[72:75], v[168:171]\n\t"
      "s_cbranch_vccnz M4_%=\n\t"
      "v_mfma_f32_16x16x32_bf16 v[172:175], v[180:183], v[76:79], v[172:175]\n\t"
      "M4_%=:\n\t"
      "ds_read_b64_tr_b16 v[8:9], v227\n\t"
      "ds_read_b64_tr_b16 v[10:11], v227 offset:512\n\t"
      "ds_read_b128 v[180:183], v230 offset:448\n\t"
      "s_waitcnt lgkmcnt(6)\n\t"
      "v_mfma_f32_16x16x32_bf16 v[160:163], v[184:187], v[80:83], v[160:163]\n\t"
      "v_mfma_f32_16x16x32_bf16 v[164:167], v[184:187], v[84:87], v[164:167]\n\t"
      "v_mfma_f32_16x16x32_bf16 v[168:171], v[184:187], v[88:91], v[168:171]\n\t"
      "s_cbranch_vccnz M5_%=\n\t"
      "v_mfma_f32_16x16x32_bf16 v[172:175], v[184:187], v[92:95], v[172:175]\n\t"
      "M5_%=:\n\t"
      "ds_read_b64_tr_b16 v[12:13], v228\n\t"
      "ds_read_b64_tr_b16 v[14:15], v228 offset:512\n\t"
      "s_mov_b32 exec_lo, 0xffff\n\t"
      "s_mov_b32 exec_hi, 0x0\n\t"
      "s_mov_b32 m0, 163328\n\t"
      "s_nop 0\n\t"
      "global_load_lds_dword v238, s[4:5]\n\t"
      "s_mov_b64 exec, -1\n\t"
      "ds_read_b128 v[184:187], v230 offset:512\n\t"
      "s_waitcnt lgkmcnt(6)\n\t"
      "v_mfma_f32_16x16x32_bf16 v[160:163], v[176:179], v[96:99], v[160:163]\n\t"
      "v_mfma_f32_16x16x32_bf16 v[164:167], v[176:179], v[100:103], v[164:167]\n\t"
      "v_mfma_f32_16x16x32_bf16 v[168:171], v[176:179], v[104:107], v[168:171]\n\t"
      "s_cbranch_vccnz M6_%=\n\t"
      "v_mfma_f32_16x16x32_bf16 v[172:175], v[176:179], v[108:111], v[172:175]\n\t"
      "M6_%=:\n\t"
      "ds_read_b64_tr_b16 v[16:17], v225 offset:28672\n\t"
      "ds_read_b64_tr_b16 v[18:19], v225 offset:29184\n\t"
      "ds_read_b128 v[176:179], v230 offset:576\n\t"
      "s_waitcnt lgkmcnt(6)\n\t"
      "v_mfma_f32_16x16x32_bf16 v[160:163], v[180:183], v[112:115], v[160:163]\n\t"
      "v_mfma_f32_16x16x32_bf16 v[164:167], v[180:183], v[116:119], v[164:167]\n\t"
      "v_mfma_f32_16x16x32_bf16 v[168:171], v[180:183], v[120:123], v[168:171]\n\t"
      "s_cbranch_vccnz M7_%=\n\t"
      "v_mfma_f32_16x16x32_bf16 v[172:175], v[180:183], v[124:127], v[172:175]\n\t"
      "M7_%=:\n\t"
      "ds_read_b64_tr_b16 v[20:21], v226 offset:28672\n\t"
      "ds_read_b64_tr_b16 v[22:23], v226 offset:29184\n\t"
      "ds_read_b128 v[180:183], v230 offset:640\n\t"
      "s_waitcnt lgkmcnt(6)\n\t"
      "v_mfma_f32_16x16x32_bf16 v[160:163], v[184:187], v[128:131], v[160:163]\n\t"
      "v_mfma_f32_16x16x32_bf16 v[164:167], v[184:187], v[132:135], v[164:167]\n\t"
      "v_mfma_f32_16x16x32_bf16 v[168:171], v[184:187], v[136:139], v[168:171]\n\t"
      "s_cbranch_vccnz M8_%=\n\t"
      "v_mfma_f32_16x16x32_bf16 v[172:175], v[184:187], v[140:143], v[172:175]\n\t"
      "M8_%=:\n\t"
      "ds_read_b64_tr_b16 v[24:25], v227 offset:28672\n\t"
      "ds_read_b64_tr_b16 v[26:27], v227 offset:29184\n\t"
      "ds_read_b128 v[184:187], v230 offset:704\n\t"
      "s_waitcnt lgkmcnt(6)\n\t"
      "v_mfma_f32_16x16x32_bf16 v[160:163], v[176:179], v[144:147], v[160:163]\n\t"
      "v_mfma_f32_16x16x32_bf16 v[164:167], v[176:179], v[148:151], v[164:167]\n\t"
      "v_mfma_f32_16x16x32_bf16 v[168:171], v[176:179], v[152:155], v[168:171]\n\t"
      "s_cbranch_vccnz M9_%=\n\t"
      "v_mfma_f32_16x16x32_bf16 v[172:175], v[176:179], v[156:159], v[172:175]\n\t"
      "M9_%=:\n\t"
      "ds_read_b64_tr_b16 v[28:29], v228 offset:28672\n\t"
      "ds_read_b64_tr_b16 v[30:31], v228 offset:29184\n\t"
      "ds_read_b128 v[176:179], v230 offset:768\n\t"
      "s_waitcnt lgkmcnt(6)\n\t"
      "v_mfma_f32_16x16x32_bf16 v[160:163], v[180:183], v[188:191], v[160:163]\n\t"
      "v_mfma_f32_16x16x32_bf16 v[164:167], v[180:183], v[192:195], v[164:167]\n\t"
      "v_mfma_f32_16x16x32_bf16 v[168:171], v[180:183], v[196:199], v[168:171]\n\t"
      "s_cbranch_vccnz M10_%=\n\t"
      "v_mfma_f32_16x16x32_bf16 v[172:175], v[180:183], v[200:203], v[172:175]\n\t"
      "M10_%=:\n\t"
      "ds_read_b128 v[180:183], v230 offset:832\n\t"
      "s_waitcnt lgkmcnt(4)\n\t"
      "v_mfma_f32_16x16x32_bf16 v[160:163], v[184:187], v[204:207], v[160:163]\n\t"
      "v_mfma_f32_16x16x32_bf16 v[164:167], v[184:187], v[208:211], v[164:167]\n\t"
      "v_mfma_f32_16x16x32_bf16 v[168:171], v[184:187], v[212:215], v[168:171]\n\t"
      "s_cbranch_vccnz M11_%=\n\t"
      "v_mfma_f32_16x16x32_bf16 v[172:175], v[184:187], v[216:219], v[172:175]\n\t"
      "M11_%=:\n\t"
      "s_waitcnt lgkmcnt(1)\n\t"
      "v_mfma_f32_16x16x32_bf16 v[160:163], v[176:179], v[0:3], v[160:163]\n\t"
      "v_mfma_f32_16x16x32_bf16 v[164:167], v[176:179], v[4:7], v[164:167]\n\t"
      "v_mfma_f32_16x16x32_bf16 v[168:171], v[176:179], v[8:11], v[168:171]\n\t"
      "s_cbranch_vccnz M12_%=\n\t"
      "v_mfma_f32_16x16x32_bf16 v[172:175], v[176:179], v[12:15], v[172:175]\n\t"
      "M12_%=:\n\t"
      "s_waitcnt lgkmcnt(0)\n\t"
      "v_mfma_f32_16x16x32_bf16 v[160:163], v[180:183], v[16:19], v[160:163]\n\t"
      "v_mfma_f32_16x16x32_bf16 v[164:167], v[180:183], v[20:23], v[164:167]\n\t"
      "v_mfma_f32_16x16x32_bf16 v[168:171], v[180:183], v[24:27], v[168:171]\n\t"
      "s_cbranch_vccnz M13_%=\n\t"
      "v_mfma_f32_16x16x32_bf16 v[172:175], v[180:183], v[28:31], v[172:175]\n\t"
      "M13_%=:\n\t"
      "s_nop 5\n\t"
      "v_pk_add_f32 v[188:189], v[160:161], v[164:165]\n\t"
      "v_pk_mul_f32 v[192:193], v[160:161], v[160:161]\n\t"
      "v_pk_add_f32 v[190:191], v[162:163], v[166:167]\n\t"
      "v_pk_mul_f32 v[194:195], v[162:163], v[162:163]\n\t"
      "v_pk_fma_f32 v[192:193], v[164:165], v[164:165], v[192:193]\n\t"
      "v_pk_fma_f32 v[194:195], v[166:167], v[166:167], v[194:195]\n\t"
      "v_pk_add_f32 v[188:189], v[188:189], v[168:169]\n\t"
      "v_pk_fma_f32 v[192:193], v[168:169], v[168:169], v[192:193]\n\t"
      "v_pk_add_f32 v[190:191], v[190:191], v[170:171]\n\t"
      "v_pk_fma_f32 v[194:195], v[170:171], v[170:171], v[194:195]\n\t"
      "s_cbranch_vccnz SB1_%=\n\t"
      "v_pk_add_f32 v[188:189], v[188:189], v[172:173]\n\t"
      "v_pk_fma_f32 v[192:193], v[172:173], v[172:173], v[192:193]\n\t"
      "v_pk_add_f32 v[190:191], v[190:191], v[174:175]\n\t"
      "v_pk_fma_f32 v[194:195], v[174:175], v[174:175], v[194:195]\n\t"
      "SB1_%=:\n\t"
      "v_add_f32_dpp v196, v188, v188 row_ror:8 row_mask:0xf bank_mask:0x3\n\t"
      "v_add_f32_dpp v197, v189, v189 row_ror:8 row_mask:0xf bank_mask:0x3\n\t"
      "v_add_f32_dpp v198, v192, v192 row_ror:8 row_mask:0xf bank_mask:0x3\n\t"
      "v_add_f32_dpp v199, v193, v193 row_ror:8 row_mask:0xf bank_mask:0x3\n\t"
      "v_add_f32_dpp v196, v190, v190 row_ror:8 row_mask:0xf bank_mask:0xc\n\t"
      "v_add_f32_dpp v197, v191, v191 row_ror:8 row_mask:0xf bank_mask:0xc\n\t"
      "v_add_f32_dpp v198, v194, v194 row_ror:8 row_mask:0xf bank_mask:0xc\n\t"
      "v_add_f32_dpp v199, v195, v195 row_ror:8 row_mask:0xf bank_mask:0xc\n\t"
      "v_add_f32_dpp v188, v196, v196 row_shl:4 row_mask:0xf bank_mask:0x5\n\t"
      "v_add_f32_dpp v189, v198, v198 row_shl:4 row_mask:0xf bank_mask:0x5\n\t"
      "s_nop 0\n\t"
      "v_add_f32_dpp v188, v197, v197 row_shr:4 row_mask:0xf bank_mask:0xa\n\t"
      "v_add_f32_dpp v189, v199, v199 row_shr:4 row_mask:0xf bank_mask:0xa\n\t"
      "s_nop 0\n\t"
      "v_add_f32_dpp v188, v188, v188 quad_perm:[1,0,3,2] row_mask:0xf bank_mask:0xf\n\t"
      "v_add_f32_dpp v189, v189, v189 quad_perm:[1,0,3,2] row_mask:0xf bank_mask:0xf\n\t"
      "s_nop 0\n\t"
      "v_add_f32_dpp v188, v188, v188 quad_perm:[2,3,0,1] row_mask:0xf bank_mask:0xf\n\t"
      "v_add_f32_dpp v189, v189, v189 quad_perm:[2,3,0,1] row_mask:0xf bank_mask:0xf\n\t"
      "ds_write_b64 v232, v[188:189]\n\t"
      "s_bitcmp1_b32 s20, 4\n\t"
      "s_cbranch_scc0 HI_%=\n\t"
      "s_mov_b64 exec, 255\n\t"
      "global_store_dwordx2 v239, v[220:221], s[8:9] sc1\n\t"
      "s_mov_b64 exec, -1\n\t"
      "HI_%=:\n\t"
      "s_waitcnt vmcnt(1)\n\t"
      "s_waitcnt lgkmcnt(0)\n\t"
      "s_barrier\n\t"
      "ds_read_b128 v[188:191], v233\n\t"
      "s_waitcnt lgkmcnt(0)\n\t"
      "v_pk_add_f32 v[188:189], v[188:189], v[190:191]\n\t"
      "s_nop 1\n\t"
      "v_add_f32_dpp v188, v188, v188 row_ror:4 row_mask:0xf bank_mask:0xf\n\t"
      "v_add_f32_dpp v189, v189, v189 row_ror:4 row_mask:0xf bank_mask:0xf\n\t"
      "s_nop 0\n\t"
      "v_add_f32_dpp v188, v188, v188 row_ror:8 row_mask:0xf bank_mask:0xf\n\t"
      "v_add_f32_dpp v189, v189, v189 row_ror:8 row_mask:0xf bank_mask:0xf\n\t"
      "v_mul_f32_e32 v188, 0x3b124925, v188\n\t"
      "v_mul_f32_e32 v189, 0x3b124925, v189\n\t"
      "v_fma_f32 v189, -v188, v188, v189\n\t"
      "v_max_f32_e32 v189, 0, v189\n\t"
      "v_add_f32_e32 v190, 0x3727c5ac, v189\n\t"
      "v_fmac_f32_e32 v189, 0x3727c5ac, v190\n\t"
      "v_rsq_f32_e32 v191, v189\n\t"
      "s_nop 0\n\t"
      "v_mul_f32_e64 v190, -v188, v191\n\t"
      "v_mov_b32_dpp v192, v191 quad_perm:[0,0,0,0] row_mask:0xf bank_mask:0xf\n\t"
      "s_nop 0\n\t"
      "v_mov_b32_dpp v196, v190 quad_perm:[0,0,0,0] row_mask:0xf bank_mask:0xf\n\t"
      "v_mov_b32_dpp v193, v191 quad_perm:[1,1,1,1] row_mask:0xf bank_mask:0xf\n\t"
      "v_mov_b32_dpp v197, v190 quad_perm:[1,1,1,1] row_mask:0xf bank_mask:0xf\n\t"
      "v_mov_b32_dpp v194, v191 quad_perm:[2,2,2,2] row_mask:0xf bank_mask:0xf\n\t"
      "v_mov_b32_dpp v198, v190 quad_perm:[2,2,2,2] row_mask:0xf bank_mask:0xf\n\t"
      "v_mov_b32_dpp v195, v191 quad_perm:[3,3,3,3] row_mask:0xf bank_mask:0xf\n\t"
      "v_mov_b32_dpp v199, v190 quad_perm:[3,3,3,3] row_mask:0xf bank_mask:0xf\n\t"
      "v_pk_fma_f32 v[160:161], v[160:161], v[192:193], v[196:197]\n\t"
      "v_pk_fma_f32 v[162:163], v[162:163], v[194:195], v[198:199]\n\t"
      "v_pk_fma_f32 v[164:165], v[164:165], v[192:193], v[196:197]\n\t"
      "v_pk_fma_f32 v[166:167], v[166:167], v[194:195], v[198:199]\n\t"
      "v_cvt_pk_bf16_f32 v212, v160, v161\n\t"
      "v_cvt_pk_bf16_f32 v213, v162, v163\n\t"
      "v_cvt_pk_bf16_f32 v214, v164, v165\n\t"
      "v_cvt_pk_bf16_f32 v215, v166, v167\n\t"
      "v_min_u32_e32 v212, -2, v212\n\t"
      "v_min_u32_e32 v213, -2, v213\n\t"
      "v_min_u32_e32 v214, -2, v214\n\t"
      "v_min_u32_e32 v215, -2, v215\n\t"
      "s_nop 0\n\t"
      "v_permlane16_swap_b32_e32 v212, v214\n\t"
      "v_permlane16_swap_b32_e32 v213, v215\n\t"
      "global_store_dwordx4 v231, v[212:215], s[14:15] sc1\n\t"
      "v_pk_fma_f32 v[168:169], v[168:169], v[192:193], v[196:197]\n\t"
      "v_pk_fma_f32 v[170:171], v[170:171], v[194:195], v[198:199]\n\t"
      "v_pk_fma_f32 v[172:173], v[172:173], v[192:193], v[196:197]\n\t"
      "v_pk_fma_f32 v[174:175], v[174:175], v[194:195], v[198:199]\n\t"
      "v_cvt_pk_bf16_f32 v216, v168, v169\n\t"
      "v_cvt_pk_bf16_f32 v217, v170, v171\n\t"
      "v_cvt_pk_bf16_f32 v218, v172, v173\n\t"
      "v_cvt_pk_bf16_f32 v219, v174, v175\n\t"
      "v_min_u32_e32 v216, -2, v216\n\t"
      "v_min_u32_e32 v217, -2, v217\n\t"
      "v_min_u32_e32 v218, -2, v218\n\t"
      "v_min_u32_e32 v219, -2, v219\n\t"
      "s_nop 0\n\t"
      "v_permlane16_swap_b32_e32 v216, v218\n\t"
      "v_permlane16_swap_b32_e32 v217, v219\n\t"
      "s_mov_b64 exec, s[24:25]\n\t"
      "global_store_dwordx4 v237, v[216:219], s[14:15] sc1\n\t"
      "s_mov_b64 exec, -1\n\t"
      "s_nop 1\n\t"
      "v_mov_b32_e32 v240, v188\n\t"
      "v_mul_f32_e32 v241, v189, v191\n\t"
      "v_add_u32_e32 v246, 65536, v234\n\t"
      "v_xor_b32_e32 v242, s12, v188\n\t"
      "v_xor_b32_e32 v243, s13, v241\n\t"
      "s_nop 1\n\t"
      "s_branch BFX_%=\n\t"
      "PWR_%=:\n\t"
      "s_mov_b32 s51, 0\n\t"
      "PWL_%=:\n\t"
      "s_add_u32 s51, s51, 1\n\t"
      "s_cmp_lt_u32 s51, 262144\n\t"
      "s_cbranch_scc0 PWX_%=\n\t"
      "s_sleep 1\n\t"
      "v_min_u32_e32 v184, 32, v236\n\t"
      "v_add_u32_e32 v184, s28, v184\n\t"
      "global_load_dwordx4 v[180:183], v184, s[8:9] sc1\n\t"
      "s_waitcnt vmcnt(0)\n\t"
      "v_xor_b32_e32 v182, v182, v180\n\t"
      "v_xor_b32_e32 v183, v183, v181\n\t"
      "v_cmp_ne_u64_e32 vcc, s[12:13], v[182:183]\n\t"
      "s_cbranch_vccnz PWL_%=\n\t"
      "PWX_%=:\n\t"
      "v_and_b32_e32 v180, 0x2000, v180\n\t"
      "s_nop 1\n\t"
      "s_branch PWB_%=\n\t"
      "BFX_%=:\n\t"
      : "+{s[4:19]}"(s), "+{s[20:27]}"(s2), "+{s[28:29]}"(par), "+{s30}"(lw), "+{v245}"(vn), "+{v247}"(vck), "+{v[224:239]}"(v), "+{v[240:243]}"(vo), "+{v246}"(vq)
      :
      : "memory", "vcc", "scc",
        "v0", "v1", "v2", "v3", "v4", "v5", "v6", "v7", "v8", "v9", "v10", "v11", "v12", "v13", "v14", "v15", "v16",
        "v17", "v18", "v19", "v20", "v21", "v22", "v23", "v24", "v25", "v26", "v27", "v28", "v29", "v30", "v31", "v32",
        "v33", "v34", "v35", "v36", "v37", "v38", "v39", "v40", "v41", "v42", "v43", "v44", "v45", "v46", "v47", "v48",
        "v49", "v50", "v51", "v52", "v53", "v54", "v55", "v56", "v57", "v58", "v59", "v60", "v61", "v62", "v63", "v64",
        "v65", "v66", "v67", "v68", "v69", "v70", "v71", "v72", "v73", "v74", "v75", "v76", "v77", "v78", "v79", "v80",
        "v81", "v82", "v83", "v84", "v85", "v86", "v87", "v88", "v89", "v90", "v91", "v92", "v93", "v94", "v95", "v96",
        "v97", "v98", "v99", "v100", "v101", "v102", "v103", "v104", "v105", "v106", "v107", "v108", "v109", "v110",
        "v111", "v112", "v113", "v114", "v115", "v116", "v117", "v118", "v119", "v120", "v121", "v122", "v123", "v124",
        "v125", "v126", "v127", "v128", "v129", "v130", "v131", "v132", "v133", "v134", "v135", "v136", "v137", "v138",
        "v139", "v140", "v141", "v142", "v143", "v144", "v145", "v146", "v147", "v148", "v149", "v150", "v151", "v152",
        "v153", "v154", "v155", "v156", "v157", "v158", "v159", "v160", "v161", "v162", "v163", "v164", "v165", "v166",
        "v167", "v168", "v169", "v170", "v171", "v172", "v173", "v174", "v175", "v176", "v177", "v178", "v179", "v180",
        "v181", "v182", "v183", "v184", "v185", "v186", "v187", "v188", "v189", "v190", "v191", "v192", "v193", "v194",
        "v195", "v196", "v197", "v198", "v199", "v200", "v201", "v202", "v203", "v204", "v205", "v206", "v207", "v208",
        "v209", "v210", "v211", "v212", "v213", "v214", "v215", "v216", "v217", "v218", "v219", "v220", "v221", "v222",
        "v223", "v248", "v249", "v250", "v251", "v252", "v253", "v254", "s40", "s41", "s42", "s43", "s44", "s45", "s46",
        "s47", "s48", "s49", "s50", "s51"
    );
    }
    PIN();
    LANE_BITS(__builtin_amdgcn_mbcnt_hi(~0u, __builtin_amdgcn_mbcnt_lo(~0u, 0u)));
    u32x8 v, s2;
    u32x16 s;
    v[0] = lane * 16;
    u64 kr = (u64)(6 - rn) | ((u64)rot << 32);
    v[1] = lr * 4096 + ((lc ^ swz_tr(lr)) << 4);
    v[2] = wave * 4096 + g * 1024 + q4 * 128 + (((2 * (wave & 3) + (p4 >> 1)) ^ swz_tr(8 * g + q4)) << 4) + (p4 & 1) * 8;
    v[3] = wave * 4096 + (wave & 3) * 1024 + ((lane * 16) ^ (g << 6));
    v[4] = (wave & 3) * 1024 + (((p4 * 16 + (lane >> 2)) * 16) ^ (p4 << 6));
    v[5] = (lane >> 2) * 896 + p4 * 8;
    v[6] = c * 2;
    const unsigned mh = (unsigned)par, mg = (unsigned)(par >> 32);
    v[7] = ST_H4 + xp * 16384 + ((side * 8192) ^ mg) + ((ib & 1) * 28 + ((7 * wave) >> 1) + p4) * 128;
    put(s, 0, (u64)(w2 + st * 128 + (wave * 224 + rn * 32) * 4096));
    put(s, 2, (u64)(ws + WS_G + (unit(blk0, wave, 0) ^ mg)));
    put(s, 4, (u64)(out + (64 * st + 16 * (wave & 3)) * 896 + dg * 128));
    unsigned od = (16 - dg) * 32;
    put(s, 6, (u64)(b2 + (64 * st + 16 * (wave & 3)) * 2));
    put(s, 8, (u64)(ws + WS_ST));
    s[12] = bits;
    s[13] = wave * G_WV;
    put(s2, 0, (u64)(ws + WS_HN + (unit(blk0, st >> 2, 0) ^ mh) + (2 * (st & 3) + ((wave >> 1) & 1)) * 1024));
    s2[2] = ST_FL + (64 * st + 16 * (wave & 3)) * 16;
    s2[3] = UNI(wave & 1 ? ~0u : 0u);
    put(s2, 4, (u64)(ws + WS_EP));
    asm volatile(
      "s_waitcnt lgkmcnt(0)\n\ts_nop 4\n\t"
      "s_lshr_b32 s31, s17, 2\n\t"
      "v_mov_b32_e32 v112, 163600\n\t"
      "ds_read_b128 v[112:115], v112\n\t"
      "v_xor_b32_e32 v248, 64, v241\n\t"
      "v_add_u32_e32 v249, 63488, v241\n\t"
      "v_add_u32_e32 v250, 95232, v248\n\t"
      "v_add_u32_e32 v248, 31744, v248\n\t"
      "v_xor_b32_e32 v251, 32, v242\n\t"
      "v_xor_b32_e32 v252, 64, v242\n\t"
      "v_xor_b32_e32 v253, 96, v242\n\t"
      "s_bitcmp1_b32 s16, 4\n\t"
      "s_cbranch_scc0 GW2_%=\n\t"
      "s_mov_b32 s48, 0xf000f\n\t"
      "s_mov_b32 s49, 0xf000f\n\t"
      "s_mov_b64 exec, s[48:49]\n\t"
      "global_store_dwordx4 v140, v[136:139], s[12:13] sc1\n\t"
      "s_mov_b64 exec, -1\n\t"
      "GW2_%=:\n\t"
      "s_add_u32 m0, s31, 66048\n\t"
      "s_nop 0\n\t"
      "global_load_lds_dwordx4 v241, s[4:5]\n\t"
      "global_load_lds_dwordx4 v248, s[4:5] offset:1024\n\t"
      "global_load_lds_dwordx4 v249, s[4:5] offset:2048\n\t"
      "global_load_lds_dwordx4 v250, s[4:5] offset:3072\n\t"
      "s_sub_u32 s28, s28, 1\n\t"
      "s_cselect_b32 s48, -6, 1\n\t"
      "s_cselect_b32 s49, -1, 0\n\t"
      "s_mul_i32 s48, s48, 131072\n\t"
      "s_add_u32 s4, s4, s48\n\t"
      "s_addc_u32 s5, s5, s49\n\t"
      "global_load_ushort v236, v246, s[10:11]\n\t"
      "s_waitcnt lgkmcnt(0)\n\t"
      "v_readfirstlane_b32 s14, v112\n\t"
      "v_readfirstlane_b32 s15, v113\n\t"
      "v_readfirstlane_b32 s26, v114\n\t"
      "v_readfirstlane_b32 s27, v115\n\t"
      "s_add_u32 s52, s6, 0\n\t"
      "s_addc_u32 s53, s7, 0\n\t"
      "s_add_u32 s54, s6, 131072\n\t"
      "s_addc_u32 s55, s7, 0\n\t"
      "s_add_u32 s56, s6, 262144\n\t"
      "s_addc_u32 s57, s7, 0\n\t"
      "s_add_u32 s58, s6, 393216\n\t"
      "s_addc_u32 s59, s7, 0\n\t"
      "s_add_u32 s60, s6, 524288\n\t"
      "s_addc_u32 s61, s7, 0\n\t"
      "s_add_u32 s62, s6, 655360\n\t"
      "s_addc_u32 s63, s7, 0\n\t"
      "s_add_u32 s64, s6, 786432\n\t"
      "s_addc_u32 s65, s7, 0\n\t"
      "s_add_u32 s48, s29, 0\n\t"
      "s_cmp_ge_u32 s48, 7\n\t"
      "s_cselect_b32 s49, 7, 0\n\t"
      "s_sub_u32 s48, s48, s49\n\t"
      "v_lshl_add_u32 v224, s48, 10, v240\n\t"
      "s_add_u32 s48, s29, 1\n\t"
      "s_cmp_ge_u32 s48, 7\n\t"
      "s_cselect_b32 s49, 7, 0\n\t"
      "s_sub_u32 s48, s48, s49\n\t"
      "v_lshl_add_u32 v225, s48, 10, v240\n\t"
      "s_add_u32 s48, s29, 2\n\t"
      "s_cmp_ge_u32 s48, 7\n\t"
      "s_cselect_b32 s49, 7, 0\n\t"
      "s_sub_u32 s48, s48, s49\n\t"
      "v_lshl_add_u32 v226, s48, 10, v240\n\t"
      "s_add_u32 s48, s29, 3\n\t"
      "s_cmp_ge_u32 s48, 7\n\t"
      "s_cselect_b32 s49, 7, 0\n\t"
      "s_sub_u32 s48, s48, s49\n\t"
      "v_lshl_add_u32 v227, s48, 10, v240\n\t"
      "s_add_u32 s48, s29, 4\n\t"
      "s_cmp_ge_u32 s48, 7\n\t"
      "s_cselect_b32 s49, 7, 0\n\t"
      "s_sub_u32 s48, s48, s49\n\t"
      "v_lshl_add_u32 v228, s48, 10, v240\n\t"
      "s_add_u32 s48, s29, 5\n\t"
      "s_cmp_ge_u32 s48, 7\n\t"
      "s_cselect_b32 s49, 7, 0\n\t"
      "s_sub_u32 s48, s48, s49\n\t"
      "v_lshl_add_u32 v229, s48, 10, v240\n\t"
      "s_add_u32 s48, s29, 6\n\t"
      "s_cmp_ge_u32 s48, 7\n\t"
      "s_cselect_b32 s49, 7, 0\n\t"
      "s_sub_u32 s48, s48, s49\n\t"
      "v_lshl_add_u32 v230, s48, 10, v240\n\t"
      "v_xor_b32_e32 v220, 1024, v243\n\t"
      "v_xor_b32_e32 v221, 2048, v243\n\t"
      "v_xor_b32_e32 v222, 3072, v243\n\t"
      "s_add_u32 s66, s8, s30\n\t"
      "s_addc_u32 s67, s9, 0\n\t"
      "v_lshrrev_b32_e32 v112, 4, v240\n\t"
      "v_and_b32_e32 v113, 15, v112\n\t"
      "v_lshrrev_b32_e32 v112, 4, v112\n\t"
      "s_lshr_b32 s48, s17, 10\n\t"
      "s_and_b32 s48, s48, 16\n\t"
      "v_lshlrev_b32_e32 v112, 3, v112\n\t"
      "v_sub_u32_e32 v112, v113, v112\n\t"
      "v_add_u32_e32 v112, s48, v112\n\t"
      "v_mov_b32_e32 v114, 0xffff\n\t"
      "v_mov_b32_e32 v115, 0xffff0000\n\t"
      "v_cmp_eq_u32_e32 vcc, 0, v112\n\t"
      "s_nop 1\n\t"
      "v_cndmask_b32_e32 v216, 0, v114, vcc\n\t"
      "v_cmp_eq_u32_e32 vcc, 1, v112\n\t"
      "s_nop 1\n\t"
      "v_cndmask_b32_e32 v216, v216, v115, vcc\n\t"
      "v_cmp_eq_u32_e32 vcc, 2, v112\n\t"
      "s_nop 1\n\t"
      "v_cndmask_b32_e32 v217, 0, v114, vcc\n\t"
      "v_cmp_eq_u32_e32 vcc, 3, v112\n\t"
      "s_nop 1\n\t"
      "v_cndmask_b32_e32 v217, v217, v115, vcc\n\t"
      "v_cmp_eq_u32_e32 vcc, 4, v112\n\t"
      "s_nop 1\n\t"
      "v_cndmask_b32_e32 v218, 0, v114, vcc\n\t"
      "v_cmp_eq_u32_e32 vcc, 5, v112\n\t"
      "s_nop 1\n\t"
      "v_cndmask_b32_e32 v218, v218, v115, vcc\n\t"
      "v_cmp_eq_u32_e32 vcc, 6, v112\n\t"
      "s_nop 1\n\t"
      "v_cndmask_b32_e32 v219, 0, v114, vcc\n\t"
      "v_cmp_eq_u32_e32 vcc, 7, v112\n\t"
      "s_nop 1\n\t"
      "v_cndmask_b32_e32 v219, v219, v115, vcc\n\t"
      "v_and_b32_e32 v116, 0xf0, v240\n\t"
      "v_add_u32_e32 v116, s22, v116\n\t"
      "v_mov_b64_e32 v[184:185], 0\n\t"
      "v_mov_b64_e32 v[186:187], 0\n\t"
      "v_mov_b64_e32 v[188:189], 0\n\t"
      "v_mov_b64_e32 v[190:191], 0\n\t"
      "v_mov_b64_e32 v[192:193], 0\n\t"
      "v_mov_b64_e32 v[194:195], 0\n\t"
      "v_mov_b64_e32 v[196:197], 0\n\t"
      "v_mov_b64_e32 v[198:199], 0\n\t"
      "v_mov_b64_e32 v[200:201], 0\n\t"
      "v_mov_b64_e32 v[202:203], 0\n\t"
      "v_mov_b64_e32 v[204:205], 0\n\t"
      "v_mov_b64_e32 v[206:207], 0\n\t"
      "v_mov_b64_e32 v[208:209], 0\n\t"
      "v_mov_b64_e32 v[210:211], 0\n\t"
      "s_waitcnt vmcnt(5)\n\t"
      "v_add_u32_e32 v242, 66048, v242\n\t"
      "v_add_u32_e32 v251, 66048, v251\n\t"
      "v_add_u32_e32 v252, 66048, v252\n\t"
      "v_add_u32_e32 v253, 66048, v253\n\t"
      "ds_read_b64_tr_b16 v[0:1], v242 offset:64512\n\t"
      "ds_read_b64_tr_b16 v[2:3], v242 offset:65024\n\t"
      "ds_read_b64_tr_b16 v[4:5], v251 offset:64512\n\t"
      "ds_read_b64_tr_b16 v[6:7], v251 offset:65024\n\t"
      "ds_read_b64_tr_b16 v[8:9], v252 offset:64512\n\t"
      "ds_read_b64_tr_b16 v[10:11], v252 offset:65024\n\t"
      "ds_read_b64_tr_b16 v[12:13], v253 offset:64512\n\t"
      "ds_read_b64_tr_b16 v[14:15], v253 offset:65024\n\t"
      "s_waitcnt vmcnt(1)\n\t"
      "s_add_u32 m0, s31, 0\n\t"
      "s_nop 0\n\t"
      "global_load_lds_dwordx4 v241, s[4:5]\n\t"
      "global_load_lds_dwordx4 v248, s[4:5] offset:1024\n\t"
      "global_load_lds_dwordx4 v249, s[4:5] offset:2048\n\t"
      "global_load_lds_dwordx4 v250, s[4:5] offset:3072\n\t"
      "s_sub_u32 s28, s28, 1\n\t"
      "s_cselect_b32 s48, -6, 1\n\t"
      "s_cselect_b32 s49, -1, 0\n\t"
      "s_mul_i32 s48, s48, 131072\n\t"
      "s_add_u32 s4, s4, s48\n\t"
      "s_addc_u32 s5, s5, s49\n\t"
      "ds_read_b64_tr_b16 v[16:17], v242\n\t"
      "ds_read_b64_tr_b16 v[18:19], v242 offset:512\n\t"
      "ds_read_b64_tr_b16 v[20:21], v251\n\t"
      "ds_read_b64_tr_b16 v[22:23], v251 offset:512\n\t"
      "ds_read_b64_tr_b16 v[24:25], v252\n\t"
      "ds_read_b64_tr_b16 v[26:27], v252 offset:512\n\t"
      "ds_read_b64_tr_b16 v[28:29], v253\n\t"
      "ds_read_b64_tr_b16 v[30:31], v253 offset:512\n\t"
      "s_waitcnt vmcnt(0)\n\t"
      "s_add_u32 m0, s31, 32768\n\t"
      "s_nop 0\n\t"
      "global_load_lds_dwordx4 v241, s[4:5]\n\t"
      "global_load_lds_dwordx4 v248, s[4:5] offset:1024\n\t"
      "global_load_lds_dwordx4 v249, s[4:5] offset:2048\n\t"
      "global_load_lds_dwordx4 v250, s[4:5] offset:3072\n\t"
      "s_sub_u32 s28, s28, 1\n\t"
      "s_cselect_b32 s48, -6, 1\n\t"
      "s_cselect_b32 s49, -1, 0\n\t"
      "s_mul_i32 s48, s48, 131072\n\t"
      "s_add_u32 s4, s4, s48\n\t"
      "s_addc_u32 s5, s5, s49\n\t"
      "v_add_u32_e32 v242, 4294901248, v242\n\t"
      "v_add_u32_e32 v251, 4294901248, v251\n\t"
      "v_add_u32_e32 v252, 4294901248, v252\n\t"
      "v_add_u32_e32 v253, 4294901248, v253\n\t"
      "ds_read_b64_tr_b16 v[32:33], v242\n\t"
      "ds_read_b64_tr_b16 v[34:35], v242 offset:512\n\t"
      "ds_read_b64_tr_b16 v[36:37], v251\n\t"
      "ds_read_b64_tr_b16 v[38:39], v251 offset:512\n\t"
      "ds_read_b64_tr_b16 v[40:41], v252\n\t"
      "ds_read_b64_tr_b16 v[42:43], v252 offset:512\n\t"
      "ds_read_b64_tr_b16 v[44:45], v253\n\t"
      "ds_read_b64_tr_b16 v[46:47], v253 offset:512\n\t"
      "s_waitcnt vmcnt(0)\n\t"
      "s_waitcnt lgkmcnt(14)\n\t"
      "s_add_u32 m0, s31, 130560\n\t"
      "s_nop 0\n\t"
      "global_load_lds_dwordx4 v241, s[4:5]\n\t"
      "global_load_lds_dwordx4 v248, s[4:5] offset:1024\n\t"
      "global_load_lds_dwordx4 v249, s[4:5] offset:2048\n\t"
      "global_load_lds_dwordx4 v250, s[4:5] offset:3072\n\t"
      "s_sub_u32 s28, s28, 1\n\t"
      "s_cselect_b32 s48, -6, 1\n\t"
      "s_cselect_b32 s49, -1, 0\n\t"
      "s_mul_i32 s48, s48, 131072\n\t"
      "s_add_u32 s4, s4, s48\n\t"
      "s_addc_u32 s5, s5, s49\n\t"
      "ds_read_b64_tr_b16 v[48:49], v242 offset:32768\n\t"
      "ds_read_b64_tr_b16 v[50:51], v242 offset:33280\n\t"
      "ds_read_b64_tr_b16 v[52:53], v251 offset:32768\n\t"
      "ds_read_b64_tr_b16 v[54:55], v251 offset:33280\n\t"
      "ds_read_b64_tr_b16 v[56:57], v252 offset:32768\n\t"
      "ds_read_b64_tr_b16 v[58:59], v252 offset:33280\n\t"
      "ds_read_b64_tr_b16 v[60:61], v253 offset:32768\n\t"
      "ds_read_b64_tr_b16 v[62:63], v253 offset:33280\n\t"
      "s_bitcmp1_b32 s16, 1\n\t"
      "s_cbranch_scc0 HWQ_%=\n\t"
      "global_load_dwordx4 v[112:115], v116, s[12:13] sc1\n\t"
      "HWQ_%=:\n\t"
      "s_waitcnt vmcnt(0)\n\t"
      "s_waitcnt lgkmcnt(14)\n\t"
      "s_add_u32 m0, s31, 66048\n\t"
      "s_nop 0\n\t"
      "global_load_lds_dwordx4 v241, s[4:5]\n\t"
      "global_load_lds_dwordx4 v248, s[4:5] offset:1024\n\t"
      "global_load_lds_dwordx4 v249, s[4:5] offset:2048\n\t"
      "global_load_lds_dwordx4 v250, s[4:5] offset:3072\n\t"
      "s_sub_u32 s28, s28, 1\n\t"
      "s_cselect_b32 s48, -6, 1\n\t"
      "s_cselect_b32 s49, -1, 0\n\t"
      "s_mul_i32 s48, s48, 131072\n\t"
      "s_add_u32 s4, s4, s48\n\t"
      "s_addc_u32 s5, s5, s49\n\t"
      "v_add_u32_e32 v242, 66048, v242\n\t"
      "v_add_u32_e32 v251, 66048, v251\n\t"
      "v_add_u32_e32 v252, 66048, v252\n\t"
      "v_add_u32_e32 v253, 66048, v253\n\t"
      "ds_read_b64_tr_b16 v[64:65], v242 offset:64512\n\t"
      "ds_read_b64_tr_b16 v[66:67], v242 offset:65024\n\t"
      "ds_read_b64_tr_b16 v[68:69], v251 offset:64512\n\t"
      "ds_read_b64_tr_b16 v[70:71], v251 offset:65024\n\t"
      "ds_read_b64_tr_b16 v[72:73], v252 offset:64512\n\t"
      "ds_read_b64_tr_b16 v[74:75], v252 offset:65024\n\t"
      "ds_read_b64_tr_b16 v[76:77], v253 offset:64512\n\t"
      "ds_read_b64_tr_b16 v[78:79], v253 offset:65024\n\t"
      "s_waitcnt vmcnt(0)\n\t"
      "s_waitcnt lgkmcnt(14)\n\t"
      "s_add_u32 m0, s31, 0\n\t"
      "s_nop 0\n\t"
      "global_load_lds_dwordx4 v241, s[4:5]\n\t"
      "global_load_lds_dwordx4 v248, s[4:5] offset:1024\n\t"
      "global_load_lds_dwordx4 v249, s[4:5] offset:2048\n\t"
      "global_load_lds_dwordx4 v250, s[4:5] offset:3072\n\t"
      "ds_read_b64_tr_b16 v[80:81], v242\n\t"
      "ds_read_b64_tr_b16 v[82:83], v242 offset:512\n\t"
      "ds_read_b64_tr_b16 v[84:85], v251\n\t"
      "ds_read_b64_tr_b16 v[86:87], v251 offset:512\n\t"
      "ds_read_b64_tr_b16 v[88:89], v252\n\t"
      "ds_read_b64_tr_b16 v[90:91], v252 offset:512\n\t"
      "ds_read_b64_tr_b16 v[92:93], v253\n\t"
      "ds_read_b64_tr_b16 v[94:95], v253 offset:512\n\t"
      "s_bitcmp1_b32 s16, 1\n\t"
      "s_cbranch_scc0 HWH_%=\n\t"
      "v_xor_b32_e32 v114, v114, v112\n\t"
      "v_xor_b32_e32 v115, v115, v113\n\t"
      "v_cmp_ne_u64_e32 vcc, s[14:15], v[114:115]\n\t"
      "s_cbranch_vccnz HWR_%=\n\t"
      "HWB_%=:\n\t"
      "s_mov_b32 exec_hi, s23\n\t"
      "s_not_b32 exec_lo, s23\n\t"
      "s_add_u32 s68, s20, 0\n\t"
      "s_addc_u32 s69, s21, 0\n\t"
      "global_load_dwordx4 v[184:187], v240, s[68:69] sc1\n\t"
      "s_add_u32 s68, s20, 131072\n\t"
      "s_addc_u32 s69, s21, 0\n\t"
      "global_load_dwordx4 v[188:191], v240, s[68:69] sc1\n\t"
      "s_add_u32 s68, s20, 262144\n\t"
      "s_addc_u32 s69, s21, 0\n\t"
      "global_load_dwordx4 v[192:195], v240, s[68:69] sc1\n\t"
      "s_add_u32 s68, s20, 393216\n\t"
      "s_addc_u32 s69, s21, 0\n\t"
      "global_load_dwordx4 v[196:199], v240, s[68:69] sc1\n\t"
      "s_add_u32 s68, s20, 524288\n\t"
      "s_addc_u32 s69, s21, 0\n\t"
      "global_load_dwordx4 v[200:203], v240, s[68:69] sc1\n\t"
      "s_add_u32 s68, s20, 655360\n\t"
      "s_addc_u32 s69, s21, 0\n\t"
      "global_load_dwordx4 v[204:207], v240, s[68:69] sc1\n\t"
      "s_add_u32 s68, s20, 786432\n\t"
      "s_addc_u32 s69, s21, 0\n\t"
      "global_load_dwordx4 v[208:211], v240, s[68:69] sc1\n\t"
      "s_mov_b64 exec, -1\n\t"
      "v_lshlrev_b32_e32 v236, 16, v236\n\t"
      "v_add_f32_e32 v236, v236, v112\n\t"
      "v_cvt_pk_bf16_f32 v114, v113, v113\n\t"
      "v_lshlrev_b32_e32 v115, 16, v114\n\t"
      "v_sub_f32_e32 v115, v113, v115\n\t"
      "v_cvt_pk_bf16_f32 v115, v115, v115\n\t"
      "v_mov_b32_e32 v237, v236\n\t"
      "v_mov_b32_e32 v238, v236\n\t"
      "v_mov_b32_e32 v239, v236\n\t"
      "v_and_b32_e32 v120, v216, v115\n\t"
      "v_and_b32_e32 v216, v216, v114\n\t"
      "v_and_b32_e32 v121, v217, v115\n\t"
      "v_and_b32_e32 v217, v217, v114\n\t"
      "v_and_b32_e32 v122, v218, v115\n\t"
      "v_and_b32_e32 v218, v218, v114\n\t"
      "v_and_b32_e32 v123, v219, v115\n\t"
      "v_and_b32_e32 v219, v219, v114\n\t"
      "HWH_%=:\n\t"
      "s_waitcnt vmcnt(0)\n\t"
      "v_add_u32_e32 v242, 4294901248, v242\n\t"
      "v_add_u32_e32 v251, 4294901248, v251\n\t"
      "v_add_u32_e32 v252, 4294901248, v252\n\t"
      "v_add_u32_e32 v253, 4294901248, v253\n\t"
      "ds_read_b64_tr_b16 v[96:97], v242\n\t"
      "ds_read_b64_tr_b16 v[98:99], v242 offset:512\n\t"
      "ds_read_b64_tr_b16 v[100:101], v251\n\t"
      "ds_read_b64_tr_b16 v[102:103], v251 offset:512\n\t"
      "ds_read_b64_tr_b16 v[104:105], v252\n\t"
      "ds_read_b64_tr_b16 v[106:107], v252 offset:512\n\t"
      "ds_read_b64_tr_b16 v[108:109], v253\n\t"
      "ds_read_b64_tr_b16 v[110:111], v253 offset:512\n\t"
      "s_mov_b64 exec, 15\n\t"
      "v_mov_b64_e32 v[232:233], 0\n\t"
      "global_atomic_or_x2 v[232:233], v247, v[232:233], s[12:13] sc0 sc1\n\t"
      "s_mov_b64 exec, -1\n\t"
      "s_bitcmp1_b32 s16, 1\n\t"
      "s_cbranch_scc0 HWE_%=\n\t"
      "s_waitcnt vmcnt(1)\n\t"
      "v_max3_u32 v234, v184, v185, v186\n\t"
      "v_max3_u32 v234, v234, v187, v188\n\t"
      "v_max3_u32 v234, v234, v189, v190\n\t"
      "v_max3_u32 v234, v234, v191, v192\n\t"
      "v_max3_u32 v234, v234, v193, v194\n\t"
      "v_max3_u32 v234, v234, v195, v196\n\t"
      "v_max3_u32 v234, v234, v197, v198\n\t"
      "v_max3_u32 v234, v234, v199, v200\n\t"
      "v_max3_u32 v234, v234, v201, v202\n\t"
      "v_max3_u32 v234, v234, v203, v204\n\t"
      "v_max3_u32 v234, v234, v205, v206\n\t"
      "v_max3_u32 v234, v234, v207, v208\n\t"
      "v_max3_u32 v234, v234, v209, v210\n\t"
      "v_max_u32_e32 v234, v234, v211\n\t"
      "v_cmp_eq_u32_e32 vcc, -1, v234\n\t"
      "s_cbranch_vccnz HXR_%=\n\t"
      "HXB_%=:\n\t"
      "v_mfma_f32_16x16x32_bf16 v[156:159], v[184:187], v[120:123], v[236:239]\n\t"
      "v_mfma_f32_16x16x32_bf16 v[160:163], v[188:191], v[120:123], v[236:239]\n\t"
      "v_mfma_f32_16x16x32_bf16 v[164:167], v[192:195], v[120:123], v[236:239]\n\t"
      "v_mfma_f32_16x16x32_bf16 v[168:171], v[196:199], v[120:123], v[236:239]\n\t"
      "v_mfma_f32_16x16x32_bf16 v[172:175], v[200:203], v[120:123], v[236:239]\n\t"
      "v_mfma_f32_16x16x32_bf16 v[176:179], v[204:207], v[120:123], v[236:239]\n\t"
      "v_mfma_f32_16x16x32_bf16 v[180:183], v[208:211], v[120:123], v[236:239]\n\t"
      "v_mfma_f32_16x16x32_bf16 v[156:159], v[184:187], v[216:219], v[156:159]\n\t"
      "v_mfma_f32_16x16x32_bf16 v[160:163], v[188:191], v[216:219], v[160:163]\n\t"
      "v_mfma_f32_16x16x32_bf16 v[164:167], v[192:195], v[216:219], v[164:167]\n\t"
      "v_mfma_f32_16x16x32_bf16 v[168:171], v[196:199], v[216:219], v[168:171]\n\t"
      "v_mfma_f32_16x16x32_bf16 v[172:175], v[200:203], v[216:219], v[172:175]\n\t"
      "v_mfma_f32_16x16x32_bf16 v[176:179], v[204:207], v[216:219], v[176:179]\n\t"
      "v_mfma_f32_16x16x32_bf16 v[180:183], v[208:211], v[216:219], v[180:183]\n\t"
      "HWE_%=:\n\t"
      "s_waitcnt lgkmcnt(0)\n\t"
      "s_barrier\n\t"
      "s_mov_b64 exec, 15\n\t"
      "s_waitcnt vmcnt(0)\n\t"
      "v_subrev_u32_e32 v232, s14, v232\n\t"
      "v_cmp_ne_u32_e64 s[48:49], s15, v233\n\t"
      "v_subrev_u32_e32 v232, 0, v232\n\t"
      "v_cmp_lt_u32_e32 vcc, 7, v232\n\t"
      "s_mov_b64 exec, -1\n\t"
      "s_or_b64 vcc, vcc, s[48:49]\n\t"
      "s_cbranch_vccz PRY_%=\n\t"
      "s_mov_b32 s51, 0\n\t"
      "s_branch PRE_%=\n\t"
      "PRL_%=:\n\t"
      "s_sleep 4\n\t"
      "s_add_u32 s51, s51, 1\n\t"
      "s_cmp_lt_u32 s51, 262144\n\t"
      "s_cbranch_scc0 PRX_%=\n\t"
      "PRE_%=:\n\t"
      "s_mov_b64 exec, 15\n\t"
      "v_mov_b64_e32 v[232:233], 0\n\t"
      "global_atomic_or_x2 v[232:233], v247, v[232:233], s[12:13] sc0 sc1\n\t"
      "s_waitcnt vmcnt(0)\n\t"
      "v_subrev_u32_e32 v232, s14, v232\n\t"
      "v_cmp_ne_u32_e64 s[48:49], s15, v233\n\t"
      "v_subrev_u32_e32 v232, 0, v232\n\t"
      "v_cmp_lt_u32_e32 vcc, 7, v232\n\t"
      "s_mov_b64 exec, -1\n\t"
      "s_or_b64 vcc, vcc, s[48:49]\n\t"
      "s_cbranch_vccnz PRL_%=\n\t"
      "PRX_%=:\n\t"
      "PRY_%=:\n\t"
      "global_load_dwordx4 v[128:131], v224, s[52:53] sc1\n\t"
      "global_load_dwordx4 v[132:135], v225, s[52:53] sc1\n\t"
      "global_load_dwordx4 v[136:139], v226, s[52:53] sc1\n\t"
      "global_load_dwordx4 v[140:143], v227, s[52:53] sc1\n\t"
      "global_load_dwordx4 v[144:147], v228, s[52:53] sc1\n\t"
      "global_load_dwordx4 v[148:151], v229, s[52:53] sc1\n\t"
      "global_load_dwordx4 v[152:155], v230, s[52:53] sc1\n\t"
      "s_cmp_ge_u32 s17, 65536\n\t"
      "s_cbranch_scc1 GQ_%=\n\t"
      ".p2align 6\n\t"
      "s_setprio 1\n\t"
      "s_waitcnt vmcnt(6)\n\t"
      "v_max3_u32 v234, v128, v129, v130\n\t"
      "v_max_u32_e32 v234, v234, v131\n\t"
      "v_cmp_eq_u32_e32 vcc, -1, v234\n\t"
      "s_cbranch_vccnz FP0R_%=\n\t"
      "FP0B_%=:\n\t"
      "v_mfma_f32_16x16x32_bf16 v[184:187], v[128:131], v[0:3], v[156:159]\n\t"
      "v_mfma_f32_16x16x32_bf16 v[188:191], v[128:131], v[4:7], 0\n\t"
      "v_mfma_f32_16x16x32_bf16 v[192:195], v[128:131], v[8:11], 0\n\t"
      "v_mfma_f32_16x16x32_bf16 v[196:199], v[128:131], v[12:15], 0\n\t"
      "global_load_dwordx4 v[128:131], v224, s[54:55] sc1\n\t"
      "s_waitcnt vmcnt(6)\n\t"
      "v_max3_u32 v234, v132, v133, v134\n\t"
      "v_max_u32_e32 v234, v234, v135\n\t"
      "v_cmp_eq_u32_e32 vcc, -1, v234\n\t"
      "s_cbranch_vccnz FP1R_%=\n\t"
      "FP1B_%=:\n\t"
      "v_mfma_f32_16x16x32_bf16 v[184:187], v[132:135], v[16:19], v[184:187]\n\t"
      "v_mfma_f32_16x16x32_bf16 v[188:191], v[132:135], v[20:23], v[188:191]\n\t"
      "v_mfma_f32_16x16x32_bf16 v[192:195], v[132:135], v[24:27], v[192:195]\n\t"
      "v_mfma_f32_16x16x32_bf16 v[196:199], v[132:135], v[28:31], v[196:199]\n\t"
      "global_load_dwordx4 v[132:135], v225, s[54:55] sc1\n\t"
      "s_waitcnt vmcnt(6)\n\t"
      "v_max3_u32 v234, v136, v137, v138\n\t"
      "v_max_u32_e32 v234, v234, v139\n\t"
      "v_cmp_eq_u32_e32 vcc, -1, v234\n\t"
      "s_cbranch_vccnz FP2R_%=\n\t"
      "FP2B_%=:\n\t"
      "v_mfma_f32_16x16x32_bf16 v[184:187], v[136:139], v[32:35], v[184:187]\n\t"
      "v_mfma_f32_16x16x32_bf16 v[188:191], v[136:139], v[36:39], v[188:191]\n\t"
      "v_mfma_f32_16x16x32_bf16 v[192:195], v[136:139], v[40:43], v[192:195]\n\t"
      "v_mfma_f32_16x16x32_bf16 v[196:199], v[136:139], v[44:47], v[196:199]\n\t"
      "global_load_dwordx4 v[136:139], v226, s[54:55] sc1\n\t"
      "s_waitcnt vmcnt(6)\n\t"
      "v_max3_u32 v234, v140, v141, v142\n\t"
      "v_max_u32_e32 v234, v234, v143\n\t"
      "v_cmp_eq_u32_e32 vcc, -1, v234\n\t"
      "s_cbranch_vccnz FP3R_%=\n\t"
      "FP3B_%=:\n\t"
      "v_mfma_f32_16x16x32_bf16 v[184:187], v[140:143], v[48:51], v[184:187]\n\t"
      "v_mfma_f32_16x16x32_bf16 v[188:191], v[140:143], v[52:55], v[188:191]\n\t"
      "v_mfma_f32_16x16x32_bf16 v[192:195], v[140:143], v[56:59], v[192:195]\n\t"
      "v_mfma_f32_16x16x32_bf16 v[196:199], v[140:143], v[60:63], v[196:199]\n\t"
      "global_load_dwordx4 v[140:143], v227, s[54:55] sc1\n\t"
      "s_waitcnt vmcnt(6)\n\t"
      "v_max3_u32 v234, v144, v145, v146\n\t"
      "v_max_u32_e32 v234, v234, v147\n\t"
      "v_cmp_eq_u32_e32 vcc, -1, v234\n\t"
      "s_cbranch_vccnz FP4R_%=\n\t"
      "FP4B_%=:\n\t"
      "v_mfma_f32_16x16x32_bf16 v[184:187], v[144:147], v[64:67], v[184:187]\n\t"
      "v_mfma_f32_16x16x32_bf16 v[188:191], v[144:147], v[68:71], v[188:191]\n\t"
      "v_mfma_f32_16x16x32_bf16 v[192:195], v[144:147], v[72:75], v[192:195]\n\t"
      "v_mfma_f32_16x16x32_bf16 v[196:199], v[144:147], v[76:79], v[196:199]\n\t"
      "global_load_dwordx4 v[144:147], v228, s[54:55] sc1\n\t"
      "s_waitcnt vmcnt(6)\n\t"
      "v_max3_u32 v234, v148, v149, v150\n\t"
      "v_max_u32_e32 v234, v234, v151\n\t"
      "v_cmp_eq_u32_e32 vcc, -1, v234\n\t"
      "s_cbranch_vccnz FP5R_%=\n\t"
      "FP5B_%=:\n\t"
      "v_mfma_f32_16x16x32_bf16 v[184:187], v[148:151], v[80:83], v[184:187]\n\t"
      "v_mfma_f32_16x16x32_bf16 v[188:191], v[148:151], v[84:87], v[188:191]\n\t"
      "v_mfma_f32_16x16x32_bf16 v[192:195], v[148:151], v[88:91], v[192:195]\n\t"
      "v_mfma_f32_16x16x32_bf16 v[196:199], v[148:151], v[92:95], v[196:199]\n\t"
      "global_load_dwordx4 v[148:151], v229, s[54:55] sc1\n\t"
      "s_waitcnt vmcnt(6)\n\t"
      "v_max3_u32 v234, v152, v153, v154\n\t"
      "v_max_u32_e32 v234, v234, v155\n\t"
      "v_cmp_eq_u32_e32 vcc, -1, v234\n\t"
      "s_cbranch_vccnz FP6R_%=\n\t"
      "FP6B_%=:\n\t"
      "v_mfma_f32_16x16x32_bf16 v[184:187], v[152:155], v[96:99], v[184:187]\n\t"
      "s_waitcnt vmcnt(5)\n\t"
      "v_cmp_eq_u32_e32 vcc, -1, v128\n\t"
      "v_mfma_f32_16x16x32_bf16 v[188:191], v[152:155], v[100:103], v[188:191]\n\t"
      "v_mfma_f32_16x16x32_bf16 v[192:195], v[152:155], v[104:107], v[192:195]\n\t"
      "s_cbranch_vccnz FP7R_%=\n\t"
      "FP7B_%=:\n\t"
      "v_mfma_f32_16x16x32_bf16 v[196:199], v[152:155], v[108:111], v[196:199]\n\t"
      "global_load_dwordx4 v[152:155], v230, s[54:55] sc1\n\t"
      "s_nop 0\n\t"
      "ds_write_b128 v243, v[184:187]\n\t"
      "s_nop 1\n\t"
      "ds_write_b128 v220, v[188:191]\n\t"
      "ds_write_b128 v221, v[192:195]\n\t"
      "s_nop 0\n\t"
      "ds_write_b128 v222, v[196:199]\n\t"
      "s_waitcnt lgkmcnt(0)\n\t"
      "s_barrier\n\t"
      "ds_read_b128 v[112:115], v244\n\t"
      "ds_read_b128 v[116:119], v244 offset:4096\n\t"
      "ds_read_b128 v[120:123], v244 offset:8192\n\t"
      "ds_read_b128 v[124:127], v244 offset:12288\n\t"
      "ds_read_b128 v[184:187], v244 offset:16384\n\t"
      "ds_read_b128 v[188:191], v244 offset:20480\n\t"
      "ds_read_b128 v[192:195], v244 offset:24576\n\t"
      "ds_read_b128 v[196:199], v244 offset:28672\n\t"
      "v_mfma_f32_16x16x32_bf16 v[200:203], v[128:131], v[0:3], v[160:163]\n\t"
      "s_waitcnt vmcnt(5)\n\t"
      "v_cmp_eq_u32_e32 vcc, -1, v132\n\t"
      "v_mfma_f32_16x16x32_bf16 v[204:207], v[128:131], v[4:7], 0\n\t"
      "s_waitcnt lgkmcnt(6)\n\t"
      "v_add_f32_e32 v112, v112, v116\n\t"
      "v_mfma_f32_16x16x32_bf16 v[208:211], v[128:131], v[8:11], 0\n\t"
      "s_cbranch_vccnz FP8R_%=\n\t"
      "FP8B_%=:\n\t"
      "v_add_f32_e32 v113, v113, v117\n\t"
      "v_add_f32_e32 v114, v114, v118\n\t"
      "v_mfma_f32_16x16x32_bf16 v[212:215], v[128:131], v[12:15], 0\n\t"
      "global_load_dwordx4 v[128:131], v224, s[56:57] sc1\n\t"
      "v_add_f32_e32 v115, v115, v119\n\t"
      "v_mfma_f32_16x16x32_bf16 v[200:203], v[132:135], v[16:19], v[200:203]\n\t"
      "s_waitcnt vmcnt(5)\n\t"
      "v_cmp_eq_u32_e32 vcc, -1, v136\n\t"
      "s_waitcnt lgkmcnt(4)\n\t"
      "v_add_f32_e32 v120, v120, v124\n\t"
      "v_mfma_f32_16x16x32_bf16 v[204:207], v[132:135], v[20:23], v[204:207]\n\t"
      "v_add_f32_e32 v121, v121, v125\n\t"
      "v_add_f32_e32 v122, v122, v126\n\t"
      "v_mfma_f32_16x16x32_bf16 v[208:211], v[132:135], v[24:27], v[208:211]\n\t"
      "s_cbranch_vccnz FP9R_%=\n\t"
      "FP9B_%=:\n\t"
      "v_add_f32_e32 v123, v123, v127\n\t"
      "s_waitcnt lgkmcnt(2)\n\t"
      "v_add_f32_e32 v184, v184, v188\n\t"
      "v_mfma_f32_16x16x32_bf16 v[212:215], v[132:135], v[28:31], v[212:215]\n\t"
      "global_load_dwordx4 v[132:135], v225, s[56:57] sc1\n\t"
      "v_add_f32_e32 v185, v185, v189\n\t"
      "v_mfma_f32_16x16x32_bf16 v[200:203], v[136:139], v[32:35], v[200:203]\n\t"
      "s_waitcnt vmcnt(5)\n\t"
      "v_cmp_eq_u32_e32 vcc, -1, v140\n\t"
      "v_add_f32_e32 v186, v186, v190\n\t"
      "v_mfma_f32_16x16x32_bf16 v[204:207], v[136:139], v[36:39], v[204:207]\n\t"
      "v_add_f32_e32 v187, v187, v191\n\t"
      "v_add_f32_e32 v112, v112, v120\n\t"
      "v_mfma_f32_16x16x32_bf16 v[208:211], v[136:139], v[40:43], v[208:211]\n\t"
      "s_cbranch_vccnz FP10R_%=\n\t"
      "FP10B_%=:\n\t"
      "v_add_f32_e32 v113, v113, v121\n\t"
      "v_add_f32_e32 v114, v114, v122\n\t"
      "v_mfma_f32_16x16x32_bf16 v[212:215], v[136:139], v[44:47], v[212:215]\n\t"
      "global_load_dwordx4 v[136:139], v226, s[56:57] sc1\n\t"
      "v_add_f32_e32 v115, v115, v123\n\t"
      "v_mfma_f32_16x16x32_bf16 v[200:203], v[140:143], v[48:51], v[200:203]\n\t"
      "s_waitcnt vmcnt(5)\n\t"
      "v_cmp_eq_u32_e32 vcc, -1, v144\n\t"
      "s_waitcnt lgkmcnt(0)\n\t"
      "v_add_f32_e32 v192, v192, v196\n\t"
      "v_mfma_f32_16x16x32_bf16 v[204:207], v[140:143], v[52:55], v[204:207]\n\t"
      "v_add_f32_e32 v193, v193, v197\n\t"
      "v_add_f32_e32 v194, v194, v198\n\t"
      "v_mfma_f32_16x16x32_bf16 v[208:211], v[140:143], v[56:59], v[208:211]\n\t"
      "s_cbranch_vccnz FP11R_%=\n\t"
      "FP11B_%=:\n\t"
      "v_add_f32_e32 v195, v195, v199\n\t"
      "v_add_f32_e32 v184, v184, v192\n\t"
      "v_mfma_f32_16x16x32_bf16 v[212:215], v[140:143], v[60:63], v[212:215]\n\t"
      "global_load_dwordx4 v[140:143], v227, s[56:57] sc1\n\t"
      "v_add_f32_e32 v185, v185, v193\n\t"
      "v_mfma_f32_16x16x32_bf16 v[200:203], v[144:147], v[64:67], v[200:203]\n\t"
      "s_waitcnt vmcnt(5)\n\t"
      "v_cmp_eq_u32_e32 vcc, -1, v148\n\t"
      "v_add_f32_e32 v186, v186, v194\n\t"
      "v_mfma_f32_16x16x32_bf16 v[204:207], v[144:147], v[68:71], v[204:207]\n\t"
      "v_add_f32_e32 v187, v187, v195\n\t"
      "v_add_f32_e32 v112, v112, v184\n\t"
      "v_mfma_f32_16x16x32_bf16 v[208:211], v[144:147], v[72:75], v[208:211]\n\t"
      "s_cbranch_vccnz FP12R_%=\n\t"
      "FP12B_%=:\n\t"
      "v_add_f32_e32 v113, v113, v185\n\t"
      "v_add_f32_e32 v114, v114, v186\n\t"
      "v_mfma_f32_16x16x32_bf16 v[212:215], v[144:147], v[76:79], v[212:215]\n\t"
      "global_load_dwordx4 v[144:147], v228, s[56:57] sc1\n\t"
      "v_add_f32_e32 v115, v115, v187\n\t"
      "v_mfma_f32_16x16x32_bf16 v[200:203], v[148:151], v[80:83], v[200:203]\n\t"
      "s_waitcnt vmcnt(5)\n\t"
      "v_cmp_eq_u32_e32 vcc, -1, v152\n\t"
      "v_cvt_pk_bf16_f32 v116, v112, v113\n\t"
      "v_mfma_f32_16x16x32_bf16 v[204:207], v[148:151], v[84:87], v[204:207]\n\t"
      "v_cvt_pk_bf16_f32 v117, v114, v115\n\t"
      "global_store_dwordx2 v245, v[116:117], s[8:9]\n\t"
      "v_mfma_f32_16x16x32_bf16 v[208:211], v[148:151], v[88:91], v[208:211]\n\t"
      "s_cbranch_vccnz FP13R_%=\n\t"
      "FP13B_%=:\n\t"
      "v_mfma_f32_16x16x32_bf16 v[212:215], v[148:151], v[92:95], v[212:215]\n\t"
      "global_load_dwordx4 v[148:151], v229, s[56:57] sc1\n\t"
      "v_mfma_f32_16x16x32_bf16 v[200:203], v[152:155], v[96:99], v[200:203]\n\t"
      "s_waitcnt vmcnt(6)\n\t"
      "v_cmp_eq_u32_e32 vcc, -1, v128\n\t"
      "v_mfma_f32_16x16x32_bf16 v[204:207], v[152:155], v[100:103], v[204:207]\n\t"
      "v_mfma_f32_16x16x32_bf16 v[208:211], v[152:155], v[104:107], v[208:211]\n\t"
      "s_cbranch_vccnz FP14R_%=\n\t"
      "FP14B_%=:\n\t"
      "v_mfma_f32_16x16x32_bf16 v[212:215], v[152:155], v[108:111], v[212:215]\n\t"
      "global_load_dwordx4 v[152:155], v230, s[56:57] sc1\n\t"
      "s_nop 0\n\t"
      "v_cmp_u_f32_e32 vcc, v200, v201\n\t"
      "v_cmp_u_f32_e64 s[48:49], v202, v203\n\t"
      "s_nop 0\n\t"
      "s_or_b64 vcc, vcc, s[48:49]\n\t"
      "s_cbranch_scc1 NP1R_%=\n\t"
      "NP1B_%=:\n\t"
      "ds_write_b128 v243, v[200:203] offset:32768\n\t"
      "ds_write_b128 v220, v[204:207] offset:32768\n\t"
      "ds_write_b128 v221, v[208:211] offset:32768\n\t"
      "ds_write_b128 v222, v[212:215] offset:32768\n\t"
      "s_waitcnt lgkmcnt(0)\n\t"
      "s_barrier\n\t"
      "ds_read_b128 v[112:115], v244 offset:32768\n\t"
      "ds_read_b128 v[116:119], v244 offset:36864\n\t"
      "ds_read_b128 v[120:123], v244 offset:40960\n\t"
      "ds_read_b128 v[124:127], v244 offset:45056\n\t"
      "ds_read_b128 v[200:203], v244 offset:49152\n\t"
      "ds_read_b128 v[204:207], v244 offset:53248\n\t"
      "ds_read_b128 v[208:211], v244 offset:57344\n\t"
      "ds_read_b128 v[212:215], v244 offset:61440\n\t"
      "v_mfma_f32_16x16x32_bf16 v[184:187], v[128:131], v[0:3], v[164:167]\n\t"
      "s_waitcnt vmcnt(6)\n\t"
      "v_cmp_eq_u32_e32 vcc, -1, v132\n\t"
      "v_mfma_f32_16x16x32_bf16 v[188:191], v[128:131], v[4:7], 0\n\t"
      "s_waitcnt lgkmcnt(6)\n\t"
      "v_add_f32_e32 v112, v112, v116\n\t"
      "v_mfma_f32_16x16x32_bf16 v[192:195], v[128:131], v[8:11], 0\n\t"
      "s_cbranch_vccnz FP15R_%=\n\t"
      "FP15B_%=:\n\t"
      "v_add_f32_e32 v113, v113, v117\n\t"
      "v_add_f32_e32 v114, v114, v118\n\t"
      "v_mfma_f32_16x16x32_bf16 v[196:199], v[128:131], v[12:15], 0\n\t"
      "global_load_dwordx4 v[128:131], v224, s[58:59] sc1\n\t"
      "v_add_f32_e32 v115, v115, v119\n\t"
      "v_mfma_f32_16x16x32_bf16 v[184:187], v[132:135], v[16:19], v[184:187]\n\t"
      "s_waitcnt vmcnt(6)\n\t"
      "v_cmp_eq_u32_e32 vcc, -1, v136\n\t"
      "s_waitcnt lgkmcnt(4)\n\t"
      "v_add_f32_e32 v120, v120, v124\n\t"
      "v_mfma_f32_16x16x32_bf16 v[188:191], v[132:135], v[20:23], v[188:191]\n\t"
      "v_add_f32_e32 v121, v121, v125\n\t"
      "v_add_f32_e32 v122, v122, v126\n\t"
      "v_mfma_f32_16x16x32_bf16 v[192:195], v[132:135], v[24:27], v[192:195]\n\t"
      "s_cbranch_vccnz FP16R_%=\n\t"
      "FP16B_%=:\n\t"
      "v_add_f32_e32 v123, v123, v127\n\t"
      "s_waitcnt lgkmcnt(2)\n\t"
      "v_add_f32_e32 v200, v200, v204\n\t"
      "v_mfma_f32_16x16x32_bf16 v[196:199], v[132:135], v[28:31], v[196:199]\n\t"
      "global_load_dwordx4 v[132:135], v225, s[58:59] sc1\n\t"
      "v_add_f32_e32 v201, v201, v205\n\t"
      "v_mfma_f32_16x16x32_bf16 v[184:187], v[136:139], v[32:35], v[184:187]\n\t"
      "s_waitcnt vmcnt(6)\n\t"
      "v_cmp_eq_u32_e32 vcc, -1, v140\n\t"
      "v_add_f32_e32 v202, v202, v206\n\t"
      "v_mfma_f32_16x16x32_bf16 v[188:191], v[136:139], v[36:39], v[188:191]\n\t"
      "v_add_f32_e32 v203, v203, v207\n\t"
      "v_add_f32_e32 v112, v112, v120\n\t"
      "v_mfma_f32_16x16x32_bf16 v[192:195], v[136:139], v[40:43], v[192:195]\n\t"
      "s_cbranch_vccnz FP17R_%=\n\t"
      "FP17B_%=:\n\t"
      "v_add_f32_e32 v113, v113, v121\n\t"
      "v_add_f32_e32 v114, v114, v122\n\t"
      "v_mfma_f32_16x16x32_bf16 v[196:199], v[136:139], v[44:47], v[196:199]\n\t"
      "global_load_dwordx4 v[136:139], v226, s[58:59] sc1\n\t"
      "v_add_f32_e32 v115, v115, v123\n\t"
      "v_mfma_f32_16x16x32_bf16 v[184:187], v[140:143], v[48:51], v[184:187]\n\t"
      "s_waitcnt vmcnt(6)\n\t"
      "v_cmp_eq_u32_e32 vcc, -1, v144\n\t"
      "s_waitcnt lgkmcnt(0)\n\t"
      "v_add_f32_e32 v208, v208, v212\n\t"
      "v_mfma_f32_16x16x32_bf16 v[188:191], v[140:143], v[52:55], v[188:191]\n\t"
      "v_add_f32_e32 v209, v209, v213\n\t"
      "v_add_f32_e32 v210, v210, v214\n\t"
      "v_mfma_f32_16x16x32_bf16 v[192:195], v[140:143], v[56:59], v[192:195]\n\t"
      "s_cbranch_vccnz FP18R_%=\n\t"
      "FP18B_%=:\n\t"
      "v_add_f32_e32 v211, v211, v215\n\t"
      "v_add_f32_e32 v200, v200, v208\n\t"
      "v_mfma_f32_16x16x32_bf16 v[196:199], v[140:143], v[60:63], v[196:199]\n\t"
      "global_load_dwordx4 v[140:143], v227, s[58:59] sc1\n\t"
      "v_add_f32_e32 v201, v201, v209\n\t"
      "v_mfma_f32_16x16x32_bf16 v[184:187], v[144:147], v[64:67], v[184:187]\n\t"
      "s_waitcnt vmcnt(5)\n\t"
      "v_cmp_eq_u32_e32 vcc, -1, v148\n\t"
      "v_add_f32_e32 v202, v202, v210\n\t"
      "v_mfma_f32_16x16x32_bf16 v[188:191], v[144:147], v[68:71], v[188:191]\n\t"
      "v_add_f32_e32 v203, v203, v211\n\t"
      "v_add_f32_e32 v112, v112, v200\n\t"
      "v_mfma_f32_16x16x32_bf16 v[192:195], v[144:147], v[72:75], v[192:195]\n\t"
      "s_cbranch_vccnz FP19R_%=\n\t"
      "FP19B_%=:\n\t"
      "v_add_f32_e32 v113, v113, v201\n\t"
      "v_add_f32_e32 v114, v114, v202\n\t"
      "v_mfma_f32_16x16x32_bf16 v[196:199], v[144:147], v[76:79], v[196:199]\n\t"
      "global_load_dwordx4 v[144:147], v228, s[58:59] sc1\n\t"
      "v_add_f32_e32 v115, v115, v203\n\t"
      "v_mfma_f32_16x16x32_bf16 v[184:187], v[148:151], v[80:83], v[184:187]\n\t"
      "s_waitcnt vmcnt(5)\n\t"
      "v_cmp_eq_u32_e32 vcc, -1, v152\n\t"
      "v_cvt_pk_bf16_f32 v116, v112, v113\n\t"
      "v_mfma_f32_16x16x32_bf16 v[188:191], v[148:151], v[84:87], v[188:191]\n\t"
      "v_cvt_pk_bf16_f32 v117, v114, v115\n\t"
      "global_store_dwordx2 v245, v[116:117], s[8:9] offset:32\n\t"
      "v_mfma_f32_16x16x32_bf16 v[192:195], v[148:151], v[88:91], v[192:195]\n\t"
      "s_cbranch_vccnz FP20R_%=\n\t"
      "FP20B_%=:\n\t"
      "v_mfma_f32_16x16x32_bf16 v[196:199], v[148:151], v[92:95], v[196:199]\n\t"
      "global_load_dwordx4 v[148:151], v229, s[58:59] sc1\n\t"
      "v_mfma_f32_16x16x32_bf16 v[184:187], v[152:155], v[96:99], v[184:187]\n\t"
      "s_waitcnt vmcnt(6)\n\t"
      "v_cmp_eq_u32_e32 vcc, -1, v128\n\t"
      "v_mfma_f32_16x16x32_bf16 v[188:191], v[152:155], v[100:103], v[188:191]\n\t"
      "v_mfma_f32_16x16x32_bf16 v[192:195], v[152:155], v[104:107], v[192:195]\n\t"
      "s_cbranch_vccnz FP21R_%=\n\t"
      "FP21B_%=:\n\t"
      "v_mfma_f32_16x16x32_bf16 v[196:199], v[152:155], v[108:111], v[196:199]\n\t"
      "global_load_dwordx4 v[152:155], v230, s[58:59] sc1\n\t"
      "s_nop 0\n\t"
      "v_cmp_u_f32_e32 vcc, v184, v185\n\t"
      "v_cmp_u_f32_e64 s[48:49], v186, v187\n\t"
      "s_nop 0\n\t"
      "s_or_b64 vcc, vcc, s[48:49]\n\t"
      "s_cbranch_scc1 NP2R_%=\n\t"
      "NP2B_%=:\n\t"
      "ds_write_b128 v243, v[184:187]\n\t"
      "ds_write_b128 v220, v[188:191]\n\t"
      "ds_write_b128 v221, v[192:195]\n\t"
      "ds_write_b128 v222, v[196:199]\n\t"
      "s_waitcnt lgkmcnt(0)\n\t"
      "s_barrier\n\t"
      "ds_read_b128 v[112:115], v244\n\t"
      "ds_read_b128 v[116:119], v244 offset:4096\n\t"
      "ds_read_b128 v[120:123], v244 offset:8192\n\t"
      "ds_read_b128 v[124:127], v244 offset:12288\n\t"
      "ds_read_b128 v[184:187], v244 offset:16384\n\t"
      "ds_read_b128 v[188:191], v244 offset:20480\n\t"
      "ds_read_b128 v[192:195], v244 offset:24576\n\t"
      "ds_read_b128 v[196:199], v244 offset:28672\n\t"
      "v_mfma_f32_16x16x32_bf16 v[200:203], v[128:131], v[0:3], v[168:171]\n\t"
      "s_waitcnt vmcnt(6)\n\t"
      "v_cmp_eq_u32_e32 vcc, -1, v132\n\t"
      "v_mfma_f32_16x16x32_bf16 v[204:207], v[128:131], v[4:7], 0\n\t"
      "s_waitcnt lgkmcnt(6)\n\t"
      "v_add_f32_e32 v112, v112, v116\n\t"
      "v_mfma_f32_16x16x32_bf16 v[208:211], v[128:131], v[8:11], 0\n\t"
      "s_cbranch_vccnz FP22R_%=\n\t"
      "FP22B_%=:\n\t"
      "v_add_f32_e32 v113, v113, v117\n\t"
      "v_add_f32_e32 v114, v114, v118\n\t"
      "v_mfma_f32_16x16x32_bf16 v[212:215], v[128:131], v[12:15], 0\n\t"
      "global_load_dwordx4 v[128:131], v224, s[60:61] sc1\n\t"
      "v_add_f32_e32 v115, v115, v119\n\t"
      "v_mfma_f32_16x16x32_bf16 v[200:203], v[132:135], v[16:19], v[200:203]\n\t"
      "s_waitcnt vmcnt(6)\n\t"
      "v_cmp_eq_u32_e32 vcc, -1, v136\n\t"
      "s_waitcnt lgkmcnt(4)\n\t"
      "v_add_f32_e32 v120, v120, v124\n\t"
      "v_mfma_f32_16x16x32_bf16 v[204:207], v[132:135], v[20:23], v[204:207]\n\t"
      "v_add_f32_e32 v121, v121, v125\n\t"
      "v_add_f32_e32 v122, v122, v126\n\t"
      "v_mfma_f32_16x16x32_bf16 v[208:211], v[132:135], v[24:27], v[208:211]\n\t"
      "s_cbranch_vccnz FP23R_%=\n\t"
      "FP23B_%=:\n\t"
      "v_add_f32_e32 v123, v123, v127\n\t"
      "s_waitcnt lgkmcnt(2)\n\t"
      "v_add_f32_e32 v184, v184, v188\n\t"
      "v_mfma_f32_16x16x32_bf16 v[212:215], v[132:135], v[28:31], v[212:215]\n\t"
      "global_load_dwordx4 v[132:135], v225, s[60:61] sc1\n\t"
      "v_add_f32_e32 v185, v185, v189\n\t"
      "v_mfma_f32_16x16x32_bf16 v[200:203], v[136:139], v[32:35], v[200:203]\n\t"
      "s_waitcnt vmcnt(6)\n\t"
      "v_cmp_eq_u32_e32 vcc, -1, v140\n\t"
      "v_add_f32_e32 v186, v186, v190\n\t"
      "v_mfma_f32_16x16x32_bf16 v[204:207], v[136:139], v[36:39], v[204:207]\n\t"
      "v_add_f32_e32 v187, v187, v191\n\t"
      "v_add_f32_e32 v112, v112, v120\n\t"
      "v_mfma_f32_16x16x32_bf16 v[208:211], v[136:139], v[40:43], v[208:211]\n\t"
      "s_cbranch_vccnz FP24R_%=\n\t"
      "FP24B_%=:\n\t"
      "v_add_f32_e32 v113, v113, v121\n\t"
      "v_add_f32_e32 v114, v114, v122\n\t"
      "v_mfma_f32_16x16x32_bf16 v[212:215], v[136:139], v[44:47], v[212:215]\n\t"
      "global_load_dwordx4 v[136:139], v226, s[60:61] sc1\n\t"
      "v_add_f32_e32 v115, v115, v123\n\t"
      "v_mfma_f32_16x16x32_bf16 v[200:203], v[140:143], v[48:51], v[200:203]\n\t"
      "s_waitcnt vmcnt(6)\n\t"
      "v_cmp_eq_u32_e32 vcc, -1, v144\n\t"
      "s_waitcnt lgkmcnt(0)\n\t"
      "v_add_f32_e32 v192, v192, v196\n\t"
      "v_mfma_f32_16x16x32_bf16 v[204:207], v[140:143], v[52:55], v[204:207]\n\t"
      "v_add_f32_e32 v193, v193, v197\n\t"
      "v_add_f32_e32 v194, v194, v198\n\t"
      "v_mfma_f32_16x16x32_bf16 v[208:211], v[140:143], v[56:59], v[208:211]\n\t"
      "s_cbranch_vccnz FP25R_%=\n\t"
      "FP25B_%=:\n\t"
      "v_add_f32_e32 v195, v195, v199\n\t"
      "v_add_f32_e32 v184, v184, v192\n\t"
      "v_mfma_f32_16x16x32_bf16 v[212:215], v[140:143], v[60:63], v[212:215]\n\t"
      "global_load_dwordx4 v[140:143], v227, s[60:61] sc1\n\t"
      "v_add_f32_e32 v185, v185, v193\n\t"
      "v_mfma_f32_16x16x32_bf16 v[200:203], v[144:147], v[64:67], v[200:203]\n\t"
      "s_waitcnt vmcnt(5)\n\t"
      "v_cmp_eq_u32_e32 vcc, -1, v148\n\t"
      "v_add_f32_e32 v186, v186, v194\n\t"
      "v_mfma_f32_16x16x32_bf16 v[204:207], v[144:147], v[68:71], v[204:207]\n\t"
      "v_add_f32_e32 v187, v187, v195\n\t"
      "v_add_f32_e32 v112, v112, v184\n\t"
      "v_mfma_f32_16x16x32_bf16 v[208:211], v[144:147], v[72:75], v[208:211]\n\t"
      "s_cbranch_vccnz FP26R_%=\n\t"
      "FP26B_%=:\n\t"
      "v_add_f32_e32 v113, v113, v185\n\t"
      "v_add_f32_e32 v114, v114, v186\n\t"
      "v_mfma_f32_16x16x32_bf16 v[212:215], v[144:147], v[76:79], v[212:215]\n\t"
      "global_load_dwordx4 v[144:147], v228, s[60:61] sc1\n\t"
      "v_add_f32_e32 v115, v115, v187\n\t"
      "v_mfma_f32_16x16x32_bf16 v[200:203], v[148:151], v[80:83], v[200:203]\n\t"
      "s_waitcnt vmcnt(5)\n\t"
      "v_cmp_eq_u32_e32 vcc, -1, v152\n\t"
      "v_cvt_pk_bf16_f32 v116, v112, v113\n\t"
      "v_mfma_f32_16x16x32_bf16 v[204:207], v[148:151], v[84:87], v[204:207]\n\t"
      "v_cvt_pk_bf16_f32 v117, v114, v115\n\t"
      "global_store_dwordx2 v245, v[116:117], s[66:67]\n\t"
      "v_mfma_f32_16x16x32_bf16 v[208:211], v[148:151], v[88:91], v[208:211]\n\t"
      "s_cbranch_vccnz FP27R_%=\n\t"
      "FP27B_%=:\n\t"
      "v_mfma_f32_16x16x32_bf16 v[212:215], v[148:151], v[92:95], v[212:215]\n\t"
      "global_load_dwordx4 v[148:151], v229, s[60:61] sc1\n\t"
      "v_mfma_f32_16x16x32_bf16 v[200:203], v[152:155], v[96:99], v[200:203]\n\t"
      "s_waitcnt vmcnt(6)\n\t"
      "v_cmp_eq_u32_e32 vcc, -1, v128\n\t"
      "v_mfma_f32_16x16x32_bf16 v[204:207], v[152:155], v[100:103], v[204:207]\n\t"
      "v_mfma_f32_16x16x32_bf16 v[208:211], v[152:155], v[104:107], v[208:211]\n\t"
      "s_cbranch_vccnz FP28R_%=\n\t"
      "FP28B_%=:\n\t"
      "v_mfma_f32_16x16x32_bf16 v[212:215], v[152:155], v[108:111], v[212:215]\n\t"
      "global_load_dwordx4 v[152:155], v230, s[60:61] sc1\n\t"
      "s_nop 0\n\t"
      "v_cmp_u_f32_e32 vcc, v200, v201\n\t"
      "v_cmp_u_f32_e64 s[48:49], v202, v203\n\t"
      "s_nop 0\n\t"
      "s_or_b64 vcc, vcc, s[48:49]\n\t"
      "s_cbranch_scc1 NP3R_%=\n\t"
      "NP3B_%=:\n\t"
      "ds_write_b128 v243, v[200:203] offset:32768\n\t"
      "ds_write_b128 v220, v[204:207] offset:32768\n\t"
      "ds_write_b128 v221, v[208:211] offset:32768\n\t"
      "ds_write_b128 v222, v[212:215] offset:32768\n\t"
      "s_waitcnt lgkmcnt(0)\n\t"
      "s_barrier\n\t"
      "ds_read_b128 v[112:115], v244 offset:32768\n\t"
      "ds_read_b128 v[116:119], v244 offset:36864\n\t"
      "ds_read_b128 v[120:123], v244 offset:40960\n\t"
      "ds_read_b128 v[124:127], v244 offset:45056\n\t"
      "ds_read_b128 v[200:203], v244 offset:49152\n\t"
      "ds_read_b128 v[204:207], v244 offset:53248\n\t"
      "ds_read_b128 v[208:211], v244 offset:57344\n\t"
      "ds_read_b128 v[212:215], v244 offset:61440\n\t"
      "v_mfma_f32_16x16x32_bf16 v[184:187], v[128:131], v[0:3], v[172:175]\n\t"
      "s_waitcnt vmcnt(6)\n\t"
      "v_cmp_eq_u32_e32 vcc, -1, v132\n\t"
      "v_mfma_f32_16x16x32_bf16 v[188:191], v[128:131], v[4:7], 0\n\t"
      "s_waitcnt lgkmcnt(6)\n\t"
      "v_add_f32_e32 v112, v112, v116\n\t"
      "v_mfma_f32_16x16x32_bf16 v[192:195], v[128:131], v[8:11], 0\n\t"
      "s_cbranch_vccnz FP29R_%=\n\t"
      "FP29B_%=:\n\t"
      "v_add_f32_e32 v113, v113, v117\n\t"
      "v_add_f32_e32 v114, v114, v118\n\t"
      "v_mfma_f32_16x16x32_bf16 v[196:199], v[128:131], v[12:15], 0\n\t"
      "global_load_dwordx4 v[128:131], v224, s[62:63] sc1\n\t"
      "v_add_f32_e32 v115, v115, v119\n\t"
      "v_mfma_f32_16x16x32_bf16 v[184:187], v[132:135], v[16:19], v[184:187]\n\t"
      "s_waitcnt vmcnt(6)\n\t"
      "v_cmp_eq_u32_e32 vcc, -1, v136\n\t"
      "s_waitcnt lgkmcnt(4)\n\t"
      "v_add_f32_e32 v120, v120, v124\n\t"
      "v_mfma_f32_16x16x32_bf16 v[188:191], v[132:135], v[20:23], v[188:191]\n\t"
      "v_add_f32_e32 v121, v121, v125\n\t"
      "v_add_f32_e32 v122, v122, v126\n\t"
      "v_mfma_f32_16x16x32_bf16 v[192:195], v[132:135], v[24:27], v[192:195]\n\t"
      "s_cbranch_vccnz FP30R_%=\n\t"
      "FP30B_%=:\n\t"
      "v_add_f32_e32 v123, v123, v127\n\t"
      "s_waitcnt lgkmcnt(2)\n\t"
      "v_add_f32_e32 v200, v200, v204\n\t"
      "v_mfma_f32_16x16x32_bf16 v[196:199], v[132:135], v[28:31], v[196:199]\n\t"
      "global_load_dwordx4 v[132:135], v225, s[62:63] sc1\n\t"
      "v_add_f32_e32 v201, v201, v205\n\t"
      "v_mfma_f32_16x16x32_bf16 v[184:187], v[136:139], v[32:35], v[184:187]\n\t"
      "s_waitcnt vmcnt(6)\n\t"
      "v_cmp_eq_u32_e32 vcc, -1, v140\n\t"
      "v_add_f32_e32 v202, v202, v206\n\t"
      "v_mfma_f32_16x16x32_bf16 v[188:191], v[136:139], v[36:39], v[188:191]\n\t"
      "v_add_f32_e32 v203, v203, v207\n\t"
      "v_add_f32_e32 v112, v112, v120\n\t"
      "v_mfma_f32_16x16x32_bf16 v[192:195], v[136:139], v[40:43], v[192:195]\n\t"
      "s_cbranch_vccnz FP31R_%=\n\t"
      "FP31B_%=:\n\t"
      "v_add_f32_e32 v113, v113, v121\n\t"
      "v_add_f32_e32 v114, v114, v122\n\t"
      "v_mfma_f32_16x16x32_bf16 v[196:199], v[136:139], v[44:47], v[196:199]\n\t"
      "global_load_dwordx4 v[136:139], v226, s[62:63] sc1\n\t"
      "v_add_f32_e32 v115, v115, v123\n\t"
      "v_mfma_f32_16x16x32_bf16 v[184:187], v[140:143], v[48:51], v[184:187]\n\t"
      "s_waitcnt vmcnt(6)\n\t"
      "v_cmp_eq_u32_e32 vcc, -1, v144\n\t"
      "s_waitcnt lgkmcnt(0)\n\t"
      "v_add_f32_e32 v208, v208, v212\n\t"
      "v_mfma_f32_16x16x32_bf16 v[188:191], v[140:143], v[52:55], v[188:191]\n\t"
      "v_add_f32_e32 v209, v209, v213\n\t"
      "v_add_f32_e32 v210, v210, v214\n\t"
      "v_mfma_f32_16x16x32_bf16 v[192:195], v[140:143], v[56:59], v[192:195]\n\t"
      "s_cbranch_vccnz FP32R_%=\n\t"
      "FP32B_%=:\n\t"
      "v_add_f32_e32 v211, v211, v215\n\t"
      "v_add_f32_e32 v200, v200, v208\n\t"
      "v_mfma_f32_16x16x32_bf16 v[196:199], v[140:143], v[60:63], v[196:199]\n\t"
      "global_load_dwordx4 v[140:143], v227, s[62:63] sc1\n\t"
      "v_add_f32_e32 v201, v201, v209\n\t"
      "v_mfma_f32_16x16x32_bf16 v[184:187], v[144:147], v[64:67], v[184:187]\n\t"
      "s_waitcnt vmcnt(5)\n\t"
      "v_cmp_eq_u32_e32 vcc, -1, v148\n\t"
      "v_add_f32_e32 v202, v202, v210\n\t"
      "v_mfma_f32_16x16x32_bf16 v[188:191], v[144:147], v[68:71], v[188:191]\n\t"
      "v_add_f32_e32 v203, v203, v211\n\t"
      "v_add_f32_e32 v112, v112, v200\n\t"
      "v_mfma_f32_16x16x32_bf16 v[192:195], v[144:147], v[72:75], v[192:195]\n\t"
      "s_cbranch_vccnz FP33R_%=\n\t"
      "FP33B_%=:\n\t"
      "v_add_f32_e32 v113, v113, v201\n\t"
      "v_add_f32_e32 v114, v114, v202\n\t"
      "v_mfma_f32_16x16x32_bf16 v[196:199], v[144:147], v[76:79], v[196:199]\n\t"
      "global_load_dwordx4 v[144:147], v228, s[62:63] sc1\n\t"
      "v_add_f32_e32 v115, v115, v203\n\t"
      "v_mfma_f32_16x16x32_bf16 v[184:187], v[148:151], v[80:83], v[184:187]\n\t"
      "s_waitcnt vmcnt(5)\n\t"
      "v_cmp_eq_u32_e32 vcc, -1, v152\n\t"
      "v_cvt_pk_bf16_f32 v116, v112, v113\n\t"
      "v_mfma_f32_16x16x32_bf16 v[188:191], v[148:151], v[84:87], v[188:191]\n\t"
      "v_cvt_pk_bf16_f32 v117, v114, v115\n\t"
      "global_store_dwordx2 v245, v[116:117], s[66:67] offset:32\n\t"
      "v_mfma_f32_16x16x32_bf16 v[192:195], v[148:151], v[88:91], v[192:195]\n\t"
      "s_cbranch_vccnz FP34R_%=\n\t"
      "FP34B_%=:\n\t"
      "v_mfma_f32_16x16x32_bf16 v[196:199], v[148:151], v[92:95], v[196:199]\n\t"
      "global_load_dwordx4 v[148:151], v229, s[62:63] sc1\n\t"
      "v_mfma_f32_16x16x32_bf16 v[184:187], v[152:155], v[96:99], v[184:187]\n\t"
      "s_waitcnt vmcnt(6)\n\t"
      "v_cmp_eq_u32_e32 vcc, -1, v128\n\t"
      "v_mfma_f32_16x16x32_bf16 v[188:191], v[152:155], v[100:103], v[188:191]\n\t"
      "v_mfma_f32_16x16x32_bf16 v[192:195], v[152:155], v[104:107], v[192:195]\n\t"
      "s_cbranch_vccnz FP35R_%=\n\t"
      "FP35B_%=:\n\t"
      "v_mfma_f32_16x16x32_bf16 v[196:199], v[152:155], v[108:111], v[196:199]\n\t"
      "global_load_dwordx4 v[152:155], v230, s[62:63] sc1\n\t"
      "s_nop 0\n\t"
      "v_cmp_u_f32_e32 vcc, v184, v185\n\t"
      "v_cmp_u_f32_e64 s[48:49], v186, v187\n\t"
      "s_nop 0\n\t"
      "s_or_b64 vcc, vcc, s[48:49]\n\t"
      "s_cbranch_scc1 NP4R_%=\n\t"
      "NP4B_%=:\n\t"
      "ds_write_b128 v243, v[184:187]\n\t"
      "ds_write_b128 v220, v[188:191]\n\t"
      "ds_write_b128 v221, v[192:195]\n\t"
      "ds_write_b128 v222, v[196:199]\n\t"
      "s_waitcnt lgkmcnt(0)\n\t"
      "s_barrier\n\t"
      "ds_read_b128 v[112:115], v244\n\t"
      "ds_read_b128 v[116:119], v244 offset:4096\n\t"
      "ds_read_b128 v[120:123], v244 offset:8192\n\t"
      "ds_read_b128 v[124:127], v244 offset:12288\n\t"
      "ds_read_b128 v[184:187], v244 offset:16384\n\t"
      "ds_read_b128 v[188:191], v244 offset:20480\n\t"
      "ds_read_b128 v[192:195], v244 offset:24576\n\t"
      "ds_read_b128 v[196:199], v244 offset:28672\n\t"
      "v_mfma_f32_16x16x32_bf16 v[200:203], v[128:131], v[0:3], v[176:179]\n\t"
      "s_waitcnt vmcnt(6)\n\t"
      "v_cmp_eq_u32_e32 vcc, -1, v132\n\t"
      "v_mfma_f32_16x16x32_bf16 v[204:207], v[128:131], v[4:7], 0\n\t"
      "s_waitcnt lgkmcnt(6)\n\t"
      "v_add_f32_e32 v112, v112, v116\n\t"
      "v_mfma_f32_16x16x32_bf16 v[208:211], v[128:131], v[8:11], 0\n\t"
      "s_cbranch_vccnz FP36R_%=\n\t"
      "FP36B_%=:\n\t"
      "v_add_f32_e32 v113, v113, v117\n\t"
      "v_add_f32_e32 v114, v114, v118\n\t"
      "v_mfma_f32_16x16x32_bf16 v[212:215], v[128:131], v[12:15], 0\n\t"
      "global_load_dwordx4 v[128:131], v224, s[64:65] sc1\n\t"
      "v_add_f32_e32 v115, v115, v119\n\t"
      "v_mfma_f32_16x16x32_bf16 v[200:203], v[132:135], v[16:19], v[200:203]\n\t"
      "s_waitcnt vmcnt(6)\n\t"
      "v_cmp_eq_u32_e32 vcc, -1, v136\n\t"
      "s_waitcnt lgkmcnt(4)\n\t"
      "v_add_f32_e32 v120, v120, v124\n\t"
      "v_mfma_f32_16x16x32_bf16 v[204:207], v[132:135], v[20:23], v[204:207]\n\t"
      "v_add_f32_e32 v121, v121, v125\n\t"
      "v_add_f32_e32 v122, v122, v126\n\t"
      "v_mfma_f32_16x16x32_bf16 v[208:211], v[132:135], v[24:27], v[208:211]\n\t"
      "s_cbranch_vccnz FP37R_%=\n\t"
      "FP37B_%=:\n\t"
      "v_add_f32_e32 v123, v123, v127\n\t"
      "s_waitcnt lgkmcnt(2)\n\t"
      "v_add_f32_e32 v184, v184, v188\n\t"
      "v_mfma_f32_16x16x32_bf16 v[212:215], v[132:135], v[28:31], v[212:215]\n\t"
      "global_load_dwordx4 v[132:135], v225, s[64:65] sc1\n\t"
      "v_add_f32_e32 v185, v185, v189\n\t"
      "v_mfma_f32_16x16x32_bf16 v[200:203], v[136:139], v[32:35], v[200:203]\n\t"
      "s_waitcnt vmcnt(6)\n\t"
      "v_cmp_eq_u32_e32 vcc, -1, v140\n\t"
      "v_add_f32_e32 v186, v186, v190\n\t"
      "v_mfma_f32_16x16x32_bf16 v[204:207], v[136:139], v[36:39], v[204:207]\n\t"
      "v_add_f32_e32 v187, v187, v191\n\t"
      "v_add_f32_e32 v112, v112, v120\n\t"
      "v_mfma_f32_16x16x32_bf16 v[208:211], v[136:139], v[40:43], v[208:211]\n\t"
      "s_cbranch_vccnz FP38R_%=\n\t"
      "FP38B_%=:\n\t"
      "v_add_f32_e32 v113, v113, v121\n\t"
      "v_add_f32_e32 v114, v114, v122\n\t"
      "v_mfma_f32_16x16x32_bf16 v[212:215], v[136:139], v[44:47], v[212:215]\n\t"
      "global_load_dwordx4 v[136:139], v226, s[64:65] sc1\n\t"
      "v_add_f32_e32 v115, v115, v123\n\t"
      "v_mfma_f32_16x16x32_bf16 v[200:203], v[140:143], v[48:51], v[200:203]\n\t"
      "s_waitcnt vmcnt(6)\n\t"
      "v_cmp_eq_u32_e32 vcc, -1, v144\n\t"
      "s_waitcnt lgkmcnt(0)\n\t"
      "v_add_f32_e32 v192, v192, v196\n\t"
      "v_mfma_f32_16x16x32_bf16 v[204:207], v[140:143], v[52:55], v[204:207]\n\t"
      "v_add_f32_e32 v193, v193, v197\n\t"
      "v_add_f32_e32 v194, v194, v198\n\t"
      "v_mfma_f32_16x16x32_bf16 v[208:211], v[140:143], v[56:59], v[208:211]\n\t"
      "s_cbranch_vccnz FP39R_%=\n\t"
      "FP39B_%=:\n\t"
      "v_add_f32_e32 v195, v195, v199\n\t"
      "v_add_f32_e32 v184, v184, v192\n\t"
      "v_mfma_f32_16x16x32_bf16 v[212:215], v[140:143], v[60:63], v[212:215]\n\t"
      "global_load_dwordx4 v[140:143], v227, s[64:65] sc1\n\t"
      "v_add_f32_e32 v185, v185, v193\n\t"
      "v_mfma_f32_16x16x32_bf16 v[200:203], v[144:147], v[64:67], v[200:203]\n\t"
      "s_waitcnt vmcnt(5)\n\t"
      "v_cmp_eq_u32_e32 vcc, -1, v148\n\t"
      "v_add_f32_e32 v186, v186, v194\n\t"
      "v_mfma_f32_16x16x32_bf16 v[204:207], v[144:147], v[68:71], v[204:207]\n\t"
      "v_add_f32_e32 v187, v187, v195\n\t"
      "v_add_f32_e32 v112, v112, v184\n\t"
      "v_mfma_f32_16x16x32_bf16 v[208:211], v[144:147], v[72:75], v[208:211]\n\t"
      "s_cbranch_vccnz FP40R_%=\n\t"
      "FP40B_%=:\n\t"
      "v_add_f32_e32 v113, v113, v185\n\t"
      "v_add_f32_e32 v114, v114, v186\n\t"
      "v_mfma_f32_16x16x32_bf16 v[212:215], v[144:147], v[76:79], v[212:215]\n\t"
      "global_load_dwordx4 v[144:147], v228, s[64:65] sc1\n\t"
      "v_add_f32_e32 v115, v115, v187\n\t"
      "v_mfma_f32_16x16x32_bf16 v[200:203], v[148:151], v[80:83], v[200:203]\n\t"
      "s_waitcnt vmcnt(5)\n\t"
      "v_cmp_eq_u32_e32 vcc, -1, v152\n\t"
      "v_cvt_pk_bf16_f32 v116, v112, v113\n\t"
      "v_mfma_f32_16x16x32_bf16 v[204:207], v[148:151], v[84:87], v[204:207]\n\t"
      "v_cvt_pk_bf16_f32 v117, v114, v115\n\t"
      "global_store_dwordx2 v245, v[116:117], s[8:9] offset:64\n\t"
      "v_mfma_f32_16x16x32_bf16 v[208:211], v[148:151], v[88:91], v[208:211]\n\t"
      "s_cbranch_vccnz FP41R_%=\n\t"
      "FP41B_%=:\n\t"
      "v_mfma_f32_16x16x32_bf16 v[212:215], v[148:151], v[92:95], v[212:215]\n\t"
      "global_load_dwordx4 v[148:151], v229, s[64:65] sc1\n\t"
      "v_mfma_f32_16x16x32_bf16 v[200:203], v[152:155], v[96:99], v[200:203]\n\t"
      "s_waitcnt vmcnt(6)\n\t"
      "v_cmp_eq_u32_e32 vcc, -1, v128\n\t"
      "v_mfma_f32_16x16x32_bf16 v[204:207], v[152:155], v[100:103], v[204:207]\n\t"
      "v_mfma_f32_16x16x32_bf16 v[208:211], v[152:155], v[104:107], v[208:211]\n\t"
      "s_cbranch_vccnz FP42R_%=\n\t"
      "FP42B_%=:\n\t"
      "v_mfma_f32_16x16x32_bf16 v[212:215], v[152:155], v[108:111], v[212:215]\n\t"
      "global_load_dwordx4 v[152:155], v230, s[64:65] sc1\n\t"
      "s_nop 0\n\t"
      "v_cmp_u_f32_e32 vcc, v200, v201\n\t"
      "v_cmp_u_f32_e64 s[48:49], v202, v203\n\t"
      "s_nop 0\n\t"
      "s_or_b64 vcc, vcc, s[48:49]\n\t"
      "s_cbranch_scc1 NP5R_%=\n\t"
      "NP5B_%=:\n\t"
      "ds_write_b128 v243, v[200:203] offset:32768\n\t"
      "ds_write_b128 v220, v[204:207] offset:32768\n\t"
      "ds_write_b128 v221, v[208:211] offset:32768\n\t"
      "ds_write_b128 v222, v[212:215] offset:32768\n\t"
      "s_waitcnt lgkmcnt(0)\n\t"
      "s_barrier\n\t"
      "ds_read_b128 v[112:115], v244 offset:32768\n\t"
      "ds_read_b128 v[116:119], v244 offset:36864\n\t"
      "ds_read_b128 v[120:123], v244 offset:40960\n\t"
      "ds_read_b128 v[124:127], v244 offset:45056\n\t"
      "ds_read_b128 v[200:203], v244 offset:49152\n\t"
      "ds_read_b128 v[204:207], v244 offset:53248\n\t"
      "ds_read_b128 v[208:211], v244 offset:57344\n\t"
      "ds_read_b128 v[212:215], v244 offset:61440\n\t"
      "v_mfma_f32_16x16x32_bf16 v[184:187], v[128:131], v[0:3], v[180:183]\n\t"
      "s_waitcnt vmcnt(6)\n\t"
      "v_cmp_eq_u32_e32 vcc, -1, v132\n\t"
      "v_mfma_f32_16x16x32_bf16 v[188:191], v[128:131], v[4:7], 0\n\t"
      "v_mfma_f32_16x16x32_bf16 v[192:195], v[128:131], v[8:11], 0\n\t"
      "s_cbranch_vccnz FP43R_%=\n\t"
      "FP43B_%=:\n\t"
      "v_mfma_f32_16x16x32_bf16 v[196:199], v[128:131], v[12:15], 0\n\t"
      "v_mfma_f32_16x16x32_bf16 v[184:187], v[132:135], v[16:19], v[184:187]\n\t"
      "s_waitcnt vmcnt(5)\n\t"
      "v_cmp_eq_u32_e32 vcc, -1, v136\n\t"
      "s_waitcnt lgkmcnt(6)\n\t"
      "v_add_f32_e32 v112, v112, v116\n\t"
      "v_mfma_f32_16x16x32_bf16 v[188:191], v[132:135], v[20:23], v[188:191]\n\t"
      "v_add_f32_e32 v113, v113, v117\n\t"
      "v_add_f32_e32 v114, v114, v118\n\t"
      "v_mfma_f32_16x16x32_bf16 v[192:195], v[132:135], v[24:27], v[192:195]\n\t"
      "s_cbranch_vccnz FP44R_%=\n\t"
      "FP44B_%=:\n\t"
      "v_add_f32_e32 v115, v115, v119\n\t"
      "s_waitcnt lgkmcnt(4)\n\t"
      "v_add_f32_e32 v120, v120, v124\n\t"
      "v_mfma_f32_16x16x32_bf16 v[196:199], v[132:135], v[28:31], v[196:199]\n\t"
      "v_add_f32_e32 v121, v121, v125\n\t"
      "v_add_f32_e32 v122, v122, v126\n\t"
      "v_mfma_f32_16x16x32_bf16 v[184:187], v[136:139], v[32:35], v[184:187]\n\t"
      "s_waitcnt vmcnt(4)\n\t"
      "v_cmp_eq_u32_e32 vcc, -1, v140\n\t"
      "v_add_f32_e32 v123, v123, v127\n\t"
      "v_mfma_f32_16x16x32_bf16 v[188:191], v[136:139], v[36:39], v[188:191]\n\t"
      "s_waitcnt lgkmcnt(2)\n\t"
      "v_add_f32_e32 v200, v200, v204\n\t"
      "v_add_f32_e32 v201, v201, v205\n\t"
      "v_mfma_f32_16x16x32_bf16 v[192:195], v[136:139], v[40:43], v[192:195]\n\t"
      "s_cbranch_vccnz FP45R_%=\n\t"
      "FP45B_%=:\n\t"
      "v_add_f32_e32 v202, v202, v206\n\t"
      "v_add_f32_e32 v203, v203, v207\n\t"
      "v_mfma_f32_16x16x32_bf16 v[196:199], v[136:139], v[44:47], v[196:199]\n\t"
      "v_add_f32_e32 v112, v112, v120\n\t"
      "v_add_f32_e32 v113, v113, v121\n\t"
      "v_mfma_f32_16x16x32_bf16 v[184:187], v[140:143], v[48:51], v[184:187]\n\t"
      "s_waitcnt vmcnt(3)\n\t"
      "v_cmp_eq_u32_e32 vcc, -1, v144\n\t"
      "v_add_f32_e32 v114, v114, v122\n\t"
      "v_mfma_f32_16x16x32_bf16 v[188:191], v[140:143], v[52:55], v[188:191]\n\t"
      "v_add_f32_e32 v115, v115, v123\n\t"
      "s_waitcnt lgkmcnt(0)\n\t"
      "v_add_f32_e32 v208, v208, v212\n\t"
      "v_mfma_f32_16x16x32_bf16 v[192:195], v[140:143], v[56:59], v[192:195]\n\t"
      "s_cbranch_vccnz FP46R_%=\n\t"
      "FP46B_%=:\n\t"
      "v_add_f32_e32 v209, v209, v213\n\t"
      "v_add_f32_e32 v210, v210, v214\n\t"
      "v_mfma_f32_16x16x32_bf16 v[196:199], v[140:143], v[60:63], v[196:199]\n\t"
      "v_add_f32_e32 v211, v211, v215\n\t"
      "v_add_f32_e32 v200, v200, v208\n\t"
      "v_mfma_f32_16x16x32_bf16 v[184:187], v[144:147], v[64:67], v[184:187]\n\t"
      "s_waitcnt vmcnt(1)\n\t"
      "v_cmp_eq_u32_e32 vcc, -1, v148\n\t"
      "v_add_f32_e32 v201, v201, v209\n\t"
      "v_mfma_f32_16x16x32_bf16 v[188:191], v[144:147], v[68:71], v[188:191]\n\t"
      "v_add_f32_e32 v202, v202, v210\n\t"
      "v_add_f32_e32 v203, v203, v211\n\t"
      "v_mfma_f32_16x16x32_bf16 v[192:195], v[144:147], v[72:75], v[192:195]\n\t"
      "s_cbranch_vccnz FP47R_%=\n\t"
      "FP47B_%=:\n\t"
      "v_add_f32_e32 v112, v112, v200\n\t"
      "v_add_f32_e32 v113, v113, v201\n\t"
      "v_mfma_f32_16x16x32_bf16 v[196:199], v[144:147], v[76:79], v[196:199]\n\t"
      "v_add_f32_e32 v114, v114, v202\n\t"
      "v_add_f32_e32 v115, v115, v203\n\t"
      "v_mfma_f32_16x16x32_bf16 v[184:187], v[148:151], v[80:83], v[184:187]\n\t"
      "s_waitcnt vmcnt(0)\n\t"
      "v_cmp_eq_u32_e32 vcc, -1, v152\n\t"
      "v_cvt_pk_bf16_f32 v116, v112, v113\n\t"
      "v_mfma_f32_16x16x32_bf16 v[188:191], v[148:151], v[84:87], v[188:191]\n\t"
      "v_cvt_pk_bf16_f32 v117, v114, v115\n\t"
      "global_store_dwordx2 v245, v[116:117], s[8:9] offset:96\n\t"
      "v_mfma_f32_16x16x32_bf16 v[192:195], v[148:151], v[88:91], v[192:195]\n\t"
      "s_cbranch_vccnz FP48R_%=\n\t"
      "FP48B_%=:\n\t"
      "v_mfma_f32_16x16x32_bf16 v[196:199], v[148:151], v[92:95], v[196:199]\n\t"
      "v_mfma_f32_16x16x32_bf16 v[184:187], v[152:155], v[96:99], v[184:187]\n\t"
      "v_mfma_f32_16x16x32_bf16 v[188:191], v[152:155], v[100:103], v[188:191]\n\t"
      "v_mfma_f32_16x16x32_bf16 v[192:195], v[152:155], v[104:107], v[192:195]\n\t"
      "v_mfma_f32_16x16x32_bf16 v[196:199], v[152:155], v[108:111], v[196:199]\n\t"
      "s_nop 4\n\t"
      "v_cmp_u_f32_e32 vcc, v184, v185\n\t"
      "v_cmp_u_f32_e64 s[48:49], v186, v187\n\t"
      "s_nop 0\n\t"
      "s_or_b64 vcc, vcc, s[48:49]\n\t"
      "s_cbranch_scc1 NP6R_%=\n\t"
      "NP6B_%=:\n\t"
      "ds_write_b128 v243, v[184:187]\n\t"
      "ds_write_b128 v220, v[188:191]\n\t"
      "ds_write_b128 v221, v[192:195]\n\t"
      "ds_write_b128 v222, v[196:199]\n\t"
      "s_waitcnt lgkmcnt(0)\n\t"
      "s_barrier\n\t"
      "ds_read_b128 v[112:115], v244\n\t"
      "ds_read_b128 v[116:119], v244 offset:4096\n\t"
      "ds_read_b128 v[120:123], v244 offset:8192\n\t"
      "ds_read_b128 v[124:127], v244 offset:12288\n\t"
      "ds_read_b128 v[184:187], v244 offset:16384\n\t"
      "ds_read_b128 v[188:191], v244 offset:20480\n\t"
      "ds_read_b128 v[192:195], v244 offset:24576\n\t"
      "ds_read_b128 v[196:199], v244 offset:28672\n\t"
      "s_waitcnt lgkmcnt(6)\n\t"
      "v_add_f32_e32 v112, v112, v116\n\t"
      "v_add_f32_e32 v113, v113, v117\n\t"
      "v_add_f32_e32 v114, v114, v118\n\t"
      "v_add_f32_e32 v115, v115, v119\n\t"
      "s_waitcnt lgkmcnt(4)\n\t"
      "v_add_f32_e32 v120, v120, v124\n\t"
      "v_add_f32_e32 v121, v121, v125\n\t"
      "v_add_f32_e32 v122, v122, v126\n\t"
      "v_add_f32_e32 v123, v123, v127\n\t"
      "s_waitcnt lgkmcnt(2)\n\t"
      "v_add_f32_e32 v184, v184, v188\n\t"
      "v_add_f32_e32 v185, v185, v189\n\t"
      "v_add_f32_e32 v186, v186, v190\n\t"
      "v_add_f32_e32 v187, v187, v191\n\t"
      "v_add_f32_e32 v112, v112, v120\n\t"
      "v_add_f32_e32 v113, v113, v121\n\t"
      "v_add_f32_e32 v114, v114, v122\n\t"
      "v_add_f32_e32 v115, v115, v123\n\t"
      "s_waitcnt lgkmcnt(0)\n\t"
      "v_add_f32_e32 v192, v192, v196\n\t"
      "v_add_f32_e32 v193, v193, v197\n\t"
      "v_add_f32_e32 v194, v194, v198\n\t"
      "v_add_f32_e32 v195, v195, v199\n\t"
      "v_add_f32_e32 v184, v184, v192\n\t"
      "v_add_f32_e32 v185, v185, v193\n\t"
      "v_add_f32_e32 v186, v186, v194\n\t"
      "v_add_f32_e32 v187, v187, v195\n\t"
      "v_add_f32_e32 v112, v112, v184\n\t"
      "v_add_f32_e32 v113, v113, v185\n\t"
      "v_add_f32_e32 v114, v114, v186\n\t"
      "v_add_f32_e32 v115, v115, v187\n\t"
      "v_cvt_pk_bf16_f32 v116, v112, v113\n\t"
      "v_cvt_pk_bf16_f32 v117, v114, v115\n\t"
      "global_store_dwordx2 v245, v[116:117], s[66:67] offset:64\n\t"
      "GX_%=:\n\t"
      "s_bitcmp1_b32 s16, 5\n\t"
      "s_cbranch_scc0 EP_%=\n\t"
      "v_mov_b32_e32 v0, s26\n\t"
      "v_mov_b32_e32 v1, s27\n\t"
      "v_mov_b32_e32 v2, 0\n\t"
      "s_mov_b64 exec, 1\n\t"
      "global_store_dwordx2 v2, v[0:1], s[24:25] sc1\n\t"
      "s_mov_b64 exec, -1\n\t"
      "EP_%=:\n\t"
      "s_endpgm\n\t"
      ".p2align 6\n\t"
      "GQ_%=:\n\t"
      "s_waitcnt vmcnt(6)\n\t"
      "v_max3_u32 v234, v128, v129, v130\n\t"
      "v_max_u32_e32 v234, v234, v131\n\t"
      "v_cmp_eq_u32_e32 vcc, -1, v234\n\t"
      "s_cbranch_vccnz FQ0R_%=\n\t"
      "FQ0B_%=:\n\t"
      "v_mfma_f32_16x16x32_bf16 v[184:187], v[128:131], v[0:3], 0\n\t"
      "v_mfma_f32_16x16x32_bf16 v[188:191], v[128:131], v[4:7], 0\n\t"
      "v_mfma_f32_16x16x32_bf16 v[192:195], v[128:131], v[8:11], 0\n\t"
      "v_mfma_f32_16x16x32_bf16 v[196:199], v[128:131], v[12:15], 0\n\t"
      "global_load_dwordx4 v[128:131], v224, s[54:55] sc1\n\t"
      "s_waitcnt vmcnt(6)\n\t"
      "v_max3_u32 v234, v132, v133, v134\n\t"
      "v_max_u32_e32 v234, v234, v135\n\t"
      "v_cmp_eq_u32_e32 vcc, -1, v234\n\t"
      "s_cbranch_vccnz FQ1R_%=\n\t"
      "FQ1B_%=:\n\t"
      "v_mfma_f32_16x16x32_bf16 v[184:187], v[132:135], v[16:19], v[184:187]\n\t"
      "v_mfma_f32_16x16x32_bf16 v[188:191], v[132:135], v[20:23], v[188:191]\n\t"
      "v_mfma_f32_16x16x32_bf16 v[192:195], v[132:135], v[24:27], v[192:195]\n\t"
      "v_mfma_f32_16x16x32_bf16 v[196:199], v[132:135], v[28:31], v[196:199]\n\t"
      "global_load_dwordx4 v[132:135], v225, s[54:55] sc1\n\t"
      "s_waitcnt vmcnt(6)\n\t"
      "v_max3_u32 v234, v136, v137, v138\n\t"
      "v_max_u32_e32 v234, v234, v139\n\t"
      "v_cmp_eq_u32_e32 vcc, -1, v234\n\t"
      "s_cbranch_vccnz FQ2R_%=\n\t"
      "FQ2B_%=:\n\t"
      "v_mfma_f32_16x16x32_bf16 v[184:187], v[136:139], v[32:35], v[184:187]\n\t"
      "v_mfma_f32_16x16x32_bf16 v[188:191], v[136:139], v[36:39], v[188:191]\n\t"
      "v_mfma_f32_16x16x32_bf16 v[192:195], v[136:139], v[40:43], v[192:195]\n\t"
      "v_mfma_f32_16x16x32_bf16 v[196:199], v[136:139], v[44:47], v[196:199]\n\t"
      "global_load_dwordx4 v[136:139], v226, s[54:55] sc1\n\t"
      "s_waitcnt vmcnt(6)\n\t"
      "v_max3_u32 v234, v140, v141, v142\n\t"
      "v_max_u32_e32 v234, v234, v143\n\t"
      "v_cmp_eq_u32_e32 vcc, -1, v234\n\t"
      "s_cbranch_vccnz FQ3R_%=\n\t"
      "FQ3B_%=:\n\t"
      "v_mfma_f32_16x16x32_bf16 v[184:187], v[140:143], v[48:51], v[184:187]\n\t"
      "v_mfma_f32_16x16x32_bf16 v[188:191], v[140:143], v[52:55], v[188:191]\n\t"
      "v_mfma_f32_16x16x32_bf16 v[192:195], v[140:143], v[56:59], v[192:195]\n\t"
      "v_mfma_f32_16x16x32_bf16 v[196:199], v[140:143], v[60:63], v[196:199]\n\t"
      "global_load_dwordx4 v[140:143], v227, s[54:55] sc1\n\t"
      "s_waitcnt vmcnt(6)\n\t"
      "v_max3_u32 v234, v144, v145, v146\n\t"
      "v_max_u32_e32 v234, v234, v147\n\t"
      "v_cmp_eq_u32_e32 vcc, -1, v234\n\t"
      "s_cbranch_vccnz FQ4R_%=\n\t"
      "FQ4B_%=:\n\t"
      "v_mfma_f32_16x16x32_bf16 v[184:187], v[144:147], v[64:67], v[184:187]\n\t"
      "v_mfma_f32_16x16x32_bf16 v[188:191], v[144:147], v[68:71], v[188:191]\n\t"
      "v_mfma_f32_16x16x32_bf16 v[192:195], v[144:147], v[72:75], v[192:195]\n\t"
      "v_mfma_f32_16x16x32_bf16 v[196:199], v[144:147], v[76:79], v[196:199]\n\t"
      "global_load_dwordx4 v[144:147], v228, s[54:55] sc1\n\t"
      "s_waitcnt vmcnt(6)\n\t"
      "v_max3_u32 v234, v148, v149, v150\n\t"
      "v_max_u32_e32 v234, v234, v151\n\t"
      "v_cmp_eq_u32_e32 vcc, -1, v234\n\t"
      "s_cbranch_vccnz FQ5R_%=\n\t"
      "FQ5B_%=:\n\t"
      "v_mfma_f32_16x16x32_bf16 v[184:187], v[148:151], v[80:83], v[184:187]\n\t"
      "v_mfma_f32_16x16x32_bf16 v[188:191], v[148:151], v[84:87], v[188:191]\n\t"
      "v_mfma_f32_16x16x32_bf16 v[192:195], v[148:151], v[88:91], v[192:195]\n\t"
      "v_mfma_f32_16x16x32_bf16 v[196:199], v[148:151], v[92:95], v[196:199]\n\t"
      "global_load_dwordx4 v[148:151], v229, s[54:55] sc1\n\t"
      "s_waitcnt vmcnt(6)\n\t"
      "v_max3_u32 v234, v152, v153, v154\n\t"
      "v_max_u32_e32 v234, v234, v155\n\t"
      "v_cmp_eq_u32_e32 vcc, -1, v234\n\t"
      "s_cbranch_vccnz FQ6R_%=\n\t"
      "FQ6B_%=:\n\t"
      "v_mfma_f32_16x16x32_bf16 v[184:187], v[152:155], v[96:99], v[184:187]\n\t"
      "s_waitcnt vmcnt(5)\n\t"
      "v_cmp_eq_u32_e32 vcc, -1, v128\n\t"
      "v_mfma_f32_16x16x32_bf16 v[188:191], v[152:155], v[100:103], v[188:191]\n\t"
      "v_mfma_f32_16x16x32_bf16 v[192:195], v[152:155], v[104:107], v[192:195]\n\t"
      "s_cbranch_vccnz FQ7R_%=\n\t"
      "FQ7B_%=:\n\t"
      "v_mfma_f32_16x16x32_bf16 v[196:199], v[152:155], v[108:111], v[196:199]\n\t"
      "global_load_dwordx4 v[152:155], v230, s[54:55] sc1\n\t"
      "s_nop 0\n\t"
      "ds_write_b128 v243, v[184:187]\n\t"
      "s_nop 1\n\t"
      "ds_write_b128 v220, v[188:191]\n\t"
      "ds_write_b128 v221, v[192:195]\n\t"
      "s_nop 0\n\t"
      "ds_write_b128 v222, v[196:199]\n\t"
      "v_mfma_f32_16x16x32_bf16 v[200:203], v[128:131], v[0:3], 0\n\t"
      "s_waitcnt vmcnt(5)\n\t"
      "v_cmp_eq_u32_e32 vcc, -1, v132\n\t"
      "v_mfma_f32_16x16x32_bf16 v[204:207], v[128:131], v[4:7], 0\n\t"
      "v_mfma_f32_16x16x32_bf16 v[208:211], v[128:131], v[8:11], 0\n\t"
      "s_cbranch_vccnz FQ8R_%=\n\t"
      "FQ8B_%=:\n\t"
      "v_mfma_f32_16x16x32_bf16 v[212:215], v[128:131], v[12:15], 0\n\t"
      "global_load_dwordx4 v[128:131], v224, s[56:57] sc1\n\t"
      "v_mfma_f32_16x16x32_bf16 v[200:203], v[132:135], v[16:19], v[200:203]\n\t"
      "s_waitcnt vmcnt(5)\n\t"
      "v_cmp_eq_u32_e32 vcc, -1, v136\n\t"
      "v_mfma_f32_16x16x32_bf16 v[204:207], v[132:135], v[20:23], v[204:207]\n\t"
      "v_mfma_f32_16x16x32_bf16 v[208:211], v[132:135], v[24:27], v[208:211]\n\t"
      "s_cbranch_vccnz FQ9R_%=\n\t"
      "FQ9B_%=:\n\t"
      "v_mfma_f32_16x16x32_bf16 v[212:215], v[132:135], v[28:31], v[212:215]\n\t"
      "global_load_dwordx4 v[132:135], v225, s[56:57] sc1\n\t"
      "v_mfma_f32_16x16x32_bf16 v[200:203], v[136:139], v[32:35], v[200:203]\n\t"
      "s_waitcnt vmcnt(5)\n\t"
      "v_cmp_eq_u32_e32 vcc, -1, v140\n\t"
      "v_mfma_f32_16x16x32_bf16 v[204:207], v[136:139], v[36:39], v[204:207]\n\t"
      "v_mfma_f32_16x16x32_bf16 v[208:211], v[136:139], v[40:43], v[208:211]\n\t"
      "s_cbranch_vccnz FQ10R_%=\n\t"
      "FQ10B_%=:\n\t"
      "v_mfma_f32_16x16x32_bf16 v[212:215], v[136:139], v[44:47], v[212:215]\n\t"
      "global_load_dwordx4 v[136:139], v226, s[56:57] sc1\n\t"
      "v_mfma_f32_16x16x32_bf16 v[200:203], v[140:143], v[48:51], v[200:203]\n\t"
      "s_waitcnt vmcnt(5)\n\t"
      "v_cmp_eq_u32_e32 vcc, -1, v144\n\t"
      "v_mfma_f32_16x16x32_bf16 v[204:207], v[140:143], v[52:55], v[204:207]\n\t"
      "v_mfma_f32_16x16x32_bf16 v[208:211], v[140:143], v[56:59], v[208:211]\n\t"
      "s_cbranch_vccnz FQ11R_%=\n\t"
      "FQ11B_%=:\n\t"
      "v_mfma_f32_16x16x32_bf16 v[212:215], v[140:143], v[60:63], v[212:215]\n\t"
      "global_load_dwordx4 v[140:143], v227, s[56:57] sc1\n\t"
      "v_mfma_f32_16x16x32_bf16 v[200:203], v[144:147], v[64:67], v[200:203]\n\t"
      "s_waitcnt vmcnt(5)\n\t"
      "v_cmp_eq_u32_e32 vcc, -1, v148\n\t"
      "v_mfma_f32_16x16x32_bf16 v[204:207], v[144:147], v[68:71], v[204:207]\n\t"
      "v_mfma_f32_16x16x32_bf16 v[208:211], v[144:147], v[72:75], v[208:211]\n\t"
      "s_cbranch_vccnz FQ12R_%=\n\t"
      "FQ12B_%=:\n\t"
      "v_mfma_f32_16x16x32_bf16 v[212:215], v[144:147], v[76:79], v[212:215]\n\t"
      "global_load_dwordx4 v[144:147], v228, s[56:57] sc1\n\t"
      "s_waitcnt lgkmcnt(0)\n\t"
      "s_barrier\n\t"
      "s_setprio 2\n\t"
      "v_mfma_f32_16x16x32_bf16 v[200:203], v[148:151], v[80:83], v[200:203]\n\t"
      "s_waitcnt vmcnt(5)\n\t"
      "v_cmp_eq_u32_e32 vcc, -1, v152\n\t"
      "v_mfma_f32_16x16x32_bf16 v[204:207], v[148:151], v[84:87], v[204:207]\n\t"
      "v_mfma_f32_16x16x32_bf16 v[208:211], v[148:151], v[88:91], v[208:211]\n\t"
      "s_cbranch_vccnz FQ13R_%=\n\t"
      "FQ13B_%=:\n\t"
      "v_mfma_f32_16x16x32_bf16 v[212:215], v[148:151], v[92:95], v[212:215]\n\t"
      "global_load_dwordx4 v[148:151], v229, s[56:57] sc1\n\t"
      "v_mfma_f32_16x16x32_bf16 v[200:203], v[152:155], v[96:99], v[200:203]\n\t"
      "s_waitcnt vmcnt(5)\n\t"
      "v_cmp_eq_u32_e32 vcc, -1, v128\n\t"
      "v_mfma_f32_16x16x32_bf16 v[204:207], v[152:155], v[100:103], v[204:207]\n\t"
      "v_mfma_f32_16x16x32_bf16 v[208:211], v[152:155], v[104:107], v[208:211]\n\t"
      "s_cbranch_vccnz FQ14R_%=\n\t"
      "FQ14B_%=:\n\t"
      "v_mfma_f32_16x16x32_bf16 v[212:215], v[152:155], v[108:111], v[212:215]\n\t"
      "global_load_dwordx4 v[152:155], v230, s[56:57] sc1\n\t"
      "s_setprio 0\n\t"
      "v_cmp_u_f32_e32 vcc, v200, v201\n\t"
      "v_cmp_u_f32_e64 s[48:49], v202, v203\n\t"
      "s_nop 0\n\t"
      "s_or_b64 vcc, vcc, s[48:49]\n\t"
      "s_cbranch_scc1 NQ1R_%=\n\t"
      "NQ1B_%=:\n\t"
      "ds_write_b128 v243, v[200:203] offset:32768\n\t"
      "ds_write_b128 v220, v[204:207] offset:32768\n\t"
      "ds_write_b128 v221, v[208:211] offset:32768\n\t"
      "ds_write_b128 v222, v[212:215] offset:32768\n\t"
      "v_mfma_f32_16x16x32_bf16 v[184:187], v[128:131], v[0:3], 0\n\t"
      "s_waitcnt vmcnt(5)\n\t"
      "v_cmp_eq_u32_e32 vcc, -1, v132\n\t"
      "v_mfma_f32_16x16x32_bf16 v[188:191], v[128:131], v[4:7], 0\n\t"
      "v_mfma_f32_16x16x32_bf16 v[192:195], v[128:131], v[8:11], 0\n\t"
      "s_cbranch_vccnz FQ15R_%=\n\t"
      "FQ15B_%=:\n\t"
      "v_mfma_f32_16x16x32_bf16 v[196:199], v[128:131], v[12:15], 0\n\t"
      "global_load_dwordx4 v[128:131], v224, s[58:59] sc1\n\t"
      "v_mfma_f32_16x16x32_bf16 v[184:187], v[132:135], v[16:19], v[184:187]\n\t"
      "s_waitcnt vmcnt(5)\n\t"
      "v_cmp_eq_u32_e32 vcc, -1, v136\n\t"
      "v_mfma_f32_16x16x32_bf16 v[188:191], v[132:135], v[20:23], v[188:191]\n\t"
      "v_mfma_f32_16x16x32_bf16 v[192:195], v[132:135], v[24:27], v[192:195]\n\t"
      "s_cbranch_vccnz FQ16R_%=\n\t"
      "FQ16B_%=:\n\t"
      "v_mfma_f32_16x16x32_bf16 v[196:199], v[132:135], v[28:31], v[196:199]\n\t"
      "global_load_dwordx4 v[132:135], v225, s[58:59] sc1\n\t"
      "v_mfma_f32_16x16x32_bf16 v[184:187], v[136:139], v[32:35], v[184:187]\n\t"
      "s_waitcnt vmcnt(5)\n\t"
      "v_cmp_eq_u32_e32 vcc, -1, v140\n\t"
      "v_mfma_f32_16x16x32_bf16 v[188:191], v[136:139], v[36:39], v[188:191]\n\t"
      "v_mfma_f32_16x16x32_bf16 v[192:195], v[136:139], v[40:43], v[192:195]\n\t"
      "s_cbranch_vccnz FQ17R_%=\n\t"
      "FQ17B_%=:\n\t"
      "v_mfma_f32_16x16x32_bf16 v[196:199], v[136:139], v[44:47], v[196:199]\n\t"
      "global_load_dwordx4 v[136:139], v226, s[58:59] sc1\n\t"
      "v_mfma_f32_16x16x32_bf16 v[184:187], v[140:143], v[48:51], v[184:187]\n\t"
      "s_waitcnt vmcnt(5)\n\t"
      "v_cmp_eq_u32_e32 vcc, -1, v144\n\t"
      "v_mfma_f32_16x16x32_bf16 v[188:191], v[140:143], v[52:55], v[188:191]\n\t"
      "v_mfma_f32_16x16x32_bf16 v[192:195], v[140:143], v[56:59], v[192:195]\n\t"
      "s_cbranch_vccnz FQ18R_%=\n\t"
      "FQ18B_%=:\n\t"
      "v_mfma_f32_16x16x32_bf16 v[196:199], v[140:143], v[60:63], v[196:199]\n\t"
      "global_load_dwordx4 v[140:143], v227, s[58:59] sc1\n\t"
      "v_mfma_f32_16x16x32_bf16 v[184:187], v[144:147], v[64:67], v[184:187]\n\t"
      "s_waitcnt vmcnt(5)\n\t"
      "v_cmp_eq_u32_e32 vcc, -1, v148\n\t"
      "v_mfma_f32_16x16x32_bf16 v[188:191], v[144:147], v[68:71], v[188:191]\n\t"
      "v_mfma_f32_16x16x32_bf16 v[192:195], v[144:147], v[72:75], v[192:195]\n\t"
      "s_cbranch_vccnz FQ19R_%=\n\t"
      "FQ19B_%=:\n\t"
      "v_mfma_f32_16x16x32_bf16 v[196:199], v[144:147], v[76:79], v[196:199]\n\t"
      "global_load_dwordx4 v[144:147], v228, s[58:59] sc1\n\t"
      "s_waitcnt lgkmcnt(0)\n\t"
      "s_barrier\n\t"
      "s_setprio 2\n\t"
      "v_mfma_f32_16x16x32_bf16 v[184:187], v[148:151], v[80:83], v[184:187]\n\t"
      "s_waitcnt vmcnt(5)\n\t"
      "v_cmp_eq_u32_e32 vcc, -1, v152\n\t"
      "v_mfma_f32_16x16x32_bf16 v[188:191], v[148:151], v[84:87], v[188:191]\n\t"
      "v_mfma_f32_16x16x32_bf16 v[192:195], v[148:151], v[88:91], v[192:195]\n\t"
      "s_cbranch_vccnz FQ20R_%=\n\t"
      "FQ20B_%=:\n\t"
      "v_mfma_f32_16x16x32_bf16 v[196:199], v[148:151], v[92:95], v[196:199]\n\t"
      "global_load_dwordx4 v[148:151], v229, s[58:59] sc1\n\t"
      "v_mfma_f32_16x16x32_bf16 v[184:187], v[152:155], v[96:99], v[184:187]\n\t"
      "s_waitcnt vmcnt(5)\n\t"
      "v_cmp_eq_u32_e32 vcc, -1, v128\n\t"
      "v_mfma_f32_16x16x32_bf16 v[188:191], v[152:155], v[100:103], v[188:191]\n\t"
      "v_mfma_f32_16x16x32_bf16 v[192:195], v[152:155], v[104:107], v[192:195]\n\t"
      "s_cbranch_vccnz FQ21R_%=\n\t"
      "FQ21B_%=:\n\t"
      "v_mfma_f32_16x16x32_bf16 v[196:199], v[152:155], v[108:111], v[196:199]\n\t"
      "global_load_dwordx4 v[152:155], v230, s[58:59] sc1\n\t"
      "s_setprio 0\n\t"
      "v_cmp_u_f32_e32 vcc, v184, v185\n\t"
      "v_cmp_u_f32_e64 s[48:49], v186, v187\n\t"
      "s_nop 0\n\t"
      "s_or_b64 vcc, vcc, s[48:49]\n\t"
      "s_cbranch_scc1 NQ2R_%=\n\t"
      "NQ2B_%=:\n\t"
      "ds_write_b128 v243, v[184:187]\n\t"
      "ds_write_b128 v220, v[188:191]\n\t"
      "ds_write_b128 v221, v[192:195]\n\t"
      "ds_write_b128 v222, v[196:199]\n\t"
      "v_mfma_f32_16x16x32_bf16 v[200:203], v[128:131], v[0:3], 0\n\t"
      "s_waitcnt vmcnt(5)\n\t"
      "v_cmp_eq_u32_e32 vcc, -1, v132\n\t"
      "v_mfma_f32_16x16x32_bf16 v[204:207], v[128:131], v[4:7], 0\n\t"
      "v_mfma_f32_16x16x32_bf16 v[208:211], v[128:131], v[8:11], 0\n\t"
      "s_cbranch_vccnz FQ22R_%=\n\t"
      "FQ22B_%=:\n\t"
      "v_mfma_f32_16x16x32_bf16 v[212:215], v[128:131], v[12:15], 0\n\t"
      "global_load_dwordx4 v[128:131], v224, s[60:61] sc1\n\t"
      "v_mfma_f32_16x16x32_bf16 v[200:203], v[132:135], v[16:19], v[200:203]\n\t"
      "s_waitcnt vmcnt(5)\n\t"
      "v_cmp_eq_u32_e32 vcc, -1, v136\n\t"
      "v_mfma_f32_16x16x32_bf16 v[204:207], v[132:135], v[20:23], v[204:207]\n\t"
      "v_mfma_f32_16x16x32_bf16 v[208:211], v[132:135], v[24:27], v[208:211]\n\t"
      "s_cbranch_vccnz FQ23R_%=\n\t"
      "FQ23B_%=:\n\t"
      "v_mfma_f32_16x16x32_bf16 v[212:215], v[132:135], v[28:31], v[212:215]\n\t"
      "global_load_dwordx4 v[132:135], v225, s[60:61] sc1\n\t"
      "v_mfma_f32_16x16x32_bf16 v[200:203], v[136:139], v[32:35], v[200:203]\n\t"
      "s_waitcnt vmcnt(5)\n\t"
      "v_cmp_eq_u32_e32 vcc, -1, v140\n\t"
      "v_mfma_f32_16x16x32_bf16 v[204:207], v[136:139], v[36:39], v[204:207]\n\t"
      "v_mfma_f32_16x16x32_bf16 v[208:211], v[136:139], v[40:43], v[208:211]\n\t"
      "s_cbranch_vccnz FQ24R_%=\n\t"
      "FQ24B_%=:\n\t"
      "v_mfma_f32_16x16x32_bf16 v[212:215], v[136:139], v[44:47], v[212:215]\n\t"
      "global_load_dwordx4 v[136:139], v226, s[60:61] sc1\n\t"
      "v_mfma_f32_16x16x32_bf16 v[200:203], v[140:143], v[48:51], v[200:203]\n\t"
      "s_waitcnt vmcnt(5)\n\t"
      "v_cmp_eq_u32_e32 vcc, -1, v144\n\t"
      "v_mfma_f32_16x16x32_bf16 v[204:207], v[140:143], v[52:55], v[204:207]\n\t"
      "v_mfma_f32_16x16x32_bf16 v[208:211], v[140:143], v[56:59], v[208:211]\n\t"
      "s_cbranch_vccnz FQ25R_%=\n\t"
      "FQ25B_%=:\n\t"
      "v_mfma_f32_16x16x32_bf16 v[212:215], v[140:143], v[60:63], v[212:215]\n\t"
      "global_load_dwordx4 v[140:143], v227, s[60:61] sc1\n\t"
      "v_mfma_f32_16x16x32_bf16 v[200:203], v[144:147], v[64:67], v[200:203]\n\t"
      "s_waitcnt vmcnt(5)\n\t"
      "v_cmp_eq_u32_e32 vcc, -1, v148\n\t"
      "v_mfma_f32_16x16x32_bf16 v[204:207], v[144:147], v[68:71], v[204:207]\n\t"
      "v_mfma_f32_16x16x32_bf16 v[208:211], v[144:147], v[72:75], v[208:211]\n\t"
      "s_cbranch_vccnz FQ26R_%=\n\t"
      "FQ26B_%=:\n\t"
      "v_mfma_f32_16x16x32_bf16 v[212:215], v[144:147], v[76:79], v[212:215]\n\t"
      "global_load_dwordx4 v[144:147], v228, s[60:61] sc1\n\t"
      "s_waitcnt lgkmcnt(0)\n\t"
      "s_barrier\n\t"
      "s_setprio 2\n\t"
      "v_mfma_f32_16x16x32_bf16 v[200:203], v[148:151], v[80:83], v[200:203]\n\t"
      "s_waitcnt vmcnt(5)\n\t"
      "v_cmp_eq_u32_e32 vcc, -1, v152\n\t"
      "v_mfma_f32_16x16x32_bf16 v[204:207], v[148:151], v[84:87], v[204:207]\n\t"
      "v_mfma_f32_16x16x32_bf16 v[208:211], v[148:151], v[88:91], v[208:211]\n\t"
      "s_cbranch_vccnz FQ27R_%=\n\t"
      "FQ27B_%=:\n\t"
      "v_mfma_f32_16x16x32_bf16 v[212:215], v[148:151], v[92:95], v[212:215]\n\t"
      "global_load_dwordx4 v[148:151], v229, s[60:61] sc1\n\t"
      "v_mfma_f32_16x16x32_bf16 v[200:203], v[152:155], v[96:99], v[200:203]\n\t"
      "s_waitcnt vmcnt(5)\n\t"
      "v_cmp_eq_u32_e32 vcc, -1, v128\n\t"
      "v_mfma_f32_16x16x32_bf16 v[204:207], v[152:155], v[100:103], v[204:207]\n\t"
      "v_mfma_f32_16x16x32_bf16 v[208:211], v[152:155], v[104:107], v[208:211]\n\t"
      "s_cbranch_vccnz FQ28R_%=\n\t"
      "FQ28B_%=:\n\t"
      "v_mfma_f32_16x16x32_bf16 v[212:215], v[152:155], v[108:111], v[212:215]\n\t"
      "global_load_dwordx4 v[152:155], v230, s[60:61] sc1\n\t"
      "s_setprio 0\n\t"
      "v_cmp_u_f32_e32 vcc, v200, v201\n\t"
      "v_cmp_u_f32_e64 s[48:49], v202, v203\n\t"
      "s_nop 0\n\t"
      "s_or_b64 vcc, vcc, s[48:49]\n\t"
      "s_cbranch_scc1 NQ3R_%=\n\t"
      "NQ3B_%=:\n\t"
      "ds_write_b128 v243, v[200:203] offset:32768\n\t"
      "ds_write_b128 v220, v[204:207] offset:32768\n\t"
      "ds_write_b128 v221, v[208:211] offset:32768\n\t"
      "ds_write_b128 v222, v[212:215] offset:32768\n\t"
      "v_mfma_f32_16x16x32_bf16 v[184:187], v[128:131], v[0:3], 0\n\t"
      "s_waitcnt vmcnt(5)\n\t"
      "v_cmp_eq_u32_e32 vcc, -1, v132\n\t"
      "v_mfma_f32_16x16x32_bf16 v[188:191], v[128:131], v[4:7], 0\n\t"
      "v_mfma_f32_16x16x32_bf16 v[192:195], v[128:131], v[8:11], 0\n\t"
      "s_cbranch_vccnz FQ29R_%=\n\t"
      "FQ29B_%=:\n\t"
      "v_mfma_f32_16x16x32_bf16 v[196:199], v[128:131], v[12:15], 0\n\t"
      "global_load_dwordx4 v[128:131], v224, s[62:63] sc1\n\t"
      "v_mfma_f32_16x16x32_bf16 v[184:187], v[132:135], v[16:19], v[184:187]\n\t"
      "s_waitcnt vmcnt(5)\n\t"
      "v_cmp_eq_u32_e32 vcc, -1, v136\n\t"
      "v_mfma_f32_16x16x32_bf16 v[188:191], v[132:135], v[20:23], v[188:191]\n\t"
      "v_mfma_f32_16x16x32_bf16 v[192:195], v[132:135], v[24:27], v[192:195]\n\t"
      "s_cbranch_vccnz FQ30R_%=\n\t"
      "FQ30B_%=:\n\t"
      "v_mfma_f32_16x16x32_bf16 v[196:199], v[132:135], v[28:31], v[196:199]\n\t"
      "global_load_dwordx4 v[132:135], v225, s[62:63] sc1\n\t"
      "v_mfma_f32_16x16x32_bf16 v[184:187], v[136:139], v[32:35], v[184:187]\n\t"
      "s_waitcnt vmcnt(5)\n\t"
      "v_cmp_eq_u32_e32 vcc, -1, v140\n\t"
      "v_mfma_f32_16x16x32_bf16 v[188:191], v[136:139], v[36:39], v[188:191]\n\t"
      "v_mfma_f32_16x16x32_bf16 v[192:195], v[136:139], v[40:43], v[192:195]\n\t"
      "s_cbranch_vccnz FQ31R_%=\n\t"
      "FQ31B_%=:\n\t"
      "v_mfma_f32_16x16x32_bf16 v[196:199], v[136:139], v[44:47], v[196:199]\n\t"
      "global_load_dwordx4 v[136:139], v226, s[62:63] sc1\n\t"
      "v_mfma_f32_16x16x32_bf16 v[184:187], v[140:143], v[48:51], v[184:187]\n\t"
      "s_waitcnt vmcnt(5)\n\t"
      "v_cmp_eq_u32_e32 vcc, -1, v144\n\t"
      "v_mfma_f32_16x16x32_bf16 v[188:191], v[140:143], v[52:55], v[188:191]\n\t"
      "v_mfma_f32_16x16x32_bf16 v[192:195], v[140:143], v[56:59], v[192:195]\n\t"
      "s_cbranch_vccnz FQ32R_%=\n\t"
      "FQ32B_%=:\n\t"
      "v_mfma_f32_16x16x32_bf16 v[196:199], v[140:143], v[60:63], v[196:199]\n\t"
      "global_load_dwordx4 v[140:143], v227, s[62:63] sc1\n\t"
      "v_mfma_f32_16x16x32_bf16 v[184:187], v[144:147], v[64:67], v[184:187]\n\t"
      "s_waitcnt vmcnt(5)\n\t"
      "v_cmp_eq_u32_e32 vcc, -1, v148\n\t"
      "v_mfma_f32_16x16x32_bf16 v[188:191], v[144:147], v[68:71], v[188:191]\n\t"
      "v_mfma_f32_16x16x32_bf16 v[192:195], v[144:147], v[72:75], v[192:195]\n\t"
      "s_cbranch_vccnz FQ33R_%=\n\t"
      "FQ33B_%=:\n\t"
      "v_mfma_f32_16x16x32_bf16 v[196:199], v[144:147], v[76:79], v[196:199]\n\t"
      "global_load_dwordx4 v[144:147], v228, s[62:63] sc1\n\t"
      "s_waitcnt lgkmcnt(0)\n\t"
      "s_barrier\n\t"
      "s_setprio 2\n\t"
      "v_mfma_f32_16x16x32_bf16 v[184:187], v[148:151], v[80:83], v[184:187]\n\t"
      "s_waitcnt vmcnt(5)\n\t"
      "v_cmp_eq_u32_e32 vcc, -1, v152\n\t"
      "v_mfma_f32_16x16x32_bf16 v[188:191], v[148:151], v[84:87], v[188:191]\n\t"
      "v_mfma_f32_16x16x32_bf16 v[192:195], v[148:151], v[88:91], v[192:195]\n\t"
      "s_cbranch_vccnz FQ34R_%=\n\t"
      "FQ34B_%=:\n\t"
      "v_mfma_f32_16x16x32_bf16 v[196:199], v[148:151], v[92:95], v[196:199]\n\t"
      "global_load_dwordx4 v[148:151], v229, s[62:63] sc1\n\t"
      "v_mfma_f32_16x16x32_bf16 v[184:187], v[152:155], v[96:99], v[184:187]\n\t"
      "s_waitcnt vmcnt(5)\n\t"
      "v_cmp_eq_u32_e32 vcc, -1, v128\n\t"
      "v_mfma_f32_16x16x32_bf16 v[188:191], v[152:155], v[100:103], v[188:191]\n\t"
      "v_mfma_f32_16x16x32_bf16 v[192:195], v[152:155], v[104:107], v[192:195]\n\t"
      "s_cbranch_vccnz FQ35R_%=\n\t"
      "FQ35B_%=:\n\t"
      "v_mfma_f32_16x16x32_bf16 v[196:199], v[152:155], v[108:111], v[196:199]\n\t"
      "global_load_dwordx4 v[152:155], v230, s[62:63] sc1\n\t"
      "s_setprio 0\n\t"
      "v_cmp_u_f32_e32 vcc, v184, v185\n\t"
      "v_cmp_u_f32_e64 s[48:49], v186, v187\n\t"
      "s_nop 0\n\t"
      "s_or_b64 vcc, vcc, s[48:49]\n\t"
      "s_cbranch_scc1 NQ4R_%=\n\t"
      "NQ4B_%=:\n\t"
      "ds_write_b128 v243, v[184:187]\n\t"
      "ds_write_b128 v220, v[188:191]\n\t"
      "ds_write_b128 v221, v[192:195]\n\t"
      "ds_write_b128 v222, v[196:199]\n\t"
      "v_mfma_f32_16x16x32_bf16 v[200:203], v[128:131], v[0:3], 0\n\t"
      "s_waitcnt vmcnt(5)\n\t"
      "v_cmp_eq_u32_e32 vcc, -1, v132\n\t"
      "v_mfma_f32_16x16x32_bf16 v[204:207], v[128:131], v[4:7], 0\n\t"
      "v_mfma_f32_16x16x32_bf16 v[208:211], v[128:131], v[8:11], 0\n\t"
      "s_cbranch_vccnz FQ36R_%=\n\t"
      "FQ36B_%=:\n\t"
      "v_mfma_f32_16x16x32_bf16 v[212:215], v[128:131], v[12:15], 0\n\t"
      "global_load_dwordx4 v[128:131], v224, s[64:65] sc1\n\t"
      "v_mfma_f32_16x16x32_bf16 v[200:203], v[132:135], v[16:19], v[200:203]\n\t"
      "s_waitcnt vmcnt(5)\n\t"
      "v_cmp_eq_u32_e32 vcc, -1, v136\n\t"
      "v_mfma_f32_16x16x32_bf16 v[204:207], v[132:135], v[20:23], v[204:207]\n\t"
      "v_mfma_f32_16x16x32_bf16 v[208:211], v[132:135], v[24:27], v[208:211]\n\t"
      "s_cbranch_vccnz FQ37R_%=\n\t"
      "FQ37B_%=:\n\t"
      "v_mfma_f32_16x16x32_bf16 v[212:215], v[132:135], v[28:31], v[212:215]\n\t"
      "global_load_dwordx4 v[132:135], v225, s[64:65] sc1\n\t"
      "v_mfma_f32_16x16x32_bf16 v[200:203], v[136:139], v[32:35], v[200:203]\n\t"
      "s_waitcnt vmcnt(5)\n\t"
      "v_cmp_eq_u32_e32 vcc, -1, v140\n\t"
      "v_mfma_f32_16x16x32_bf16 v[204:207], v[136:139], v[36:39], v[204:207]\n\t"
      "v_mfma_f32_16x16x32_bf16 v[208:211], v[136:139], v[40:43], v[208:211]\n\t"
      "s_cbranch_vccnz FQ38R_%=\n\t"
      "FQ38B_%=:\n\t"
      "v_mfma_f32_16x16x32_bf16 v[212:215], v[136:139], v[44:47], v[212:215]\n\t"
      "global_load_dwordx4 v[136:139], v226, s[64:65] sc1\n\t"
      "v_mfma_f32_16x16x32_bf16 v[200:203], v[140:143], v[48:51], v[200:203]\n\t"
      "s_waitcnt vmcnt(5)\n\t"
      "v_cmp_eq_u32_e32 vcc, -1, v144\n\t"
      "v_mfma_f32_16x16x32_bf16 v[204:207], v[140:143], v[52:55], v[204:207]\n\t"
      "v_mfma_f32_16x16x32_bf16 v[208:211], v[140:143], v[56:59], v[208:211]\n\t"
      "s_cbranch_vccnz FQ39R_%=\n\t"
      "FQ39B_%=:\n\t"
      "v_mfma_f32_16x16x32_bf16 v[212:215], v[140:143], v[60:63], v[212:215]\n\t"
      "global_load_dwordx4 v[140:143], v227, s[64:65] sc1\n\t"
      "v_mfma_f32_16x16x32_bf16 v[200:203], v[144:147], v[64:67], v[200:203]\n\t"
      "s_waitcnt vmcnt(5)\n\t"
      "v_cmp_eq_u32_e32 vcc, -1, v148\n\t"
      "v_mfma_f32_16x16x32_bf16 v[204:207], v[144:147], v[68:71], v[204:207]\n\t"
      "v_mfma_f32_16x16x32_bf16 v[208:211], v[144:147], v[72:75], v[208:211]\n\t"
      "s_cbranch_vccnz FQ40R_%=\n\t"
      "FQ40B_%=:\n\t"
      "v_mfma_f32_16x16x32_bf16 v[212:215], v[144:147], v[76:79], v[212:215]\n\t"
      "global_load_dwordx4 v[144:147], v228, s[64:65] sc1\n\t"
      "s_waitcnt lgkmcnt(0)\n\t"
      "s_barrier\n\t"
      "s_setprio 2\n\t"
      "v_mfma_f32_16x16x32_bf16 v[200:203], v[148:151], v[80:83], v[200:203]\n\t"
      "s_waitcnt vmcnt(5)\n\t"
      "v_cmp_eq_u32_e32 vcc, -1, v152\n\t"
      "v_mfma_f32_16x16x32_bf16 v[204:207], v[148:151], v[84:87], v[204:207]\n\t"
      "v_mfma_f32_16x16x32_bf16 v[208:211], v[148:151], v[88:91], v[208:211]\n\t"
      "s_cbranch_vccnz FQ41R_%=\n\t"
      "FQ41B_%=:\n\t"
      "v_mfma_f32_16x16x32_bf16 v[212:215], v[148:151], v[92:95], v[212:215]\n\t"
      "global_load_dwordx4 v[148:151], v229, s[64:65] sc1\n\t"
      "v_mfma_f32_16x16x32_bf16 v[200:203], v[152:155], v[96:99], v[200:203]\n\t"
      "s_waitcnt vmcnt(5)\n\t"
      "v_cmp_eq_u32_e32 vcc, -1, v128\n\t"
      "v_mfma_f32_16x16x32_bf16 v[204:207], v[152:155], v[100:103], v[204:207]\n\t"
      "v_mfma_f32_16x16x32_bf16 v[208:211], v[152:155], v[104:107], v[208:211]\n\t"
      "s_cbranch_vccnz FQ42R_%=\n\t"
      "FQ42B_%=:\n\t"
      "v_mfma_f32_16x16x32_bf16 v[212:215], v[152:155], v[108:111], v[212:215]\n\t"
      "global_load_dwordx4 v[152:155], v230, s[64:65] sc1\n\t"
      "s_setprio 0\n\t"
      "v_cmp_u_f32_e32 vcc, v200, v201\n\t"
      "v_cmp_u_f32_e64 s[48:49], v202, v203\n\t"
      "s_nop 0\n\t"
      "s_or_b64 vcc, vcc, s[48:49]\n\t"
      "s_cbranch_scc1 NQ5R_%=\n\t"
      "NQ5B_%=:\n\t"
      "ds_write_b128 v243, v[200:203] offset:32768\n\t"
      "ds_write_b128 v220, v[204:207] offset:32768\n\t"
      "ds_write_b128 v221, v[208:211] offset:32768\n\t"
      "ds_write_b128 v222, v[212:215] offset:32768\n\t"
      "v_mfma_f32_16x16x32_bf16 v[184:187], v[128:131], v[0:3], 0\n\t"
      "s_waitcnt vmcnt(5)\n\t"
      "v_cmp_eq_u32_e32 vcc, -1, v132\n\t"
      "v_mfma_f32_16x16x32_bf16 v[188:191], v[128:131], v[4:7], 0\n\t"
      "v_mfma_f32_16x16x32_bf16 v[192:195], v[128:131], v[8:11], 0\n\t"
      "s_cbranch_vccnz FQ43R_%=\n\t"
      "FQ43B_%=:\n\t"
      "v_mfma_f32_16x16x32_bf16 v[196:199], v[128:131], v[12:15], 0\n\t"
      "v_mfma_f32_16x16x32_bf16 v[184:187], v[132:135], v[16:19], v[184:187]\n\t"
      "s_waitcnt vmcnt(4)\n\t"
      "v_cmp_eq_u32_e32 vcc, -1, v136\n\t"
      "v_mfma_f32_16x16x32_bf16 v[188:191], v[132:135], v[20:23], v[188:191]\n\t"
      "v_mfma_f32_16x16x32_bf16 v[192:195], v[132:135], v[24:27], v[192:195]\n\t"
      "s_cbranch_vccnz FQ44R_%=\n\t"
      "FQ44B_%=:\n\t"
      "v_mfma_f32_16x16x32_bf16 v[196:199], v[132:135], v[28:31], v[196:199]\n\t"
      "v_mfma_f32_16x16x32_bf16 v[184:187], v[136:139], v[32:35], v[184:187]\n\t"
      "s_waitcnt vmcnt(3)\n\t"
      "v_cmp_eq_u32_e32 vcc, -1, v140\n\t"
      "v_mfma_f32_16x16x32_bf16 v[188:191], v[136:139], v[36:39], v[188:191]\n\t"
      "v_mfma_f32_16x16x32_bf16 v[192:195], v[136:139], v[40:43], v[192:195]\n\t"
      "s_cbranch_vccnz FQ45R_%=\n\t"
      "FQ45B_%=:\n\t"
      "v_mfma_f32_16x16x32_bf16 v[196:199], v[136:139], v[44:47], v[196:199]\n\t"
      "v_mfma_f32_16x16x32_bf16 v[184:187], v[140:143], v[48:51], v[184:187]\n\t"
      "s_waitcnt vmcnt(2)\n\t"
      "v_cmp_eq_u32_e32 vcc, -1, v144\n\t"
      "v_mfma_f32_16x16x32_bf16 v[188:191], v[140:143], v[52:55], v[188:191]\n\t"
      "v_mfma_f32_16x16x32_bf16 v[192:195], v[140:143], v[56:59], v[192:195]\n\t"
      "s_cbranch_vccnz FQ46R_%=\n\t"
      "FQ46B_%=:\n\t"
      "v_mfma_f32_16x16x32_bf16 v[196:199], v[140:143], v[60:63], v[196:199]\n\t"
      "v_mfma_f32_16x16x32_bf16 v[184:187], v[144:147], v[64:67], v[184:187]\n\t"
      "s_waitcnt vmcnt(1)\n\t"
      "v_cmp_eq_u32_e32 vcc, -1, v148\n\t"
      "v_mfma_f32_16x16x32_bf16 v[188:191], v[144:147], v[68:71], v[188:191]\n\t"
      "v_mfma_f32_16x16x32_bf16 v[192:195], v[144:147], v[72:75], v[192:195]\n\t"
      "s_cbranch_vccnz FQ47R_%=\n\t"
      "FQ47B_%=:\n\t"
      "v_mfma_f32_16x16x32_bf16 v[196:199], v[144:147], v[76:79], v[196:199]\n\t"
      "s_waitcnt lgkmcnt(0)\n\t"
      "s_barrier\n\t"
      "s_setprio 2\n\t"
      "v_mfma_f32_16x16x32_bf16 v[184:187], v[148:151], v[80:83], v[184:187]\n\t"
      "s_waitcnt vmcnt(0)\n\t"
      "v_cmp_eq_u32_e32 vcc, -1, v152\n\t"
      "v_mfma_f32_16x16x32_bf16 v[188:191], v[148:151], v[84:87], v[188:191]\n\t"
      "v_mfma_f32_16x16x32_bf16 v[192:195], v[148:151], v[88:91], v[192:195]\n\t"
      "s_cbranch_vccnz FQ48R_%=\n\t"
      "FQ48B_%=:\n\t"
      "v_mfma_f32_16x16x32_bf16 v[196:199], v[148:151], v[92:95], v[196:199]\n\t"
      "v_mfma_f32_16x16x32_bf16 v[184:187], v[152:155], v[96:99], v[184:187]\n\t"
      "v_mfma_f32_16x16x32_bf16 v[188:191], v[152:155], v[100:103], v[188:191]\n\t"
      "v_mfma_f32_16x16x32_bf16 v[192:195], v[152:155], v[104:107], v[192:195]\n\t"
      "v_mfma_f32_16x16x32_bf16 v[196:199], v[152:155], v[108:111], v[196:199]\n\t"
      "s_nop 4\n\t"
      "v_cmp_u_f32_e32 vcc, v184, v185\n\t"
      "v_cmp_u_f32_e64 s[48:49], v186, v187\n\t"
      "s_nop 0\n\t"
      "s_or_b64 vcc, vcc, s[48:49]\n\t"
      "s_cbranch_scc1 NQ6R_%=\n\t"
      "NQ6B_%=:\n\t"
      "ds_write_b128 v243, v[184:187]\n\t"
      "ds_write_b128 v220, v[188:191]\n\t"
      "ds_write_b128 v221, v[192:195]\n\t"
      "ds_write_b128 v222, v[196:199]\n\t"
      "s_waitcnt lgkmcnt(0)\n\t"
      "s_barrier\n\t"
      "s_branch GX_%=\n\t"
      "HWR_%=:\n\t"
      "s_mov_b32 s51, 0\n\t"
      "HWL_%=:\n\t"
      "s_add_u32 s51, s51, 1\n\t"
      "s_cmp_lt_u32 s51, 262144\n\t"
      "s_cbranch_scc0 HWX_%=\n\t"
      "s_sleep 2\n\t"
      "global_load_dwordx4 v[112:115], v116, s[12:13] sc1\n\t"
      "s_waitcnt vmcnt(0)\n\t"
      "v_xor_b32_e32 v114, v114, v112\n\t"
      "v_xor_b32_e32 v115, v115, v113\n\t"
      "v_cmp_ne_u64_e32 vcc, s[14:15], v[114:115]\n\t"
      "s_cbranch_vccnz HWL_%=\n\t"
      "HWX_%=:\n\t"
      "s_branch HWB_%=\n\t"
      "HXR_%=:\n\t"
      "s_mov_b32 s51, 0\n\t"
      "HXL_%=:\n\t"
      "s_add_u32 s51, s51, 1\n\t"
      "s_cmp_lt_u32 s51, 262144\n\t"
      "s_cbranch_scc0 HXX_%=\n\t"
      "s_sleep 2\n\t"
      "s_mov_b32 exec_hi, s23\n\t"
      "s_not_b32 exec_lo, s23\n\t"
      "s_add_u32 s68, s20, 0\n\t"
      "s_addc_u32 s69, s21, 0\n\t"
      "global_load_dwordx4 v[184:187], v240, s[68:69] sc1\n\t"
      "s_add_u32 s68, s20, 131072\n\t"
      "s_addc_u32 s69, s21, 0\n\t"
      "global_load_dwordx4 v[188:191], v240, s[68:69] sc1\n\t"
      "s_add_u32 s68, s20, 262144\n\t"
      "s_addc_u32 s69, s21, 0\n\t"
      "global_load_dwordx4 v[192:195], v240, s[68:69] sc1\n\t"
      "s_add_u32 s68, s20, 393216\n\t"
      "s_addc_u32 s69, s21, 0\n\t"
      "global_load_dwordx4 v[196:199], v240, s[68:69] sc1\n\t"
      "s_add_u32 s68, s20, 524288\n\t"
      "s_addc_u32 s69, s21, 0\n\t"
      "global_load_dwordx4 v[200:203], v240, s[68:69] sc1\n\t"
      "s_add_u32 s68, s20, 655360\n\t"
      "s_addc_u32 s69, s21, 0\n\t"
      "global_load_dwordx4 v[204:207], v240, s[68:69] sc1\n\t"
      "s_add_u32 s68, s20, 786432\n\t"
      "s_addc_u32 s69, s21, 0\n\t"
      "global_load_dwordx4 v[208:211], v240, s[68:69] sc1\n\t"
      "s_mov_b64 exec, -1\n\t"
      "s_waitcnt vmcnt(0)\n\t"
      "v_max3_u32 v234, v184, v185, v186\n\t"
      "v_max3_u32 v234, v234, v187, v188\n\t"
      "v_max3_u32 v234, v234, v189, v190\n\t"
      "v_max3_u32 v234, v234, v191, v192\n\t"
      "v_max3_u32 v234, v234, v193, v194\n\t"
      "v_max3_u32 v234, v234, v195, v196\n\t"
      "v_max3_u32 v234, v234, v197, v198\n\t"
      "v_max3_u32 v234, v234, v199, v200\n\t"
      "v_max3_u32 v234, v234, v201, v202\n\t"
      "v_max3_u32 v234, v234, v203, v204\n\t"
      "v_max3_u32 v234, v234, v205, v206\n\t"
      "v_max3_u32 v234, v234, v207, v208\n\t"
      "v_max3_u32 v234, v234, v209, v210\n\t"
      "v_max_u32_e32 v234, v234, v211\n\t"
      "v_cmp_eq_u32_e32 vcc, -1, v234\n\t"
      "s_cbranch_vccnz HXL_%=\n\t"
      "HXX_%=:\n\t"
      "s_nop 1\n\t"
      "s_branch HXB_%=\n\t"
      "FQ0R_%=:\n\t"
      "s_mov_b32 s51, 0\n\t"
      "FQ0L_%=:\n\t"
      "s_add_u32 s51, s51, 1\n\t"
      "s_cmp_lt_u32 s51, 262144\n\t"
      "s_cbranch_scc0 FQ0X_%=\n\t"
      "s_waitcnt vmcnt(0)\n\t"
      "s_sleep 1\n\t"
      "global_load_dwordx4 v[128:131], v224, s[52:53] sc1\n\t"
      "global_load_dwordx4 v[132:135], v225, s[52:53] sc1\n\t"
      "global_load_dwordx4 v[136:139], v226, s[52:53] sc1\n\t"
      "global_load_dwordx4 v[140:143], v227, s[52:53] sc1\n\t"
      "global_load_dwordx4 v[144:147], v228, s[52:53] sc1\n\t"
      "global_load_dwordx4 v[148:151], v229, s[52:53] sc1\n\t"
      "global_load_dwordx4 v[152:155], v230, s[52:53] sc1\n\t"
      "s_waitcnt vmcnt(6)\n\t"
      "v_max3_u32 v234, v128, v129, v130\n\t"
      "v_max_u32_e32 v234, v234, v131\n\t"
      "v_cmp_eq_u32_e32 vcc, -1, v234\n\t"
      "s_cbranch_vccnz FQ0L_%=\n\t"
      "FQ0X_%=:\n\t"
      "s_branch FQ0B_%=\n\t"
      "FQ1R_%=:\n\t"
      "s_mov_b32 s51, 0\n\t"
      "FQ1L_%=:\n\t"
      "s_add_u32 s51, s51, 1\n\t"
      "s_cmp_lt_u32 s51, 262144\n\t"
      "s_cbranch_scc0 FQ1X_%=\n\t"
      "s_waitcnt vmcnt(0)\n\t"
      "s_sleep 1\n\t"
      "global_load_dwordx4 v[132:135], v225, s[52:53] sc1\n\t"
      "global_load_dwordx4 v[136:139], v226, s[52:53] sc1\n\t"
      "global_load_dwordx4 v[140:143], v227, s[52:53] sc1\n\t"
      "global_load_dwordx4 v[144:147], v228, s[52:53] sc1\n\t"
      "global_load_dwordx4 v[148:151], v229, s[52:53] sc1\n\t"
      "global_load_dwordx4 v[152:155], v230, s[52:53] sc1\n\t"
      "global_load_dwordx4 v[128:131], v224, s[54:55] sc1\n\t"
      "s_waitcnt vmcnt(6)\n\t"
      "v_max3_u32 v234, v132, v133, v134\n\t"
      "v_max_u32_e32 v234, v234, v135\n\t"
      "v_cmp_eq_u32_e32 vcc, -1, v234\n\t"
      "s_cbranch_vccnz FQ1L_%=\n\t"
      "FQ1X_%=:\n\t"
      "s_branch FQ1B_%=\n\t"
      "FQ2R_%=:\n\t"
      "s_mov_b32 s51, 0\n\t"
      "FQ2L_%=:\n\t"
      "s_add_u32 s51, s51, 1\n\t"
      "s_cmp_lt_u32 s51, 262144\n\t"
      "s_cbranch_scc0 FQ2X_%=\n\t"
      "s_waitcnt vmcnt(0)\n\t"
      "s_sleep 1\n\t"
      "global_load_dwordx4 v[136:139], v226, s[52:53] sc1\n\t"
      "global_load_dwordx4 v[140:143], v227, s[52:53] sc1\n\t"
      "global_load_dwordx4 v[144:147], v228, s[52:53] sc1\n\t"
      "global_load_dwordx4 v[148:151], v229, s[52:53] sc1\n\t"
      "global_load_dwordx4 v[152:155], v230, s[52:53] sc1\n\t"
      "global_load_dwordx4 v[128:131], v224, s[54:55] sc1\n\t"
      "global_load_dwordx4 v[132:135], v225, s[54:55] sc1\n\t"
      "s_waitcnt vmcnt(6)\n\t"
      "v_max3_u32 v234, v136, v137, v138\n\t"
      "v_max_u32_e32 v234, v234, v139\n\t"
      "v_cmp_eq_u32_e32 vcc, -1, v234\n\t"
      "s_cbranch_vccnz FQ2L_%=\n\t"
      "FQ2X_%=:\n\t"
      "s_branch FQ2B_%=\n\t"
      "FQ3R_%=:\n\t"
      "s_mov_b32 s51, 0\n\t"
      "FQ3L_%=:\n\t"
      "s_add_u32 s51, s51, 1\n\t"
      "s_cmp_lt_u32 s51, 262144\n\t"
      "s_cbranch_scc0 FQ3X_%=\n\t"
      "s_waitcnt vmcnt(0)\n\t"
      "s_sleep 1\n\t"
      "global_load_dwordx4 v[140:143], v227, s[52:53] sc1\n\t"
      "global_load_dwordx4 v[144:147], v228, s[52:53] sc1\n\t"
      "global_load_dwordx4 v[148:151], v229, s[52:53] sc1\n\t"
      "global_load_dwordx4 v[152:155], v230, s[52:53] sc1\n\t"
      "global_load_dwordx4 v[128:131], v224, s[54:55] sc1\n\t"
      "global_load_dwordx4 v[132:135], v225, s[54:55] sc1\n\t"
      "global_load_dwordx4 v[136:139], v226, s[54:55] sc1\n\t"
      "s_waitcnt vmcnt(6)\n\t"
      "v_max3_u32 v234, v140, v141, v142\n\t"
      "v_max_u32_e32 v234, v234, v143\n\t"
      "v_cmp_eq_u32_e32 vcc, -1, v234\n\t"
      "s_cbranch_vccnz FQ3L_%=\n\t"
      "FQ3X_%=:\n\t"
      "s_branch FQ3B_%=\n\t"
      "FQ4R_%=:\n\t"
      "s_mov_b32 s51, 0\n\t"
      "FQ4L_%=:\n\t"
      "s_add_u32 s51, s51, 1\n\t"
      "s_cmp_lt_u32 s51, 262144\n\t"
      "s_cbranch_scc0 FQ4X_%=\n\t"
      "s_waitcnt vmcnt(0)\n\t"
      "s_sleep 1\n\t"
      "global_load_dwordx4 v[144:147], v228, s[52:53] sc1\n\t"
      "global_load_dwordx4 v[148:151], v229, s[52:53] sc1\n\t"
      "global_load_dwordx4 v[152:155], v230, s[52:53] sc1\n\t"
      "global_load_dwordx4 v[128:131], v224, s[54:55] sc1\n\t"
      "global_load_dwordx4 v[132:135], v225, s[54:55] sc1\n\t"
      "global_load_dwordx4 v[136:139], v226, s[54:55] sc1\n\t"
      "global_load_dwordx4 v[140:143], v227, s[54:55] sc1\n\t"
      "s_waitcnt vmcnt(6)\n\t"
      "v_max3_u32 v234, v144, v145, v146\n\t"
      "v_max_u32_e32 v234, v234, v147\n\t"
      "v_cmp_eq_u32_e32 vcc, -1, v234\n\t"
      "s_cbranch_vccnz FQ4L_%=\n\t"
      "FQ4X_%=:\n\t"
      "s_branch FQ4B_%=\n\t"
      "FQ5R_%=:\n\t"
      "s_mov_b32 s51, 0\n\t"
      "FQ5L_%=:\n\t"
      "s_add_u32 s51, s51, 1\n\t"
      "s_cmp_lt_u32 s51, 262144\n\t"
      "s_cbranch_scc0 FQ5X_%=\n\t"
      "s_waitcnt vmcnt(0)\n\t"
      "s_sleep 1\n\t"
      "global_load_dwordx4 v[148:151], v229, s[52:53] sc1\n\t"
      "global_load_dwordx4 v[152:155], v230, s[52:53] sc1\n\t"
      "global_load_dwordx4 v[128:131], v224, s[54:55] sc1\n\t"
      "global_load_dwordx4 v[132:135], v225, s[54:55] sc1\n\t"
      "global_load_dwordx4 v[136:139], v226, s[54:55] sc1\n\t"
      "global_load_dwordx4 v[140:143], v227, s[54:55] sc1\n\t"
      "global_load_dwordx4 v[144:147], v228, s[54:55] sc1\n\t"
      "s_waitcnt vmcnt(6)\n\t"
      "v_max3_u32 v234, v148, v149, v150\n\t"
      "v_max_u32_e32 v234, v234, v151\n\t"
      "v_cmp_eq_u32_e32 vcc, -1, v234\n\t"
      "s_cbranch_vccnz FQ5L_%=\n\t"
      "FQ5X_%=:\n\t"
      "s_branch FQ5B_%=\n\t"
      "FQ6R_%=:\n\t"
      "s_mov_b32 s51, 0\n\t"
      "FQ6L_%=:\n\t"
      "s_add_u32 s51, s51, 1\n\t"
      "s_cmp_lt_u32 s51, 262144\n\t"
      "s_cbranch_scc0 FQ6X_%=\n\t"
      "s_waitcnt vmcnt(0)\n\t"
      "s_sleep 1\n\t"
      "global_load_dwordx4 v[152:155], v230, s[52:53] sc1\n\t"
      "global_load_dwordx4 v[128:131], v224, s[54:55] sc1\n\t"
      "global_load_dwordx4 v[132:135], v225, s[54:55] sc1\n\t"
      "global_load_dwordx4 v[136:139], v226, s[54:55] sc1\n\t"
      "global_load_dwordx4 v[140:143], v227, s[54:55] sc1\n\t"
      "global_load_dwordx4 v[144:147], v228, s[54:55] sc1\n\t"
      "global_load_dwordx4 v[148:151], v229, s[54:55] sc1\n\t"
      "s_waitcnt vmcnt(6)\n\t"
      "v_max3_u32 v234, v152, v153, v154\n\t"
      "v_max_u32_e32 v234, v234, v155\n\t"
      "v_cmp_eq_u32_e32 vcc, -1, v234\n\t"
      "s_cbranch_vccnz FQ6L_%=\n\t"
      "FQ6X_%=:\n\t"
      "s_branch FQ6B_%=\n\t"
      "FQ7R_%=:\n\t"
      "s_mov_b32 s51, 0\n\t"
      "FQ7L_%=:\n\t"
      "s_add_u32 s51, s51, 1\n\t"
      "s_cmp_lt_u32 s51, 262144\n\t"
      "s_cbranch_scc0 FQ7X_%=\n\t"
      "s_waitcnt vmcnt(0)\n\t"
      "s_sleep 4\n\t"
      "global_load_dwordx4 v[128:131], v224, s[54:55] sc1\n\t"
      "global_load_dwordx4 v[132:135], v225, s[54:55] sc1\n\t"
      "global_load_dwordx4 v[136:139], v226, s[54:55] sc1\n\t"
      "global_load_dwordx4 v[140:143], v227, s[54:55] sc1\n\t"
      "global_load_dwordx4 v[144:147], v228, s[54:55] sc1\n\t"
      "global_load_dwordx4 v[148:151], v229, s[54:55] sc1\n\t"
      "s_waitcnt vmcnt(5)\n\t"
      "v_max3_u32 v234, v128, v129, v130\n\t"
      "v_max_u32_e32 v234, v234, v131\n\t"
      "v_cmp_eq_u32_e32 vcc, -1, v234\n\t"
      "s_cbranch_vccnz FQ7L_%=\n\t"
      "FQ7X_%=:\n\t"
      "s_branch FQ7B_%=\n\t"
      "FQ8R_%=:\n\t"
      "s_mov_b32 s51, 0\n\t"
      "FQ8L_%=:\n\t"
      "s_add_u32 s51, s51, 1\n\t"
      "s_cmp_lt_u32 s51, 262144\n\t"
      "s_cbranch_scc0 FQ8X_%=\n\t"
      "s_waitcnt vmcnt(0)\n\t"
      "s_sleep 4\n\t"
      "global_load_dwordx4 v[132:135], v225, s[54:55] sc1\n\t"
      "global_load_dwordx4 v[136:139], v226, s[54:55] sc1\n\t"
      "global_load_dwordx4 v[140:143], v227, s[54:55] sc1\n\t"
      "global_load_dwordx4 v[144:147], v228, s[54:55] sc1\n\t"
      "global_load_dwordx4 v[148:151], v229, s[54:55] sc1\n\t"
      "global_load_dwordx4 v[152:155], v230, s[54:55] sc1\n\t"
      "s_waitcnt vmcnt(5)\n\t"
      "v_max3_u32 v234, v132, v133, v134\n\t"
      "v_max_u32_e32 v234, v234, v135\n\t"
      "v_cmp_eq_u32_e32 vcc, -1, v234\n\t"
      "s_cbranch_vccnz FQ8L_%=\n\t"
      "FQ8X_%=:\n\t"
      "s_branch FQ8B_%=\n\t"
      "FQ9R_%=:\n\t"
      "s_mov_b32 s51, 0\n\t"
      "FQ9L_%=:\n\t"
      "s_add_u32 s51, s51, 1\n\t"
      "s_cmp_lt_u32 s51, 262144\n\t"
      "s_cbranch_scc0 FQ9X_%=\n\t"
      "s_waitcnt vmcnt(0)\n\t"
      "s_sleep 4\n\t"
      "global_load_dwordx4 v[136:139], v226, s[54:55] sc1\n\t"
      "global_load_dwordx4 v[140:143], v227, s[54:55] sc1\n\t"
      "global_load_dwordx4 v[144:147], v228, s[54:55] sc1\n\t"
      "global_load_dwordx4 v[148:151], v229, s[54:55] sc1\n\t"
      "global_load_dwordx4 v[152:155], v230, s[54:55] sc1\n\t"
      "global_load_dwordx4 v[128:131], v224, s[56:57] sc1\n\t"
      "s_waitcnt vmcnt(5)\n\t"
      "v_max3_u32 v234, v136, v137, v138\n\t"
      "v_max_u32_e32 v234, v234, v139\n\t"
      "v_cmp_eq_u32_e32 vcc, -1, v234\n\t"
      "s_cbranch_vccnz FQ9L_%=\n\t"
      "FQ9X_%=:\n\t"
      "s_branch FQ9B_%=\n\t"
      "FQ10R_%=:\n\t"
      "s_mov_b32 s51, 0\n\t"
      "FQ10L_%=:\n\t"
      "s_add_u32 s51, s51, 1\n\t"
      "s_cmp_lt_u32 s51, 262144\n\t"
      "s_cbranch_scc0 FQ10X_%=\n\t"
      "s_waitcnt vmcnt(0)\n\t"
      "s_sleep 4\n\t"
      "global_load_dwordx4 v[140:143], v227, s[54:55] sc1\n\t"
      "global_load_dwordx4 v[144:147], v228, s[54:55] sc1\n\t"
      "global_load_dwordx4 v[148:151], v229, s[54:55] sc1\n\t"
      "global_load_dwordx4 v[152:155], v230, s[54:55] sc1\n\t"
      "global_load_dwordx4 v[128:131], v224, s[56:57] sc1\n\t"
      "global_load_dwordx4 v[132:135], v225, s[56:57] sc1\n\t"
      "s_waitcnt vmcnt(5)\n\t"
      "v_max3_u32 v234, v140, v141, v142\n\t"
      "v_max_u32_e32 v234, v234, v143\n\t"
      "v_cmp_eq_u32_e32 vcc, -1, v234\n\t"
      "s_cbranch_vccnz FQ10L_%=\n\t"
      "FQ10X_%=:\n\t"
      "s_branch FQ10B_%=\n\t"
      "FQ11R_%=:\n\t"
      "s_mov_b32 s51, 0\n\t"
      "FQ11L_%=:\n\t"
      "s_add_u32 s51, s51, 1\n\t"
      "s_cmp_lt_u32 s51, 262144\n\t"
      "s_cbranch_scc0 FQ11X_%=\n\t"
      "s_waitcnt vmcnt(0)\n\t"
      "s_sleep 4\n\t"
      "global_load_dwordx4 v[144:147], v228, s[54:55] sc1\n\t"
      "global_load_dwordx4 v[148:151], v229, s[54:55] sc1\n\t"
      "global_load_dwordx4 v[152:155], v230, s[54:55] sc1\n\t"
      "global_load_dwordx4 v[128:131], v224, s[56:57] sc1\n\t"
      "global_load_dwordx4 v[132:135], v225, s[56:57] sc1\n\t"
      "global_load_dwordx4 v[136:139], v226, s[56:57] sc1\n\t"
      "s_waitcnt vmcnt(5)\n\t"
      "v_max3_u32 v234, v144, v145, v146\n\t"
      "v_max_u32_e32 v234, v234, v147\n\t"
      "v_cmp_eq_u32_e32 vcc, -1, v234\n\t"
      "s_cbranch_vccnz FQ11L_%=\n\t"
      "FQ11X_%=:\n\t"
      "s_branch FQ11B_%=\n\t"
      "FQ12R_%=:\n\t"
      "s_mov_b32 s51, 0\n\t"
      "FQ12L_%=:\n\t"
      "s_add_u32 s51, s51, 1\n\t"
      "s_cmp_lt_u32 s51, 262144\n\t"
      "s_cbranch_scc0 FQ12X_%=\n\t"
      "s_waitcnt vmcnt(0)\n\t"
      "s_sleep 4\n\t"
      "global_load_dwordx4 v[148:151], v229, s[54:55] sc1\n\t"
      "global_load_dwordx4 v[152:155], v230, s[54:55] sc1\n\t"
      "global_load_dwordx4 v[128:131], v224, s[56:57] sc1\n\t"
      "global_load_dwordx4 v[132:135], v225, s[56:57] sc1\n\t"
      "global_load_dwordx4 v[136:139], v226, s[56:57] sc1\n\t"
      "global_load_dwordx4 v[140:143], v227, s[56:57] sc1\n\t"
      "s_waitcnt vmcnt(5)\n\t"
      "v_max3_u32 v234, v148, v149, v150\n\t"
      "v_max_u32_e32 v234, v234, v151\n\t"
      "v_cmp_eq_u32_e32 vcc, -1, v234\n\t"
      "s_cbranch_vccnz FQ12L_%=\n\t"
      "FQ12X_%=:\n\t"
      "s_branch FQ12B_%=\n\t"
      "FQ13R_%=:\n\t"
      "s_mov_b32 s51, 0\n\t"
      "FQ13L_%=:\n\t"
      "s_add_u32 s51, s51, 1\n\t"
      "s_cmp_lt_u32 s51, 262144\n\t"
      "s_cbranch_scc0 FQ13X_%=\n\t"
      "s_waitcnt vmcnt(0)\n\t"
      "s_sleep 4\n\t"
      "global_load_dwordx4 v[152:155], v230, s[54:55] sc1\n\t"
      "global_load_dwordx4 v[128:131], v224, s[56:57] sc1\n\t"
      "global_load_dwordx4 v[132:135], v225, s[56:57] sc1\n\t"
      "global_load_dwordx4 v[136:139], v226, s[56:57] sc1\n\t"
      "global_load_dwordx4 v[140:143], v227, s[56:57] sc1\n\t"
      "global_load_dwordx4 v[144:147], v228, s[56:57] sc1\n\t"
      "s_waitcnt vmcnt(5)\n\t"
      "v_max3_u32 v234, v152, v153, v154\n\t"
      "v_max_u32_e32 v234, v234, v155\n\t"
      "v_cmp_eq_u32_e32 vcc, -1, v234\n\t"
      "s_cbranch_vccnz FQ13L_%=\n\t"
      "FQ13X_%=:\n\t"
      "s_branch FQ13B_%=\n\t"
      "FQ14R_%=:\n\t"
      "s_mov_b32 s51, 0\n\t"
      "FQ14L_%=:\n\t"
      "s_add_u32 s51, s51, 1\n\t"
      "s_cmp_lt_u32 s51, 262144\n\t"
      "s_cbranch_scc0 FQ14X_%=\n\t"
      "s_waitcnt vmcnt(0)\n\t"
      "s_sleep 4\n\t"
      "global_load_dwordx4 v[128:131], v224, s[56:57] sc1\n\t"
      "global_load_dwordx4 v[132:135], v225, s[56:57] sc1\n\t"
      "global_load_dwordx4 v[136:139], v226, s[56:57] sc1\n\t"
      "global_load_dwordx4 v[140:143], v227, s[56:57] sc1\n\t"
      "global_load_dwordx4 v[144:147], v228, s[56:57] sc1\n\t"
      "global_load_dwordx4 v[148:151], v229, s[56:57] sc1\n\t"
      "s_waitcnt vmcnt(5)\n\t"
      "v_max3_u32 v234, v128, v129, v130\n\t"
      "v_max_u32_e32 v234, v234, v131\n\t"
      "v_cmp_eq_u32_e32 vcc, -1, v234\n\t"
      "s_cbranch_vccnz FQ14L_%=\n\t"
      "FQ14X_%=:\n\t"
      "s_branch FQ14B_%=\n\t"
      "NQ1R_%=:\n\t"
      "s_mov_b32 s51, 0\n\t"
      "NQ1L_%=:\n\t"
      "s_add_u32 s51, s51, 1\n\t"
      "s_cmp_lt_u32 s51, 262144\n\t"
      "s_cbranch_scc0 NQ1X_%=\n\t"
      "s_waitcnt vmcnt(0)\n\t"
      "s_sleep 1\n\t"
      "global_load_dwordx4 v[128:131], v224, s[54:55] sc1\n\t"
      "global_load_dwordx4 v[132:135], v225, s[54:55] sc1\n\t"
      "global_load_dwordx4 v[136:139], v226, s[54:55] sc1\n\t"
      "global_load_dwordx4 v[140:143], v227, s[54:55] sc1\n\t"
      "global_load_dwordx4 v[144:147], v228, s[54:55] sc1\n\t"
      "global_load_dwordx4 v[148:151], v229, s[54:55] sc1\n\t"
      "global_load_dwordx4 v[152:155], v230, s[54:55] sc1\n\t"
      "s_waitcnt vmcnt(0)\n\t"
      "v_max3_u32 v234, v128, v129, v130\n\t"
      "v_max3_u32 v234, v234, v131, v132\n\t"
      "v_max3_u32 v234, v234, v133, v134\n\t"
      "v_max3_u32 v234, v234, v135, v136\n\t"
      "v_max3_u32 v234, v234, v137, v138\n\t"
      "v_max3_u32 v234, v234, v139, v140\n\t"
      "v_max3_u32 v234, v234, v141, v142\n\t"
      "v_max3_u32 v234, v234, v143, v144\n\t"
      "v_max3_u32 v234, v234, v145, v146\n\t"
      "v_max3_u32 v234, v234, v147, v148\n\t"
      "v_max3_u32 v234, v234, v149, v150\n\t"
      "v_max3_u32 v234, v234, v151, v152\n\t"
      "v_max3_u32 v234, v234, v153, v154\n\t"
      "v_max_u32_e32 v234, v234, v155\n\t"
      "v_cmp_eq_u32_e32 vcc, -1, v234\n\t"
      "s_cbranch_vccnz NQ1L_%=\n\t"
      "NQ1X_%=:\n\t"
      "v_mfma_f32_16x16x32_bf16 v[200:203], v[128:131], v[0:3], 0\n\t"
      "v_mfma_f32_16x16x32_bf16 v[204:207], v[128:131], v[4:7], 0\n\t"
      "v_mfma_f32_16x16x32_bf16 v[208:211], v[128:131], v[8:11], 0\n\t"
      "v_mfma_f32_16x16x32_bf16 v[212:215], v[128:131], v[12:15], 0\n\t"
      "v_mfma_f32_16x16x32_bf16 v[200:203], v[132:135], v[16:19], v[200:203]\n\t"
      "v_mfma_f32_16x16x32_bf16 v[204:207], v[132:135], v[20:23], v[204:207]\n\t"
      "v_mfma_f32_16x16x32_bf16 v[208:211], v[132:135], v[24:27], v[208:211]\n\t"
      "v_mfma_f32_16x16x32_bf16 v[212:215], v[132:135], v[28:31], v[212:215]\n\t"
      "v_mfma_f32_16x16x32_bf16 v[200:203], v[136:139], v[32:35], v[200:203]\n\t"
      "v_mfma_f32_16x16x32_bf16 v[204:207], v[136:139], v[36:39], v[204:207]\n\t"
      "v_mfma_f32_16x16x32_bf16 v[208:211], v[136:139], v[40:43], v[208:211]\n\t"
      "v_mfma_f32_16x16x32_bf16 v[212:215], v[136:139], v[44:47], v[212:215]\n\t"
      "v_mfma_f32_16x16x32_bf16 v[200:203], v[140:143], v[48:51], v[200:203]\n\t"
      "v_mfma_f32_16x16x32_bf16 v[204:207], v[140:143], v[52:55], v[204:207]\n\t"
      "v_mfma_f32_16x16x32_bf16 v[208:211], v[140:143], v[56:59], v[208:211]\n\t"
      "v_mfma_f32_16x16x32_bf16 v[212:215], v[140:143], v[60:63], v[212:215]\n\t"
      "v_mfma_f32_16x16x32_bf16 v[200:203], v[144:147], v[64:67], v[200:203]\n\t"
      "v_mfma_f32_16x16x32_bf16 v[204:207], v[144:147], v[68:71], v[204:207]\n\t"
      "v_mfma_f32_16x16x32_bf16 v[208:211], v[144:147], v[72:75], v[208:211]\n\t"
      "v_mfma_f32_16x16x32_bf16 v[212:215], v[144:147], v[76:79], v[212:215]\n\t"
      "v_mfma_f32_16x16x32_bf16 v[200:203], v[148:151], v[80:83], v[200:203]\n\t"
      "v_mfma_f32_16x16x32_bf16 v[204:207], v[148:151], v[84:87], v[204:207]\n\t"
      "v_mfma_f32_16x16x32_bf16 v[208:211], v[148:151], v[88:91], v[208:211]\n\t"
      "v_mfma_f32_16x16x32_bf16 v[212:215], v[148:151], v[92:95], v[212:215]\n\t"
      "v_mfma_f32_16x16x32_bf16 v[200:203], v[152:155], v[96:99], v[200:203]\n\t"
      "v_mfma_f32_16x16x32_bf16 v[204:207], v[152:155], v[100:103], v[204:207]\n\t"
      "v_mfma_f32_16x16x32_bf16 v[208:211], v[152:155], v[104:107], v[208:211]\n\t"
      "v_mfma_f32_16x16x32_bf16 v[212:215], v[152:155], v[108:111], v[212:215]\n\t"
      "global_load_dwordx4 v[128:131], v224, s[56:57] sc1\n\t"
      "global_load_dwordx4 v[132:135], v225, s[56:57] sc1\n\t"
      "global_load_dwordx4 v[136:139], v226, s[56:57] sc1\n\t"
      "global_load_dwordx4 v[140:143], v227, s[56:57] sc1\n\t"
      "global_load_dwordx4 v[144:147], v228, s[56:57] sc1\n\t"
      "global_load_dwordx4 v[148:151], v229, s[56:57] sc1\n\t"
      "global_load_dwordx4 v[152:155], v230, s[56:57] sc1\n\t"
      "s_waitcnt vmcnt(0)\n\t"
      "s_nop 7\n\t"
      "s_branch NQ1B_%=\n\t"
      "FQ15R_%=:\n\t"
      "s_mov_b32 s51, 0\n\t"
      "FQ15L_%=:\n\t"
      "s_add_u32 s51, s51, 1\n\t"
      "s_cmp_lt_u32 s51, 262144\n\t"
      "s_cbranch_scc0 FQ15X_%=\n\t"
      "s_waitcnt vmcnt(0)\n\t"
      "s_sleep 4\n\t"
      "global_load_dwordx4 v[132:135], v225, s[56:57] sc1\n\t"
      "global_load_dwordx4 v[136:139], v226, s[56:57] sc1\n\t"
      "global_load_dwordx4 v[140:143], v227, s[56:57] sc1\n\t"
      "global_load_dwordx4 v[144:147], v228, s[56:57] sc1\n\t"
      "global_load_dwordx4 v[148:151], v229, s[56:57] sc1\n\t"
      "global_load_dwordx4 v[152:155], v230, s[56:57] sc1\n\t"
      "s_waitcnt vmcnt(5)\n\t"
      "v_max3_u32 v234, v132, v133, v134\n\t"
      "v_max_u32_e32 v234, v234, v135\n\t"
      "v_cmp_eq_u32_e32 vcc, -1, v234\n\t"
      "s_cbranch_vccnz FQ15L_%=\n\t"
      "FQ15X_%=:\n\t"
      "s_branch FQ15B_%=\n\t"
      "FQ16R_%=:\n\t"
      "s_mov_b32 s51, 0\n\t"
      "FQ16L_%=:\n\t"
      "s_add_u32 s51, s51, 1\n\t"
      "s_cmp_lt_u32 s51, 262144\n\t"
      "s_cbranch_scc0 FQ16X_%=\n\t"
      "s_waitcnt vmcnt(0)\n\t"
      "s_sleep 4\n\t"
      "global_load_dwordx4 v[136:139], v226, s[56:57] sc1\n\t"
      "global_load_dwordx4 v[140:143], v227, s[56:57] sc1\n\t"
      "global_load_dwordx4 v[144:147], v228, s[56:57] sc1\n\t"
      "global_load_dwordx4 v[148:151], v229, s[56:57] sc1\n\t"
      "global_load_dwordx4 v[152:155], v230, s[56:57] sc1\n\t"
      "global_load_dwordx4 v[128:131], v224, s[58:59] sc1\n\t"
      "s_waitcnt vmcnt(5)\n\t"
      "v_max3_u32 v234, v136, v137, v138\n\t"
      "v_max_u32_e32 v234, v234, v139\n\t"
      "v_cmp_eq_u32_e32 vcc, -1, v234\n\t"
      "s_cbranch_vccnz FQ16L_%=\n\t"
      "FQ16X_%=:\n\t"
      "s_branch FQ16B_%=\n\t"
      "FQ17R_%=:\n\t"
      "s_mov_b32 s51, 0\n\t"
      "FQ17L_%=:\n\t"
      "s_add_u32 s51, s51, 1\n\t"
      "s_cmp_lt_u32 s51, 262144\n\t"
      "s_cbranch_scc0 FQ17X_%=\n\t"
      "s_waitcnt vmcnt(0)\n\t"
      "s_sleep 4\n\t"
      "global_load_dwordx4 v[140:143], v227, s[56:57] sc1\n\t"
      "global_load_dwordx4 v[144:147], v228, s[56:57] sc1\n\t"
      "global_load_dwordx4 v[148:151], v229, s[56:57] sc1\n\t"
      "global_load_dwordx4 v[152:155], v230, s[56:57] sc1\n\t"
      "global_load_dwordx4 v[128:131], v224, s[58:59] sc1\n\t"
      "global_load_dwordx4 v[132:135], v225, s[58:59] sc1\n\t"
      "s_waitcnt vmcnt(5)\n\t"
      "v_max3_u32 v234, v140, v141, v142\n\t"
      "v_max_u32_e32 v234, v234, v143\n\t"
      "v_cmp_eq_u32_e32 vcc, -1, v234\n\t"
      "s_cbranch_vccnz FQ17L_%=\n\t"
      "FQ17X_%=:\n\t"
      "s_branch FQ17B_%=\n\t"
      "FQ18R_%=:\n\t"
      "s_mov_b32 s51, 0\n\t"
      "FQ18L_%=:\n\t"
      "s_add_u32 s51, s51, 1\n\t"
      "s_cmp_lt_u32 s51, 262144\n\t"
      "s_cbranch_scc0 FQ18X_%=\n\t"
      "s_waitcnt vmcnt(0)\n\t"
      "s_sleep 4\n\t"
      "global_load_dwordx4 v[144:147], v228, s[56:57] sc1\n\t"
      "global_load_dwordx4 v[148:151], v229, s[56:57] sc1\n\t"
      "global_load_dwordx4 v[152:155], v230, s[56:57] sc1\n\t"
      "global_load_dwordx4 v[128:131], v224, s[58:59] sc1\n\t"
      "global_load_dwordx4 v[132:135], v225, s[58:59] sc1\n\t"
      "global_load_dwordx4 v[136:139], v226, s[58:59] sc1\n\t"
      "s_waitcnt vmcnt(5)\n\t"
      "v_max3_u32 v234, v144, v145, v146\n\t"
      "v_max_u32_e32 v234, v234, v147\n\t"
      "v_cmp_eq_u32_e32 vcc, -1, v234\n\t"
      "s_cbranch_vccnz FQ18L_%=\n\t"
      "FQ18X_%=:\n\t"
      "s_branch FQ18B_%=\n\t"
      "FQ19R_%=:\n\t"
      "s_mov_b32 s51, 0\n\t"
      "FQ19L_%=:\n\t"
      "s_add_u32 s51, s51, 1\n\t"
      "s_cmp_lt_u32 s51, 262144\n\t"
      "s_cbranch_scc0 FQ19X_%=\n\t"
      "s_waitcnt vmcnt(0)\n\t"
      "s_sleep 4\n\t"
      "global_load_dwordx4 v[148:151], v229, s[56:57] sc1\n\t"
      "global_load_dwordx4 v[152:155], v230, s[56:57] sc1\n\t"
      "global_load_dwordx4 v[128:131], v224, s[58:59] sc1\n\t"
      "global_load_dwordx4 v[132:135], v225, s[58:59] sc1\n\t"
      "global_load_dwordx4 v[136:139], v226, s[58:59] sc1\n\t"
      "global_load_dwordx4 v[140:143], v227, s[58:59] sc1\n\t"
      "s_waitcnt vmcnt(5)\n\t"
      "v_max3_u32 v234, v148, v149, v150\n\t"
      "v_max_u32_e32 v234, v234, v151\n\t"
      "v_cmp_eq_u32_e32 vcc, -1, v234\n\t"
      "s_cbranch_vccnz FQ19L_%=\n\t"
      "FQ19X_%=:\n\t"
      "s_branch FQ19B_%=\n\t"
      "FQ20R_%=:\n\t"
      "s_mov_b32 s51, 0\n\t"
      "FQ20L_%=:\n\t"
      "s_add_u32 s51, s51, 1\n\t"
      "s_cmp_lt_u32 s51, 262144\n\t"
      "s_cbranch_scc0 FQ20X_%=\n\t"
      "s_waitcnt vmcnt(0)\n\t"
      "s_sleep 4\n\t"
      "global_load_dwordx4 v[152:155], v230, s[56:57] sc1\n\t"
      "global_load_dwordx4 v[128:131], v224, s[58:59] sc1\n\t"
      "global_load_dwordx4 v[132:135], v225, s[58:59] sc1\n\t"
      "global_load_dwordx4 v[136:139], v226, s[58:59] sc1\n\t"
      "global_load_dwordx4 v[140:143], v227, s[58:59] sc1\n\t"
      "global_load_dwordx4 v[144:147], v228, s[58:59] sc1\n\t"
      "s_waitcnt vmcnt(5)\n\t"
      "v_max3_u32 v234, v152, v153, v154\n\t"
      "v_max_u32_e32 v234, v234, v155\n\t"
      "v_cmp_eq_u32_e32 vcc, -1, v234\n\t"
      "s_cbranch_vccnz FQ20L_%=\n\t"
      "FQ20X_%=:\n\t"
      "s_branch FQ20B_%=\n\t"
      "FQ21R_%=:\n\t"
      "s_mov_b32 s51, 0\n\t"
      "FQ21L_%=:\n\t"
      "s_add_u32 s51, s51, 1\n\t"
      "s_cmp_lt_u32 s51, 262144\n\t"
      "s_cbranch_scc0 FQ21X_%=\n\t"
      "s_waitcnt vmcnt(0)\n\t"
      "s_sleep 4\n\t"
      "global_load_dwordx4 v[128:131], v224, s[58:59] sc1\n\t"
      "global_load_dwordx4 v[132:135], v225, s[58:59] sc1\n\t"
      "global_load_dwordx4 v[136:139], v226, s[58:59] sc1\n\t"
      "global_load_dwordx4 v[140:143], v227, s[58:59] sc1\n\t"
      "global_load_dwordx4 v[144:147], v228, s[58:59] sc1\n\t"
      "global_load_dwordx4 v[148:151], v229, s[58:59] sc1\n\t"
      "s_waitcnt vmcnt(5)\n\t"
      "v_max3_u32 v234, v128, v129, v130\n\t"
      "v_max_u32_e32 v234, v234, v131\n\t"
      "v_cmp_eq_u32_e32 vcc, -1, v234\n\t"
      "s_cbranch_vccnz FQ21L_%=\n\t"
      "FQ21X_%=:\n\t"
      "s_branch FQ21B_%=\n\t"
      "NQ2R_%=:\n\t"
      "s_mov_b32 s51, 0\n\t"
      "NQ2L_%=:\n\t"
      "s_add_u32 s51, s51, 1\n\t"
      "s_cmp_lt_u32 s51, 262144\n\t"
      "s_cbranch_scc0 NQ2X_%=\n\t"
      "s_waitcnt vmcnt(0)\n\t"
      "s_sleep 1\n\t"
      "global_load_dwordx4 v[128:131], v224, s[56:57] sc1\n\t"
      "global_load_dwordx4 v[132:135], v225, s[56:57] sc1\n\t"
      "global_load_dwordx4 v[136:139], v226, s[56:57] sc1\n\t"
      "global_load_dwordx4 v[140:143], v227, s[56:57] sc1\n\t"
      "global_load_dwordx4 v[144:147], v228, s[56:57] sc1\n\t"
      "global_load_dwordx4 v[148:151], v229, s[56:57] sc1\n\t"
      "global_load_dwordx4 v[152:155], v230, s[56:57] sc1\n\t"
      "s_waitcnt vmcnt(0)\n\t"
      "v_max3_u32 v234, v128, v129, v130\n\t"
      "v_max3_u32 v234, v234, v131, v132\n\t"
      "v_max3_u32 v234, v234, v133, v134\n\t"
      "v_max3_u32 v234, v234, v135, v136\n\t"
      "v_max3_u32 v234, v234, v137, v138\n\t"
      "v_max3_u32 v234, v234, v139, v140\n\t"
      "v_max3_u32 v234, v234, v141, v142\n\t"
      "v_max3_u32 v234, v234, v143, v144\n\t"
      "v_max3_u32 v234, v234, v145, v146\n\t"
      "v_max3_u32 v234, v234, v147, v148\n\t"
      "v_max3_u32 v234, v234, v149, v150\n\t"
      "v_max3_u32 v234, v234, v151, v152\n\t"
      "v_max3_u32 v234, v234, v153, v154\n\t"
      "v_max_u32_e32 v234, v234, v155\n\t"
      "v_cmp_eq_u32_e32 vcc, -1, v234\n\t"
      "s_cbranch_vccnz NQ2L_%=\n\t"
      "NQ2X_%=:\n\t"
      "v_mfma_f32_16x16x32_bf16 v[184:187], v[128:131], v[0:3], 0\n\t"
      "v_mfma_f32_16x16x32_bf16 v[188:191], v[128:131], v[4:7], 0\n\t"
      "v_mfma_f32_16x16x32_bf16 v[192:195], v[128:131], v[8:11], 0\n\t"
      "v_mfma_f32_16x16x32_bf16 v[196:199], v[128:131], v[12:15], 0\n\t"
      "v_mfma_f32_16x16x32_bf16 v[184:187], v[132:135], v[16:19], v[184:187]\n\t"
      "v_mfma_f32_16x16x32_bf16 v[188:191], v[132:135], v[20:23], v[188:191]\n\t"
      "v_mfma_f32_16x16x32_bf16 v[192:195], v[132:135], v[24:27], v[192:195]\n\t"
      "v_mfma_f32_16x16x32_bf16 v[196:199], v[132:135], v[28:31], v[196:199]\n\t"
      "v_mfma_f32_16x16x32_bf16 v[184:187], v[136:139], v[32:35], v[184:187]\n\t"
      "v_mfma_f32_16x16x32_bf16 v[188:191], v[136:139], v[36:39], v[188:191]\n\t"
      "v_mfma_f32_16x16x32_bf16 v[192:195], v[136:139], v[40:43], v[192:195]\n\t"
      "v_mfma_f32_16x16x32_bf16 v[196:199], v[136:139], v[44:47], v[196:199]\n\t"
      "v_mfma_f32_16x16x32_bf16 v[184:187], v[140:143], v[48:51], v[184:187]\n\t"
      "v_mfma_f32_16x16x32_bf16 v[188:191], v[140:143], v[52:55], v[188:191]\n\t"
      "v_mfma_f32_16x16x32_bf16 v[192:195], v[140:143], v[56:59], v[192:195]\n\t"
      "v_mfma_f32_16x16x32_bf16 v[196:199], v[140:143], v[60:63], v[196:199]\n\t"
      "v_mfma_f32_16x16x32_bf16 v[184:187], v[144:147], v[64:67], v[184:187]\n\t"
      "v_mfma_f32_16x16x32_bf16 v[188:191], v[144:147], v[68:71], v[188:191]\n\t"
      "v_mfma_f32_16x16x32_bf16 v[192:195], v[144:147], v[72:75], v[192:195]\n\t"
      "v_mfma_f32_16x16x32_bf16 v[196:199], v[144:147], v[76:79], v[196:199]\n\t"
      "v_mfma_f32_16x16x32_bf16 v[184:187], v[148:151], v[80:83], v[184:187]\n\t"
      "v_mfma_f32_16x16x32_bf16 v[188:191], v[148:151], v[84:87], v[188:191]\n\t"
      "v_mfma_f32_16x16x32_bf16 v[192:195], v[148:151], v[88:91], v[192:195]\n\t"
      "v_mfma_f32_16x16x32_bf16 v[196:199], v[148:151], v[92:95], v[196:199]\n\t"
      "v_mfma_f32_16x16x32_bf16 v[184:187], v[152:155], v[96:99], v[184:187]\n\t"
      "v_mfma_f32_16x16x32_bf16 v[188:191], v[152:155], v[100:103], v[188:191]\n\t"
      "v_mfma_f32_16x16x32_bf16 v[192:195], v[152:155], v[104:107], v[192:195]\n\t"
      "v_mfma_f32_16x16x32_bf16 v[196:199], v[152:155], v[108:111], v[196:199]\n\t"
      "global_load_dwordx4 v[128:131], v224, s[58:59] sc1\n\t"
      "global_load_dwordx4 v[132:135], v225, s[58:59] sc1\n\t"
      "global_load_dwordx4 v[136:139], v226, s[58:59] sc1\n\t"
      "global_load_dwordx4 v[140:143], v227, s[58:59] sc1\n\t"
      "global_load_dwordx4 v[144:147], v228, s[58:59] sc1\n\t"
      "global_load_dwordx4 v[148:151], v229, s[58:59] sc1\n\t"
      "global_load_dwordx4 v[152:155], v230, s[58:59] sc1\n\t"
      "s_waitcnt vmcnt(0)\n\t"
      "s_nop 7\n\t"
      "s_branch NQ2B_%=\n\t"
      "FQ22R_%=:\n\t"
      "s_mov_b32 s51, 0\n\t"
      "FQ22L_%=:\n\t"
      "s_add_u32 s51, s51, 1\n\t"
      "s_cmp_lt_u32 s51, 262144\n\t"
      "s_cbranch_scc0 FQ22X_%=\n\t"
      "s_waitcnt vmcnt(0)\n\t"
      "s_sleep 4\n\t"
      "global_load_dwordx4 v[132:135], v225, s[58:59] sc1\n\t"
      "global_load_dwordx4 v[136:139], v226, s[58:59] sc1\n\t"
      "global_load_dwordx4 v[140:143], v227, s[58:59] sc1\n\t"
      "global_load_dwordx4 v[144:147], v228, s[58:59] sc1\n\t"
      "global_load_dwordx4 v[148:151], v229, s[58:59] sc1\n\t"
      "global_load_dwordx4 v[152:155], v230, s[58:59] sc1\n\t"
      "s_waitcnt vmcnt(5)\n\t"
      "v_max3_u32 v234, v132, v133, v134\n\t"
      "v_max_u32_e32 v234, v234, v135\n\t"
      "v_cmp_eq_u32_e32 vcc, -1, v234\n\t"
      "s_cbranch_vccnz FQ22L_%=\n\t"
      "FQ22X_%=:\n\t"
      "s_branch FQ22B_%=\n\t"
      "FQ23R_%=:\n\t"
      "s_mov_b32 s51, 0\n\t"
      "FQ23L_%=:\n\t"
      "s_add_u32 s51, s51, 1\n\t"
      "s_cmp_lt_u32 s51, 262144\n\t"
      "s_cbranch_scc0 FQ23X_%=\n\t"
      "s_waitcnt vmcnt(0)\n\t"
      "s_sleep 4\n\t"
      "global_load_dwordx4 v[136:139], v226, s[58:59] sc1\n\t"
      "global_load_dwordx4 v[140:143], v227, s[58:59] sc1\n\t"
      "global_load_dwordx4 v[144:147], v228, s[58:59] sc1\n\t"
      "global_load_dwordx4 v[148:151], v229, s[58:59] sc1\n\t"
      "global_load_dwordx4 v[152:155], v230, s[58:59] sc1\n\t"
      "global_load_dwordx4 v[128:131], v224, s[60:61] sc1\n\t"
      "s_waitcnt vmcnt(5)\n\t"
      "v_max3_u32 v234, v136, v137, v138\n\t"
      "v_max_u32_e32 v234, v234, v139\n\t"
      "v_cmp_eq_u32_e32 vcc, -1, v234\n\t"
      "s_cbranch_vccnz FQ23L_%=\n\t"
      "FQ23X_%=:\n\t"
      "s_branch FQ23B_%=\n\t"
      "FQ24R_%=:\n\t"
      "s_mov_b32 s51, 0\n\t"
      "FQ24L_%=:\n\t"
      "s_add_u32 s51, s51, 1\n\t"
      "s_cmp_lt_u32 s51, 262144\n\t"
      "s_cbranch_scc0 FQ24X_%=\n\t"
      "s_waitcnt vmcnt(0)\n\t"
      "s_sleep 4\n\t"
      "global_load_dwordx4 v[140:143], v227, s[58:59] sc1\n\t"
      "global_load_dwordx4 v[144:147], v228, s[58:59] sc1\n\t"
      "global_load_dwordx4 v[148:151], v229, s[58:59] sc1\n\t"
      "global_load_dwordx4 v[152:155], v230, s[58:59] sc1\n\t"
      "global_load_dwordx4 v[128:131], v224, s[60:61] sc1\n\t"
      "global_load_dwordx4 v[132:135], v225, s[60:61] sc1\n\t"
      "s_waitcnt vmcnt(5)\n\t"
      "v_max3_u32 v234, v140, v141, v142\n\t"
      "v_max_u32_e32 v234, v234, v143\n\t"
      "v_cmp_eq_u32_e32 vcc, -1, v234\n\t"
      "s_cbranch_vccnz FQ24L_%=\n\t"
      "FQ24X_%=:\n\t"
      "s_branch FQ24B_%=\n\t"
      "FQ25R_%=:\n\t"
      "s_mov_b32 s51, 0\n\t"
      "FQ25L_%=:\n\t"
      "s_add_u32 s51, s51, 1\n\t"
      "s_cmp_lt_u32 s51, 262144\n\t"
      "s_cbranch_scc0 FQ25X_%=\n\t"
      "s_waitcnt vmcnt(0)\n\t"
      "s_sleep 4\n\t"
      "global_load_dwordx4 v[144:147], v228, s[58:59] sc1\n\t"
      "global_load_dwordx4 v[148:151], v229, s[58:59] sc1\n\t"
      "global_load_dwordx4 v[152:155], v230, s[58:59] sc1\n\t"
      "global_load_dwordx4 v[128:131], v224, s[60:61] sc1\n\t"
      "global_load_dwordx4 v[132:135], v225, s[60:61] sc1\n\t"
      "global_load_dwordx4 v[136:139], v226, s[60:61] sc1\n\t"
      "s_waitcnt vmcnt(5)\n\t"
      "v_max3_u32 v234, v144, v145, v146\n\t"
      "v_max_u32_e32 v234, v234, v147\n\t"
      "v_cmp_eq_u32_e32 vcc, -1, v234\n\t"
      "s_cbranch_vccnz FQ25L_%=\n\t"
      "FQ25X_%=:\n\t"
      "s_branch FQ25B_%=\n\t"
      "FQ26R_%=:\n\t"
      "s_mov_b32 s51, 0\n\t"
      "FQ26L_%=:\n\t"
      "s_add_u32 s51, s51, 1\n\t"
      "s_cmp_lt_u32 s51, 262144\n\t"
      "s_cbranch_scc0 FQ26X_%=\n\t"
      "s_waitcnt vmcnt(0)\n\t"
      "s_sleep 4\n\t"
      "global_load_dwordx4 v[148:151], v229, s[58:59] sc1\n\t"
      "global_load_dwordx4 v[152:155], v230, s[58:59] sc1\n\t"
      "global_load_dwordx4 v[128:131], v224, s[60:61] sc1\n\t"
      "global_load_dwordx4 v[132:135], v225, s[60:61] sc1\n\t"
      "global_load_dwordx4 v[136:139], v226, s[60:61] sc1\n\t"
      "global_load_dwordx4 v[140:143], v227, s[60:61] sc1\n\t"
      "s_waitcnt vmcnt(5)\n\t"
      "v_max3_u32 v234, v148, v149, v150\n\t"
      "v_max_u32_e32 v234, v234, v151\n\t"
      "v_cmp_eq_u32_e32 vcc, -1, v234\n\t"
      "s_cbranch_vccnz FQ26L_%=\n\t"
      "FQ26X_%=:\n\t"
      "s_branch FQ26B_%=\n\t"
      "FQ27R_%=:\n\t"
      "s_mov_b32 s51, 0\n\t"
      "FQ27L_%=:\n\t"
      "s_add_u32 s51, s51, 1\n\t"
      "s_cmp_lt_u32 s51, 262144\n\t"
      "s_cbranch_scc0 FQ27X_%=\n\t"
      "s_waitcnt vmcnt(0)\n\t"
      "s_sleep 4\n\t"
      "global_load_dwordx4 v[152:155], v230, s[58:59] sc1\n\t"
      "global_load_dwordx4 v[128:131], v224, s[60:61] sc1\n\t"
      "global_load_dwordx4 v[132:135], v225, s[60:61] sc1\n\t"
      "global_load_dwordx4 v[136:139], v226, s[60:61] sc1\n\t"
      "global_load_dwordx4 v[140:143], v227, s[60:61] sc1\n\t"
      "global_load_dwordx4 v[144:147], v228, s[60:61] sc1\n\t"
      "s_waitcnt vmcnt(5)\n\t"
      "v_max3_u32 v234, v152, v153, v154\n\t"
      "v_max_u32_e32 v234, v234, v155\n\t"
      "v_cmp_eq_u32_e32 vcc, -1, v234\n\t"
      "s_cbranch_vccnz FQ27L_%=\n\t"
      "FQ27X_%=:\n\t"
      "s_branch FQ27B_%=\n\t"
      "FQ28R_%=:\n\t"
      "s_mov_b32 s51, 0\n\t"
      "FQ28L_%=:\n\t"
      "s_add_u32 s51, s51, 1\n\t"
      "s_cmp_lt_u32 s51, 262144\n\t"
      "s_cbranch_scc0 FQ28X_%=\n\t"
      "s_waitcnt vmcnt(0)\n\t"
      "s_sleep 4\n\t"
      "global_load_dwordx4 v[128:131], v224, s[60:61] sc1\n\t"
      "global_load_dwordx4 v[132:135], v225, s[60:61] sc1\n\t"
      "global_load_dwordx4 v[136:139], v226, s[60:61] sc1\n\t"
      "global_load_dwordx4 v[140:143], v227, s[60:61] sc1\n\t"
      "global_load_dwordx4 v[144:147], v228, s[60:61] sc1\n\t"
      "global_load_dwordx4 v[148:151], v229, s[60:61] sc1\n\t"
      "s_waitcnt vmcnt(5)\n\t"
      "v_max3_u32 v234, v128, v129, v130\n\t"
      "v_max_u32_e32 v234, v234, v131\n\t"
      "v_cmp_eq_u32_e32 vcc, -1, v234\n\t"
      "s_cbranch_vccnz FQ28L_%=\n\t"
      "FQ28X_%=:\n\t"
      "s_branch FQ28B_%=\n\t"
      "NQ3R_%=:\n\t"
      "s_mov_b32 s51, 0\n\t"
      "NQ3L_%=:\n\t"
      "s_add_u32 s51, s51, 1\n\t"
      "s_cmp_lt_u32 s51, 262144\n\t"
      "s_cbranch_scc0 NQ3X_%=\n\t"
      "s_waitcnt vmcnt(0)\n\t"
      "s_sleep 1\n\t"
      "global_load_dwordx4 v[128:131], v224, s[58:59] sc1\n\t"
      "global_load_dwordx4 v[132:135], v225, s[58:59] sc1\n\t"
      "global_load_dwordx4 v[136:139], v226, s[58:59] sc1\n\t"
      "global_load_dwordx4 v[140:143], v227, s[58:59] sc1\n\t"
      "global_load_dwordx4 v[144:147], v228, s[58:59] sc1\n\t"
      "global_load_dwordx4 v[148:151], v229, s[58:59] sc1\n\t"
      "global_load_dwordx4 v[152:155], v230, s[58:59] sc1\n\t"
      "s_waitcnt vmcnt(0)\n\t"
      "v_max3_u32 v234, v128, v129, v130\n\t"
      "v_max3_u32 v234, v234, v131, v132\n\t"
      "v_max3_u32 v234, v234, v133, v134\n\t"
      "v_max3_u32 v234, v234, v135, v136\n\t"
      "v_max3_u32 v234, v234, v137, v138\n\t"
      "v_max3_u32 v234, v234, v139, v140\n\t"
      "v_max3_u32 v234, v234, v141, v142\n\t"
      "v_max3_u32 v234, v234, v143, v144\n\t"
      "v_max3_u32 v234, v234, v145, v146\n\t"
      "v_max3_u32 v234, v234, v147, v148\n\t"
      "v_max3_u32 v234, v234, v149, v150\n\t"
      "v_max3_u32 v234, v234, v151, v152\n\t"
      "v_max3_u32 v234, v234, v153, v154\n\t"
      "v_max_u32_e32 v234, v234, v155\n\t"
      "v_cmp_eq_u32_e32 vcc, -1, v234\n\t"
      "s_cbranch_vccnz NQ3L_%=\n\t"
      "NQ3X_%=:\n\t"
      "v_mfma_f32_16x16x32_bf16 v[200:203], v[128:131], v[0:3], 0\n\t"
      "v_mfma_f32_16x16x32_bf16 v[204:207], v[128:131], v[4:7], 0\n\t"
      "v_mfma_f32_16x16x32_bf16 v[208:211], v[128:131], v[8:11], 0\n\t"
      "v_mfma_f32_16x16x32_bf16 v[212:215], v[128:131], v[12:15], 0\n\t"
      "v_mfma_f32_16x16x32_bf16 v[200:203], v[132:135], v[16:19], v[200:203]\n\t"
      "v_mfma_f32_16x16x32_bf16 v[204:207], v[132:135], v[20:23], v[204:207]\n\t"
      "v_mfma_f32_16x16x32_bf16 v[208:211], v[132:135], v[24:27], v[208:211]\n\t"
      "v_mfma_f32_16x16x32_bf16 v[212:215], v[132:135], v[28:31], v[212:215]\n\t"
      "v_mfma_f32_16x16x32_bf16 v[200:203], v[136:139], v[32:35], v[200:203]\n\t"
      "v_mfma_f32_16x16x32_bf16 v[204:207], v[136:139], v[36:39], v[204:207]\n\t"
      "v_mfma_f32_16x16x32_bf16 v[208:211], v[136:139], v[40:43], v[208:211]\n\t"
      "v_mfma_f32_16x16x32_bf16 v[212:215], v[136:139], v[44:47], v[212:215]\n\t"
      "v_mfma_f32_16x16x32_bf16 v[200:203], v[140:143], v[48:51], v[200:203]\n\t"
      "v_mfma_f32_16x16x32_bf16 v[204:207], v[140:143], v[52:55], v[204:207]\n\t"
      "v_mfma_f32_16x16x32_bf16 v[208:211], v[140:143], v[56:59], v[208:211]\n\t"
      "v_mfma_f32_16x16x32_bf16 v[212:215], v[140:143], v[60:63], v[212:215]\n\t"
      "v_mfma_f32_16x16x32_bf16 v[200:203], v[144:147], v[64:67], v[200:203]\n\t"
      "v_mfma_f32_16x16x32_bf16 v[204:207], v[144:147], v[68:71], v[204:207]\n\t"
      "v_mfma_f32_16x16x32_bf16 v[208:211], v[144:147], v[72:75], v[208:211]\n\t"
      "v_mfma_f32_16x16x32_bf16 v[212:215], v[144:147], v[76:79], v[212:215]\n\t"
      "v_mfma_f32_16x16x32_bf16 v[200:203], v[148:151], v[80:83], v[200:203]\n\t"
      "v_mfma_f32_16x16x32_bf16 v[204:207], v[148:151], v[84:87], v[204:207]\n\t"
      "v_mfma_f32_16x16x32_bf16 v[208:211], v[148:151], v[88:91], v[208:211]\n\t"
      "v_mfma_f32_16x16x32_bf16 v[212:215], v[148:151], v[92:95], v[212:215]\n\t"
      "v_mfma_f32_16x16x32_bf16 v[200:203], v[152:155], v[96:99], v[200:203]\n\t"
      "v_mfma_f32_16x16x32_bf16 v[204:207], v[152:155], v[100:103], v[204:207]\n\t"
      "v_mfma_f32_16x16x32_bf16 v[208:211], v[152:155], v[104:107], v[208:211]\n\t"
      "v_mfma_f32_16x16x32_bf16 v[212:215], v[152:155], v[108:111], v[212:215]\n\t"
      "global_load_dwordx4 v[128:131], v224, s[60:61] sc1\n\t"
      "global_load_dwordx4 v[132:135], v225, s[60:61] sc1\n\t"
      "global_load_dwordx4 v[136:139], v226, s[60:61] sc1\n\t"
      "global_load_dwordx4 v[140:143], v227, s[60:61] sc1\n\t"
      "global_load_dwordx4 v[144:147], v228, s[60:61] sc1\n\t"
      "global_load_dwordx4 v[148:151], v229, s[60:61] sc1\n\t"
      "global_load_dwordx4 v[152:155], v230, s[60:61] sc1\n\t"
      "s_waitcnt vmcnt(0)\n\t"
      "s_nop 7\n\t"
      "s_branch NQ3B_%=\n\t"
      "FQ29R_%=:\n\t"
      "s_mov_b32 s51, 0\n\t"
      "FQ29L_%=:\n\t"
      "s_add_u32 s51, s51, 1\n\t"
      "s_cmp_lt_u32 s51, 262144\n\t"
      "s_cbranch_scc0 FQ29X_%=\n\t"
      "s_waitcnt vmcnt(0)\n\t"
      "s_sleep 4\n\t"
      "global_load_dwordx4 v[132:135], v225, s[60:61] sc1\n\t"
      "global_load_dwordx4 v[136:139], v226, s[60:61] sc1\n\t"
      "global_load_dwordx4 v[140:143], v227, s[60:61] sc1\n\t"
      "global_load_dwordx4 v[144:147], v228, s[60:61] sc1\n\t"
      "global_load_dwordx4 v[148:151], v229, s[60:61] sc1\n\t"
      "global_load_dwordx4 v[152:155], v230, s[60:61] sc1\n\t"
      "s_waitcnt vmcnt(5)\n\t"
      "v_max3_u32 v234, v132, v133, v134\n\t"
      "v_max_u32_e32 v234, v234, v135\n\t"
      "v_cmp_eq_u32_e32 vcc, -1, v234\n\t"
      "s_cbranch_vccnz FQ29L_%=\n\t"
      "FQ29X_%=:\n\t"
      "s_branch FQ29B_%=\n\t"
      "FQ30R_%=:\n\t"
      "s_mov_b32 s51, 0\n\t"
      "FQ30L_%=:\n\t"
      "s_add_u32 s51, s51, 1\n\t"
      "s_cmp_lt_u32 s51, 262144\n\t"
      "s_cbranch_scc0 FQ30X_%=\n\t"
      "s_waitcnt vmcnt(0)\n\t"
      "s_sleep 4\n\t"
      "global_load_dwordx4 v[136:139], v226, s[60:61] sc1\n\t"
      "global_load_dwordx4 v[140:143], v227, s[60:61] sc1\n\t"
      "global_load_dwordx4 v[144:147], v228, s[60:61] sc1\n\t"
      "global_load_dwordx4 v[148:151], v229, s[60:61] sc1\n\t"
      "global_load_dwordx4 v[152:155], v230, s[60:61] sc1\n\t"
      "global_load_dwordx4 v[128:131], v224, s[62:63] sc1\n\t"
      "s_waitcnt vmcnt(5)\n\t"
      "v_max3_u32 v234, v136, v137, v138\n\t"
      "v_max_u32_e32 v234, v234, v139\n\t"
      "v_cmp_eq_u32_e32 vcc, -1, v234\n\t"
      "s_cbranch_vccnz FQ30L_%=\n\t"
      "FQ30X_%=:\n\t"
      "s_branch FQ30B_%=\n\t"
      "FQ31R_%=:\n\t"
      "s_mov_b32 s51, 0\n\t"
      "FQ31L_%=:\n\t"
      "s_add_u32 s51, s51, 1\n\t"
      "s_cmp_lt_u32 s51, 262144\n\t"
      "s_cbranch_scc0 FQ31X_%=\n\t"
      "s_waitcnt vmcnt(0)\n\t"
      "s_sleep 4\n\t"
      "global_load_dwordx4 v[140:143], v227, s[60:61] sc1\n\t"
      "global_load_dwordx4 v[144:147], v228, s[60:61] sc1\n\t"
      "global_load_dwordx4 v[148:151], v229, s[60:61] sc1\n\t"
      "global_load_dwordx4 v[152:155], v230, s[60:61] sc1\n\t"
      "global_load_dwordx4 v[128:131], v224, s[62:63] sc1\n\t"
      "global_load_dwordx4 v[132:135], v225, s[62:63] sc1\n\t"
      "s_waitcnt vmcnt(5)\n\t"
      "v_max3_u32 v234, v140, v141, v142\n\t"
      "v_max_u32_e32 v234, v234, v143\n\t"
      "v_cmp_eq_u32_e32 vcc, -1, v234\n\t"
      "s_cbranch_vccnz FQ31L_%=\n\t"
      "FQ31X_%=:\n\t"
      "s_branch FQ31B_%=\n\t"
      "FQ32R_%=:\n\t"
      "s_mov_b32 s51, 0\n\t"
      "FQ32L_%=:\n\t"
      "s_add_u32 s51, s51, 1\n\t"
      "s_cmp_lt_u32 s51, 262144\n\t"
      "s_cbranch_scc0 FQ32X_%=\n\t"
      "s_waitcnt vmcnt(0)\n\t"
      "s_sleep 4\n\t"
      "global_load_dwordx4 v[144:147], v228, s[60:61] sc1\n\t"
      "global_load_dwordx4 v[148:151], v229, s[60:61] sc1\n\t"
      "global_load_dwordx4 v[152:155], v230, s[60:61] sc1\n\t"
      "global_load_dwordx4 v[128:131], v224, s[62:63] sc1\n\t"
      "global_load_dwordx4 v[132:135], v225, s[62:63] sc1\n\t"
      "global_load_dwordx4 v[136:139], v226, s[62:63] sc1\n\t"
      "s_waitcnt vmcnt(5)\n\t"
      "v_max3_u32 v234, v144, v145, v146\n\t"
      "v_max_u32_e32 v234, v234, v147\n\t"
      "v_cmp_eq_u32_e32 vcc, -1, v234\n\t"
      "s_cbranch_vccnz FQ32L_%=\n\t"
      "FQ32X_%=:\n\t"
      "s_branch FQ32B_%=\n\t"
      "FQ33R_%=:\n\t"
      "s_mov_b32 s51, 0\n\t"
      "FQ33L_%=:\n\t"
      "s_add_u32 s51, s51, 1\n\t"
      "s_cmp_lt_u32 s51, 262144\n\t"
      "s_cbranch_scc0 FQ33X_%=\n\t"
      "s_waitcnt vmcnt(0)\n\t"
      "s_sleep 4\n\t"
      "global_load_dwordx4 v[148:151], v229, s[60:61] sc1\n\t"
      "global_load_dwordx4 v[152:155], v230, s[60:61] sc1\n\t"
      "global_load_dwordx4 v[128:131], v224, s[62:63] sc1\n\t"
      "global_load_dwordx4 v[132:135], v225, s[62:63] sc1\n\t"
      "global_load_dwordx4 v[136:139], v226, s[62:63] sc1\n\t"
      "global_load_dwordx4 v[140:143], v227, s[62:63] sc1\n\t"
      "s_waitcnt vmcnt(5)\n\t"
      "v_max3_u32 v234, v148, v149, v150\n\t"
      "v_max_u32_e32 v234, v234, v151\n\t"
      "v_cmp_eq_u32_e32 vcc, -1, v234\n\t"
      "s_cbranch_vccnz FQ33L_%=\n\t"
      "FQ33X_%=:\n\t"
      "s_branch FQ33B_%=\n\t"
      "FQ34R_%=:\n\t"
      "s_mov_b32 s51, 0\n\t"
      "FQ34L_%=:\n\t"
      "s_add_u32 s51, s51, 1\n\t"
      "s_cmp_lt_u32 s51, 262144\n\t"
      "s_cbranch_scc0 FQ34X_%=\n\t"
      "s_waitcnt vmcnt(0)\n\t"
      "s_sleep 4\n\t"
      "global_load_dwordx4 v[152:155], v230, s[60:61] sc1\n\t"
      "global_load_dwordx4 v[128:131], v224, s[62:63] sc1\n\t"
      "global_load_dwordx4 v[132:135], v225, s[62:63] sc1\n\t"
      "global_load_dwordx4 v[136:139], v226, s[62:63] sc1\n\t"
      "global_load_dwordx4 v[140:143], v227, s[62:63] sc1\n\t"
      "global_load_dwordx4 v[144:147], v228, s[62:63] sc1\n\t"
      "s_waitcnt vmcnt(5)\n\t"
      "v_max3_u32 v234, v152, v153, v154\n\t"
      "v_max_u32_e32 v234, v234, v155\n\t"
      "v_cmp_eq_u32_e32 vcc, -1, v234\n\t"
      "s_cbranch_vccnz FQ34L_%=\n\t"
      "FQ34X_%=:\n\t"
      "s_branch FQ34B_%=\n\t"
      "FQ35R_%=:\n\t"
      "s_mov_b32 s51, 0\n\t"
      "FQ35L_%=:\n\t"
      "s_add_u32 s51, s51, 1\n\t"
      "s_cmp_lt_u32 s51, 262144\n\t"
      "s_cbranch_scc0 FQ35X_%=\n\t"
      "s_waitcnt vmcnt(0)\n\t"
      "s_sleep 4\n\t"
      "global_load_dwordx4 v[128:131], v224, s[62:63] sc1\n\t"
      "global_load_dwordx4 v[132:135], v225, s[62:63] sc1\n\t"
      "global_load_dwordx4 v[136:139], v226, s[62:63] sc1\n\t"
      "global_load_dwordx4 v[140:143], v227, s[62:63] sc1\n\t"
      "global_load_dwordx4 v[144:147], v228, s[62:63] sc1\n\t"
      "global_load_dwordx4 v[148:151], v229, s[62:63] sc1\n\t"
      "s_waitcnt vmcnt(5)\n\t"
      "v_max3_u32 v234, v128, v129, v130\n\t"
      "v_max_u32_e32 v234, v234, v131\n\t"
      "v_cmp_eq_u32_e32 vcc, -1, v234\n\t"
      "s_cbranch_vccnz FQ35L_%=\n\t"
      "FQ35X_%=:\n\t"
      "s_branch FQ35B_%=\n\t"
      "NQ4R_%=:\n\t"
      "s_mov_b32 s51, 0\n\t"
      "NQ4L_%=:\n\t"
      "s_add_u32 s51, s51, 1\n\t"
      "s_cmp_lt_u32 s51, 262144\n\t"
      "s_cbranch_scc0 NQ4X_%=\n\t"
      "s_waitcnt vmcnt(0)\n\t"
      "s_sleep 1\n\t"
      "global_load_dwordx4 v[128:131], v224, s[60:61] sc1\n\t"
      "global_load_dwordx4 v[132:135], v225, s[60:61] sc1\n\t"
      "global_load_dwordx4 v[136:139], v226, s[60:61] sc1\n\t"
      "global_load_dwordx4 v[140:143], v227, s[60:61] sc1\n\t"
      "global_load_dwordx4 v[144:147], v228, s[60:61] sc1\n\t"
      "global_load_dwordx4 v[148:151], v229, s[60:61] sc1\n\t"
      "global_load_dwordx4 v[152:155], v230, s[60:61] sc1\n\t"
      "s_waitcnt vmcnt(0)\n\t"
      "v_max3_u32 v234, v128, v129, v130\n\t"
      "v_max3_u32 v234, v234, v131, v132\n\t"
      "v_max3_u32 v234, v234, v133, v134\n\t"
      "v_max3_u32 v234, v234, v135, v136\n\t"
      "v_max3_u32 v234, v234, v137, v138\n\t"
      "v_max3_u32 v234, v234, v139, v140\n\t"
      "v_max3_u32 v234, v234, v141, v142\n\t"
      "v_max3_u32 v234, v234, v143, v144\n\t"
      "v_max3_u32 v234, v234, v145, v146\n\t"
      "v_max3_u32 v234, v234, v147, v148\n\t"
      "v_max3_u32 v234, v234, v149, v150\n\t"
      "v_max3_u32 v234, v234, v151, v152\n\t"
      "v_max3_u32 v234, v234, v153, v154\n\t"
      "v_max_u32_e32 v234, v234, v155\n\t"
      "v_cmp_eq_u32_e32 vcc, -1, v234\n\t"
      "s_cbranch_vccnz NQ4L_%=\n\t"
      "NQ4X_%=:\n\t"
      "v_mfma_f32_16x16x32_bf16 v[184:187], v[128:131], v[0:3], 0\n\t"
      "v_mfma_f32_16x16x32_bf16 v[188:191], v[128:131], v[4:7], 0\n\t"
      "v_mfma_f32_16x16x32_bf16 v[192:195], v[128:131], v[8:11], 0\n\t"
      "v_mfma_f32_16x16x32_bf16 v[196:199], v[128:131], v[12:15], 0\n\t"
      "v_mfma_f32_16x16x32_bf16 v[184:187], v[132:135], v[16:19], v[184:187]\n\t"
      "v_mfma_f32_16x16x32_bf16 v[188:191], v[132:135], v[20:23], v[188:191]\n\t"
      "v_mfma_f32_16x16x32_bf16 v[192:195], v[132:135], v[24:27], v[192:195]\n\t"
      "v_mfma_f32_16x16x32_bf16 v[196:199], v[132:135], v[28:31], v[196:199]\n\t"
      "v_mfma_f32_16x16x32_bf16 v[184:187], v[136:139], v[32:35], v[184:187]\n\t"
      "v_mfma_f32_16x16x32_bf16 v[188:191], v[136:139], v[36:39], v[188:191]\n\t"
      "v_mfma_f32_16x16x32_bf16 v[192:195], v[136:139], v[40:43], v[192:195]\n\t"
      "v_mfma_f32_16x16x32_bf16 v[196:199], v[136:139], v[44:47], v[196:199]\n\t"
      "v_mfma_f32_16x16x32_bf16 v[184:187], v[140:143], v[48:51], v[184:187]\n\t"
      "v_mfma_f32_16x16x32_bf16 v[188:191], v[140:143], v[52:55], v[188:191]\n\t"
      "v_mfma_f32_16x16x32_bf16 v[192:195], v[140:143], v[56:59], v[192:195]\n\t"
      "v_mfma_f32_16x16x32_bf16 v[196:199], v[140:143], v[60:63], v[196:199]\n\t"
      "v_mfma_f32_16x16x32_bf16 v[184:187], v[144:147], v[64:67], v[184:187]\n\t"
      "v_mfma_f32_16x16x32_bf16 v[188:191], v[144:147], v[68:71], v[188:191]\n\t"
      "v_mfma_f32_16x16x32_bf16 v[192:195], v[144:147], v[72:75], v[192:195]\n\t"
      "v_mfma_f32_16x16x32_bf16 v[196:199], v[144:147], v[76:79], v[196:199]\n\t"
      "v_mfma_f32_16x16x32_bf16 v[184:187], v[148:151], v[80:83], v[184:187]\n\t"
      "v_mfma_f32_16x16x32_bf16 v[188:191], v[148:151], v[84:87], v[188:191]\n\t"
      "v_mfma_f32_16x16x32_bf16 v[192:195], v[148:151], v[88:91], v[192:195]\n\t"
      "v_mfma_f32_16x16x32_bf16 v[196:199], v[148:151], v[92:95], v[196:199]\n\t"
      "v_mfma_f32_16x16x32_bf16 v[184:187], v[152:155], v[96:99], v[184:187]\n\t"
      "v_mfma_f32_16x16x32_bf16 v[188:191], v[152:155], v[100:103], v[188:191]\n\t"
      "v_mfma_f32_16x16x32_bf16 v[192:195], v[152:155], v[104:107], v[192:195]\n\t"
      "v_mfma_f32_16x16x32_bf16 v[196:199], v[152:155], v[108:111], v[196:199]\n\t"
      "global_load_dwordx4 v[128:131], v224, s[62:63] sc1\n\t"
      "global_load_dwordx4 v[132:135], v225, s[62:63] sc1\n\t"
      "global_load_dwordx4 v[136:139], v226, s[62:63] sc1\n\t"
      "global_load_dwordx4 v[140:143], v227, s[62:63] sc1\n\t"
      "global_load_dwordx4 v[144:147], v228, s[62:63] sc1\n\t"
      "global_load_dwordx4 v[148:151], v229, s[62:63] sc1\n\t"
      "global_load_dwordx4 v[152:155], v230, s[62:63] sc1\n\t"
      "s_waitcnt vmcnt(0)\n\t"
      "s_nop 7\n\t"
      "s_branch NQ4B_%=\n\t"
      "FQ36R_%=:\n\t"
      "s_mov_b32 s51, 0\n\t"
      "FQ36L_%=:\n\t"
      "s_add_u32 s51, s51, 1\n\t"
      "s_cmp_lt_u32 s51, 262144\n\t"
      "s_cbranch_scc0 FQ36X_%=\n\t"
      "s_waitcnt vmcnt(0)\n\t"
      "s_sleep 4\n\t"
      "global_load_dwordx4 v[132:135], v225, s[62:63] sc1\n\t"
      "global_load_dwordx4 v[136:139], v226, s[62:63] sc1\n\t"
      "global_load_dwordx4 v[140:143], v227, s[62:63] sc1\n\t"
      "global_load_dwordx4 v[144:147], v228, s[62:63] sc1\n\t"
      "global_load_dwordx4 v[148:151], v229, s[62:63] sc1\n\t"
      "global_load_dwordx4 v[152:155], v230, s[62:63] sc1\n\t"
      "s_waitcnt vmcnt(5)\n\t"
      "v_max3_u32 v234, v132, v133, v134\n\t"
      "v_max_u32_e32 v234, v234, v135\n\t"
      "v_cmp_eq_u32_e32 vcc, -1, v234\n\t"
      "s_cbranch_vccnz FQ36L_%=\n\t"
      "FQ36X_%=:\n\t"
      "s_branch FQ36B_%=\n\t"
      "FQ37R_%=:\n\t"
      "s_mov_b32 s51, 0\n\t"
      "FQ37L_%=:\n\t"
      "s_add_u32 s51, s51, 1\n\t"
      "s_cmp_lt_u32 s51, 262144\n\t"
      "s_cbranch_scc0 FQ37X_%=\n\t"
      "s_waitcnt vmcnt(0)\n\t"
      "s_sleep 4\n\t"
      "global_load_dwordx4 v[136:139], v226, s[62:63] sc1\n\t"
      "global_load_dwordx4 v[140:143], v227, s[62:63] sc1\n\t"
      "global_load_dwordx4 v[144:147], v228, s[62:63] sc1\n\t"
      "global_load_dwordx4 v[148:151], v229, s[62:63] sc1\n\t"
      "global_load_dwordx4 v[152:155], v230, s[62:63] sc1\n\t"
      "global_load_dwordx4 v[128:131], v224, s[64:65] sc1\n\t"
      "s_waitcnt vmcnt(5)\n\t"
      "v_max3_u32 v234, v136, v137, v138\n\t"
      "v_max_u32_e32 v234, v234, v139\n\t"
      "v_cmp_eq_u32_e32 vcc, -1, v234\n\t"
      "s_cbranch_vccnz FQ37L_%=\n\t"
      "FQ37X_%=:\n\t"
      "s_branch FQ37B_%=\n\t"
      "FQ38R_%=:\n\t"
      "s_mov_b32 s51, 0\n\t"
      "FQ38L_%=:\n\t"
      "s_add_u32 s51, s51, 1\n\t"
      "s_cmp_lt_u32 s51, 262144\n\t"
      "s_cbranch_scc0 FQ38X_%=\n\t"
      "s_waitcnt vmcnt(0)\n\t"
      "s_sleep 4\n\t"
      "global_load_dwordx4 v[140:143], v227, s[62:63] sc1\n\t"
      "global_load_dwordx4 v[144:147], v228, s[62:63] sc1\n\t"
      "global_load_dwordx4 v[148:151], v229, s[62:63] sc1\n\t"
      "global_load_dwordx4 v[152:155], v230, s[62:63] sc1\n\t"
      "global_load_dwordx4 v[128:131], v224, s[64:65] sc1\n\t"
      "global_load_dwordx4 v[132:135], v225, s[64:65] sc1\n\t"
      "s_waitcnt vmcnt(5)\n\t"
      "v_max3_u32 v234, v140, v141, v142\n\t"
      "v_max_u32_e32 v234, v234, v143\n\t"
      "v_cmp_eq_u32_e32 vcc, -1, v234\n\t"
      "s_cbranch_vccnz FQ38L_%=\n\t"
      "FQ38X_%=:\n\t"
      "s_branch FQ38B_%=\n\t"
      "FQ39R_%=:\n\t"
      "s_mov_b32 s51, 0\n\t"
      "FQ39L_%=:\n\t"
      "s_add_u32 s51, s51, 1\n\t"
      "s_cmp_lt_u32 s51, 262144\n\t"
      "s_cbranch_scc0 FQ39X_%=\n\t"
      "s_waitcnt vmcnt(0)\n\t"
      "s_sleep 4\n\t"
      "global_load_dwordx4 v[144:147], v228, s[62:63] sc1\n\t"
      "global_load_dwordx4 v[148:151], v229, s[62:63] sc1\n\t"
      "global_load_dwordx4 v[152:155], v230, s[62:63] sc1\n\t"
      "global_load_dwordx4 v[128:131], v224, s[64:65] sc1\n\t"
      "global_load_dwordx4 v[132:135], v225, s[64:65] sc1\n\t"
      "global_load_dwordx4 v[136:139], v226, s[64:65] sc1\n\t"
      "s_waitcnt vmcnt(5)\n\t"
      "v_max3_u32 v234, v144, v145, v146\n\t"
      "v_max_u32_e32 v234, v234, v147\n\t"
      "v_cmp_eq_u32_e32 vcc, -1, v234\n\t"
      "s_cbranch_vccnz FQ39L_%=\n\t"
      "FQ39X_%=:\n\t"
      "s_branch FQ39B_%=\n\t"
      "FQ40R_%=:\n\t"
      "s_mov_b32 s51, 0\n\t"
      "FQ40L_%=:\n\t"
      "s_add_u32 s51, s51, 1\n\t"
      "s_cmp_lt_u32 s51, 262144\n\t"
      "s_cbranch_scc0 FQ40X_%=\n\t"
      "s_waitcnt vmcnt(0)\n\t"
      "s_sleep 4\n\t"
      "global_load_dwordx4 v[148:151], v229, s[62:63] sc1\n\t"
      "global_load_dwordx4 v[152:155], v230, s[62:63] sc1\n\t"
      "global_load_dwordx4 v[128:131], v224, s[64:65] sc1\n\t"
      "global_load_dwordx4 v[132:135], v225, s[64:65] sc1\n\t"
      "global_load_dwordx4 v[136:139], v226, s[64:65] sc1\n\t"
      "global_load_dwordx4 v[140:143], v227, s[64:65] sc1\n\t"
      "s_waitcnt vmcnt(5)\n\t"
      "v_max3_u32 v234, v148, v149, v150\n\t"
      "v_max_u32_e32 v234, v234, v151\n\t"
      "v_cmp_eq_u32_e32 vcc, -1, v234\n\t"
      "s_cbranch_vccnz FQ40L_%=\n\t"
      "FQ40X_%=:\n\t"
      "s_branch FQ40B_%=\n\t"
      "FQ41R_%=:\n\t"
      "s_mov_b32 s51, 0\n\t"
      "FQ41L_%=:\n\t"
      "s_add_u32 s51, s51, 1\n\t"
      "s_cmp_lt_u32 s51, 262144\n\t"
      "s_cbranch_scc0 FQ41X_%=\n\t"
      "s_waitcnt vmcnt(0)\n\t"
      "s_sleep 4\n\t"
      "global_load_dwordx4 v[152:155], v230, s[62:63] sc1\n\t"
      "global_load_dwordx4 v[128:131], v224, s[64:65] sc1\n\t"
      "global_load_dwordx4 v[132:135], v225, s[64:65] sc1\n\t"
      "global_load_dwordx4 v[136:139], v226, s[64:65] sc1\n\t"
      "global_load_dwordx4 v[140:143], v227, s[64:65] sc1\n\t"
      "global_load_dwordx4 v[144:147], v228, s[64:65] sc1\n\t"
      "s_waitcnt vmcnt(5)\n\t"
      "v_max3_u32 v234, v152, v153, v154\n\t"
      "v_max_u32_e32 v234, v234, v155\n\t"
      "v_cmp_eq_u32_e32 vcc, -1, v234\n\t"
      "s_cbranch_vccnz FQ41L_%=\n\t"
      "FQ41X_%=:\n\t"
      "s_branch FQ41B_%=\n\t"
      "FQ42R_%=:\n\t"
      "s_mov_b32 s51, 0\n\t"
      "FQ42L_%=:\n\t"
      "s_add_u32 s51, s51, 1\n\t"
      "s_cmp_lt_u32 s51, 262144\n\t"
      "s_cbranch_scc0 FQ42X_%=\n\t"
      "s_sleep 1\n\t"
      "s_mov_b64 exec, 15\n\t"
      "v_mov_b64_e32 v[232:233], 0\n\t"
      "global_atomic_or_x2 v[232:233], v247, v[232:233], s[12:13] sc0 sc1\n\t"
      "s_waitcnt vmcnt(0)\n\t"
      "v_subrev_u32_e32 v232, s14, v232\n\t"
      "v_cmp_ne_u32_e64 s[48:49], s15, v233\n\t"
      "v_subrev_u32_e32 v232, 6, v232\n\t"
      "v_cmp_lt_u32_e32 vcc, 7, v232\n\t"
      "s_mov_b64 exec, -1\n\t"
      "s_or_b64 vcc, vcc, s[48:49]\n\t"
      "s_cbranch_vccnz FQ42L_%=\n\t"
      "FQ42X_%=:\n\t"
      "FQ42DR_%=:\n\t"
      "s_mov_b32 s51, 0\n\t"
      "FQ42DL_%=:\n\t"
      "s_add_u32 s51, s51, 1\n\t"
      "s_cmp_lt_u32 s51, 262144\n\t"
      "s_cbranch_scc0 FQ42DX_%=\n\t"
      "s_waitcnt vmcnt(0)\n\t"
      "s_sleep 1\n\t"
      "global_load_dwordx4 v[128:131], v224, s[64:65] sc1\n\t"
      "global_load_dwordx4 v[132:135], v225, s[64:65] sc1\n\t"
      "global_load_dwordx4 v[136:139], v226, s[64:65] sc1\n\t"
      "global_load_dwordx4 v[140:143], v227, s[64:65] sc1\n\t"
      "global_load_dwordx4 v[144:147], v228, s[64:65] sc1\n\t"
      "global_load_dwordx4 v[148:151], v229, s[64:65] sc1\n\t"
      "s_waitcnt vmcnt(5)\n\t"
      "v_max3_u32 v234, v128, v129, v130\n\t"
      "v_max_u32_e32 v234, v234, v131\n\t"
      "v_cmp_eq_u32_e32 vcc, -1, v234\n\t"
      "s_cbranch_vccnz FQ42DL_%=\n\t"
      "FQ42DX_%=:\n\t"
      "s_branch FQ42B_%=\n\t"
      "NQ5R_%=:\n\t"
      "s_mov_b32 s51, 0\n\t"
      "NQ5L_%=:\n\t"
      "s_add_u32 s51, s51, 1\n\t"
      "s_cmp_lt_u32 s51, 262144\n\t"
      "s_cbranch_scc0 NQ5X_%=\n\t"
      "s_waitcnt vmcnt(0)\n\t"
      "s_sleep 1\n\t"
      "global_load_dwordx4 v[128:131], v224, s[62:63] sc1\n\t"
      "global_load_dwordx4 v[132:135], v225, s[62:63] sc1\n\t"
      "global_load_dwordx4 v[136:139], v226, s[62:63] sc1\n\t"
      "global_load_dwordx4 v[140:143], v227, s[62:63] sc1\n\t"
      "global_load_dwordx4 v[144:147], v228, s[62:63] sc1\n\t"
      "global_load_dwordx4 v[148:151], v229, s[62:63] sc1\n\t"
      "global_load_dwordx4 v[152:155], v230, s[62:63] sc1\n\t"
      "s_waitcnt vmcnt(0)\n\t"
      "v_max3_u32 v234, v128, v129, v130\n\t"
      "v_max3_u32 v234, v234, v131, v132\n\t"
      "v_max3_u32 v234, v234, v133, v134\n\t"
      "v_max3_u32 v234, v234, v135, v136\n\t"
      "v_max3_u32 v234, v234, v137, v138\n\t"
      "v_max3_u32 v234, v234, v139, v140\n\t"
      "v_max3_u32 v234, v234, v141, v142\n\t"
      "v_max3_u32 v234, v234, v143, v144\n\t"
      "v_max3_u32 v234, v234, v145, v146\n\t"
      "v_max3_u32 v234, v234, v147, v148\n\t"
      "v_max3_u32 v234, v234, v149, v150\n\t"
      "v_max3_u32 v234, v234, v151, v152\n\t"
      "v_max3_u32 v234, v234, v153, v154\n\t"
      "v_max_u32_e32 v234, v234, v155\n\t"
      "v_cmp_eq_u32_e32 vcc, -1, v234\n\t"
      "s_cbranch_vccnz NQ5L_%=\n\t"
      "NQ5X_%=:\n\t"
      "v_mfma_f32_16x16x32_bf16 v[200:203], v[128:131], v[0:3], 0\n\t"
      "v_mfma_f32_16x16x32_bf16 v[204:207], v[128:131], v[4:7], 0\n\t"
      "v_mfma_f32_16x16x32_bf16 v[208:211], v[128:131], v[8:11], 0\n\t"
      "v_mfma_f32_16x16x32_bf16 v[212:215], v[128:131], v[12:15], 0\n\t"
      "v_mfma_f32_16x16x32_bf16 v[200:203], v[132:135], v[16:19], v[200:203]\n\t"
      "v_mfma_f32_16x16x32_bf16 v[204:207], v[132:135], v[20:23], v[204:207]\n\t"
      "v_mfma_f32_16x16x32_bf16 v[208:211], v[132:135], v[24:27], v[208:211]\n\t"
      "v_mfma_f32_16x16x32_bf16 v[212:215], v[132:135], v[28:31], v[212:215]\n\t"
      "v_mfma_f32_16x16x32_bf16 v[200:203], v[136:139], v[32:35], v[200:203]\n\t"
      "v_mfma_f32_16x16x32_bf16 v[204:207], v[136:139], v[36:39], v[204:207]\n\t"
      "v_mfma_f32_16x16x32_bf16 v[208:211], v[136:139], v[40:43], v[208:211]\n\t"
      "v_mfma_f32_16x16x32_bf16 v[212:215], v[136:139], v[44:47], v[212:215]\n\t"
      "v_mfma_f32_16x16x32_bf16 v[200:203], v[140:143], v[48:51], v[200:203]\n\t"
      "v_mfma_f32_16x16x32_bf16 v[204:207], v[140:143], v[52:55], v[204:207]\n\t"
      "v_mfma_f32_16x16x32_bf16 v[208:211], v[140:143], v[56:59], v[208:211]\n\t"
      "v_mfma_f32_16x16x32_bf16 v[212:215], v[140:143], v[60:63], v[212:215]\n\t"
      "v_mfma_f32_16x16x32_bf16 v[200:203], v[144:147], v[64:67], v[200:203]\n\t"
      "v_mfma_f32_16x16x32_bf16 v[204:207], v[144:147], v[68:71], v[204:207]\n\t"
      "v_mfma_f32_16x16x32_bf16 v[208:211], v[144:147], v[72:75], v[208:211]\n\t"
      "v_mfma_f32_16x16x32_bf16 v[212:215], v[144:147], v[76:79], v[212:215]\n\t"
      "v_mfma_f32_16x16x32_bf16 v[200:203], v[148:151], v[80:83], v[200:203]\n\t"
      "v_mfma_f32_16x16x32_bf16 v[204:207], v[148:151], v[84:87], v[204:207]\n\t"
      "v_mfma_f32_16x16x32_bf16 v[208:211], v[148:151], v[88:91], v[208:211]\n\t"
      "v_mfma_f32_16x16x32_bf16 v[212:215], v[148:151], v[92:95], v[212:215]\n\t"
      "v_mfma_f32_16x16x32_bf16 v[200:203], v[152:155], v[96:99], v[200:203]\n\t"
      "v_mfma_f32_16x16x32_bf16 v[204:207], v[152:155], v[100:103], v[204:207]\n\t"
      "v_mfma_f32_16x16x32_bf16 v[208:211], v[152:155], v[104:107], v[208:211]\n\t"
      "v_mfma_f32_16x16x32_bf16 v[212:215], v[152:155], v[108:111], v[212:215]\n\t"
      "global_load_dwordx4 v[128:131], v224, s[64:65] sc1\n\t"
      "global_load_dwordx4 v[132:135], v225, s[64:65] sc1\n\t"
      "global_load_dwordx4 v[136:139], v226, s[64:65] sc1\n\t"
      "global_load_dwordx4 v[140:143], v227, s[64:65] sc1\n\t"
      "global_load_dwordx4 v[144:147], v228, s[64:65] sc1\n\t"
      "global_load_dwordx4 v[148:151], v229, s[64:65] sc1\n\t"
      "global_load_dwordx4 v[152:155], v230, s[64:65] sc1\n\t"
      "s_waitcnt vmcnt(0)\n\t"
      "s_nop 7\n\t"
      "s_branch NQ5B_%=\n\t"
      "FQ43R_%=:\n\t"
      "s_mov_b32 s51, 0\n\t"
      "FQ43L_%=:\n\t"
      "s_add_u32 s51, s51, 1\n\t"
      "s_cmp_lt_u32 s51, 262144\n\t"
      "s_cbranch_scc0 FQ43X_%=\n\t"
      "s_sleep 1\n\t"
      "s_mov_b64 exec, 15\n\t"
      "v_mov_b64_e32 v[232:233], 0\n\t"
      "global_atomic_or_x2 v[232:233], v247, v[232:233], s[12:13] sc0 sc1\n\t"
      "s_waitcnt vmcnt(0)\n\t"
      "v_subrev_u32_e32 v232, s14, v232\n\t"
      "v_cmp_ne_u32_e64 s[48:49], s15, v233\n\t"
      "v_subrev_u32_e32 v232, 6, v232\n\t"
      "v_cmp_lt_u32_e32 vcc, 7, v232\n\t"
      "s_mov_b64 exec, -1\n\t"
      "s_or_b64 vcc, vcc, s[48:49]\n\t"
      "s_cbranch_vccnz FQ43L_%=\n\t"
      "FQ43X_%=:\n\t"
      "FQ43DR_%=:\n\t"
      "s_mov_b32 s51, 0\n\t"
      "FQ43DL_%=:\n\t"
      "s_add_u32 s51, s51, 1\n\t"
      "s_cmp_lt_u32 s51, 262144\n\t"
      "s_cbranch_scc0 FQ43DX_%=\n\t"
      "s_waitcnt vmcnt(0)\n\t"
      "s_sleep 1\n\t"
      "global_load_dwordx4 v[132:135], v225, s[64:65] sc1\n\t"
      "global_load_dwordx4 v[136:139], v226, s[64:65] sc1\n\t"
      "global_load_dwordx4 v[140:143], v227, s[64:65] sc1\n\t"
      "global_load_dwordx4 v[144:147], v228, s[64:65] sc1\n\t"
      "global_load_dwordx4 v[148:151], v229, s[64:65] sc1\n\t"
      "global_load_dwordx4 v[152:155], v230, s[64:65] sc1\n\t"
      "s_waitcnt vmcnt(5)\n\t"
      "v_max3_u32 v234, v132, v133, v134\n\t"
      "v_max_u32_e32 v234, v234, v135\n\t"
      "v_cmp_eq_u32_e32 vcc, -1, v234\n\t"
      "s_cbranch_vccnz FQ43DL_%=\n\t"
      "FQ43DX_%=:\n\t"
      "s_branch FQ43B_%=\n\t"
      "FQ44R_%=:\n\t"
      "s_mov_b32 s51, 0\n\t"
      "FQ44L_%=:\n\t"
      "s_add_u32 s51, s51, 1\n\t"
      "s_cmp_lt_u32 s51, 262144\n\t"
      "s_cbranch_scc0 FQ44X_%=\n\t"
      "s_sleep 1\n\t"
      "s_mov_b64 exec, 15\n\t"
      "v_mov_b64_e32 v[232:233], 0\n\t"
      "global_atomic_or_x2 v[232:233], v247, v[232:233], s[12:13] sc0 sc1\n\t"
      "s_waitcnt vmcnt(0)\n\t"
      "v_subrev_u32_e32 v232, s14, v232\n\t"
      "v_cmp_ne_u32_e64 s[48:49], s15, v233\n\t"
      "v_subrev_u32_e32 v232, 6, v232\n\t"
      "v_cmp_lt_u32_e32 vcc, 7, v232\n\t"
      "s_mov_b64 exec, -1\n\t"
      "s_or_b64 vcc, vcc, s[48:49]\n\t"
      "s_cbranch_vccnz FQ44L_%=\n\t"
      "FQ44X_%=:\n\t"
      "FQ44DR_%=:\n\t"
      "s_mov_b32 s51, 0\n\t"
      "FQ44DL_%=:\n\t"
      "s_add_u32 s51, s51, 1\n\t"
      "s_cmp_lt_u32 s51, 262144\n\t"
      "s_cbranch_scc0 FQ44DX_%=\n\t"
      "s_waitcnt vmcnt(0)\n\t"
      "s_sleep 1\n\t"
      "global_load_dwordx4 v[136:139], v226, s[64:65] sc1\n\t"
      "global_load_dwordx4 v[140:143], v227, s[64:65] sc1\n\t"
      "global_load_dwordx4 v[144:147], v228, s[64:65] sc1\n\t"
      "global_load_dwordx4 v[148:151], v229, s[64:65] sc1\n\t"
      "global_load_dwordx4 v[152:155], v230, s[64:65] sc1\n\t"
      "s_waitcnt vmcnt(4)\n\t"
      "v_max3_u32 v234, v136, v137, v138\n\t"
      "v_max_u32_e32 v234, v234, v139\n\t"
      "v_cmp_eq_u32_e32 vcc, -1, v234\n\t"
      "s_cbranch_vccnz FQ44DL_%=\n\t"
      "FQ44DX_%=:\n\t"
      "s_branch FQ44B_%=\n\t"
      "FQ45R_%=:\n\t"
      "s_mov_b32 s51, 0\n\t"
      "FQ45L_%=:\n\t"
      "s_add_u32 s51, s51, 1\n\t"
      "s_cmp_lt_u32 s51, 262144\n\t"
      "s_cbranch_scc0 FQ45X_%=\n\t"
      "s_sleep 1\n\t"
      "s_mov_b64 exec, 15\n\t"
      "v_mov_b64_e32 v[232:233], 0\n\t"
      "global_atomic_or_x2 v[232:233], v247, v[232:233], s[12:13] sc0 sc1\n\t"
      "s_waitcnt vmcnt(0)\n\t"
      "v_subrev_u32_e32 v232, s14, v232\n\t"
      "v_cmp_ne_u32_e64 s[48:49], s15, v233\n\t"
      "v_subrev_u32_e32 v232, 6, v232\n\t"
      "v_cmp_lt_u32_e32 vcc, 7, v232\n\t"
      "s_mov_b64 exec, -1\n\t"
      "s_or_b64 vcc, vcc, s[48:49]\n\t"
      "s_cbranch_vccnz FQ45L_%=\n\t"
      "FQ45X_%=:\n\t"
      "FQ45DR_%=:\n\t"
      "s_mov_b32 s51, 0\n\t"
      "FQ45DL_%=:\n\t"
      "s_add_u32 s51, s51, 1\n\t"
      "s_cmp_lt_u32 s51, 262144\n\t"
      "s_cbranch_scc0 FQ45DX_%=\n\t"
      "s_waitcnt vmcnt(0)\n\t"
      "s_sleep 1\n\t"
      "global_load_dwordx4 v[140:143], v227, s[64:65] sc1\n\t"
      "global_load_dwordx4 v[144:147], v228, s[64:65] sc1\n\t"
      "global_load_dwordx4 v[148:151], v229, s[64:65] sc1\n\t"
      "global_load_dwordx4 v[152:155], v230, s[64:65] sc1\n\t"
      "s_waitcnt vmcnt(3)\n\t"
      "v_max3_u32 v234, v140, v141, v142\n\t"
      "v_max_u32_e32 v234, v234, v143\n\t"
      "v_cmp_eq_u32_e32 vcc, -1, v234\n\t"
      "s_cbranch_vccnz FQ45DL_%=\n\t"
      "FQ45DX_%=:\n\t"
      "s_branch FQ45B_%=\n\t"
      "FQ46R_%=:\n\t"
      "s_mov_b32 s51, 0\n\t"
      "FQ46L_%=:\n\t"
      "s_add_u32 s51, s51, 1\n\t"
      "s_cmp_lt_u32 s51, 262144\n\t"
      "s_cbranch_scc0 FQ46X_%=\n\t"
      "s_sleep 1\n\t"
      "s_mov_b64 exec, 15\n\t"
      "v_mov_b64_e32 v[232:233], 0\n\t"
      "global_atomic_or_x2 v[232:233], v247, v[232:233], s[12:13] sc0 sc1\n\t"
      "s_waitcnt vmcnt(0)\n\t"
      "v_subrev_u32_e32 v232, s14, v232\n\t"
      "v_cmp_ne_u32_e64 s[48:49], s15, v233\n\t"
      "v_subrev_u32_e32 v232, 6, v232\n\t"
      "v_cmp_lt_u32_e32 vcc, 7, v232\n\t"
      "s_mov_b64 exec, -1\n\t"
      "s_or_b64 vcc, vcc, s[48:49]\n\t"
      "s_cbranch_vccnz FQ46L_%=\n\t"
      "FQ46X_%=:\n\t"
      "FQ46DR_%=:\n\t"
      "s_mov_b32 s51, 0\n\t"
      "FQ46DL_%=:\n\t"
      "s_add_u32 s51, s51, 1\n\t"
      "s_cmp_lt_u32 s51, 262144\n\t"
      "s_cbranch_scc0 FQ46DX_%=\n\t"
      "s_waitcnt vmcnt(0)\n\t"
      "s_sleep 1\n\t"
      "global_load_dwordx4 v[144:147], v228, s[64:65] sc1\n\t"
      "global_load_dwordx4 v[148:151], v229, s[64:65] sc1\n\t"
      "global_load_dwordx4 v[152:155], v230, s[64:65] sc1\n\t"
      "s_waitcnt vmcnt(2)\n\t"
      "v_max3_u32 v234, v144, v145, v146\n\t"
      "v_max_u32_e32 v234, v234, v147\n\t"
      "v_cmp_eq_u32_e32 vcc, -1, v234\n\t"
      "s_cbranch_vccnz FQ46DL_%=\n\t"
      "FQ46DX_%=:\n\t"
      "s_branch FQ46B_%=\n\t"
      "FQ47R_%=:\n\t"
      "s_mov_b32 s51, 0\n\t"
      "FQ47L_%=:\n\t"
      "s_add_u32 s51, s51, 1\n\t"
      "s_cmp_lt_u32 s51, 262144\n\t"
      "s_cbranch_scc0 FQ47X_%=\n\t"
      "s_sleep 1\n\t"
      "s_mov_b64 exec, 15\n\t"
      "v_mov_b64_e32 v[232:233], 0\n\t"
      "global_atomic_or_x2 v[232:233], v247, v[232:233], s[12:13] sc0 sc1\n\t"
      "s_waitcnt vmcnt(0)\n\t"
      "v_subrev_u32_e32 v232, s14, v232\n\t"
      "v_cmp_ne_u32_e64 s[48:49], s15, v233\n\t"
      "v_subrev_u32_e32 v232, 6, v232\n\t"
      "v_cmp_lt_u32_e32 vcc, 7, v232\n\t"
      "s_mov_b64 exec, -1\n\t"
      "s_or_b64 vcc, vcc, s[48:49]\n\t"
      "s_cbranch_vccnz FQ47L_%=\n\t"
      "FQ47X_%=:\n\t"
      "FQ47DR_%=:\n\t"
      "s_mov_b32 s51, 0\n\t"
      "FQ47DL_%=:\n\t"
      "s_add_u32 s51, s51, 1\n\t"
      "s_cmp_lt_u32 s51, 262144\n\t"
      "s_cbranch_scc0 FQ47DX_%=\n\t"
      "s_waitcnt vmcnt(0)\n\t"
      "s_sleep 1\n\t"
      "global_load_dwordx4 v[148:151], v229, s[64:65] sc1\n\t"
      "global_load_dwordx4 v[152:155], v230, s[64:65] sc1\n\t"
      "s_waitcnt vmcnt(1)\n\t"
      "v_max3_u32 v234, v148, v149, v150\n\t"
      "v_max_u32_e32 v234, v234, v151\n\t"
      "v_cmp_eq_u32_e32 vcc, -1, v234\n\t"
      "s_cbranch_vccnz FQ47DL_%=\n\t"
      "FQ47DX_%=:\n\t"
      "s_branch FQ47B_%=\n\t"
      "FQ48R_%=:\n\t"
      "s_mov_b32 s51, 0\n\t"
      "FQ48L_%=:\n\t"
      "s_add_u32 s51, s51, 1\n\t"
      "s_cmp_lt_u32 s51, 262144\n\t"
      "s_cbranch_scc0 FQ48X_%=\n\t"
      "s_sleep 1\n\t"
      "s_mov_b64 exec, 15\n\t"
      "v_mov_b64_e32 v[232:233], 0\n\t"
      "global_atomic_or_x2 v[232:233], v247, v[232:233], s[12:13] sc0 sc1\n\t"
      "s_waitcnt vmcnt(0)\n\t"
      "v_subrev_u32_e32 v232, s14, v232\n\t"
      "v_cmp_ne_u32_e64 s[48:49], s15, v233\n\t"
      "v_subrev_u32_e32 v232, 6, v232\n\t"
      "v_cmp_lt_u32_e32 vcc, 7, v232\n\t"
      "s_mov_b64 exec, -1\n\t"
      "s_or_b64 vcc, vcc, s[48:49]\n\t"
      "s_cbranch_vccnz FQ48L_%=\n\t"
      "FQ48X_%=:\n\t"
      "FQ48DR_%=:\n\t"
      "s_mov_b32 s51, 0\n\t"
      "FQ48DL_%=:\n\t"
      "s_add_u32 s51, s51, 1\n\t"
      "s_cmp_lt_u32 s51, 262144\n\t"
      "s_cbranch_scc0 FQ48DX_%=\n\t"
      "s_waitcnt vmcnt(0)\n\t"
      "s_sleep 1\n\t"
      "global_load_dwordx4 v[152:155], v230, s[64:65] sc1\n\t"
      "s_waitcnt vmcnt(0)\n\t"
      "v_max3_u32 v234, v152, v153, v154\n\t"
      "v_max_u32_e32 v234, v234, v155\n\t"
      "v_cmp_eq_u32_e32 vcc, -1, v234\n\t"
      "s_cbranch_vccnz FQ48DL_%=\n\t"
      "FQ48DX_%=:\n\t"
      "s_branch FQ48B_%=\n\t"
      "NQ6R_%=:\n\t"
      "s_mov_b32 s51, 0\n\t"
      "NQ6L_%=:\n\t"
      "s_add_u32 s51, s51, 1\n\t"
      "s_cmp_lt_u32 s51, 262144\n\t"
      "s_cbranch_scc0 NQ6X_%=\n\t"
      "s_waitcnt vmcnt(0)\n\t"
      "s_sleep 1\n\t"
      "global_load_dwordx4 v[128:131], v224, s[64:65] sc1\n\t"
      "global_load_dwordx4 v[132:135], v225, s[64:65] sc1\n\t"
      "global_load_dwordx4 v[136:139], v226, s[64:65] sc1\n\t"
      "global_load_dwordx4 v[140:143], v227, s[64:65] sc1\n\t"
      "global_load_dwordx4 v[144:147], v228, s[64:65] sc1\n\t"
      "global_load_dwordx4 v[148:151], v229, s[64:65] sc1\n\t"
      "global_load_dwordx4 v[152:155], v230, s[64:65] sc1\n\t"
      "s_waitcnt vmcnt(0)\n\t"
      "v_max3_u32 v234, v128, v129, v130\n\t"
      "v_max3_u32 v234, v234, v131, v132\n\t"
      "v_max3_u32 v234, v234, v133, v134\n\t"
      "v_max3_u32 v234, v234, v135, v136\n\t"
      "v_max3_u32 v234, v234, v137, v138\n\t"
      "v_max3_u32 v234, v234, v139, v140\n\t"
      "v_max3_u32 v234, v234, v141, v142\n\t"
      "v_max3_u32 v234, v234, v143, v144\n\t"
      "v_max3_u32 v234, v234, v145, v146\n\t"
      "v_max3_u32 v234, v234, v147, v148\n\t"
      "v_max3_u32 v234, v234, v149, v150\n\t"
      "v_max3_u32 v234, v234, v151, v152\n\t"
      "v_max3_u32 v234, v234, v153, v154\n\t"
      "v_max_u32_e32 v234, v234, v155\n\t"
      "v_cmp_eq_u32_e32 vcc, -1, v234\n\t"
      "s_cbranch_vccnz NQ6L_%=\n\t"
      "NQ6X_%=:\n\t"
      "v_mfma_f32_16x16x32_bf16 v[184:187], v[128:131], v[0:3], 0\n\t"
      "v_mfma_f32_16x16x32_bf16 v[188:191], v[128:131], v[4:7], 0\n\t"
      "v_mfma_f32_16x16x32_bf16 v[192:195], v[128:131], v[8:11], 0\n\t"
      "v_mfma_f32_16x16x32_bf16 v[196:199], v[128:131], v[12:15], 0\n\t"
      "v_mfma_f32_16x16x32_bf16 v[184:187], v[132:135], v[16:19], v[184:187]\n\t"
      "v_mfma_f32_16x16x32_bf16 v[188:191], v[132:135], v[20:23], v[188:191]\n\t"
      "v_mfma_f32_16x16x32_bf16 v[192:195], v[132:135], v[24:27], v[192:195]\n\t"
      "v_mfma_f32_16x16x32_bf16 v[196:199], v[132:135], v[28:31], v[196:199]\n\t"
      "v_mfma_f32_16x16x32_bf16 v[184:187], v[136:139], v[32:35], v[184:187]\n\t"
      "v_mfma_f32_16x16x32_bf16 v[188:191], v[136:139], v[36:39], v[188:191]\n\t"
      "v_mfma_f32_16x16x32_bf16 v[192:195], v[136:139], v[40:43], v[192:195]\n\t"
      "v_mfma_f32_16x16x32_bf16 v[196:199], v[136:139], v[44:47], v[196:199]\n\t"
      "v_mfma_f32_16x16x32_bf16 v[184:187], v[140:143], v[48:51], v[184:187]\n\t"
      "v_mfma_f32_16x16x32_bf16 v[188:191], v[140:143], v[52:55], v[188:191]\n\t"
      "v_mfma_f32_16x16x32_bf16 v[192:195], v[140:143], v[56:59], v[192:195]\n\t"
      "v_mfma_f32_16x16x32_bf16 v[196:199], v[140:143], v[60:63], v[196:199]\n\t"
      "v_mfma_f32_16x16x32_bf16 v[184:187], v[144:147], v[64:67], v[184:187]\n\t"
      "v_mfma_f32_16x16x32_bf16 v[188:191], v[144:147], v[68:71], v[188:191]\n\t"
      "v_mfma_f32_16x16x32_bf16 v[192:195], v[144:147], v[72:75], v[192:195]\n\t"
      "v_mfma_f32_16x16x32_bf16 v[196:199], v[144:147], v[76:79], v[196:199]\n\t"
      "v_mfma_f32_16x16x32_bf16 v[184:187], v[148:151], v[80:83], v[184:187]\n\t"
      "v_mfma_f32_16x16x32_bf16 v[188:191], v[148:151], v[84:87], v[188:191]\n\t"
      "v_mfma_f32_16x16x32_bf16 v[192:195], v[148:151], v[88:91], v[192:195]\n\t"
      "v_mfma_f32_16x16x32_bf16 v[196:199], v[148:151], v[92:95], v[196:199]\n\t"
      "v_mfma_f32_16x16x32_bf16 v[184:187], v[152:155], v[96:99], v[184:187]\n\t"
      "v_mfma_f32_16x16x32_bf16 v[188:191], v[152:155], v[100:103], v[188:191]\n\t"
      "v_mfma_f32_16x16x32_bf16 v[192:195], v[152:155], v[104:107], v[192:195]\n\t"
      "v_mfma_f32_16x16x32_bf16 v[196:199], v[152:155], v[108:111], v[196:199]\n\t"
      "s_waitcnt vmcnt(0)\n\t"
      "s_nop 7\n\t"
      "s_branch NQ6B_%=\n\t"
      "FP0R_%=:\n\t"
      "s_mov_b32 s51, 0\n\t"
      "FP0L_%=:\n\t"
      "s_add_u32 s51, s51, 1\n\t"
      "s_cmp_lt_u32 s51, 262144\n\t"
      "s_cbranch_scc0 FP0X_%=\n\t"
      "s_waitcnt vmcnt(0)\n\t"
      "s_sleep 1\n\t"
      "global_load_dwordx4 v[128:131], v224, s[52:53] sc1\n\t"
      "global_load_dwordx4 v[132:135], v225, s[52:53] sc1\n\t"
      "global_load_dwordx4 v[136:139], v226, s[52:53] sc1\n\t"
      "global_load_dwordx4 v[140:143], v227, s[52:53] sc1\n\t"
      "global_load_dwordx4 v[144:147], v228, s[52:53] sc1\n\t"
      "global_load_dwordx4 v[148:151], v229, s[52:53] sc1\n\t"
      "global_load_dwordx4 v[152:155], v230, s[52:53] sc1\n\t"
      "s_waitcnt vmcnt(6)\n\t"
      "v_max3_u32 v234, v128, v129, v130\n\t"
      "v_max_u32_e32 v234, v234, v131\n\t"
      "v_cmp_eq_u32_e32 vcc, -1, v234\n\t"
      "s_cbranch_vccnz FP0L_%=\n\t"
      "FP0X_%=:\n\t"
      "s_branch FP0B_%=\n\t"
      "FP1R_%=:\n\t"
      "s_mov_b32 s51, 0\n\t"
      "FP1L_%=:\n\t"
      "s_add_u32 s51, s51, 1\n\t"
      "s_cmp_lt_u32 s51, 262144\n\t"
      "s_cbranch_scc0 FP1X_%=\n\t"
      "s_waitcnt vmcnt(0)\n\t"
      "s_sleep 1\n\t"
      "global_load_dwordx4 v[132:135], v225, s[52:53] sc1\n\t"
      "global_load_dwordx4 v[136:139], v226, s[52:53] sc1\n\t"
      "global_load_dwordx4 v[140:143], v227, s[52:53] sc1\n\t"
      "global_load_dwordx4 v[144:147], v228, s[52:53] sc1\n\t"
      "global_load_dwordx4 v[148:151], v229, s[52:53] sc1\n\t"
      "global_load_dwordx4 v[152:155], v230, s[52:53] sc1\n\t"
      "global_load_dwordx4 v[128:131], v224, s[54:55] sc1\n\t"
      "s_waitcnt vmcnt(6)\n\t"
      "v_max3_u32 v234, v132, v133, v134\n\t"
      "v_max_u32_e32 v234, v234, v135\n\t"
      "v_cmp_eq_u32_e32 vcc, -1, v234\n\t"
      "s_cbranch_vccnz FP1L_%=\n\t"
      "FP1X_%=:\n\t"
      "s_branch FP1B_%=\n\t"
      "FP2R_%=:\n\t"
      "s_mov_b32 s51, 0\n\t"
      "FP2L_%=:\n\t"
      "s_add_u32 s51, s51, 1\n\t"
      "s_cmp_lt_u32 s51, 262144\n\t"
      "s_cbranch_scc0 FP2X_%=\n\t"
      "s_waitcnt vmcnt(0)\n\t"
      "s_sleep 1\n\t"
      "global_load_dwordx4 v[136:139], v226, s[52:53] sc1\n\t"
      "global_load_dwordx4 v[140:143], v227, s[52:53] sc1\n\t"
      "global_load_dwordx4 v[144:147], v228, s[52:53] sc1\n\t"
      "global_load_dwordx4 v[148:151], v229, s[52:53] sc1\n\t"
      "global_load_dwordx4 v[152:155], v230, s[52:53] sc1\n\t"
      "global_load_dwordx4 v[128:131], v224, s[54:55] sc1\n\t"
      "global_load_dwordx4 v[132:135], v225, s[54:55] sc1\n\t"
      "s_waitcnt vmcnt(6)\n\t"
      "v_max3_u32 v234, v136, v137, v138\n\t"
      "v_max_u32_e32 v234, v234, v139\n\t"
      "v_cmp_eq_u32_e32 vcc, -1, v234\n\t"
      "s_cbranch_vccnz FP2L_%=\n\t"
      "FP2X_%=:\n\t"
      "s_branch FP2B_%=\n\t"
      "FP3R_%=:\n\t"
      "s_mov_b32 s51, 0\n\t"
      "FP3L_%=:\n\t"
      "s_add_u32 s51, s51, 1\n\t"
      "s_cmp_lt_u32 s51, 262144\n\t"
      "s_cbranch_scc0 FP3X_%=\n\t"
      "s_waitcnt vmcnt(0)\n\t"
      "s_sleep 1\n\t"
      "global_load_dwordx4 v[140:143], v227, s[52:53] sc1\n\t"
      "global_load_dwordx4 v[144:147], v228, s[52:53] sc1\n\t"
      "global_load_dwordx4 v[148:151], v229, s[52:53] sc1\n\t"
      "global_load_dwordx4 v[152:155], v230, s[52:53] sc1\n\t"
      "global_load_dwordx4 v[128:131], v224, s[54:55] sc1\n\t"
      "global_load_dwordx4 v[132:135], v225, s[54:55] sc1\n\t"
      "global_load_dwordx4 v[136:139], v226, s[54:55] sc1\n\t"
      "s_waitcnt vmcnt(6)\n\t"
      "v_max3_u32 v234, v140, v141, v142\n\t"
      "v_max_u32_e32 v234, v234, v143\n\t"
      "v_cmp_eq_u32_e32 vcc, -1, v234\n\t"
      "s_cbranch_vccnz FP3L_%=\n\t"
      "FP3X_%=:\n\t"
      "s_branch FP3B_%=\n\t"
      "FP4R_%=:\n\t"
      "s_mov_b32 s51, 0\n\t"
      "FP4L_%=:\n\t"
      "s_add_u32 s51, s51, 1\n\t"
      "s_cmp_lt_u32 s51, 262144\n\t"
      "s_cbranch_scc0 FP4X_%=\n\t"
      "s_waitcnt vmcnt(0)\n\t"
      "s_sleep 1\n\t"
      "global_load_dwordx4 v[144:147], v228, s[52:53] sc1\n\t"
      "global_load_dwordx4 v[148:151], v229, s[52:53] sc1\n\t"
      "global_load_dwordx4 v[152:155], v230, s[52:53] sc1\n\t"
      "global_load_dwordx4 v[128:131], v224, s[54:55] sc1\n\t"
      "global_load_dwordx4 v[132:135], v225, s[54:55] sc1\n\t"
      "global_load_dwordx4 v[136:139], v226, s[54:55] sc1\n\t"
      "global_load_dwordx4 v[140:143], v227, s[54:55] sc1\n\t"
      "s_waitcnt vmcnt(6)\n\t"
      "v_max3_u32 v234, v144, v145, v146\n\t"
      "v_max_u32_e32 v234, v234, v147\n\t"
      "v_cmp_eq_u32_e32 vcc, -1, v234\n\t"
      "s_cbranch_vccnz FP4L_%=\n\t"
      "FP4X_%=:\n\t"
      "s_branch FP4B_%=\n\t"
      "FP5R_%=:\n\t"
      "s_mov_b32 s51, 0\n\t"
      "FP5L_%=:\n\t"
      "s_add_u32 s51, s51, 1\n\t"
      "s_cmp_lt_u32 s51, 262144\n\t"
      "s_cbranch_scc0 FP5X_%=\n\t"
      "s_waitcnt vmcnt(0)\n\t"
      "s_sleep 1\n\t"
      "global_load_dwordx4 v[148:151], v229, s[52:53] sc1\n\t"
      "global_load_dwordx4 v[152:155], v230, s[52:53] sc1\n\t"
      "global_load_dwordx4 v[128:131], v224, s[54:55] sc1\n\t"
      "global_load_dwordx4 v[132:135], v225, s[54:55] sc1\n\t"
      "global_load_dwordx4 v[136:139], v226, s[54:55] sc1\n\t"
      "global_load_dwordx4 v[140:143], v227, s[54:55] sc1\n\t"
      "global_load_dwordx4 v[144:147], v228, s[54:55] sc1\n\t"
      "s_waitcnt vmcnt(6)\n\t"
      "v_max3_u32 v234, v148, v149, v150\n\t"
      "v_max_u32_e32 v234, v234, v151\n\t"
      "v_cmp_eq_u32_e32 vcc, -1, v234\n\t"
      "s_cbranch_vccnz FP5L_%=\n\t"
      "FP5X_%=:\n\t"
      "s_branch FP5B_%=\n\t"
      "FP6R_%=:\n\t"
      "s_mov_b32 s51, 0\n\t"
      "FP6L_%=:\n\t"
      "s_add_u32 s51, s51, 1\n\t"
      "s_cmp_lt_u32 s51, 262144\n\t"
      "s_cbranch_scc0 FP6X_%=\n\t"
      "s_waitcnt vmcnt(0)\n\t"
      "s_sleep 1\n\t"
      "global_load_dwordx4 v[152:155], v230, s[52:53] sc1\n\t"
      "global_load_dwordx4 v[128:131], v224, s[54:55] sc1\n\t"
      "global_load_dwordx4 v[132:135], v225, s[54:55] sc1\n\t"
      "global_load_dwordx4 v[136:139], v226, s[54:55] sc1\n\t"
      "global_load_dwordx4 v[140:143], v227, s[54:55] sc1\n\t"
      "global_load_dwordx4 v[144:147], v228, s[54:55] sc1\n\t"
      "global_load_dwordx4 v[148:151], v229, s[54:55] sc1\n\t"
      "s_waitcnt vmcnt(6)\n\t"
      "v_max3_u32 v234, v152, v153, v154\n\t"
      "v_max_u32_e32 v234, v234, v155\n\t"
      "v_cmp_eq_u32_e32 vcc, -1, v234\n\t"
      "s_cbranch_vccnz FP6L_%=\n\t"
      "FP6X_%=:\n\t"
      "s_branch FP6B_%=\n\t"
      "FP7R_%=:\n\t"
      "s_mov_b32 s51, 0\n\t"
      "FP7L_%=:\n\t"
      "s_add_u32 s51, s51, 1\n\t"
      "s_cmp_lt_u32 s51, 262144\n\t"
      "s_cbranch_scc0 FP7X_%=\n\t"
      "s_waitcnt vmcnt(0)\n\t"
      "s_sleep 4\n\t"
      "global_load_dwordx4 v[128:131], v224, s[54:55] sc1\n\t"
      "global_load_dwordx4 v[132:135], v225, s[54:55] sc1\n\t"
      "global_load_dwordx4 v[136:139], v226, s[54:55] sc1\n\t"
      "global_load_dwordx4 v[140:143], v227, s[54:55] sc1\n\t"
      "global_load_dwordx4 v[144:147], v228, s[54:55] sc1\n\t"
      "global_load_dwordx4 v[148:151], v229, s[54:55] sc1\n\t"
      "s_waitcnt vmcnt(5)\n\t"
      "v_max3_u32 v234, v128, v129, v130\n\t"
      "v_max_u32_e32 v234, v234, v131\n\t"
      "v_cmp_eq_u32_e32 vcc, -1, v234\n\t"
      "s_cbranch_vccnz FP7L_%=\n\t"
      "FP7X_%=:\n\t"
      "s_branch FP7B_%=\n\t"
      "FP8R_%=:\n\t"
      "s_mov_b32 s51, 0\n\t"
      "FP8L_%=:\n\t"
      "s_add_u32 s51, s51, 1\n\t"
      "s_cmp_lt_u32 s51, 262144\n\t"
      "s_cbranch_scc0 FP8X_%=\n\t"
      "s_waitcnt vmcnt(0)\n\t"
      "s_sleep 4\n\t"
      "global_load_dwordx4 v[132:135], v225, s[54:55] sc1\n\t"
      "global_load_dwordx4 v[136:139], v226, s[54:55] sc1\n\t"
      "global_load_dwordx4 v[140:143], v227, s[54:55] sc1\n\t"
      "global_load_dwordx4 v[144:147], v228, s[54:55] sc1\n\t"
      "global_load_dwordx4 v[148:151], v229, s[54:55] sc1\n\t"
      "global_load_dwordx4 v[152:155], v230, s[54:55] sc1\n\t"
      "s_waitcnt vmcnt(5)\n\t"
      "v_max3_u32 v234, v132, v133, v134\n\t"
      "v_max_u32_e32 v234, v234, v135\n\t"
      "v_cmp_eq_u32_e32 vcc, -1, v234\n\t"
      "s_cbranch_vccnz FP8L_%=\n\t"
      "FP8X_%=:\n\t"
      "s_branch FP8B_%=\n\t"
      "FP9R_%=:\n\t"
      "s_mov_b32 s51, 0\n\t"
      "FP9L_%=:\n\t"
      "s_add_u32 s51, s51, 1\n\t"
      "s_cmp_lt_u32 s51, 262144\n\t"
      "s_cbranch_scc0 FP9X_%=\n\t"
      "s_waitcnt vmcnt(0)\n\t"
      "s_sleep 4\n\t"
      "global_load_dwordx4 v[136:139], v226, s[54:55] sc1\n\t"
      "global_load_dwordx4 v[140:143], v227, s[54:55] sc1\n\t"
      "global_load_dwordx4 v[144:147], v228, s[54:55] sc1\n\t"
      "global_load_dwordx4 v[148:151], v229, s[54:55] sc1\n\t"
      "global_load_dwordx4 v[152:155], v230, s[54:55] sc1\n\t"
      "global_load_dwordx4 v[128:131], v224, s[56:57] sc1\n\t"
      "s_waitcnt vmcnt(5)\n\t"
      "v_max3_u32 v234, v136, v137, v138\n\t"
      "v_max_u32_e32 v234, v234, v139\n\t"
      "v_cmp_eq_u32_e32 vcc, -1, v234\n\t"
      "s_cbranch_vccnz FP9L_%=\n\t"
      "FP9X_%=:\n\t"
      "s_branch FP9B_%=\n\t"
      "FP10R_%=:\n\t"
      "s_mov_b32 s51, 0\n\t"
      "FP10L_%=:\n\t"
      "s_add_u32 s51, s51, 1\n\t"
      "s_cmp_lt_u32 s51, 262144\n\t"
      "s_cbranch_scc0 FP10X_%=\n\t"
      "s_waitcnt vmcnt(0)\n\t"
      "s_sleep 4\n\t"
      "global_load_dwordx4 v[140:143], v227, s[54:55] sc1\n\t"
      "global_load_dwordx4 v[144:147], v228, s[54:55] sc1\n\t"
      "global_load_dwordx4 v[148:151], v229, s[54:55] sc1\n\t"
      "global_load_dwordx4 v[152:155], v230, s[54:55] sc1\n\t"
      "global_load_dwordx4 v[128:131], v224, s[56:57] sc1\n\t"
      "global_load_dwordx4 v[132:135], v225, s[56:57] sc1\n\t"
      "s_waitcnt vmcnt(5)\n\t"
      "v_max3_u32 v234, v140, v141, v142\n\t"
      "v_max_u32_e32 v234, v234, v143\n\t"
      "v_cmp_eq_u32_e32 vcc, -1, v234\n\t"
      "s_cbranch_vccnz FP10L_%=\n\t"
      "FP10X_%=:\n\t"
      "s_branch FP10B_%=\n\t"
      "FP11R_%=:\n\t"
      "s_mov_b32 s51, 0\n\t"
      "FP11L_%=:\n\t"
      "s_add_u32 s51, s51, 1\n\t"
      "s_cmp_lt_u32 s51, 262144\n\t"
      "s_cbranch_scc0 FP11X_%=\n\t"
      "s_waitcnt vmcnt(0)\n\t"
      "s_sleep 4\n\t"
      "global_load_dwordx4 v[144:147], v228, s[54:55] sc1\n\t"
      "global_load_dwordx4 v[148:151], v229, s[54:55] sc1\n\t"
      "global_load_dwordx4 v[152:155], v230, s[54:55] sc1\n\t"
      "global_load_dwordx4 v[128:131], v224, s[56:57] sc1\n\t"
      "global_load_dwordx4 v[132:135], v225, s[56:57] sc1\n\t"
      "global_load_dwordx4 v[136:139], v226, s[56:57] sc1\n\t"
      "s_waitcnt vmcnt(5)\n\t"
      "v_max3_u32 v234, v144, v145, v146\n\t"
      "v_max_u32_e32 v234, v234, v147\n\t"
      "v_cmp_eq_u32_e32 vcc, -1, v234\n\t"
      "s_cbranch_vccnz FP11L_%=\n\t"
      "FP11X_%=:\n\t"
      "s_branch FP11B_%=\n\t"
      "FP12R_%=:\n\t"
      "s_mov_b32 s51, 0\n\t"
      "FP12L_%=:\n\t"
      "s_add_u32 s51, s51, 1\n\t"
      "s_cmp_lt_u32 s51, 262144\n\t"
      "s_cbranch_scc0 FP12X_%=\n\t"
      "s_waitcnt vmcnt(0)\n\t"
      "s_sleep 4\n\t"
      "global_load_dwordx4 v[148:151], v229, s[54:55] sc1\n\t"
      "global_load_dwordx4 v[152:155], v230, s[54:55] sc1\n\t"
      "global_load_dwordx4 v[128:131], v224, s[56:57] sc1\n\t"
      "global_load_dwordx4 v[132:135], v225, s[56:57] sc1\n\t"
      "global_load_dwordx4 v[136:139], v226, s[56:57] sc1\n\t"
      "global_load_dwordx4 v[140:143], v227, s[56:57] sc1\n\t"
      "s_waitcnt vmcnt(5)\n\t"
      "v_max3_u32 v234, v148, v149, v150\n\t"
      "v_max_u32_e32 v234, v234, v151\n\t"
      "v_cmp_eq_u32_e32 vcc, -1, v234\n\t"
      "s_cbranch_vccnz FP12L_%=\n\t"
      "FP12X_%=:\n\t"
      "s_branch FP12B_%=\n\t"
      "FP13R_%=:\n\t"
      "s_mov_b32 s51, 0\n\t"
      "FP13L_%=:\n\t"
      "s_add_u32 s51, s51, 1\n\t"
      "s_cmp_lt_u32 s51, 262144\n\t"
      "s_cbranch_scc0 FP13X_%=\n\t"
      "s_waitcnt vmcnt(0)\n\t"
      "s_sleep 4\n\t"
      "global_load_dwordx4 v[152:155], v230, s[54:55] sc1\n\t"
      "global_load_dwordx4 v[128:131], v224, s[56:57] sc1\n\t"
      "global_load_dwordx4 v[132:135], v225, s[56:57] sc1\n\t"
      "global_load_dwordx4 v[136:139], v226, s[56:57] sc1\n\t"
      "global_load_dwordx4 v[140:143], v227, s[56:57] sc1\n\t"
      "global_load_dwordx4 v[144:147], v228, s[56:57] sc1\n\t"
      "s_waitcnt vmcnt(5)\n\t"
      "v_max3_u32 v234, v152, v153, v154\n\t"
      "v_max_u32_e32 v234, v234, v155\n\t"
      "v_cmp_eq_u32_e32 vcc, -1, v234\n\t"
      "s_cbranch_vccnz FP13L_%=\n\t"
      "FP13X_%=:\n\t"
      "s_waitcnt vmcnt(0)\n\t"
      "s_branch FP13B_%=\n\t"
      "FP14R_%=:\n\t"
      "s_mov_b32 s51, 0\n\t"
      "FP14L_%=:\n\t"
      "s_add_u32 s51, s51, 1\n\t"
      "s_cmp_lt_u32 s51, 262144\n\t"
      "s_cbranch_scc0 FP14X_%=\n\t"
      "s_waitcnt vmcnt(0)\n\t"
      "s_sleep 4\n\t"
      "global_load_dwordx4 v[128:131], v224, s[56:57] sc1\n\t"
      "global_load_dwordx4 v[132:135], v225, s[56:57] sc1\n\t"
      "global_load_dwordx4 v[136:139], v226, s[56:57] sc1\n\t"
      "global_load_dwordx4 v[140:143], v227, s[56:57] sc1\n\t"
      "global_load_dwordx4 v[144:147], v228, s[56:57] sc1\n\t"
      "global_load_dwordx4 v[148:151], v229, s[56:57] sc1\n\t"
      "s_waitcnt vmcnt(5)\n\t"
      "v_max3_u32 v234, v128, v129, v130\n\t"
      "v_max_u32_e32 v234, v234, v131\n\t"
      "v_cmp_eq_u32_e32 vcc, -1, v234\n\t"
      "s_cbranch_vccnz FP14L_%=\n\t"
      "FP14X_%=:\n\t"
      "s_waitcnt vmcnt(0)\n\t"
      "s_branch FP14B_%=\n\t"
      "NP1R_%=:\n\t"
      "s_mov_b32 s51, 0\n\t"
      "NP1L_%=:\n\t"
      "s_add_u32 s51, s51, 1\n\t"
      "s_cmp_lt_u32 s51, 262144\n\t"
      "s_cbranch_scc0 NP1X_%=\n\t"
      "s_waitcnt vmcnt(0)\n\t"
      "s_sleep 1\n\t"
      "global_load_dwordx4 v[128:131], v224, s[54:55] sc1\n\t"
      "global_load_dwordx4 v[132:135], v225, s[54:55] sc1\n\t"
      "global_load_dwordx4 v[136:139], v226, s[54:55] sc1\n\t"
      "global_load_dwordx4 v[140:143], v227, s[54:55] sc1\n\t"
      "global_load_dwordx4 v[144:147], v228, s[54:55] sc1\n\t"
      "global_load_dwordx4 v[148:151], v229, s[54:55] sc1\n\t"
      "global_load_dwordx4 v[152:155], v230, s[54:55] sc1\n\t"
      "s_waitcnt vmcnt(0)\n\t"
      "v_max3_u32 v234, v128, v129, v130\n\t"
      "v_max3_u32 v234, v234, v131, v132\n\t"
      "v_max3_u32 v234, v234, v133, v134\n\t"
      "v_max3_u32 v234, v234, v135, v136\n\t"
      "v_max3_u32 v234, v234, v137, v138\n\t"
      "v_max3_u32 v234, v234, v139, v140\n\t"
      "v_max3_u32 v234, v234, v141, v142\n\t"
      "v_max3_u32 v234, v234, v143, v144\n\t"
      "v_max3_u32 v234, v234, v145, v146\n\t"
      "v_max3_u32 v234, v234, v147, v148\n\t"
      "v_max3_u32 v234, v234, v149, v150\n\t"
      "v_max3_u32 v234, v234, v151, v152\n\t"
      "v_max3_u32 v234, v234, v153, v154\n\t"
      "v_max_u32_e32 v234, v234, v155\n\t"
      "v_cmp_eq_u32_e32 vcc, -1, v234\n\t"
      "s_cbranch_vccnz NP1L_%=\n\t"
      "NP1X_%=:\n\t"
      "v_mfma_f32_16x16x32_bf16 v[200:203], v[128:131], v[0:3], v[160:163]\n\t"
      "v_mfma_f32_16x16x32_bf16 v[204:207], v[128:131], v[4:7], 0\n\t"
      "v_mfma_f32_16x16x32_bf16 v[208:211], v[128:131], v[8:11], 0\n\t"
      "v_mfma_f32_16x16x32_bf16 v[212:215], v[128:131], v[12:15], 0\n\t"
      "v_mfma_f32_16x16x32_bf16 v[200:203], v[132:135], v[16:19], v[200:203]\n\t"
      "v_mfma_f32_16x16x32_bf16 v[204:207], v[132:135], v[20:23], v[204:207]\n\t"
      "v_mfma_f32_16x16x32_bf16 v[208:211], v[132:135], v[24:27], v[208:211]\n\t"
      "v_mfma_f32_16x16x32_bf16 v[212:215], v[132:135], v[28:31], v[212:215]\n\t"
      "v_mfma_f32_16x16x32_bf16 v[200:203], v[136:139], v[32:35], v[200:203]\n\t"
      "v_mfma_f32_16x16x32_bf16 v[204:207], v[136:139], v[36:39], v[204:207]\n\t"
      "v_mfma_f32_16x16x32_bf16 v[208:211], v[136:139], v[40:43], v[208:211]\n\t"
      "v_mfma_f32_16x16x32_bf16 v[212:215], v[136:139], v[44:47], v[212:215]\n\t"
      "v_mfma_f32_16x16x32_bf16 v[200:203], v[140:143], v[48:51], v[200:203]\n\t"
      "v_mfma_f32_16x16x32_bf16 v[204:207], v[140:143], v[52:55], v[204:207]\n\t"
      "v_mfma_f32_16x16x32_bf16 v[208:211], v[140:143], v[56:59], v[208:211]\n\t"
      "v_mfma_f32_16x16x32_bf16 v[212:215], v[140:143], v[60:63], v[212:215]\n\t"
      "v_mfma_f32_16x16x32_bf16 v[200:203], v[144:147], v[64:67], v[200:203]\n\t"
      "v_mfma_f32_16x16x32_bf16 v[204:207], v[144:147], v[68:71], v[204:207]\n\t"
      "v_mfma_f32_16x16x32_bf16 v[208:211], v[144:147], v[72:75], v[208:211]\n\t"
      "v_mfma_f32_16x16x32_bf16 v[212:215], v[144:147], v[76:79], v[212:215]\n\t"
      "v_mfma_f32_16x16x32_bf16 v[200:203], v[148:151], v[80:83], v[200:203]\n\t"
      "v_mfma_f32_16x16x32_bf16 v[204:207], v[148:151], v[84:87], v[204:207]\n\t"
      "v_mfma_f32_16x16x32_bf16 v[208:211], v[148:151], v[88:91], v[208:211]\n\t"
      "v_mfma_f32_16x16x32_bf16 v[212:215], v[148:151], v[92:95], v[212:215]\n\t"
      "v_mfma_f32_16x16x32_bf16 v[200:203], v[152:155], v[96:99], v[200:203]\n\t"
      "v_mfma_f32_16x16x32_bf16 v[204:207], v[152:155], v[100:103], v[204:207]\n\t"
      "v_mfma_f32_16x16x32_bf16 v[208:211], v[152:155], v[104:107], v[208:211]\n\t"
      "v_mfma_f32_16x16x32_bf16 v[212:215], v[152:155], v[108:111], v[212:215]\n\t"
      "global_load_dwordx4 v[128:131], v224, s[56:57] sc1\n\t"
      "global_load_dwordx4 v[132:135], v225, s[56:57] sc1\n\t"
      "global_load_dwordx4 v[136:139], v226, s[56:57] sc1\n\t"
      "global_load_dwordx4 v[140:143], v227, s[56:57] sc1\n\t"
      "global_load_dwordx4 v[144:147], v228, s[56:57] sc1\n\t"
      "global_load_dwordx4 v[148:151], v229, s[56:57] sc1\n\t"
      "global_load_dwordx4 v[152:155], v230, s[56:57] sc1\n\t"
      "s_waitcnt vmcnt(0)\n\t"
      "s_nop 7\n\t"
      "s_branch NP1B_%=\n\t"
      "FP15R_%=:\n\t"
      "s_mov_b32 s51, 0\n\t"
      "FP15L_%=:\n\t"
      "s_add_u32 s51, s51, 1\n\t"
      "s_cmp_lt_u32 s51, 262144\n\t"
      "s_cbranch_scc0 FP15X_%=\n\t"
      "s_waitcnt vmcnt(0)\n\t"
      "s_sleep 4\n\t"
      "global_load_dwordx4 v[132:135], v225, s[56:57] sc1\n\t"
      "global_load_dwordx4 v[136:139], v226, s[56:57] sc1\n\t"
      "global_load_dwordx4 v[140:143], v227, s[56:57] sc1\n\t"
      "global_load_dwordx4 v[144:147], v228, s[56:57] sc1\n\t"
      "global_load_dwordx4 v[148:151], v229, s[56:57] sc1\n\t"
      "global_load_dwordx4 v[152:155], v230, s[56:57] sc1\n\t"
      "s_waitcnt vmcnt(5)\n\t"
      "v_max3_u32 v234, v132, v133, v134\n\t"
      "v_max_u32_e32 v234, v234, v135\n\t"
      "v_cmp_eq_u32_e32 vcc, -1, v234\n\t"
      "s_cbranch_vccnz FP15L_%=\n\t"
      "FP15X_%=:\n\t"
      "s_waitcnt vmcnt(0)\n\t"
      "s_branch FP15B_%=\n\t"
      "FP16R_%=:\n\t"
      "s_mov_b32 s51, 0\n\t"
      "FP16L_%=:\n\t"
      "s_add_u32 s51, s51, 1\n\t"
      "s_cmp_lt_u32 s51, 262144\n\t"
      "s_cbranch_scc0 FP16X_%=\n\t"
      "s_waitcnt vmcnt(0)\n\t"
      "s_sleep 4\n\t"
      "global_load_dwordx4 v[136:139], v226, s[56:57] sc1\n\t"
      "global_load_dwordx4 v[140:143], v227, s[56:57] sc1\n\t"
      "global_load_dwordx4 v[144:147], v228, s[56:57] sc1\n\t"
      "global_load_dwordx4 v[148:151], v229, s[56:57] sc1\n\t"
      "global_load_dwordx4 v[152:155], v230, s[56:57] sc1\n\t"
      "global_load_dwordx4 v[128:131], v224, s[58:59] sc1\n\t"
      "s_waitcnt vmcnt(5)\n\t"
      "v_max3_u32 v234, v136, v137, v138\n\t"
      "v_max_u32_e32 v234, v234, v139\n\t"
      "v_cmp_eq_u32_e32 vcc, -1, v234\n\t"
      "s_cbranch_vccnz FP16L_%=\n\t"
      "FP16X_%=:\n\t"
      "s_waitcnt vmcnt(0)\n\t"
      "s_branch FP16B_%=\n\t"
      "FP17R_%=:\n\t"
      "s_mov_b32 s51, 0\n\t"
      "FP17L_%=:\n\t"
      "s_add_u32 s51, s51, 1\n\t"
      "s_cmp_lt_u32 s51, 262144\n\t"
      "s_cbranch_scc0 FP17X_%=\n\t"
      "s_waitcnt vmcnt(0)\n\t"
      "s_sleep 4\n\t"
      "global_load_dwordx4 v[140:143], v227, s[56:57] sc1\n\t"
      "global_load_dwordx4 v[144:147], v228, s[56:57] sc1\n\t"
      "global_load_dwordx4 v[148:151], v229, s[56:57] sc1\n\t"
      "global_load_dwordx4 v[152:155], v230, s[56:57] sc1\n\t"
      "global_load_dwordx4 v[128:131], v224, s[58:59] sc1\n\t"
      "global_load_dwordx4 v[132:135], v225, s[58:59] sc1\n\t"
      "s_waitcnt vmcnt(5)\n\t"
      "v_max3_u32 v234, v140, v141, v142\n\t"
      "v_max_u32_e32 v234, v234, v143\n\t"
      "v_cmp_eq_u32_e32 vcc, -1, v234\n\t"
      "s_cbranch_vccnz FP17L_%=\n\t"
      "FP17X_%=:\n\t"
      "s_waitcnt vmcnt(0)\n\t"
      "s_branch FP17B_%=\n\t"
      "FP18R_%=:\n\t"
      "s_mov_b32 s51, 0\n\t"
      "FP18L_%=:\n\t"
      "s_add_u32 s51, s51, 1\n\t"
      "s_cmp_lt_u32 s51, 262144\n\t"
      "s_cbranch_scc0 FP18X_%=\n\t"
      "s_waitcnt vmcnt(0)\n\t"
      "s_sleep 4\n\t"
      "global_load_dwordx4 v[144:147], v228, s[56:57] sc1\n\t"
      "global_load_dwordx4 v[148:151], v229, s[56:57] sc1\n\t"
      "global_load_dwordx4 v[152:155], v230, s[56:57] sc1\n\t"
      "global_load_dwordx4 v[128:131], v224, s[58:59] sc1\n\t"
      "global_load_dwordx4 v[132:135], v225, s[58:59] sc1\n\t"
      "global_load_dwordx4 v[136:139], v226, s[58:59] sc1\n\t"
      "s_waitcnt vmcnt(5)\n\t"
      "v_max3_u32 v234, v144, v145, v146\n\t"
      "v_max_u32_e32 v234, v234, v147\n\t"
      "v_cmp_eq_u32_e32 vcc, -1, v234\n\t"
      "s_cbranch_vccnz FP18L_%=\n\t"
      "FP18X_%=:\n\t"
      "s_waitcnt vmcnt(0)\n\t"
      "s_branch FP18B_%=\n\t"
      "FP19R_%=:\n\t"
      "s_mov_b32 s51, 0\n\t"
      "FP19L_%=:\n\t"
      "s_add_u32 s51, s51, 1\n\t"
      "s_cmp_lt_u32 s51, 262144\n\t"
      "s_cbranch_scc0 FP19X_%=\n\t"
      "s_waitcnt vmcnt(0)\n\t"
      "s_sleep 4\n\t"
      "global_load_dwordx4 v[148:151], v229, s[56:57] sc1\n\t"
      "global_load_dwordx4 v[152:155], v230, s[56:57] sc1\n\t"
      "global_load_dwordx4 v[128:131], v224, s[58:59] sc1\n\t"
      "global_load_dwordx4 v[132:135], v225, s[58:59] sc1\n\t"
      "global_load_dwordx4 v[136:139], v226, s[58:59] sc1\n\t"
      "global_load_dwordx4 v[140:143], v227, s[58:59] sc1\n\t"
      "s_waitcnt vmcnt(5)\n\t"
      "v_max3_u32 v234, v148, v149, v150\n\t"
      "v_max_u32_e32 v234, v234, v151\n\t"
      "v_cmp_eq_u32_e32 vcc, -1, v234\n\t"
      "s_cbranch_vccnz FP19L_%=\n\t"
      "FP19X_%=:\n\t"
      "s_branch FP19B_%=\n\t"
      "FP20R_%=:\n\t"
      "s_mov_b32 s51, 0\n\t"
      "FP20L_%=:\n\t"
      "s_add_u32 s51, s51, 1\n\t"
      "s_cmp_lt_u32 s51, 262144\n\t"
      "s_cbranch_scc0 FP20X_%=\n\t"
      "s_waitcnt vmcnt(0)\n\t"
      "s_sleep 4\n\t"
      "global_load_dwordx4 v[152:155], v230, s[56:57] sc1\n\t"
      "global_load_dwordx4 v[128:131], v224, s[58:59] sc1\n\t"
      "global_load_dwordx4 v[132:135], v225, s[58:59] sc1\n\t"
      "global_load_dwordx4 v[136:139], v226, s[58:59] sc1\n\t"
      "global_load_dwordx4 v[140:143], v227, s[58:59] sc1\n\t"
      "global_load_dwordx4 v[144:147], v228, s[58:59] sc1\n\t"
      "s_waitcnt vmcnt(5)\n\t"
      "v_max3_u32 v234, v152, v153, v154\n\t"
      "v_max_u32_e32 v234, v234, v155\n\t"
      "v_cmp_eq_u32_e32 vcc, -1, v234\n\t"
      "s_cbranch_vccnz FP20L_%=\n\t"
      "FP20X_%=:\n\t"
      "s_waitcnt vmcnt(0)\n\t"
      "s_branch FP20B_%=\n\t"
      "FP21R_%=:\n\t"
      "s_mov_b32 s51, 0\n\t"
      "FP21L_%=:\n\t"
      "s_add_u32 s51, s51, 1\n\t"
      "s_cmp_lt_u32 s51, 262144\n\t"
      "s_cbranch_scc0 FP21X_%=\n\t"
      "s_waitcnt vmcnt(0)\n\t"
      "s_sleep 4\n\t"
      "global_load_dwordx4 v[128:131], v224, s[58:59] sc1\n\t"
      "global_load_dwordx4 v[132:135], v225, s[58:59] sc1\n\t"
      "global_load_dwordx4 v[136:139], v226, s[58:59] sc1\n\t"
      "global_load_dwordx4 v[140:143], v227, s[58:59] sc1\n\t"
      "global_load_dwordx4 v[144:147], v228, s[58:59] sc1\n\t"
      "global_load_dwordx4 v[148:151], v229, s[58:59] sc1\n\t"
      "s_waitcnt vmcnt(5)\n\t"
      "v_max3_u32 v234, v128, v129, v130\n\t"
      "v_max_u32_e32 v234, v234, v131\n\t"
      "v_cmp_eq_u32_e32 vcc, -1, v234\n\t"
      "s_cbranch_vccnz FP21L_%=\n\t"
      "FP21X_%=:\n\t"
      "s_waitcnt vmcnt(0)\n\t"
      "s_branch FP21B_%=\n\t"
      "NP2R_%=:\n\t"
      "s_mov_b32 s51, 0\n\t"
      "NP2L_%=:\n\t"
      "s_add_u32 s51, s51, 1\n\t"
      "s_cmp_lt_u32 s51, 262144\n\t"
      "s_cbranch_scc0 NP2X_%=\n\t"
      "s_waitcnt vmcnt(0)\n\t"
      "s_sleep 1\n\t"
      "global_load_dwordx4 v[128:131], v224, s[56:57] sc1\n\t"
      "global_load_dwordx4 v[132:135], v225, s[56:57] sc1\n\t"
      "global_load_dwordx4 v[136:139], v226, s[56:57] sc1\n\t"
      "global_load_dwordx4 v[140:143], v227, s[56:57] sc1\n\t"
      "global_load_dwordx4 v[144:147], v228, s[56:57] sc1\n\t"
      "global_load_dwordx4 v[148:151], v229, s[56:57] sc1\n\t"
      "global_load_dwordx4 v[152:155], v230, s[56:57] sc1\n\t"
      "s_waitcnt vmcnt(0)\n\t"
      "v_max3_u32 v234, v128, v129, v130\n\t"
      "v_max3_u32 v234, v234, v131, v132\n\t"
      "v_max3_u32 v234, v234, v133, v134\n\t"
      "v_max3_u32 v234, v234, v135, v136\n\t"
      "v_max3_u32 v234, v234, v137, v138\n\t"
      "v_max3_u32 v234, v234, v139, v140\n\t"
      "v_max3_u32 v234, v234, v141, v142\n\t"
      "v_max3_u32 v234, v234, v143, v144\n\t"
      "v_max3_u32 v234, v234, v145, v146\n\t"
      "v_max3_u32 v234, v234, v147, v148\n\t"
      "v_max3_u32 v234, v234, v149, v150\n\t"
      "v_max3_u32 v234, v234, v151, v152\n\t"
      "v_max3_u32 v234, v234, v153, v154\n\t"
      "v_max_u32_e32 v234, v234, v155\n\t"
      "v_cmp_eq_u32_e32 vcc, -1, v234\n\t"
      "s_cbranch_vccnz NP2L_%=\n\t"
      "NP2X_%=:\n\t"
      "v_mfma_f32_16x16x32_bf16 v[184:187], v[128:131], v[0:3], v[164:167]\n\t"
      "v_mfma_f32_16x16x32_bf16 v[188:191], v[128:131], v[4:7], 0\n\t"
      "v_mfma_f32_16x16x32_bf16 v[192:195], v[128:131], v[8:11], 0\n\t"
      "v_mfma_f32_16x16x32_bf16 v[196:199], v[128:131], v[12:15], 0\n\t"
      "v_mfma_f32_16x16x32_bf16 v[184:187], v[132:135], v[16:19], v[184:187]\n\t"
      "v_mfma_f32_16x16x32_bf16 v[188:191], v[132:135], v[20:23], v[188:191]\n\t"
      "v_mfma_f32_16x16x32_bf16 v[192:195], v[132:135], v[24:27], v[192:195]\n\t"
      "v_mfma_f32_16x16x32_bf16 v[196:199], v[132:135], v[28:31], v[196:199]\n\t"
      "v_mfma_f32_16x16x32_bf16 v[184:187], v[136:139], v[32:35], v[184:187]\n\t"
      "v_mfma_f32_16x16x32_bf16 v[188:191], v[136:139], v[36:39], v[188:191]\n\t"
      "v_mfma_f32_16x16x32_bf16 v[192:195], v[136:139], v[40:43], v[192:195]\n\t"
      "v_mfma_f32_16x16x32_bf16 v[196:199], v[136:139], v[44:47], v[196:199]\n\t"
      "v_mfma_f32_16x16x32_bf16 v[184:187], v[140:143], v[48:51], v[184:187]\n\t"
      "v_mfma_f32_16x16x32_bf16 v[188:191], v[140:143], v[52:55], v[188:191]\n\t"
      "v_mfma_f32_16x16x32_bf16 v[192:195], v[140:143], v[56:59], v[192:195]\n\t"
      "v_mfma_f32_16x16x32_bf16 v[196:199], v[140:143], v[60:63], v[196:199]\n\t"
      "v_mfma_f32_16x16x32_bf16 v[184:187], v[144:147], v[64:67], v[184:187]\n\t"
      "v_mfma_f32_16x16x32_bf16 v[188:191], v[144:147], v[68:71], v[188:191]\n\t"
      "v_mfma_f32_16x16x32_bf16 v[192:195], v[144:147], v[72:75], v[192:195]\n\t"
      "v_mfma_f32_16x16x32_bf16 v[196:199], v[144:147], v[76:79], v[196:199]\n\t"
      "v_mfma_f32_16x16x32_bf16 v[184:187], v[148:151], v[80:83], v[184:187]\n\t"
      "v_mfma_f32_16x16x32_bf16 v[188:191], v[148:151], v[84:87], v[188:191]\n\t"
      "v_mfma_f32_16x16x32_bf16 v[192:195], v[148:151], v[88:91], v[192:195]\n\t"
      "v_mfma_f32_16x16x32_bf16 v[196:199], v[148:151], v[92:95], v[196:199]\n\t"
      "v_mfma_f32_16x16x32_bf16 v[184:187], v[152:155], v[96:99], v[184:187]\n\t"
      "v_mfma_f32_16x16x32_bf16 v[188:191], v[152:155], v[100:103], v[188:191]\n\t"
      "v_mfma_f32_16x16x32_bf16 v[192:195], v[152:155], v[104:107], v[192:195]\n\t"
      "v_mfma_f32_16x16x32_bf16 v[196:199], v[152:155], v[108:111], v[196:199]\n\t"
      "global_load_dwordx4 v[128:131], v224, s[58:59] sc1\n\t"
      "global_load_dwordx4 v[132:135], v225, s[58:59] sc1\n\t"
      "global_load_dwordx4 v[136:139], v226, s[58:59] sc1\n\t"
      "global_load_dwordx4 v[140:143], v227, s[58:59] sc1\n\t"
      "global_load_dwordx4 v[144:147], v228, s[58:59] sc1\n\t"
      "global_load_dwordx4 v[148:151], v229, s[58:59] sc1\n\t"
      "global_load_dwordx4 v[152:155], v230, s[58:59] sc1\n\t"
      "s_waitcnt vmcnt(0)\n\t"
      "s_nop 7\n\t"
      "s_branch NP2B_%=\n\t"
      "FP22R_%=:\n\t"
      "s_mov_b32 s51, 0\n\t"
      "FP22L_%=:\n\t"
      "s_add_u32 s51, s51, 1\n\t"
      "s_cmp_lt_u32 s51, 262144\n\t"
      "s_cbranch_scc0 FP22X_%=\n\t"
      "s_waitcnt vmcnt(0)\n\t"
      "s_sleep 4\n\t"
      "global_load_dwordx4 v[132:135], v225, s[58:59] sc1\n\t"
      "global_load_dwordx4 v[136:139], v226, s[58:59] sc1\n\t"
      "global_load_dwordx4 v[140:143], v227, s[58:59] sc1\n\t"
      "global_load_dwordx4 v[144:147], v228, s[58:59] sc1\n\t"
      "global_load_dwordx4 v[148:151], v229, s[58:59] sc1\n\t"
      "global_load_dwordx4 v[152:155], v230, s[58:59] sc1\n\t"
      "s_waitcnt vmcnt(5)\n\t"
      "v_max3_u32 v234, v132, v133, v134\n\t"
      "v_max_u32_e32 v234, v234, v135\n\t"
      "v_cmp_eq_u32_e32 vcc, -1, v234\n\t"
      "s_cbranch_vccnz FP22L_%=\n\t"
      "FP22X_%=:\n\t"
      "s_waitcnt vmcnt(0)\n\t"
      "s_branch FP22B_%=\n\t"
      "FP23R_%=:\n\t"
      "s_mov_b32 s51, 0\n\t"
      "FP23L_%=:\n\t"
      "s_add_u32 s51, s51, 1\n\t"
      "s_cmp_lt_u32 s51, 262144\n\t"
      "s_cbranch_scc0 FP23X_%=\n\t"
      "s_waitcnt vmcnt(0)\n\t"
      "s_sleep 4\n\t"
      "global_load_dwordx4 v[136:139], v226, s[58:59] sc1\n\t"
      "global_load_dwordx4 v[140:143], v227, s[58:59] sc1\n\t"
      "global_load_dwordx4 v[144:147], v228, s[58:59] sc1\n\t"
      "global_load_dwordx4 v[148:151], v229, s[58:59] sc1\n\t"
      "global_load_dwordx4 v[152:155], v230, s[58:59] sc1\n\t"
      "global_load_dwordx4 v[128:131], v224, s[60:61] sc1\n\t"
      "s_waitcnt vmcnt(5)\n\t"
      "v_max3_u32 v234, v136, v137, v138\n\t"
      "v_max_u32_e32 v234, v234, v139\n\t"
      "v_cmp_eq_u32_e32 vcc, -1, v234\n\t"
      "s_cbranch_vccnz FP23L_%=\n\t"
      "FP23X_%=:\n\t"
      "s_waitcnt vmcnt(0)\n\t"
      "s_branch FP23B_%=\n\t"
      "FP24R_%=:\n\t"
      "s_mov_b32 s51, 0\n\t"
      "FP24L_%=:\n\t"
      "s_add_u32 s51, s51, 1\n\t"
      "s_cmp_lt_u32 s51, 262144\n\t"
      "s_cbranch_scc0 FP24X_%=\n\t"
      "s_waitcnt vmcnt(0)\n\t"
      "s_sleep 4\n\t"
      "global_load_dwordx4 v[140:143], v227, s[58:59] sc1\n\t"
      "global_load_dwordx4 v[144:147], v228, s[58:59] sc1\n\t"
      "global_load_dwordx4 v[148:151], v229, s[58:59] sc1\n\t"
      "global_load_dwordx4 v[152:155], v230, s[58:59] sc1\n\t"
      "global_load_dwordx4 v[128:131], v224, s[60:61] sc1\n\t"
      "global_load_dwordx4 v[132:135], v225, s[60:61] sc1\n\t"
      "s_waitcnt vmcnt(5)\n\t"
      "v_max3_u32 v234, v140, v141, v142\n\t"
      "v_max_u32_e32 v234, v234, v143\n\t"
      "v_cmp_eq_u32_e32 vcc, -1, v234\n\t"
      "s_cbranch_vccnz FP24L_%=\n\t"
      "FP24X_%=:\n\t"
      "s_waitcnt vmcnt(0)\n\t"
      "s_branch FP24B_%=\n\t"
      "FP25R_%=:\n\t"
      "s_mov_b32 s51, 0\n\t"
      "FP25L_%=:\n\t"
      "s_add_u32 s51, s51, 1\n\t"
      "s_cmp_lt_u32 s51, 262144\n\t"
      "s_cbranch_scc0 FP25X_%=\n\t"
      "s_waitcnt vmcnt(0)\n\t"
      "s_sleep 4\n\t"
      "global_load_dwordx4 v[144:147], v228, s[58:59] sc1\n\t"
      "global_load_dwordx4 v[148:151], v229, s[58:59] sc1\n\t"
      "global_load_dwordx4 v[152:155], v230, s[58:59] sc1\n\t"
      "global_load_dwordx4 v[128:131], v224, s[60:61] sc1\n\t"
      "global_load_dwordx4 v[132:135], v225, s[60:61] sc1\n\t"
      "global_load_dwordx4 v[136:139], v226, s[60:61] sc1\n\t"
      "s_waitcnt vmcnt(5)\n\t"
      "v_max3_u32 v234, v144, v145, v146\n\t"
      "v_max_u32_e32 v234, v234, v147\n\t"
      "v_cmp_eq_u32_e32 vcc, -1, v234\n\t"
      "s_cbranch_vccnz FP25L_%=\n\t"
      "FP25X_%=:\n\t"
      "s_waitcnt vmcnt(0)\n\t"
      "s_branch FP25B_%=\n\t"
      "FP26R_%=:\n\t"
      "s_mov_b32 s51, 0\n\t"
      "FP26L_%=:\n\t"
      "s_add_u32 s51, s51, 1\n\t"
      "s_cmp_lt_u32 s51, 262144\n\t"
      "s_cbranch_scc0 FP26X_%=\n\t"
      "s_waitcnt vmcnt(0)\n\t"
      "s_sleep 4\n\t"
      "global_load_dwordx4 v[148:151], v229, s[58:59] sc1\n\t"
      "global_load_dwordx4 v[152:155], v230, s[58:59] sc1\n\t"
      "global_load_dwordx4 v[128:131], v224, s[60:61] sc1\n\t"
      "global_load_dwordx4 v[132:135], v225, s[60:61] sc1\n\t"
      "global_load_dwordx4 v[136:139], v226, s[60:61] sc1\n\t"
      "global_load_dwordx4 v[140:143], v227, s[60:61] sc1\n\t"
      "s_waitcnt vmcnt(5)\n\t"
      "v_max3_u32 v234, v148, v149, v150\n\t"
      "v_max_u32_e32 v234, v234, v151\n\t"
      "v_cmp_eq_u32_e32 vcc, -1, v234\n\t"
      "s_cbranch_vccnz FP26L_%=\n\t"
      "FP26X_%=:\n\t"
      "s_branch FP26B_%=\n\t"
      "FP27R_%=:\n\t"
      "s_mov_b32 s51, 0\n\t"
      "FP27L_%=:\n\t"
      "s_add_u32 s51, s51, 1\n\t"
      "s_cmp_lt_u32 s51, 262144\n\t"
      "s_cbranch_scc0 FP27X_%=\n\t"
      "s_waitcnt vmcnt(0)\n\t"
      "s_sleep 4\n\t"
      "global_load_dwordx4 v[152:155], v230, s[58:59] sc1\n\t"
      "global_load_dwordx4 v[128:131], v224, s[60:61] sc1\n\t"
      "global_load_dwordx4 v[132:135], v225, s[60:61] sc1\n\t"
      "global_load_dwordx4 v[136:139], v226, s[60:61] sc1\n\t"
      "global_load_dwordx4 v[140:143], v227, s[60:61] sc1\n\t"
      "global_load_dwordx4 v[144:147], v228, s[60:61] sc1\n\t"
      "s_waitcnt vmcnt(5)\n\t"
      "v_max3_u32 v234, v152, v153, v154\n\t"
      "v_max_u32_e32 v234, v234, v155\n\t"
      "v_cmp_eq_u32_e32 vcc, -1, v234\n\t"
      "s_cbranch_vccnz FP27L_%=\n\t"
      "FP27X_%=:\n\t"
      "s_waitcnt vmcnt(0)\n\t"
      "s_branch FP27B_%=\n\t"
      "FP28R_%=:\n\t"
      "s_mov_b32 s51, 0\n\t"
      "FP28L_%=:\n\t"
      "s_add_u32 s51, s51, 1\n\t"
      "s_cmp_lt_u32 s51, 262144\n\t"
      "s_cbranch_scc0 FP28X_%=\n\t"
      "s_waitcnt vmcnt(0)\n\t"
      "s_sleep 4\n\t"
      "global_load_dwordx4 v[128:131], v224, s[60:61] sc1\n\t"
      "global_load_dwordx4 v[132:135], v225, s[60:61] sc1\n\t"
      "global_load_dwordx4 v[136:139], v226, s[60:61] sc1\n\t"
      "global_load_dwordx4 v[140:143], v227, s[60:61] sc1\n\t"
      "global_load_dwordx4 v[144:147], v228, s[60:61] sc1\n\t"
      "global_load_dwordx4 v[148:151], v229, s[60:61] sc1\n\t"
      "s_waitcnt vmcnt(5)\n\t"
      "v_max3_u32 v234, v128, v129, v130\n\t"
      "v_max_u32_e32 v234, v234, v131\n\t"
      "v_cmp_eq_u32_e32 vcc, -1, v234\n\t"
      "s_cbranch_vccnz FP28L_%=\n\t"
      "FP28X_%=:\n\t"
      "s_waitcnt vmcnt(0)\n\t"
      "s_branch FP28B_%=\n\t"
      "NP3R_%=:\n\t"
      "s_mov_b32 s51, 0\n\t"
      "NP3L_%=:\n\t"
      "s_add_u32 s51, s51, 1\n\t"
      "s_cmp_lt_u32 s51, 262144\n\t"
      "s_cbranch_scc0 NP3X_%=\n\t"
      "s_waitcnt vmcnt(0)\n\t"
      "s_sleep 1\n\t"
      "global_load_dwordx4 v[128:131], v224, s[58:59] sc1\n\t"
      "global_load_dwordx4 v[132:135], v225, s[58:59] sc1\n\t"
      "global_load_dwordx4 v[136:139], v226, s[58:59] sc1\n\t"
      "global_load_dwordx4 v[140:143], v227, s[58:59] sc1\n\t"
      "global_load_dwordx4 v[144:147], v228, s[58:59] sc1\n\t"
      "global_load_dwordx4 v[148:151], v229, s[58:59] sc1\n\t"
      "global_load_dwordx4 v[152:155], v230, s[58:59] sc1\n\t"
      "s_waitcnt vmcnt(0)\n\t"
      "v_max3_u32 v234, v128, v129, v130\n\t"
      "v_max3_u32 v234, v234, v131, v132\n\t"
      "v_max3_u32 v234, v234, v133, v134\n\t"
      "v_max3_u32 v234, v234, v135, v136\n\t"
      "v_max3_u32 v234, v234, v137, v138\n\t"
      "v_max3_u32 v234, v234, v139, v140\n\t"
      "v_max3_u32 v234, v234, v141, v142\n\t"
      "v_max3_u32 v234, v234, v143, v144\n\t"
      "v_max3_u32 v234, v234, v145, v146\n\t"
      "v_max3_u32 v234, v234, v147, v148\n\t"
      "v_max3_u32 v234, v234, v149, v150\n\t"
      "v_max3_u32 v234, v234, v151, v152\n\t"
      "v_max3_u32 v234, v234, v153, v154\n\t"
      "v_max_u32_e32 v234, v234, v155\n\t"
      "v_cmp_eq_u32_e32 vcc, -1, v234\n\t"
      "s_cbranch_vccnz NP3L_%=\n\t"
      "NP3X_%=:\n\t"
      "v_mfma_f32_16x16x32_bf16 v[200:203], v[128:131], v[0:3], v[168:171]\n\t"
      "v_mfma_f32_16x16x32_bf16 v[204:207], v[128:131], v[4:7], 0\n\t"
      "v_mfma_f32_16x16x32_bf16 v[208:211], v[128:131], v[8:11], 0\n\t"
      "v_mfma_f32_16x16x32_bf16 v[212:215], v[128:131], v[12:15], 0\n\t"
      "v_mfma_f32_16x16x32_bf16 v[200:203], v[132:135], v[16:19], v[200:203]\n\t"
      "v_mfma_f32_16x16x32_bf16 v[204:207], v[132:135], v[20:23], v[204:207]\n\t"
      "v_mfma_f32_16x16x32_bf16 v[208:211], v[132:135], v[24:27], v[208:211]\n\t"
      "v_mfma_f32_16x16x32_bf16 v[212:215], v[132:135], v[28:31], v[212:215]\n\t"
      "v_mfma_f32_16x16x32_bf16 v[200:203], v[136:139], v[32:35], v[200:203]\n\t"
      "v_mfma_f32_16x16x32_bf16 v[204:207], v[136:139], v[36:39], v[204:207]\n\t"
      "v_mfma_f32_16x16x32_bf16 v[208:211], v[136:139], v[40:43], v[208:211]\n\t"
      "v_mfma_f32_16x16x32_bf16 v[212:215], v[136:139], v[44:47], v[212:215]\n\t"
      "v_mfma_f32_16x16x32_bf16 v[200:203], v[140:143], v[48:51], v[200:203]\n\t"
      "v_mfma_f32_16x16x32_bf16 v[204:207], v[140:143], v[52:55], v[204:207]\n\t"
      "v_mfma_f32_16x16x32_bf16 v[208:211], v[140:143], v[56:59], v[208:211]\n\t"
      "v_mfma_f32_16x16x32_bf16 v[212:215], v[140:143], v[60:63], v[212:215]\n\t"
      "v_mfma_f32_16x16x32_bf16 v[200:203], v[144:147], v[64:67], v[200:203]\n\t"
      "v_mfma_f32_16x16x32_bf16 v[204:207], v[144:147], v[68:71], v[204:207]\n\t"
      "v_mfma_f32_16x16x32_bf16 v[208:211], v[144:147], v[72:75], v[208:211]\n\t"
      "v_mfma_f32_16x16x32_bf16 v[212:215], v[144:147], v[76:79], v[212:215]\n\t"
      "v_mfma_f32_16x16x32_bf16 v[200:203], v[148:151], v[80:83], v[200:203]\n\t"
      "v_mfma_f32_16x16x32_bf16 v[204:207], v[148:151], v[84:87], v[204:207]\n\t"
      "v_mfma_f32_16x16x32_bf16 v[208:211], v[148:151], v[88:91], v[208:211]\n\t"
      "v_mfma_f32_16x16x32_bf16 v[212:215], v[148:151], v[92:95], v[212:215]\n\t"
      "v_mfma_f32_16x16x32_bf16 v[200:203], v[152:155], v[96:99], v[200:203]\n\t"
      "v_mfma_f32_16x16x32_bf16 v[204:207], v[152:155], v[100:103], v[204:207]\n\t"
      "v_mfma_f32_16x16x32_bf16 v[208:211], v[152:155], v[104:107], v[208:211]\n\t"
      "v_mfma_f32_16x16x32_bf16 v[212:215], v[152:155], v[108:111], v[212:215]\n\t"
      "global_load_dwordx4 v[128:131], v224, s[60:61] sc1\n\t"
      "global_load_dwordx4 v[132:135], v225, s[60:61] sc1\n\t"
      "global_load_dwordx4 v[136:139], v226, s[60:61] sc1\n\t"
      "global_load_dwordx4 v[140:143], v227, s[60:61] sc1\n\t"
      "global_load_dwordx4 v[144:147], v228, s[60:61] sc1\n\t"
      "global_load_dwordx4 v[148:151], v229, s[60:61] sc1\n\t"
      "global_load_dwordx4 v[152:155], v230, s[60:61] sc1\n\t"
      "s_waitcnt vmcnt(0)\n\t"
      "s_nop 7\n\t"
      "s_branch NP3B_%=\n\t"
      "FP29R_%=:\n\t"
      "s_mov_b32 s51, 0\n\t"
      "FP29L_%=:\n\t"
      "s_add_u32 s51, s51, 1\n\t"
      "s_cmp_lt_u32 s51, 262144\n\t"
      "s_cbranch_scc0 FP29X_%=\n\t"
      "s_waitcnt vmcnt(0)\n\t"
      "s_sleep 4\n\t"
      "global_load_dwordx4 v[132:135], v225, s[60:61] sc1\n\t"
      "global_load_dwordx4 v[136:139], v226, s[60:61] sc1\n\t"
      "global_load_dwordx4 v[140:143], v227, s[60:61] sc1\n\t"
      "global_load_dwordx4 v[144:147], v228, s[60:61] sc1\n\t"
      "global_load_dwordx4 v[148:151], v229, s[60:61] sc1\n\t"
      "global_load_dwordx4 v[152:155], v230, s[60:61] sc1\n\t"
      "s_waitcnt vmcnt(5)\n\t"
      "v_max3_u32 v234, v132, v133, v134\n\t"
      "v_max_u32_e32 v234, v234, v135\n\t"
      "v_cmp_eq_u32_e32 vcc, -1, v234\n\t"
      "s_cbranch_vccnz FP29L_%=\n\t"
      "FP29X_%=:\n\t"
      "s_waitcnt vmcnt(0)\n\t"
      "s_branch FP29B_%=\n\t"
      "FP30R_%=:\n\t"
      "s_mov_b32 s51, 0\n\t"
      "FP30L_%=:\n\t"
      "s_add_u32 s51, s51, 1\n\t"
      "s_cmp_lt_u32 s51, 262144\n\t"
      "s_cbranch_scc0 FP30X_%=\n\t"
      "s_waitcnt vmcnt(0)\n\t"
      "s_sleep 4\n\t"
      "global_load_dwordx4 v[136:139], v226, s[60:61] sc1\n\t"
      "global_load_dwordx4 v[140:143], v227, s[60:61] sc1\n\t"
      "global_load_dwordx4 v[144:147], v228, s[60:61] sc1\n\t"
      "global_load_dwordx4 v[148:151], v229, s[60:61] sc1\n\t"
      "global_load_dwordx4 v[152:155], v230, s[60:61] sc1\n\t"
      "global_load_dwordx4 v[128:131], v224, s[62:63] sc1\n\t"
      "s_waitcnt vmcnt(5)\n\t"
      "v_max3_u32 v234, v136, v137, v138\n\t"
      "v_max_u32_e32 v234, v234, v139\n\t"
      "v_cmp_eq_u32_e32 vcc, -1, v234\n\t"
      "s_cbranch_vccnz FP30L_%=\n\t"
      "FP30X_%=:\n\t"
      "s_waitcnt vmcnt(0)\n\t"
      "s_branch FP30B_%=\n\t"
      "FP31R_%=:\n\t"
      "s_mov_b32 s51, 0\n\t"
      "FP31L_%=:\n\t"
      "s_add_u32 s51, s51, 1\n\t"
      "s_cmp_lt_u32 s51, 262144\n\t"
      "s_cbranch_scc0 FP31X_%=\n\t"
      "s_waitcnt vmcnt(0)\n\t"
      "s_sleep 4\n\t"
      "global_load_dwordx4 v[140:143], v227, s[60:61] sc1\n\t"
      "global_load_dwordx4 v[144:147], v228, s[60:61] sc1\n\t"
      "global_load_dwordx4 v[148:151], v229, s[60:61] sc1\n\t"
      "global_load_dwordx4 v[152:155], v230, s[60:61] sc1\n\t"
      "global_load_dwordx4 v[128:131], v224, s[62:63] sc1\n\t"
      "global_load_dwordx4 v[132:135], v225, s[62:63] sc1\n\t"
      "s_waitcnt vmcnt(5)\n\t"
      "v_max3_u32 v234, v140, v141, v142\n\t"
      "v_max_u32_e32 v234, v234, v143\n\t"
      "v_cmp_eq_u32_e32 vcc, -1, v234\n\t"
      "s_cbranch_vccnz FP31L_%=\n\t"
      "FP31X_%=:\n\t"
      "s_waitcnt vmcnt(0)\n\t"
      "s_branch FP31B_%=\n\t"
      "FP32R_%=:\n\t"
      "s_mov_b32 s51, 0\n\t"
      "FP32L_%=:\n\t"
      "s_add_u32 s51, s51, 1\n\t"
      "s_cmp_lt_u32 s51, 262144\n\t"
      "s_cbranch_scc0 FP32X_%=\n\t"
      "s_waitcnt vmcnt(0)\n\t"
      "s_sleep 4\n\t"
      "global_load_dwordx4 v[144:147], v228, s[60:61] sc1\n\t"
      "global_load_dwordx4 v[148:151], v229, s[60:61] sc1\n\t"
      "global_load_dwordx4 v[152:155], v230, s[60:61] sc1\n\t"
      "global_load_dwordx4 v[128:131], v224, s[62:63] sc1\n\t"
      "global_load_dwordx4 v[132:135], v225, s[62:63] sc1\n\t"
      "global_load_dwordx4 v[136:139], v226, s[62:63] sc1\n\t"
      "s_waitcnt vmcnt(5)\n\t"
      "v_max3_u32 v234, v144, v145, v146\n\t"
      "v_max_u32_e32 v234, v234, v147\n\t"
      "v_cmp_eq_u32_e32 vcc, -1, v234\n\t"
      "s_cbranch_vccnz FP32L_%=\n\t"
      "FP32X_%=:\n\t"
      "s_waitcnt vmcnt(0)\n\t"
      "s_branch FP32B_%=\n\t"
      "FP33R_%=:\n\t"
      "s_mov_b32 s51, 0\n\t"
      "FP33L_%=:\n\t"
      "s_add_u32 s51, s51, 1\n\t"
      "s_cmp_lt_u32 s51, 262144\n\t"
      "s_cbranch_scc0 FP33X_%=\n\t"
      "s_waitcnt vmcnt(0)\n\t"
      "s_sleep 4\n\t"
      "global_load_dwordx4 v[148:151], v229, s[60:61] sc1\n\t"
      "global_load_dwordx4 v[152:155], v230, s[60:61] sc1\n\t"
      "global_load_dwordx4 v[128:131], v224, s[62:63] sc1\n\t"
      "global_load_dwordx4 v[132:135], v225, s[62:63] sc1\n\t"
      "global_load_dwordx4 v[136:139], v226, s[62:63] sc1\n\t"
      "global_load_dwordx4 v[140:143], v227, s[62:63] sc1\n\t"
      "s_waitcnt vmcnt(5)\n\t"
      "v_max3_u32 v234, v148, v149, v150\n\t"
      "v_max_u32_e32 v234, v234, v151\n\t"
      "v_cmp_eq_u32_e32 vcc, -1, v234\n\t"
      "s_cbranch_vccnz FP33L_%=\n\t"
      "FP33X_%=:\n\t"
      "s_branch FP33B_%=\n\t"
      "FP34R_%=:\n\t"
      "s_mov_b32 s51, 0\n\t"
      "FP34L_%=:\n\t"
      "s_add_u32 s51, s51, 1\n\t"
      "s_cmp_lt_u32 s51, 262144\n\t"
      "s_cbranch_scc0 FP34X_%=\n\t"
      "s_waitcnt vmcnt(0)\n\t"
      "s_sleep 4\n\t"
      "global_load_dwordx4 v[152:155], v230, s[60:61] sc1\n\t"
      "global_load_dwordx4 v[128:131], v224, s[62:63] sc1\n\t"
      "global_load_dwordx4 v[132:135], v225, s[62:63] sc1\n\t"
      "global_load_dwordx4 v[136:139], v226, s[62:63] sc1\n\t"
      "global_load_dwordx4 v[140:143], v227, s[62:63] sc1\n\t"
      "global_load_dwordx4 v[144:147], v228, s[62:63] sc1\n\t"
      "s_waitcnt vmcnt(5)\n\t"
      "v_max3_u32 v234, v152, v153, v154\n\t"
      "v_max_u32_e32 v234, v234, v155\n\t"
      "v_cmp_eq_u32_e32 vcc, -1, v234\n\t"
      "s_cbranch_vccnz FP34L_%=\n\t"
      "FP34X_%=:\n\t"
      "s_waitcnt vmcnt(0)\n\t"
      "s_branch FP34B_%=\n\t"
      "FP35R_%=:\n\t"
      "s_mov_b32 s51, 0\n\t"
      "FP35L_%=:\n\t"
      "s_add_u32 s51, s51, 1\n\t"
      "s_cmp_lt_u32 s51, 262144\n\t"
      "s_cbranch_scc0 FP35X_%=\n\t"
      "s_waitcnt vmcnt(0)\n\t"
      "s_sleep 4\n\t"
      "global_load_dwordx4 v[128:131], v224, s[62:63] sc1\n\t"
      "global_load_dwordx4 v[132:135], v225, s[62:63] sc1\n\t"
      "global_load_dwordx4 v[136:139], v226, s[62:63] sc1\n\t"
      "global_load_dwordx4 v[140:143], v227, s[62:63] sc1\n\t"
      "global_load_dwordx4 v[144:147], v228, s[62:63] sc1\n\t"
      "global_load_dwordx4 v[148:151], v229, s[62:63] sc1\n\t"
      "s_waitcnt vmcnt(5)\n\t"
      "v_max3_u32 v234, v128, v129, v130\n\t"
      "v_max_u32_e32 v234, v234, v131\n\t"
      "v_cmp_eq_u32_e32 vcc, -1, v234\n\t"
      "s_cbranch_vccnz FP35L_%=\n\t"
      "FP35X_%=:\n\t"
      "s_waitcnt vmcnt(0)\n\t"
      "s_branch FP35B_%=\n\t"
      "NP4R_%=:\n\t"
      "s_mov_b32 s51, 0\n\t"
      "NP4L_%=:\n\t"
      "s_add_u32 s51, s51, 1\n\t"
      "s_cmp_lt_u32 s51, 262144\n\t"
      "s_cbranch_scc0 NP4X_%=\n\t"
      "s_waitcnt vmcnt(0)\n\t"
      "s_sleep 1\n\t"
      "global_load_dwordx4 v[128:131], v224, s[60:61] sc1\n\t"
      "global_load_dwordx4 v[132:135], v225, s[60:61] sc1\n\t"
      "global_load_dwordx4 v[136:139], v226, s[60:61] sc1\n\t"
      "global_load_dwordx4 v[140:143], v227, s[60:61] sc1\n\t"
      "global_load_dwordx4 v[144:147], v228, s[60:61] sc1\n\t"
      "global_load_dwordx4 v[148:151], v229, s[60:61] sc1\n\t"
      "global_load_dwordx4 v[152:155], v230, s[60:61] sc1\n\t"
      "s_waitcnt vmcnt(0)\n\t"
      "v_max3_u32 v234, v128, v129, v130\n\t"
      "v_max3_u32 v234, v234, v131, v132\n\t"
      "v_max3_u32 v234, v234, v133, v134\n\t"
      "v_max3_u32 v234, v234, v135, v136\n\t"
      "v_max3_u32 v234, v234, v137, v138\n\t"
      "v_max3_u32 v234, v234, v139, v140\n\t"
      "v_max3_u32 v234, v234, v141, v142\n\t"
      "v_max3_u32 v234, v234, v143, v144\n\t"
      "v_max3_u32 v234, v234, v145, v146\n\t"
      "v_max3_u32 v234, v234, v147, v148\n\t"
      "v_max3_u32 v234, v234, v149, v150\n\t"
      "v_max3_u32 v234, v234, v151, v152\n\t"
      "v_max3_u32 v234, v234, v153, v154\n\t"
      "v_max_u32_e32 v234, v234, v155\n\t"
      "v_cmp_eq_u32_e32 vcc, -1, v234\n\t"
      "s_cbranch_vccnz NP4L_%=\n\t"
      "NP4X_%=:\n\t"
      "v_mfma_f32_16x16x32_bf16 v[184:187], v[128:131], v[0:3], v[172:175]\n\t"
      "v_mfma_f32_16x16x32_bf16 v[188:191], v[128:131], v[4:7], 0\n\t"
      "v_mfma_f32_16x16x32_bf16 v[192:195], v[128:131], v[8:11], 0\n\t"
      "v_mfma_f32_16x16x32_bf16 v[196:199], v[128:131], v[12:15], 0\n\t"
      "v_mfma_f32_16x16x32_bf16 v[184:187], v[132:135], v[16:19], v[184:187]\n\t"
      "v_mfma_f32_16x16x32_bf16 v[188:191], v[132:135], v[20:23], v[188:191]\n\t"
      "v_mfma_f32_16x16x32_bf16 v[192:195], v[132:135], v[24:27], v[192:195]\n\t"
      "v_mfma_f32_16x16x32_bf16 v[196:199], v[132:135], v[28:31], v[196:199]\n\t"
      "v_mfma_f32_16x16x32_bf16 v[184:187], v[136:139], v[32:35], v[184:187]\n\t"
      "v_mfma_f32_16x16x32_bf16 v[188:191], v[136:139], v[36:39], v[188:191]\n\t"
      "v_mfma_f32_16x16x32_bf16 v[192:195], v[136:139], v[40:43], v[192:195]\n\t"
      "v_mfma_f32_16x16x32_bf16 v[196:199], v[136:139], v[44:47], v[196:199]\n\t"
      "v_mfma_f32_16x16x32_bf16 v[184:187], v[140:143], v[48:51], v[184:187]\n\t"
      "v_mfma_f32_16x16x32_bf16 v[188:191], v[140:143], v[52:55], v[188:191]\n\t"
      "v_mfma_f32_16x16x32_bf16 v[192:195], v[140:143], v[56:59], v[192:195]\n\t"
      "v_mfma_f32_16x16x32_bf16 v[196:199], v[140:143], v[60:63], v[196:199]\n\t"
      "v_mfma_f32_16x16x32_bf16 v[184:187], v[144:147], v[64:67], v[184:187]\n\t"
      "v_mfma_f32_16x16x32_bf16 v[188:191], v[144:147], v[68:71], v[188:191]\n\t"
      "v_mfma_f32_16x16x32_bf16 v[192:195], v[144:147], v[72:75], v[192:195]\n\t"
      "v_mfma_f32_16x16x32_bf16 v[196:199], v[144:147], v[76:79], v[196:199]\n\t"
      "v_mfma_f32_16x16x32_bf16 v[184:187], v[148:151], v[80:83], v[184:187]\n\t"
      "v_mfma_f32_16x16x32_bf16 v[188:191], v[148:151], v[84:87], v[188:191]\n\t"
      "v_mfma_f32_16x16x32_bf16 v[192:195], v[148:151], v[88:91], v[192:195]\n\t"
      "v_mfma_f32_16x16x32_bf16 v[196:199], v[148:151], v[92:95], v[196:199]\n\t"
      "v_mfma_f32_16x16x32_bf16 v[184:187], v[152:155], v[96:99], v[184:187]\n\t"
      "v_mfma_f32_16x16x32_bf16 v[188:191], v[152:155], v[100:103], v[188:191]\n\t"
      "v_mfma_f32_16x16x32_bf16 v[192:195], v[152:155], v[104:107], v[192:195]\n\t"
      "v_mfma_f32_16x16x32_bf16 v[196:199], v[152:155], v[108:111], v[196:199]\n\t"
      "global_load_dwordx4 v[128:131], v224, s[62:63] sc1\n\t"
      "global_load_dwordx4 v[132:135], v225, s[62:63] sc1\n\t"
      "global_load_dwordx4 v[136:139], v226, s[62:63] sc1\n\t"
      "global_load_dwordx4 v[140:143], v227, s[62:63] sc1\n\t"
      "global_load_dwordx4 v[144:147], v228, s[62:63] sc1\n\t"
      "global_load_dwordx4 v[148:151], v229, s[62:63] sc1\n\t"
      "global_load_dwordx4 v[152:155], v230, s[62:63] sc1\n\t"
      "s_waitcnt vmcnt(0)\n\t"
      "s_nop 7\n\t"
      "s_branch NP4B_%=\n\t"
      "FP36R_%=:\n\t"
      "s_mov_b32 s51, 0\n\t"
      "FP36L_%=:\n\t"
      "s_add_u32 s51, s51, 1\n\t"
      "s_cmp_lt_u32 s51, 262144\n\t"
      "s_cbranch_scc0 FP36X_%=\n\t"
      "s_waitcnt vmcnt(0)\n\t"
      "s_sleep 4\n\t"
      "global_load_dwordx4 v[132:135], v225, s[62:63] sc1\n\t"
      "global_load_dwordx4 v[136:139], v226, s[62:63] sc1\n\t"
      "global_load_dwordx4 v[140:143], v227, s[62:63] sc1\n\t"
      "global_load_dwordx4 v[144:147], v228, s[62:63] sc1\n\t"
      "global_load_dwordx4 v[148:151], v229, s[62:63] sc1\n\t"
      "global_load_dwordx4 v[152:155], v230, s[62:63] sc1\n\t"
      "s_waitcnt vmcnt(5)\n\t"
      "v_max3_u32 v234, v132, v133, v134\n\t"
      "v_max_u32_e32 v234, v234, v135\n\t"
      "v_cmp_eq_u32_e32 vcc, -1, v234\n\t"
      "s_cbranch_vccnz FP36L_%=\n\t"
      "FP36X_%=:\n\t"
      "s_waitcnt vmcnt(0)\n\t"
      "s_branch FP36B_%=\n\t"
      "FP37R_%=:\n\t"
      "s_mov_b32 s51, 0\n\t"
      "FP37L_%=:\n\t"
      "s_add_u32 s51, s51, 1\n\t"
      "s_cmp_lt_u32 s51, 262144\n\t"
      "s_cbranch_scc0 FP37X_%=\n\t"
      "s_waitcnt vmcnt(0)\n\t"
      "s_sleep 4\n\t"
      "global_load_dwordx4 v[136:139], v226, s[62:63] sc1\n\t"
      "global_load_dwordx4 v[140:143], v227, s[62:63] sc1\n\t"
      "global_load_dwordx4 v[144:147], v228, s[62:63] sc1\n\t"
      "global_load_dwordx4 v[148:151], v229, s[62:63] sc1\n\t"
      "global_load_dwordx4 v[152:155], v230, s[62:63] sc1\n\t"
      "global_load_dwordx4 v[128:131], v224, s[64:65] sc1\n\t"
      "s_waitcnt vmcnt(5)\n\t"
      "v_max3_u32 v234, v136, v137, v138\n\t"
      "v_max_u32_e32 v234, v234, v139\n\t"
      "v_cmp_eq_u32_e32 vcc, -1, v234\n\t"
      "s_cbranch_vccnz FP37L_%=\n\t"
      "FP37X_%=:\n\t"
      "s_waitcnt vmcnt(0)\n\t"
      "s_branch FP37B_%=\n\t"
      "FP38R_%=:\n\t"
      "s_mov_b32 s51, 0\n\t"
      "FP38L_%=:\n\t"
      "s_add_u32 s51, s51, 1\n\t"
      "s_cmp_lt_u32 s51, 262144\n\t"
      "s_cbranch_scc0 FP38X_%=\n\t"
      "s_waitcnt vmcnt(0)\n\t"
      "s_sleep 4\n\t"
      "global_load_dwordx4 v[140:143], v227, s[62:63] sc1\n\t"
      "global_load_dwordx4 v[144:147], v228, s[62:63] sc1\n\t"
      "global_load_dwordx4 v[148:151], v229, s[62:63] sc1\n\t"
      "global_load_dwordx4 v[152:155], v230, s[62:63] sc1\n\t"
      "global_load_dwordx4 v[128:131], v224, s[64:65] sc1\n\t"
      "global_load_dwordx4 v[132:135], v225, s[64:65] sc1\n\t"
      "s_waitcnt vmcnt(5)\n\t"
      "v_max3_u32 v234, v140, v141, v142\n\t"
      "v_max_u32_e32 v234, v234, v143\n\t"
      "v_cmp_eq_u32_e32 vcc, -1, v234\n\t"
      "s_cbranch_vccnz FP38L_%=\n\t"
      "FP38X_%=:\n\t"
      "s_waitcnt vmcnt(0)\n\t"
      "s_branch FP38B_%=\n\t"
      "FP39R_%=:\n\t"
      "s_mov_b32 s51, 0\n\t"
      "FP39L_%=:\n\t"
      "s_add_u32 s51, s51, 1\n\t"
      "s_cmp_lt_u32 s51, 262144\n\t"
      "s_cbranch_scc0 FP39X_%=\n\t"
      "s_waitcnt vmcnt(0)\n\t"
      "s_sleep 4\n\t"
      "global_load_dwordx4 v[144:147], v228, s[62:63] sc1\n\t"
      "global_load_dwordx4 v[148:151], v229, s[62:63] sc1\n\t"
      "global_load_dwordx4 v[152:155], v230, s[62:63] sc1\n\t"
      "global_load_dwordx4 v[128:131], v224, s[64:65] sc1\n\t"
      "global_load_dwordx4 v[132:135], v225, s[64:65] sc1\n\t"
      "global_load_dwordx4 v[136:139], v226, s[64:65] sc1\n\t"
      "s_waitcnt vmcnt(5)\n\t"
      "v_max3_u32 v234, v144, v145, v146\n\t"
      "v_max_u32_e32 v234, v234, v147\n\t"
      "v_cmp_eq_u32_e32 vcc, -1, v234\n\t"
      "s_cbranch_vccnz FP39L_%=\n\t"
      "FP39X_%=:\n\t"
      "s_waitcnt vmcnt(0)\n\t"
      "s_branch FP39B_%=\n\t"
      "FP40R_%=:\n\t"
      "s_mov_b32 s51, 0\n\t"
      "FP40L_%=:\n\t"
      "s_add_u32 s51, s51, 1\n\t"
      "s_cmp_lt_u32 s51, 262144\n\t"
      "s_cbranch_scc0 FP40X_%=\n\t"
      "s_waitcnt vmcnt(0)\n\t"
      "s_sleep 4\n\t"
      "global_load_dwordx4 v[148:151], v229, s[62:63] sc1\n\t"
      "global_load_dwordx4 v[152:155], v230, s[62:63] sc1\n\t"
      "global_load_dwordx4 v[128:131], v224, s[64:65] sc1\n\t"
      "global_load_dwordx4 v[132:135], v225, s[64:65] sc1\n\t"
      "global_load_dwordx4 v[136:139], v226, s[64:65] sc1\n\t"
      "global_load_dwordx4 v[140:143], v227, s[64:65] sc1\n\t"
      "s_waitcnt vmcnt(5)\n\t"
      "v_max3_u32 v234, v148, v149, v150\n\t"
      "v_max_u32_e32 v234, v234, v151\n\t"
      "v_cmp_eq_u32_e32 vcc, -1, v234\n\t"
      "s_cbranch_vccnz FP40L_%=\n\t"
      "FP40X_%=:\n\t"
      "s_branch FP40B_%=\n\t"
      "FP41R_%=:\n\t"
      "s_mov_b32 s51, 0\n\t"
      "FP41L_%=:\n\t"
      "s_add_u32 s51, s51, 1\n\t"
      "s_cmp_lt_u32 s51, 262144\n\t"
      "s_cbranch_scc0 FP41X_%=\n\t"
      "s_waitcnt vmcnt(0)\n\t"
      "s_sleep 4\n\t"
      "global_load_dwordx4 v[152:155], v230, s[62:63] sc1\n\t"
      "global_load_dwordx4 v[128:131], v224, s[64:65] sc1\n\t"
      "global_load_dwordx4 v[132:135], v225, s[64:65] sc1\n\t"
      "global_load_dwordx4 v[136:139], v226, s[64:65] sc1\n\t"
      "global_load_dwordx4 v[140:143], v227, s[64:65] sc1\n\t"
      "global_load_dwordx4 v[144:147], v228, s[64:65] sc1\n\t"
      "s_waitcnt vmcnt(5)\n\t"
      "v_max3_u32 v234, v152, v153, v154\n\t"
      "v_max_u32_e32 v234, v234, v155\n\t"
      "v_cmp_eq_u32_e32 vcc, -1, v234\n\t"
      "s_cbranch_vccnz FP41L_%=\n\t"
      "FP41X_%=:\n\t"
      "s_waitcnt vmcnt(0)\n\t"
      "s_branch FP41B_%=\n\t"
      "FP42R_%=:\n\t"
      "s_mov_b32 s51, 0\n\t"
      "FP42L_%=:\n\t"
      "s_add_u32 s51, s51, 1\n\t"
      "s_cmp_lt_u32 s51, 262144\n\t"
      "s_cbranch_scc0 FP42X_%=\n\t"
      "s_sleep 1\n\t"
      "s_mov_b64 exec, 15\n\t"
      "v_mov_b64_e32 v[232:233], 0\n\t"
      "global_atomic_or_x2 v[232:233], v247, v[232:233], s[12:13] sc0 sc1\n\t"
      "s_waitcnt vmcnt(0)\n\t"
      "v_subrev_u32_e32 v232, s14, v232\n\t"
      "v_cmp_ne_u32_e64 s[48:49], s15, v233\n\t"
      "v_subrev_u32_e32 v232, 6, v232\n\t"
      "v_cmp_lt_u32_e32 vcc, 7, v232\n\t"
      "s_mov_b64 exec, -1\n\t"
      "s_or_b64 vcc, vcc, s[48:49]\n\t"
      "s_cbranch_vccnz FP42L_%=\n\t"
      "FP42X_%=:\n\t"
      "FP42DR_%=:\n\t"
      "s_mov_b32 s51, 0\n\t"
      "FP42DL_%=:\n\t"
      "s_add_u32 s51, s51, 1\n\t"
      "s_cmp_lt_u32 s51, 262144\n\t"
      "s_cbranch_scc0 FP42DX_%=\n\t"
      "s_waitcnt vmcnt(0)\n\t"
      "s_sleep 1\n\t"
      "global_load_dwordx4 v[128:131], v224, s[64:65] sc1\n\t"
      "global_load_dwordx4 v[132:135], v225, s[64:65] sc1\n\t"
      "global_load_dwordx4 v[136:139], v226, s[64:65] sc1\n\t"
      "global_load_dwordx4 v[140:143], v227, s[64:65] sc1\n\t"
      "global_load_dwordx4 v[144:147], v228, s[64:65] sc1\n\t"
      "global_load_dwordx4 v[148:151], v229, s[64:65] sc1\n\t"
      "s_waitcnt vmcnt(5)\n\t"
      "v_max3_u32 v234, v128, v129, v130\n\t"
      "v_max_u32_e32 v234, v234, v131\n\t"
      "v_cmp_eq_u32_e32 vcc, -1, v234\n\t"
      "s_cbranch_vccnz FP42DL_%=\n\t"
      "FP42DX_%=:\n\t"
      "s_waitcnt vmcnt(0)\n\t"
      "s_branch FP42B_%=\n\t"
      "NP5R_%=:\n\t"
      "s_mov_b32 s51, 0\n\t"
      "NP5L_%=:\n\t"
      "s_add_u32 s51, s51, 1\n\t"
      "s_cmp_lt_u32 s51, 262144\n\t"
      "s_cbranch_scc0 NP5X_%=\n\t"
      "s_waitcnt vmcnt(0)\n\t"
      "s_sleep 1\n\t"
      "global_load_dwordx4 v[128:131], v224, s[62:63] sc1\n\t"
      "global_load_dwordx4 v[132:135], v225, s[62:63] sc1\n\t"
      "global_load_dwordx4 v[136:139], v226, s[62:63] sc1\n\t"
      "global_load_dwordx4 v[140:143], v227, s[62:63] sc1\n\t"
      "global_load_dwordx4 v[144:147], v228, s[62:63] sc1\n\t"
      "global_load_dwordx4 v[148:151], v229, s[62:63] sc1\n\t"
      "global_load_dwordx4 v[152:155], v230, s[62:63] sc1\n\t"
      "s_waitcnt vmcnt(0)\n\t"
      "v_max3_u32 v234, v128, v129, v130\n\t"
      "v_max3_u32 v234, v234, v131, v132\n\t"
      "v_max3_u32 v234, v234, v133, v134\n\t"
      "v_max3_u32 v234, v234, v135, v136\n\t"
      "v_max3_u32 v234, v234, v137, v138\n\t"
      "v_max3_u32 v234, v234, v139, v140\n\t"
      "v_max3_u32 v234, v234, v141, v142\n\t"
      "v_max3_u32 v234, v234, v143, v144\n\t"
      "v_max3_u32 v234, v234, v145, v146\n\t"
      "v_max3_u32 v234, v234, v147, v148\n\t"
      "v_max3_u32 v234, v234, v149, v150\n\t"
      "v_max3_u32 v234, v234, v151, v152\n\t"
      "v_max3_u32 v234, v234, v153, v154\n\t"
      "v_max_u32_e32 v234, v234, v155\n\t"
      "v_cmp_eq_u32_e32 vcc, -1, v234\n\t"
      "s_cbranch_vccnz NP5L_%=\n\t"
      "NP5X_%=:\n\t"
      "v_mfma_f32_16x16x32_bf16 v[200:203], v[128:131], v[0:3], v[176:179]\n\t"
      "v_mfma_f32_16x16x32_bf16 v[204:207], v[128:131], v[4:7], 0\n\t"
      "v_mfma_f32_16x16x32_bf16 v[208:211], v[128:131], v[8:11], 0\n\t"
      "v_mfma_f32_16x16x32_bf16 v[212:215], v[128:131], v[12:15], 0\n\t"
      "v_mfma_f32_16x16x32_bf16 v[200:203], v[132:135], v[16:19], v[200:203]\n\t"
      "v_mfma_f32_16x16x32_bf16 v[204:207], v[132:135], v[20:23], v[204:207]\n\t"
      "v_mfma_f32_16x16x32_bf16 v[208:211], v[132:135], v[24:27], v[208:211]\n\t"
      "v_mfma_f32_16x16x32_bf16 v[212:215], v[132:135], v[28:31], v[212:215]\n\t"
      "v_mfma_f32_16x16x32_bf16 v[200:203], v[136:139], v[32:35], v[200:203]\n\t"
      "v_mfma_f32_16x16x32_bf16 v[204:207], v[136:139], v[36:39], v[204:207]\n\t"
      "v_mfma_f32_16x16x32_bf16 v[208:211], v[136:139], v[40:43], v[208:211]\n\t"
      "v_mfma_f32_16x16x32_bf16 v[212:215], v[136:139], v[44:47], v[212:215]\n\t"
      "v_mfma_f32_16x16x32_bf16 v[200:203], v[140:143], v[48:51], v[200:203]\n\t"
      "v_mfma_f32_16x16x32_bf16 v[204:207], v[140:143], v[52:55], v[204:207]\n\t"
      "v_mfma_f32_16x16x32_bf16 v[208:211], v[140:143], v[56:59], v[208:211]\n\t"
      "v_mfma_f32_16x16x32_bf16 v[212:215], v[140:143], v[60:63], v[212:215]\n\t"
      "v_mfma_f32_16x16x32_bf16 v[200:203], v[144:147], v[64:67], v[200:203]\n\t"
      "v_mfma_f32_16x16x32_bf16 v[204:207], v[144:147], v[68:71], v[204:207]\n\t"
      "v_mfma_f32_16x16x32_bf16 v[208:211], v[144:147], v[72:75], v[208:211]\n\t"
      "v_mfma_f32_16x16x32_bf16 v[212:215], v[144:147], v[76:79], v[212:215]\n\t"
      "v_mfma_f32_16x16x32_bf16 v[200:203], v[148:151], v[80:83], v[200:203]\n\t"
      "v_mfma_f32_16x16x32_bf16 v[204:207], v[148:151], v[84:87], v[204:207]\n\t"
      "v_mfma_f32_16x16x32_bf16 v[208:211], v[148:151], v[88:91], v[208:211]\n\t"
      "v_mfma_f32_16x16x32_bf16 v[212:215], v[148:151], v[92:95], v[212:215]\n\t"
      "v_mfma_f32_16x16x32_bf16 v[200:203], v[152:155], v[96:99], v[200:203]\n\t"
      "v_mfma_f32_16x16x32_bf16 v[204:207], v[152:155], v[100:103], v[204:207]\n\t"
      "v_mfma_f32_16x16x32_bf16 v[208:211], v[152:155], v[104:107], v[208:211]\n\t"
      "v_mfma_f32_16x16x32_bf16 v[212:215], v[152:155], v[108:111], v[212:215]\n\t"
      "global_load_dwordx4 v[128:131], v224, s[64:65] sc1\n\t"
      "global_load_dwordx4 v[132:135], v225, s[64:65] sc1\n\t"
      "global_load_dwordx4 v[136:139], v226, s[64:65] sc1\n\t"
      "global_load_dwordx4 v[140:143], v227, s[64:65] sc1\n\t"
      "global_load_dwordx4 v[144:147], v228, s[64:65] sc1\n\t"
      "global_load_dwordx4 v[148:151], v229, s[64:65] sc1\n\t"
      "global_load_dwordx4 v[152:155], v230, s[64:65] sc1\n\t"
      "s_waitcnt vmcnt(0)\n\t"
      "s_nop 7\n\t"
      "s_branch NP5B_%=\n\t"
      "FP43R_%=:\n\t"
      "s_mov_b32 s51, 0\n\t"
      "FP43L_%=:\n\t"
      "s_add_u32 s51, s51, 1\n\t"
      "s_cmp_lt_u32 s51, 262144\n\t"
      "s_cbranch_scc0 FP43X_%=\n\t"
      "s_sleep 1\n\t"
      "s_mov_b64 exec, 15\n\t"
      "v_mov_b64_e32 v[232:233], 0\n\t"
      "global_atomic_or_x2 v[232:233], v247, v[232:233], s[12:13] sc0 sc1\n\t"
      "s_waitcnt vmcnt(0)\n\t"
      "v_subrev_u32_e32 v232, s14, v232\n\t"
      "v_cmp_ne_u32_e64 s[48:49], s15, v233\n\t"
      "v_subrev_u32_e32 v232, 6, v232\n\t"
      "v_cmp_lt_u32_e32 vcc, 7, v232\n\t"
      "s_mov_b64 exec, -1\n\t"
      "s_or_b64 vcc, vcc, s[48:49]\n\t"
      "s_cbranch_vccnz FP43L_%=\n\t"
      "FP43X_%=:\n\t"
      "FP43DR_%=:\n\t"
      "s_mov_b32 s51, 0\n\t"
      "FP43DL_%=:\n\t"
      "s_add_u32 s51, s51, 1\n\t"
      "s_cmp_lt_u32 s51, 262144\n\t"
      "s_cbranch_scc0 FP43DX_%=\n\t"
      "s_waitcnt vmcnt(0)\n\t"
      "s_sleep 1\n\t"
      "global_load_dwordx4 v[132:135], v225, s[64:65] sc1\n\t"
      "global_load_dwordx4 v[136:139], v226, s[64:65] sc1\n\t"
      "global_load_dwordx4 v[140:143], v227, s[64:65] sc1\n\t"
      "global_load_dwordx4 v[144:147], v228, s[64:65] sc1\n\t"
      "global_load_dwordx4 v[148:151], v229, s[64:65] sc1\n\t"
      "global_load_dwordx4 v[152:155], v230, s[64:65] sc1\n\t"
      "s_waitcnt vmcnt(5)\n\t"
      "v_max3_u32 v234, v132, v133, v134\n\t"
      "v_max_u32_e32 v234, v234, v135\n\t"
      "v_cmp_eq_u32_e32 vcc, -1, v234\n\t"
      "s_cbranch_vccnz FP43DL_%=\n\t"
      "FP43DX_%=:\n\t"
      "s_waitcnt vmcnt(0)\n\t"
      "s_branch FP43B_%=\n\t"
      "FP44R_%=:\n\t"
      "s_mov_b32 s51, 0\n\t"
      "FP44L_%=:\n\t"
      "s_add_u32 s51, s51, 1\n\t"
      "s_cmp_lt_u32 s51, 262144\n\t"
      "s_cbranch_scc0 FP44X_%=\n\t"
      "s_sleep 1\n\t"
      "s_mov_b64 exec, 15\n\t"
      "v_mov_b64_e32 v[232:233], 0\n\t"
      "global_atomic_or_x2 v[232:233], v247, v[232:233], s[12:13] sc0 sc1\n\t"
      "s_waitcnt vmcnt(0)\n\t"
      "v_subrev_u32_e32 v232, s14, v232\n\t"
      "v_cmp_ne_u32_e64 s[48:49], s15, v233\n\t"
      "v_subrev_u32_e32 v232, 6, v232\n\t"
      "v_cmp_lt_u32_e32 vcc, 7, v232\n\t"
      "s_mov_b64 exec, -1\n\t"
      "s_or_b64 vcc, vcc, s[48:49]\n\t"
      "s_cbranch_vccnz FP44L_%=\n\t"
      "FP44X_%=:\n\t"
      "FP44DR_%=:\n\t"
      "s_mov_b32 s51, 0\n\t"
      "FP44DL_%=:\n\t"
      "s_add_u32 s51, s51, 1\n\t"
      "s_cmp_lt_u32 s51, 262144\n\t"
      "s_cbranch_scc0 FP44DX_%=\n\t"
      "s_waitcnt vmcnt(0)\n\t"
      "s_sleep 1\n\t"
      "global_load_dwordx4 v[136:139], v226, s[64:65] sc1\n\t"
      "global_load_dwordx4 v[140:143], v227, s[64:65] sc1\n\t"
      "global_load_dwordx4 v[144:147], v228, s[64:65] sc1\n\t"
      "global_load_dwordx4 v[148:151], v229, s[64:65] sc1\n\t"
      "global_load_dwordx4 v[152:155], v230, s[64:65] sc1\n\t"
      "s_waitcnt vmcnt(4)\n\t"
      "v_max3_u32 v234, v136, v137, v138\n\t"
      "v_max_u32_e32 v234, v234, v139\n\t"
      "v_cmp_eq_u32_e32 vcc, -1, v234\n\t"
      "s_cbranch_vccnz FP44DL_%=\n\t"
      "FP44DX_%=:\n\t"
      "s_waitcnt vmcnt(0)\n\t"
      "s_branch FP44B_%=\n\t"
      "FP45R_%=:\n\t"
      "s_mov_b32 s51, 0\n\t"
      "FP45L_%=:\n\t"
      "s_add_u32 s51, s51, 1\n\t"
      "s_cmp_lt_u32 s51, 262144\n\t"
      "s_cbranch_scc0 FP45X_%=\n\t"
      "s_sleep 1\n\t"
      "s_mov_b64 exec, 15\n\t"
      "v_mov_b64_e32 v[232:233], 0\n\t"
      "global_atomic_or_x2 v[232:233], v247, v[232:233], s[12:13] sc0 sc1\n\t"
      "s_waitcnt vmcnt(0)\n\t"
      "v_subrev_u32_e32 v232, s14, v232\n\t"
      "v_cmp_ne_u32_e64 s[48:49], s15, v233\n\t"
      "v_subrev_u32_e32 v232, 6, v232\n\t"
      "v_cmp_lt_u32_e32 vcc, 7, v232\n\t"
      "s_mov_b64 exec, -1\n\t"
      "s_or_b64 vcc, vcc, s[48:49]\n\t"
      "s_cbranch_vccnz FP45L_%=\n\t"
      "FP45X_%=:\n\t"
      "FP45DR_%=:\n\t"
      "s_mov_b32 s51, 0\n\t"
      "FP45DL_%=:\n\t"
      "s_add_u32 s51, s51, 1\n\t"
      "s_cmp_lt_u32 s51, 262144\n\t"
      "s_cbranch_scc0 FP45DX_%=\n\t"
      "s_waitcnt vmcnt(0)\n\t"
      "s_sleep 1\n\t"
      "global_load_dwordx4 v[140:143], v227, s[64:65] sc1\n\t"
      "global_load_dwordx4 v[144:147], v228, s[64:65] sc1\n\t"
      "global_load_dwordx4 v[148:151], v229, s[64:65] sc1\n\t"
      "global_load_dwordx4 v[152:155], v230, s[64:65] sc1\n\t"
      "s_waitcnt vmcnt(3)\n\t"
      "v_max3_u32 v234, v140, v141, v142\n\t"
      "v_max_u32_e32 v234, v234, v143\n\t"
      "v_cmp_eq_u32_e32 vcc, -1, v234\n\t"
      "s_cbranch_vccnz FP45DL_%=\n\t"
      "FP45DX_%=:\n\t"
      "s_waitcnt vmcnt(0)\n\t"
      "s_branch FP45B_%=\n\t"
      "FP46R_%=:\n\t"
      "s_mov_b32 s51, 0\n\t"
      "FP46L_%=:\n\t"
      "s_add_u32 s51, s51, 1\n\t"
      "s_cmp_lt_u32 s51, 262144\n\t"
      "s_cbranch_scc0 FP46X_%=\n\t"
      "s_sleep 1\n\t"
      "s_mov_b64 exec, 15\n\t"
      "v_mov_b64_e32 v[232:233], 0\n\t"
      "global_atomic_or_x2 v[232:233], v247, v[232:233], s[12:13] sc0 sc1\n\t"
      "s_waitcnt vmcnt(0)\n\t"
      "v_subrev_u32_e32 v232, s14, v232\n\t"
      "v_cmp_ne_u32_e64 s[48:49], s15, v233\n\t"
      "v_subrev_u32_e32 v232, 6, v232\n\t"
      "v_cmp_lt_u32_e32 vcc, 7, v232\n\t"
      "s_mov_b64 exec, -1\n\t"
      "s_or_b64 vcc, vcc, s[48:49]\n\t"
      "s_cbranch_vccnz FP46L_%=\n\t"
      "FP46X_%=:\n\t"
      "FP46DR_%=:\n\t"
      "s_mov_b32 s51, 0\n\t"
      "FP46DL_%=:\n\t"
      "s_add_u32 s51, s51, 1\n\t"
      "s_cmp_lt_u32 s51, 262144\n\t"
      "s_cbranch_scc0 FP46DX_%=\n\t"
      "s_waitcnt vmcnt(0)\n\t"
      "s_sleep 1\n\t"
      "global_load_dwordx4 v[144:147], v228, s[64:65] sc1\n\t"
      "global_load_dwordx4 v[148:151], v229, s[64:65] sc1\n\t"
      "global_load_dwordx4 v[152:155], v230, s[64:65] sc1\n\t"
      "s_waitcnt vmcnt(2)\n\t"
      "v_max3_u32 v234, v144, v145, v146\n\t"
      "v_max_u32_e32 v234, v234, v147\n\t"
      "v_cmp_eq_u32_e32 vcc, -1, v234\n\t"
      "s_cbranch_vccnz FP46DL_%=\n\t"
      "FP46DX_%=:\n\t"
      "s_waitcnt vmcnt(0)\n\t"
      "s_branch FP46B_%=\n\t"
      "FP47R_%=:\n\t"
      "s_mov_b32 s51, 0\n\t"
      "FP47L_%=:\n\t"
      "s_add_u32 s51, s51, 1\n\t"
      "s_cmp_lt_u32 s51, 262144\n\t"
      "s_cbranch_scc0 FP47X_%=\n\t"
      "s_sleep 1\n\t"
      "s_mov_b64 exec, 15\n\t"
      "v_mov_b64_e32 v[232:233], 0\n\t"
      "global_atomic_or_x2 v[232:233], v247, v[232:233], s[12:13] sc0 sc1\n\t"
      "s_waitcnt vmcnt(0)\n\t"
      "v_subrev_u32_e32 v232, s14, v232\n\t"
      "v_cmp_ne_u32_e64 s[48:49], s15, v233\n\t"
      "v_subrev_u32_e32 v232, 6, v232\n\t"
      "v_cmp_lt_u32_e32 vcc, 7, v232\n\t"
      "s_mov_b64 exec, -1\n\t"
      "s_or_b64 vcc, vcc, s[48:49]\n\t"
      "s_cbranch_vccnz FP47L_%=\n\t"
      "FP47X_%=:\n\t"
      "FP47DR_%=:\n\t"
      "s_mov_b32 s51, 0\n\t"
      "FP47DL_%=:\n\t"
      "s_add_u32 s51, s51, 1\n\t"
      "s_cmp_lt_u32 s51, 262144\n\t"
      "s_cbranch_scc0 FP47DX_%=\n\t"
      "s_waitcnt vmcnt(0)\n\t"
      "s_sleep 1\n\t"
      "global_load_dwordx4 v[148:151], v229, s[64:65] sc1\n\t"
      "global_load_dwordx4 v[152:155], v230, s[64:65] sc1\n\t"
      "s_waitcnt vmcnt(1)\n\t"
      "v_max3_u32 v234, v148, v149, v150\n\t"
      "v_max_u32_e32 v234, v234, v151\n\t"
      "v_cmp_eq_u32_e32 vcc, -1, v234\n\t"
      "s_cbranch_vccnz FP47DL_%=\n\t"
      "FP47DX_%=:\n\t"
      "s_branch FP47B_%=\n\t"
      "FP48R_%=:\n\t"
      "s_mov_b32 s51, 0\n\t"
      "FP48L_%=:\n\t"
      "s_add_u32 s51, s51, 1\n\t"
      "s_cmp_lt_u32 s51, 262144\n\t"
      "s_cbranch_scc0 FP48X_%=\n\t"
      "s_sleep 1\n\t"
      "s_mov_b64 exec, 15\n\t"
      "v_mov_b64_e32 v[232:233], 0\n\t"
      "global_atomic_or_x2 v[232:233], v247, v[232:233], s[12:13] sc0 sc1\n\t"
      "s_waitcnt vmcnt(0)\n\t"
      "v_subrev_u32_e32 v232, s14, v232\n\t"
      "v_cmp_ne_u32_e64 s[48:49], s15, v233\n\t"
      "v_subrev_u32_e32 v232, 6, v232\n\t"
      "v_cmp_lt_u32_e32 vcc, 7, v232\n\t"
      "s_mov_b64 exec, -1\n\t"
      "s_or_b64 vcc, vcc, s[48:49]\n\t"
      "s_cbranch_vccnz FP48L_%=\n\t"
      "FP48X_%=:\n\t"
      "FP48DR_%=:\n\t"
      "s_mov_b32 s51, 0\n\t"
      "FP48DL_%=:\n\t"
      "s_add_u32 s51, s51, 1\n\t"
      "s_cmp_lt_u32 s51, 262144\n\t"
      "s_cbranch_scc0 FP48DX_%=\n\t"
      "s_waitcnt vmcnt(0)\n\t"
      "s_sleep 1\n\t"
      "global_load_dwordx4 v[152:155], v230, s[64:65] sc1\n\t"
      "s_waitcnt vmcnt(0)\n\t"
      "v_max3_u32 v234, v152, v153, v154\n\t"
      "v_max_u32_e32 v234, v234, v155\n\t"
      "v_cmp_eq_u32_e32 vcc, -1, v234\n\t"
      "s_cbranch_vccnz FP48DL_%=\n\t"
      "FP48DX_%=:\n\t"
      "s_waitcnt vmcnt(0)\n\t"
      "s_branch FP48B_%=\n\t"
      "NP6R_%=:\n\t"
      "s_mov_b32 s51, 0\n\t"
      "NP6L_%=:\n\t"
      "s_add_u32 s51, s51, 1\n\t"
      "s_cmp_lt_u32 s51, 262144\n\t"
      "s_cbranch_scc0 NP6X_%=\n\t"
      "s_waitcnt vmcnt(0)\n\t"
      "s_sleep 1\n\t"
      "global_load_dwordx4 v[128:131], v224, s[64:65] sc1\n\t"
      "global_load_dwordx4 v[132:135], v225, s[64:65] sc1\n\t"
      "global_load_dwordx4 v[136:139], v226, s[64:65] sc1\n\t"
      "global_load_dwordx4 v[140:143], v227, s[64:65] sc1\n\t"
      "global_load_dwordx4 v[144:147], v228, s[64:65] sc1\n\t"
      "global_load_dwordx4 v[148:151], v229, s[64:65] sc1\n\t"
      "global_load_dwordx4 v[152:155], v230, s[64:65] sc1\n\t"
      "s_waitcnt vmcnt(0)\n\t"
      "v_max3_u32 v234, v128, v129, v130\n\t"
      "v_max3_u32 v234, v234, v131, v132\n\t"
      "v_max3_u32 v234, v234, v133, v134\n\t"
      "v_max3_u32 v234, v234, v135, v136\n\t"
      "v_max3_u32 v234, v234, v137, v138\n\t"
      "v_max3_u32 v234, v234, v139, v140\n\t"
      "v_max3_u32 v234, v234, v141, v142\n\t"
      "v_max3_u32 v234, v234, v143, v144\n\t"
      "v_max3_u32 v234, v234, v145, v146\n\t"
      "v_max3_u32 v234, v234, v147, v148\n\t"
      "v_max3_u32 v234, v234, v149, v150\n\t"
      "v_max3_u32 v234, v234, v151, v152\n\t"
      "v_max3_u32 v234, v234, v153, v154\n\t"
      "v_max_u32_e32 v234, v234, v155\n\t"
      "v_cmp_eq_u32_e32 vcc, -1, v234\n\t"
      "s_cbranch_vccnz NP6L_%=\n\t"
      "NP6X_%=:\n\t"
      "v_mfma_f32_16x16x32_bf16 v[184:187], v[128:131], v[0:3], v[180:183]\n\t"
      "v_mfma_f32_16x16x32_bf16 v[188:191], v[128:131], v[4:7], 0\n\t"
      "v_mfma_f32_16x16x32_bf16 v[192:195], v[128:131], v[8:11], 0\n\t"
      "v_mfma_f32_16x16x32_bf16 v[196:199], v[128:131], v[12:15], 0\n\t"
      "v_mfma_f32_16x16x32_bf16 v[184:187], v[132:135], v[16:19], v[184:187]\n\t"
      "v_mfma_f32_16x16x32_bf16 v[188:191], v[132:135], v[20:23], v[188:191]\n\t"
      "v_mfma_f32_16x16x32_bf16 v[192:195], v[132:135], v[24:27], v[192:195]\n\t"
      "v_mfma_f32_16x16x32_bf16 v[196:199], v[132:135], v[28:31], v[196:199]\n\t"
      "v_mfma_f32_16x16x32_bf16 v[184:187], v[136:139], v[32:35], v[184:187]\n\t"
      "v_mfma_f32_16x16x32_bf16 v[188:191], v[136:139], v[36:39], v[188:191]\n\t"
      "v_mfma_f32_16x16x32_bf16 v[192:195], v[136:139], v[40:43], v[192:195]\n\t"
      "v_mfma_f32_16x16x32_bf16 v[196:199], v[136:139], v[44:47], v[196:199]\n\t"
      "v_mfma_f32_16x16x32_bf16 v[184:187], v[140:143], v[48:51], v[184:187]\n\t"
      "v_mfma_f32_16x16x32_bf16 v[188:191], v[140:143], v[52:55], v[188:191]\n\t"
      "v_mfma_f32_16x16x32_bf16 v[192:195], v[140:143], v[56:59], v[192:195]\n\t"
      "v_mfma_f32_16x16x32_bf16 v[196:199], v[140:143], v[60:63], v[196:199]\n\t"
      "v_mfma_f32_16x16x32_bf16 v[184:187], v[144:147], v[64:67], v[184:187]\n\t"
      "v_mfma_f32_16x16x32_bf16 v[188:191], v[144:147], v[68:71], v[188:191]\n\t"
      "v_mfma_f32_16x16x32_bf16 v[192:195], v[144:147], v[72:75], v[192:195]\n\t"
      "v_mfma_f32_16x16x32_bf16 v[196:199], v[144:147], v[76:79], v[196:199]\n\t"
      "v_mfma_f32_16x16x32_bf16 v[184:187], v[148:151], v[80:83], v[184:187]\n\t"
      "v_mfma_f32_16x16x32_bf16 v[188:191], v[148:151], v[84:87], v[188:191]\n\t"
      "v_mfma_f32_16x16x32_bf16 v[192:195], v[148:151], v[88:91], v[192:195]\n\t"
      "v_mfma_f32_16x16x32_bf16 v[196:199], v[148:151], v[92:95], v[196:199]\n\t"
      "v_mfma_f32_16x16x32_bf16 v[184:187], v[152:155], v[96:99], v[184:187]\n\t"
      "v_mfma_f32_16x16x32_bf16 v[188:191], v[152:155], v[100:103], v[188:191]\n\t"
      "v_mfma_f32_16x16x32_bf16 v[192:195], v[152:155], v[104:107], v[192:195]\n\t"
      "v_mfma_f32_16x16x32_bf16 v[196:199], v[152:155], v[108:111], v[196:199]\n\t"
      "s_waitcnt vmcnt(0)\n\t"
      "s_nop 7\n\t"
      "s_branch NP6B_%=\n\t"
      : "+{s[4:19]}"(s), "+{s[20:27]}"(s2), "+{s[28:29]}"(kr), "+{s30}"(od), "+{v[240:247]}"(v), "+{v[136:139]}"(vo), "+{v140}"(vq)
      :
      : "memory", "vcc", "scc",
        "v0", "v1", "v2", "v3", "v4", "v5", "v6", "v7", "v8", "v9", "v10", "v11", "v12", "v13", "v14", "v15", "v16",
        "v17", "v18", "v19", "v20", "v21", "v22", "v23", "v24", "v25", "v26", "v27", "v28", "v29", "v30", "v31", "v32",
        "v33", "v34", "v35", "v36", "v37", "v38", "v39", "v40", "v41", "v42", "v43", "v44", "v45", "v46", "v47", "v48",
        "v49", "v50", "v51", "v52", "v53", "v54", "v55", "v56", "v57", "v58", "v59", "v60", "v61", "v62", "v63", "v64",
        "v65", "v66", "v67", "v68", "v69", "v70", "v71", "v72", "v73", "v74", "v75", "v76", "v77", "v78", "v79", "v80",
        "v81", "v82", "v83", "v84", "v85", "v86", "v87", "v88", "v89", "v90", "v91", "v92", "v93", "v94", "v95", "v96",
        "v97", "v98", "v99", "v100", "v101", "v102", "v103", "v104", "v105", "v106", "v107", "v108", "v109", "v110",
        "v111", "v112", "v113", "v114", "v115", "v116", "v117", "v118", "v119", "v120", "v121", "v122", "v123", "v124",
        "v125", "v126", "v127", "v128", "v129", "v130", "v131", "v132", "v133", "v134", "v135", "v141", "v142", "v143",
        "v144", "v145", "v146", "v147", "v148", "v149", "v150", "v151", "v152", "v153", "v154", "v155", "v156", "v157",
        "v158", "v159", "v160", "v161", "v162", "v163", "v164", "v165", "v166", "v167", "v168", "v169", "v170", "v171",
        "v172", "v173", "v174", "v175", "v176", "v177", "v178", "v179", "v180", "v181", "v182", "v183", "v184", "v185",
        "v186", "v187", "v188", "v189", "v190", "v191", "v192", "v193", "v194", "v195", "v196", "v197", "v198", "v199",
        "v200", "v201", "v202", "v203", "v204", "v205", "v206", "v207", "v208", "v209", "v210", "v211", "v212", "v213",
        "v214", "v215", "v216", "v217", "v218", "v219", "v220", "v221", "v222", "v223", "v224", "v225", "v226", "v227",
        "v228", "v229", "v230", "v231", "v232", "v233", "v234", "v235", "v236", "v237", "v238", "v239", "v248", "v249",
        "v250", "v251", "v252", "v253", "s31", "s44", "s45", "s46", "s47", "s48", "s49", "s50", "s51", "s52", "s53",
        "s54", "s55", "s56", "s57", "s58", "s59", "s60", "s61", "s62", "s63", "s64", "s65", "s66", "s67", "s68", "s69",
        "s70"
    );
    __builtin_amdgcn_endpgm();
  }
}

extern "C" void kernel_launch(void* const* d_in, const int* in_sizes, int n_in, void* d_out, int out_size,
                              void* d_ws, size_t ws_size, hipStream_t stream) {
  (void)hipFuncSetAttribute((const void*)fused_block, hipFuncAttributeMaxDynamicSharedMemorySize, LDS_BYTES);
  fused_block<<<256, 512, LDS_BYTES, stream>>>((const char*)d_in[0], (const char*)d_in[1], (const char*)d_in[2],
                                               (const char*)d_in[4], (const char*)d_in[5], (const char*)d_in[6],
                                               (const char*)d_in[7], (char*)d_out, (char*)d_ws);
}
```

```cpp
#include <hip/hip_runtime.h>
#include <stdint.h>

#define WS_ST 0
#define WS_HN 2097152
#define WS_O 4194304
#define WS_G 6291456
#define ST_FL 65536
#define ST_PW 114688
#define WS_EP 131072
#define PROBE 1835008
#define ST_CK 3981312
#define ST_H2 4112384
#define ST_H3 8175616
#define ST_H4 8306688
#define A_UNIT 5120
#define A_HALO 40960
#define A_SLOT 43008
#define A_CS 131072
#define A_ST 132096
#define A_EX 142336
#define A_FL 154624
#define A_MK 154752
#define B_UNIT 7168
#define B_SLOT 57344
#define B_X 114688
#define B_OP 928
#define B_PS 129536
#define B_WX 130560
#define B_WY 66048
#define G_WV 16384
#define A_W0 49152
#define A_WD 40960
#define G_RED 0
#define G_RED2 32768
#define LDS_KEY 163600
#define LDS_SINK 163328
#define LDS_BYTES 163840
#define XO 0
#define M44 1
#define KROT 14
#define KMUL 1
#define HP4 1
#define W2E 1
#define R4 0
#define SP3 1

typedef unsigned short u16;
typedef unsigned long long u64;
typedef __attribute__((ext_vector_type(4))) unsigned int u32x4;
typedef __attribute__((ext_vector_type(8))) unsigned int u32x8;
typedef __attribute__((ext_vector_type(16))) unsigned int u32x16;
#define UNI(x) (unsigned)__builtin_amdgcn_readfirstlane((int)(x))
#define PIN() __builtin_amdgcn_sched_barrier(0)

__device__ __forceinline__ int swz_tr(int row) { return (((row >> 1) & 1) | (((row >> 3) & 1) << 1)) << 1; }
__device__ __forceinline__ int swz_row(int row) { return (row >> 1) & 7; }
__device__ __forceinline__ int unit(int blk, int slice, int par) { return (((blk < 14 ? blk : blk - 14) * 8 + slice) * 2 + ((blk < 14 ? 0 : 1) ^ par)) * 8192; }
__device__ __forceinline__ int part(int n, int side) { return (n >> 13) * 16384 + side * 8192 + (n & 8191); }
template <class T>
__device__ __forceinline__ void put(T& s, int i, u64 p) { s[i] = (unsigned)p; s[i + 1] = (unsigned)(p >> 32); }
template <class T>
__device__ __forceinline__ void put2(T& s, int i, unsigned lo, unsigned hi) { s[i] = UNI(lo); s[i + 1] = UNI(hi); }

__global__ __launch_bounds__(512, 2) void fused_block(const char* __restrict__ x, const char* __restrict__ wqkv,
                                                   const char* __restrict__ wo, const char* __restrict__ w1,
                                                   const char* __restrict__ b1, const char* __restrict__ w2,
                                                   const char* __restrict__ b2, char* __restrict__ out,
                                                   char* __restrict__ ws0) {
  u32x8 f;
  asm volatile(
      "s_load_dwordx4 s[4:7], s[0:1], 0x0\n\t"
      "v_readfirstlane_b32 s3, v0\n\t"
      "s_getpc_b64 s[40:41]\n\t"
      "s_lshr_b32 s12, s2, 3\n\t"
      "s_or_b32 s13, s3, s12\n\t"
      "s_and_b32 s13, s13, 0x1d0\n\t"
      "s_cmp_lg_u32 s13, 16\n\t"
      "s_cbranch_scc1 ET2_%=\n\t"
      "s_and_b32 s12, s12, 7\n\t"
      "s_mov_b32 m0, 163328\n\t"
      "v_lshl_add_u32 v3, s12, 6, v0\n\t"
      "v_min_u32_e32 v3, 505, v3\n\t"
      "v_lshlrev_b32_e32 v3, 7, v3\n\t"
      "global_load_lds_dword v3, s[40:41]\n\t"
      "ET2_%=:\n\t"
      "v_lshrrev_b32_e32 v1, 3, v0\n\t"
      "v_and_b32_e32 v2, 7, v0\n\t"
      "s_and_b32 s17, s2, 7\n\t"
      "s_lshr_b32 s18, s2, 3\n\t"
      "s_lshr_b32 s3, s3, 6\n\t"
      "s_cmpk_gt_u32 s2, 0x7f\n\t"
      "s_cbranch_scc1 EB_%=\n\t"
      "v_and_b32_e32 v3, 2, v1\n\t"
      "v_lshrrev_b32_e32 v4, 1, v1\n\t"
      "s_lshr_b32 s19, s18, 2\n\t"
      "s_and_b32 s12, s17, 6\n\t"
      "s_lshl1_add_u32 s19, s12, s19\n\t"
      "v_and_or_b32 v3, v4, 4, v3\n\t"
      "v_xor_b32_e32 v3, v3, v2\n\t"
      "v_min_u32_e32 v3, 6, v3\n\t"
      "v_mul_u32_u24_e32 v5, 2688, v1\n\t"
      "v_lshl_add_u32 v3, v3, 4, v5\n\t"
      "v_add_u32_e32 v195, 2048, v3\n\t"
      "v_add_u32_e32 v196, 1920, v3\n\t"
      "v_add_u32_e32 v197, 5888, v3\n\t"
      "s_mul_i32 s12, s19, 114688\n\t"
      "v_and_b32_e32 v5, 0x38, v1\n\t"
      "v_add_u32_e32 v5, v5, v1\n\t"
      "v_mul_u32_u24_e32 v5, 896, v5\n\t"
      "s_and_b32 s14, s17, 1\n\t"
      "s_add_u32 s14, s14, 3\n\t"
      "s_and_b32 s13, s18, 3\n\t"
      "s_xor_b32 s13, s13, s14\n\t"
      "s_mul_i32 s13, s13, 112\n\t"
      "v_and_b32_e32 v6, 3, v4\n\t"
      "v_xor_b32_e32 v6, v6, v2\n\t"
      "v_lshl_add_u32 v5, v6, 4, v5\n\t"
      "s_sub_u32 s12, s12, 15360\n\t"
      "s_ashr_i32 s14, s12, 31\n\t"
      "s_sub_u32 s13, s13, 4096\n\t"
      "v_add_u32_e32 v192, 15360, v5\n\t"
      "v_xor_b32_e32 v5, 64, v5\n\t"
      "v_add_u32_e32 v193, 21504, v5\n\t"
      "s_mul_i32 s9, s3, 5120\n\t"
      "s_sub_u32 s3, s3, 4\n\t"
      "s_cbranch_scc0 EH_%=\n\t"
      "s_waitcnt lgkmcnt(0)\n\t"
      "s_add_u32 s4, s4, s12\n\t"
      "s_addc_u32 s5, s5, s14\n\t"
      "s_add_u32 s6, s6, s13\n\t"
      "s_addc_u32 s7, s7, -1\n\t"
      "s_add_u32 m0, s9, 0\n\t"
      "s_nop 0\n\t"
      "global_load_lds_dwordx4 v195, s[6:7] offset:2048\n\t"
      "global_load_lds_dwordx4 v192, s[4:5]\n\t"
      "global_load_lds_dwordx4 v193, s[4:5] offset:1024\n\t"
      "global_load_lds_dwordx4 v196, s[6:7] offset:3072\n\t"
      "s_add_u32 m0, s9, 4096\n\t"
      "s_nop 0\n\t"
      "global_load_lds_dwordx4 v197, s[6:7]\n\t"
      "s_branch EX_%=\n\t"
      "EH_%=:\n\t"
      "s_cmp_eq_u32 s19, 0\n\t"
      "s_cselect_b32 s13, 16, 0\n\t"
      "s_lshl2_add_u32 s13, s3, s13\n\t"
      "v_and_or_b32 v3, v1, 3, s13\n\t"
      "v_bfe_u32 v4, v3, 1, 3\n\t"
      "v_mul_u32_u24_e32 v3, 896, v3\n\t"
      "v_xor_b32_e32 v4, v4, v2\n\t"
      "v_lshl_add_u32 v3, v4, 4, v3\n\t"
      "v_add_u32_e32 v194, 1024, v3\n\t"
      "s_waitcnt lgkmcnt(0)\n\t"
      "s_branch EX_%=\n\t"
      "EB_%=:\n\t"
      "v_and_b32_e32 v3, 63, v0\n\t"
      "v_lshlrev_b32_e32 v3, 4, v3\n\t"
      "s_sub_u32 s18, s18, 16\n\t"
      "s_min_u32 s16, s3, 6\n\t"
      "s_xor_b32 s15, s17, 1\n\t"
      "s_lshl4_add_u32 s15, s15, s18\n\t"
      "s_mul_i32 s15, s15, 14336\n\t"
      "s_lshl_b32 s12, s16, 7\n\t"
      "s_add_u32 s15, s15, s12\n\t"
      "v_mul_u32_u24_e32 v6, 56, v3\n\t"
      "v_add_u32_e32 v6, s15, v6\n\t"
      "s_add_u32 s19, s17, 0\n\t"
      "s_and_b32 s19, s19, 3\n\t"
      "s_and_b32 s12, s17, 4\n\t"
      "s_or_b32 s19, s19, s12\n\t"
      "s_lshl4_add_u32 s19, s19, s18\n\t"
      "s_lshl3_add_u32 s12, s19, s3\n\t"
      "s_lshl_b32 s12, s12, 11\n\t"
      "s_mul_i32 s19, s19, 14336\n\t"
      "s_lshl_b32 s16, s16, 11\n\t"
      "s_add_u32 s19, s19, s16\n\t"
      "s_add_u32 s8, s16, 114688\n\t"
      "v_add_u32_e32 v1, s19, v3\n\t"
      "s_lshl4_add_u32 s16, s3, s18\n\t"
      "v_bfe_u32 v4, v0, 2, 2\n\t"
      "v_mul_u32_u24_e32 v4, 896, v4\n\t"
      "v_and_b32_e32 v5, 48, v3\n\t"
      "v_lshl_add_u32 v4, v5, 3, v4\n\t"
      "s_and_b32 s13, s17, 1\n\t"
      "s_mul_i32 s13, s13, 384\n\t"
      "v_add_u32_e32 v4, s13, v4\n\t"
      "v_lshrrev_b32_e32 v2, 8, v3\n\t"
      "s_mul_i32 s13, s16, 1\n\t"
      "s_mul_i32 s14, s16, 3\n\t"
      "s_add_u32 s14, s14, 128\n\t"
      "v_add_u32_e32 v5, s13, v2\n\t"
      "v_add_u32_e32 v2, s14, v2\n\t"
      "v_min_u32_e32 v2, 447, v2\n\t"
      "s_movk_i32 s13, 2688\n\t"
      "v_mad_u32_u24 v5, v5, s13, v4\n\t"
      "v_mad_u32_u24 v4, v2, s13, v4\n\t"
      "s_mov_b32 m0, 163328\n\t"
      "s_waitcnt lgkmcnt(0)\n\t"
      "s_mov_b32 m0, s8\n\t"
      "s_nop 0\n\t"
      "global_load_lds_dwordx4 v1, s[4:5]\n\t"
      "global_load_lds_dwordx4 v1, s[4:5] offset:1024\n\t"
      "s_mov_b32 m0, 163328\n\t"
      "s_mov_b32 exec_lo, 0xfff\n\t"
      "s_mov_b32 exec_hi, 0x0\n\t"
      "global_load_lds_dword v5, s[6:7]\n\t"
      "s_mov_b64 exec, -1\n\t"
      "s_barrier\n\t"
      "s_mov_b32 exec_lo, 0xfff0fff\n\t"
      "s_mov_b32 exec_hi, 0xfff\n\t"
      "global_load_lds_dword v4, s[6:7]\n\t"
      "s_mov_b64 exec, -1\n\t"
      "s_mov_b64 exec, 0xffff\n\t"
      "global_load_lds_dword v6, s[4:5]\n\t"
      "s_mov_b64 exec, -1\n\t"
      "EX_%=:\n\t"
      : "={v[192:199]}"(f)
      : "{s[0:1]}"(__builtin_amdgcn_kernarg_segment_ptr()), "{s2}"(blockIdx.x), "{v0}"(threadIdx.x)
      : "memory", "scc", "s3", "s4", "s5", "s6", "s7", "s8", "s9", "s10", "s11", "s12", "s13", "s14", "s15", "s16", "s17", "s18", "s19", "v1", "v2",
        "v3", "v4", "v5", "v6", "s40", "s41", "s42", "s43", "s48", "s49", "s50"
      );
  PIN();
  char* const ws = ws0 + (-(unsigned)(uintptr_t)ws0 & ((2u << 20) - 1));
  const int wave = __builtin_amdgcn_readfirstlane(threadIdx.x >> 6);
  const int xcd = blockIdx.x & 7, idx = blockIdx.x >> 3, side = xcd >> 2, xp = xcd & 3;
#define LANE_BITS(l) \
  const int lane = (l), g = lane >> 4, c = lane & 15, q4 = c >> 2, p4 = lane & 3, lr = lane >> 3, lc = lane & 7

  if (__builtin_expect(idx < 16, 1)) {
    const int rt = M44 ? 2 * (xcd & 6) + (idx >> 2) : 2 * xcd + (idx >> 3), hp = M44 ? (idx & 3) ^ (3 + (xcd & 1)) : idx & 7, hh = (wave >> 1) & 1, half = wave & 1;
    const unsigned sta = A_ST + half * 1024;
    if (wave < 4) {
      LANE_BITS(threadIdx.x & 63);
      u32x16 v;
      u32x8 s;
      v[0] = 4 * half * A_UNIT + c * 128 + ((g ^ swz_row(c)) << 4);
      v[1] = A_HALO + c * 128 + ((g ^ swz_row(c)) << 4);
#pragma unroll
      for (int j = 0; j < 2; ++j) {
        const int by = 56 * hh + 32 * j + 8 * p4;
        v[2 + j] = g * A_UNIT + 2048 + q4 * 128 + (((by >> 4) ^ swz_tr(8 * g + q4)) << 4) + (by & 8);
      }
      const unsigned csa = A_CS + hh * 384, exa = A_EX + hh * 3 * 2048;
      v[4] = csa + g * 16;
      v[5] = csa + c * 4;
      v[6] = v[4] + 64 * half;
      v[7] = v[5] + 64 * half;
      v[8] = A_FL + (wave ^ 1) * 8;
      v[9] = sta + g * 16;
      v[10] = sta + c * 4;
      const int key = 4 * g + (c & 1);
      v[11] = sta + 384 + (key <= c ? 128 : 64) + key * 4;
      unsigned vk1 = sta + 384 + (key + 2 <= c ? 128 : 64) + key * 4 + 8;
      v[12] = exa + half * 2048 + lane * 16;
      v[13] = exa + half * 2048 + lane * 8;
      v[14] = exa + lane * 16 + 8 * half;
      v[15] = lane * 16;
      put(s, 0, (u64)(ws + WS_O + part(((8 * (rt & 7) + 4 * half) * 16 + 2 * hp + hh) * 1024, 0)));
      s[2] = half;
      s[3] = UNI(rt == 0 && half == 0 ? 0xff800000u : 0u);
      s[4] = exa + 1024 + 512 * half;
      s[5] = A_FL + 32 + 4 * half;
      put(s, 6, (u64)(ws + WS_ST));
      unsigned hint = ST_H2 + part(((((rt & 7) * 8 + hp) * 2 + half) * 2 + hh) * 64, side);
      unsigned chk = ST_CK + part((((rt & 7) * 2 + half) * 16 + 2 * hp + hh) * 256, side);
    asm volatile(
      "s_waitcnt lgkmcnt(0)\n\ts_nop 4\n\t"
      ".p2align 3\n\t"
      "v_xor_b32_e64 v248, 64, v224\n\t"
      "v_xor_b32_e32 v249, 64, v225\n\t"
      "s_mov_b32 s36, 0xaaaaaaaa\n\t"
      "s_mov_b32 s37, 0xaaaaaaaa\n\t"
      "s_mov_b32 s38, 0xfff0ffff\n\t"
      "s_mov_b32 s39, 0xf000ff00\n\t"
      "s_mov_b32 s40, 0xffc0fffc\n\t"
      "s_mov_b32 s41, 0xc000fc00\n\t"
      "s_mov_b32 s42, 0x0\n\t"
      "s_mov_b32 s43, 0xffff0000\n\t"
      "s_mov_b32 s44, 0x10001\n\t"
      "s_mov_b32 s45, 0x10001\n\t"
      "v_mov_b32_e32 v218, 0x0c0c0c0c\n\t"
      "v_mov_b32_e32 v216, 0xc0c0100\n\t"
      "v_mov_b32_e32 v217, 0x1000c0c\n\t"
      "v_cndmask_b32_e64 v216, v216, v217, s[36:37]\n\t"
      "v_cndmask_b32_e64 v240, v216, v218, s[38:39]\n\t"
      "v_cndmask_b32_e64 v242, v218, v216, s[38:39]\n\t"
      "v_mov_b32_e32 v216, 0xc0c0302\n\t"
      "v_mov_b32_e32 v217, 0x3020c0c\n\t"
      "v_cndmask_b32_e64 v216, v216, v217, s[36:37]\n\t"
      "v_cndmask_b32_e64 v241, v216, v218, s[40:41]\n\t"
      "v_cndmask_b32_e64 v243, v218, v216, s[40:41]\n\t"
      "v_xor_b32_e32 v213, 8, v232\n\t"
      "v_lshrrev_b32_e32 v214, 1, v239\n\t"
      "v_add_u32_e32 v214, s8, v214\n\t"
      "v_mov_b32_e32 v215, s9\n\t"
      "v_mov_b32_e32 v220, 154752\n\t"
      "v_lshrrev_b32_e32 v221, 2, v239\n\t"
      "s_add_u32 s56, s4, 32768\n\t"
      "s_addc_u32 s57, s5, 0\n\t"
      "s_add_u32 s58, s4, 65536\n\t"
      "s_addc_u32 s59, s5, 0\n\t"
      "s_add_u32 s60, s4, 98304\n\t"
      "s_addc_u32 s61, s5, 0\n\t"
      "s_and_b32 s52, s12, 0x2000\n\t"
      "v_mov_b32_e32 v244, s7\n\t"
      "v_mov_b32_e32 v245, s7\n\t"
      "v_mov_b32_e32 v246, s7\n\t"
      "v_mov_b32_e32 v247, s7\n\t"
      "v_mov_b64_e32 v[0:1], 0\n\t"
      "v_mov_b64_e32 v[2:3], 0\n\t"
      "v_mov_b64_e32 v[4:5], 0\n\t"
      "v_mov_b64_e32 v[6:7], 0\n\t"
      "v_mov_b64_e32 v[8:9], 0\n\t"
      "v_mov_b64_e32 v[10:11], 0\n\t"
      "v_mov_b64_e32 v[12:13], 0\n\t"
      "v_mov_b64_e32 v[14:15], 0\n\t"
      "v_mov_b64_e32 v[16:17], 0\n\t"
      "v_mov_b64_e32 v[18:19], 0\n\t"
      "v_mov_b64_e32 v[20:21], 0\n\t"
      "v_mov_b64_e32 v[22:23], 0\n\t"
      "v_mov_b64_e32 v[24:25], 0\n\t"
      "v_mov_b64_e32 v[26:27], 0\n\t"
      "v_mov_b64_e32 v[28:29], 0\n\t"
      "v_mov_b64_e32 v[30:31], 0\n\t"
      "v_mov_b64_e32 v[32:33], 0\n\t"
      "v_mov_b64_e32 v[34:35], 0\n\t"
      "v_mov_b64_e32 v[36:37], 0\n\t"
      "v_mov_b64_e32 v[38:39], 0\n\t"
      "v_mov_b64_e32 v[40:41], 0\n\t"
      "v_mov_b64_e32 v[42:43], 0\n\t"
      "v_mov_b64_e32 v[44:45], 0\n\t"
      "v_mov_b64_e32 v[46:47], 0\n\t"
      "v_mov_b64_e32 v[48:49], 0\n\t"
      "v_mov_b64_e32 v[50:51], 0\n\t"
      "v_mov_b64_e32 v[52:53], 0\n\t"
      "v_mov_b64_e32 v[54:55], 0\n\t"
      "v_mov_b64_e32 v[56:57], 0\n\t"
      "v_mov_b64_e32 v[58:59], 0\n\t"
      "v_mov_b64_e32 v[60:61], 0\n\t"
      "v_mov_b64_e32 v[62:63], 0\n\t"
      "v_mov_b64_e32 v[64:65], 0\n\t"
      "v_mov_b64_e32 v[66:67], 0\n\t"
      "v_mov_b64_e32 v[68:69], 0\n\t"
      "v_mov_b64_e32 v[70:71], 0\n\t"
      "v_mov_b64_e32 v[72:73], 0\n\t"
      "v_mov_b64_e32 v[74:75], 0\n\t"
      "v_mov_b64_e32 v[76:77], 0\n\t"
      "v_mov_b64_e32 v[78:79], 0\n\t"
      "v_mov_b64_e32 v[80:81], 0\n\t"
      "v_mov_b64_e32 v[82:83], 0\n\t"
      "v_mov_b64_e32 v[84:85], 0\n\t"
      "v_mov_b64_e32 v[86:87], 0\n\t"
      "v_mov_b64_e32 v[88:89], 0\n\t"
      "v_mov_b64_e32 v[90:91], 0\n\t"
      "v_mov_b64_e32 v[92:93], 0\n\t"
      "v_mov_b64_e32 v[94:95], 0\n\t"
      "v_mov_b64_e32 v[96:97], 0\n\t"
      "v_mov_b64_e32 v[98:99], 0\n\t"
      "v_mov_b64_e32 v[100:101], 0\n\t"
      "v_mov_b64_e32 v[102:103], 0\n\t"
      "v_mov_b64_e32 v[104:105], 0\n\t"
      "v_mov_b64_e32 v[106:107], 0\n\t"
      "s_mov_b32 s47, 6\n\t"
      "s_cmp_eq_u32 s6, 0\n\t"
      "s_cbranch_scc0 LOOP1_%=\n\t"
      "s_waitcnt vmcnt(0)\n\t"
      "s_barrier\n\t"
      "ds_read_b64_tr_b16 v[108:109], v226\n\t"
      "ds_read_b64_tr_b16 v[110:111], v226 offset:512\n\t"
      "ds_read_b128 v[132:135], v224\n\t"
      "ds_read_b128 v[136:139], v224 offset:5120\n\t"
      "ds_read_b128 v[140:143], v224 offset:10240\n\t"
      "ds_read_b128 v[144:147], v224 offset:15360\n\t"
      "ds_read_b64_tr_b16 v[112:113], v227\n\t"
      "ds_read_b64_tr_b16 v[114:115], v227 offset:512\n\t"
      "ds_read_b128 v[148:151], v225\n\t"
      "ds_read_b64_tr_b16 v[116:117], v226 offset:1024\n\t"
      "ds_read_b64_tr_b16 v[118:119], v226 offset:1536\n\t"
      "ds_read_b64_tr_b16 v[120:121], v227 offset:1024\n\t"
      "ds_read_b64_tr_b16 v[122:123], v227 offset:1536\n\t"
      "ds_read_b64_tr_b16 v[124:125], v226 offset:2048\n\t"
      "ds_read_b64_tr_b16 v[126:127], v226 offset:2560\n\t"
      "ds_read_b64_tr_b16 v[128:129], v227 offset:2048\n\t"
      "ds_read_b64_tr_b16 v[130:131], v227 offset:2560\n\t"
      "s_branch EVEN0_%=\n\t"
      "s_waitcnt lgkmcnt(0)\n\t"
      ".p2align 6\n\t"
      "ODD0_%=:\n\t"
      "s_waitcnt lgkmcnt(0)\n\t"
      "v_mfma_f32_16x16x32_bf16 v[0:3], v[152:155], v[176:179], v[0:3]\n\t"
      "v_add_u32_e32 v224, s46, v224\n\t"
      "v_add_u32_e32 v226, s46, v226\n\t"
      "v_mfma_f32_16x16x32_bf16 v[24:27], v[152:155], v[180:183], v[24:27]\n\t"
      "v_add_u32_e32 v227, s46, v227\n\t"
      "v_add_u32_e32 v225, s46, v225\n\t"
      "v_mfma_f32_16x16x32_bf16 v[48:51], v[152:155], v[184:187], v[48:51]\n\t"
      "s_barrier\n\t"
      "ds_read_b64_tr_b16 v[108:109], v226\n\t"
      "v_mfma_f32_16x16x32_bf16 v[72:75], v[152:155], v[188:191], v[72:75]\n\t"
      "ds_read_b64_tr_b16 v[110:111], v226 offset:512\n\t"
      "v_mfma_f32_16x16x32_bf16 v[4:7], v[156:159], v[176:179], v[4:7]\n\t"
      "ds_read_b128 v[132:135], v224\n\t"
      "v_mfma_f32_16x16x32_bf16 v[28:31], v[156:159], v[180:183], v[28:31]\n\t"
      "ds_read_b128 v[136:139], v224 offset:5120\n\t"
      "v_mfma_f32_16x16x32_bf16 v[52:55], v[156:159], v[184:187], v[52:55]\n\t"
      "ds_read_b128 v[140:143], v224 offset:10240\n\t"
      "v_mfma_f32_16x16x32_bf16 v[76:79], v[156:159], v[188:191], v[76:79]\n\t"
      "ds_read_b128 v[144:147], v224 offset:15360\n\t"
      "v_mfma_f32_16x16x32_bf16 v[96:99], v[152:155], v[192:195], v[96:99]\n\t"
      "v_mfma_f32_16x16x32_bf16 v[8:11], v[160:163], v[176:179], v[8:11]\n\t"
      "ds_read_b64_tr_b16 v[112:113], v227\n\t"
      "v_mfma_f32_16x16x32_bf16 v[32:35], v[160:163], v[180:183], v[32:35]\n\t"
      "ds_read_b64_tr_b16 v[114:115], v227 offset:512\n\t"
      "v_mfma_f32_16x16x32_bf16 v[56:59], v[160:163], v[184:187], v[56:59]\n\t"
      "ds_read_b128 v[148:151], v225\n\t"
      "v_mfma_f32_16x16x32_bf16 v[80:83], v[160:163], v[188:191], v[80:83]\n\t"
      "ds_read_b64_tr_b16 v[116:117], v226 offset:1024\n\t"
      "v_mfma_f32_16x16x32_bf16 v[12:15], v[164:167], v[176:179], v[12:15]\n\t"
      "ds_read_b64_tr_b16 v[118:119], v226 offset:1536\n\t"
      "v_mfma_f32_16x16x32_bf16 v[36:39], v[164:167], v[180:183], v[36:39]\n\t"
      "ds_read_b64_tr_b16 v[120:121], v227 offset:1024\n\t"
      "v_mfma_f32_16x16x32_bf16 v[60:63], v[164:167], v[184:187], v[60:63]\n\t"
      "v_mfma_f32_16x16x32_bf16 v[84:87], v[164:167], v[188:191], v[84:87]\n\t"
      "ds_read_b64_tr_b16 v[122:123], v227 offset:1536\n\t"
      "v_mfma_f32_16x16x32_bf16 v[100:103], v[160:163], v[192:195], v[100:103]\n\t"
      "ds_read_b64_tr_b16 v[124:125], v226 offset:2048\n\t"
      "v_mfma_f32_16x16x32_bf16 v[16:19], v[176:179], v[168:171], v[16:19]\n\t"
      "ds_read_b64_tr_b16 v[126:127], v226 offset:2560\n\t"
      "v_mfma_f32_16x16x32_bf16 v[40:43], v[180:183], v[168:171], v[40:43]\n\t"
      "ds_read_b64_tr_b16 v[128:129], v227 offset:2048\n\t"
      "v_mfma_f32_16x16x32_bf16 v[64:67], v[184:187], v[168:171], v[64:67]\n\t"
      "ds_read_b64_tr_b16 v[130:131], v227 offset:2560\n\t"
      "v_mfma_f32_16x16x32_bf16 v[88:91], v[188:191], v[168:171], v[88:91]\n\t"
      "v_mfma_f32_16x16x32_bf16 v[20:23], v[176:179], v[172:175], v[20:23]\n\t"
      "v_mfma_f32_16x16x32_bf16 v[44:47], v[180:183], v[172:175], v[44:47]\n\t"
      "v_add_u32_e32 v248, s46, v248\n\t"
      "v_add_u32_e32 v249, s46, v249\n\t"
      "v_mfma_f32_16x16x32_bf16 v[68:71], v[184:187], v[172:175], v[68:71]\n\t"
      "v_mfma_f32_16x16x32_bf16 v[92:95], v[188:191], v[172:175], v[92:95]\n\t"
      "v_mfma_f32_16x16x32_bf16 v[104:107], v[192:195], v[168:171], v[104:107]\n\t"
      "EVEN0_%=:\n\t"
      "s_waitcnt lgkmcnt(0)\n\t"
      "v_mfma_f32_16x16x32_bf16 v[0:3], v[108:111], v[132:135], v[0:3]\n\t"
      "ds_read_b64_tr_b16 v[152:153], v226 offset:20480\n\t"
      "v_mfma_f32_16x16x32_bf16 v[24:27], v[108:111], v[136:139], v[24:27]\n\t"
      "ds_read_b64_tr_b16 v[154:155], v226 offset:20992\n\t"
      "v_mfma_f32_16x16x32_bf16 v[48:51], v[108:111], v[140:143], v[48:51]\n\t"
      "ds_read_b128 v[176:179], v248\n\t"
      "v_mfma_f32_16x16x32_bf16 v[72:75], v[108:111], v[144:147], v[72:75]\n\t"
      "ds_read_b128 v[180:183], v248 offset:5120\n\t"
      "v_mfma_f32_16x16x32_bf16 v[4:7], v[112:115], v[132:135], v[4:7]\n\t"
      "v_mfma_f32_16x16x32_bf16 v[28:31], v[112:115], v[136:139], v[28:31]\n\t"
      "ds_read_b128 v[184:187], v248 offset:10240\n\t"
      "v_mfma_f32_16x16x32_bf16 v[52:55], v[112:115], v[140:143], v[52:55]\n\t"
      "ds_read_b128 v[188:191], v248 offset:15360\n\t"
      "v_mfma_f32_16x16x32_bf16 v[76:79], v[112:115], v[144:147], v[76:79]\n\t"
      "ds_read_b64_tr_b16 v[156:157], v227 offset:20480\n\t"
      "v_mfma_f32_16x16x32_bf16 v[96:99], v[108:111], v[148:151], v[96:99]\n\t"
      "v_mfma_f32_16x16x32_bf16 v[8:11], v[116:119], v[132:135], v[8:11]\n\t"
      "ds_read_b64_tr_b16 v[158:159], v227 offset:20992\n\t"
      "v_mfma_f32_16x16x32_bf16 v[32:35], v[116:119], v[136:139], v[32:35]\n\t"
      "ds_read_b128 v[192:195], v249\n\t"
      "v_mfma_f32_16x16x32_bf16 v[56:59], v[116:119], v[140:143], v[56:59]\n\t"
      "ds_read_b64_tr_b16 v[160:161], v226 offset:21504\n\t"
      "v_mfma_f32_16x16x32_bf16 v[80:83], v[116:119], v[144:147], v[80:83]\n\t"
      "ds_read_b64_tr_b16 v[162:163], v226 offset:22016\n\t"
      "v_mfma_f32_16x16x32_bf16 v[12:15], v[120:123], v[132:135], v[12:15]\n\t"
      "v_mfma_f32_16x16x32_bf16 v[36:39], v[120:123], v[136:139], v[36:39]\n\t"
      "ds_read_b64_tr_b16 v[164:165], v227 offset:21504\n\t"
      "v_mfma_f32_16x16x32_bf16 v[60:63], v[120:123], v[140:143], v[60:63]\n\t"
      "ds_read_b64_tr_b16 v[166:167], v227 offset:22016\n\t"
      "v_mfma_f32_16x16x32_bf16 v[84:87], v[120:123], v[144:147], v[84:87]\n\t"
      "ds_read_b64_tr_b16 v[168:169], v226 offset:22528\n\t"
      "v_mfma_f32_16x16x32_bf16 v[100:103], v[116:119], v[148:151], v[100:103]\n\t"
      "v_mfma_f32_16x16x32_bf16 v[16:19], v[132:135], v[124:127], v[16:19]\n\t"
      "ds_read_b64_tr_b16 v[170:171], v226 offset:23040\n\t"
      "v_mfma_f32_16x16x32_bf16 v[40:43], v[136:139], v[124:127], v[40:43]\n\t"
      "ds_read_b64_tr_b16 v[172:173], v227 offset:22528\n\t"
      "v_mfma_f32_16x16x32_bf16 v[64:67], v[140:143], v[124:127], v[64:67]\n\t"
      "ds_read_b64_tr_b16 v[174:175], v227 offset:23040\n\t"
      "s_cmp_eq_u32 s47, 4\n\t"
      "s_cselect_b32 s46, -2, 1\n\t"
      "v_mfma_f32_16x16x32_bf16 v[88:91], v[144:147], v[124:127], v[88:91]\n\t"
      "s_cmp_eq_u32 s47, 1\n\t"
      "s_cselect_b32 s46, -2, s46\n\t"
      "v_mfma_f32_16x16x32_bf16 v[20:23], v[132:135], v[128:131], v[20:23]\n\t"
      "s_mul_i32 s46, s46, 43008\n\t"
      "v_mfma_f32_16x16x32_bf16 v[44:47], v[136:139], v[128:131], v[44:47]\n\t"
      "v_mfma_f32_16x16x32_bf16 v[68:71], v[140:143], v[128:131], v[68:71]\n\t"
      "v_mfma_f32_16x16x32_bf16 v[92:95], v[144:147], v[128:131], v[92:95]\n\t"
      "s_sub_u32 s47, s47, 1\n\t"
      "v_mfma_f32_16x16x32_bf16 v[104:107], v[148:151], v[124:127], v[104:107]\n\t"
      "s_cbranch_scc0 ODD0_%=\n\t"
      "s_waitcnt lgkmcnt(0)\n\t"
      "v_mfma_f32_16x16x32_bf16 v[96:99], v[152:155], v[192:195], v[96:99]\n\t"
      "ds_read_b32 v212, v215\n\t"
      "v_mfma_f32_16x16x32_bf16 v[100:103], v[160:163], v[192:195], v[100:103]\n\t"
      "ds_read2_b32 v[134:135], v234 offset0:80 offset1:80\n\t"
      "v_mfma_f32_16x16x32_bf16 v[104:107], v[192:195], v[168:171], v[104:107]\n\t"
      "ds_read_b128 v[148:151], v233 offset:320\n\t"
      "v_mfma_f32_16x16x32_bf16 v[80:83], v[160:163], v[188:191], v[80:83]\n\t"
      "ds_read2_b32 v[248:249], v234 offset0:0 offset1:0\n\t"
      "v_mfma_f32_16x16x32_bf16 v[84:87], v[164:167], v[188:191], v[84:87]\n\t"
      "ds_read_b128 v[196:199], v233\n\t"
      "v_mfma_f32_16x16x32_bf16 v[88:91], v[188:191], v[168:171], v[88:91]\n\t"
      "ds_read2_b32 v[132:133], v234 offset0:64 offset1:64\n\t"
      "v_mfma_f32_16x16x32_bf16 v[92:95], v[188:191], v[172:175], v[92:95]\n\t"
      "s_mov_b64 exec, s[44:45]\n\t"
      "ds_write_b128 v230, v[96:99]\n\t"
      "ds_write_b128 v230, v[100:103] offset:128\n\t"
      "s_mov_b64 exec, 0xffff\n\t"
      "ds_write_b32 v231, v104 offset:256\n\t"
      "ds_write_b32 v213, v213\n\t"
      "s_mov_b64 exec, -1\n\t"
      "v_mfma_f32_16x16x32_bf16 v[56:59], v[160:163], v[184:187], v[56:59]\n\t"
      "ds_read2_b32 v[130:131], v234 offset0:48 offset1:48\n\t"
      "v_mfma_f32_16x16x32_bf16 v[60:63], v[164:167], v[184:187], v[60:63]\n\t"
      "ds_read2_b32 v[128:129], v234 offset0:32 offset1:32\n\t"
      "v_mfma_f32_16x16x32_bf16 v[64:67], v[184:187], v[168:171], v[64:67]\n\t"
      "s_waitcnt lgkmcnt(0)\n\t"
      "v_mfma_f32_16x16x32_bf16 v[68:71], v[184:187], v[172:175], v[68:71]\n\t"
      "ds_read_b32 v226, v232\n\t"
      "ds_read_b128 v[116:119], v228 offset:128\n\t"
      "v_mfma_f32_16x16x32_bf16 v[32:35], v[160:163], v[180:183], v[32:35]\n\t"
      "ds_read_b128 v[120:123], v228 offset:192\n\t"
      "v_mfma_f32_16x16x32_bf16 v[36:39], v[164:167], v[180:183], v[36:39]\n\t"
      "ds_read2_b32 v[250:251], v229 offset0:64 offset1:64\n\t"
      "v_mfma_f32_16x16x32_bf16 v[40:43], v[180:183], v[168:171], v[40:43]\n\t"
      "ds_read2_b32 v[252:253], v229 offset0:80 offset1:80\n\t"
      "v_mfma_f32_16x16x32_bf16 v[44:47], v[180:183], v[172:175], v[44:47]\n\t"
      "ds_read_b128 v[124:127], v230 offset:128\n\t"
      "v_mfma_f32_16x16x32_bf16 v[8:11], v[160:163], v[176:179], v[8:11]\n\t"
      "ds_read2_b32 v[224:225], v231 offset0:64 offset1:64\n\t"
      "v_mfma_f32_16x16x32_bf16 v[12:15], v[164:167], v[176:179], v[12:15]\n\t"
      "v_mfma_f32_16x16x32_bf16 v[16:19], v[176:179], v[168:171], v[16:19]\n\t"
      "s_waitcnt lgkmcnt(6)\n\t"
      "v_min_u32_e32 v226, v226, v212\n\t"
      "v_cmp_eq_u32_e32 vcc, 0, v226\n\t"
      "v_mfma_f32_16x16x32_bf16 v[20:23], v[176:179], v[172:175], v[20:23]\n\t"
      "LOOPX_%=:\n\t"
      "s_cbranch_vccnz XAR_%=\n\t"
      "XAB_%=:\n\t"
      "v_mfma_f32_16x16x32_bf16 v[72:75], v[152:155], v[188:191], v[72:75]\n\t"
      "s_waitcnt lgkmcnt(4)\n\t"
      "v_fmac_f32_e32 v80, v134, v116\n\t"
      "v_fmac_f32_e32 v81, v134, v117\n\t"
      "v_mfma_f32_16x16x32_bf16 v[76:79], v[156:159], v[188:191], v[76:79]\n\t"
      "v_fmac_f32_e32 v82, v134, v118\n\t"
      "v_fmac_f32_e32 v83, v134, v119\n\t"
      "v_mfma_f32_16x16x32_bf16 v[48:51], v[152:155], v[184:187], v[48:51]\n\t"
      "v_fmac_f32_e32 v84, v134, v120\n\t"
      "v_fmac_f32_e32 v85, v134, v121\n\t"
      "v_mfma_f32_16x16x32_bf16 v[52:55], v[156:159], v[184:187], v[52:55]\n\t"
      "v_fmac_f32_e32 v86, v134, v122\n\t"
      "v_fmac_f32_e32 v87, v134, v123\n\t"
      "v_mfma_f32_16x16x32_bf16 v[24:27], v[152:155], v[180:183], v[24:27]\n\t"
      "v_cvt_pk_bf16_f32 v80, v80, v81\n\t"
      "v_cvt_pk_bf16_f32 v81, v82, v83\n\t"
      "v_mfma_f32_16x16x32_bf16 v[28:31], v[156:159], v[180:183], v[28:31]\n\t"
      "v_cvt_pk_bf16_f32 v82, v84, v85\n\t"
      "v_cvt_pk_bf16_f32 v83, v86, v87\n\t"
      "v_mfma_f32_16x16x32_bf16 v[0:3], v[152:155], v[176:179], v[0:3]\n\t"
      "s_waitcnt lgkmcnt(2)\n\t"
      "v_fmac_f32_e32 v88, v148, v250\n\t"
      "v_fmac_f32_e32 v89, v149, v250\n\t"
      "v_mfma_f32_16x16x32_bf16 v[4:7], v[156:159], v[176:179], v[4:7]\n\t"
      "v_fmac_f32_e32 v90, v150, v250\n\t"
      "v_fmac_f32_e32 v91, v151, v250\n\t"
      "ds_write_b128 v236, v[80:83] offset:2048\n\t"
      "v_fmac_f32_e32 v92, v148, v252\n\t"
      "v_fmac_f32_e32 v93, v149, v252\n\t"
      "v_fmac_f32_e32 v94, v150, v252\n\t"
      "v_fmac_f32_e32 v95, v151, v252\n\t"
      "v_cvt_pk_bf16_f32 v164, v88, v89\n\t"
      "v_cvt_pk_bf16_f32 v165, v90, v91\n\t"
      "v_cvt_pk_bf16_f32 v174, v92, v93\n\t"
      "v_cvt_pk_bf16_f32 v175, v94, v95\n\t"
      "ds_read_b128 v[108:111], v228\n\t"
      "ds_read_b128 v[112:115], v228 offset:64\n\t"
      "s_waitcnt lgkmcnt(3)\n\t"
      "v_pk_fma_f32 v[100:101], v[248:249], v[124:125], v[100:101]\n\t"
      "v_pk_fma_f32 v[104:105], v[196:197], v[224:225], v[104:105]\n\t"
      "v_pk_fma_f32 v[102:103], v[248:249], v[126:127], v[102:103]\n\t"
      "v_pk_fma_f32 v[106:107], v[198:199], v[224:225], v[106:107]\n\t"
      "ds_write_b64 v237, v[164:165] offset:3072\n\t"
      "ds_write_b64 v237, v[174:175] offset:3584\n\t"
      "v_cvt_pk_bf16_f32 v100, v100, v101\n\t"
      "v_cvt_pk_bf16_f32 v101, v102, v103\n\t"
      "v_cvt_pk_bf16_f32 v102, v104, v105\n\t"
      "v_cvt_pk_bf16_f32 v103, v106, v107\n\t"
      "v_pk_fma_f32 v[56:57], v[132:133], v[116:117], v[56:57]\n\t"
      "v_pk_fma_f32 v[58:59], v[132:133], v[118:119], v[58:59]\n\t"
      "v_pk_fma_f32 v[60:61], v[132:133], v[120:121], v[60:61]\n\t"
      "v_pk_fma_f32 v[62:63], v[132:133], v[122:123], v[62:63]\n\t"
      "ds_write_b64 v238, v[100:101]\n\t"
      "ds_write_b64 v214, v[102:103]\n\t"
      "ds_write_b32 v213, v213 offset:4\n\t"
      "v_cvt_pk_bf16_f32 v56, v56, v57\n\t"
      "v_cvt_pk_bf16_f32 v57, v58, v59\n\t"
      "v_cvt_pk_bf16_f32 v58, v60, v61\n\t"
      "v_cvt_pk_bf16_f32 v59, v62, v63\n\t"
      "ds_read_b128 v[144:147], v233 offset:256\n\t"
      "ds_read_b128 v[140:143], v233 offset:192\n\t"
      "ds_read_b128 v[136:139], v233 offset:128\n\t"
      "v_pk_fma_f32 v[32:33], v[130:131], v[116:117], v[32:33]\n\t"
      "v_pk_fma_f32 v[34:35], v[130:131], v[118:119], v[34:35]\n\t"
      "v_pk_fma_f32 v[36:37], v[130:131], v[120:121], v[36:37]\n\t"
      "v_pk_fma_f32 v[38:39], v[130:131], v[122:123], v[38:39]\n\t"
      "v_pk_fma_f32 v[8:9], v[128:129], v[116:117], v[8:9]\n\t"
      "v_pk_fma_f32 v[10:11], v[128:129], v[118:119], v[10:11]\n\t"
      "v_pk_fma_f32 v[12:13], v[128:129], v[120:121], v[12:13]\n\t"
      "v_pk_fma_f32 v[14:15], v[128:129], v[122:123], v[14:15]\n\t"
      "s_waitcnt lgkmcnt(8)\n\t"
      "s_nop 0\n\t"
      "v_pk_fma_f32 v[72:73], v[134:135], v[108:109], v[72:73]\n\t"
      "v_pk_fma_f32 v[74:75], v[134:135], v[110:111], v[74:75]\n\t"
      "v_pk_fma_f32 v[76:77], v[134:135], v[112:113], v[76:77]\n\t"
      "v_pk_fma_f32 v[78:79], v[134:135], v[114:115], v[78:79]\n\t"
      "v_pk_fma_f32 v[48:49], v[132:133], v[108:109], v[48:49]\n\t"
      "v_pk_fma_f32 v[50:51], v[132:133], v[110:111], v[50:51]\n\t"
      "v_pk_fma_f32 v[52:53], v[132:133], v[112:113], v[52:53]\n\t"
      "v_pk_fma_f32 v[54:55], v[132:133], v[114:115], v[54:55]\n\t"
      "s_waitcnt lgkmcnt(3)\n\t"
      "ds_read2_b32 v[204:205], v235 offset0:0 offset1:16\n\t"
      "ds_read2_b32 v[206:207], v235 offset0:32 offset1:48\n\t"
      "v_pk_fma_f32 v[24:25], v[130:131], v[108:109], v[24:25]\n\t"
      "v_pk_fma_f32 v[26:27], v[130:131], v[110:111], v[26:27]\n\t"
      "v_pk_fma_f32 v[28:29], v[130:131], v[112:113], v[28:29]\n\t"
      "v_pk_fma_f32 v[30:31], v[130:131], v[114:115], v[30:31]\n\t"
      "ds_read2_b32 v[208:209], v254 offset0:0 offset1:16\n\t"
      "ds_read2_b32 v[210:211], v254 offset0:32 offset1:48\n\t"
      "v_pk_fma_f32 v[0:1], v[128:129], v[108:109], v[0:1]\n\t"
      "v_pk_fma_f32 v[2:3], v[128:129], v[110:111], v[2:3]\n\t"
      "v_pk_fma_f32 v[4:5], v[128:129], v[112:113], v[4:5]\n\t"
      "v_pk_fma_f32 v[6:7], v[128:129], v[114:115], v[6:7]\n\t"
      "ds_read2_b32 v[200:201], v234 offset0:192 offset1:208\n\t"
      "ds_read2_b32 v[202:203], v234 offset0:224 offset1:240\n\t"
      "ds_read_b32 v226, v232 offset:4\n\t"
      "ds_read_b128 v[152:155], v236\n\t"
      "ds_read_b64 v[156:157], v237 offset:1024\n\t"
      "ds_read_b64 v[166:167], v237 offset:1536\n\t"
      "s_waitcnt lgkmcnt(12)\n\t"
      "v_pk_fma_f32 v[64:65], v[144:145], v[250:251], v[64:65]\n\t"
      "v_pk_fma_f32 v[66:67], v[146:147], v[250:251], v[66:67]\n\t"
      "v_pk_fma_f32 v[68:69], v[144:145], v[252:253], v[68:69]\n\t"
      "v_pk_fma_f32 v[70:71], v[146:147], v[252:253], v[70:71]\n\t"
      "s_waitcnt lgkmcnt(11)\n\t"
      "v_pk_fma_f32 v[40:41], v[140:141], v[250:251], v[40:41]\n\t"
      "v_pk_fma_f32 v[42:43], v[142:143], v[250:251], v[42:43]\n\t"
      "v_pk_fma_f32 v[44:45], v[140:141], v[252:253], v[44:45]\n\t"
      "v_pk_fma_f32 v[46:47], v[142:143], v[252:253], v[46:47]\n\t"
      "s_waitcnt lgkmcnt(10)\n\t"
      "v_pk_fma_f32 v[16:17], v[136:137], v[250:251], v[16:17]\n\t"
      "v_pk_fma_f32 v[18:19], v[138:139], v[250:251], v[18:19]\n\t"
      "v_pk_fma_f32 v[20:21], v[136:137], v[252:253], v[20:21]\n\t"
      "v_pk_fma_f32 v[22:23], v[138:139], v[252:253], v[22:23]\n\t"
      "ds_read_b64 v[250:251], v220 offset:8848\n\t"
      "ds_read_b64 v[196:197], v220\n\t"
      "v_cvt_pk_bf16_f32 v72, v72, v73\n\t"
      "v_cvt_pk_bf16_f32 v73, v74, v75\n\t"
      "v_cvt_pk_bf16_f32 v74, v76, v77\n\t"
      "v_cvt_pk_bf16_f32 v75, v78, v79\n\t"
      "v_cndmask_b32_e64 v74, v74, 0, s[42:43]\n\t"
      "v_cndmask_b32_e64 v75, v75, 0, s[42:43]\n\t"
      "v_cvt_pk_bf16_f32 v48, v48, v49\n\t"
      "v_cvt_pk_bf16_f32 v49, v50, v51\n\t"
      "v_cvt_pk_bf16_f32 v50, v52, v53\n\t"
      "v_cvt_pk_bf16_f32 v51, v54, v55\n\t"
      "v_cndmask_b32_e64 v50, v50, 0, s[42:43]\n\t"
      "v_cndmask_b32_e64 v51, v51, 0, s[42:43]\n\t"
      "v_cvt_pk_bf16_f32 v32, v32, v33\n\t"
      "v_cvt_pk_bf16_f32 v33, v34, v35\n\t"
      "v_cvt_pk_bf16_f32 v34, v36, v37\n\t"
      "v_cvt_pk_bf16_f32 v35, v38, v39\n\t"
      "v_cvt_pk_bf16_f32 v8, v8, v9\n\t"
      "v_cvt_pk_bf16_f32 v9, v10, v11\n\t"
      "v_cvt_pk_bf16_f32 v10, v12, v13\n\t"
      "v_cvt_pk_bf16_f32 v11, v14, v15\n\t"
      "v_mfma_f32_16x16x32_bf16 v[76:79], v[56:59], v[72:75], 0\n\t"
      "v_cvt_pk_bf16_f32 v24, v24, v25\n\t"
      "v_cvt_pk_bf16_f32 v25, v26, v27\n\t"
      "v_mfma_f32_16x16x32_bf16 v[84:87], v[80:83], v[72:75], 0\n\t"
      "v_cvt_pk_bf16_f32 v26, v28, v29\n\t"
      "v_cvt_pk_bf16_f32 v27, v30, v31\n\t"
      "v_mfma_f32_16x16x32_bf16 v[52:55], v[32:35], v[48:51], 0\n\t"
      "v_cndmask_b32_e64 v26, v26, 0, s[42:43]\n\t"
      "v_cndmask_b32_e64 v27, v27, 0, s[42:43]\n\t"
      "v_mfma_f32_16x16x32_bf16 v[60:63], v[56:59], v[48:51], 0\n\t"
      "s_waitcnt lgkmcnt(5)\n\t"
      "v_cmp_eq_u32_e32 vcc, 0, v226\n\t"
      "v_cvt_pk_bf16_f32 v0, v0, v1\n\t"
      "v_mfma_f32_16x16x32_bf16 v[28:31], v[8:11], v[24:27], 0\n\t"
      "v_cvt_pk_bf16_f32 v1, v2, v3\n\t"
      "v_cvt_pk_bf16_f32 v2, v4, v5\n\t"
      "v_mfma_f32_16x16x32_bf16 v[36:39], v[32:35], v[24:27], 0\n\t"
      "v_cvt_pk_bf16_f32 v3, v6, v7\n\t"
      "v_cndmask_b32_e64 v2, v2, 0, s[42:43]\n\t"
      "v_cndmask_b32_e64 v3, v3, 0, s[42:43]\n\t"
      "s_cbranch_vccnz XBR_%=\n\t"
      "XBB_%=:\n\t"
      "v_cvt_pk_bf16_f32 v162, v64, v65\n\t"
      "s_waitcnt lgkmcnt(4)\n\t"
      "v_mfma_f32_16x16x32_bf16 v[4:7], v[152:155], v[0:3], v[244:247]\n\t"
      "v_cvt_pk_bf16_f32 v163, v66, v67\n\t"
      "v_cvt_pk_bf16_f32 v172, v68, v69\n\t"
      "v_mfma_f32_16x16x32_bf16 v[12:15], v[8:11], v[0:3], 0\n\t"
      "v_cvt_pk_bf16_f32 v173, v70, v71\n\t"
      "v_cvt_pk_bf16_f32 v160, v40, v41\n\t"
      "v_cndmask_b32_e64 v76, v76, v77, s[36:37]\n\t"
      "v_cndmask_b32_e64 v84, v84, v85, s[36:37]\n\t"
      "v_cndmask_b32_e64 v78, v78, v79, s[36:37]\n\t"
      "v_cndmask_b32_e64 v86, v86, v87, s[36:37]\n\t"
      "v_cndmask_b32_e64 v179, v76, v84, s[38:39]\n\t"
      "v_cndmask_b32_e64 v183, v78, v86, s[40:41]\n\t"
      "v_cndmask_b32_e64 v52, v52, v53, s[36:37]\n\t"
      "v_cndmask_b32_e64 v60, v60, v61, s[36:37]\n\t"
      "v_cndmask_b32_e64 v54, v54, v55, s[36:37]\n\t"
      "v_cndmask_b32_e64 v62, v62, v63, s[36:37]\n\t"
      "v_cndmask_b32_e64 v178, v52, v60, s[38:39]\n\t"
      "v_cndmask_b32_e64 v182, v54, v62, s[40:41]\n\t"
      "v_cndmask_b32_e64 v28, v28, v29, s[36:37]\n\t"
      "v_cndmask_b32_e64 v36, v36, v37, s[36:37]\n\t"
      "v_cndmask_b32_e64 v30, v30, v31, s[36:37]\n\t"
      "v_cndmask_b32_e64 v38, v38, v39, s[36:37]\n\t"
      "v_cndmask_b32_e64 v177, v28, v36, s[38:39]\n\t"
      "v_cndmask_b32_e64 v181, v30, v38, s[40:41]\n\t"
      "v_cndmask_b32_e64 v4, v4, v5, s[36:37]\n\t"
      "v_cndmask_b32_e64 v12, v12, v13, s[36:37]\n\t"
      "v_cndmask_b32_e64 v6, v6, v7, s[36:37]\n\t"
      "v_cndmask_b32_e64 v14, v14, v15, s[36:37]\n\t"
      "v_cndmask_b32_e64 v176, v4, v12, s[38:39]\n\t"
      "v_cndmask_b32_e64 v180, v6, v14, s[40:41]\n\t"
      "v_pk_mul_f32 v[178:179], v[178:179], v[206:207]\n\t"
      "v_pk_mul_f32 v[182:183], v[182:183], v[210:211]\n\t"
      "v_pk_mul_f32 v[176:177], v[176:177], v[204:205]\n\t"
      "v_pk_mul_f32 v[180:181], v[180:181], v[208:209]\n\t"
      "v_max_f32_e32 v219, v179, v183\n\t"
      "v_max_f32_e32 v217, v177, v181\n\t"
      "v_max_f32_e32 v218, v178, v182\n\t"
      "v_max_f32_e32 v216, v176, v180\n\t"
      "v_cvt_pk_bf16_f32 v161, v42, v43\n\t"
      "v_permlane32_swap_b32_e32 v217, v219\n\t"
      "v_permlane32_swap_b32_e32 v216, v218\n\t"
      "v_max_f32_e32 v217, v217, v219\n\t"
      "v_max_f32_e32 v216, v216, v218\n\t"
      "v_cvt_pk_bf16_f32 v170, v44, v45\n\t"
      "v_cvt_pk_bf16_f32 v171, v46, v47\n\t"
      "v_permlane16_swap_b32_e32 v216, v217\n\t"
      "v_max_f32_e32 v216, v216, v217\n\t"
      "v_mov_b32_e32 v217, v216\n\t"
      "v_cvt_pk_bf16_f32 v158, v16, v17\n\t"
      "s_waitcnt lgkmcnt(0)\n\t"
      "v_xad_u32 v252, s13, v197, v221\n\t"
      "v_permlane16_swap_b32_e32 v216, v217\n\t"
      "v_mov_b32_e32 v218, v216\n\t"
      "v_mov_b32_e64 v219, v217\n\t"
      "v_xad_u32 v196, s52, v196, v239\n\t"
      "v_permlane32_swap_b32_e32 v216, v218\n\t"
      "v_permlane32_swap_b32_e32 v217, v219\n\t"
      "v_pk_add_f32 v[178:179], v[178:179], v[218:219] neg_lo:[0,1] neg_hi:[0,1]\n\t"
      "v_pk_add_f32 v[182:183], v[182:183], v[218:219] neg_lo:[0,1] neg_hi:[0,1]\n\t"
      "v_pk_add_f32 v[176:177], v[176:177], v[216:217] neg_lo:[0,1] neg_hi:[0,1]\n\t"
      "v_pk_add_f32 v[180:181], v[180:181], v[216:217] neg_lo:[0,1] neg_hi:[0,1]\n\t"
      "v_pk_mul_f32 v[178:179], v[178:179], v[202:203]\n\t"
      "v_pk_mul_f32 v[182:183], v[182:183], v[202:203]\n\t"
      "v_pk_mul_f32 v[176:177], v[176:177], v[200:201]\n\t"
      "v_pk_mul_f32 v[180:181], v[180:181], v[200:201]\n\t"
      "v_exp_f32_e32 v179, v179\n\t"
      "v_exp_f32_e32 v183, v183\n\t"
      "v_exp_f32_e32 v178, v178\n\t"
      "v_exp_f32_e32 v182, v182\n\t"
      "v_exp_f32_e32 v177, v177\n\t"
      "v_exp_f32_e32 v181, v181\n\t"
      "v_exp_f32_e32 v176, v176\n\t"
      "v_exp_f32_e32 v180, v180\n\t"
      "v_xor_b32_e64 v253, s12, v197\n\t"
      "s_mov_b64 exec, 1\n\t"
      "global_store_dwordx2 v253, v[250:251], s[10:11] sc1\n\t"
      "s_mov_b64 exec, -1\n\t"
      "v_pk_add_f32 v[218:219], v[178:179], v[182:183]\n\t"
      "v_pk_add_f32 v[216:217], v[176:177], v[180:181]\n\t"
      "v_cvt_pk_bf16_f32 v159, v18, v19\n\t"
      "v_cvt_pk_bf16_f32 v168, v20, v21\n\t"
      "v_permlane32_swap_b32_e32 v217, v219\n\t"
      "v_permlane32_swap_b32_e32 v216, v218\n\t"
      "v_add_f32_e32 v217, v217, v219\n\t"
      "v_add_f32_e32 v216, v216, v218\n\t"
      "v_pk_mul_f32 v[178:179], v[178:179], v[206:207]\n\t"
      "v_pk_mul_f32 v[182:183], v[182:183], v[210:211]\n\t"
      "v_permlane16_swap_b32_e32 v216, v217\n\t"
      "v_add_f32_e32 v216, v216, v217\n\t"
      "v_rcp_f32_e32 v216, v216\n\t"
      "v_pk_mul_f32 v[176:177], v[176:177], v[204:205]\n\t"
      "v_mov_b32_e32 v217, v216\n\t"
      "v_pk_mul_f32 v[180:181], v[180:181], v[208:209]\n\t"
      "v_cvt_pk_bf16_f32 v169, v22, v23\n\t"
      "v_permlane16_swap_b32_e32 v216, v217\n\t"
      "v_mov_b32_e32 v218, v216\n\t"
      "v_mov_b32_e32 v219, v217\n\t"
      "s_nop 0\n\t"
      "v_permlane32_swap_b32_e32 v216, v218\n\t"
      "v_permlane32_swap_b32_e32 v217, v219\n\t"
      "v_pk_mul_f32 v[178:179], v[178:179], v[218:219]\n\t"
      "v_pk_mul_f32 v[182:183], v[182:183], v[218:219]\n\t"
      "v_pk_mul_f32 v[176:177], v[176:177], v[216:217]\n\t"
      "v_pk_mul_f32 v[180:181], v[180:181], v[216:217]\n\t"
      "v_cvt_pk_bf16_f32 v179, v179, v183\n\t"
      "v_cvt_pk_bf16_f32 v178, v178, v182\n\t"
      "v_cvt_pk_bf16_f32 v177, v177, v181\n\t"
      "v_cvt_pk_bf16_f32 v176, v176, v180\n\t"
      "v_perm_b32 v72, v179, v179, v240\n\t"
      "v_perm_b32 v73, v179, v179, v241\n\t"
      "v_perm_b32 v74, v179, v179, v242\n\t"
      "v_perm_b32 v75, v179, v179, v243\n\t"
      "v_perm_b32 v48, v178, v178, v240\n\t"
      "v_perm_b32 v49, v178, v178, v241\n\t"
      "v_mfma_f32_16x16x32_bf16 v[88:91], v[162:165], v[72:75], 0\n\t"
      "v_perm_b32 v50, v178, v178, v242\n\t"
      "v_perm_b32 v51, v178, v178, v243\n\t"
      "v_mfma_f32_16x16x32_bf16 v[92:95], v[172:175], v[72:75], 0\n\t"
      "v_perm_b32 v24, v177, v177, v240\n\t"
      "v_perm_b32 v25, v177, v177, v241\n\t"
      "v_mfma_f32_16x16x32_bf16 v[64:67], v[160:163], v[48:51], 0\n\t"
      "v_perm_b32 v26, v177, v177, v242\n\t"
      "v_perm_b32 v27, v177, v177, v243\n\t"
      "v_mfma_f32_16x16x32_bf16 v[68:71], v[170:173], v[48:51], 0\n\t"
      "v_perm_b32 v0, v176, v176, v240\n\t"
      "v_perm_b32 v1, v176, v176, v241\n\t"
      "v_mfma_f32_16x16x32_bf16 v[40:43], v[158:161], v[24:27], 0\n\t"
      "v_perm_b32 v2, v176, v176, v242\n\t"
      "v_perm_b32 v3, v176, v176, v243\n\t"
      "v_mfma_f32_16x16x32_bf16 v[44:47], v[168:171], v[24:27], 0\n\t"
      "s_nop 0\n\t"
      "v_mfma_f32_16x16x32_bf16 v[16:19], v[156:159], v[0:3], 0\n\t"
      "v_cvt_pk_bf16_f32 v80, v88, v89\n\t"
      "v_cvt_pk_bf16_f32 v81, v90, v91\n\t"
      "v_mfma_f32_16x16x32_bf16 v[20:23], v[166:169], v[0:3], 0\n\t"
      "v_cvt_pk_bf16_f32 v82, v92, v93\n\t"
      "v_cvt_pk_bf16_f32 v83, v94, v95\n\t"
      "global_store_dwordx4 v196, v[80:83], s[60:61] sc1\n\t"
      "v_add3_u32 v219, v80, v81, v82\n\t"
      "v_add_u32_e32 v219, v219, v83\n\t"
      "v_cvt_pk_bf16_f32 v56, v64, v65\n\t"
      "v_cvt_pk_bf16_f32 v57, v66, v67\n\t"
      "v_cvt_pk_bf16_f32 v58, v68, v69\n\t"
      "v_cvt_pk_bf16_f32 v59, v70, v71\n\t"
      "global_store_dwordx4 v196, v[56:59], s[58:59] sc1\n\t"
      "v_add3_u32 v218, v56, v57, v58\n\t"
      "v_add_u32_e64 v218, v218, v59\n\t"
      "v_cvt_pk_bf16_f32 v32, v40, v41\n\t"
      "v_cvt_pk_bf16_f32 v33, v42, v43\n\t"
      "v_cvt_pk_bf16_f32 v34, v44, v45\n\t"
      "v_cvt_pk_bf16_f32 v35, v46, v47\n\t"
      "global_store_dwordx4 v196, v[32:35], s[56:57] sc1\n\t"
      "v_add3_u32 v217, v32, v33, v34\n\t"
      "v_add_u32_e64 v217, v217, v35\n\t"
      "v_cvt_pk_bf16_f32 v8, v16, v17\n\t"
      "v_cvt_pk_bf16_f32 v9, v18, v19\n\t"
      "v_cvt_pk_bf16_f32 v10, v20, v21\n\t"
      "v_cvt_pk_bf16_f32 v11, v22, v23\n\t"
      "global_store_dwordx4 v196, v[8:11], s[4:5] sc1\n\t"
      "v_add3_u32 v216, v8, v9, v10\n\t"
      "v_add_u32_e32 v216, v216, v11\n\t"
      "v_permlane32_swap_b32_e32 v217, v219\n\t"
      "v_add_u32_e32 v217, v217, v219\n\t"
      "v_permlane32_swap_b32_e32 v216, v218\n\t"
      "v_add_u32_e64 v216, v216, v218\n\t"
      "s_nop 1\n\t"
      "v_permlane16_swap_b32_e32 v216, v217\n\t"
      "v_add3_u32 v216, v216, v217, v250\n\t"
      "global_store_dword v252, v216, s[10:11] sc1\n\t"
      "s_barrier\n\t"
      "s_nop 1\n\t"
      "s_branch CEND_%=\n\t"
      "LOOP1_%=:\n\t"
      "s_waitcnt vmcnt(0)\n\t"
      "s_barrier\n\t"
      "ds_read_b64_tr_b16 v[108:109], v226\n\t"
      "ds_read_b64_tr_b16 v[110:111], v226 offset:512\n\t"
      "ds_read_b128 v[132:135], v224\n\t"
      "ds_read_b128 v[136:139], v224 offset:5120\n\t"
      "ds_read_b128 v[140:143], v224 offset:10240\n\t"
      "ds_read_b128 v[144:147], v224 offset:15360\n\t"
      "ds_read_b64_tr_b16 v[112:113], v227\n\t"
      "ds_read_b64_tr_b16 v[114:115], v227 offset:512\n\t"
      "ds_read_b128 v[148:151], v225\n\t"
      "ds_read_b64_tr_b16 v[116:117], v226 offset:1024\n\t"
      "ds_read_b64_tr_b16 v[118:119], v226 offset:1536\n\t"
      "ds_read_b64_tr_b16 v[120:121], v227 offset:1024\n\t"
      "ds_read_b64_tr_b16 v[122:123], v227 offset:1536\n\t"
      "ds_read_b64_tr_b16 v[124:125], v226 offset:2048\n\t"
      "ds_read_b64_tr_b16 v[126:127], v226 offset:2560\n\t"
      "ds_read_b64_tr_b16 v[128:129], v227 offset:2048\n\t"
      "ds_read_b64_tr_b16 v[130:131], v227 offset:2560\n\t"
      "s_branch EVEN1_%=\n\t"
      "s_waitcnt lgkmcnt(0)\n\t"
      ".p2align 6\n\t"
      "ODD1_%=:\n\t"
      "s_waitcnt lgkmcnt(0)\n\t"
      "v_mfma_f32_16x16x32_bf16 v[0:3], v[152:155], v[176:179], v[0:3]\n\t"
      "v_add_u32_e32 v224, s46, v224\n\t"
      "v_add_u32_e32 v226, s46, v226\n\t"
      "v_mfma_f32_16x16x32_bf16 v[24:27], v[152:155], v[180:183], v[24:27]\n\t"
      "v_add_u32_e32 v227, s46, v227\n\t"
      "v_add_u32_e32 v225, s46, v225\n\t"
      "v_mfma_f32_16x16x32_bf16 v[48:51], v[152:155], v[184:187], v[48:51]\n\t"
      "s_barrier\n\t"
      "ds_read_b64_tr_b16 v[108:109], v226\n\t"
      "v_mfma_f32_16x16x32_bf16 v[72:75], v[152:155], v[188:191], v[72:75]\n\t"
      "ds_read_b64_tr_b16 v[110:111], v226 offset:512\n\t"
      "v_mfma_f32_16x16x32_bf16 v[4:7], v[156:159], v[176:179], v[4:7]\n\t"
      "ds_read_b128 v[132:135], v224\n\t"
      "v_mfma_f32_16x16x32_bf16 v[28:31], v[156:159], v[180:183], v[28:31]\n\t"
      "ds_read_b128 v[136:139], v224 offset:5120\n\t"
      "v_mfma_f32_16x16x32_bf16 v[52:55], v[156:159], v[184:187], v[52:55]\n\t"
      "ds_read_b128 v[140:143], v224 offset:10240\n\t"
      "v_mfma_f32_16x16x32_bf16 v[76:79], v[156:159], v[188:191], v[76:79]\n\t"
      "ds_read_b128 v[144:147], v224 offset:15360\n\t"
      "v_mfma_f32_16x16x32_bf16 v[96:99], v[156:159], v[192:195], v[96:99]\n\t"
      "v_mfma_f32_16x16x32_bf16 v[8:11], v[160:163], v[176:179], v[8:11]\n\t"
      "ds_read_b64_tr_b16 v[112:113], v227\n\t"
      "v_mfma_f32_16x16x32_bf16 v[32:35], v[160:163], v[180:183], v[32:35]\n\t"
      "ds_read_b64_tr_b16 v[114:115], v227 offset:512\n\t"
      "v_mfma_f32_16x16x32_bf16 v[56:59], v[160:163], v[184:187], v[56:59]\n\t"
      "ds_read_b128 v[148:151], v225\n\t"
      "v_mfma_f32_16x16x32_bf16 v[80:83], v[160:163], v[188:191], v[80:83]\n\t"
      "ds_read_b64_tr_b16 v[116:117], v226 offset:1024\n\t"
      "v_mfma_f32_16x16x32_bf16 v[12:15], v[164:167], v[176:179], v[12:15]\n\t"
      "ds_read_b64_tr_b16 v[118:119], v226 offset:1536\n\t"
      "v_mfma_f32_16x16x32_bf16 v[36:39], v[164:167], v[180:183], v[36:39]\n\t"
      "ds_read_b64_tr_b16 v[120:121], v227 offset:1024\n\t"
      "v_mfma_f32_16x16x32_bf16 v[60:63], v[164:167], v[184:187], v[60:63]\n\t"
      "v_mfma_f32_16x16x32_bf16 v[84:87], v[164:167], v[188:191], v[84:87]\n\t"
      "ds_read_b64_tr_b16 v[122:123], v227 offset:1536\n\t"
      "v_mfma_f32_16x16x32_bf16 v[100:103], v[164:167], v[192:195], v[100:103]\n\t"
      "ds_read_b64_tr_b16 v[124:125], v226 offset:2048\n\t"
      "v_mfma_f32_16x16x32_bf16 v[16:19], v[176:179], v[168:171], v[16:19]\n\t"
      "ds_read_b64_tr_b16 v[126:127], v226 offset:2560\n\t"
      "v_mfma_f32_16x16x32_bf16 v[40:43], v[180:183], v[168:171], v[40:43]\n\t"
      "ds_read_b64_tr_b16 v[128:129], v227 offset:2048\n\t"
      "v_mfma_f32_16x16x32_bf16 v[64:67], v[184:187], v[168:171], v[64:67]\n\t"
      "ds_read_b64_tr_b16 v[130:131], v227 offset:2560\n\t"
      "v_mfma_f32_16x16x32_bf16 v[88:91], v[188:191], v[168:171], v[88:91]\n\t"
      "v_mfma_f32_16x16x32_bf16 v[20:23], v[176:179], v[172:175], v[20:23]\n\t"
      "v_mfma_f32_16x16x32_bf16 v[44:47], v[180:183], v[172:175], v[44:47]\n\t"
      "v_add_u32_e32 v248, s46, v248\n\t"
      "v_add_u32_e32 v249, s46, v249\n\t"
      "v_mfma_f32_16x16x32_bf16 v[68:71], v[184:187], v[172:175], v[68:71]\n\t"
      "v_mfma_f32_16x16x32_bf16 v[92:95], v[188:191], v[172:175], v[92:95]\n\t"
      "v_mfma_f32_16x16x32_bf16 v[104:107], v[192:195], v[172:175], v[104:107]\n\t"
      "EVEN1_%=:\n\t"
      "s_waitcnt lgkmcnt(0)\n\t"
      "v_mfma_f32_16x16x32_bf16 v[0:3], v[108:111], v[132:135], v[0:3]\n\t"
      "ds_read_b64_tr_b16 v[152:153], v226 offset:20480\n\t"
      "v_mfma_f32_16x16x32_bf16 v[24:27], v[108:111], v[136:139], v[24:27]\n\t"
      "ds_read_b64_tr_b16 v[154:155], v226 offset:20992\n\t"
      "v_mfma_f32_16x16x32_bf16 v[48:51], v[108:111], v[140:143], v[48:51]\n\t"
      "ds_read_b128 v[176:179], v248\n\t"
      "v_mfma_f32_16x16x32_bf16 v[72:75], v[108:111], v[144:147], v[72:75]\n\t"
      "ds_read_b128 v[180:183], v248 offset:5120\n\t"
      "v_mfma_f32_16x16x32_bf16 v[4:7], v[112:115], v[132:135], v[4:7]\n\t"
      "v_mfma_f32_16x16x32_bf16 v[28:31], v[112:115], v[136:139], v[28:31]\n\t"
      "ds_read_b128 v[184:187], v248 offset:10240\n\t"
      "v_mfma_f32_16x16x32_bf16 v[52:55], v[112:115], v[140:143], v[52:55]\n\t"
      "ds_read_b128 v[188:191], v248 offset:15360\n\t"
      "v_mfma_f32_16x16x32_bf16 v[76:79], v[112:115], v[144:147], v[76:79]\n\t"
      "ds_read_b64_tr_b16 v[156:157], v227 offset:20480\n\t"
      "v_mfma_f32_16x16x32_bf16 v[96:99], v[112:115], v[148:151], v[96:99]\n\t"
      "v_mfma_f32_16x16x32_bf16 v[8:11], v[116:119], v[132:135], v[8:11]\n\t"
      "ds_read_b64_tr_b16 v[158:159], v227 offset:20992\n\t"
      "v_mfma_f32_16x16x32_bf16 v[32:35], v[116:119], v[136:139], v[32:35]\n\t"
      "ds_read_b128 v[192:195], v249\n\t"
      "v_mfma_f32_16x16x32_bf16 v[56:59], v[116:119], v[140:143], v[56:59]\n\t"
      "ds_read_b64_tr_b16 v[160:161], v226 offset:21504\n\t"
      "v_mfma_f32_16x16x32_bf16 v[80:83], v[116:119], v[144:147], v[80:83]\n\t"
      "ds_read_b64_tr_b16 v[162:163], v226 offset:22016\n\t"
      "v_mfma_f32_16x16x32_bf16 v[12:15], v[120:123], v[132:135], v[12:15]\n\t"
      "v_mfma_f32_16x16x32_bf16 v[36:39], v[120:123], v[136:139], v[36:39]\n\t"
      "ds_read_b64_tr_b16 v[164:165], v227 offset:21504\n\t"
      "v_mfma_f32_16x16x32_bf16 v[60:63], v[120:123], v[140:143], v[60:63]\n\t"
      "ds_read_b64_tr_b16 v[166:167], v227 offset:22016\n\t"
      "v_mfma_f32_16x16x32_bf16 v[84:87], v[120:123], v[144:147], v[84:87]\n\t"
      "ds_read_b64_tr_b16 v[168:169], v226 offset:22528\n\t"
      "v_mfma_f32_16x16x32_bf16 v[100:103], v[120:123], v[148:151], v[100:103]\n\t"
      "v_mfma_f32_16x16x32_bf16 v[16:19], v[132:135], v[124:127], v[16:19]\n\t"
      "ds_read_b64_tr_b16 v[170:171], v226 offset:23040\n\t"
      "v_mfma_f32_16x16x32_bf16 v[40:43], v[136:139], v[124:127], v[40:43]\n\t"
      "ds_read_b64_tr_b16 v[172:173], v227 offset:22528\n\t"
      "v_mfma_f32_16x16x32_bf16 v[64:67], v[140:143], v[124:127], v[64:67]\n\t"
      "ds_read_b64_tr_b16 v[174:175], v227 offset:23040\n\t"
      "s_cmp_eq_u32 s47, 4\n\t"
      "s_cselect_b32 s46, -2, 1\n\t"
      "v_mfma_f32_16x16x32_bf16 v[88:91], v[144:147], v[124:127], v[88:91]\n\t"
      "s_cmp_eq_u32 s47, 1\n\t"
      "s_cselect_b32 s46, -2, s46\n\t"
      "v_mfma_f32_16x16x32_bf16 v[20:23], v[132:135], v[128:131], v[20:23]\n\t"
      "s_mul_i32 s46, s46, 43008\n\t"
      "v_mfma_f32_16x16x32_bf16 v[44:47], v[136:139], v[128:131], v[44:47]\n\t"
      "v_mfma_f32_16x16x32_bf16 v[68:71], v[140:143], v[128:131], v[68:71]\n\t"
      "v_mfma_f32_16x16x32_bf16 v[92:95], v[144:147], v[128:131], v[92:95]\n\t"
      "s_sub_u32 s47, s47, 1\n\t"
      "v_mfma_f32_16x16x32_bf16 v[104:107], v[148:151], v[128:131], v[104:107]\n\t"
      "s_cbranch_scc0 ODD1_%=\n\t"
      "s_waitcnt lgkmcnt(0)\n\t"
      "v_mfma_f32_16x16x32_bf16 v[96:99], v[156:159], v[192:195], v[96:99]\n\t"
      "ds_read_b32 v212, v215\n\t"
      "v_mfma_f32_16x16x32_bf16 v[100:103], v[164:167], v[192:195], v[100:103]\n\t"
      "ds_read2_b32 v[134:135], v234 offset0:80 offset1:80\n\t"
      "v_mfma_f32_16x16x32_bf16 v[104:107], v[192:195], v[172:175], v[104:107]\n\t"
      "ds_read_b128 v[148:151], v233 offset:320\n\t"
      "v_mfma_f32_16x16x32_bf16 v[80:83], v[160:163], v[188:191], v[80:83]\n\t"
      "ds_read2_b32 v[248:249], v234 offset0:0 offset1:0\n\t"
      "v_mfma_f32_16x16x32_bf16 v[84:87], v[164:167], v[188:191], v[84:87]\n\t"
      "ds_read_b128 v[196:199], v233\n\t"
      "v_mfma_f32_16x16x32_bf16 v[88:91], v[188:191], v[168:171], v[88:91]\n\t"
      "ds_read2_b32 v[132:133], v234 offset0:64 offset1:64\n\t"
      "v_mfma_f32_16x16x32_bf16 v[92:95], v[188:191], v[172:175], v[92:95]\n\t"
      "s_mov_b64 exec, s[44:45]\n\t"
      "ds_write_b128 v230, v[96:99]\n\t"
      "ds_write_b128 v230, v[100:103] offset:128\n\t"
      "s_mov_b64 exec, 0xffff\n\t"
      "ds_write_b32 v231, v104 offset:256\n\t"
      "ds_write_b32 v213, v213\n\t"
      "s_mov_b64 exec, -1\n\t"
      "v_mfma_f32_16x16x32_bf16 v[56:59], v[160:163], v[184:187], v[56:59]\n\t"
      "ds_read2_b32 v[130:131], v234 offset0:48 offset1:48\n\t"
      "v_mfma_f32_16x16x32_bf16 v[60:63], v[164:167], v[184:187], v[60:63]\n\t"
      "ds_read2_b32 v[128:129], v234 offset0:32 offset1:32\n\t"
      "v_mfma_f32_16x16x32_bf16 v[64:67], v[184:187], v[168:171], v[64:67]\n\t"
      "s_waitcnt lgkmcnt(0)\n\t"
      "v_mfma_f32_16x16x32_bf16 v[68:71], v[184:187], v[172:175], v[68:71]\n\t"
      "ds_read_b32 v226, v232\n\t"
      "ds_read_b128 v[116:119], v228 offset:128\n\t"
      "v_mfma_f32_16x16x32_bf16 v[32:35], v[160:163], v[180:183], v[32:35]\n\t"
      "ds_read_b128 v[120:123], v228 offset:192\n\t"
      "v_mfma_f32_16x16x32_bf16 v[36:39], v[164:167], v[180:183], v[36:39]\n\t"
      "ds_read2_b32 v[250:251], v229 offset0:64 offset1:64\n\t"
      "v_mfma_f32_16x16x32_bf16 v[40:43], v[180:183], v[168:171], v[40:43]\n\t"
      "ds_read2_b32 v[252:253], v229 offset0:80 offset1:80\n\t"
      "v_mfma_f32_16x16x32_bf16 v[44:47], v[180:183], v[172:175], v[44:47]\n\t"
      "ds_read_b128 v[124:127], v230 offset:128\n\t"
      "v_mfma_f32_16x16x32_bf16 v[8:11], v[160:163], v[176:179], v[8:11]\n\t"
      "ds_read2_b32 v[224:225], v231 offset0:64 offset1:64\n\t"
      "v_mfma_f32_16x16x32_bf16 v[12:15], v[164:167], v[176:179], v[12:15]\n\t"
      "v_mfma_f32_16x16x32_bf16 v[16:19], v[176:179], v[168:171], v[16:19]\n\t"
      "s_waitcnt lgkmcnt(6)\n\t"
      "v_min_u32_e32 v226, v226, v212\n\t"
      "v_cmp_eq_u32_e32 vcc, 0, v226\n\t"
      "v_mfma_f32_16x16x32_bf16 v[20:23], v[176:179], v[172:175], v[20:23]\n\t"
      "s_branch LOOPX_%=\n\t"
      "XAR_%=:\n\t"
      "s_mov_b32 s51, 0\n\t"
      "XAL_%=:\n\t"
      "s_add_u32 s51, s51, 1\n\t"
      "s_cmp_lt_u32 s51, 262144\n\t"
      "s_cbranch_scc0 XAX_%=\n\t"
      "s_waitcnt lgkmcnt(0)\n\t"
      "ds_read_b32 v212, v215\n\t"
      "ds_read2_b32 v[134:135], v234 offset0:80 offset1:80\n\t"
      "ds_read_b128 v[148:151], v233 offset:320\n\t"
      "ds_read2_b32 v[248:249], v234 offset0:0 offset1:0\n\t"
      "ds_read_b128 v[196:199], v233\n\t"
      "ds_read2_b32 v[132:133], v234 offset0:64 offset1:64\n\t"
      "ds_read2_b32 v[130:131], v234 offset0:48 offset1:48\n\t"
      "ds_read2_b32 v[128:129], v234 offset0:32 offset1:32\n\t"
      "s_waitcnt lgkmcnt(0)\n\t"
      "ds_read_b32 v226, v232\n\t"
      "ds_read_b128 v[116:119], v228 offset:128\n\t"
      "ds_read_b128 v[120:123], v228 offset:192\n\t"
      "ds_read2_b32 v[250:251], v229 offset0:64 offset1:64\n\t"
      "ds_read2_b32 v[252:253], v229 offset0:80 offset1:80\n\t"
      "ds_read_b128 v[124:127], v230 offset:128\n\t"
      "ds_read2_b32 v[224:225], v231 offset0:64 offset1:64\n\t"
      "s_waitcnt lgkmcnt(0)\n\t"
      "v_min_u32_e32 v226, v226, v212\n\t"
      "v_cmp_eq_u32_e32 vcc, 0, v226\n\t"
      "s_cbranch_vccnz XAL_%=\n\t"
      "XAX_%=:\n\t"
      "s_branch XAB_%=\n\t"
      "XBR_%=:\n\t"
      "s_mov_b32 s51, 0\n\t"
      "XBL_%=:\n\t"
      "s_add_u32 s51, s51, 1\n\t"
      "s_cmp_lt_u32 s51, 262144\n\t"
      "s_cbranch_scc0 XBX_%=\n\t"
      "s_waitcnt lgkmcnt(0)\n\t"
      "ds_read_b32 v226, v232 offset:4\n\t"
      "ds_read_b128 v[152:155], v236\n\t"
      "ds_read_b64 v[156:157], v237 offset:1024\n\t"
      "ds_read_b64 v[166:167], v237 offset:1536\n\t"
      "s_waitcnt lgkmcnt(0)\n\t"
      "v_cmp_eq_u32_e32 vcc, 0, v226\n\t"
      "s_cbranch_vccnz XBL_%=\n\t"
      "XBX_%=:\n\t"
      "s_branch XBB_%=\n\t"
      "CEND_%=:\n\t"
      : "+{s[4:11]}"(s), "+{s12}"(hint), "+{s13}"(chk), "+{v[224:239]}"(v), "+{v254}"(vk1)
      :
      : "memory", "vcc", "scc",
        "v0", "v1", "v2", "v3", "v4", "v5", "v6", "v7", "v8", "v9", "v10", "v11", "v12", "v13", "v14", "v15", "v16",
        "v17", "v18", "v19", "v20", "v21", "v22", "v23", "v24", "v25", "v26", "v27", "v28", "v29", "v30", "v31", "v32",
        "v33", "v34", "v35", "v36", "v37", "v38", "v39", "v40", "v41", "v42", "v43", "v44", "v45", "v46", "v47", "v48",
        "v49", "v50", "v51", "v52", "v53", "v54", "v55", "v56", "v57", "v58", "v59", "v60", "v61", "v62", "v63", "v64",
        "v65", "v66", "v67", "v68", "v69", "v70", "v71", "v72", "v73", "v74", "v75", "v76", "v77", "v78", "v79", "v80",
        "v81", "v82", "v83", "v84", "v85", "v86", "v87", "v88", "v89", "v90", "v91", "v92", "v93", "v94", "v95", "v96",
        "v97", "v98", "v99", "v100", "v101", "v102", "v103", "v104", "v105", "v106", "v107", "v108", "v109", "v110",
        "v111", "v112", "v113", "v114", "v115", "v116", "v117", "v118", "v119", "v120", "v121", "v122", "v123", "v124",
        "v125", "v126", "v127", "v128", "v129", "v130", "v131", "v132", "v133", "v134", "v135", "v136", "v137", "v138",
        "v139", "v140", "v141", "v142", "v143", "v144", "v145", "v146", "v147", "v148", "v149", "v150", "v151", "v152",
        "v153", "v154", "v155", "v156", "v157", "v158", "v159", "v160", "v161", "v162", "v163", "v164", "v165", "v166",
        "v167", "v168", "v169", "v170", "v171", "v172", "v173", "v174", "v175", "v176", "v177", "v178", "v179", "v180",
        "v181", "v182", "v183", "v184", "v185", "v186", "v187", "v188", "v189", "v190", "v191", "v192", "v193", "v194",
        "v195", "v196", "v197", "v198", "v199", "v200", "v201", "v202", "v203", "v204", "v205", "v206", "v207", "v208",
        "v209", "v210", "v211", "v212", "v213", "v214", "v215", "v216", "v217", "v218", "v219", "v220", "v221", "v222",
        "v223", "v240", "v241", "v242", "v243", "v244", "v245", "v246", "v247", "v248", "v249", "v250", "v251", "v252",
        "v253", "s36", "s37", "s38", "s39", "s40", "s41", "s42", "s43", "s44", "s45", "s46", "s47", "s48", "s49", "s50",
        "s51", "s52", "s53", "s54", "s55", "s56", "s57", "s58", "s59", "s60", "s61"
    );
    } else {
      LANE_BITS(threadIdx.x & 63);
      u32x8 v;
      u32x16 s;
      v[0] = ((8 * rt + 4 * half + g) * 16 + c) * 16;
      const int hi = -(lane >> 5), m3 = min(c, 2);
      v[1] = (((max(8 * rt + ((g & 1) ? 0 : 4 * half) - 1, 0) * 16 + c) * 16) & ~hi) | ((ST_PW + xcd * 128 + m3 * 16) & hi);
      v[2] = sta + 128 + lane * 4;
      v[3] = ((sta + ((g & 1) ? 0 : 64) + c * 4) & ~hi) | ((A_MK + m3 * 4) & hi);
      v[4] = lr * A_SLOT + A_HALO + lc * 16;
      v[6] = (side * 1024 + (idx * 4 + wave - 4) * 8 + lr) * 3584 + (7 * xp + min(lc, 6)) * 128;
      v[7] = v[6] + (1 - 2 * side) * 1024 * 3584;
      u64 tw = (u64)w1;
      s[4] = UNI(idx == 15 && xcd < 3 && wave == 4 ? (xcd == 0 ? WS_O : xcd == 1 ? WS_HN + 16 : WS_G + 32) + PROBE : 0);
      v[5] = lane * (8192 + 640);
      put(s, 0, (u64)(x + (rt * 128 * 896 + 128) - (16 * 896 + 1024)));
      put(s, 2, (u64)(wqkv + (hp * 112 + 64 * 2688) - 4096));
      s[5] = wave * A_UNIT;
      put2(s, 6, wave > 4 ? 0xffffffffu : 0xffffff00u, 0u);
      s[8] = A_HALO + (wave - 4) * 512;
      s[9] = A_FL + 32 + 4 * half;
      put(s, 10, (u64)(ws + WS_ST));
    asm volatile(
      "s_waitcnt lgkmcnt(0)\n\ts_nop 4\n\t"
      ".p2align 3\n\t"
      "s_load_dwordx2 s[18:19], s[14:15], 0x20000\n\t"
      "s_sub_u32 s40, s4, 57344\n\t"
      "s_subb_u32 s41, s5, 0\n\t"
      "s_sub_u32 s42, s6, 86016\n\t"
      "s_subb_u32 s43, s7, 0\n\t"
      "v_mov_b32_e32 v0, 0x3f803f80\n\t"
      "v_mov_b32_e32 v1, 0x3f803f80\n\t"
      "v_mov_b32_e32 v2, 0x3f803f80\n\t"
      "v_mov_b32_e32 v3, 0x3f803f80\n\t"
      "s_mov_b64 exec, 0xffffff\n\t"
      "ds_write_b128 v204, v[0:3]\n\t"
      "s_mov_b64 exec, -1\n\t"
      "v_mov_b32_e32 v32, 154624\n\t"
      "v_mov_b32_e32 v33, s13\n\t"
      "v_mov_b32_e32 v36, 0\n\t"
      "v_mov_b32_e32 v37, 0\n\t"
      "v_mov_b32_e32 v38, 0\n\t"
      "v_mov_b32_e32 v39, 0\n\t"
      "ds_write_b128 v32, v[36:39]\n\t"
      "ds_write_b128 v32, v[36:39] offset:16\n\t"
      "ds_write_b128 v32, v[36:39] offset:32\n\t"
      "s_sub_u32 s4, s4, 128\n\t"
      "s_subb_u32 s5, s5, 0\n\t"
      "s_sub_u32 s6, s6, 172032\n\t"
      "s_subb_u32 s7, s7, 0\n\t"
      "s_add_u32 m0, s9, 0\n\t"
      "s_nop 0\n\t"
      "global_load_lds_dwordx4 v195, s[6:7] offset:2048\n\t"
      "global_load_lds_dwordx4 v192, s[4:5]\n\t"
      "global_load_lds_dwordx4 v193, s[4:5] offset:1024\n\t"
      "global_load_lds_dwordx4 v196, s[6:7] offset:3072\n\t"
      "s_add_u32 m0, s9, 4096\n\t"
      "s_nop 0\n\t"
      "global_load_lds_dwordx4 v197, s[6:7]\n\t"
      "s_add_u32 m0, s12, 0\n\t"
      "s_mov_b64 exec, s[10:11]\n\t"
      "global_load_lds_dwordx4 v194, s[4:5]\n\t"
      "s_mov_b64 exec, -1\n\t"
      "s_add_u32 s4, s4, 128\n\t"
      "s_addc_u32 s5, s5, 0\n\t"
      "s_add_u32 s6, s6, 172032\n\t"
      "s_addc_u32 s7, s7, 0\n\t"
      "s_waitcnt lgkmcnt(0)\n\t"
      "s_add_u32 s18, s18, 1\n\t"
      "s_addc_u32 s19, s19, 0\n\t"
      "s_mul_i32 s48, s18, 0x9e3779b9\n\t"
      "s_mul_hi_u32 s17, s18, 0x7f4a7c15\n\t"
      "s_add_u32 s17, s17, s48\n\t"
      "s_mul_i32 s48, s19, 0x7f4a7c15\n\t"
      "s_add_u32 s17, s17, s48\n\t"
      "s_mul_i32 s16, s18, 0x7f4a7c15\n\t"
      "s_add_u32 s16, s16, 0xd192ed03\n\t"
      "s_addc_u32 s17, s17, 0xd1b54a32\n\t"
      "v_mov_b32_e32 v12, s16\n\t"
      "v_mov_b32_e32 v13, s17\n\t"
      "v_mov_b32_e32 v14, s18\n\t"
      "v_mov_b32_e32 v15, s19\n\t"
      "v_mov_b32_e32 v16, 163600\n\t"
      "s_mov_b64 exec, 1\n\t"
      "ds_write_b128 v16, v[12:15]\n\t"
      "s_mov_b64 exec, -1\n\t"
      "s_and_b32 s47, s8, 48\n\t"
      "s_add_u32 s47, s47, 114688\n\t"
      "v_mbcnt_lo_u32_b32 v28, -1, 0\n\t"
      "v_mov_b32_e32 v25, 0\n\t"
      "v_mov_b32_e32 v26, s16\n\t"
      "v_mov_b32_e32 v27, s17\n\t"
      "v_lshl_add_u32 v28, v28, 7, s47\n\t"
      "s_cmp_eq_u32 s8, 0\n\t"
      "s_cbranch_scc1 PRt_%=\n\t"
      "s_add_u32 s52, s14, s8\n\t"
      "s_addc_u32 s53, s15, 0\n\t"
      "s_mov_b32 s54, 0\n\t"
      "s_mov_b64 s[56:57], -1\n\t"
      "s_load_dword s50, s[52:53], 0x1f80\n\t"
      "PRt_%=:\n\t"
      "s_cmp_eq_u32 s8, 0\n\t"
      "s_cbranch_scc1 PRu_%=\n\t"
      "s_mov_b64 exec, 3\n\t"
      "global_store_dword v205, v0, s[52:53] sc1\n\t"
      "s_mov_b64 exec, -1\n\t"
      "PRu_%=:\n\t"
      "s_add_u32 m0, s9, 22528\n\t"
      "s_nop 0\n\t"
      "global_load_lds_dwordx4 v195, s[42:43] offset:2048\n\t"
      "global_load_lds_dwordx4 v192, s[40:41]\n\t"
      "global_load_lds_dwordx4 v193, s[40:41] offset:1024\n\t"
      "global_load_lds_dwordx4 v196, s[42:43] offset:3072\n\t"
      "s_add_u32 m0, s9, 26624\n\t"
      "s_nop 0\n\t"
      "global_load_lds_dwordx4 v197, s[42:43]\n\t"
      "s_add_u32 m0, s9, 43008\n\t"
      "s_nop 0\n\t"
      "global_load_lds_dwordx4 v195, s[6:7] offset:2048\n\t"
      "global_load_lds_dwordx4 v192, s[4:5]\n\t"
      "global_load_lds_dwordx4 v193, s[4:5] offset:1024\n\t"
      "global_load_lds_dwordx4 v196, s[6:7] offset:3072\n\t"
      "s_add_u32 m0, s9, 47104\n\t"
      "s_nop 0\n\t"
      "global_load_lds_dwordx4 v197, s[6:7]\n\t"
      "s_add_u32 m0, s12, 43008\n\t"
      "s_mov_b64 exec, s[10:11]\n\t"
      "global_load_lds_dwordx4 v194, s[4:5]\n\t"
      "s_mov_b64 exec, -1\n\t"
      "s_waitcnt vmcnt(11) lgkmcnt(0)\n\t"
      "s_barrier\n\t"
      "s_cmp_eq_u32 s8, 0\n\t"
      "s_cbranch_scc1 PRC0_%=\n\t"
      "s_mov_b32 s55, 8832\n\t"
      "s_call_b64 s[44:45], PRB_%=\n\t"
      "PRC0_%=:\n\t"
      "s_add_u32 s4, s4, 128\n\t"
      "s_addc_u32 s5, s5, 0\n\t"
      "s_add_u32 s6, s6, 172032\n\t"
      "s_addc_u32 s7, s7, 0\n\t"
      "s_add_u32 s40, s40, 128\n\t"
      "s_addc_u32 s41, s41, 0\n\t"
      "s_add_u32 s42, s42, 172032\n\t"
      "s_addc_u32 s43, s43, 0\n\t"
      "s_add_u32 m0, s9, 65536\n\t"
      "s_nop 0\n\t"
      "global_load_lds_dwordx4 v195, s[42:43] offset:2048\n\t"
      "global_load_lds_dwordx4 v192, s[40:41]\n\t"
      "global_load_lds_dwordx4 v193, s[40:41] offset:1024\n\t"
      "global_load_lds_dwordx4 v196, s[42:43] offset:3072\n\t"
      "s_add_u32 m0, s9, 69632\n\t"
      "s_nop 0\n\t"
      "global_load_lds_dwordx4 v197, s[42:43]\n\t"
      "s_add_u32 m0, s9, 86016\n\t"
      "s_nop 0\n\t"
      "global_load_lds_dwordx4 v195, s[6:7] offset:2048\n\t"
      "global_load_lds_dwordx4 v192, s[4:5]\n\t"
      "global_load_lds_dwordx4 v193, s[4:5] offset:1024\n\t"
      "global_load_lds_dwordx4 v196, s[6:7] offset:3072\n\t"
      "s_add_u32 m0, s9, 90112\n\t"
      "s_nop 0\n\t"
      "global_load_lds_dwordx4 v197, s[6:7]\n\t"
      "s_add_u32 m0, s12, 86016\n\t"
      "s_mov_b64 exec, s[10:11]\n\t"
      "global_load_lds_dwordx4 v194, s[4:5]\n\t"
      "s_mov_b64 exec, -1\n\t"
      "s_waitcnt vmcnt(11)\n\t"
      "s_barrier\n\t"
      "s_cmp_eq_u32 s8, 0\n\t"
      "s_cbranch_scc1 PRC1_%=\n\t"
      "s_mov_b32 s55, 17664\n\t"
      "s_call_b64 s[44:45], PRB_%=\n\t"
      "PRC1_%=:\n\t"
      "s_add_u32 s4, s4, 128\n\t"
      "s_addc_u32 s5, s5, 0\n\t"
      "s_add_u32 s6, s6, 172032\n\t"
      "s_addc_u32 s7, s7, 0\n\t"
      "s_add_u32 s40, s40, 128\n\t"
      "s_addc_u32 s41, s41, 0\n\t"
      "s_add_u32 s42, s42, 172032\n\t"
      "s_addc_u32 s43, s43, 0\n\t"
      "s_add_u32 m0, s9, 4294946816\n\t"
      "s_nop 0\n\t"
      "global_load_lds_dwordx4 v195, s[42:43] offset:2048\n\t"
      "global_load_lds_dwordx4 v192, s[40:41]\n\t"
      "global_load_lds_dwordx4 v193, s[40:41] offset:1024\n\t"
      "global_load_lds_dwordx4 v196, s[42:43] offset:3072\n\t"
      "s_add_u32 m0, s9, 4294950912\n\t"
      "s_nop 0\n\t"
      "global_load_lds_dwordx4 v197, s[42:43]\n\t"
      "s_add_u32 m0, s9, 0\n\t"
      "s_nop 0\n\t"
      "global_load_lds_dwordx4 v195, s[6:7] offset:2048\n\t"
      "global_load_lds_dwordx4 v192, s[4:5]\n\t"
      "global_load_lds_dwordx4 v193, s[4:5] offset:1024\n\t"
      "global_load_lds_dwordx4 v196, s[6:7] offset:3072\n\t"
      "s_add_u32 m0, s9, 4096\n\t"
      "s_nop 0\n\t"
      "global_load_lds_dwordx4 v197, s[6:7]\n\t"
      "s_add_u32 m0, s12, 0\n\t"
      "s_mov_b64 exec, s[10:11]\n\t"
      "global_load_lds_dwordx4 v194, s[4:5]\n\t"
      "s_mov_b64 exec, -1\n\t"
      "s_cmp_eq_u32 s8, 0\n\t"
      "s_cbranch_scc1 PRS_%=\n\t"
      "s_cmp_lt_u32 s57, s56\n\t"
      "s_cselect_b32 s46, 0x2000, 0\n\t"
      "v_mov_b32_e32 v24, s46\n\t"
      "v_xor_b32_e32 v26, s46, v26\n\t"
      "s_mov_b64 exec, 0xff\n\t"
      "global_store_dwordx4 v28, v[24:27], s[14:15] sc1\n\t"
      "s_mov_b64 exec, -1\n\t"
      "PRS_%=:\n\t"
      "s_waitcnt vmcnt(11)\n\t"
      "s_barrier\n\t"
      "s_add_u32 s4, s4, 128\n\t"
      "s_addc_u32 s5, s5, 0\n\t"
      "s_add_u32 s6, s6, 172032\n\t"
      "s_addc_u32 s7, s7, 0\n\t"
      "s_add_u32 s40, s40, 128\n\t"
      "s_addc_u32 s41, s41, 0\n\t"
      "s_add_u32 s42, s42, 172032\n\t"
      "s_addc_u32 s43, s43, 0\n\t"
      "s_add_u32 m0, s9, 22528\n\t"
      "s_nop 0\n\t"
      "global_load_lds_dwordx4 v195, s[42:43] offset:2048\n\t"
      "global_load_lds_dwordx4 v192, s[40:41]\n\t"
      "global_load_lds_dwordx4 v193, s[40:41] offset:1024\n\t"
      "global_load_lds_dwordx4 v196, s[42:43] offset:3072\n\t"
      "s_add_u32 m0, s9, 26624\n\t"
      "s_nop 0\n\t"
      "global_load_lds_dwordx4 v197, s[42:43]\n\t"
      "s_add_u32 m0, s9, 43008\n\t"
      "s_nop 0\n\t"
      "global_load_lds_dwordx4 v195, s[6:7] offset:2048\n\t"
      "global_load_lds_dwordx4 v192, s[4:5]\n\t"
      "global_load_lds_dwordx4 v193, s[4:5] offset:1024\n\t"
      "global_load_lds_dwordx4 v196, s[6:7] offset:3072\n\t"
      "s_add_u32 m0, s9, 47104\n\t"
      "s_nop 0\n\t"
      "global_load_lds_dwordx4 v197, s[6:7]\n\t"
      "s_add_u32 m0, s12, 43008\n\t"
      "s_mov_b64 exec, s[10:11]\n\t"
      "global_load_lds_dwordx4 v194, s[4:5]\n\t"
      "s_mov_b64 exec, -1\n\t"
      "s_waitcnt vmcnt(11)\n\t"
      "s_barrier\n\t"
      "s_add_u32 s4, s4, 128\n\t"
      "s_addc_u32 s5, s5, 0\n\t"
      "s_add_u32 s6, s6, 172032\n\t"
      "s_addc_u32 s7, s7, 0\n\t"
      "s_add_u32 s40, s40, 128\n\t"
      "s_addc_u32 s41, s41, 0\n\t"
      "s_add_u32 s42, s42, 172032\n\t"
      "s_addc_u32 s43, s43, 0\n\t"
      "s_add_u32 m0, s9, 65536\n\t"
      "s_nop 0\n\t"
      "global_load_lds_dwordx4 v195, s[42:43] offset:2048\n\t"
      "global_load_lds_dwordx4 v192, s[40:41]\n\t"
      "global_load_lds_dwordx4 v193, s[40:41] offset:1024\n\t"
      "global_load_lds_dwordx4 v196, s[42:43] offset:3072\n\t"
      "s_add_u32 m0, s9, 69632\n\t"
      "s_nop 0\n\t"
      "global_load_lds_dwordx4 v197, s[42:43]\n\t"
      "s_add_u32 m0, s9, 86016\n\t"
      "s_nop 0\n\t"
      "global_load_lds_dwordx4 v195, s[6:7] offset:2048\n\t"
      "global_load_lds_dwordx4 v192, s[4:5]\n\t"
      "global_load_lds_dwordx4 v193, s[4:5] offset:1024\n\t"
      "global_load_lds_dwordx4 v196, s[6:7] offset:3072\n\t"
      "s_add_u32 m0, s9, 90112\n\t"
      "s_nop 0\n\t"
      "global_load_lds_dwordx4 v197, s[6:7]\n\t"
      "s_add_u32 m0, s12, 86016\n\t"
      "s_mov_b64 exec, s[10:11]\n\t"
      "global_load_lds_dwordx4 v194, s[4:5]\n\t"
      "s_mov_b64 exec, -1\n\t"
      "s_waitcnt vmcnt(11)\n\t"
      "s_barrier\n\t"
      "s_add_u32 s4, s4, 128\n\t"
      "s_addc_u32 s5, s5, 0\n\t"
      "s_add_u32 s6, s6, 172032\n\t"
      "s_addc_u32 s7, s7, 0\n\t"
      "s_add_u32 s40, s40, 128\n\t"
      "s_addc_u32 s41, s41, 0\n\t"
      "s_add_u32 s42, s42, 172032\n\t"
      "s_addc_u32 s43, s43, 0\n\t"
      "s_add_u32 m0, s9, 4294946816\n\t"
      "s_nop 0\n\t"
      "global_load_lds_dwordx4 v195, s[42:43] offset:2048\n\t"
      "global_load_lds_dwordx4 v192, s[40:41]\n\t"
      "global_load_lds_dwordx4 v193, s[40:41] offset:1024\n\t"
      "global_load_lds_dwordx4 v196, s[42:43] offset:3072\n\t"
      "s_add_u32 m0, s9, 4294950912\n\t"
      "s_nop 0\n\t"
      "global_load_lds_dwordx4 v197, s[42:43]\n\t"
      "s_add_u32 m0, s9, 0\n\t"
      "s_nop 0\n\t"
      "global_load_lds_dwordx4 v195, s[6:7] offset:2048\n\t"
      "global_load_lds_dwordx4 v192, s[4:5]\n\t"
      "global_load_lds_dwordx4 v193, s[4:5] offset:1024\n\t"
      "global_load_lds_dwordx4 v196, s[6:7] offset:3072\n\t"
      "s_add_u32 m0, s9, 4096\n\t"
      "s_nop 0\n\t"
      "global_load_lds_dwordx4 v197, s[6:7]\n\t"
      "s_add_u32 m0, s12, 0\n\t"
      "s_mov_b64 exec, s[10:11]\n\t"
      "global_load_lds_dwordx4 v194, s[4:5]\n\t"
      "s_mov_b64 exec, -1\n\t"
      "global_load_dwordx4 v[4:7], v200, s[14:15] sc1\n\t"
      "global_load_dwordx4 v[8:11], v201, s[14:15] sc1\n\t"
      "s_mov_b32 m0, 163328\n\t"
      "s_add_u32 s44, s20, 0\n\t"
      "s_addc_u32 s45, s21, 0\n\t"
      "global_load_lds_dword v206, s[44:45]\n\t"
      "s_waitcnt vmcnt(14)\n\t"
      "s_barrier\n\t"
      "s_mov_b32 m0, 163328\n\t"
      "s_add_u32 s44, s20, 1835008\n\t"
      "s_addc_u32 s45, s21, 0\n\t"
      "global_load_lds_dword v206, s[44:45]\n\t"
      "s_waitcnt vmcnt(4)\n\t"
      "s_barrier\n\t"
      "s_waitcnt vmcnt(2)\n\t"
      "v_xor_b32_e32 v6, v6, v4\n\t"
      "v_xor_b32_e32 v7, v7, v5\n\t"
      "v_xor_b32_e32 v10, v10, v8\n\t"
      "v_xor_b32_e64 v11, v11, v9\n\t"
      "v_cmp_ne_u64_e64 s[48:49], s[16:17], v[6:7]\n\t"
      "v_cmp_ne_u64_e32 vcc, s[16:17], v[10:11]\n\t"
      "s_nop 0\n\t"
      "s_or_b64 vcc, vcc, s[48:49]\n\t"
      "s_cbranch_scc1 GRR_%=\n\t"
      "GRB_%=:\n\t"
      "v_mul_f32_e32 v6, 0x3e8b97f6, v5\n\t"
      "ds_write2_b32 v202, v4, v5 offset0:0 offset1:96\n\t"
      "ds_write2_b32 v203, v8, v9 offset0:0 offset1:96\n\t"
      "ds_write_b32 v202, v6 offset:640\n\t"
      "s_mov_b64 exec, 1\n\t"
      "ds_write_b32 v33, v33\n\t"
      "s_mov_b64 exec, -1\n\t"
      "s_mov_b32 m0, 163328\n\t"
      "s_add_u32 s44, s20, 0\n\t"
      "s_addc_u32 s45, s21, 0\n\t"
      "global_load_lds_dword v207, s[44:45]\n\t"
      "s_mov_b32 m0, 163328\n\t"
      "s_add_u32 s44, s20, 1835008\n\t"
      "s_addc_u32 s45, s21, 0\n\t"
      "global_load_lds_dword v207, s[44:45]\n\t"
      "s_waitcnt lgkmcnt(0)\n\t"
      "s_branch HEND_%=\n\t"
      ".p2align 6\n\t"
      "PRB_%=:\n\t"
      "s_waitcnt lgkmcnt(0)\n\t"
      "s_memtime s[58:59]\n\t"
      "s_waitcnt lgkmcnt(0)\n\t"
      "s_load_dword s50, s[52:53], s54\n\t"
      "s_waitcnt lgkmcnt(0)\n\t"
      "s_memtime s[46:47]\n\t"
      "s_add_u32 s54, s54, 8832\n\t"
      "s_waitcnt lgkmcnt(0)\n\t"
      "s_sub_u32 s46, s46, s58\n\t"
      "s_min_u32 s46, s56, s46\n\t"
      "s_mov_b32 s56, s57\n\t"
      "s_mov_b32 s57, s46\n\t"
      "s_cmp_lt_u32 s54, s55\n\t"
      "s_cbranch_scc1 PRB_%=\n\t"
      "s_setpc_b64 s[44:45]\n\t"
      "GRR_%=:\n\t"
      "s_mov_b32 s51, 0\n\t"
      "GRL_%=:\n\t"
      "s_add_u32 s51, s51, 1\n\t"
      "s_cmp_lt_u32 s51, 262144\n\t"
      "s_cbranch_scc0 GRX_%=\n\t"
      "s_sleep 2\n\t"
      "global_load_dwordx4 v[4:7], v200, s[14:15] sc1\n\t"
      "global_load_dwordx4 v[8:11], v201, s[14:15] sc1\n\t"
      "s_waitcnt vmcnt(0)\n\t"
      "v_xor_b32_e32 v6, v6, v4\n\t"
      "v_xor_b32_e32 v7, v7, v5\n\t"
      "v_xor_b32_e32 v10, v10, v8\n\t"
      "v_xor_b32_e32 v11, v11, v9\n\t"
      "v_cmp_ne_u64_e64 s[48:49], s[16:17], v[6:7]\n\t"
      "v_cmp_ne_u64_e32 vcc, s[16:17], v[10:11]\n\t"
      "s_nop 0\n\t"
      "s_or_b64 vcc, vcc, s[48:49]\n\t"
      "s_cbranch_scc1 GRL_%=\n\t"
      "GRX_%=:\n\t"
      "s_mov_b32 exec_lo, 0\n\t"
      "v_and_b32_e32 v8, 0x2000, v8\n\t"
      "s_mov_b32 exec_lo, -1\n\t"
      "s_branch GRB_%=\n\t"
      "HEND_%=:\n\t"
      : "+{s[4:19]}"(s), "+{s[20:21]}"(tw), "+{v[192:199]}"(f), "+{v[200:207]}"(v)
      :
      : "memory", "vcc", "scc",
        "v0", "v1", "v2", "v3", "v4", "v5", "v6", "v7", "v8", "v9", "v10", "v11", "v12", "v13", "v14", "v15", "v16",
        "v17", "v18", "v19", "v20", "v21", "v22", "v23", "v24", "v25", "v26", "v27", "v28", "v29", "v30", "v31", "v32",
        "v33", "v34", "v35", "v36", "v37", "v38", "v39", "s40", "s41", "s42", "s43", "s44", "s45", "s46", "s47", "s48",
        "s49", "s50", "s51", "s52", "s53", "s54", "s55", "s56", "s57", "s58", "s59"
    );
    }
    PIN();
    LANE_BITS(__builtin_amdgcn_mbcnt_hi(~0u, __builtin_amdgcn_mbcnt_lo(~0u, 0u)));
    const bool has3 = idx < 14;
    const int ft = has3 ? 7 * xp + (idx >> 1) : 0, blk0 = 7 * (2 * side + (idx & 1));
    u32x8 v;
    u32x16 s;
    v[0] = lane * 16;
    const int rot = 4 * (idx & 1), kg = 2 * ft + ((wave >> 1) & 1);
    u64 kr = (u64)(7 - rot) | ((u64)rot << 32);
    v[1] = has3 ? lr * 3584 + ((lc ^ swz_tr(lr)) << 4) : SP3 ? (min(lane, 55) + 96 * (idx & 1)) * 4096 + 384 + xp * 1024 : (lr + 8 * side) * 4096 + lc * 128 + xp * 1024;
    v[2] = wave * G_WV + g * 1024 + q4 * 128 + (((2 * (wave & 3) + (p4 >> 1)) ^ swz_tr(8 * g + q4)) << 4) + (p4 & 1) * 8;
    v[3] = wave * 4096 + (wave & 3) * 1024 + ((lane * 16) ^ ((g & 1) << 7));
    const int gg = 2 * (lane >> 5) + (lane & 1);
    v[4] = (wave & 3) * 1024 + (((gg * 16 + ((lane >> 1) & 15)) * 16) ^ ((gg & 1) << 7));
    v[5] = (wave & 1) * 512 + lane * 8;
    v[6] = g * 8;
    v[7] = ST_H3 + part(xp * 16384 + (wave * 16 + c) * 128, side);
    put(s, 0, has3 ? (u64)(w1 + ft * 128 + (wave * 256 + rot * 32) * 3584) : (u64)(w2 + (wave * 224 + (idx & 1) * 16) * 4096));
    put(s, 2, (u64)(ws + WS_HN + unit(blk0, wave, 0)));
    put(s, 4, (u64)(ws + WS_G + unit(blk0, kg / 7, 0) + kg % 7 * 1024));
    put(s, 6, (u64)(b1 + (64 * ft + 16 * (wave & 3)) * 2));
    put(s, 8, (u64)(ws + WS_ST));
    unsigned hint = ST_H4 + side * 8192 + ((idx & 1) * 28 + ft) * 128;
    s[12] = UNI((has3 ? 4 : 0) |  (wave == 4 * side + ((xp + XO) & 3) ? 16 : 0));
    s[13] = wave * G_WV;
    asm volatile(
      "s_waitcnt lgkmcnt(0)\n\ts_nop 4\n\t"
      ".p2align 3\n\t"
      "s_bitcmp0_b32 s16, 2\n\t"
      "s_cbranch_scc1 ASP_%=\n\t"
      "v_mov_b32_e32 v216, 163600\n\t"
      "ds_read_b128 v[216:219], v216\n\t"
      "v_mov_b32_e32 v220, 154756\n\t"
      "ds_read2_b32 v[220:221], v220 offset0:0 offset1:1\n\t"
      "v_xor_b32_e64 v248, 64, v241\n\t"
      "v_add_u32_e32 v249, 55296, v241\n\t"
      "v_add_u32_e32 v250, 82944, v248\n\t"
      "v_add_u32_e32 v248, 27648, v248\n\t"
      "v_xor_b32_e32 v251, 32, v242\n\t"
      "v_xor_b32_e32 v252, 64, v242\n\t"
      "v_xor_b32_e32 v253, 96, v242\n\t"
      "s_cmp_ge_u32 s17, 65536\n\t"
      "s_cbranch_scc0 BOH_%=\n\t"
      "s_barrier\n\t"
      "BOH_%=:\n\t"
      "s_add_u32 m0, s17, 0\n\t"
      "s_nop 0\n\t"
      "global_load_lds_dwordx4 v241, s[4:5]\n\t"
      "global_load_lds_dwordx4 v248, s[4:5] offset:1024\n\t"
      "global_load_lds_dwordx4 v249, s[4:5] offset:2048\n\t"
      "global_load_lds_dwordx4 v250, s[4:5] offset:3072\n\t"
      "s_sub_u32 s20, s20, 1\n\t"
      "s_cselect_b32 s48, -7, 1\n\t"
      "s_cselect_b32 s49, -1, 0\n\t"
      "s_mul_i32 s48, s48, 114688\n\t"
      "s_add_u32 s4, s4, s48\n\t"
      "s_addc_u32 s5, s5, s49\n\t"
      "global_load_dwordx2 v[238:239], v246, s[10:11]\n\t"
      "s_waitcnt lgkmcnt(0)\n\t"
      "v_readfirstlane_b32 s14, v216\n\t"
      "v_readfirstlane_b32 s15, v217\n\t"
      "v_readfirstlane_b32 s46, v218\n\t"
      "v_readfirstlane_b32 s47, v219\n\t"
      "v_readfirstlane_b32 s48, v220\n\t"
      "v_readfirstlane_b32 s49, v221\n\t"
      "v_xor_b32_e64 v247, v247, v221\n\t"
      "s_nop 0\n\t"
      "s_xor_b32 s6, s6, s48\n\t"
      "s_xor_b32 s8, s8, s49\n\t"
      "s_xor_b32 s22, s22, s49\n\t"
      "s_add_u32 s52, s6, 0\n\t"
      "s_addc_u32 s53, s7, 0\n\t"
      "s_add_u32 s54, s6, 131072\n\t"
      "s_addc_u32 s55, s7, 0\n\t"
      "s_add_u32 s56, s6, 262144\n\t"
      "s_addc_u32 s57, s7, 0\n\t"
      "s_add_u32 s58, s6, 393216\n\t"
      "s_addc_u32 s59, s7, 0\n\t"
      "s_add_u32 s60, s6, 524288\n\t"
      "s_addc_u32 s61, s7, 0\n\t"
      "s_add_u32 s62, s6, 655360\n\t"
      "s_addc_u32 s63, s7, 0\n\t"
      "s_add_u32 s64, s6, 786432\n\t"
      "s_addc_u32 s65, s7, 0\n\t"
      "s_add_u32 s48, s21, 0\n\t"
      "s_cmp_ge_u32 s48, 8\n\t"
      "s_cselect_b32 s49, 8, 0\n\t"
      "s_sub_u32 s48, s48, s49\n\t"
      "v_lshl_add_u32 v176, s48, 10, v240\n\t"
      "s_add_u32 s48, s21, 1\n\t"
      "s_cmp_ge_u32 s48, 8\n\t"
      "s_cselect_b32 s49, 8, 0\n\t"
      "s_sub_u32 s48, s48, s49\n\t"
      "v_lshl_add_u32 v177, s48, 10, v240\n\t"
      "s_add_u32 s48, s21, 2\n\t"
      "s_cmp_ge_u32 s48, 8\n\t"
      "s_cselect_b32 s49, 8, 0\n\t"
      "s_sub_u32 s48, s48, s49\n\t"
      "v_lshl_add_u32 v178, s48, 10, v240\n\t"
      "s_add_u32 s48, s21, 3\n\t"
      "s_cmp_ge_u32 s48, 8\n\t"
      "s_cselect_b32 s49, 8, 0\n\t"
      "s_sub_u32 s48, s48, s49\n\t"
      "v_lshl_add_u32 v179, s48, 10, v240\n\t"
      "s_add_u32 s48, s21, 4\n\t"
      "s_cmp_ge_u32 s48, 8\n\t"
      "s_cselect_b32 s49, 8, 0\n\t"
      "s_sub_u32 s48, s48, s49\n\t"
      "v_lshl_add_u32 v180, s48, 10, v240\n\t"
      "s_add_u32 s48, s21, 5\n\t"
      "s_cmp_ge_u32 s48, 8\n\t"
      "s_cselect_b32 s49, 8, 0\n\t"
      "s_sub_u32 s48, s48, s49\n\t"
      "v_lshl_add_u32 v181, s48, 10, v240\n\t"
      "s_add_u32 s48, s21, 6\n\t"
      "s_cmp_ge_u32 s48, 8\n\t"
      "s_cselect_b32 s49, 8, 0\n\t"
      "s_sub_u32 s48, s48, s49\n\t"
      "v_lshl_add_u32 v182, s48, 10, v240\n\t"
      "s_add_u32 s48, s21, 7\n\t"
      "s_cmp_ge_u32 s48, 8\n\t"
      "s_cselect_b32 s49, 8, 0\n\t"
      "s_sub_u32 s48, s48, s49\n\t"
      "v_lshl_add_u32 v183, s48, 10, v240\n\t"
      "s_add_u32 m0, s17, 4096\n\t"
      "s_nop 0\n\t"
      "global_load_lds_dwordx4 v241, s[4:5]\n\t"
      "global_load_lds_dwordx4 v248, s[4:5] offset:1024\n\t"
      "global_load_lds_dwordx4 v249, s[4:5] offset:2048\n\t"
      "global_load_lds_dwordx4 v250, s[4:5] offset:3072\n\t"
      "s_sub_u32 s20, s20, 1\n\t"
      "s_cselect_b32 s48, -7, 1\n\t"
      "s_cselect_b32 s49, -1, 0\n\t"
      "s_mul_i32 s48, s48, 114688\n\t"
      "s_add_u32 s4, s4, s48\n\t"
      "s_addc_u32 s5, s5, s49\n\t"
      "s_waitcnt vmcnt(5)\n\t"
      "s_add_u32 m0, s17, 8192\n\t"
      "s_nop 0\n\t"
      "global_load_lds_dwordx4 v241, s[4:5]\n\t"
      "global_load_lds_dwordx4 v248, s[4:5] offset:1024\n\t"
      "global_load_lds_dwordx4 v249, s[4:5] offset:2048\n\t"
      "global_load_lds_dwordx4 v250, s[4:5] offset:3072\n\t"
      "s_sub_u32 s20, s20, 1\n\t"
      "s_cselect_b32 s48, -7, 1\n\t"
      "s_cselect_b32 s49, -1, 0\n\t"
      "s_mul_i32 s48, s48, 114688\n\t"
      "s_add_u32 s4, s4, s48\n\t"
      "s_addc_u32 s5, s5, s49\n\t"
      "ds_read_b64_tr_b16 v[0:1], v242\n\t"
      "ds_read_b64_tr_b16 v[2:3], v242 offset:512\n\t"
      "ds_read_b64_tr_b16 v[4:5], v251\n\t"
      "ds_read_b64_tr_b16 v[6:7], v251 offset:512\n\t"
      "ds_read_b64_tr_b16 v[8:9], v252\n\t"
      "ds_read_b64_tr_b16 v[10:11], v252 offset:512\n\t"
      "ds_read_b64_tr_b16 v[12:13], v253\n\t"
      "ds_read_b64_tr_b16 v[14:15], v253 offset:512\n\t"
      "s_waitcnt vmcnt(4)\n\t"
      "s_add_u32 m0, s17, 12288\n\t"
      "s_nop 0\n\t"
      "global_load_lds_dwordx4 v241, s[4:5]\n\t"
      "global_load_lds_dwordx4 v248, s[4:5] offset:1024\n\t"
      "global_load_lds_dwordx4 v249, s[4:5] offset:2048\n\t"
      "global_load_lds_dwordx4 v250, s[4:5] offset:3072\n\t"
      "s_sub_u32 s20, s20, 1\n\t"
      "s_cselect_b32 s48, -7, 1\n\t"
      "s_cselect_b32 s49, -1, 0\n\t"
      "s_mul_i32 s48, s48, 114688\n\t"
      "s_add_u32 s4, s4, s48\n\t"
      "s_addc_u32 s5, s5, s49\n\t"
      "ds_read_b64_tr_b16 v[16:17], v242 offset:4096\n\t"
      "ds_read_b64_tr_b16 v[18:19], v242 offset:4608\n\t"
      "ds_read_b64_tr_b16 v[20:21], v251 offset:4096\n\t"
      "ds_read_b64_tr_b16 v[22:23], v251 offset:4608\n\t"
      "ds_read_b64_tr_b16 v[24:25], v252 offset:4096\n\t"
      "ds_read_b64_tr_b16 v[26:27], v252 offset:4608\n\t"
      "ds_read_b64_tr_b16 v[28:29], v253 offset:4096\n\t"
      "ds_read_b64_tr_b16 v[30:31], v253 offset:4608\n\t"
      "v_xor_b32_e32 v164, 1024, v243\n\t"
      "v_xor_b32_e32 v165, 2048, v243\n\t"
      "v_xor_b32_e32 v166, 3072, v243\n\t"
      "v_lshlrev_b32_e32 v236, 16, v238\n\t"
      "v_and_b32_e32 v237, 0xffff0000, v238\n\t"
      "v_lshlrev_b32_e32 v238, 16, v239\n\t"
      "v_and_b32_e32 v239, 0xffff0000, v239\n\t"
      "v_mov_b32_e32 v235, 0xbdd2d3e8\n\t"
      "s_waitcnt vmcnt(4)\n\t"
      "s_waitcnt lgkmcnt(8)\n\t"
      "s_add_u32 m0, s17, 0\n\t"
      "s_nop 0\n\t"
      "global_load_lds_dwordx4 v241, s[4:5]\n\t"
      "global_load_lds_dwordx4 v248, s[4:5] offset:1024\n\t"
      "global_load_lds_dwordx4 v249, s[4:5] offset:2048\n\t"
      "global_load_lds_dwordx4 v250, s[4:5] offset:3072\n\t"
      "s_sub_u32 s20, s20, 1\n\t"
      "s_cselect_b32 s48, -7, 1\n\t"
      "s_cselect_b32 s49, -1, 0\n\t"
      "s_mul_i32 s48, s48, 114688\n\t"
      "s_add_u32 s4, s4, s48\n\t"
      "s_addc_u32 s5, s5, s49\n\t"
      "ds_read_b64_tr_b16 v[32:33], v242 offset:8192\n\t"
      "ds_read_b64_tr_b16 v[34:35], v242 offset:8704\n\t"
      "ds_read_b64_tr_b16 v[36:37], v251 offset:8192\n\t"
      "ds_read_b64_tr_b16 v[38:39], v251 offset:8704\n\t"
      "ds_read_b64_tr_b16 v[40:41], v252 offset:8192\n\t"
      "ds_read_b64_tr_b16 v[42:43], v252 offset:8704\n\t"
      "ds_read_b64_tr_b16 v[44:45], v253 offset:8192\n\t"
      "ds_read_b64_tr_b16 v[46:47], v253 offset:8704\n\t"
      "s_waitcnt vmcnt(4)\n\t"
      "s_waitcnt lgkmcnt(8)\n\t"
      "s_add_u32 m0, s17, 4096\n\t"
      "s_nop 0\n\t"
      "global_load_lds_dwordx4 v241, s[4:5]\n\t"
      "global_load_lds_dwordx4 v248, s[4:5] offset:1024\n\t"
      "global_load_lds_dwordx4 v249, s[4:5] offset:2048\n\t"
      "global_load_lds_dwordx4 v250, s[4:5] offset:3072\n\t"
      "s_sub_u32 s20, s20, 1\n\t"
      "s_cselect_b32 s48, -7, 1\n\t"
      "s_cselect_b32 s49, -1, 0\n\t"
      "s_mul_i32 s48, s48, 114688\n\t"
      "s_add_u32 s4, s4, s48\n\t"
      "s_addc_u32 s5, s5, s49\n\t"
      "ds_read_b64_tr_b16 v[48:49], v242 offset:12288\n\t"
      "ds_read_b64_tr_b16 v[50:51], v242 offset:12800\n\t"
      "ds_read_b64_tr_b16 v[52:53], v251 offset:12288\n\t"
      "ds_read_b64_tr_b16 v[54:55], v251 offset:12800\n\t"
      "ds_read_b64_tr_b16 v[56:57], v252 offset:12288\n\t"
      "ds_read_b64_tr_b16 v[58:59], v252 offset:12800\n\t"
      "ds_read_b64_tr_b16 v[60:61], v253 offset:12288\n\t"
      "ds_read_b64_tr_b16 v[62:63], v253 offset:12800\n\t"
      "s_waitcnt vmcnt(4)\n\t"
      "s_waitcnt lgkmcnt(8)\n\t"
      "s_add_u32 m0, s17, 8192\n\t"
      "s_nop 0\n\t"
      "global_load_lds_dwordx4 v241, s[4:5]\n\t"
      "global_load_lds_dwordx4 v248, s[4:5] offset:1024\n\t"
      "global_load_lds_dwordx4 v249, s[4:5] offset:2048\n\t"
      "global_load_lds_dwordx4 v250, s[4:5] offset:3072\n\t"
      "s_sub_u32 s20, s20, 1\n\t"
      "s_cselect_b32 s48, -7, 1\n\t"
      "s_cselect_b32 s49, -1, 0\n\t"
      "s_mul_i32 s48, s48, 114688\n\t"
      "s_add_u32 s4, s4, s48\n\t"
      "s_addc_u32 s5, s5, s49\n\t"
      "ds_read_b64_tr_b16 v[64:65], v242\n\t"
      "ds_read_b64_tr_b16 v[66:67], v242 offset:512\n\t"
      "ds_read_b64_tr_b16 v[68:69], v251\n\t"
      "ds_read_b64_tr_b16 v[70:71], v251 offset:512\n\t"
      "ds_read_b64_tr_b16 v[72:73], v252\n\t"
      "ds_read_b64_tr_b16 v[74:75], v252 offset:512\n\t"
      "ds_read_b64_tr_b16 v[76:77], v253\n\t"
      "ds_read_b64_tr_b16 v[78:79], v253 offset:512\n\t"
      "s_waitcnt vmcnt(4)\n\t"
      "s_waitcnt lgkmcnt(8)\n\t"
      "s_add_u32 m0, s17, 12288\n\t"
      "s_nop 0\n\t"
      "global_load_lds_dwordx4 v241, s[4:5]\n\t"
      "global_load_lds_dwordx4 v248, s[4:5] offset:1024\n\t"
      "global_load_lds_dwordx4 v249, s[4:5] offset:2048\n\t"
      "global_load_lds_dwordx4 v250, s[4:5] offset:3072\n\t"
      "ds_read_b64_tr_b16 v[80:81], v242 offset:4096\n\t"
      "ds_read_b64_tr_b16 v[82:83], v242 offset:4608\n\t"
      "ds_read_b64_tr_b16 v[84:85], v251 offset:4096\n\t"
      "ds_read_b64_tr_b16 v[86:87], v251 offset:4608\n\t"
      "ds_read_b64_tr_b16 v[88:89], v252 offset:4096\n\t"
      "ds_read_b64_tr_b16 v[90:91], v252 offset:4608\n\t"
      "ds_read_b64_tr_b16 v[92:93], v253 offset:4096\n\t"
      "ds_read_b64_tr_b16 v[94:95], v253 offset:4608\n\t"
      "s_waitcnt vmcnt(4)\n\t"
      "ds_read_b64_tr_b16 v[96:97], v242 offset:8192\n\t"
      "ds_read_b64_tr_b16 v[98:99], v242 offset:8704\n\t"
      "ds_read_b64_tr_b16 v[100:101], v251 offset:8192\n\t"
      "ds_read_b64_tr_b16 v[102:103], v251 offset:8704\n\t"
      "ds_read_b64_tr_b16 v[104:105], v252 offset:8192\n\t"
      "ds_read_b64_tr_b16 v[106:107], v252 offset:8704\n\t"
      "ds_read_b64_tr_b16 v[108:109], v253 offset:8192\n\t"
      "ds_read_b64_tr_b16 v[110:111], v253 offset:8704\n\t"
      "s_waitcnt vmcnt(0)\n\t"
      "ds_read_b64_tr_b16 v[112:113], v242 offset:12288\n\t"
      "ds_read_b64_tr_b16 v[114:115], v242 offset:12800\n\t"
      "ds_read_b64_tr_b16 v[116:117], v251 offset:12288\n\t"
      "ds_read_b64_tr_b16 v[118:119], v251 offset:12800\n\t"
      "ds_read_b64_tr_b16 v[120:121], v252 offset:12288\n\t"
      "ds_read_b64_tr_b16 v[122:123], v252 offset:12800\n\t"
      "ds_read_b64_tr_b16 v[124:125], v253 offset:12288\n\t"
      "ds_read_b64_tr_b16 v[126:127], v253 offset:12800\n\t"
      "s_waitcnt lgkmcnt(0)\n\t"
      "s_barrier\n\t"
      "s_mov_b32 s51, 0\n\t"
      "s_branch PRE_%=\n\t"
      "PRL_%=:\n\t"
      "s_sleep 4\n\t"
      "s_add_u32 s51, s51, 1\n\t"
      "s_cmp_lt_u32 s51, 262144\n\t"
      "s_cbranch_scc0 PRX_%=\n\t"
      "PRE_%=:\n\t"
      "s_mov_b64 exec, 65535\n\t"
      "s_bitcmp1_b32 s16, 4\n\t"
      "s_cbranch_scc1 PM_%=\n\t"
      "global_load_dwordx2 v[232:233], v247, s[12:13] sc1\n\t"
      "s_waitcnt vmcnt(0)\n\t"
      "PM_%=:\n\t"
      "s_cbranch_scc0 PN_%=\n\t"
      "v_mov_b64_e32 v[232:233], 0\n\t"
      "global_atomic_or_x2 v[232:233], v247, v[232:233], s[12:13] sc0 sc1\n\t"
      "s_waitcnt vmcnt(0)\n\t"
      "PN_%=:\n\t"
      "v_subrev_u32_e32 v232, s14, v232\n\t"
      "v_cmp_ne_u32_e64 s[48:49], s15, v233\n\t"
      "v_subrev_u32_e32 v232, 0, v232\n\t"
      "v_cmp_lt_u32_e32 vcc, 7, v232\n\t"
      "s_mov_b64 exec, -1\n\t"
      "s_or_b64 vcc, vcc, s[48:49]\n\t"
      "s_cbranch_vccnz PRL_%=\n\t"
      "PRX_%=:\n\t"
      "PRY_%=:\n\t"
      "global_load_dwordx4 v[128:131], v176, s[52:53] sc1\n\t"
      "global_load_dwordx4 v[132:135], v177, s[52:53] sc1\n\t"
      "global_load_dwordx4 v[136:139], v178, s[52:53] sc1\n\t"
      "global_load_dwordx4 v[140:143], v179, s[52:53] sc1\n\t"
      "global_load_dwordx4 v[144:147], v180, s[52:53] sc1\n\t"
      "global_load_dwordx4 v[148:151], v181, s[52:53] sc1\n\t"
      "global_load_dwordx4 v[152:155], v182, s[52:53] sc1\n\t"
      "global_load_dwordx4 v[156:159], v183, s[52:53] sc1\n\t"
      "s_cmp_ge_u32 s17, 65536\n\t"
      "s_cbranch_scc1 GQ_%=\n\t"
      ".p2align 6\n\t"
      "s_setprio 1\n\t"
      "v_mov_b32_e32 v232, s14\n\t"
      "v_mov_b32_e32 v233, s15\n\t"
      "v_lshl_add_u32 v247, v240, 10, s22\n\t"
      "s_waitcnt vmcnt(7)\n\t"
      "v_max3_u32 v234, v128, v129, v130\n\t"
      "v_max_u32_e32 v234, v234, v131\n\t"
      "v_cmp_eq_u32_e32 vcc, -1, v234\n\t"
      "s_cbranch_vccnz FP0R_%=\n\t"
      "FP0B_%=:\n\t"
      "v_mfma_f32_16x16x32_bf16 v[184:187], v[0:3], v[128:131], 0\n\t"
      "v_mfma_f32_16x16x32_bf16 v[188:191], v[4:7], v[128:131], 0\n\t"
      "v_mfma_f32_16x16x32_bf16 v[192:195], v[8:11], v[128:131], 0\n\t"
      "v_mfma_f32_16x16x32_bf16 v[196:199], v[12:15], v[128:131], 0\n\t"
      "global_load_dwordx4 v[128:131], v176, s[54:55] sc1\n\t"
      "s_waitcnt vmcnt(7)\n\t"
      "v_max3_u32 v234, v132, v133, v134\n\t"
      "v_max_u32_e32 v234, v234, v135\n\t"
      "v_cmp_eq_u32_e32 vcc, -1, v234\n\t"
      "s_cbranch_vccnz FP1R_%=\n\t"
      "FP1B_%=:\n\t"
      "v_mfma_f32_16x16x32_bf16 v[184:187], v[16:19], v[132:135], v[184:187]\n\t"
      "v_mfma_f32_16x16x32_bf16 v[188:191], v[20:23], v[132:135], v[188:191]\n\t"
      "v_mfma_f32_16x16x32_bf16 v[192:195], v[24:27], v[132:135], v[192:195]\n\t"
      "v_mfma_f32_16x16x32_bf16 v[196:199], v[28:31], v[132:135], v[196:199]\n\t"
      "global_load_dwordx4 v[132:135], v177, s[54:55] sc1\n\t"
      "s_waitcnt vmcnt(7)\n\t"
      "v_max3_u32 v234, v136, v137, v138\n\t"
      "v_max_u32_e32 v234, v234, v139\n\t"
      "v_cmp_eq_u32_e32 vcc, -1, v234\n\t"
      "s_cbranch_vccnz FP2R_%=\n\t"
      "FP2B_%=:\n\t"
      "v_mfma_f32_16x16x32_bf16 v[184:187], v[32:35], v[136:139], v[184:187]\n\t"
      "v_mfma_f32_16x16x32_bf16 v[188:191], v[36:39], v[136:139], v[188:191]\n\t"
      "v_mfma_f32_16x16x32_bf16 v[192:195], v[40:43], v[136:139], v[192:195]\n\t"
      "v_mfma_f32_16x16x32_bf16 v[196:199], v[44:47], v[136:139], v[196:199]\n\t"
      "global_load_dwordx4 v[136:139], v178, s[54:55] sc1\n\t"
      "s_waitcnt vmcnt(7)\n\t"
      "v_max3_u32 v234, v140, v141, v142\n\t"
      "v_max_u32_e32 v234, v234, v143\n\t"
      "v_cmp_eq_u32_e32 vcc, -1, v234\n\t"
      "s_cbranch_vccnz FP3R_%=\n\t"
      "FP3B_%=:\n\t"
      "v_mfma_f32_16x16x32_bf16 v[184:187], v[48:51], v[140:143], v[184:187]\n\t"
      "v_mfma_f32_16x16x32_bf16 v[188:191], v[52:55], v[140:143], v[188:191]\n\t"
      "v_mfma_f32_16x16x32_bf16 v[192:195], v[56:59], v[140:143], v[192:195]\n\t"
      "v_mfma_f32_16x16x32_bf16 v[196:199], v[60:63], v[140:143], v[196:199]\n\t"
      "global_load_dwordx4 v[140:143], v179, s[54:55] sc1\n\t"
      "s_waitcnt vmcnt(7)\n\t"
      "v_max3_u32 v234, v144, v145, v146\n\t"
      "v_max_u32_e32 v234, v234, v147\n\t"
      "v_cmp_eq_u32_e32 vcc, -1, v234\n\t"
      "s_cbranch_vccnz FP4R_%=\n\t"
      "FP4B_%=:\n\t"
      "v_mfma_f32_16x16x32_bf16 v[184:187], v[64:67], v[144:147], v[184:187]\n\t"
      "v_mfma_f32_16x16x32_bf16 v[188:191], v[68:71], v[144:147], v[188:191]\n\t"
      "v_mfma_f32_16x16x32_bf16 v[192:195], v[72:75], v[144:147], v[192:195]\n\t"
      "v_mfma_f32_16x16x32_bf16 v[196:199], v[76:79], v[144:147], v[196:199]\n\t"
      "global_load_dwordx4 v[144:147], v180, s[54:55] sc1\n\t"
      "s_waitcnt vmcnt(7)\n\t"
      "v_max3_u32 v234, v148, v149, v150\n\t"
      "v_max_u32_e32 v234, v234, v151\n\t"
      "v_cmp_eq_u32_e32 vcc, -1, v234\n\t"
      "s_cbranch_vccnz FP5R_%=\n\t"
      "FP5B_%=:\n\t"
      "v_mfma_f32_16x16x32_bf16 v[184:187], v[80:83], v[148:151], v[184:187]\n\t"
      "v_mfma_f32_16x16x32_bf16 v[188:191], v[84:87], v[148:151], v[188:191]\n\t"
      "v_mfma_f32_16x16x32_bf16 v[192:195], v[88:91], v[148:151], v[192:195]\n\t"
      "v_mfma_f32_16x16x32_bf16 v[196:199], v[92:95], v[148:151], v[196:199]\n\t"
      "global_load_dwordx4 v[148:151], v181, s[54:55] sc1\n\t"
      "s_waitcnt vmcnt(7)\n\t"
      "v_max3_u32 v234, v152, v153, v154\n\t"
      "v_max_u32_e32 v234, v234, v155\n\t"
      "v_cmp_eq_u32_e32 vcc, -1, v234\n\t"
      "s_cbranch_vccnz FP6R_%=\n\t"
      "FP6B_%=:\n\t"
      "v_mfma_f32_16x16x32_bf16 v[184:187], v[96:99], v[152:155], v[184:187]\n\t"
      "s_waitcnt vmcnt(6)\n\t"
      "v_max3_u32 v234, v156, v157, v158\n\t"
      "v_max_u32_e32 v234, v234, v159\n\t"
      "v_cmp_eq_u32_e32 vcc, -1, v234\n\t"
      "s_cbranch_vccnz FP7R_%=\n\t"
      "FP7B_%=:\n\t"
      "v_mfma_f32_16x16x32_bf16 v[184:187], v[112:115], v[156:159], v[184:187]\n\t"
      "v_mfma_f32_16x16x32_bf16 v[188:191], v[100:103], v[152:155], v[188:191]\n\t"
      "v_mfma_f32_16x16x32_bf16 v[188:191], v[116:119], v[156:159], v[188:191]\n\t"
      "v_mfma_f32_16x16x32_bf16 v[192:195], v[104:107], v[152:155], v[192:195]\n\t"
      "v_mfma_f32_16x16x32_bf16 v[192:195], v[120:123], v[156:159], v[192:195]\n\t"
      "s_nop 3\n\t"
      "ds_write_b128 v243, v[184:187]\n\t"
      "v_mfma_f32_16x16x32_bf16 v[196:199], v[108:111], v[152:155], v[196:199]\n\t"
      "global_load_dwordx4 v[152:155], v182, s[54:55] sc1\n\t"
      "v_mfma_f32_16x16x32_bf16 v[196:199], v[124:127], v[156:159], v[196:199]\n\t"
      "global_load_dwordx4 v[156:159], v183, s[54:55] sc1\n\t"
      "ds_write_b128 v164, v[188:191]\n\t"
      "ds_write_b128 v165, v[192:195]\n\t"
      "s_nop 4\n\t"
      "ds_write_b128 v166, v[196:199]\n\t"
      "s_waitcnt lgkmcnt(0)\n\t"
      "s_barrier\n\t"
      "ds_read_b128 v[216:219], v244\n\t"
      "ds_read_b128 v[220:223], v244 offset:4096\n\t"
      "ds_read_b128 v[224:227], v244 offset:8192\n\t"
      "ds_read_b128 v[228:231], v244 offset:12288\n\t"
      "ds_read_b128 v[184:187], v244 offset:16384\n\t"
      "ds_read_b128 v[188:191], v244 offset:20480\n\t"
      "ds_read_b128 v[192:195], v244 offset:24576\n\t"
      "ds_read_b128 v[196:199], v244 offset:28672\n\t"
      "s_waitcnt lgkmcnt(6)\n\t"
      "v_add_f32_e32 v216, v216, v220\n\t"
      "v_add_f32_e32 v217, v217, v221\n\t"
      "v_add_f32_e32 v218, v218, v222\n\t"
      "v_add_f32_e32 v219, v219, v223\n\t"
      "s_waitcnt lgkmcnt(4)\n\t"
      "v_add_f32_e32 v224, v224, v228\n\t"
      "v_add_f32_e32 v225, v225, v229\n\t"
      "v_add_f32_e32 v226, v226, v230\n\t"
      "v_add_f32_e32 v227, v227, v231\n\t"
      "s_waitcnt lgkmcnt(2)\n\t"
      "v_add_f32_e32 v184, v184, v188\n\t"
      "v_add_f32_e32 v185, v185, v189\n\t"
      "v_add_f32_e32 v186, v186, v190\n\t"
      "v_add_f32_e32 v187, v187, v191\n\t"
      "v_add_f32_e32 v216, v216, v224\n\t"
      "v_add_f32_e32 v217, v217, v225\n\t"
      "v_add_f32_e32 v218, v218, v226\n\t"
      "v_add_f32_e32 v219, v219, v227\n\t"
      "s_waitcnt lgkmcnt(0)\n\t"
      "v_add_f32_e32 v192, v192, v196\n\t"
      "v_add_f32_e32 v193, v193, v197\n\t"
      "v_add_f32_e32 v194, v194, v198\n\t"
      "v_add_f32_e32 v195, v195, v199\n\t"
      "v_add_f32_e32 v184, v184, v192\n\t"
      "v_add_f32_e32 v185, v185, v193\n\t"
      "v_add_f32_e32 v186, v186, v194\n\t"
      "v_add_f32_e32 v187, v187, v195\n\t"
      "v_add_f32_e32 v216, v216, v184\n\t"
      "v_add_f32_e32 v217, v217, v185\n\t"
      "s_waitcnt vmcnt(7)\n\t"
      "v_mfma_f32_16x16x32_bf16 v[200:203], v[0:3], v[128:131], 0\n\t"
      "v_add_f32_e32 v218, v218, v186\n\t"
      "v_add_f32_e32 v219, v219, v187\n\t"
      "v_mfma_f32_16x16x32_bf16 v[204:207], v[4:7], v[128:131], 0\n\t"
      "v_mul_f32_e32 v220, v216, v216\n\t"
      "v_mul_f32_e32 v221, v217, v217\n\t"
      "v_mfma_f32_16x16x32_bf16 v[208:211], v[8:11], v[128:131], 0\n\t"
      "v_mul_f32_e32 v222, v218, v218\n\t"
      "v_mul_f32_e32 v223, v219, v219\n\t"
      "v_mfma_f32_16x16x32_bf16 v[212:215], v[12:15], v[128:131], 0\n\t"
      "global_load_dwordx4 v[128:131], v176, s[56:57] sc1\n\t"
      "s_waitcnt vmcnt(7)\n\t"
      "v_mfma_f32_16x16x32_bf16 v[200:203], v[16:19], v[132:135], v[200:203]\n\t"
      "v_fmaak_f32 v220, v235, v220, 0xc0135761\n\t"
      "v_fmaak_f32 v221, v235, v221, 0xc0135761\n\t"
      "v_mfma_f32_16x16x32_bf16 v[204:207], v[20:23], v[132:135], v[204:207]\n\t"
      "v_fmaak_f32 v222, v235, v222, 0xc0135761\n\t"
      "v_fmaak_f32 v223, v235, v223, 0xc0135761\n\t"
      "v_mfma_f32_16x16x32_bf16 v[208:211], v[24:27], v[132:135], v[208:211]\n\t"
      "v_mul_f32_e32 v220, v216, v220\n\t"
      "v_mul_f32_e32 v221, v217, v221\n\t"
      "v_mfma_f32_16x16x32_bf16 v[212:215], v[28:31], v[132:135], v[212:215]\n\t"
      "global_load_dwordx4 v[132:135], v177, s[56:57] sc1\n\t"
      "s_waitcnt vmcnt(7)\n\t"
      "v_mfma_f32_16x16x32_bf16 v[200:203], v[32:35], v[136:139], v[200:203]\n\t"
      "v_mul_f32_e32 v222, v218, v222\n\t"
      "v_mul_f32_e32 v223, v219, v223\n\t"
      "v_mfma_f32_16x16x32_bf16 v[204:207], v[36:39], v[136:139], v[204:207]\n\t"
      "v_exp_f32_e32 v220, v220\n\t"
      "v_mfma_f32_16x16x32_bf16 v[208:211], v[40:43], v[136:139], v[208:211]\n\t"
      "v_exp_f32_e32 v221, v221\n\t"
      "v_mfma_f32_16x16x32_bf16 v[212:215], v[44:47], v[136:139], v[212:215]\n\t"
      "global_load_dwordx4 v[136:139], v178, s[56:57] sc1\n\t"
      "s_waitcnt vmcnt(7)\n\t"
      "v_mfma_f32_16x16x32_bf16 v[200:203], v[48:51], v[140:143], v[200:203]\n\t"
      "v_exp_f32_e32 v222, v222\n\t"
      "v_mfma_f32_16x16x32_bf16 v[204:207], v[52:55], v[140:143], v[204:207]\n\t"
      "v_exp_f32_e32 v223, v223\n\t"
      "v_mfma_f32_16x16x32_bf16 v[208:211], v[56:59], v[140:143], v[208:211]\n\t"
      "v_add_f32_e32 v220, 1.0, v220\n\t"
      "v_add_f32_e32 v221, 1.0, v221\n\t"
      "v_mfma_f32_16x16x32_bf16 v[212:215], v[60:63], v[140:143], v[212:215]\n\t"
      "global_load_dwordx4 v[140:143], v179, s[56:57] sc1\n\t"
      "s_waitcnt vmcnt(7)\n\t"
      "v_mfma_f32_16x16x32_bf16 v[200:203], v[64:67], v[144:147], v[200:203]\n\t"
      "v_add_f32_e32 v222, 1.0, v222\n\t"
      "v_add_f32_e32 v223, 1.0, v223\n\t"
      "v_mfma_f32_16x16x32_bf16 v[204:207], v[68:71], v[144:147], v[204:207]\n\t"
      "v_rcp_f32_e32 v220, v220\n\t"
      "v_mfma_f32_16x16x32_bf16 v[208:211], v[72:75], v[144:147], v[208:211]\n\t"
      "v_rcp_f32_e32 v221, v221\n\t"
      "v_mfma_f32_16x16x32_bf16 v[212:215], v[76:79], v[144:147], v[212:215]\n\t"
      "global_load_dwordx4 v[144:147], v180, s[56:57] sc1\n\t"
      "s_waitcnt vmcnt(7)\n\t"
      "v_mfma_f32_16x16x32_bf16 v[200:203], v[80:83], v[148:151], v[200:203]\n\t"
      "v_rcp_f32_e32 v222, v222\n\t"
      "v_mfma_f32_16x16x32_bf16 v[204:207], v[84:87], v[148:151], v[204:207]\n\t"
      "v_rcp_f32_e32 v223, v223\n\t"
      "v_mfma_f32_16x16x32_bf16 v[208:211], v[88:91], v[148:151], v[208:211]\n\t"
      "v_mul_f32_e32 v216, v216, v220\n\t"
      "v_mfma_f32_16x16x32_bf16 v[212:215], v[92:95], v[148:151], v[212:215]\n\t"
      "global_load_dwordx4 v[148:151], v181, s[56:57] sc1\n\t"
      "s_waitcnt vmcnt(7)\n\t"
      "v_mfma_f32_16x16x32_bf16 v[200:203], v[96:99], v[152:155], v[200:203]\n\t"
      "v_mul_f32_e32 v217, v217, v221\n\t"
      "v_mul_f32_e32 v218, v218, v222\n\t"
      "s_waitcnt vmcnt(6)\n\t"
      "v_mfma_f32_16x16x32_bf16 v[200:203], v[112:115], v[156:159], v[200:203]\n\t"
      "v_mul_f32_e32 v219, v219, v223\n\t"
      "v_cvt_pk_bf16_f32 v220, v216, v217\n\t"
      "v_mfma_f32_16x16x32_bf16 v[204:207], v[100:103], v[152:155], v[204:207]\n\t"
      "v_cvt_pk_bf16_f32 v221, v218, v219\n\t"
      "v_min_u32_e32 v220, -2, v220\n\t"
      "v_mfma_f32_16x16x32_bf16 v[204:207], v[116:119], v[156:159], v[204:207]\n\t"
      "s_nop 1\n\t"
      "v_cmp_u_f32_e32 vcc, v200, v200\n\t"
      "v_mfma_f32_16x16x32_bf16 v[208:211], v[104:107], v[152:155], v[208:211]\n\t"
      "v_min_u32_e32 v221, -2, v221\n\t"
      "global_store_dwordx2 v245, v[220:221], s[8:9] sc1\n\t"
      "s_add_u32 s8, s8, 131072\n\t"
      "s_addc_u32 s9, s9, 0\n\t"
      "v_mfma_f32_16x16x32_bf16 v[208:211], v[120:123], v[156:159], v[208:211]\n\t"
      "s_cbranch_vccnz NP1R_%=\n\t"
      "NP1B_%=:\n\t"
      "ds_write_b128 v243, v[200:203] offset:32768\n\t"
      "v_mfma_f32_16x16x32_bf16 v[212:215], v[108:111], v[152:155], v[212:215]\n\t"
      "global_load_dwordx4 v[152:155], v182, s[56:57] sc1\n\t"
      "v_mfma_f32_16x16x32_bf16 v[212:215], v[124:127], v[156:159], v[212:215]\n\t"
      "global_load_dwordx4 v[156:159], v183, s[56:57] sc1\n\t"
      "ds_write_b128 v164, v[204:207] offset:32768\n\t"
      "s_nop 0\n\t"
      "ds_write_b128 v165, v[208:211] offset:32768\n\t"
      "s_nop 3\n\t"
      "ds_write_b128 v166, v[212:215] offset:32768\n\t"
      "s_waitcnt lgkmcnt(0)\n\t"
      "s_barrier\n\t"
      "ds_read_b128 v[216:219], v244 offset:32768\n\t"
      "ds_read_b128 v[220:223], v244 offset:36864\n\t"
      "ds_read_b128 v[224:227], v244 offset:40960\n\t"
      "ds_read_b128 v[228:231], v244 offset:45056\n\t"
      "ds_read_b128 v[200:203], v244 offset:49152\n\t"
      "ds_read_b128 v[204:207], v244 offset:53248\n\t"
      "ds_read_b128 v[208:211], v244 offset:57344\n\t"
      "ds_read_b128 v[212:215], v244 offset:61440\n\t"
      "s_waitcnt lgkmcnt(6)\n\t"
      "v_add_f32_e32 v216, v216, v220\n\t"
      "v_add_f32_e32 v217, v217, v221\n\t"
      "v_add_f32_e32 v218, v218, v222\n\t"
      "v_add_f32_e32 v219, v219, v223\n\t"
      "s_waitcnt lgkmcnt(4)\n\t"
      "v_add_f32_e32 v224, v224, v228\n\t"
      "v_add_f32_e32 v225, v225, v229\n\t"
      "v_add_f32_e32 v226, v226, v230\n\t"
      "v_add_f32_e32 v227, v227, v231\n\t"
      "s_waitcnt lgkmcnt(2)\n\t"
      "v_add_f32_e32 v200, v200, v204\n\t"
      "v_add_f32_e32 v201, v201, v205\n\t"
      "v_add_f32_e32 v202, v202, v206\n\t"
      "v_add_f32_e32 v203, v203, v207\n\t"
      "v_add_f32_e32 v216, v216, v224\n\t"
      "v_add_f32_e32 v217, v217, v225\n\t"
      "v_add_f32_e32 v218, v218, v226\n\t"
      "v_add_f32_e32 v219, v219, v227\n\t"
      "s_waitcnt lgkmcnt(0)\n\t"
      "v_add_f32_e32 v208, v208, v212\n\t"
      "v_add_f32_e32 v209, v209, v213\n\t"
      "v_add_f32_e32 v210, v210, v214\n\t"
      "v_add_f32_e32 v211, v211, v215\n\t"
      "v_add_f32_e32 v200, v200, v208\n\t"
      "v_add_f32_e32 v201, v201, v209\n\t"
      "v_add_f32_e32 v202, v202, v210\n\t"
      "v_add_f32_e32 v203, v203, v211\n\t"
      "v_add_f32_e32 v216, v216, v200\n\t"
      "v_add_f32_e32 v217, v217, v201\n\t"
      "s_waitcnt vmcnt(8)\n\t"
      "v_mfma_f32_16x16x32_bf16 v[184:187], v[0:3], v[128:131], 0\n\t"
      "v_add_f32_e32 v218, v218, v202\n\t"
      "v_add_f32_e32 v219, v219, v203\n\t"
      "s_cmp_eq_u32 s17, 0\n\t"
      "s_cbranch_scc0 HI0_%=\n\t"
      "s_mov_b64 exec, 0xf\n\t"
      "global_store_dwordx2 v247, v[232:233], s[12:13] sc1\n\t"
      "s_mov_b64 exec, -1\n\t"
      "s_nop 0\n\t"
      "v_add_u32_e32 v232, 6, v232\n\t"
      "HI0_%=:\n\t"
      "v_mfma_f32_16x16x32_bf16 v[188:191], v[4:7], v[128:131], 0\n\t"
      "v_mul_f32_e32 v220, v216, v216\n\t"
      "v_mul_f32_e32 v221, v217, v217\n\t"
      "v_mfma_f32_16x16x32_bf16 v[192:195], v[8:11], v[128:131], 0\n\t"
      "v_mul_f32_e32 v222, v218, v218\n\t"
      "v_mul_f32_e32 v223, v219, v219\n\t"
      "v_mfma_f32_16x16x32_bf16 v[196:199], v[12:15], v[128:131], 0\n\t"
      "global_load_dwordx4 v[128:131], v176, s[58:59] sc1\n\t"
      "s_waitcnt vmcnt(8)\n\t"
      "v_mfma_f32_16x16x32_bf16 v[184:187], v[16:19], v[132:135], v[184:187]\n\t"
      "v_fmaak_f32 v220, v235, v220, 0xc0135761\n\t"
      "v_fmaak_f32 v221, v235, v221, 0xc0135761\n\t"
      "v_mfma_f32_16x16x32_bf16 v[188:191], v[20:23], v[132:135], v[188:191]\n\t"
      "v_fmaak_f32 v222, v235, v222, 0xc0135761\n\t"
      "v_fmaak_f32 v223, v235, v223, 0xc0135761\n\t"
      "v_mfma_f32_16x16x32_bf16 v[192:195], v[24:27], v[132:135], v[192:195]\n\t"
      "v_mul_f32_e32 v220, v216, v220\n\t"
      "v_mul_f32_e32 v221, v217, v221\n\t"
      "v_mfma_f32_16x16x32_bf16 v[196:199], v[28:31], v[132:135], v[196:199]\n\t"
      "global_load_dwordx4 v[132:135], v177, s[58:59] sc1\n\t"
      "s_waitcnt vmcnt(8)\n\t"
      "v_mfma_f32_16x16x32_bf16 v[184:187], v[32:35], v[136:139], v[184:187]\n\t"
      "v_mul_f32_e32 v222, v218, v222\n\t"
      "v_mul_f32_e32 v223, v219, v223\n\t"
      "v_mfma_f32_16x16x32_bf16 v[188:191], v[36:39], v[136:139], v[188:191]\n\t"
      "v_exp_f32_e32 v220, v220\n\t"
      "v_mfma_f32_16x16x32_bf16 v[192:195], v[40:43], v[136:139], v[192:195]\n\t"
      "v_exp_f32_e32 v221, v221\n\t"
      "v_mfma_f32_16x16x32_bf16 v[196:199], v[44:47], v[136:139], v[196:199]\n\t"
      "global_load_dwordx4 v[136:139], v178, s[58:59] sc1\n\t"
      "s_waitcnt vmcnt(8)\n\t"
      "v_mfma_f32_16x16x32_bf16 v[184:187], v[48:51], v[140:143], v[184:187]\n\t"
      "v_exp_f32_e32 v222, v222\n\t"
      "v_mfma_f32_16x16x32_bf16 v[188:191], v[52:55], v[140:143], v[188:191]\n\t"
      "v_exp_f32_e32 v223, v223\n\t"
      "v_mfma_f32_16x16x32_bf16 v[192:195], v[56:59], v[140:143], v[192:195]\n\t"
      "v_add_f32_e32 v220, 1.0, v220\n\t"
      "v_add_f32_e32 v221, 1.0, v221\n\t"
      "v_mfma_f32_16x16x32_bf16 v[196:199], v[60:63], v[140:143], v[196:199]\n\t"
      "global_load_dwordx4 v[140:143], v179, s[58:59] sc1\n\t"
      "s_waitcnt vmcnt(8)\n\t"
      "v_mfma_f32_16x16x32_bf16 v[184:187], v[64:67], v[144:147], v[184:187]\n\t"
      "v_add_f32_e32 v222, 1.0, v222\n\t"
      "v_add_f32_e32 v223, 1.0, v223\n\t"
      "v_mfma_f32_16x16x32_bf16 v[188:191], v[68:71], v[144:147], v[188:191]\n\t"
      "v_rcp_f32_e32 v220, v220\n\t"
      "v_mfma_f32_16x16x32_bf16 v[192:195], v[72:75], v[144:147], v[192:195]\n\t"
      "v_rcp_f32_e32 v221, v221\n\t"
      "v_mfma_f32_16x16x32_bf16 v[196:199], v[76:79], v[144:147], v[196:199]\n\t"
      "global_load_dwordx4 v[144:147], v180, s[58:59] sc1\n\t"
      "s_waitcnt vmcnt(8)\n\t"
      "v_mfma_f32_16x16x32_bf16 v[184:187], v[80:83], v[148:151], v[184:187]\n\t"
      "v_rcp_f32_e32 v222, v222\n\t"
      "v_mfma_f32_16x16x32_bf16 v[188:191], v[84:87], v[148:151], v[188:191]\n\t"
      "v_rcp_f32_e32 v223, v223\n\t"
      "v_mfma_f32_16x16x32_bf16 v[192:195], v[88:91], v[148:151], v[192:195]\n\t"
      "v_mul_f32_e32 v216, v216, v220\n\t"
      "v_mfma_f32_16x16x32_bf16 v[196:199], v[92:95], v[148:151], v[196:199]\n\t"
      "global_load_dwordx4 v[148:151], v181, s[58:59] sc1\n\t"
      "s_waitcnt vmcnt(7)\n\t"
      "v_mfma_f32_16x16x32_bf16 v[184:187], v[96:99], v[152:155], v[184:187]\n\t"
      "v_mul_f32_e32 v217, v217, v221\n\t"
      "v_mul_f32_e32 v218, v218, v222\n\t"
      "s_waitcnt vmcnt(6)\n\t"
      "v_mfma_f32_16x16x32_bf16 v[184:187], v[112:115], v[156:159], v[184:187]\n\t"
      "v_mul_f32_e32 v219, v219, v223\n\t"
      "v_cvt_pk_bf16_f32 v220, v216, v217\n\t"
      "v_mfma_f32_16x16x32_bf16 v[188:191], v[100:103], v[152:155], v[188:191]\n\t"
      "v_cvt_pk_bf16_f32 v221, v218, v219\n\t"
      "v_min_u32_e32 v220, -2, v220\n\t"
      "v_mfma_f32_16x16x32_bf16 v[188:191], v[116:119], v[156:159], v[188:191]\n\t"
      "s_nop 1\n\t"
      "v_cmp_u_f32_e32 vcc, v184, v184\n\t"
      "v_mfma_f32_16x16x32_bf16 v[192:195], v[104:107], v[152:155], v[192:195]\n\t"
      "v_min_u32_e32 v221, -2, v221\n\t"
      "global_store_dwordx2 v245, v[220:221], s[8:9] sc1\n\t"
      "s_add_u32 s8, s8, 131072\n\t"
      "s_addc_u32 s9, s9, 0\n\t"
      "v_mfma_f32_16x16x32_bf16 v[192:195], v[120:123], v[156:159], v[192:195]\n\t"
      "s_cbranch_vccnz NP2R_%=\n\t"
      "NP2B_%=:\n\t"
      "ds_write_b128 v243, v[184:187]\n\t"
      "v_mfma_f32_16x16x32_bf16 v[196:199], v[108:111], v[152:155], v[196:199]\n\t"
      "global_load_dwordx4 v[152:155], v182, s[58:59] sc1\n\t"
      "v_mfma_f32_16x16x32_bf16 v[196:199], v[124:127], v[156:159], v[196:199]\n\t"
      "global_load_dwordx4 v[156:159], v183, s[58:59] sc1\n\t"
      "ds_write_b128 v164, v[188:191]\n\t"
      "s_nop 0\n\t"
      "ds_write_b128 v165, v[192:195]\n\t"
      "s_nop 3\n\t"
      "ds_write_b128 v166, v[196:199]\n\t"
      "s_waitcnt lgkmcnt(0)\n\t"
      "s_barrier\n\t"
      "ds_read_b128 v[216:219], v244\n\t"
      "ds_read_b128 v[220:223], v244 offset:4096\n\t"
      "ds_read_b128 v[224:227], v244 offset:8192\n\t"
      "ds_read_b128 v[228:231], v244 offset:12288\n\t"
      "ds_read_b128 v[184:187], v244 offset:16384\n\t"
      "ds_read_b128 v[188:191], v244 offset:20480\n\t"
      "ds_read_b128 v[192:195], v244 offset:24576\n\t"
      "ds_read_b128 v[196:199], v244 offset:28672\n\t"
      "s_waitcnt lgkmcnt(6)\n\t"
      "v_add_f32_e32 v216, v216, v220\n\t"
      "v_add_f32_e32 v217, v217, v221\n\t"
      "v_add_f32_e32 v218, v218, v222\n\t"
      "v_add_f32_e32 v219, v219, v223\n\t"
      "s_waitcnt lgkmcnt(4)\n\t"
      "v_add_f32_e32 v224, v224, v228\n\t"
      "v_add_f32_e32 v225, v225, v229\n\t"
      "v_add_f32_e32 v226, v226, v230\n\t"
      "v_add_f32_e32 v227, v227, v231\n\t"
      "s_waitcnt lgkmcnt(2)\n\t"
      "v_add_f32_e32 v184, v184, v188\n\t"
      "v_add_f32_e32 v185, v185, v189\n\t"
      "v_add_f32_e32 v186, v186, v190\n\t"
      "v_add_f32_e32 v187, v187, v191\n\t"
      "v_add_f32_e32 v216, v216, v224\n\t"
      "v_add_f32_e32 v217, v217, v225\n\t"
      "v_add_f32_e32 v218, v218, v226\n\t"
      "v_add_f32_e32 v219, v219, v227\n\t"
      "s_waitcnt lgkmcnt(0)\n\t"
      "v_add_f32_e32 v192, v192, v196\n\t"
      "v_add_f32_e32 v193, v193, v197\n\t"
      "v_add_f32_e32 v194, v194, v198\n\t"
      "v_add_f32_e32 v195, v195, v199\n\t"
      "v_add_f32_e32 v184, v184, v192\n\t"
      "v_add_f32_e32 v185, v185, v193\n\t"
      "v_add_f32_e32 v186, v186, v194\n\t"
      "v_add_f32_e32 v187, v187, v195\n\t"
      "v_add_f32_e32 v216, v216, v184\n\t"
      "v_add_f32_e32 v217, v217, v185\n\t"
      "s_waitcnt vmcnt(8)\n\t"
      "v_mfma_f32_16x16x32_bf16 v[200:203], v[0:3], v[128:131], 0\n\t"
      "v_add_f32_e32 v218, v218, v186\n\t"
      "v_add_f32_e32 v219, v219, v187\n\t"
      "v_mfma_f32_16x16x32_bf16 v[204:207], v[4:7], v[128:131], 0\n\t"
      "v_mul_f32_e32 v220, v216, v216\n\t"
      "v_mul_f32_e32 v221, v217, v217\n\t"
      "v_mfma_f32_16x16x32_bf16 v[208:211], v[8:11], v[128:131], 0\n\t"
      "v_mul_f32_e32 v222, v218, v218\n\t"
      "v_mul_f32_e32 v223, v219, v219\n\t"
      "v_mfma_f32_16x16x32_bf16 v[212:215], v[12:15], v[128:131], 0\n\t"
      "global_load_dwordx4 v[128:131], v176, s[60:61] sc1\n\t"
      "s_waitcnt vmcnt(8)\n\t"
      "v_mfma_f32_16x16x32_bf16 v[200:203], v[16:19], v[132:135], v[200:203]\n\t"
      "v_fmaak_f32 v220, v235, v220, 0xc0135761\n\t"
      "v_fmaak_f32 v221, v235, v221, 0xc0135761\n\t"
      "v_mfma_f32_16x16x32_bf16 v[204:207], v[20:23], v[132:135], v[204:207]\n\t"
      "v_fmaak_f32 v222, v235, v222, 0xc0135761\n\t"
      "v_fmaak_f32 v223, v235, v223, 0xc0135761\n\t"
      "v_mfma_f32_16x16x32_bf16 v[208:211], v[24:27], v[132:135], v[208:211]\n\t"
      "v_mul_f32_e32 v220, v216, v220\n\t"
      "v_mul_f32_e32 v221, v217, v221\n\t"
      "v_mfma_f32_16x16x32_bf16 v[212:215], v[28:31], v[132:135], v[212:215]\n\t"
      "global_load_dwordx4 v[132:135], v177, s[60:61] sc1\n\t"
      "s_waitcnt vmcnt(8)\n\t"
      "v_mfma_f32_16x16x32_bf16 v[200:203], v[32:35], v[136:139], v[200:203]\n\t"
      "v_mul_f32_e32 v222, v218, v222\n\t"
      "v_mul_f32_e32 v223, v219, v223\n\t"
      "v_mfma_f32_16x16x32_bf16 v[204:207], v[36:39], v[136:139], v[204:207]\n\t"
      "v_exp_f32_e32 v220, v220\n\t"
      "v_mfma_f32_16x16x32_bf16 v[208:211], v[40:43], v[136:139], v[208:211]\n\t"
      "v_exp_f32_e32 v221, v221\n\t"
      "v_mfma_f32_16x16x32_bf16 v[212:215], v[44:47], v[136:139], v[212:215]\n\t"
      "global_load_dwordx4 v[136:139], v178, s[60:61] sc1\n\t"
      "s_waitcnt vmcnt(8)\n\t"
      "v_mfma_f32_16x16x32_bf16 v[200:203], v[48:51], v[140:143], v[200:203]\n\t"
      "v_exp_f32_e32 v222, v222\n\t"
      "v_mfma_f32_16x16x32_bf16 v[204:207], v[52:55], v[140:143], v[204:207]\n\t"
      "v_exp_f32_e32 v223, v223\n\t"
      "v_mfma_f32_16x16x32_bf16 v[208:211], v[56:59], v[140:143], v[208:211]\n\t"
      "v_add_f32_e32 v220, 1.0, v220\n\t"
      "v_add_f32_e32 v221, 1.0, v221\n\t"
      "v_mfma_f32_16x16x32_bf16 v[212:215], v[60:63], v[140:143], v[212:215]\n\t"
      "global_load_dwordx4 v[140:143], v179, s[60:61] sc1\n\t"
      "s_waitcnt vmcnt(8)\n\t"
      "v_mfma_f32_16x16x32_bf16 v[200:203], v[64:67], v[144:147], v[200:203]\n\t"
      "v_add_f32_e32 v222, 1.0, v222\n\t"
      "v_add_f32_e32 v223, 1.0, v223\n\t"
      "v_mfma_f32_16x16x32_bf16 v[204:207], v[68:71], v[144:147], v[204:207]\n\t"
      "v_rcp_f32_e32 v220, v220\n\t"
      "v_mfma_f32_16x16x32_bf16 v[208:211], v[72:75], v[144:147], v[208:211]\n\t"
      "v_rcp_f32_e32 v221, v221\n\t"
      "v_mfma_f32_16x16x32_bf16 v[212:215], v[76:79], v[144:147], v[212:215]\n\t"
      "global_load_dwordx4 v[144:147], v180, s[60:61] sc1\n\t"
      "s_waitcnt vmcnt(8)\n\t"
      "v_mfma_f32_16x16x32_bf16 v[200:203], v[80:83], v[148:151], v[200:203]\n\t"
      "v_rcp_f32_e32 v222, v222\n\t"
      "v_mfma_f32_16x16x32_bf16 v[204:207], v[84:87], v[148:151], v[204:207]\n\t"
      "v_rcp_f32_e32 v223, v223\n\t"
      "v_mfma_f32_16x16x32_bf16 v[208:211], v[88:91], v[148:151], v[208:211]\n\t"
      "v_mul_f32_e32 v216, v216, v220\n\t"
      "v_mfma_f32_16x16x32_bf16 v[212:215], v[92:95], v[148:151], v[212:215]\n\t"
      "global_load_dwordx4 v[148:151], v181, s[60:61] sc1\n\t"
      "s_waitcnt vmcnt(7)\n\t"
      "v_mfma_f32_16x16x32_bf16 v[200:203], v[96:99], v[152:155], v[200:203]\n\t"
      "v_mul_f32_e32 v217, v217, v221\n\t"
      "v_mul_f32_e32 v218, v218, v222\n\t"
      "s_waitcnt vmcnt(6)\n\t"
      "v_mfma_f32_16x16x32_bf16 v[200:203], v[112:115], v[156:159], v[200:203]\n\t"
      "v_mul_f32_e32 v219, v219, v223\n\t"
      "v_cvt_pk_bf16_f32 v220, v216, v217\n\t"
      "v_mfma_f32_16x16x32_bf16 v[204:207], v[100:103], v[152:155], v[204:207]\n\t"
      "v_cvt_pk_bf16_f32 v221, v218, v219\n\t"
      "v_min_u32_e32 v220, -2, v220\n\t"
      "v_mfma_f32_16x16x32_bf16 v[204:207], v[116:119], v[156:159], v[204:207]\n\t"
      "s_nop 1\n\t"
      "v_cmp_u_f32_e32 vcc, v200, v200\n\t"
      "v_mfma_f32_16x16x32_bf16 v[208:211], v[104:107], v[152:155], v[208:211]\n\t"
      "v_min_u32_e32 v221, -2, v221\n\t"
      "global_store_dwordx2 v245, v[220:221], s[8:9] sc1\n\t"
      "s_add_u32 s8, s8, 131072\n\t"
      "s_addc_u32 s9, s9, 0\n\t"
      "v_mfma_f32_16x16x32_bf16 v[208:211], v[120:123], v[156:159], v[208:211]\n\t"
      "s_cbranch_vccnz NP3R_%=\n\t"
      "NP3B_%=:\n\t"
      "ds_write_b128 v243, v[200:203] offset:32768\n\t"
      "v_mfma_f32_16x16x32_bf16 v[212:215], v[108:111], v[152:155], v[212:215]\n\t"
      "global_load_dwordx4 v[152:155], v182, s[60:61] sc1\n\t"
      "v_mfma_f32_16x16x32_bf16 v[212:215], v[124:127], v[156:159], v[212:215]\n\t"
      "global_load_dwordx4 v[156:159], v183, s[60:61] sc1\n\t"
      "ds_write_b128 v164, v[204:207] offset:32768\n\t"
      "s_nop 0\n\t"
      "ds_write_b128 v165, v[208:211] offset:32768\n\t"
      "s_nop 3\n\t"
      "ds_write_b128 v166, v[212:215] offset:32768\n\t"
      "s_waitcnt lgkmcnt(0)\n\t"
      "s_barrier\n\t"
      "ds_read_b128 v[216:219], v244 offset:32768\n\t"
      "ds_read_b128 v[220:223], v244 offset:36864\n\t"
      "ds_read_b128 v[224:227], v244 offset:40960\n\t"
      "ds_read_b128 v[228:231], v244 offset:45056\n\t"
      "ds_read_b128 v[200:203], v244 offset:49152\n\t"
      "ds_read_b128 v[204:207], v244 offset:53248\n\t"
      "ds_read_b128 v[208:211], v244 offset:57344\n\t"
      "ds_read_b128 v[212:215], v244 offset:61440\n\t"
      "s_waitcnt lgkmcnt(6)\n\t"
      "v_add_f32_e32 v216, v216, v220\n\t"
      "v_add_f32_e32 v217, v217, v221\n\t"
      "v_add_f32_e32 v218, v218, v222\n\t"
      "v_add_f32_e32 v219, v219, v223\n\t"
      "s_waitcnt lgkmcnt(4)\n\t"
      "v_add_f32_e32 v224, v224, v228\n\t"
      "v_add_f32_e32 v225, v225, v229\n\t"
      "v_add_f32_e32 v226, v226, v230\n\t"
      "v_add_f32_e32 v227, v227, v231\n\t"
      "s_waitcnt lgkmcnt(2)\n\t"
      "v_add_f32_e32 v200, v200, v204\n\t"
      "v_add_f32_e32 v201, v201, v205\n\t"
      "v_add_f32_e32 v202, v202, v206\n\t"
      "v_add_f32_e32 v203, v203, v207\n\t"
      "v_add_f32_e32 v216, v216, v224\n\t"
      "v_add_f32_e32 v217, v217, v225\n\t"
      "v_add_f32_e32 v218, v218, v226\n\t"
      "v_add_f32_e32 v219, v219, v227\n\t"
      "s_waitcnt lgkmcnt(0)\n\t"
      "v_add_f32_e32 v208, v208, v212\n\t"
      "v_add_f32_e32 v209, v209, v213\n\t"
      "v_add_f32_e32 v210, v210, v214\n\t"
      "v_add_f32_e32 v211, v211, v215\n\t"
      "v_add_f32_e32 v200, v200, v208\n\t"
      "v_add_f32_e32 v201, v201, v209\n\t"
      "v_add_f32_e32 v202, v202, v210\n\t"
      "v_add_f32_e32 v203, v203, v211\n\t"
      "v_add_f32_e32 v216, v216, v200\n\t"
      "v_add_f32_e32 v217, v217, v201\n\t"
      "s_waitcnt vmcnt(8)\n\t"
      "v_mfma_f32_16x16x32_bf16 v[184:187], v[0:3], v[128:131], 0\n\t"
      "v_add_f32_e32 v218, v218, v202\n\t"
      "v_add_f32_e32 v219, v219, v203\n\t"
      "v_mfma_f32_16x16x32_bf16 v[188:191], v[4:7], v[128:131], 0\n\t"
      "v_mul_f32_e32 v220, v216, v216\n\t"
      "v_mul_f32_e32 v221, v217, v217\n\t"
      "v_mfma_f32_16x16x32_bf16 v[192:195], v[8:11], v[128:131], 0\n\t"
      "v_mul_f32_e32 v222, v218, v218\n\t"
      "v_mul_f32_e32 v223, v219, v219\n\t"
      "v_mfma_f32_16x16x32_bf16 v[196:199], v[12:15], v[128:131], 0\n\t"
      "global_load_dwordx4 v[128:131], v176, s[62:63] sc1\n\t"
      "s_waitcnt vmcnt(8)\n\t"
      "v_mfma_f32_16x16x32_bf16 v[184:187], v[16:19], v[132:135], v[184:187]\n\t"
      "v_fmaak_f32 v220, v235, v220, 0xc0135761\n\t"
      "v_fmaak_f32 v221, v235, v221, 0xc0135761\n\t"
      "v_mfma_f32_16x16x32_bf16 v[188:191], v[20:23], v[132:135], v[188:191]\n\t"
      "v_fmaak_f32 v222, v235, v222, 0xc0135761\n\t"
      "v_fmaak_f32 v223, v235, v223, 0xc0135761\n\t"
      "v_mfma_f32_16x16x32_bf16 v[192:195], v[24:27], v[132:135], v[192:195]\n\t"
      "v_mul_f32_e32 v220, v216, v220\n\t"
      "v_mul_f32_e32 v221, v217, v221\n\t"
      "v_mfma_f32_16x16x32_bf16 v[196:199], v[28:31], v[132:135], v[196:199]\n\t"
      "global_load_dwordx4 v[132:135], v177, s[62:63] sc1\n\t"
      "s_waitcnt vmcnt(8)\n\t"
      "v_mfma_f32_16x16x32_bf16 v[184:187], v[32:35], v[136:139], v[184:187]\n\t"
      "v_mul_f32_e32 v222, v218, v222\n\t"
      "v_mul_f32_e32 v223, v219, v223\n\t"
      "v_mfma_f32_16x16x32_bf16 v[188:191], v[36:39], v[136:139], v[188:191]\n\t"
      "v_exp_f32_e32 v220, v220\n\t"
      "v_mfma_f32_16x16x32_bf16 v[192:195], v[40:43], v[136:139], v[192:195]\n\t"
      "v_exp_f32_e32 v221, v221\n\t"
      "v_mfma_f32_16x16x32_bf16 v[196:199], v[44:47], v[136:139], v[196:199]\n\t"
      "global_load_dwordx4 v[136:139], v178, s[62:63] sc1\n\t"
      "s_waitcnt vmcnt(8)\n\t"
      "v_mfma_f32_16x16x32_bf16 v[184:187], v[48:51], v[140:143], v[184:187]\n\t"
      "v_exp_f32_e32 v222, v222\n\t"
      "v_mfma_f32_16x16x32_bf16 v[188:191], v[52:55], v[140:143], v[188:191]\n\t"
      "v_exp_f32_e32 v223, v223\n\t"
      "v_mfma_f32_16x16x32_bf16 v[192:195], v[56:59], v[140:143], v[192:195]\n\t"
      "v_add_f32_e32 v220, 1.0, v220\n\t"
      "v_add_f32_e32 v221, 1.0, v221\n\t"
      "v_mfma_f32_16x16x32_bf16 v[196:199], v[60:63], v[140:143], v[196:199]\n\t"
      "global_load_dwordx4 v[140:143], v179, s[62:63] sc1\n\t"
      "s_waitcnt vmcnt(8)\n\t"
      "v_mfma_f32_16x16x32_bf16 v[184:187], v[64:67], v[144:147], v[184:187]\n\t"
      "v_add_f32_e32 v222, 1.0, v222\n\t"
      "v_add_f32_e32 v223, 1.0, v223\n\t"
      "v_mfma_f32_16x16x32_bf16 v[188:191], v[68:71], v[144:147], v[188:191]\n\t"
      "v_rcp_f32_e32 v220, v220\n\t"
      "v_mfma_f32_16x16x32_bf16 v[192:195], v[72:75], v[144:147], v[192:195]\n\t"
      "v_rcp_f32_e32 v221, v221\n\t"
      "v_mfma_f32_16x16x32_bf16 v[196:199], v[76:79], v[144:147], v[196:199]\n\t"
      "global_load_dwordx4 v[144:147], v180, s[62:63] sc1\n\t"
      "s_waitcnt vmcnt(8)\n\t"
      "v_mfma_f32_16x16x32_bf16 v[184:187], v[80:83], v[148:151], v[184:187]\n\t"
      "v_rcp_f32_e32 v222, v222\n\t"
      "v_mfma_f32_16x16x32_bf16 v[188:191], v[84:87], v[148:151], v[188:191]\n\t"
      "v_rcp_f32_e32 v223, v223\n\t"
      "v_mfma_f32_16x16x32_bf16 v[192:195], v[88:91], v[148:151], v[192:195]\n\t"
      "v_mul_f32_e32 v216, v216, v220\n\t"
      "v_mfma_f32_16x16x32_bf16 v[196:199], v[92:95], v[148:151], v[196:199]\n\t"
      "global_load_dwordx4 v[148:151], v181, s[62:63] sc1\n\t"
      "s_waitcnt vmcnt(7)\n\t"
      "v_mfma_f32_16x16x32_bf16 v[184:187], v[96:99], v[152:155], v[184:187]\n\t"
      "v_mul_f32_e32 v217, v217, v221\n\t"
      "v_mul_f32_e32 v218, v218, v222\n\t"
      "s_waitcnt vmcnt(6)\n\t"
      "v_mfma_f32_16x16x32_bf16 v[184:187], v[112:115], v[156:159], v[184:187]\n\t"
      "v_mul_f32_e32 v219, v219, v223\n\t"
      "v_cvt_pk_bf16_f32 v220, v216, v217\n\t"
      "v_mfma_f32_16x16x32_bf16 v[188:191], v[100:103], v[152:155], v[188:191]\n\t"
      "v_cvt_pk_bf16_f32 v221, v218, v219\n\t"
      "v_min_u32_e32 v220, -2, v220\n\t"
      "v_mfma_f32_16x16x32_bf16 v[188:191], v[116:119], v[156:159], v[188:191]\n\t"
      "s_nop 1\n\t"
      "v_cmp_u_f32_e32 vcc, v184, v184\n\t"
      "v_mfma_f32_16x16x32_bf16 v[192:195], v[104:107], v[152:155], v[192:195]\n\t"
      "v_min_u32_e32 v221, -2, v221\n\t"
      "global_store_dwordx2 v245, v[220:221], s[8:9] sc1\n\t"
      "s_add_u32 s8, s8, 131072\n\t"
      "s_addc_u32 s9, s9, 0\n\t"
      "v_mfma_f32_16x16x32_bf16 v[192:195], v[120:123], v[156:159], v[192:195]\n\t"
      "s_cbranch_vccnz NP4R_%=\n\t"
      "NP4B_%=:\n\t"
      "ds_write_b128 v243, v[184:187]\n\t"
      "v_mfma_f32_16x16x32_bf16 v[196:199], v[108:111], v[152:155], v[196:199]\n\t"
      "global_load_dwordx4 v[152:155], v182, s[62:63] sc1\n\t"
      "v_mfma_f32_16x16x32_bf16 v[196:199], v[124:127], v[156:159], v[196:199]\n\t"
      "global_load_dwordx4 v[156:159], v183, s[62:63] sc1\n\t"
      "ds_write_b128 v164, v[188:191]\n\t"
      "s_nop 0\n\t"
      "ds_write_b128 v165, v[192:195]\n\t"
      "s_nop 3\n\t"
      "ds_write_b128 v166, v[196:199]\n\t"
      "s_waitcnt lgkmcnt(0)\n\t"
      "s_barrier\n\t"
      "ds_read_b128 v[216:219], v244\n\t"
      "ds_read_b128 v[220:223], v244 offset:4096\n\t"
      "ds_read_b128 v[224:227], v244 offset:8192\n\t"
      "ds_read_b128 v[228:231], v244 offset:12288\n\t"
      "ds_read_b128 v[184:187], v244 offset:16384\n\t"
      "ds_read_b128 v[188:191], v244 offset:20480\n\t"
      "ds_read_b128 v[192:195], v244 offset:24576\n\t"
      "ds_read_b128 v[196:199], v244 offset:28672\n\t"
      "s_waitcnt lgkmcnt(6)\n\t"
      "v_add_f32_e32 v216, v216, v220\n\t"
      "v_add_f32_e32 v217, v217, v221\n\t"
      "v_add_f32_e32 v218, v218, v222\n\t"
      "v_add_f32_e32 v219, v219, v223\n\t"
      "s_waitcnt lgkmcnt(4)\n\t"
      "v_add_f32_e32 v224, v224, v228\n\t"
      "v_add_f32_e32 v225, v225, v229\n\t"
      "v_add_f32_e32 v226, v226, v230\n\t"
      "v_add_f32_e32 v227, v227, v231\n\t"
      "s_waitcnt lgkmcnt(2)\n\t"
      "v_add_f32_e32 v184, v184, v188\n\t"
      "v_add_f32_e32 v185, v185, v189\n\t"
      "v_add_f32_e32 v186, v186, v190\n\t"
      "v_add_f32_e32 v187, v187, v191\n\t"
      "v_add_f32_e32 v216, v216, v224\n\t"
      "v_add_f32_e32 v217, v217, v225\n\t"
      "v_add_f32_e32 v218, v218, v226\n\t"
      "v_add_f32_e32 v219, v219, v227\n\t"
      "s_waitcnt lgkmcnt(0)\n\t"
      "v_add_f32_e32 v192, v192, v196\n\t"
      "v_add_f32_e32 v193, v193, v197\n\t"
      "v_add_f32_e32 v194, v194, v198\n\t"
      "v_add_f32_e32 v195, v195, v199\n\t"
      "v_add_f32_e32 v184, v184, v192\n\t"
      "v_add_f32_e32 v185, v185, v193\n\t"
      "v_add_f32_e32 v186, v186, v194\n\t"
      "v_add_f32_e32 v187, v187, v195\n\t"
      "v_add_f32_e32 v216, v216, v184\n\t"
      "v_add_f32_e32 v217, v217, v185\n\t"
      "s_waitcnt vmcnt(8)\n\t"
      "v_mfma_f32_16x16x32_bf16 v[200:203], v[0:3], v[128:131], 0\n\t"
      "v_add_f32_e32 v218, v218, v186\n\t"
      "v_add_f32_e32 v219, v219, v187\n\t"
      "v_mfma_f32_16x16x32_bf16 v[204:207], v[4:7], v[128:131], 0\n\t"
      "v_mul_f32_e32 v220, v216, v216\n\t"
      "v_mul_f32_e32 v221, v217, v217\n\t"
      "v_mfma_f32_16x16x32_bf16 v[208:211], v[8:11], v[128:131], 0\n\t"
      "v_mul_f32_e32 v222, v218, v218\n\t"
      "v_mul_f32_e32 v223, v219, v219\n\t"
      "v_mfma_f32_16x16x32_bf16 v[212:215], v[12:15], v[128:131], 0\n\t"
      "global_load_dwordx4 v[128:131], v176, s[64:65] sc1\n\t"
      "s_waitcnt vmcnt(8)\n\t"
      "v_mfma_f32_16x16x32_bf16 v[200:203], v[16:19], v[132:135], v[200:203]\n\t"
      "v_fmaak_f32 v220, v235, v220, 0xc0135761\n\t"
      "v_fmaak_f32 v221, v235, v221, 0xc0135761\n\t"
      "v_mfma_f32_16x16x32_bf16 v[204:207], v[20:23], v[132:135], v[204:207]\n\t"
      "v_fmaak_f32 v222, v235, v222, 0xc0135761\n\t"
      "v_fmaak_f32 v223, v235, v223, 0xc0135761\n\t"
      "v_mfma_f32_16x16x32_bf16 v[208:211], v[24:27], v[132:135], v[208:211]\n\t"
      "v_mul_f32_e32 v220, v216, v220\n\t"
      "v_mul_f32_e32 v221, v217, v221\n\t"
      "v_mfma_f32_16x16x32_bf16 v[212:215], v[28:31], v[132:135], v[212:215]\n\t"
      "global_load_dwordx4 v[132:135], v177, s[64:65] sc1\n\t"
      "s_waitcnt vmcnt(8)\n\t"
      "v_mfma_f32_16x16x32_bf16 v[200:203], v[32:35], v[136:139], v[200:203]\n\t"
      "v_mul_f32_e32 v222, v218, v222\n\t"
      "v_mul_f32_e32 v223, v219, v223\n\t"
      "v_mfma_f32_16x16x32_bf16 v[204:207], v[36:39], v[136:139], v[204:207]\n\t"
      "v_exp_f32_e32 v220, v220\n\t"
      "v_mfma_f32_16x16x32_bf16 v[208:211], v[40:43], v[136:139], v[208:211]\n\t"
      "v_exp_f32_e32 v221, v221\n\t"
      "v_mfma_f32_16x16x32_bf16 v[212:215], v[44:47], v[136:139], v[212:215]\n\t"
      "global_load_dwordx4 v[136:139], v178, s[64:65] sc1\n\t"
      "s_waitcnt vmcnt(8)\n\t"
      "v_mfma_f32_16x16x32_bf16 v[200:203], v[48:51], v[140:143], v[200:203]\n\t"
      "v_exp_f32_e32 v222, v222\n\t"
      "v_mfma_f32_16x16x32_bf16 v[204:207], v[52:55], v[140:143], v[204:207]\n\t"
      "v_exp_f32_e32 v223, v223\n\t"
      "v_mfma_f32_16x16x32_bf16 v[208:211], v[56:59], v[140:143], v[208:211]\n\t"
      "v_add_f32_e32 v220, 1.0, v220\n\t"
      "v_add_f32_e32 v221, 1.0, v221\n\t"
      "v_mfma_f32_16x16x32_bf16 v[212:215], v[60:63], v[140:143], v[212:215]\n\t"
      "global_load_dwordx4 v[140:143], v179, s[64:65] sc1\n\t"
      "s_waitcnt vmcnt(8)\n\t"
      "v_mfma_f32_16x16x32_bf16 v[200:203], v[64:67], v[144:147], v[200:203]\n\t"
      "v_add_f32_e32 v222, 1.0, v222\n\t"
      "v_add_f32_e32 v223, 1.0, v223\n\t"
      "v_mfma_f32_16x16x32_bf16 v[204:207], v[68:71], v[144:147], v[204:207]\n\t"
      "v_rcp_f32_e32 v220, v220\n\t"
      "v_mfma_f32_16x16x32_bf16 v[208:211], v[72:75], v[144:147], v[208:211]\n\t"
      "v_rcp_f32_e32 v221, v221\n\t"
      "v_mfma_f32_16x16x32_bf16 v[212:215], v[76:79], v[144:147], v[212:215]\n\t"
      "global_load_dwordx4 v[144:147], v180, s[64:65] sc1\n\t"
      "s_waitcnt vmcnt(8)\n\t"
      "v_mfma_f32_16x16x32_bf16 v[200:203], v[80:83], v[148:151], v[200:203]\n\t"
      "v_rcp_f32_e32 v222, v222\n\t"
      "v_mfma_f32_16x16x32_bf16 v[204:207], v[84:87], v[148:151], v[204:207]\n\t"
      "v_rcp_f32_e32 v223, v223\n\t"
      "v_mfma_f32_16x16x32_bf16 v[208:211], v[88:91], v[148:151], v[208:211]\n\t"
      "v_mul_f32_e32 v216, v216, v220\n\t"
      "v_mfma_f32_16x16x32_bf16 v[212:215], v[92:95], v[148:151], v[212:215]\n\t"
      "global_load_dwordx4 v[148:151], v181, s[64:65] sc1\n\t"
      "s_waitcnt vmcnt(7)\n\t"
      "v_mfma_f32_16x16x32_bf16 v[200:203], v[96:99], v[152:155], v[200:203]\n\t"
      "v_mul_f32_e32 v217, v217, v221\n\t"
      "v_mul_f32_e32 v218, v218, v222\n\t"
      "s_waitcnt vmcnt(6)\n\t"
      "v_mfma_f32_16x16x32_bf16 v[200:203], v[112:115], v[156:159], v[200:203]\n\t"
      "v_mul_f32_e32 v219, v219, v223\n\t"
      "v_cvt_pk_bf16_f32 v220, v216, v217\n\t"
      "v_mfma_f32_16x16x32_bf16 v[204:207], v[100:103], v[152:155], v[204:207]\n\t"
      "v_cvt_pk_bf16_f32 v221, v218, v219\n\t"
      "v_min_u32_e32 v220, -2, v220\n\t"
      "v_mfma_f32_16x16x32_bf16 v[204:207], v[116:119], v[156:159], v[204:207]\n\t"
      "s_nop 1\n\t"
      "v_cmp_u_f32_e32 vcc, v200, v200\n\t"
      "v_mfma_f32_16x16x32_bf16 v[208:211], v[104:107], v[152:155], v[208:211]\n\t"
      "v_min_u32_e32 v221, -2, v221\n\t"
      "global_store_dwordx2 v245, v[220:221], s[8:9] sc1\n\t"
      "s_add_u32 s8, s8, 131072\n\t"
      "s_addc_u32 s9, s9, 0\n\t"
      "v_mfma_f32_16x16x32_bf16 v[208:211], v[120:123], v[156:159], v[208:211]\n\t"
      "s_cbranch_vccnz NP5R_%=\n\t"
      "NP5B_%=:\n\t"
      "ds_write_b128 v243, v[200:203] offset:32768\n\t"
      "v_mfma_f32_16x16x32_bf16 v[212:215], v[108:111], v[152:155], v[212:215]\n\t"
      "global_load_dwordx4 v[152:155], v182, s[64:65] sc1\n\t"
      "v_mfma_f32_16x16x32_bf16 v[212:215], v[124:127], v[156:159], v[212:215]\n\t"
      "global_load_dwordx4 v[156:159], v183, s[64:65] sc1\n\t"
      "ds_write_b128 v164, v[204:207] offset:32768\n\t"
      "s_nop 0\n\t"
      "ds_write_b128 v165, v[208:211] offset:32768\n\t"
      "s_nop 3\n\t"
      "ds_write_b128 v166, v[212:215] offset:32768\n\t"
      "s_waitcnt lgkmcnt(0)\n\t"
      "s_barrier\n\t"
      "ds_read_b128 v[216:219], v244 offset:32768\n\t"
      "ds_read_b128 v[220:223], v244 offset:36864\n\t"
      "ds_read_b128 v[224:227], v244 offset:40960\n\t"
      "ds_read_b128 v[228:231], v244 offset:45056\n\t"
      "ds_read_b128 v[200:203], v244 offset:49152\n\t"
      "ds_read_b128 v[204:207], v244 offset:53248\n\t"
      "ds_read_b128 v[208:211], v244 offset:57344\n\t"
      "ds_read_b128 v[212:215], v244 offset:61440\n\t"
      "s_waitcnt lgkmcnt(6)\n\t"
      "v_add_f32_e32 v216, v216, v220\n\t"
      "v_add_f32_e32 v217, v217, v221\n\t"
      "v_add_f32_e32 v218, v218, v222\n\t"
      "v_add_f32_e32 v219, v219, v223\n\t"
      "s_waitcnt lgkmcnt(4)\n\t"
      "v_add_f32_e32 v224, v224, v228\n\t"
      "v_add_f32_e32 v225, v225, v229\n\t"
      "v_add_f32_e32 v226, v226, v230\n\t"
      "v_add_f32_e32 v227, v227, v231\n\t"
      "s_waitcnt lgkmcnt(2)\n\t"
      "v_add_f32_e32 v200, v200, v204\n\t"
      "v_add_f32_e32 v201, v201, v205\n\t"
      "v_add_f32_e32 v202, v202, v206\n\t"
      "v_add_f32_e32 v203, v203, v207\n\t"
      "s_waitcnt vmcnt(8)\n\t"
      "v_mfma_f32_16x16x32_bf16 v[184:187], v[0:3], v[128:131], 0\n\t"
      "v_add_f32_e32 v216, v216, v224\n\t"
      "v_add_f32_e32 v217, v217, v225\n\t"
      "v_mfma_f32_16x16x32_bf16 v[188:191], v[4:7], v[128:131], 0\n\t"
      "v_add_f32_e32 v218, v218, v226\n\t"
      "v_add_f32_e32 v219, v219, v227\n\t"
      "v_mfma_f32_16x16x32_bf16 v[192:195], v[8:11], v[128:131], 0\n\t"
      "s_waitcnt lgkmcnt(0)\n\t"
      "v_add_f32_e32 v208, v208, v212\n\t"
      "v_add_f32_e32 v209, v209, v213\n\t"
      "v_mfma_f32_16x16x32_bf16 v[196:199], v[12:15], v[128:131], 0\n\t"
      "v_add_f32_e32 v210, v210, v214\n\t"
      "v_add_f32_e32 v211, v211, v215\n\t"
      "s_waitcnt vmcnt(7)\n\t"
      "v_mfma_f32_16x16x32_bf16 v[184:187], v[16:19], v[132:135], v[184:187]\n\t"
      "v_add_f32_e32 v200, v200, v208\n\t"
      "v_add_f32_e32 v201, v201, v209\n\t"
      "v_mfma_f32_16x16x32_bf16 v[188:191], v[20:23], v[132:135], v[188:191]\n\t"
      "v_add_f32_e32 v202, v202, v210\n\t"
      "v_add_f32_e32 v203, v203, v211\n\t"
      "v_mfma_f32_16x16x32_bf16 v[192:195], v[24:27], v[132:135], v[192:195]\n\t"
      "v_add_f32_e32 v216, v216, v200\n\t"
      "v_add_f32_e32 v217, v217, v201\n\t"
      "v_mfma_f32_16x16x32_bf16 v[196:199], v[28:31], v[132:135], v[196:199]\n\t"
      "v_add_f32_e32 v218, v218, v202\n\t"
      "v_add_f32_e32 v219, v219, v203\n\t"
      "s_waitcnt vmcnt(6)\n\t"
      "v_mfma_f32_16x16x32_bf16 v[184:187], v[32:35], v[136:139], v[184:187]\n\t"
      "v_mul_f32_e32 v220, v216, v216\n\t"
      "v_mul_f32_e32 v221, v217, v217\n\t"
      "v_mfma_f32_16x16x32_bf16 v[188:191], v[36:39], v[136:139], v[188:191]\n\t"
      "v_mul_f32_e32 v222, v218, v218\n\t"
      "v_mul_f32_e32 v223, v219, v219\n\t"
      "v_mfma_f32_16x16x32_bf16 v[192:195], v[40:43], v[136:139], v[192:195]\n\t"
      "v_fmaak_f32 v220, v235, v220, 0xc0135761\n\t"
      "v_fmaak_f32 v221, v235, v221, 0xc0135761\n\t"
      "v_mfma_f32_16x16x32_bf16 v[196:199], v[44:47], v[136:139], v[196:199]\n\t"
      "v_fmaak_f32 v222, v235, v222, 0xc0135761\n\t"
      "v_fmaak_f32 v223, v235, v223, 0xc0135761\n\t"
      "s_waitcnt vmcnt(5)\n\t"
      "v_mfma_f32_16x16x32_bf16 v[184:187], v[48:51], v[140:143], v[184:187]\n\t"
      "v_mul_f32_e32 v220, v216, v220\n\t"
      "v_mul_f32_e32 v221, v217, v221\n\t"
      "v_mfma_f32_16x16x32_bf16 v[188:191], v[52:55], v[140:143], v[188:191]\n\t"
      "v_mul_f32_e32 v222, v218, v222\n\t"
      "v_mul_f32_e32 v223, v219, v223\n\t"
      "v_mfma_f32_16x16x32_bf16 v[192:195], v[56:59], v[140:143], v[192:195]\n\t"
      "v_exp_f32_e32 v220, v220\n\t"
      "v_mfma_f32_16x16x32_bf16 v[196:199], v[60:63], v[140:143], v[196:199]\n\t"
      "v_exp_f32_e32 v221, v221\n\t"
      "s_waitcnt vmcnt(4)\n\t"
      "v_mfma_f32_16x16x32_bf16 v[184:187], v[64:67], v[144:147], v[184:187]\n\t"
      "v_exp_f32_e32 v222, v222\n\t"
      "v_mfma_f32_16x16x32_bf16 v[188:191], v[68:71], v[144:147], v[188:191]\n\t"
      "v_exp_f32_e32 v223, v223\n\t"
      "v_mfma_f32_16x16x32_bf16 v[192:195], v[72:75], v[144:147], v[192:195]\n\t"
      "v_add_f32_e32 v220, 1.0, v220\n\t"
      "v_add_f32_e32 v221, 1.0, v221\n\t"
      "v_mfma_f32_16x16x32_bf16 v[196:199], v[76:79], v[144:147], v[196:199]\n\t"
      "v_add_f32_e32 v222, 1.0, v222\n\t"
      "v_add_f32_e32 v223, 1.0, v223\n\t"
      "s_waitcnt vmcnt(3)\n\t"
      "v_mfma_f32_16x16x32_bf16 v[184:187], v[80:83], v[148:151], v[184:187]\n\t"
      "v_rcp_f32_e32 v220, v220\n\t"
      "v_mfma_f32_16x16x32_bf16 v[188:191], v[84:87], v[148:151], v[188:191]\n\t"
      "v_rcp_f32_e32 v221, v221\n\t"
      "v_mfma_f32_16x16x32_bf16 v[192:195], v[88:91], v[148:151], v[192:195]\n\t"
      "v_rcp_f32_e32 v222, v222\n\t"
      "v_mfma_f32_16x16x32_bf16 v[196:199], v[92:95], v[148:151], v[196:199]\n\t"
      "v_rcp_f32_e32 v223, v223\n\t"
      "s_waitcnt vmcnt(1)\n\t"
      "v_mfma_f32_16x16x32_bf16 v[184:187], v[96:99], v[152:155], v[184:187]\n\t"
      "v_mul_f32_e32 v216, v216, v220\n\t"
      "s_waitcnt vmcnt(0)\n\t"
      "v_mfma_f32_16x16x32_bf16 v[184:187], v[112:115], v[156:159], v[184:187]\n\t"
      "v_mul_f32_e32 v217, v217, v221\n\t"
      "v_mul_f32_e32 v218, v218, v222\n\t"
      "v_mfma_f32_16x16x32_bf16 v[188:191], v[100:103], v[152:155], v[188:191]\n\t"
      "v_mul_f32_e32 v219, v219, v223\n\t"
      "v_cvt_pk_bf16_f32 v220, v216, v217\n\t"
      "v_mfma_f32_16x16x32_bf16 v[188:191], v[116:119], v[156:159], v[188:191]\n\t"
      "s_nop 1\n\t"
      "v_cmp_u_f32_e32 vcc, v184, v184\n\t"
      "v_cvt_pk_bf16_f32 v221, v218, v219\n\t"
      "v_min_u32_e32 v220, -2, v220\n\t"
      "v_mfma_f32_16x16x32_bf16 v[192:195], v[104:107], v[152:155], v[192:195]\n\t"
      "v_min_u32_e32 v221, -2, v221\n\t"
      "global_store_dwordx2 v245, v[220:221], s[8:9] sc1\n\t"
      "s_add_u32 s8, s8, 131072\n\t"
      "s_addc_u32 s9, s9, 0\n\t"
      "v_mfma_f32_16x16x32_bf16 v[192:195], v[120:123], v[156:159], v[192:195]\n\t"
      "s_cbranch_vccnz NP6R_%=\n\t"
      "NP6B_%=:\n\t"
      "ds_write_b128 v243, v[184:187]\n\t"
      "v_mfma_f32_16x16x32_bf16 v[196:199], v[108:111], v[152:155], v[196:199]\n\t"
      "v_mfma_f32_16x16x32_bf16 v[196:199], v[124:127], v[156:159], v[196:199]\n\t"
      "ds_write_b128 v164, v[188:191]\n\t"
      "s_nop 2\n\t"
      "ds_write_b128 v165, v[192:195]\n\t"
      "s_nop 2\n\t"
      "ds_write_b128 v166, v[196:199]\n\t"
      "s_waitcnt lgkmcnt(0)\n\t"
      "s_barrier\n\t"
      "ds_read_b128 v[216:219], v244\n\t"
      "ds_read_b128 v[220:223], v244 offset:4096\n\t"
      "ds_read_b128 v[224:227], v244 offset:8192\n\t"
      "ds_read_b128 v[228:231], v244 offset:12288\n\t"
      "ds_read_b128 v[184:187], v244 offset:16384\n\t"
      "ds_read_b128 v[188:191], v244 offset:20480\n\t"
      "ds_read_b128 v[192:195], v244 offset:24576\n\t"
      "ds_read_b128 v[196:199], v244 offset:28672\n\t"
      "s_waitcnt lgkmcnt(6)\n\t"
      "v_add_f32_e32 v216, v216, v220\n\t"
      "v_add_f32_e32 v217, v217, v221\n\t"
      "v_add_f32_e32 v218, v218, v222\n\t"
      "v_add_f32_e32 v219, v219, v223\n\t"
      "s_waitcnt lgkmcnt(4)\n\t"
      "v_add_f32_e32 v224, v224, v228\n\t"
      "v_add_f32_e32 v225, v225, v229\n\t"
      "v_add_f32_e32 v226, v226, v230\n\t"
      "v_add_f32_e32 v227, v227, v231\n\t"
      "s_waitcnt lgkmcnt(2)\n\t"
      "v_add_f32_e32 v184, v184, v188\n\t"
      "v_add_f32_e32 v185, v185, v189\n\t"
      "v_add_f32_e32 v186, v186, v190\n\t"
      "v_add_f32_e32 v187, v187, v191\n\t"
      "v_add_f32_e32 v216, v216, v224\n\t"
      "v_add_f32_e32 v217, v217, v225\n\t"
      "v_add_f32_e32 v218, v218, v226\n\t"
      "v_add_f32_e32 v219, v219, v227\n\t"
      "s_waitcnt lgkmcnt(0)\n\t"
      "v_add_f32_e32 v192, v192, v196\n\t"
      "v_add_f32_e32 v193, v193, v197\n\t"
      "v_add_f32_e32 v194, v194, v198\n\t"
      "v_add_f32_e32 v195, v195, v199\n\t"
      "v_add_f32_e32 v184, v184, v192\n\t"
      "v_add_f32_e32 v185, v185, v193\n\t"
      "v_add_f32_e32 v186, v186, v194\n\t"
      "v_add_f32_e32 v187, v187, v195\n\t"
      "v_add_f32_e32 v216, v216, v184\n\t"
      "v_add_f32_e32 v217, v217, v185\n\t"
      "v_add_f32_e32 v218, v218, v186\n\t"
      "v_add_f32_e32 v219, v219, v187\n\t"
      "v_mul_f32_e32 v220, v216, v216\n\t"
      "v_mul_f32_e32 v221, v217, v217\n\t"
      "v_mul_f32_e32 v222, v218, v218\n\t"
      "v_mul_f32_e64 v223, v219, v219\n\t"
      "v_fmaak_f32 v220, v235, v220, 0xc0135761\n\t"
      "v_fmaak_f32 v221, v235, v221, 0xc0135761\n\t"
      "v_fmaak_f32 v222, v235, v222, 0xc0135761\n\t"
      "v_fmaak_f32 v223, v235, v223, 0xc0135761\n\t"
      "v_mul_f32_e32 v220, v216, v220\n\t"
      "v_mul_f32_e32 v221, v217, v221\n\t"
      "v_mul_f32_e32 v222, v218, v222\n\t"
      "v_mul_f32_e32 v223, v219, v223\n\t"
      "v_exp_f32_e32 v220, v220\n\t"
      "v_exp_f32_e32 v221, v221\n\t"
      "v_exp_f32_e32 v222, v222\n\t"
      "v_exp_f32_e32 v223, v223\n\t"
      "v_add_f32_e32 v220, 1.0, v220\n\t"
      "v_add_f32_e32 v221, 1.0, v221\n\t"
      "v_add_f32_e32 v222, 1.0, v222\n\t"
      "v_add_f32_e32 v223, 1.0, v223\n\t"
      "v_rcp_f32_e32 v220, v220\n\t"
      "v_rcp_f32_e32 v221, v221\n\t"
      "v_rcp_f32_e32 v222, v222\n\t"
      "v_rcp_f32_e32 v223, v223\n\t"
      "v_mul_f32_e32 v216, v216, v220\n\t"
      "v_mul_f32_e32 v217, v217, v221\n\t"
      "v_mul_f32_e32 v218, v218, v222\n\t"
      "v_mul_f32_e32 v219, v219, v223\n\t"
      "v_cvt_pk_bf16_f32 v220, v216, v217\n\t"
      "v_cvt_pk_bf16_f32 v221, v218, v219\n\t"
      "v_min_u32_e32 v220, -2, v220\n\t"
      "v_min_u32_e32 v221, -2, v221\n\t"
      "s_cmp_eq_u32 s17, 0\n\t"
      "s_cbranch_scc0 HI6_%=\n\t"
      "s_mov_b64 exec, 0xf\n\t"
      "global_store_dwordx2 v247, v[232:233], s[12:13] sc1\n\t"
      "s_mov_b64 exec, -1\n\t"
      "HI6_%=:\n\t"
      "global_store_dwordx2 v245, v[220:221], s[8:9] sc1\n\t"
      "GX_%=:\n\t"
      "AEND_%=:\n\t"
      "s_endpgm\n\t"
      ".p2align 6\n\t"
      "GQ_%=:\n\t"
      "s_waitcnt vmcnt(7)\n\t"
      "v_max3_u32 v234, v128, v129, v130\n\t"
      "v_max_u32_e32 v234, v234, v131\n\t"
      "v_cmp_eq_u32_e32 vcc, -1, v234\n\t"
      "s_cbranch_vccnz FQ0R_%=\n\t"
      "FQ0B_%=:\n\t"
      "v_mfma_f32_16x16x32_bf16 v[184:187], v[0:3], v[128:131], v[236:239]\n\t"
      "v_mfma_f32_16x16x32_bf16 v[188:191], v[4:7], v[128:131], 0\n\t"
      "v_mfma_f32_16x16x32_bf16 v[192:195], v[8:11], v[128:131], 0\n\t"
      "v_mfma_f32_16x16x32_bf16 v[196:199], v[12:15], v[128:131], 0\n\t"
      "global_load_dwordx4 v[128:131], v176, s[54:55] sc1\n\t"
      "s_waitcnt vmcnt(7)\n\t"
      "v_max3_u32 v234, v132, v133, v134\n\t"
      "v_max_u32_e32 v234, v234, v135\n\t"
      "v_cmp_eq_u32_e32 vcc, -1, v234\n\t"
      "s_cbranch_vccnz FQ1R_%=\n\t"
      "FQ1B_%=:\n\t"
      "v_mfma_f32_16x16x32_bf16 v[184:187], v[16:19], v[132:135], v[184:187]\n\t"
      "v_mfma_f32_16x16x32_bf16 v[188:191], v[20:23], v[132:135], v[188:191]\n\t"
      "v_mfma_f32_16x16x32_bf16 v[192:195], v[24:27], v[132:135], v[192:195]\n\t"
      "v_mfma_f32_16x16x32_bf16 v[196:199], v[28:31], v[132:135], v[196:199]\n\t"
      "global_load_dwordx4 v[132:135], v177, s[54:55] sc1\n\t"
      "s_waitcnt vmcnt(7)\n\t"
      "v_max3_u32 v234, v136, v137, v138\n\t"
      "v_max_u32_e32 v234, v234, v139\n\t"
      "v_cmp_eq_u32_e32 vcc, -1, v234\n\t"
      "s_cbranch_vccnz FQ2R_%=\n\t"
      "FQ2B_%=:\n\t"
      "v_mfma_f32_16x16x32_bf16 v[184:187], v[32:35], v[136:139], v[184:187]\n\t"
      "v_mfma_f32_16x16x32_bf16 v[188:191], v[36:39], v[136:139], v[188:191]\n\t"
      "v_mfma_f32_16x16x32_bf16 v[192:195], v[40:43], v[136:139], v[192:195]\n\t"
      "v_mfma_f32_16x16x32_bf16 v[196:199], v[44:47], v[136:139], v[196:199]\n\t"
      "global_load_dwordx4 v[136:139], v178, s[54:55] sc1\n\t"
      "s_waitcnt vmcnt(7)\n\t"
      "v_max3_u32 v234, v140, v141, v142\n\t"
      "v_max_u32_e32 v234, v234, v143\n\t"
      "v_cmp_eq_u32_e32 vcc, -1, v234\n\t"
      "s_cbranch_vccnz FQ3R_%=\n\t"
      "FQ3B_%=:\n\t"
      "v_mfma_f32_16x16x32_bf16 v[184:187], v[48:51], v[140:143], v[184:187]\n\t"
      "v_mfma_f32_16x16x32_bf16 v[188:191], v[52:55], v[140:143], v[188:191]\n\t"
      "v_mfma_f32_16x16x32_bf16 v[192:195], v[56:59], v[140:143], v[192:195]\n\t"
      "v_mfma_f32_16x16x32_bf16 v[196:199], v[60:63], v[140:143], v[196:199]\n\t"
      "global_load_dwordx4 v[140:143], v179, s[54:55] sc1\n\t"
      "s_waitcnt vmcnt(7)\n\t"
      "v_max3_u32 v234, v144, v145, v146\n\t"
      "v_max_u32_e32 v234, v234, v147\n\t"
      "v_cmp_eq_u32_e32 vcc, -1, v234\n\t"
      "s_cbranch_vccnz FQ4R_%=\n\t"
      "FQ4B_%=:\n\t"
      "v_mfma_f32_16x16x32_bf16 v[184:187], v[64:67], v[144:147], v[184:187]\n\t"
      "v_mfma_f32_16x16x32_bf16 v[188:191], v[68:71], v[144:147], v[188:191]\n\t"
      "v_mfma_f32_16x16x32_bf16 v[192:195], v[72:75], v[144:147], v[192:195]\n\t"
      "v_mfma_f32_16x16x32_bf16 v[196:199], v[76:79], v[144:147], v[196:199]\n\t"
      "global_load_dwordx4 v[144:147], v180, s[54:55] sc1\n\t"
      "s_waitcnt vmcnt(7)\n\t"
      "v_max3_u32 v234, v148, v149, v150\n\t"
      "v_max_u32_e32 v234, v234, v151\n\t"
      "v_cmp_eq_u32_e32 vcc, -1, v234\n\t"
      "s_cbranch_vccnz FQ5R_%=\n\t"
      "FQ5B_%=:\n\t"
      "v_mfma_f32_16x16x32_bf16 v[184:187], v[80:83], v[148:151], v[184:187]\n\t"
      "v_mfma_f32_16x16x32_bf16 v[188:191], v[84:87], v[148:151], v[188:191]\n\t"
      "v_mfma_f32_16x16x32_bf16 v[192:195], v[88:91], v[148:151], v[192:195]\n\t"
      "v_mfma_f32_16x16x32_bf16 v[196:199], v[92:95], v[148:151], v[196:199]\n\t"
      "global_load_dwordx4 v[148:151], v181, s[54:55] sc1\n\t"
      "s_waitcnt vmcnt(7)\n\t"
      "v_max3_u32 v234, v152, v153, v154\n\t"
      "v_max_u32_e32 v234, v234, v155\n\t"
      "v_cmp_eq_u32_e32 vcc, -1, v234\n\t"
      "s_cbranch_vccnz FQ6R_%=\n\t"
      "FQ6B_%=:\n\t"
      "v_mfma_f32_16x16x32_bf16 v[184:187], v[96:99], v[152:155], v[184:187]\n\t"
      "v_mfma_f32_16x16x32_bf16 v[188:191], v[100:103], v[152:155], v[188:191]\n\t"
      "v_mfma_f32_16x16x32_bf16 v[192:195], v[104:107], v[152:155], v[192:195]\n\t"
      "v_mfma_f32_16x16x32_bf16 v[196:199], v[108:111], v[152:155], v[196:199]\n\t"
      "global_load_dwordx4 v[152:155], v182, s[54:55] sc1\n\t"
      "s_waitcnt vmcnt(7)\n\t"
      "v_max3_u32 v234, v156, v157, v158\n\t"
      "v_max_u32_e32 v234, v234, v159\n\t"
      "v_cmp_eq_u32_e32 vcc, -1, v234\n\t"
      "s_cbranch_vccnz FQ7R_%=\n\t"
      "FQ7B_%=:\n\t"
      "v_mfma_f32_16x16x32_bf16 v[184:187], v[112:115], v[156:159], v[184:187]\n\t"
      "v_mfma_f32_16x16x32_bf16 v[188:191], v[116:119], v[156:159], v[188:191]\n\t"
      "v_mfma_f32_16x16x32_bf16 v[192:195], v[120:123], v[156:159], v[192:195]\n\t"
      "v_mfma_f32_16x16x32_bf16 v[196:199], v[124:127], v[156:159], v[196:199]\n\t"
      "global_load_dwordx4 v[156:159], v183, s[54:55] sc1\n\t"
      "s_nop 3\n\t"
      "ds_write_b128 v243, v[184:187]\n\t"
      "ds_write_b128 v164, v[188:191]\n\t"
      "ds_write_b128 v165, v[192:195]\n\t"
      "ds_write_b128 v166, v[196:199]\n\t"
      "s_waitcnt vmcnt(7)\n\t"
      "v_mfma_f32_16x16x32_bf16 v[200:203], v[0:3], v[128:131], v[236:239]\n\t"
      "v_mfma_f32_16x16x32_bf16 v[204:207], v[4:7], v[128:131], 0\n\t"
      "v_mfma_f32_16x16x32_bf16 v[208:211], v[8:11], v[128:131], 0\n\t"
      "v_mfma_f32_16x16x32_bf16 v[212:215], v[12:15], v[128:131], 0\n\t"
      "global_load_dwordx4 v[128:131], v176, s[56:57] sc1\n\t"
      "s_waitcnt vmcnt(7)\n\t"
      "v_mfma_f32_16x16x32_bf16 v[200:203], v[16:19], v[132:135], v[200:203]\n\t"
      "v_mfma_f32_16x16x32_bf16 v[204:207], v[20:23], v[132:135], v[204:207]\n\t"
      "v_mfma_f32_16x16x32_bf16 v[208:211], v[24:27], v[132:135], v[208:211]\n\t"
      "v_mfma_f32_16x16x32_bf16 v[212:215], v[28:31], v[132:135], v[212:215]\n\t"
      "global_load_dwordx4 v[132:135], v177, s[56:57] sc1\n\t"
      "s_waitcnt vmcnt(7)\n\t"
      "v_mfma_f32_16x16x32_bf16 v[200:203], v[32:35], v[136:139], v[200:203]\n\t"
      "v_mfma_f32_16x16x32_bf16 v[204:207], v[36:39], v[136:139], v[204:207]\n\t"
      "v_mfma_f32_16x16x32_bf16 v[208:211], v[40:43], v[136:139], v[208:211]\n\t"
      "v_mfma_f32_16x16x32_bf16 v[212:215], v[44:47], v[136:139], v[212:215]\n\t"
      "global_load_dwordx4 v[136:139], v178, s[56:57] sc1\n\t"
      "s_waitcnt vmcnt(7)\n\t"
      "v_mfma_f32_16x16x32_bf16 v[200:203], v[48:51], v[140:143], v[200:203]\n\t"
      "v_mfma_f32_16x16x32_bf16 v[204:207], v[52:55], v[140:143], v[204:207]\n\t"
      "v_mfma_f32_16x16x32_bf16 v[208:211], v[56:59], v[140:143], v[208:211]\n\t"
      "v_mfma_f32_16x16x32_bf16 v[212:215], v[60:63], v[140:143], v[212:215]\n\t"
      "global_load_dwordx4 v[140:143], v179, s[56:57] sc1\n\t"
      "s_waitcnt vmcnt(7)\n\t"
      "v_mfma_f32_16x16x32_bf16 v[200:203], v[64:67], v[144:147], v[200:203]\n\t"
      "v_mfma_f32_16x16x32_bf16 v[204:207], v[68:71], v[144:147], v[204:207]\n\t"
      "v_mfma_f32_16x16x32_bf16 v[208:211], v[72:75], v[144:147], v[208:211]\n\t"
      "v_mfma_f32_16x16x32_bf16 v[212:215], v[76:79], v[144:147], v[212:215]\n\t"
      "global_load_dwordx4 v[144:147], v180, s[56:57] sc1\n\t"
      "s_waitcnt vmcnt(7)\n\t"
      "v_mfma_f32_16x16x32_bf16 v[200:203], v[80:83], v[148:151], v[200:203]\n\t"
      "v_mfma_f32_16x16x32_bf16 v[204:207], v[84:87], v[148:151], v[204:207]\n\t"
      "s_waitcnt lgkmcnt(0)\n\t"
      "s_barrier\n\t"
      "s_setprio 2\n\t"
      "v_mfma_f32_16x16x32_bf16 v[208:211], v[88:91], v[148:151], v[208:211]\n\t"
      "v_mfma_f32_16x16x32_bf16 v[212:215], v[92:95], v[148:151], v[212:215]\n\t"
      "global_load_dwordx4 v[148:151], v181, s[56:57] sc1\n\t"
      "s_waitcnt vmcnt(7)\n\t"
      "v_mfma_f32_16x16x32_bf16 v[200:203], v[96:99], v[152:155], v[200:203]\n\t"
      "v_mfma_f32_16x16x32_bf16 v[204:207], v[100:103], v[152:155], v[204:207]\n\t"
      "v_mfma_f32_16x16x32_bf16 v[208:211], v[104:107], v[152:155], v[208:211]\n\t"
      "v_mfma_f32_16x16x32_bf16 v[212:215], v[108:111], v[152:155], v[212:215]\n\t"
      "global_load_dwordx4 v[152:155], v182, s[56:57] sc1\n\t"
      "s_waitcnt vmcnt(7)\n\t"
      "v_mfma_f32_16x16x32_bf16 v[200:203], v[112:115], v[156:159], v[200:203]\n\t"
      "v_mfma_f32_16x16x32_bf16 v[204:207], v[116:119], v[156:159], v[204:207]\n\t"
      "v_mfma_f32_16x16x32_bf16 v[208:211], v[120:123], v[156:159], v[208:211]\n\t"
      "v_mfma_f32_16x16x32_bf16 v[212:215], v[124:127], v[156:159], v[212:215]\n\t"
      "global_load_dwordx4 v[156:159], v183, s[56:57] sc1\n\t"
      "s_setprio 0\n\t"
      "s_nop 2\n\t"
      "v_cmp_u_f32_e32 vcc, v200, v200\n\t"
      "s_cbranch_vccnz NQ1R_%=\n\t"
      "NQ1B_%=:\n\t"
      "ds_write_b128 v243, v[200:203] offset:32768\n\t"
      "ds_write_b128 v164, v[204:207] offset:32768\n\t"
      "ds_write_b128 v165, v[208:211] offset:32768\n\t"
      "ds_write_b128 v166, v[212:215] offset:32768\n\t"
      "s_waitcnt vmcnt(7)\n\t"
      "v_mfma_f32_16x16x32_bf16 v[184:187], v[0:3], v[128:131], v[236:239]\n\t"
      "v_mfma_f32_16x16x32_bf16 v[188:191], v[4:7], v[128:131], 0\n\t"
      "v_mfma_f32_16x16x32_bf16 v[192:195], v[8:11], v[128:131], 0\n\t"
      "v_mfma_f32_16x16x32_bf16 v[196:199], v[12:15], v[128:131], 0\n\t"
      "global_load_dwordx4 v[128:131], v176, s[58:59] sc1\n\t"
      "s_waitcnt vmcnt(7)\n\t"
      "v_mfma_f32_16x16x32_bf16 v[184:187], v[16:19], v[132:135], v[184:187]\n\t"
      "v_mfma_f32_16x16x32_bf16 v[188:191], v[20:23], v[132:135], v[188:191]\n\t"
      "v_mfma_f32_16x16x32_bf16 v[192:195], v[24:27], v[132:135], v[192:195]\n\t"
      "v_mfma_f32_16x16x32_bf16 v[196:199], v[28:31], v[132:135], v[196:199]\n\t"
      "global_load_dwordx4 v[132:135], v177, s[58:59] sc1\n\t"
      "s_waitcnt vmcnt(7)\n\t"
      "v_mfma_f32_16x16x32_bf16 v[184:187], v[32:35], v[136:139], v[184:187]\n\t"
      "v_mfma_f32_16x16x32_bf16 v[188:191], v[36:39], v[136:139], v[188:191]\n\t"
      "v_mfma_f32_16x16x32_bf16 v[192:195], v[40:43], v[136:139], v[192:195]\n\t"
      "v_mfma_f32_16x16x32_bf16 v[196:199], v[44:47], v[136:139], v[196:199]\n\t"
      "global_load_dwordx4 v[136:139], v178, s[58:59] sc1\n\t"
      "s_waitcnt vmcnt(7)\n\t"
      "v_mfma_f32_16x16x32_bf16 v[184:187], v[48:51], v[140:143], v[184:187]\n\t"
      "v_mfma_f32_16x16x32_bf16 v[188:191], v[52:55], v[140:143], v[188:191]\n\t"
      "v_mfma_f32_16x16x32_bf16 v[192:195], v[56:59], v[140:143], v[192:195]\n\t"
      "v_mfma_f32_16x16x32_bf16 v[196:199], v[60:63], v[140:143], v[196:199]\n\t"
      "global_load_dwordx4 v[140:143], v179, s[58:59] sc1\n\t"
      "s_waitcnt vmcnt(7)\n\t"
      "v_mfma_f32_16x16x32_bf16 v[184:187], v[64:67], v[144:147], v[184:187]\n\t"
      "v_mfma_f32_16x16x32_bf16 v[188:191], v[68:71], v[144:147], v[188:191]\n\t"
      "v_mfma_f32_16x16x32_bf16 v[192:195], v[72:75], v[144:147], v[192:195]\n\t"
      "v_mfma_f32_16x16x32_bf16 v[196:199], v[76:79], v[144:147], v[196:199]\n\t"
      "global_load_dwordx4 v[144:147], v180, s[58:59] sc1\n\t"
      "s_waitcnt vmcnt(7)\n\t"
      "v_mfma_f32_16x16x32_bf16 v[184:187], v[80:83], v[148:151], v[184:187]\n\t"
      "v_mfma_f32_16x16x32_bf16 v[188:191], v[84:87], v[148:151], v[188:191]\n\t"
      "s_waitcnt lgkmcnt(0)\n\t"
      "s_barrier\n\t"
      "s_setprio 2\n\t"
      "v_mfma_f32_16x16x32_bf16 v[192:195], v[88:91], v[148:151], v[192:195]\n\t"
      "v_mfma_f32_16x16x32_bf16 v[196:199], v[92:95], v[148:151], v[196:199]\n\t"
      "global_load_dwordx4 v[148:151], v181, s[58:59] sc1\n\t"
      "s_waitcnt vmcnt(7)\n\t"
      "v_mfma_f32_16x16x32_bf16 v[184:187], v[96:99], v[152:155], v[184:187]\n\t"
      "v_mfma_f32_16x16x32_bf16 v[188:191], v[100:103], v[152:155], v[188:191]\n\t"
      "v_mfma_f32_16x16x32_bf16 v[192:195], v[104:107], v[152:155], v[192:195]\n\t"
      "v_mfma_f32_16x16x32_bf16 v[196:199], v[108:111], v[152:155], v[196:199]\n\t"
      "global_load_dwordx4 v[152:155], v182, s[58:59] sc1\n\t"
      "s_waitcnt vmcnt(7)\n\t"
      "v_mfma_f32_16x16x32_bf16 v[184:187], v[112:115], v[156:159], v[184:187]\n\t"
      "v_mfma_f32_16x16x32_bf16 v[188:191], v[116:119], v[156:159], v[188:191]\n\t"
      "v_mfma_f32_16x16x32_bf16 v[192:195], v[120:123], v[156:159], v[192:195]\n\t"
      "v_mfma_f32_16x16x32_bf16 v[196:199], v[124:127], v[156:159], v[196:199]\n\t"
      "global_load_dwordx4 v[156:159], v183, s[58:59] sc1\n\t"
      "s_setprio 0\n\t"
      "s_nop 2\n\t"
      "v_cmp_u_f32_e32 vcc, v184, v184\n\t"
      "s_cbranch_vccnz NQ2R_%=\n\t"
      "NQ2B_%=:\n\t"
      "ds_write_b128 v243, v[184:187]\n\t"
      "ds_write_b128 v164, v[188:191]\n\t"
      "ds_write_b128 v165, v[192:195]\n\t"
      "ds_write_b128 v166, v[196:199]\n\t"
      "s_waitcnt vmcnt(7)\n\t"
      "v_mfma_f32_16x16x32_bf16 v[200:203], v[0:3], v[128:131], v[236:239]\n\t"
      "v_mfma_f32_16x16x32_bf16 v[204:207], v[4:7], v[128:131], 0\n\t"
      "v_mfma_f32_16x16x32_bf16 v[208:211], v[8:11], v[128:131], 0\n\t"
      "v_mfma_f32_16x16x32_bf16 v[212:215], v[12:15], v[128:131], 0\n\t"
      "global_load_dwordx4 v[128:131], v176, s[60:61] sc1\n\t"
      "s_waitcnt vmcnt(7)\n\t"
      "v_mfma_f32_16x16x32_bf16 v[200:203], v[16:19], v[132:135], v[200:203]\n\t"
      "v_mfma_f32_16x16x32_bf16 v[204:207], v[20:23], v[132:135], v[204:207]\n\t"
      "v_mfma_f32_16x16x32_bf16 v[208:211], v[24:27], v[132:135], v[208:211]\n\t"
      "v_mfma_f32_16x16x32_bf16 v[212:215], v[28:31], v[132:135], v[212:215]\n\t"
      "global_load_dwordx4 v[132:135], v177, s[60:61] sc1\n\t"
      "s_waitcnt vmcnt(7)\n\t"
      "v_mfma_f32_16x16x32_bf16 v[200:203], v[32:35], v[136:139], v[200:203]\n\t"
      "v_mfma_f32_16x16x32_bf16 v[204:207], v[36:39], v[136:139], v[204:207]\n\t"
      "v_mfma_f32_16x16x32_bf16 v[208:211], v[40:43], v[136:139], v[208:211]\n\t"
      "v_mfma_f32_16x16x32_bf16 v[212:215], v[44:47], v[136:139], v[212:215]\n\t"
      "global_load_dwordx4 v[136:139], v178, s[60:61] sc1\n\t"
      "s_waitcnt vmcnt(7)\n\t"
      "v_mfma_f32_16x16x32_bf16 v[200:203], v[48:51], v[140:143], v[200:203]\n\t"
      "v_mfma_f32_16x16x32_bf16 v[204:207], v[52:55], v[140:143], v[204:207]\n\t"
      "v_mfma_f32_16x16x32_bf16 v[208:211], v[56:59], v[140:143], v[208:211]\n\t"
      "v_mfma_f32_16x16x32_bf16 v[212:215], v[60:63], v[140:143], v[212:215]\n\t"
      "global_load_dwordx4 v[140:143], v179, s[60:61] sc1\n\t"
      "s_waitcnt vmcnt(7)\n\t"
      "v_mfma_f32_16x16x32_bf16 v[200:203], v[64:67], v[144:147], v[200:203]\n\t"
      "v_mfma_f32_16x16x32_bf16 v[204:207], v[68:71], v[144:147], v[204:207]\n\t"
      "v_mfma_f32_16x16x32_bf16 v[208:211], v[72:75], v[144:147], v[208:211]\n\t"
      "v_mfma_f32_16x16x32_bf16 v[212:215], v[76:79], v[144:147], v[212:215]\n\t"
      "global_load_dwordx4 v[144:147], v180, s[60:61] sc1\n\t"
      "s_waitcnt vmcnt(7)\n\t"
      "v_mfma_f32_16x16x32_bf16 v[200:203], v[80:83], v[148:151], v[200:203]\n\t"
      "v_mfma_f32_16x16x32_bf16 v[204:207], v[84:87], v[148:151], v[204:207]\n\t"
      "s_waitcnt lgkmcnt(0)\n\t"
      "s_barrier\n\t"
      "s_setprio 2\n\t"
      "v_mfma_f32_16x16x32_bf16 v[208:211], v[88:91], v[148:151], v[208:211]\n\t"
      "v_mfma_f32_16x16x32_bf16 v[212:215], v[92:95], v[148:151], v[212:215]\n\t"
      "global_load_dwordx4 v[148:151], v181, s[60:61] sc1\n\t"
      "s_waitcnt vmcnt(7)\n\t"
      "v_mfma_f32_16x16x32_bf16 v[200:203], v[96:99], v[152:155], v[200:203]\n\t"
      "v_mfma_f32_16x16x32_bf16 v[204:207], v[100:103], v[152:155], v[204:207]\n\t"
      "v_mfma_f32_16x16x32_bf16 v[208:211], v[104:107], v[152:155], v[208:211]\n\t"
      "v_mfma_f32_16x16x32_bf16 v[212:215], v[108:111], v[152:155], v[212:215]\n\t"
      "global_load_dwordx4 v[152:155], v182, s[60:61] sc1\n\t"
      "s_waitcnt vmcnt(7)\n\t"
      "v_mfma_f32_16x16x32_bf16 v[200:203], v[112:115], v[156:159], v[200:203]\n\t"
      "v_mfma_f32_16x16x32_bf16 v[204:207], v[116:119], v[156:159], v[204:207]\n\t"
      "v_mfma_f32_16x16x32_bf16 v[208:211], v[120:123], v[156:159], v[208:211]\n\t"
      "v_mfma_f32_16x16x32_bf16 v[212:215], v[124:127], v[156:159], v[212:215]\n\t"
      "global_load_dwordx4 v[156:159], v183, s[60:61] sc1\n\t"
      "s_setprio 0\n\t"
      "s_nop 2\n\t"
      "v_cmp_u_f32_e32 vcc, v200, v200\n\t"
      "s_cbranch_vccnz NQ3R_%=\n\t"
      "NQ3B_%=:\n\t"
      "ds_write_b128 v243, v[200:203] offset:32768\n\t"
      "ds_write_b128 v164, v[204:207] offset:32768\n\t"
      "ds_write_b128 v165, v[208:211] offset:32768\n\t"
      "ds_write_b128 v166, v[212:215] offset:32768\n\t"
      "s_waitcnt vmcnt(7)\n\t"
      "v_mfma_f32_16x16x32_bf16 v[184:187], v[0:3], v[128:131], v[236:239]\n\t"
      "v_mfma_f32_16x16x32_bf16 v[188:191], v[4:7], v[128:131], 0\n\t"
      "v_mfma_f32_16x16x32_bf16 v[192:195], v[8:11], v[128:131], 0\n\t"
      "v_mfma_f32_16x16x32_bf16 v[196:199], v[12:15], v[128:131], 0\n\t"
      "global_load_dwordx4 v[128:131], v176, s[62:63] sc1\n\t"
      "s_waitcnt vmcnt(7)\n\t"
      "v_mfma_f32_16x16x32_bf16 v[184:187], v[16:19], v[132:135], v[184:187]\n\t"
      "v_mfma_f32_16x16x32_bf16 v[188:191], v[20:23], v[132:135], v[188:191]\n\t"
      "v_mfma_f32_16x16x32_bf16 v[192:195], v[24:27], v[132:135], v[192:195]\n\t"
      "v_mfma_f32_16x16x32_bf16 v[196:199], v[28:31], v[132:135], v[196:199]\n\t"
      "global_load_dwordx4 v[132:135], v177, s[62:63] sc1\n\t"
      "s_waitcnt vmcnt(7)\n\t"
      "v_mfma_f32_16x16x32_bf16 v[184:187], v[32:35], v[136:139], v[184:187]\n\t"
      "v_mfma_f32_16x16x32_bf16 v[188:191], v[36:39], v[136:139], v[188:191]\n\t"
      "v_mfma_f32_16x16x32_bf16 v[192:195], v[40:43], v[136:139], v[192:195]\n\t"
      "v_mfma_f32_16x16x32_bf16 v[196:199], v[44:47], v[136:139], v[196:199]\n\t"
      "global_load_dwordx4 v[136:139], v178, s[62:63] sc1\n\t"
      "s_waitcnt vmcnt(7)\n\t"
      "v_mfma_f32_16x16x32_bf16 v[184:187], v[48:51], v[140:143], v[184:187]\n\t"
      "v_mfma_f32_16x16x32_bf16 v[188:191], v[52:55], v[140:143], v[188:191]\n\t"
      "v_mfma_f32_16x16x32_bf16 v[192:195], v[56:59], v[140:143], v[192:195]\n\t"
      "v_mfma_f32_16x16x32_bf16 v[196:199], v[60:63], v[140:143], v[196:199]\n\t"
      "global_load_dwordx4 v[140:143], v179, s[62:63] sc1\n\t"
      "s_waitcnt vmcnt(7)\n\t"
      "v_mfma_f32_16x16x32_bf16 v[184:187], v[64:67], v[144:147], v[184:187]\n\t"
      "v_mfma_f32_16x16x32_bf16 v[188:191], v[68:71], v[144:147], v[188:191]\n\t"
      "v_mfma_f32_16x16x32_bf16 v[192:195], v[72:75], v[144:147], v[192:195]\n\t"
      "v_mfma_f32_16x16x32_bf16 v[196:199], v[76:79], v[144:147], v[196:199]\n\t"
      "global_load_dwordx4 v[144:147], v180, s[62:63] sc1\n\t"
      "s_waitcnt vmcnt(7)\n\t"
      "v_mfma_f32_16x16x32_bf16 v[184:187], v[80:83], v[148:151], v[184:187]\n\t"
      "v_mfma_f32_16x16x32_bf16 v[188:191], v[84:87], v[148:151], v[188:191]\n\t"
      "s_waitcnt lgkmcnt(0)\n\t"
      "s_barrier\n\t"
      "s_setprio 2\n\t"
      "v_mfma_f32_16x16x32_bf16 v[192:195], v[88:91], v[148:151], v[192:195]\n\t"
      "v_mfma_f32_16x16x32_bf16 v[196:199], v[92:95], v[148:151], v[196:199]\n\t"
      "global_load_dwordx4 v[148:151], v181, s[62:63] sc1\n\t"
      "s_waitcnt vmcnt(7)\n\t"
      "v_mfma_f32_16x16x32_bf16 v[184:187], v[96:99], v[152:155], v[184:187]\n\t"
      "v_mfma_f32_16x16x32_bf16 v[188:191], v[100:103], v[152:155], v[188:191]\n\t"
      "v_mfma_f32_16x16x32_bf16 v[192:195], v[104:107], v[152:155], v[192:195]\n\t"
      "v_mfma_f32_16x16x32_bf16 v[196:199], v[108:111], v[152:155], v[196:199]\n\t"
      "global_load_dwordx4 v[152:155], v182, s[62:63] sc1\n\t"
      "s_waitcnt vmcnt(7)\n\t"
      "v_mfma_f32_16x16x32_bf16 v[184:187], v[112:115], v[156:159], v[184:187]\n\t"
      "v_mfma_f32_16x16x32_bf16 v[188:191], v[116:119], v[156:159], v[188:191]\n\t"
      "v_mfma_f32_16x16x32_bf16 v[192:195], v[120:123], v[156:159], v[192:195]\n\t"
      "v_mfma_f32_16x16x32_bf16 v[196:199], v[124:127], v[156:159], v[196:199]\n\t"
      "global_load_dwordx4 v[156:159], v183, s[62:63] sc1\n\t"
      "s_setprio 0\n\t"
      "s_nop 2\n\t"
      "v_cmp_u_f32_e32 vcc, v184, v184\n\t"
      "s_cbranch_vccnz NQ4R_%=\n\t"
      "NQ4B_%=:\n\t"
      "ds_write_b128 v243, v[184:187]\n\t"
      "ds_write_b128 v164, v[188:191]\n\t"
      "ds_write_b128 v165, v[192:195]\n\t"
      "ds_write_b128 v166, v[196:199]\n\t"
      "s_waitcnt vmcnt(7)\n\t"
      "v_mfma_f32_16x16x32_bf16 v[200:203], v[0:3], v[128:131], v[236:239]\n\t"
      "v_mfma_f32_16x16x32_bf16 v[204:207], v[4:7], v[128:131], 0\n\t"
      "v_mfma_f32_16x16x32_bf16 v[208:211], v[8:11], v[128:131], 0\n\t"
      "v_mfma_f32_16x16x32_bf16 v[212:215], v[12:15], v[128:131], 0\n\t"
      "global_load_dwordx4 v[128:131], v176, s[64:65] sc1\n\t"
      "s_waitcnt vmcnt(7)\n\t"
      "v_mfma_f32_16x16x32_bf16 v[200:203], v[16:19], v[132:135], v[200:203]\n\t"
      "v_mfma_f32_16x16x32_bf16 v[204:207], v[20:23], v[132:135], v[204:207]\n\t"
      "v_mfma_f32_16x16x32_bf16 v[208:211], v[24:27], v[132:135], v[208:211]\n\t"
      "v_mfma_f32_16x16x32_bf16 v[212:215], v[28:31], v[132:135], v[212:215]\n\t"
      "global_load_dwordx4 v[132:135], v177, s[64:65] sc1\n\t"
      "s_waitcnt vmcnt(7)\n\t"
      "v_mfma_f32_16x16x32_bf16 v[200:203], v[32:35], v[136:139], v[200:203]\n\t"
      "v_mfma_f32_16x16x32_bf16 v[204:207], v[36:39], v[136:139], v[204:207]\n\t"
      "v_mfma_f32_16x16x32_bf16 v[208:211], v[40:43], v[136:139], v[208:211]\n\t"
      "v_mfma_f32_16x16x32_bf16 v[212:215], v[44:47], v[136:139], v[212:215]\n\t"
      "global_load_dwordx4 v[136:139], v178, s[64:65] sc1\n\t"
      "s_waitcnt vmcnt(7)\n\t"
      "v_mfma_f32_16x16x32_bf16 v[200:203], v[48:51], v[140:143], v[200:203]\n\t"
      "v_mfma_f32_16x16x32_bf16 v[204:207], v[52:55], v[140:143], v[204:207]\n\t"
      "v_mfma_f32_16x16x32_bf16 v[208:211], v[56:59], v[140:143], v[208:211]\n\t"
      "v_mfma_f32_16x16x32_bf16 v[212:215], v[60:63], v[140:143], v[212:215]\n\t"
      "global_load_dwordx4 v[140:143], v179, s[64:65] sc1\n\t"
      "s_waitcnt vmcnt(7)\n\t"
      "v_mfma_f32_16x16x32_bf16 v[200:203], v[64:67], v[144:147], v[200:203]\n\t"
      "v_mfma_f32_16x16x32_bf16 v[204:207], v[68:71], v[144:147], v[204:207]\n\t"
      "v_mfma_f32_16x16x32_bf16 v[208:211], v[72:75], v[144:147], v[208:211]\n\t"
      "v_mfma_f32_16x16x32_bf16 v[212:215], v[76:79], v[144:147], v[212:215]\n\t"
      "global_load_dwordx4 v[144:147], v180, s[64:65] sc1\n\t"
      "s_waitcnt vmcnt(7)\n\t"
      "v_mfma_f32_16x16x32_bf16 v[200:203], v[80:83], v[148:151], v[200:203]\n\t"
      "v_mfma_f32_16x16x32_bf16 v[204:207], v[84:87], v[148:151], v[204:207]\n\t"
      "s_waitcnt lgkmcnt(0)\n\t"
      "s_barrier\n\t"
      "s_setprio 2\n\t"
      "v_mfma_f32_16x16x32_bf16 v[208:211], v[88:91], v[148:151], v[208:211]\n\t"
      "v_mfma_f32_16x16x32_bf16 v[212:215], v[92:95], v[148:151], v[212:215]\n\t"
      "global_load_dwordx4 v[148:151], v181, s[64:65] sc1\n\t"
      "s_waitcnt vmcnt(7)\n\t"
      "v_mfma_f32_16x16x32_bf16 v[200:203], v[96:99], v[152:155], v[200:203]\n\t"
      "v_mfma_f32_16x16x32_bf16 v[204:207], v[100:103], v[152:155], v[204:207]\n\t"
      "v_mfma_f32_16x16x32_bf16 v[208:211], v[104:107], v[152:155], v[208:211]\n\t"
      "v_mfma_f32_16x16x32_bf16 v[212:215], v[108:111], v[152:155], v[212:215]\n\t"
      "global_load_dwordx4 v[152:155], v182, s[64:65] sc1\n\t"
      "s_waitcnt vmcnt(7)\n\t"
      "v_mfma_f32_16x16x32_bf16 v[200:203], v[112:115], v[156:159], v[200:203]\n\t"
      "v_mfma_f32_16x16x32_bf16 v[204:207], v[116:119], v[156:159], v[204:207]\n\t"
      "v_mfma_f32_16x16x32_bf16 v[208:211], v[120:123], v[156:159], v[208:211]\n\t"
      "v_mfma_f32_16x16x32_bf16 v[212:215], v[124:127], v[156:159], v[212:215]\n\t"
      "global_load_dwordx4 v[156:159], v183, s[64:65] sc1\n\t"
      "s_setprio 0\n\t"
      "s_nop 2\n\t"
      "v_cmp_u_f32_e32 vcc, v200, v200\n\t"
      "s_cbranch_vccnz NQ5R_%=\n\t"
      "NQ5B_%=:\n\t"
      "ds_write_b128 v243, v[200:203] offset:32768\n\t"
      "ds_write_b128 v164, v[204:207] offset:32768\n\t"
      "ds_write_b128 v165, v[208:211] offset:32768\n\t"
      "ds_write_b128 v166, v[212:215] offset:32768\n\t"
      "s_waitcnt vmcnt(7)\n\t"
      "v_mfma_f32_16x16x32_bf16 v[184:187], v[0:3], v[128:131], v[236:239]\n\t"
      "v_mfma_f32_16x16x32_bf16 v[188:191], v[4:7], v[128:131], 0\n\t"
      "v_mfma_f32_16x16x32_bf16 v[192:195], v[8:11], v[128:131], 0\n\t"
      "v_mfma_f32_16x16x32_bf16 v[196:199], v[12:15], v[128:131], 0\n\t"
      "s_waitcnt vmcnt(6)\n\t"
      "v_mfma_f32_16x16x32_bf16 v[184:187], v[16:19], v[132:135], v[184:187]\n\t"
      "v_mfma_f32_16x16x32_bf16 v[188:191], v[20:23], v[132:135], v[188:191]\n\t"
      "v_mfma_f32_16x16x32_bf16 v[192:195], v[24:27], v[132:135], v[192:195]\n\t"
      "v_mfma_f32_16x16x32_bf16 v[196:199], v[28:31], v[132:135], v[196:199]\n\t"
      "s_waitcnt vmcnt(5)\n\t"
      "v_mfma_f32_16x16x32_bf16 v[184:187], v[32:35], v[136:139], v[184:187]\n\t"
      "v_mfma_f32_16x16x32_bf16 v[188:191], v[36:39], v[136:139], v[188:191]\n\t"
      "v_mfma_f32_16x16x32_bf16 v[192:195], v[40:43], v[136:139], v[192:195]\n\t"
      "v_mfma_f32_16x16x32_bf16 v[196:199], v[44:47], v[136:139], v[196:199]\n\t"
      "s_waitcnt vmcnt(4)\n\t"
      "v_mfma_f32_16x16x32_bf16 v[184:187], v[48:51], v[140:143], v[184:187]\n\t"
      "v_mfma_f32_16x16x32_bf16 v[188:191], v[52:55], v[140:143], v[188:191]\n\t"
      "v_mfma_f32_16x16x32_bf16 v[192:195], v[56:59], v[140:143], v[192:195]\n\t"
      "v_mfma_f32_16x16x32_bf16 v[196:199], v[60:63], v[140:143], v[196:199]\n\t"
      "s_waitcnt vmcnt(3)\n\t"
      "v_mfma_f32_16x16x32_bf16 v[184:187], v[64:67], v[144:147], v[184:187]\n\t"
      "v_mfma_f32_16x16x32_bf16 v[188:191], v[68:71], v[144:147], v[188:191]\n\t"
      "v_mfma_f32_16x16x32_bf16 v[192:195], v[72:75], v[144:147], v[192:195]\n\t"
      "v_mfma_f32_16x16x32_bf16 v[196:199], v[76:79], v[144:147], v[196:199]\n\t"
      "s_waitcnt vmcnt(2)\n\t"
      "v_mfma_f32_16x16x32_bf16 v[184:187], v[80:83], v[148:151], v[184:187]\n\t"
      "v_mfma_f32_16x16x32_bf16 v[188:191], v[84:87], v[148:151], v[188:191]\n\t"
      "s_waitcnt lgkmcnt(0)\n\t"
      "s_barrier\n\t"
      "s_setprio 2\n\t"
      "v_mfma_f32_16x16x32_bf16 v[192:195], v[88:91], v[148:151], v[192:195]\n\t"
      "v_mfma_f32_16x16x32_bf16 v[196:199], v[92:95], v[148:151], v[196:199]\n\t"
      "s_waitcnt vmcnt(1)\n\t"
      "v_mfma_f32_16x16x32_bf16 v[184:187], v[96:99], v[152:155], v[184:187]\n\t"
      "v_mfma_f32_16x16x32_bf16 v[188:191], v[100:103], v[152:155], v[188:191]\n\t"
      "v_mfma_f32_16x16x32_bf16 v[192:195], v[104:107], v[152:155], v[192:195]\n\t"
      "v_mfma_f32_16x16x32_bf16 v[196:199], v[108:111], v[152:155], v[196:199]\n\t"
      "s_waitcnt vmcnt(0)\n\t"
      "v_mfma_f32_16x16x32_bf16 v[184:187], v[112:115], v[156:159], v[184:187]\n\t"
      "v_mfma_f32_16x16x32_bf16 v[188:191], v[116:119], v[156:159], v[188:191]\n\t"
      "v_mfma_f32_16x16x32_bf16 v[192:195], v[120:123], v[156:159], v[192:195]\n\t"
      "v_mfma_f32_16x16x32_bf16 v[196:199], v[124:127], v[156:159], v[196:199]\n\t"
      "s_nop 4\n\t"
      "v_cmp_u_f32_e32 vcc, v184, v184\n\t"
      "s_cbranch_vccnz NQ6R_%=\n\t"
      "NQ6B_%=:\n\t"
      "ds_write_b128 v243, v[184:187]\n\t"
      "ds_write_b128 v164, v[188:191]\n\t"
      "ds_write_b128 v165, v[192:195]\n\t"
      "ds_write_b128 v166, v[196:199]\n\t"
      "s_waitcnt lgkmcnt(0)\n\t"
      "s_barrier\n\t"
      "s_branch GX_%=\n\t"
      "ASP_%=:\n\t"
      "s_mov_b32 m0, 163328\n\t"
      "s_cmp_ge_u32 s17, 65536\n\t"
      "s_cbranch_scc0 BOT_%=\n\t"
      "s_barrier\n\t"
      "BOT_%=:\n\t"
      "global_load_lds_dword v241, s[4:5]\n\t"
      "s_add_u32 s4, s4, 229376\n\t"
      "s_addc_u32 s5, s5, 0\n\t"
      "global_load_lds_dword v241, s[4:5]\n\t"
      "s_add_u32 s4, s4, 229376\n\t"
      "s_addc_u32 s5, s5, 0\n\t"
      "s_waitcnt vmcnt(0)\n\t"
      "s_sleep 1\n\t"
      "s_getpc_b64 s[44:45]\n\t"
      "s_add_u32 s44, s44, _Z11fused_blockPKcS0_S0_S0_S0_S0_S0_PcS1_@rel32@lo+4\n\t"
      "s_addc_u32 s45, s45, _Z11fused_blockPKcS0_S0_S0_S0_S0_S0_PcS1_@rel32@hi+12\n\t"
      "s_lshr_b32 s46, s17, 8\n\t"
      "v_lshrrev_b32_e32 v0, 4, v240\n\t"
      "v_add_u32_e64 v0, s46, v0\n\t"
      "v_min_u32_e32 v0, 505, v0\n\t"
      "v_lshlrev_b32_e32 v0, 7, v0\n\t"
      "global_load_lds_dword v0, s[44:45] sc1\n\t"
      "s_branch AEND_%=\n\t"
      "FQ0R_%=:\n\t"
      "s_mov_b32 s51, 0\n\t"
      "FQ0L_%=:\n\t"
      "s_add_u32 s51, s51, 1\n\t"
      "s_cmp_lt_u32 s51, 262144\n\t"
      "s_cbranch_scc0 FQ0X_%=\n\t"
      "s_waitcnt vmcnt(0)\n\t"
      "s_sleep 1\n\t"
      "global_load_dwordx4 v[128:131], v176, s[52:53] sc1\n\t"
      "global_load_dwordx4 v[132:135], v177, s[52:53] sc1\n\t"
      "global_load_dwordx4 v[136:139], v178, s[52:53] sc1\n\t"
      "global_load_dwordx4 v[140:143], v179, s[52:53] sc1\n\t"
      "global_load_dwordx4 v[144:147], v180, s[52:53] sc1\n\t"
      "global_load_dwordx4 v[148:151], v181, s[52:53] sc1\n\t"
      "global_load_dwordx4 v[152:155], v182, s[52:53] sc1\n\t"
      "global_load_dwordx4 v[156:159], v183, s[52:53] sc1\n\t"
      "s_waitcnt vmcnt(7)\n\t"
      "v_max3_u32 v234, v128, v129, v130\n\t"
      "v_max_u32_e32 v234, v234, v131\n\t"
      "v_cmp_eq_u32_e32 vcc, -1, v234\n\t"
      "s_cbranch_vccnz FQ0L_%=\n\t"
      "FQ0X_%=:\n\t"
      "s_branch FQ0B_%=\n\t"
      "FQ1R_%=:\n\t"
      "s_mov_b32 s51, 0\n\t"
      "FQ1L_%=:\n\t"
      "s_add_u32 s51, s51, 1\n\t"
      "s_cmp_lt_u32 s51, 262144\n\t"
      "s_cbranch_scc0 FQ1X_%=\n\t"
      "s_waitcnt vmcnt(0)\n\t"
      "s_sleep 1\n\t"
      "global_load_dwordx4 v[132:135], v177, s[52:53] sc1\n\t"
      "global_load_dwordx4 v[136:139], v178, s[52:53] sc1\n\t"
      "global_load_dwordx4 v[140:143], v179, s[52:53] sc1\n\t"
      "global_load_dwordx4 v[144:147], v180, s[52:53] sc1\n\t"
      "global_load_dwordx4 v[148:151], v181, s[52:53] sc1\n\t"
      "global_load_dwordx4 v[152:155], v182, s[52:53] sc1\n\t"
      "global_load_dwordx4 v[156:159], v183, s[52:53] sc1\n\t"
      "global_load_dwordx4 v[128:131], v176, s[54:55] sc1\n\t"
      "s_waitcnt vmcnt(7)\n\t"
      "v_max3_u32 v234, v132, v133, v134\n\t"
      "v_max_u32_e32 v234, v234, v135\n\t"
      "v_cmp_eq_u32_e32 vcc, -1, v234\n\t"
      "s_cbranch_vccnz FQ1L_%=\n\t"
      "FQ1X_%=:\n\t"
      "s_branch FQ1B_%=\n\t"
      "FQ2R_%=:\n\t"
      "s_mov_b32 s51, 0\n\t"
      "FQ2L_%=:\n\t"
      "s_add_u32 s51, s51, 1\n\t"
      "s_cmp_lt_u32 s51, 262144\n\t"
      "s_cbranch_scc0 FQ2X_%=\n\t"
      "s_waitcnt vmcnt(0)\n\t"
      "s_sleep 1\n\t"
      "global_load_dwordx4 v[136:139], v178, s[52:53] sc1\n\t"
      "global_load_dwordx4 v[140:143], v179, s[52:53] sc1\n\t"
      "global_load_dwordx4 v[144:147], v180, s[52:53] sc1\n\t"
      "global_load_dwordx4 v[148:151], v181, s[52:53] sc1\n\t"
      "global_load_dwordx4 v[152:155], v182, s[52:53] sc1\n\t"
      "global_load_dwordx4 v[156:159], v183, s[52:53] sc1\n\t"
      "global_load_dwordx4 v[128:131], v176, s[54:55] sc1\n\t"
      "global_load_dwordx4 v[132:135], v177, s[54:55] sc1\n\t"
      "s_waitcnt vmcnt(7)\n\t"
      "v_max3_u32 v234, v136, v137, v138\n\t"
      "v_max_u32_e32 v234, v234, v139\n\t"
      "v_cmp_eq_u32_e32 vcc, -1, v234\n\t"
      "s_cbranch_vccnz FQ2L_%=\n\t"
      "FQ2X_%=:\n\t"
      "s_branch FQ2B_%=\n\t"
      "FQ3R_%=:\n\t"
      "s_mov_b32 s51, 0\n\t"
      "FQ3L_%=:\n\t"
      "s_add_u32 s51, s51, 1\n\t"
      "s_cmp_lt_u32 s51, 262144\n\t"
      "s_cbranch_scc0 FQ3X_%=\n\t"
      "s_waitcnt vmcnt(0)\n\t"
      "s_sleep 1\n\t"
      "global_load_dwordx4 v[140:143], v179, s[52:53] sc1\n\t"
      "global_load_dwordx4 v[144:147], v180, s[52:53] sc1\n\t"
      "global_load_dwordx4 v[148:151], v181, s[52:53] sc1\n\t"
      "global_load_dwordx4 v[152:155], v182, s[52:53] sc1\n\t"
      "global_load_dwordx4 v[156:159], v183, s[52:53] sc1\n\t"
      "global_load_dwordx4 v[128:131], v176, s[54:55] sc1\n\t"
      "global_load_dwordx4 v[132:135], v177, s[54:55] sc1\n\t"
      "global_load_dwordx4 v[136:139], v178, s[54:55] sc1\n\t"
      "s_waitcnt vmcnt(7)\n\t"
      "v_max3_u32 v234, v140, v141, v142\n\t"
      "v_max_u32_e32 v234, v234, v143\n\t"
      "v_cmp_eq_u32_e32 vcc, -1, v234\n\t"
      "s_cbranch_vccnz FQ3L_%=\n\t"
      "FQ3X_%=:\n\t"
      "s_branch FQ3B_%=\n\t"
      "FQ4R_%=:\n\t"
      "s_mov_b32 s51, 0\n\t"
      "FQ4L_%=:\n\t"
      "s_add_u32 s51, s51, 1\n\t"
      "s_cmp_lt_u32 s51, 262144\n\t"
      "s_cbranch_scc0 FQ4X_%=\n\t"
      "s_waitcnt vmcnt(0)\n\t"
      "s_sleep 1\n\t"
      "global_load_dwordx4 v[144:147], v180, s[52:53] sc1\n\t"
      "global_load_dwordx4 v[148:151], v181, s[52:53] sc1\n\t"
      "global_load_dwordx4 v[152:155], v182, s[52:53] sc1\n\t"
      "global_load_dwordx4 v[156:159], v183, s[52:53] sc1\n\t"
      "global_load_dwordx4 v[128:131], v176, s[54:55] sc1\n\t"
      "global_load_dwordx4 v[132:135], v177, s[54:55] sc1\n\t"
      "global_load_dwordx4 v[136:139], v178, s[54:55] sc1\n\t"
      "global_load_dwordx4 v[140:143], v179, s[54:55] sc1\n\t"
      "s_waitcnt vmcnt(7)\n\t"
      "v_max3_u32 v234, v144, v145, v146\n\t"
      "v_max_u32_e32 v234, v234, v147\n\t"
      "v_cmp_eq_u32_e32 vcc, -1, v234\n\t"
      "s_cbranch_vccnz FQ4L_%=\n\t"
      "FQ4X_%=:\n\t"
      "s_branch FQ4B_%=\n\t"
      "FQ5R_%=:\n\t"
      "s_mov_b32 s51, 0\n\t"
      "FQ5L_%=:\n\t"
      "s_add_u32 s51, s51, 1\n\t"
      "s_cmp_lt_u32 s51, 262144\n\t"
      "s_cbranch_scc0 FQ5X_%=\n\t"
      "s_waitcnt vmcnt(0)\n\t"
      "s_sleep 1\n\t"
      "global_load_dwordx4 v[148:151], v181, s[52:53] sc1\n\t"
      "global_load_dwordx4 v[152:155], v182, s[52:53] sc1\n\t"
      "global_load_dwordx4 v[156:159], v183, s[52:53] sc1\n\t"
      "global_load_dwordx4 v[128:131], v176, s[54:55] sc1\n\t"
      "global_load_dwordx4 v[132:135], v177, s[54:55] sc1\n\t"
      "global_load_dwordx4 v[136:139], v178, s[54:55] sc1\n\t"
      "global_load_dwordx4 v[140:143], v179, s[54:55] sc1\n\t"
      "global_load_dwordx4 v[144:147], v180, s[54:55] sc1\n\t"
      "s_waitcnt vmcnt(7)\n\t"
      "v_max3_u32 v234, v148, v149, v150\n\t"
      "v_max_u32_e32 v234, v234, v151\n\t"
      "v_cmp_eq_u32_e32 vcc, -1, v234\n\t"
      "s_cbranch_vccnz FQ5L_%=\n\t"
      "FQ5X_%=:\n\t"
      "s_branch FQ5B_%=\n\t"
      "FQ6R_%=:\n\t"
      "s_mov_b32 s51, 0\n\t"
      "FQ6L_%=:\n\t"
      "s_add_u32 s51, s51, 1\n\t"
      "s_cmp_lt_u32 s51, 262144\n\t"
      "s_cbranch_scc0 FQ6X_%=\n\t"
      "s_waitcnt vmcnt(0)\n\t"
      "s_sleep 1\n\t"
      "global_load_dwordx4 v[152:155], v182, s[52:53] sc1\n\t"
      "global_load_dwordx4 v[156:159], v183, s[52:53] sc1\n\t"
      "global_load_dwordx4 v[128:131], v176, s[54:55] sc1\n\t"
      "global_load_dwordx4 v[132:135], v177, s[54:55] sc1\n\t"
      "global_load_dwordx4 v[136:139], v178, s[54:55] sc1\n\t"
      "global_load_dwordx4 v[140:143], v179, s[54:55] sc1\n\t"
      "global_load_dwordx4 v[144:147], v180, s[54:55] sc1\n\t"
      "global_load_dwordx4 v[148:151], v181, s[54:55] sc1\n\t"
      "s_waitcnt vmcnt(7)\n\t"
      "v_max3_u32 v234, v152, v153, v154\n\t"
      "v_max_u32_e32 v234, v234, v155\n\t"
      "v_cmp_eq_u32_e32 vcc, -1, v234\n\t"
      "s_cbranch_vccnz FQ6L_%=\n\t"
      "FQ6X_%=:\n\t"
      "s_branch FQ6B_%=\n\t"
      "FQ7R_%=:\n\t"
      "s_mov_b32 s51, 0\n\t"
      "FQ7L_%=:\n\t"
      "s_add_u32 s51, s51, 1\n\t"
      "s_cmp_lt_u32 s51, 262144\n\t"
      "s_cbranch_scc0 FQ7X_%=\n\t"
      "s_waitcnt vmcnt(0)\n\t"
      "s_sleep 1\n\t"
      "global_load_dwordx4 v[156:159], v183, s[52:53] sc1\n\t"
      "global_load_dwordx4 v[128:131], v176, s[54:55] sc1\n\t"
      "global_load_dwordx4 v[132:135], v177, s[54:55] sc1\n\t"
      "global_load_dwordx4 v[136:139], v178, s[54:55] sc1\n\t"
      "global_load_dwordx4 v[140:143], v179, s[54:55] sc1\n\t"
      "global_load_dwordx4 v[144:147], v180, s[54:55] sc1\n\t"
      "global_load_dwordx4 v[148:151], v181, s[54:55] sc1\n\t"
      "global_load_dwordx4 v[152:155], v182, s[54:55] sc1\n\t"
      "s_waitcnt vmcnt(7)\n\t"
      "v_max3_u32 v234, v156, v157, v158\n\t"
      "v_max_u32_e32 v234, v234, v159\n\t"
      "v_cmp_eq_u32_e32 vcc, -1, v234\n\t"
      "s_cbranch_vccnz FQ7L_%=\n\t"
      "FQ7X_%=:\n\t"
      "s_branch FQ7B_%=\n\t"
      "NQ1R_%=:\n\t"
      "s_mov_b32 s51, 0\n\t"
      "NQ1L_%=:\n\t"
      "s_add_u32 s51, s51, 1\n\t"
      "s_cmp_lt_u32 s51, 262144\n\t"
      "s_cbranch_scc0 NQ1X_%=\n\t"
      "s_waitcnt vmcnt(0)\n\t"
      "s_sleep 1\n\t"
      "global_load_dwordx4 v[128:131], v176, s[54:55] sc1\n\t"
      "global_load_dwordx4 v[132:135], v177, s[54:55] sc1\n\t"
      "global_load_dwordx4 v[136:139], v178, s[54:55] sc1\n\t"
      "global_load_dwordx4 v[140:143], v179, s[54:55] sc1\n\t"
      "global_load_dwordx4 v[144:147], v180, s[54:55] sc1\n\t"
      "global_load_dwordx4 v[148:151], v181, s[54:55] sc1\n\t"
      "global_load_dwordx4 v[152:155], v182, s[54:55] sc1\n\t"
      "global_load_dwordx4 v[156:159], v183, s[54:55] sc1\n\t"
      "s_waitcnt vmcnt(0)\n\t"
      "v_max3_u32 v234, v128, v129, v130\n\t"
      "v_max3_u32 v234, v234, v131, v132\n\t"
      "v_max3_u32 v234, v234, v133, v134\n\t"
      "v_max3_u32 v234, v234, v135, v136\n\t"
      "v_max3_u32 v234, v234, v137, v138\n\t"
      "v_max3_u32 v234, v234, v139, v140\n\t"
      "v_max3_u32 v234, v234, v141, v142\n\t"
      "v_max3_u32 v234, v234, v143, v144\n\t"
      "v_max3_u32 v234, v234, v145, v146\n\t"
      "v_max3_u32 v234, v234, v147, v148\n\t"
      "v_max3_u32 v234, v234, v149, v150\n\t"
      "v_max3_u32 v234, v234, v151, v152\n\t"
      "v_max3_u32 v234, v234, v153, v154\n\t"
      "v_max3_u32 v234, v234, v155, v156\n\t"
      "v_max3_u32 v234, v234, v157, v158\n\t"
      "v_max_u32_e32 v234, v234, v159\n\t"
      "v_cmp_eq_u32_e32 vcc, -1, v234\n\t"
      "s_cbranch_vccnz NQ1L_%=\n\t"
      "NQ1X_%=:\n\t"
      "v_mfma_f32_16x16x32_bf16 v[200:203], v[0:3], v[128:131], v[236:239]\n\t"
      "v_mfma_f32_16x16x32_bf16 v[204:207], v[4:7], v[128:131], 0\n\t"
      "v_mfma_f32_16x16x32_bf16 v[208:211], v[8:11], v[128:131], 0\n\t"
      "v_mfma_f32_16x16x32_bf16 v[212:215], v[12:15], v[128:131], 0\n\t"
      "v_mfma_f32_16x16x32_bf16 v[200:203], v[16:19], v[132:135], v[200:203]\n\t"
      "v_mfma_f32_16x16x32_bf16 v[204:207], v[20:23], v[132:135], v[204:207]\n\t"
      "v_mfma_f32_16x16x32_bf16 v[208:211], v[24:27], v[132:135], v[208:211]\n\t"
      "v_mfma_f32_16x16x32_bf16 v[212:215], v[28:31], v[132:135], v[212:215]\n\t"
      "v_mfma_f32_16x16x32_bf16 v[200:203], v[32:35], v[136:139], v[200:203]\n\t"
      "v_mfma_f32_16x16x32_bf16 v[204:207], v[36:39], v[136:139], v[204:207]\n\t"
      "v_mfma_f32_16x16x32_bf16 v[208:211], v[40:43], v[136:139], v[208:211]\n\t"
      "v_mfma_f32_16x16x32_bf16 v[212:215], v[44:47], v[136:139], v[212:215]\n\t"
      "v_mfma_f32_16x16x32_bf16 v[200:203], v[48:51], v[140:143], v[200:203]\n\t"
      "v_mfma_f32_16x16x32_bf16 v[204:207], v[52:55], v[140:143], v[204:207]\n\t"
      "v_mfma_f32_16x16x32_bf16 v[208:211], v[56:59], v[140:143], v[208:211]\n\t"
      "v_mfma_f32_16x16x32_bf16 v[212:215], v[60:63], v[140:143], v[212:215]\n\t"
      "v_mfma_f32_16x16x32_bf16 v[200:203], v[64:67], v[144:147], v[200:203]\n\t"
      "v_mfma_f32_16x16x32_bf16 v[204:207], v[68:71], v[144:147], v[204:207]\n\t"
      "v_mfma_f32_16x16x32_bf16 v[208:211], v[72:75], v[144:147], v[208:211]\n\t"
      "v_mfma_f32_16x16x32_bf16 v[212:215], v[76:79], v[144:147], v[212:215]\n\t"
      "v_mfma_f32_16x16x32_bf16 v[200:203], v[80:83], v[148:151], v[200:203]\n\t"
      "v_mfma_f32_16x16x32_bf16 v[204:207], v[84:87], v[148:151], v[204:207]\n\t"
      "v_mfma_f32_16x16x32_bf16 v[208:211], v[88:91], v[148:151], v[208:211]\n\t"
      "v_mfma_f32_16x16x32_bf16 v[212:215], v[92:95], v[148:151], v[212:215]\n\t"
      "v_mfma_f32_16x16x32_bf16 v[200:203], v[96:99], v[152:155], v[200:203]\n\t"
      "v_mfma_f32_16x16x32_bf16 v[204:207], v[100:103], v[152:155], v[204:207]\n\t"
      "v_mfma_f32_16x16x32_bf16 v[208:211], v[104:107], v[152:155], v[208:211]\n\t"
      "v_mfma_f32_16x16x32_bf16 v[212:215], v[108:111], v[152:155], v[212:215]\n\t"
      "v_mfma_f32_16x16x32_bf16 v[200:203], v[112:115], v[156:159], v[200:203]\n\t"
      "v_mfma_f32_16x16x32_bf16 v[204:207], v[116:119], v[156:159], v[204:207]\n\t"
      "v_mfma_f32_16x16x32_bf16 v[208:211], v[120:123], v[156:159], v[208:211]\n\t"
      "v_mfma_f32_16x16x32_bf16 v[212:215], v[124:127], v[156:159], v[212:215]\n\t"
      "global_load_dwordx4 v[128:131], v176, s[56:57] sc1\n\t"
      "global_load_dwordx4 v[132:135], v177, s[56:57] sc1\n\t"
      "global_load_dwordx4 v[136:139], v178, s[56:57] sc1\n\t"
      "global_load_dwordx4 v[140:143], v179, s[56:57] sc1\n\t"
      "global_load_dwordx4 v[144:147], v180, s[56:57] sc1\n\t"
      "global_load_dwordx4 v[148:151], v181, s[56:57] sc1\n\t"
      "global_load_dwordx4 v[152:155], v182, s[56:57] sc1\n\t"
      "global_load_dwordx4 v[156:159], v183, s[56:57] sc1\n\t"
      "s_waitcnt vmcnt(0)\n\t"
      "s_nop 7\n\t"
      "s_branch NQ1B_%=\n\t"
      "NQ2R_%=:\n\t"
      "s_mov_b32 s51, 0\n\t"
      "NQ2L_%=:\n\t"
      "s_add_u32 s51, s51, 1\n\t"
      "s_cmp_lt_u32 s51, 262144\n\t"
      "s_cbranch_scc0 NQ2X_%=\n\t"
      "s_waitcnt vmcnt(0)\n\t"
      "s_sleep 1\n\t"
      "global_load_dwordx4 v[128:131], v176, s[56:57] sc1\n\t"
      "global_load_dwordx4 v[132:135], v177, s[56:57] sc1\n\t"
      "global_load_dwordx4 v[136:139], v178, s[56:57] sc1\n\t"
      "global_load_dwordx4 v[140:143], v179, s[56:57] sc1\n\t"
      "global_load_dwordx4 v[144:147], v180, s[56:57] sc1\n\t"
      "global_load_dwordx4 v[148:151], v181, s[56:57] sc1\n\t"
      "global_load_dwordx4 v[152:155], v182, s[56:57] sc1\n\t"
      "global_load_dwordx4 v[156:159], v183, s[56:57] sc1\n\t"
      "s_waitcnt vmcnt(0)\n\t"
      "v_max3_u32 v234, v128, v129, v130\n\t"
      "v_max3_u32 v234, v234, v131, v132\n\t"
      "v_max3_u32 v234, v234, v133, v134\n\t"
      "v_max3_u32 v234, v234, v135, v136\n\t"
      "v_max3_u32 v234, v234, v137, v138\n\t"
      "v_max3_u32 v234, v234, v139, v140\n\t"
      "v_max3_u32 v234, v234, v141, v142\n\t"
      "v_max3_u32 v234, v234, v143, v144\n\t"
      "v_max3_u32 v234, v234, v145, v146\n\t"
      "v_max3_u32 v234, v234, v147, v148\n\t"
      "v_max3_u32 v234, v234, v149, v150\n\t"
      "v_max3_u32 v234, v234, v151, v152\n\t"
      "v_max3_u32 v234, v234, v153, v154\n\t"
      "v_max3_u32 v234, v234, v155, v156\n\t"
      "v_max3_u32 v234, v234, v157, v158\n\t"
      "v_max_u32_e32 v234, v234, v159\n\t"
      "v_cmp_eq_u32_e32 vcc, -1, v234\n\t"
      "s_cbranch_vccnz NQ2L_%=\n\t"
      "NQ2X_%=:\n\t"
      "v_mfma_f32_16x16x32_bf16 v[184:187], v[0:3], v[128:131], v[236:239]\n\t"
      "v_mfma_f32_16x16x32_bf16 v[188:191], v[4:7], v[128:131], 0\n\t"
      "v_mfma_f32_16x16x32_bf16 v[192:195], v[8:11], v[128:131], 0\n\t"
      "v_mfma_f32_16x16x32_bf16 v[196:199], v[12:15], v[128:131], 0\n\t"
      "v_mfma_f32_16x16x32_bf16 v[184:187], v[16:19], v[132:135], v[184:187]\n\t"
      "v_mfma_f32_16x16x32_bf16 v[188:191], v[20:23], v[132:135], v[188:191]\n\t"
      "v_mfma_f32_16x16x32_bf16 v[192:195], v[24:27], v[132:135], v[192:195]\n\t"
      "v_mfma_f32_16x16x32_bf16 v[196:199], v[28:31], v[132:135], v[196:199]\n\t"
      "v_mfma_f32_16x16x32_bf16 v[184:187], v[32:35], v[136:139], v[184:187]\n\t"
      "v_mfma_f32_16x16x32_bf16 v[188:191], v[36:39], v[136:139], v[188:191]\n\t"
      "v_mfma_f32_16x16x32_bf16 v[192:195], v[40:43], v[136:139], v[192:195]\n\t"
      "v_mfma_f32_16x16x32_bf16 v[196:199], v[44:47], v[136:139], v[196:199]\n\t"
      "v_mfma_f32_16x16x32_bf16 v[184:187], v[48:51], v[140:143], v[184:187]\n\t"
      "v_mfma_f32_16x16x32_bf16 v[188:191], v[52:55], v[140:143], v[188:191]\n\t"
      "v_mfma_f32_16x16x32_bf16 v[192:195], v[56:59], v[140:143], v[192:195]\n\t"
      "v_mfma_f32_16x16x32_bf16 v[196:199], v[60:63], v[140:143], v[196:199]\n\t"
      "v_mfma_f32_16x16x32_bf16 v[184:187], v[64:67], v[144:147], v[184:187]\n\t"
      "v_mfma_f32_16x16x32_bf16 v[188:191], v[68:71], v[144:147], v[188:191]\n\t"
      "v_mfma_f32_16x16x32_bf16 v[192:195], v[72:75], v[144:147], v[192:195]\n\t"
      "v_mfma_f32_16x16x32_bf16 v[196:199], v[76:79], v[144:147], v[196:199]\n\t"
      "v_mfma_f32_16x16x32_bf16 v[184:187], v[80:83], v[148:151], v[184:187]\n\t"
      "v_mfma_f32_16x16x32_bf16 v[188:191], v[84:87], v[148:151], v[188:191]\n\t"
      "v_mfma_f32_16x16x32_bf16 v[192:195], v[88:91], v[148:151], v[192:195]\n\t"
      "v_mfma_f32_16x16x32_bf16 v[196:199], v[92:95], v[148:151], v[196:199]\n\t"
      "v_mfma_f32_16x16x32_bf16 v[184:187], v[96:99], v[152:155], v[184:187]\n\t"
      "v_mfma_f32_16x16x32_bf16 v[188:191], v[100:103], v[152:155], v[188:191]\n\t"
      "v_mfma_f32_16x16x32_bf16 v[192:195], v[104:107], v[152:155], v[192:195]\n\t"
      "v_mfma_f32_16x16x32_bf16 v[196:199], v[108:111], v[152:155], v[196:199]\n\t"
      "v_mfma_f32_16x16x32_bf16 v[184:187], v[112:115], v[156:159], v[184:187]\n\t"
      "v_mfma_f32_16x16x32_bf16 v[188:191], v[116:119], v[156:159], v[188:191]\n\t"
      "v_mfma_f32_16x16x32_bf16 v[192:195], v[120:123], v[156:159], v[192:195]\n\t"
      "v_mfma_f32_16x16x32_bf16 v[196:199], v[124:127], v[156:159], v[196:199]\n\t"
      "global_load_dwordx4 v[128:131], v176, s[58:59] sc1\n\t"
      "global_load_dwordx4 v[132:135], v177, s[58:59] sc1\n\t"
      "global_load_dwordx4 v[136:139], v178, s[58:59] sc1\n\t"
      "global_load_dwordx4 v[140:143], v179, s[58:59] sc1\n\t"
      "global_load_dwordx4 v[144:147], v180, s[58:59] sc1\n\t"
      "global_load_dwordx4 v[148:151], v181, s[58:59] sc1\n\t"
      "global_load_dwordx4 v[152:155], v182, s[58:59] sc1\n\t"
      "global_load_dwordx4 v[156:159], v183, s[58:59] sc1\n\t"
      "s_waitcnt vmcnt(0)\n\t"
      "s_nop 7\n\t"
      "s_branch NQ2B_%=\n\t"
      "NQ3R_%=:\n\t"
      "s_mov_b32 s51, 0\n\t"
      "NQ3L_%=:\n\t"
      "s_add_u32 s51, s51, 1\n\t"
      "s_cmp_lt_u32 s51, 262144\n\t"
      "s_cbranch_scc0 NQ3X_%=\n\t"
      "s_waitcnt vmcnt(0)\n\t"
      "s_sleep 1\n\t"
      "global_load_dwordx4 v[128:131], v176, s[58:59] sc1\n\t"
      "global_load_dwordx4 v[132:135], v177, s[58:59] sc1\n\t"
      "global_load_dwordx4 v[136:139], v178, s[58:59] sc1\n\t"
      "global_load_dwordx4 v[140:143], v179, s[58:59] sc1\n\t"
      "global_load_dwordx4 v[144:147], v180, s[58:59] sc1\n\t"
      "global_load_dwordx4 v[148:151], v181, s[58:59] sc1\n\t"
      "global_load_dwordx4 v[152:155], v182, s[58:59] sc1\n\t"
      "global_load_dwordx4 v[156:159], v183, s[58:59] sc1\n\t"
      "s_waitcnt vmcnt(0)\n\t"
      "v_max3_u32 v234, v128, v129, v130\n\t"
      "v_max3_u32 v234, v234, v131, v132\n\t"
      "v_max3_u32 v234, v234, v133, v134\n\t"
      "v_max3_u32 v234, v234, v135, v136\n\t"
      "v_max3_u32 v234, v234, v137, v138\n\t"
      "v_max3_u32 v234, v234, v139, v140\n\t"
      "v_max3_u32 v234, v234, v141, v142\n\t"
      "v_max3_u32 v234, v234, v143, v144\n\t"
      "v_max3_u32 v234, v234, v145, v146\n\t"
      "v_max3_u32 v234, v234, v147, v148\n\t"
      "v_max3_u32 v234, v234, v149, v150\n\t"
      "v_max3_u32 v234, v234, v151, v152\n\t"
      "v_max3_u32 v234, v234, v153, v154\n\t"
      "v_max3_u32 v234, v234, v155, v156\n\t"
      "v_max3_u32 v234, v234, v157, v158\n\t"
      "v_max_u32_e32 v234, v234, v159\n\t"
      "v_cmp_eq_u32_e32 vcc, -1, v234\n\t"
      "s_cbranch_vccnz NQ3L_%=\n\t"
      "NQ3X_%=:\n\t"
      "v_mfma_f32_16x16x32_bf16 v[200:203], v[0:3], v[128:131], v[236:239]\n\t"
      "v_mfma_f32_16x16x32_bf16 v[204:207], v[4:7], v[128:131], 0\n\t"
      "v_mfma_f32_16x16x32_bf16 v[208:211], v[8:11], v[128:131], 0\n\t"
      "v_mfma_f32_16x16x32_bf16 v[212:215], v[12:15], v[128:131], 0\n\t"
      "v_mfma_f32_16x16x32_bf16 v[200:203], v[16:19], v[132:135], v[200:203]\n\t"
      "v_mfma_f32_16x16x32_bf16 v[204:207], v[20:23], v[132:135], v[204:207]\n\t"
      "v_mfma_f32_16x16x32_bf16 v[208:211], v[24:27], v[132:135], v[208:211]\n\t"
      "v_mfma_f32_16x16x32_bf16 v[212:215], v[28:31], v[132:135], v[212:215]\n\t"
      "v_mfma_f32_16x16x32_bf16 v[200:203], v[32:35], v[136:139], v[200:203]\n\t"
      "v_mfma_f32_16x16x32_bf16 v[204:207], v[36:39], v[136:139], v[204:207]\n\t"
      "v_mfma_f32_16x16x32_bf16 v[208:211], v[40:43], v[136:139], v[208:211]\n\t"
      "v_mfma_f32_16x16x32_bf16 v[212:215], v[44:47], v[136:139], v[212:215]\n\t"
      "v_mfma_f32_16x16x32_bf16 v[200:203], v[48:51], v[140:143], v[200:203]\n\t"
      "v_mfma_f32_16x16x32_bf16 v[204:207], v[52:55], v[140:143], v[204:207]\n\t"
      "v_mfma_f32_16x16x32_bf16 v[208:211], v[56:59], v[140:143], v[208:211]\n\t"
      "v_mfma_f32_16x16x32_bf16 v[212:215], v[60:63], v[140:143], v[212:215]\n\t"
      "v_mfma_f32_16x16x32_bf16 v[200:203], v[64:67], v[144:147], v[200:203]\n\t"
      "v_mfma_f32_16x16x32_bf16 v[204:207], v[68:71], v[144:147], v[204:207]\n\t"
      "v_mfma_f32_16x16x32_bf16 v[208:211], v[72:75], v[144:147], v[208:211]\n\t"
      "v_mfma_f32_16x16x32_bf16 v[212:215], v[76:79], v[144:147], v[212:215]\n\t"
      "v_mfma_f32_16x16x32_bf16 v[200:203], v[80:83], v[148:151], v[200:203]\n\t"
      "v_mfma_f32_16x16x32_bf16 v[204:207], v[84:87], v[148:151], v[204:207]\n\t"
      "v_mfma_f32_16x16x32_bf16 v[208:211], v[88:91], v[148:151], v[208:211]\n\t"
      "v_mfma_f32_16x16x32_bf16 v[212:215], v[92:95], v[148:151], v[212:215]\n\t"
      "v_mfma_f32_16x16x32_bf16 v[200:203], v[96:99], v[152:155], v[200:203]\n\t"
      "v_mfma_f32_16x16x32_bf16 v[204:207], v[100:103], v[152:155], v[204:207]\n\t"
      "v_mfma_f32_16x16x32_bf16 v[208:211], v[104:107], v[152:155], v[208:211]\n\t"
      "v_mfma_f32_16x16x32_bf16 v[212:215], v[108:111], v[152:155], v[212:215]\n\t"
      "v_mfma_f32_16x16x32_bf16 v[200:203], v[112:115], v[156:159], v[200:203]\n\t"
      "v_mfma_f32_16x16x32_bf16 v[204:207], v[116:119], v[156:159], v[204:207]\n\t"
      "v_mfma_f32_16x16x32_bf16 v[208:211], v[120:123], v[156:159], v[208:211]\n\t"
      "v_mfma_f32_16x16x32_bf16 v[212:215], v[124:127], v[156:159], v[212:215]\n\t"
      "global_load_dwordx4 v[128:131], v176, s[60:61] sc1\n\t"
      "global_load_dwordx4 v[132:135], v177, s[60:61] sc1\n\t"
      "global_load_dwordx4 v[136:139], v178, s[60:61] sc1\n\t"
      "global_load_dwordx4 v[140:143], v179, s[60:61] sc1\n\t"
      "global_load_dwordx4 v[144:147], v180, s[60:61] sc1\n\t"
      "global_load_dwordx4 v[148:151], v181, s[60:61] sc1\n\t"
      "global_load_dwordx4 v[152:155], v182, s[60:61] sc1\n\t"
      "global_load_dwordx4 v[156:159], v183, s[60:61] sc1\n\t"
      "s_waitcnt vmcnt(0)\n\t"
      "s_nop 7\n\t"
      "s_branch NQ3B_%=\n\t"
      "NQ4R_%=:\n\t"
      "s_mov_b32 s51, 0\n\t"
      "NQ4L_%=:\n\t"
      "s_add_u32 s51, s51, 1\n\t"
      "s_cmp_lt_u32 s51, 262144\n\t"
      "s_cbranch_scc0 NQ4X_%=\n\t"
      "s_waitcnt vmcnt(0)\n\t"
      "s_sleep 1\n\t"
      "global_load_dwordx4 v[128:131], v176, s[60:61] sc1\n\t"
      "global_load_dwordx4 v[132:135], v177, s[60:61] sc1\n\t"
      "global_load_dwordx4 v[136:139], v178, s[60:61] sc1\n\t"
      "global_load_dwordx4 v[140:143], v179, s[60:61] sc1\n\t"
      "global_load_dwordx4 v[144:147], v180, s[60:61] sc1\n\t"
      "global_load_dwordx4 v[148:151], v181, s[60:61] sc1\n\t"
      "global_load_dwordx4 v[152:155], v182, s[60:61] sc1\n\t"
      "global_load_dwordx4 v[156:159], v183, s[60:61] sc1\n\t"
      "s_waitcnt vmcnt(0)\n\t"
      "v_max3_u32 v234, v128, v129, v130\n\t"
      "v_max3_u32 v234, v234, v131, v132\n\t"
      "v_max3_u32 v234, v234, v133, v134\n\t"
      "v_max3_u32 v234, v234, v135, v136\n\t"
      "v_max3_u32 v234, v234, v137, v138\n\t"
      "v_max3_u32 v234, v234, v139, v140\n\t"
      "v_max3_u32 v234, v234, v141, v142\n\t"
      "v_max3_u32 v234, v234, v143, v144\n\t"
      "v_max3_u32 v234, v234, v145, v146\n\t"
      "v_max3_u32 v234, v234, v147, v148\n\t"
      "v_max3_u32 v234, v234, v149, v150\n\t"
      "v_max3_u32 v234, v234, v151, v152\n\t"
      "v_max3_u32 v234, v234, v153, v154\n\t"
      "v_max3_u32 v234, v234, v155, v156\n\t"
      "v_max3_u32 v234, v234, v157, v158\n\t"
      "v_max_u32_e32 v234, v234, v159\n\t"
      "v_cmp_eq_u32_e32 vcc, -1, v234\n\t"
      "s_cbranch_vccnz NQ4L_%=\n\t"
      "NQ4X_%=:\n\t"
      "v_mfma_f32_16x16x32_bf16 v[184:187], v[0:3], v[128:131], v[236:239]\n\t"
      "v_mfma_f32_16x16x32_bf16 v[188:191], v[4:7], v[128:131], 0\n\t"
      "v_mfma_f32_16x16x32_bf16 v[192:195], v[8:11], v[128:131], 0\n\t"
      "v_mfma_f32_16x16x32_bf16 v[196:199], v[12:15], v[128:131], 0\n\t"
      "v_mfma_f32_16x16x32_bf16 v[184:187], v[16:19], v[132:135], v[184:187]\n\t"
      "v_mfma_f32_16x16x32_bf16 v[188:191], v[20:23], v[132:135], v[188:191]\n\t"
      "v_mfma_f32_16x16x32_bf16 v[192:195], v[24:27], v[132:135], v[192:195]\n\t"
      "v_mfma_f32_16x16x32_bf16 v[196:199], v[28:31], v[132:135], v[196:199]\n\t"
      "v_mfma_f32_16x16x32_bf16 v[184:187], v[32:35], v[136:139], v[184:187]\n\t"
      "v_mfma_f32_16x16x32_bf16 v[188:191], v[36:39], v[136:139], v[188:191]\n\t"
      "v_mfma_f32_16x16x32_bf16 v[192:195], v[40:43], v[136:139], v[192:195]\n\t"
      "v_mfma_f32_16x16x32_bf16 v[196:199], v[44:47], v[136:139], v[196:199]\n\t"
      "v_mfma_f32_16x16x32_bf16 v[184:187], v[48:51], v[140:143], v[184:187]\n\t"
      "v_mfma_f32_16x16x32_bf16 v[188:191], v[52:55], v[140:143], v[188:191]\n\t"
      "v_mfma_f32_16x16x32_bf16 v[192:195], v[56:59], v[140:143], v[192:195]\n\t"
      "v_mfma_f32_16x16x32_bf16 v[196:199], v[60:63], v[140:143], v[196:199]\n\t"
      "v_mfma_f32_16x16x32_bf16 v[184:187], v[64:67], v[144:147], v[184:187]\n\t"
      "v_mfma_f32_16x16x32_bf16 v[188:191], v[68:71], v[144:147], v[188:191]\n\t"
      "v_mfma_f32_16x16x32_bf16 v[192:195], v[72:75], v[144:147], v[192:195]\n\t"
      "v_mfma_f32_16x16x32_bf16 v[196:199], v[76:79], v[144:147], v[196:199]\n\t"
      "v_mfma_f32_16x16x32_bf16 v[184:187], v[80:83], v[148:151], v[184:187]\n\t"
      "v_mfma_f32_16x16x32_bf16 v[188:191], v[84:87], v[148:151], v[188:191]\n\t"
      "v_mfma_f32_16x16x32_bf16 v[192:195], v[88:91], v[148:151], v[192:195]\n\t"
      "v_mfma_f32_16x16x32_bf16 v[196:199], v[92:95], v[148:151], v[196:199]\n\t"
      "v_mfma_f32_16x16x32_bf16 v[184:187], v[96:99], v[152:155], v[184:187]\n\t"
      "v_mfma_f32_16x16x32_bf16 v[188:191], v[100:103], v[152:155], v[188:191]\n\t"
      "v_mfma_f32_16x16x32_bf16 v[192:195], v[104:107], v[152:155], v[192:195]\n\t"
      "v_mfma_f32_16x16x32_bf16 v[196:199], v[108:111], v[152:155], v[196:199]\n\t"
      "v_mfma_f32_16x16x32_bf16 v[184:187], v[112:115], v[156:159], v[184:187]\n\t"
      "v_mfma_f32_16x16x32_bf16 v[188:191], v[116:119], v[156:159], v[188:191]\n\t"
      "v_mfma_f32_16x16x32_bf16 v[192:195], v[120:123], v[156:159], v[192:195]\n\t"
      "v_mfma_f32_16x16x32_bf16 v[196:199], v[124:127], v[156:159], v[196:199]\n\t"
      "global_load_dwordx4 v[128:131], v176, s[62:63] sc1\n\t"
      "global_load_dwordx4 v[132:135], v177, s[62:63] sc1\n\t"
      "global_load_dwordx4 v[136:139], v178, s[62:63] sc1\n\t"
      "global_load_dwordx4 v[140:143], v179, s[62:63] sc1\n\t"
      "global_load_dwordx4 v[144:147], v180, s[62:63] sc1\n\t"
      "global_load_dwordx4 v[148:151], v181, s[62:63] sc1\n\t"
      "global_load_dwordx4 v[152:155], v182, s[62:63] sc1\n\t"
      "global_load_dwordx4 v[156:159], v183, s[62:63] sc1\n\t"
      "s_waitcnt vmcnt(0)\n\t"
      "s_nop 7\n\t"
      "s_branch NQ4B_%=\n\t"
      "NQ5R_%=:\n\t"
      "s_mov_b32 s51, 0\n\t"
      "NQ5L_%=:\n\t"
      "s_add_u32 s51, s51, 1\n\t"
      "s_cmp_lt_u32 s51, 262144\n\t"
      "s_cbranch_scc0 NQ5X_%=\n\t"
      "s_waitcnt vmcnt(0)\n\t"
      "s_sleep 1\n\t"
      "global_load_dwordx4 v[128:131], v176, s[62:63] sc1\n\t"
      "global_load_dwordx4 v[132:135], v177, s[62:63] sc1\n\t"
      "global_load_dwordx4 v[136:139], v178, s[62:63] sc1\n\t"
      "global_load_dwordx4 v[140:143], v179, s[62:63] sc1\n\t"
      "global_load_dwordx4 v[144:147], v180, s[62:63] sc1\n\t"
      "global_load_dwordx4 v[148:151], v181, s[62:63] sc1\n\t"
      "global_load_dwordx4 v[152:155], v182, s[62:63] sc1\n\t"
      "global_load_dwordx4 v[156:159], v183, s[62:63] sc1\n\t"
      "s_waitcnt vmcnt(0)\n\t"
      "v_max3_u32 v234, v128, v129, v130\n\t"
      "v_max3_u32 v234, v234, v131, v132\n\t"
      "v_max3_u32 v234, v234, v133, v134\n\t"
      "v_max3_u32 v234, v234, v135, v136\n\t"
      "v_max3_u32 v234, v234, v137, v138\n\t"
      "v_max3_u32 v234, v234, v139, v140\n\t"
      "v_max3_u32 v234, v234, v141, v142\n\t"
      "v_max3_u32 v234, v234, v143, v144\n\t"
      "v_max3_u32 v234, v234, v145, v146\n\t"
      "v_max3_u32 v234, v234, v147, v148\n\t"
      "v_max3_u32 v234, v234, v149, v150\n\t"
      "v_max3_u32 v234, v234, v151, v152\n\t"
      "v_max3_u32 v234, v234, v153, v154\n\t"
      "v_max3_u32 v234, v234, v155, v156\n\t"
      "v_max3_u32 v234, v234, v157, v158\n\t"
      "v_max_u32_e32 v234, v234, v159\n\t"
      "v_cmp_eq_u32_e32 vcc, -1, v234\n\t"
      "s_cbranch_vccnz NQ5L_%=\n\t"
      "NQ5X_%=:\n\t"
      "v_mfma_f32_16x16x32_bf16 v[200:203], v[0:3], v[128:131], v[236:239]\n\t"
      "v_mfma_f32_16x16x32_bf16 v[204:207], v[4:7], v[128:131], 0\n\t"
      "v_mfma_f32_16x16x32_bf16 v[208:211], v[8:11], v[128:131], 0\n\t"
      "v_mfma_f32_16x16x32_bf16 v[212:215], v[12:15], v[128:131], 0\n\t"
      "v_mfma_f32_16x16x32_bf16 v[200:203], v[16:19], v[132:135], v[200:203]\n\t"
      "v_mfma_f32_16x16x32_bf16 v[204:207], v[20:23], v[132:135], v[204:207]\n\t"
      "v_mfma_f32_16x16x32_bf16 v[208:211], v[24:27], v[132:135], v[208:211]\n\t"
      "v_mfma_f32_16x16x32_bf16 v[212:215], v[28:31], v[132:135], v[212:215]\n\t"
      "v_mfma_f32_16x16x32_bf16 v[200:203], v[32:35], v[136:139], v[200:203]\n\t"
      "v_mfma_f32_16x16x32_bf16 v[204:207], v[36:39], v[136:139], v[204:207]\n\t"
      "v_mfma_f32_16x16x32_bf16 v[208:211], v[40:43], v[136:139], v[208:211]\n\t"
      "v_mfma_f32_16x16x32_bf16 v[212:215], v[44:47], v[136:139], v[212:215]\n\t"
      "v_mfma_f32_16x16x32_bf16 v[200:203], v[48:51], v[140:143], v[200:203]\n\t"
      "v_mfma_f32_16x16x32_bf16 v[204:207], v[52:55], v[140:143], v[204:207]\n\t"
      "v_mfma_f32_16x16x32_bf16 v[208:211], v[56:59], v[140:143], v[208:211]\n\t"
      "v_mfma_f32_16x16x32_bf16 v[212:215], v[60:63], v[140:143], v[212:215]\n\t"
      "v_mfma_f32_16x16x32_bf16 v[200:203], v[64:67], v[144:147], v[200:203]\n\t"
      "v_mfma_f32_16x16x32_bf16 v[204:207], v[68:71], v[144:147], v[204:207]\n\t"
      "v_mfma_f32_16x16x32_bf16 v[208:211], v[72:75], v[144:147], v[208:211]\n\t"
      "v_mfma_f32_16x16x32_bf16 v[212:215], v[76:79], v[144:147], v[212:215]\n\t"
      "v_mfma_f32_16x16x32_bf16 v[200:203], v[80:83], v[148:151], v[200:203]\n\t"
      "v_mfma_f32_16x16x32_bf16 v[204:207], v[84:87], v[148:151], v[204:207]\n\t"
      "v_mfma_f32_16x16x32_bf16 v[208:211], v[88:91], v[148:151], v[208:211]\n\t"
      "v_mfma_f32_16x16x32_bf16 v[212:215], v[92:95], v[148:151], v[212:215]\n\t"
      "v_mfma_f32_16x16x32_bf16 v[200:203], v[96:99], v[152:155], v[200:203]\n\t"
      "v_mfma_f32_16x16x32_bf16 v[204:207], v[100:103], v[152:155], v[204:207]\n\t"
      "v_mfma_f32_16x16x32_bf16 v[208:211], v[104:107], v[152:155], v[208:211]\n\t"
      "v_mfma_f32_16x16x32_bf16 v[212:215], v[108:111], v[152:155], v[212:215]\n\t"
      "v_mfma_f32_16x16x32_bf16 v[200:203], v[112:115], v[156:159], v[200:203]\n\t"
      "v_mfma_f32_16x16x32_bf16 v[204:207], v[116:119], v[156:159], v[204:207]\n\t"
      "v_mfma_f32_16x16x32_bf16 v[208:211], v[120:123], v[156:159], v[208:211]\n\t"
      "v_mfma_f32_16x16x32_bf16 v[212:215], v[124:127], v[156:159], v[212:215]\n\t"
      "global_load_dwordx4 v[128:131], v176, s[64:65] sc1\n\t"
      "global_load_dwordx4 v[132:135], v177, s[64:65] sc1\n\t"
      "global_load_dwordx4 v[136:139], v178, s[64:65] sc1\n\t"
      "global_load_dwordx4 v[140:143], v179, s[64:65] sc1\n\t"
      "global_load_dwordx4 v[144:147], v180, s[64:65] sc1\n\t"
      "global_load_dwordx4 v[148:151], v181, s[64:65] sc1\n\t"
      "global_load_dwordx4 v[152:155], v182, s[64:65] sc1\n\t"
      "global_load_dwordx4 v[156:159], v183, s[64:65] sc1\n\t"
      "s_waitcnt vmcnt(0)\n\t"
      "s_nop 7\n\t"
      "s_branch NQ5B_%=\n\t"
      "NQ6R_%=:\n\t"
      "s_mov_b32 s51, 0\n\t"
      "NQ6L_%=:\n\t"
      "s_add_u32 s51, s51, 1\n\t"
      "s_cmp_lt_u32 s51, 262144\n\t"
      "s_cbranch_scc0 NQ6X_%=\n\t"
      "s_waitcnt vmcnt(0)\n\t"
      "s_sleep 1\n\t"
      "global_load_dwordx4 v[128:131], v176, s[64:65] sc1\n\t"
      "global_load_dwordx4 v[132:135], v177, s[64:65] sc1\n\t"
      "global_load_dwordx4 v[136:139], v178, s[64:65] sc1\n\t"
      "global_load_dwordx4 v[140:143], v179, s[64:65] sc1\n\t"
      "global_load_dwordx4 v[144:147], v180, s[64:65] sc1\n\t"
      "global_load_dwordx4 v[148:151], v181, s[64:65] sc1\n\t"
      "global_load_dwordx4 v[152:155], v182, s[64:65] sc1\n\t"
      "global_load_dwordx4 v[156:159], v183, s[64:65] sc1\n\t"
      "s_waitcnt vmcnt(0)\n\t"
      "v_max3_u32 v234, v128, v129, v130\n\t"
      "v_max3_u32 v234, v234, v131, v132\n\t"
      "v_max3_u32 v234, v234, v133, v134\n\t"
      "v_max3_u32 v234, v234, v135, v136\n\t"
      "v_max3_u32 v234, v234, v137, v138\n\t"
      "v_max3_u32 v234, v234, v139, v140\n\t"
      "v_max3_u32 v234, v234, v141, v142\n\t"
      "v_max3_u32 v234, v234, v143, v144\n\t"
      "v_max3_u32 v234, v234, v145, v146\n\t"
      "v_max3_u32 v234, v234, v147, v148\n\t"
      "v_max3_u32 v234, v234, v149, v150\n\t"
      "v_max3_u32 v234, v234, v151, v152\n\t"
      "v_max3_u32 v234, v234, v153, v154\n\t"
      "v_max3_u32 v234, v234, v155, v156\n\t"
      "v_max3_u32 v234, v234, v157, v158\n\t"
      "v_max_u32_e32 v234, v234, v159\n\t"
      "v_cmp_eq_u32_e32 vcc, -1, v234\n\t"
      "s_cbranch_vccnz NQ6L_%=\n\t"
      "NQ6X_%=:\n\t"
      "v_mfma_f32_16x16x32_bf16 v[184:187], v[0:3], v[128:131], v[236:239]\n\t"
      "v_mfma_f32_16x16x32_bf16 v[188:191], v[4:7], v[128:131], 0\n\t"
      "v_mfma_f32_16x16x32_bf16 v[192:195], v[8:11], v[128:131], 0\n\t"
      "v_mfma_f32_16x16x32_bf16 v[196:199], v[12:15], v[128:131], 0\n\t"
      "v_mfma_f32_16x16x32_bf16 v[184:187], v[16:19], v[132:135], v[184:187]\n\t"
      "v_mfma_f32_16x16x32_bf16 v[188:191], v[20:23], v[132:135], v[188:191]\n\t"
      "v_mfma_f32_16x16x32_bf16 v[192:195], v[24:27], v[132:135], v[192:195]\n\t"
      "v_mfma_f32_16x16x32_bf16 v[196:199], v[28:31], v[132:135], v[196:199]\n\t"
      "v_mfma_f32_16x16x32_bf16 v[184:187], v[32:35], v[136:139], v[184:187]\n\t"
      "v_mfma_f32_16x16x32_bf16 v[188:191], v[36:39], v[136:139], v[188:191]\n\t"
      "v_mfma_f32_16x16x32_bf16 v[192:195], v[40:43], v[136:139], v[192:195]\n\t"
      "v_mfma_f32_16x16x32_bf16 v[196:199], v[44:47], v[136:139], v[196:199]\n\t"
      "v_mfma_f32_16x16x32_bf16 v[184:187], v[48:51], v[140:143], v[184:187]\n\t"
      "v_mfma_f32_16x16x32_bf16 v[188:191], v[52:55], v[140:143], v[188:191]\n\t"
      "v_mfma_f32_16x16x32_bf16 v[192:195], v[56:59], v[140:143], v[192:195]\n\t"
      "v_mfma_f32_16x16x32_bf16 v[196:199], v[60:63], v[140:143], v[196:199]\n\t"
      "v_mfma_f32_16x16x32_bf16 v[184:187], v[64:67], v[144:147], v[184:187]\n\t"
      "v_mfma_f32_16x16x32_bf16 v[188:191], v[68:71], v[144:147], v[188:191]\n\t"
      "v_mfma_f32_16x16x32_bf16 v[192:195], v[72:75], v[144:147], v[192:195]\n\t"
      "v_mfma_f32_16x16x32_bf16 v[196:199], v[76:79], v[144:147], v[196:199]\n\t"
      "v_mfma_f32_16x16x32_bf16 v[184:187], v[80:83], v[148:151], v[184:187]\n\t"
      "v_mfma_f32_16x16x32_bf16 v[188:191], v[84:87], v[148:151], v[188:191]\n\t"
      "v_mfma_f32_16x16x32_bf16 v[192:195], v[88:91], v[148:151], v[192:195]\n\t"
      "v_mfma_f32_16x16x32_bf16 v[196:199], v[92:95], v[148:151], v[196:199]\n\t"
      "v_mfma_f32_16x16x32_bf16 v[184:187], v[96:99], v[152:155], v[184:187]\n\t"
      "v_mfma_f32_16x16x32_bf16 v[188:191], v[100:103], v[152:155], v[188:191]\n\t"
      "v_mfma_f32_16x16x32_bf16 v[192:195], v[104:107], v[152:155], v[192:195]\n\t"
      "v_mfma_f32_16x16x32_bf16 v[196:199], v[108:111], v[152:155], v[196:199]\n\t"
      "v_mfma_f32_16x16x32_bf16 v[184:187], v[112:115], v[156:159], v[184:187]\n\t"
      "v_mfma_f32_16x16x32_bf16 v[188:191], v[116:119], v[156:159], v[188:191]\n\t"
      "v_mfma_f32_16x16x32_bf16 v[192:195], v[120:123], v[156:159], v[192:195]\n\t"
      "v_mfma_f32_16x16x32_bf16 v[196:199], v[124:127], v[156:159], v[196:199]\n\t"
      "s_waitcnt vmcnt(0)\n\t"
      "s_nop 7\n\t"
      "s_branch NQ6B_%=\n\t"
      "FP0R_%=:\n\t"
      "s_mov_b32 s51, 0\n\t"
      "FP0L_%=:\n\t"
      "s_add_u32 s51, s51, 1\n\t"
      "s_cmp_lt_u32 s51, 262144\n\t"
      "s_cbranch_scc0 FP0X_%=\n\t"
      "s_waitcnt vmcnt(0)\n\t"
      "s_sleep 1\n\t"
      "global_load_dwordx4 v[128:131], v176, s[52:53] sc1\n\t"
      "global_load_dwordx4 v[132:135], v177, s[52:53] sc1\n\t"
      "global_load_dwordx4 v[136:139], v178, s[52:53] sc1\n\t"
      "global_load_dwordx4 v[140:143], v179, s[52:53] sc1\n\t"
      "global_load_dwordx4 v[144:147], v180, s[52:53] sc1\n\t"
      "global_load_dwordx4 v[148:151], v181, s[52:53] sc1\n\t"
      "global_load_dwordx4 v[152:155], v182, s[52:53] sc1\n\t"
      "global_load_dwordx4 v[156:159], v183, s[52:53] sc1\n\t"
      "s_waitcnt vmcnt(7)\n\t"
      "v_max3_u32 v234, v128, v129, v130\n\t"
      "v_max_u32_e32 v234, v234, v131\n\t"
      "v_cmp_eq_u32_e32 vcc, -1, v234\n\t"
      "s_cbranch_vccnz FP0L_%=\n\t"
      "FP0X_%=:\n\t"
      "s_branch FP0B_%=\n\t"
      "FP1R_%=:\n\t"
      "s_mov_b32 s51, 0\n\t"
      "FP1L_%=:\n\t"
      "s_add_u32 s51, s51, 1\n\t"
      "s_cmp_lt_u32 s51, 262144\n\t"
      "s_cbranch_scc0 FP1X_%=\n\t"
      "s_waitcnt vmcnt(0)\n\t"
      "s_sleep 1\n\t"
      "global_load_dwordx4 v[132:135], v177, s[52:53] sc1\n\t"
      "global_load_dwordx4 v[136:139], v178, s[52:53] sc1\n\t"
      "global_load_dwordx4 v[140:143], v179, s[52:53] sc1\n\t"
      "global_load_dwordx4 v[144:147], v180, s[52:53] sc1\n\t"
      "global_load_dwordx4 v[148:151], v181, s[52:53] sc1\n\t"
      "global_load_dwordx4 v[152:155], v182, s[52:53] sc1\n\t"
      "global_load_dwordx4 v[156:159], v183, s[52:53] sc1\n\t"
      "global_load_dwordx4 v[128:131], v176, s[54:55] sc1\n\t"
      "s_waitcnt vmcnt(7)\n\t"
      "v_max3_u32 v234, v132, v133, v134\n\t"
      "v_max_u32_e32 v234, v234, v135\n\t"
      "v_cmp_eq_u32_e32 vcc, -1, v234\n\t"
      "s_cbranch_vccnz FP1L_%=\n\t"
      "FP1X_%=:\n\t"
      "s_branch FP1B_%=\n\t"
      "FP2R_%=:\n\t"
      "s_mov_b32 s51, 0\n\t"
      "FP2L_%=:\n\t"
      "s_add_u32 s51, s51, 1\n\t"
      "s_cmp_lt_u32 s51, 262144\n\t"
      "s_cbranch_scc0 FP2X_%=\n\t"
      "s_waitcnt vmcnt(0)\n\t"
      "s_sleep 1\n\t"
      "global_load_dwordx4 v[136:139], v178, s[52:53] sc1\n\t"
      "global_load_dwordx4 v[140:143], v179, s[52:53] sc1\n\t"
      "global_load_dwordx4 v[144:147], v180, s[52:53] sc1\n\t"
      "global_load_dwordx4 v[148:151], v181, s[52:53] sc1\n\t"
      "global_load_dwordx4 v[152:155], v182, s[52:53] sc1\n\t"
      "global_load_dwordx4 v[156:159], v183, s[52:53] sc1\n\t"
      "global_load_dwordx4 v[128:131], v176, s[54:55] sc1\n\t"
      "global_load_dwordx4 v[132:135], v177, s[54:55] sc1\n\t"
      "s_waitcnt vmcnt(7)\n\t"
      "v_max3_u32 v234, v136, v137, v138\n\t"
      "v_max_u32_e32 v234, v234, v139\n\t"
      "v_cmp_eq_u32_e32 vcc, -1, v234\n\t"
      "s_cbranch_vccnz FP2L_%=\n\t"
      "FP2X_%=:\n\t"
      "s_branch FP2B_%=\n\t"
      "FP3R_%=:\n\t"
      "s_mov_b32 s51, 0\n\t"
      "FP3L_%=:\n\t"
      "s_add_u32 s51, s51, 1\n\t"
      "s_cmp_lt_u32 s51, 262144\n\t"
      "s_cbranch_scc0 FP3X_%=\n\t"
      "s_waitcnt vmcnt(0)\n\t"
      "s_sleep 1\n\t"
      "global_load_dwordx4 v[140:143], v179, s[52:53] sc1\n\t"
      "global_load_dwordx4 v[144:147], v180, s[52:53] sc1\n\t"
      "global_load_dwordx4 v[148:151], v181, s[52:53] sc1\n\t"
      "global_load_dwordx4 v[152:155], v182, s[52:53] sc1\n\t"
      "global_load_dwordx4 v[156:159], v183, s[52:53] sc1\n\t"
      "global_load_dwordx4 v[128:131], v176, s[54:55] sc1\n\t"
      "global_load_dwordx4 v[132:135], v177, s[54:55] sc1\n\t"
      "global_load_dwordx4 v[136:139], v178, s[54:55] sc1\n\t"
      "s_waitcnt vmcnt(7)\n\t"
      "v_max3_u32 v234, v140, v141, v142\n\t"
      "v_max_u32_e32 v234, v234, v143\n\t"
      "v_cmp_eq_u32_e32 vcc, -1, v234\n\t"
      "s_cbranch_vccnz FP3L_%=\n\t"
      "FP3X_%=:\n\t"
      "s_branch FP3B_%=\n\t"
      "FP4R_%=:\n\t"
      "s_mov_b32 s51, 0\n\t"
      "FP4L_%=:\n\t"
      "s_add_u32 s51, s51, 1\n\t"
      "s_cmp_lt_u32 s51, 262144\n\t"
      "s_cbranch_scc0 FP4X_%=\n\t"
      "s_waitcnt vmcnt(0)\n\t"
      "s_sleep 1\n\t"
      "global_load_dwordx4 v[144:147], v180, s[52:53] sc1\n\t"
      "global_load_dwordx4 v[148:151], v181, s[52:53] sc1\n\t"
      "global_load_dwordx4 v[152:155], v182, s[52:53] sc1\n\t"
      "global_load_dwordx4 v[156:159], v183, s[52:53] sc1\n\t"
      "global_load_dwordx4 v[128:131], v176, s[54:55] sc1\n\t"
      "global_load_dwordx4 v[132:135], v177, s[54:55] sc1\n\t"
      "global_load_dwordx4 v[136:139], v178, s[54:55] sc1\n\t"
      "global_load_dwordx4 v[140:143], v179, s[54:55] sc1\n\t"
      "s_waitcnt vmcnt(7)\n\t"
      "v_max3_u32 v234, v144, v145, v146\n\t"
      "v_max_u32_e32 v234, v234, v147\n\t"
      "v_cmp_eq_u32_e32 vcc, -1, v234\n\t"
      "s_cbranch_vccnz FP4L_%=\n\t"
      "FP4X_%=:\n\t"
      "s_branch FP4B_%=\n\t"
      "FP5R_%=:\n\t"
      "s_mov_b32 s51, 0\n\t"
      "FP5L_%=:\n\t"
      "s_add_u32 s51, s51, 1\n\t"
      "s_cmp_lt_u32 s51, 262144\n\t"
      "s_cbranch_scc0 FP5X_%=\n\t"
      "s_waitcnt vmcnt(0)\n\t"
      "s_sleep 1\n\t"
      "global_load_dwordx4 v[148:151], v181, s[52:53] sc1\n\t"
      "global_load_dwordx4 v[152:155], v182, s[52:53] sc1\n\t"
      "global_load_dwordx4 v[156:159], v183, s[52:53] sc1\n\t"
      "global_load_dwordx4 v[128:131], v176, s[54:55] sc1\n\t"
      "global_load_dwordx4 v[132:135], v177, s[54:55] sc1\n\t"
      "global_load_dwordx4 v[136:139], v178, s[54:55] sc1\n\t"
      "global_load_dwordx4 v[140:143], v179, s[54:55] sc1\n\t"
      "global_load_dwordx4 v[144:147], v180, s[54:55] sc1\n\t"
      "s_waitcnt vmcnt(7)\n\t"
      "v_max3_u32 v234, v148, v149, v150\n\t"
      "v_max_u32_e32 v234, v234, v151\n\t"
      "v_cmp_eq_u32_e32 vcc, -1, v234\n\t"
      "s_cbranch_vccnz FP5L_%=\n\t"
      "FP5X_%=:\n\t"
      "s_branch FP5B_%=\n\t"
      "FP6R_%=:\n\t"
      "s_mov_b32 s51, 0\n\t"
      "FP6L_%=:\n\t"
      "s_add_u32 s51, s51, 1\n\t"
      "s_cmp_lt_u32 s51, 262144\n\t"
      "s_cbranch_scc0 FP6X_%=\n\t"
      "s_waitcnt vmcnt(0)\n\t"
      "s_sleep 1\n\t"
      "global_load_dwordx4 v[152:155], v182, s[52:53] sc1\n\t"
      "global_load_dwordx4 v[156:159], v183, s[52:53] sc1\n\t"
      "global_load_dwordx4 v[128:131], v176, s[54:55] sc1\n\t"
      "global_load_dwordx4 v[132:135], v177, s[54:55] sc1\n\t"
      "global_load_dwordx4 v[136:139], v178, s[54:55] sc1\n\t"
      "global_load_dwordx4 v[140:143], v179, s[54:55] sc1\n\t"
      "global_load_dwordx4 v[144:147], v180, s[54:55] sc1\n\t"
      "global_load_dwordx4 v[148:151], v181, s[54:55] sc1\n\t"
      "s_waitcnt vmcnt(7)\n\t"
      "v_max3_u32 v234, v152, v153, v154\n\t"
      "v_max_u32_e32 v234, v234, v155\n\t"
      "v_cmp_eq_u32_e32 vcc, -1, v234\n\t"
      "s_cbranch_vccnz FP6L_%=\n\t"
      "FP6X_%=:\n\t"
      "s_branch FP6B_%=\n\t"
      "FP7R_%=:\n\t"
      "s_mov_b32 s51, 0\n\t"
      "FP7L_%=:\n\t"
      "s_add_u32 s51, s51, 1\n\t"
      "s_cmp_lt_u32 s51, 262144\n\t"
      "s_cbranch_scc0 FP7X_%=\n\t"
      "s_waitcnt vmcnt(0)\n\t"
      "s_sleep 1\n\t"
      "global_load_dwordx4 v[156:159], v183, s[52:53] sc1\n\t"
      "global_load_dwordx4 v[128:131], v176, s[54:55] sc1\n\t"
      "global_load_dwordx4 v[132:135], v177, s[54:55] sc1\n\t"
      "global_load_dwordx4 v[136:139], v178, s[54:55] sc1\n\t"
      "global_load_dwordx4 v[140:143], v179, s[54:55] sc1\n\t"
      "global_load_dwordx4 v[144:147], v180, s[54:55] sc1\n\t"
      "global_load_dwordx4 v[148:151], v181, s[54:55] sc1\n\t"
      "s_waitcnt vmcnt(6)\n\t"
      "v_max3_u32 v234, v156, v157, v158\n\t"
      "v_max_u32_e32 v234, v234, v159\n\t"
      "v_cmp_eq_u32_e32 vcc, -1, v234\n\t"
      "s_cbranch_vccnz FP7L_%=\n\t"
      "FP7X_%=:\n\t"
      "s_branch FP7B_%=\n\t"
      "NP1R_%=:\n\t"
      "s_mov_b32 s51, 0\n\t"
      "NP1L_%=:\n\t"
      "s_add_u32 s51, s51, 1\n\t"
      "s_cmp_lt_u32 s51, 262144\n\t"
      "s_cbranch_scc0 NP1X_%=\n\t"
      "s_waitcnt vmcnt(0)\n\t"
      "s_sleep 1\n\t"
      "global_load_dwordx4 v[128:131], v176, s[54:55] sc1\n\t"
      "global_load_dwordx4 v[132:135], v177, s[54:55] sc1\n\t"
      "global_load_dwordx4 v[136:139], v178, s[54:55] sc1\n\t"
      "global_load_dwordx4 v[140:143], v179, s[54:55] sc1\n\t"
      "global_load_dwordx4 v[144:147], v180, s[54:55] sc1\n\t"
      "global_load_dwordx4 v[148:151], v181, s[54:55] sc1\n\t"
      "global_load_dwordx4 v[152:155], v182, s[54:55] sc1\n\t"
      "global_load_dwordx4 v[156:159], v183, s[54:55] sc1\n\t"
      "s_waitcnt vmcnt(0)\n\t"
      "v_max3_u32 v234, v128, v129, v130\n\t"
      "v_max3_u32 v234, v234, v131, v132\n\t"
      "v_max3_u32 v234, v234, v133, v134\n\t"
      "v_max3_u32 v234, v234, v135, v136\n\t"
      "v_max3_u32 v234, v234, v137, v138\n\t"
      "v_max3_u32 v234, v234, v139, v140\n\t"
      "v_max3_u32 v234, v234, v141, v142\n\t"
      "v_max3_u32 v234, v234, v143, v144\n\t"
      "v_max3_u32 v234, v234, v145, v146\n\t"
      "v_max3_u32 v234, v234, v147, v148\n\t"
      "v_max3_u32 v234, v234, v149, v150\n\t"
      "v_max3_u32 v234, v234, v151, v152\n\t"
      "v_max3_u32 v234, v234, v153, v154\n\t"
      "v_max3_u32 v234, v234, v155, v156\n\t"
      "v_max3_u32 v234, v234, v157, v158\n\t"
      "v_max_u32_e32 v234, v234, v159\n\t"
      "v_cmp_eq_u32_e32 vcc, -1, v234\n\t"
      "s_cbranch_vccnz NP1L_%=\n\t"
      "NP1X_%=:\n\t"
      "v_mfma_f32_16x16x32_bf16 v[200:203], v[0:3], v[128:131], 0\n\t"
      "v_mfma_f32_16x16x32_bf16 v[204:207], v[4:7], v[128:131], 0\n\t"
      "v_mfma_f32_16x16x32_bf16 v[208:211], v[8:11], v[128:131], 0\n\t"
      "v_mfma_f32_16x16x32_bf16 v[212:215], v[12:15], v[128:131], 0\n\t"
      "v_mfma_f32_16x16x32_bf16 v[200:203], v[16:19], v[132:135], v[200:203]\n\t"
      "v_mfma_f32_16x16x32_bf16 v[204:207], v[20:23], v[132:135], v[204:207]\n\t"
      "v_mfma_f32_16x16x32_bf16 v[208:211], v[24:27], v[132:135], v[208:211]\n\t"
      "v_mfma_f32_16x16x32_bf16 v[212:215], v[28:31], v[132:135], v[212:215]\n\t"
      "v_mfma_f32_16x16x32_bf16 v[200:203], v[32:35], v[136:139], v[200:203]\n\t"
      "v_mfma_f32_16x16x32_bf16 v[204:207], v[36:39], v[136:139], v[204:207]\n\t"
      "v_mfma_f32_16x16x32_bf16 v[208:211], v[40:43], v[136:139], v[208:211]\n\t"
      "v_mfma_f32_16x16x32_bf16 v[212:215], v[44:47], v[136:139], v[212:215]\n\t"
      "v_mfma_f32_16x16x32_bf16 v[200:203], v[48:51], v[140:143], v[200:203]\n\t"
      "v_mfma_f32_16x16x32_bf16 v[204:207], v[52:55], v[140:143], v[204:207]\n\t"
      "v_mfma_f32_16x16x32_bf16 v[208:211], v[56:59], v[140:143], v[208:211]\n\t"
      "v_mfma_f32_16x16x32_bf16 v[212:215], v[60:63], v[140:143], v[212:215]\n\t"
      "v_mfma_f32_16x16x32_bf16 v[200:203], v[64:67], v[144:147], v[200:203]\n\t"
      "v_mfma_f32_16x16x32_bf16 v[204:207], v[68:71], v[144:147], v[204:207]\n\t"
      "v_mfma_f32_16x16x32_bf16 v[208:211], v[72:75], v[144:147], v[208:211]\n\t"
      "v_mfma_f32_16x16x32_bf16 v[212:215], v[76:79], v[144:147], v[212:215]\n\t"
      "v_mfma_f32_16x16x32_bf16 v[200:203], v[80:83], v[148:151], v[200:203]\n\t"
      "v_mfma_f32_16x16x32_bf16 v[204:207], v[84:87], v[148:151], v[204:207]\n\t"
      "v_mfma_f32_16x16x32_bf16 v[208:211], v[88:91], v[148:151], v[208:211]\n\t"
      "v_mfma_f32_16x16x32_bf16 v[212:215], v[92:95], v[148:151], v[212:215]\n\t"
      "v_mfma_f32_16x16x32_bf16 v[200:203], v[96:99], v[152:155], v[200:203]\n\t"
      "v_mfma_f32_16x16x32_bf16 v[204:207], v[100:103], v[152:155], v[204:207]\n\t"
      "v_mfma_f32_16x16x32_bf16 v[208:211], v[104:107], v[152:155], v[208:211]\n\t"
      "v_mfma_f32_16x16x32_bf16 v[200:203], v[112:115], v[156:159], v[200:203]\n\t"
      "v_mfma_f32_16x16x32_bf16 v[204:207], v[116:119], v[156:159], v[204:207]\n\t"
      "v_mfma_f32_16x16x32_bf16 v[208:211], v[120:123], v[156:159], v[208:211]\n\t"
      "global_load_dwordx4 v[128:131], v176, s[56:57] sc1\n\t"
      "global_load_dwordx4 v[132:135], v177, s[56:57] sc1\n\t"
      "global_load_dwordx4 v[136:139], v178, s[56:57] sc1\n\t"
      "global_load_dwordx4 v[140:143], v179, s[56:57] sc1\n\t"
      "global_load_dwordx4 v[144:147], v180, s[56:57] sc1\n\t"
      "global_load_dwordx4 v[148:151], v181, s[56:57] sc1\n\t"
      "s_waitcnt vmcnt(0)\n\t"
      "s_nop 7\n\t"
      "s_branch NP1B_%=\n\t"
      "NP2R_%=:\n\t"
      "s_mov_b32 s51, 0\n\t"
      "NP2L_%=:\n\t"
      "s_add_u32 s51, s51, 1\n\t"
      "s_cmp_lt_u32 s51, 262144\n\t"
      "s_cbranch_scc0 NP2X_%=\n\t"
      "s_waitcnt vmcnt(0)\n\t"
      "s_sleep 1\n\t"
      "global_load_dwordx4 v[128:131], v176, s[56:57] sc1\n\t"
      "global_load_dwordx4 v[132:135], v177, s[56:57] sc1\n\t"
      "global_load_dwordx4 v[136:139], v178, s[56:57] sc1\n\t"
      "global_load_dwordx4 v[140:143], v179, s[56:57] sc1\n\t"
      "global_load_dwordx4 v[144:147], v180, s[56:57] sc1\n\t"
      "global_load_dwordx4 v[148:151], v181, s[56:57] sc1\n\t"
      "global_load_dwordx4 v[152:155], v182, s[56:57] sc1\n\t"
      "global_load_dwordx4 v[156:159], v183, s[56:57] sc1\n\t"
      "s_waitcnt vmcnt(0)\n\t"
      "v_max3_u32 v234, v128, v129, v130\n\t"
      "v_max3_u32 v234, v234, v131, v132\n\t"
      "v_max3_u32 v234, v234, v133, v134\n\t"
      "v_max3_u32 v234, v234, v135, v136\n\t"
      "v_max3_u32 v234, v234, v137, v138\n\t"
      "v_max3_u32 v234, v234, v139, v140\n\t"
      "v_max3_u32 v234, v234, v141, v142\n\t"
      "v_max3_u32 v234, v234, v143, v144\n\t"
      "v_max3_u32 v234, v234, v145, v146\n\t"
      "v_max3_u32 v234, v234, v147, v148\n\t"
      "v_max3_u32 v234, v234, v149, v150\n\t"
      "v_max3_u32 v234, v234, v151, v152\n\t"
      "v_max3_u32 v234, v234, v153, v154\n\t"
      "v_max3_u32 v234, v234, v155, v156\n\t"
      "v_max3_u32 v234, v234, v157, v158\n\t"
      "v_max_u32_e32 v234, v234, v159\n\t"
      "v_cmp_eq_u32_e32 vcc, -1, v234\n\t"
      "s_cbranch_vccnz NP2L_%=\n\t"
      "NP2X_%=:\n\t"
      "v_mfma_f32_16x16x32_bf16 v[184:187], v[0:3], v[128:131], 0\n\t"
      "v_mfma_f32_16x16x32_bf16 v[188:191], v[4:7], v[128:131], 0\n\t"
      "v_mfma_f32_16x16x32_bf16 v[192:195], v[8:11], v[128:131], 0\n\t"
      "v_mfma_f32_16x16x32_bf16 v[196:199], v[12:15], v[128:131], 0\n\t"
      "v_mfma_f32_16x16x32_bf16 v[184:187], v[16:19], v[132:135], v[184:187]\n\t"
      "v_mfma_f32_16x16x32_bf16 v[188:191], v[20:23], v[132:135], v[188:191]\n\t"
      "v_mfma_f32_16x16x32_bf16 v[192:195], v[24:27], v[132:135], v[192:195]\n\t"
      "v_mfma_f32_16x16x32_bf16 v[196:199], v[28:31], v[132:135], v[196:199]\n\t"
      "v_mfma_f32_16x16x32_bf16 v[184:187], v[32:35], v[136:139], v[184:187]\n\t"
      "v_mfma_f32_16x16x32_bf16 v[188:191], v[36:39], v[136:139], v[188:191]\n\t"
      "v_mfma_f32_16x16x32_bf16 v[192:195], v[40:43], v[136:139], v[192:195]\n\t"
      "v_mfma_f32_16x16x32_bf16 v[196:199], v[44:47], v[136:139], v[196:199]\n\t"
      "v_mfma_f32_16x16x32_bf16 v[184:187], v[48:51], v[140:143], v[184:187]\n\t"
      "v_mfma_f32_16x16x32_bf16 v[188:191], v[52:55], v[140:143], v[188:191]\n\t"
      "v_mfma_f32_16x16x32_bf16 v[192:195], v[56:59], v[140:143], v[192:195]\n\t"
      "v_mfma_f32_16x16x32_bf16 v[196:199], v[60:63], v[140:143], v[196:199]\n\t"
      "v_mfma_f32_16x16x32_bf16 v[184:187], v[64:67], v[144:147], v[184:187]\n\t"
      "v_mfma_f32_16x16x32_bf16 v[188:191], v[68:71], v[144:147], v[188:191]\n\t"
      "v_mfma_f32_16x16x32_bf16 v[192:195], v[72:75], v[144:147], v[192:195]\n\t"
      "v_mfma_f32_16x16x32_bf16 v[196:199], v[76:79], v[144:147], v[196:199]\n\t"
      "v_mfma_f32_16x16x32_bf16 v[184:187], v[80:83], v[148:151], v[184:187]\n\t"
      "v_mfma_f32_16x16x32_bf16 v[188:191], v[84:87], v[148:151], v[188:191]\n\t"
      "v_mfma_f32_16x16x32_bf16 v[192:195], v[88:91], v[148:151], v[192:195]\n\t"
      "v_mfma_f32_16x16x32_bf16 v[196:199], v[92:95], v[148:151], v[196:199]\n\t"
      "v_mfma_f32_16x16x32_bf16 v[184:187], v[96:99], v[152:155], v[184:187]\n\t"
      "v_mfma_f32_16x16x32_bf16 v[188:191], v[100:103], v[152:155], v[188:191]\n\t"
      "v_mfma_f32_16x16x32_bf16 v[192:195], v[104:107], v[152:155], v[192:195]\n\t"
      "v_mfma_f32_16x16x32_bf16 v[184:187], v[112:115], v[156:159], v[184:187]\n\t"
      "v_mfma_f32_16x16x32_bf16 v[188:191], v[116:119], v[156:159], v[188:191]\n\t"
      "v_mfma_f32_16x16x32_bf16 v[192:195], v[120:123], v[156:159], v[192:195]\n\t"
      "global_load_dwordx4 v[128:131], v176, s[58:59] sc1\n\t"
      "global_load_dwordx4 v[132:135], v177, s[58:59] sc1\n\t"
      "global_load_dwordx4 v[136:139], v178, s[58:59] sc1\n\t"
      "global_load_dwordx4 v[140:143], v179, s[58:59] sc1\n\t"
      "global_load_dwordx4 v[144:147], v180, s[58:59] sc1\n\t"
      "global_load_dwordx4 v[148:151], v181, s[58:59] sc1\n\t"
      "s_waitcnt vmcnt(0)\n\t"
      "s_nop 7\n\t"
      "s_branch NP2B_%=\n\t"
      "NP3R_%=:\n\t"
      "s_mov_b32 s51, 0\n\t"
      "NP3L_%=:\n\t"
      "s_add_u32 s51, s51, 1\n\t"
      "s_cmp_lt_u32 s51, 262144\n\t"
      "s_cbranch_scc0 NP3X_%=\n\t"
      "s_waitcnt vmcnt(0)\n\t"
      "s_sleep 1\n\t"
      "global_load_dwordx4 v[128:131], v176, s[58:59] sc1\n\t"
      "global_load_dwordx4 v[132:135], v177, s[58:59] sc1\n\t"
      "global_load_dwordx4 v[136:139], v178, s[58:59] sc1\n\t"
      "global_load_dwordx4 v[140:143], v179, s[58:59] sc1\n\t"
      "global_load_dwordx4 v[144:147], v180, s[58:59] sc1\n\t"
      "global_load_dwordx4 v[148:151], v181, s[58:59] sc1\n\t"
      "global_load_dwordx4 v[152:155], v182, s[58:59] sc1\n\t"
      "global_load_dwordx4 v[156:159], v183, s[58:59] sc1\n\t"
      "s_waitcnt vmcnt(0)\n\t"
      "v_max3_u32 v234, v128, v129, v130\n\t"
      "v_max3_u32 v234, v234, v131, v132\n\t"
      "v_max3_u32 v234, v234, v133, v134\n\t"
      "v_max3_u32 v234, v234, v135, v136\n\t"
      "v_max3_u32 v234, v234, v137, v138\n\t"
      "v_max3_u32 v234, v234, v139, v140\n\t"
      "v_max3_u32 v234, v234, v141, v142\n\t"
      "v_max3_u32 v234, v234, v143, v144\n\t"
      "v_max3_u32 v234, v234, v145, v146\n\t"
      "v_max3_u32 v234, v234, v147, v148\n\t"
      "v_max3_u32 v234, v234, v149, v150\n\t"
      "v_max3_u32 v234, v234, v151, v152\n\t"
      "v_max3_u32 v234, v234, v153, v154\n\t"
      "v_max3_u32 v234, v234, v155, v156\n\t"
      "v_max3_u32 v234, v234, v157, v158\n\t"
      "v_max_u32_e32 v234, v234, v159\n\t"
      "v_cmp_eq_u32_e32 vcc, -1, v234\n\t"
      "s_cbranch_vccnz NP3L_%=\n\t"
      "NP3X_%=:\n\t"
      "v_mfma_f32_16x16x32_bf16 v[200:203], v[0:3], v[128:131], 0\n\t"
      "v_mfma_f32_16x16x32_bf16 v[204:207], v[4:7], v[128:131], 0\n\t"
      "v_mfma_f32_16x16x32_bf16 v[208:211], v[8:11], v[128:131], 0\n\t"
      "v_mfma_f32_16x16x32_bf16 v[212:215], v[12:15], v[128:131], 0\n\t"
      "v_mfma_f32_16x16x32_bf16 v[200:203], v[16:19], v[132:135], v[200:203]\n\t"
      "v_mfma_f32_16x16x32_bf16 v[204:207], v[20:23], v[132:135], v[204:207]\n\t"
      "v_mfma_f32_16x16x32_bf16 v[208:211], v[24:27], v[132:135], v[208:211]\n\t"
      "v_mfma_f32_16x16x32_bf16 v[212:215], v[28:31], v[132:135], v[212:215]\n\t"
      "v_mfma_f32_16x16x32_bf16 v[200:203], v[32:35], v[136:139], v[200:203]\n\t"
      "v_mfma_f32_16x16x32_bf16 v[204:207], v[36:39], v[136:139], v[204:207]\n\t"
      "v_mfma_f32_16x16x32_bf16 v[208:211], v[40:43], v[136:139], v[208:211]\n\t"
      "v_mfma_f32_16x16x32_bf16 v[212:215], v[44:47], v[136:139], v[212:215]\n\t"
      "v_mfma_f32_16x16x32_bf16 v[200:203], v[48:51], v[140:143], v[200:203]\n\t"
      "v_mfma_f32_16x16x32_bf16 v[204:207], v[52:55], v[140:143], v[204:207]\n\t"
      "v_mfma_f32_16x16x32_bf16 v[208:211], v[56:59], v[140:143], v[208:211]\n\t"
      "v_mfma_f32_16x16x32_bf16 v[212:215], v[60:63], v[140:143], v[212:215]\n\t"
      "v_mfma_f32_16x16x32_bf16 v[200:203], v[64:67], v[144:147], v[200:203]\n\t"
      "v_mfma_f32_16x16x32_bf16 v[204:207], v[68:71], v[144:147], v[204:207]\n\t"
      "v_mfma_f32_16x16x32_bf16 v[208:211], v[72:75], v[144:147], v[208:211]\n\t"
      "v_mfma_f32_16x16x32_bf16 v[212:215], v[76:79], v[144:147], v[212:215]\n\t"
      "v_mfma_f32_16x16x32_bf16 v[200:203], v[80:83], v[148:151], v[200:203]\n\t"
      "v_mfma_f32_16x16x32_bf16 v[204:207], v[84:87], v[148:151], v[204:207]\n\t"
      "v_mfma_f32_16x16x32_bf16 v[208:211], v[88:91], v[148:151], v[208:211]\n\t"
      "v_mfma_f32_16x16x32_bf16 v[212:215], v[92:95], v[148:151], v[212:215]\n\t"
      "v_mfma_f32_16x16x32_bf16 v[200:203], v[96:99], v[152:155], v[200:203]\n\t"
      "v_mfma_f32_16x16x32_bf16 v[204:207], v[100:103], v[152:155], v[204:207]\n\t"
      "v_mfma_f32_16x16x32_bf16 v[208:211], v[104:107], v[152:155], v[208:211]\n\t"
      "v_mfma_f32_16x16x32_bf16 v[200:203], v[112:115], v[156:159], v[200:203]\n\t"
      "v_mfma_f32_16x16x32_bf16 v[204:207], v[116:119], v[156:159], v[204:207]\n\t"
      "v_mfma_f32_16x16x32_bf16 v[208:211], v[120:123], v[156:159], v[208:211]\n\t"
      "global_load_dwordx4 v[128:131], v176, s[60:61] sc1\n\t"
      "global_load_dwordx4 v[132:135], v177, s[60:61] sc1\n\t"
      "global_load_dwordx4 v[136:139], v178, s[60:61] sc1\n\t"
      "global_load_dwordx4 v[140:143], v179, s[60:61] sc1\n\t"
      "global_load_dwordx4 v[144:147], v180, s[60:61] sc1\n\t"
      "global_load_dwordx4 v[148:151], v181, s[60:61] sc1\n\t"
      "s_waitcnt vmcnt(0)\n\t"
      "s_nop 7\n\t"
      "s_branch NP3B_%=\n\t"
      "NP4R_%=:\n\t"
      "s_mov_b32 s51, 0\n\t"
      "NP4L_%=:\n\t"
      "s_add_u32 s51, s51, 1\n\t"
      "s_cmp_lt_u32 s51, 262144\n\t"
      "s_cbranch_scc0 NP4X_%=\n\t"
      "s_waitcnt vmcnt(0)\n\t"
      "s_sleep 1\n\t"
      "global_load_dwordx4 v[128:131], v176, s[60:61] sc1\n\t"
      "global_load_dwordx4 v[132:135], v177, s[60:61] sc1\n\t"
      "global_load_dwordx4 v[136:139], v178, s[60:61] sc1\n\t"
      "global_load_dwordx4 v[140:143], v179, s[60:61] sc1\n\t"
      "global_load_dwordx4 v[144:147], v180, s[60:61] sc1\n\t"
      "global_load_dwordx4 v[148:151], v181, s[60:61] sc1\n\t"
      "global_load_dwordx4 v[152:155], v182, s[60:61] sc1\n\t"
      "global_load_dwordx4 v[156:159], v183, s[60:61] sc1\n\t"
      "s_waitcnt vmcnt(0)\n\t"
      "v_max3_u32 v234, v128, v129, v130\n\t"
      "v_max3_u32 v234, v234, v131, v132\n\t"
      "v_max3_u32 v234, v234, v133, v134\n\t"
      "v_max3_u32 v234, v234, v135, v136\n\t"
      "v_max3_u32 v234, v234, v137, v138\n\t"
      "v_max3_u32 v234, v234, v139, v140\n\t"
      "v_max3_u32 v234, v234, v141, v142\n\t"
      "v_max3_u32 v234, v234, v143, v144\n\t"
      "v_max3_u32 v234, v234, v145, v146\n\t"
      "v_max3_u32 v234, v234, v147, v148\n\t"
      "v_max3_u32 v234, v234, v149, v150\n\t"
      "v_max3_u32 v234, v234, v151, v152\n\t"
      "v_max3_u32 v234, v234, v153, v154\n\t"
      "v_max3_u32 v234, v234, v155, v156\n\t"
      "v_max3_u32 v234, v234, v157, v158\n\t"
      "v_max_u32_e32 v234, v234, v159\n\t"
      "v_cmp_eq_u32_e32 vcc, -1, v234\n\t"
      "s_cbranch_vccnz NP4L_%=\n\t"
      "NP4X_%=:\n\t"
      "v_mfma_f32_16x16x32_bf16 v[184:187], v[0:3], v[128:131], 0\n\t"
      "v_mfma_f32_16x16x32_bf16 v[188:191], v[4:7], v[128:131], 0\n\t"
      "v_mfma_f32_16x16x32_bf16 v[192:195], v[8:11], v[128:131], 0\n\t"
      "v_mfma_f32_16x16x32_bf16 v[196:199], v[12:15], v[128:131], 0\n\t"
      "v_mfma_f32_16x16x32_bf16 v[184:187], v[16:19], v[132:135], v[184:187]\n\t"
      "v_mfma_f32_16x16x32_bf16 v[188:191], v[20:23], v[132:135], v[188:191]\n\t"
      "v_mfma_f32_16x16x32_bf16 v[192:195], v[24:27], v[132:135], v[192:195]\n\t"
      "v_mfma_f32_16x16x32_bf16 v[196:199], v[28:31], v[132:135], v[196:199]\n\t"
      "v_mfma_f32_16x16x32_bf16 v[184:187], v[32:35], v[136:139], v[184:187]\n\t"
      "v_mfma_f32_16x16x32_bf16 v[188:191], v[36:39], v[136:139], v[188:191]\n\t"
      "v_mfma_f32_16x16x32_bf16 v[192:195], v[40:43], v[136:139], v[192:195]\n\t"
      "v_mfma_f32_16x16x32_bf16 v[196:199], v[44:47], v[136:139], v[196:199]\n\t"
      "v_mfma_f32_16x16x32_bf16 v[184:187], v[48:51], v[140:143], v[184:187]\n\t"
      "v_mfma_f32_16x16x32_bf16 v[188:191], v[52:55], v[140:143], v[188:191]\n\t"
      "v_mfma_f32_16x16x32_bf16 v[192:195], v[56:59], v[140:143], v[192:195]\n\t"
      "v_mfma_f32_16x16x32_bf16 v[196:199], v[60:63], v[140:143], v[196:199]\n\t"
      "v_mfma_f32_16x16x32_bf16 v[184:187], v[64:67], v[144:147], v[184:187]\n\t"
      "v_mfma_f32_16x16x32_bf16 v[188:191], v[68:71], v[144:147], v[188:191]\n\t"
      "v_mfma_f32_16x16x32_bf16 v[192:195], v[72:75], v[144:147], v[192:195]\n\t"
      "v_mfma_f32_16x16x32_bf16 v[196:199], v[76:79], v[144:147], v[196:199]\n\t"
      "v_mfma_f32_16x16x32_bf16 v[184:187], v[80:83], v[148:151], v[184:187]\n\t"
      "v_mfma_f32_16x16x32_bf16 v[188:191], v[84:87], v[148:151], v[188:191]\n\t"
      "v_mfma_f32_16x16x32_bf16 v[192:195], v[88:91], v[148:151], v[192:195]\n\t"
      "v_mfma_f32_16x16x32_bf16 v[196:199], v[92:95], v[148:151], v[196:199]\n\t"
      "v_mfma_f32_16x16x32_bf16 v[184:187], v[96:99], v[152:155], v[184:187]\n\t"
      "v_mfma_f32_16x16x32_bf16 v[188:191], v[100:103], v[152:155], v[188:191]\n\t"
      "v_mfma_f32_16x16x32_bf16 v[192:195], v[104:107], v[152:155], v[192:195]\n\t"
      "v_mfma_f32_16x16x32_bf16 v[184:187], v[112:115], v[156:159], v[184:187]\n\t"
      "v_mfma_f32_16x16x32_bf16 v[188:191], v[116:119], v[156:159], v[188:191]\n\t"
      "v_mfma_f32_16x16x32_bf16 v[192:195], v[120:123], v[156:159], v[192:195]\n\t"
      "global_load_dwordx4 v[128:131], v176, s[62:63] sc1\n\t"
      "global_load_dwordx4 v[132:135], v177, s[62:63] sc1\n\t"
      "global_load_dwordx4 v[136:139], v178, s[62:63] sc1\n\t"
      "global_load_dwordx4 v[140:143], v179, s[62:63] sc1\n\t"
      "global_load_dwordx4 v[144:147], v180, s[62:63] sc1\n\t"
      "global_load_dwordx4 v[148:151], v181, s[62:63] sc1\n\t"
      "s_waitcnt vmcnt(0)\n\t"
      "s_nop 7\n\t"
      "s_branch NP4B_%=\n\t"
      "NP5R_%=:\n\t"
      "s_mov_b32 s51, 0\n\t"
      "NP5L_%=:\n\t"
      "s_add_u32 s51, s51, 1\n\t"
      "s_cmp_lt_u32 s51, 262144\n\t"
      "s_cbranch_scc0 NP5X_%=\n\t"
      "s_waitcnt vmcnt(0)\n\t"
      "s_sleep 1\n\t"
      "global_load_dwordx4 v[128:131], v176, s[62:63] sc1\n\t"
      "global_load_dwordx4 v[132:135], v177, s[62:63] sc1\n\t"
      "global_load_dwordx4 v[136:139], v178, s[62:63] sc1\n\t"
      "global_load_dwordx4 v[140:143], v179, s[62:63] sc1\n\t"
      "global_load_dwordx4 v[144:147], v180, s[62:63] sc1\n\t"
      "global_load_dwordx4 v[148:151], v181, s[62:63] sc1\n\t"
      "global_load_dwordx4 v[152:155], v182, s[62:63] sc1\n\t"
      "global_load_dwordx4 v[156:159], v183, s[62:63] sc1\n\t"
      "s_waitcnt vmcnt(0)\n\t"
      "v_max3_u32 v234, v128, v129, v130\n\t"
      "v_max3_u32 v234, v234, v131, v132\n\t"
      "v_max3_u32 v234, v234, v133, v134\n\t"
      "v_max3_u32 v234, v234, v135, v136\n\t"
      "v_max3_u32 v234, v234, v137, v138\n\t"
      "v_max3_u32 v234, v234, v139, v140\n\t"
      "v_max3_u32 v234, v234, v141, v142\n\t"
      "v_max3_u32 v234, v234, v143, v144\n\t"
      "v_max3_u32 v234, v234, v145, v146\n\t"
      "v_max3_u32 v234, v234, v147, v148\n\t"
      "v_max3_u32 v234, v234, v149, v150\n\t"
      "v_max3_u32 v234, v234, v151, v152\n\t"
      "v_max3_u32 v234, v234, v153, v154\n\t"
      "v_max3_u32 v234, v234, v155, v156\n\t"
      "v_max3_u32 v234, v234, v157, v158\n\t"
      "v_max_u32_e32 v234, v234, v159\n\t"
      "v_cmp_eq_u32_e32 vcc, -1, v234\n\t"
      "s_cbranch_vccnz NP5L_%=\n\t"
      "NP5X_%=:\n\t"
      "v_mfma_f32_16x16x32_bf16 v[200:203], v[0:3], v[128:131], 0\n\t"
      "v_mfma_f32_16x16x32_bf16 v[204:207], v[4:7], v[128:131], 0\n\t"
      "v_mfma_f32_16x16x32_bf16 v[208:211], v[8:11], v[128:131], 0\n\t"
      "v_mfma_f32_16x16x32_bf16 v[212:215], v[12:15], v[128:131], 0\n\t"
      "v_mfma_f32_16x16x32_bf16 v[200:203], v[16:19], v[132:135], v[200:203]\n\t"
      "v_mfma_f32_16x16x32_bf16 v[204:207], v[20:23], v[132:135], v[204:207]\n\t"
      "v_mfma_f32_16x16x32_bf16 v[208:211], v[24:27], v[132:135], v[208:211]\n\t"
      "v_mfma_f32_16x16x32_bf16 v[212:215], v[28:31], v[132:135], v[212:215]\n\t"
      "v_mfma_f32_16x16x32_bf16 v[200:203], v[32:35], v[136:139], v[200:203]\n\t"
      "v_mfma_f32_16x16x32_bf16 v[204:207], v[36:39], v[136:139], v[204:207]\n\t"
      "v_mfma_f32_16x16x32_bf16 v[208:211], v[40:43], v[136:139], v[208:211]\n\t"
      "v_mfma_f32_16x16x32_bf16 v[212:215], v[44:47], v[136:139], v[212:215]\n\t"
      "v_mfma_f32_16x16x32_bf16 v[200:203], v[48:51], v[140:143], v[200:203]\n\t"
      "v_mfma_f32_16x16x32_bf16 v[204:207], v[52:55], v[140:143], v[204:207]\n\t"
      "v_mfma_f32_16x16x32_bf16 v[208:211], v[56:59], v[140:143], v[208:211]\n\t"
      "v_mfma_f32_16x16x32_bf16 v[212:215], v[60:63], v[140:143], v[212:215]\n\t"
      "v_mfma_f32_16x16x32_bf16 v[200:203], v[64:67], v[144:147], v[200:203]\n\t"
      "v_mfma_f32_16x16x32_bf16 v[204:207], v[68:71], v[144:147], v[204:207]\n\t"
      "v_mfma_f32_16x16x32_bf16 v[208:211], v[72:75], v[144:147], v[208:211]\n\t"
      "v_mfma_f32_16x16x32_bf16 v[212:215], v[76:79], v[144:147], v[212:215]\n\t"
      "v_mfma_f32_16x16x32_bf16 v[200:203], v[80:83], v[148:151], v[200:203]\n\t"
      "v_mfma_f32_16x16x32_bf16 v[204:207], v[84:87], v[148:151], v[204:207]\n\t"
      "v_mfma_f32_16x16x32_bf16 v[208:211], v[88:91], v[148:151], v[208:211]\n\t"
      "v_mfma_f32_16x16x32_bf16 v[212:215], v[92:95], v[148:151], v[212:215]\n\t"
      "v_mfma_f32_16x16x32_bf16 v[200:203], v[96:99], v[152:155], v[200:203]\n\t"
      "v_mfma_f32_16x16x32_bf16 v[204:207], v[100:103], v[152:155], v[204:207]\n\t"
      "v_mfma_f32_16x16x32_bf16 v[208:211], v[104:107], v[152:155], v[208:211]\n\t"
      "v_mfma_f32_16x16x32_bf16 v[200:203], v[112:115], v[156:159], v[200:203]\n\t"
      "v_mfma_f32_16x16x32_bf16 v[204:207], v[116:119], v[156:159], v[204:207]\n\t"
      "v_mfma_f32_16x16x32_bf16 v[208:211], v[120:123], v[156:159], v[208:211]\n\t"
      "global_load_dwordx4 v[128:131], v176, s[64:65] sc1\n\t"
      "global_load_dwordx4 v[132:135], v177, s[64:65] sc1\n\t"
      "global_load_dwordx4 v[136:139], v178, s[64:65] sc1\n\t"
      "global_load_dwordx4 v[140:143], v179, s[64:65] sc1\n\t"
      "global_load_dwordx4 v[144:147], v180, s[64:65] sc1\n\t"
      "global_load_dwordx4 v[148:151], v181, s[64:65] sc1\n\t"
      "s_waitcnt vmcnt(0)\n\t"
      "s_nop 7\n\t"
      "s_branch NP5B_%=\n\t"
      "NP6R_%=:\n\t"
      "s_mov_b32 s51, 0\n\t"
      "NP6L_%=:\n\t"
      "s_add_u32 s51, s51, 1\n\t"
      "s_cmp_lt_u32 s51, 262144\n\t"
      "s_cbranch_scc0 NP6X_%=\n\t"
      "s_waitcnt vmcnt(0)\n\t"
      "s_sleep 1\n\t"
      "global_load_dwordx4 v[128:131], v176, s[64:65] sc1\n\t"
      "global_load_dwordx4 v[132:135], v177, s[64:65] sc1\n\t"
      "global_load_dwordx4 v[136:139], v178, s[64:65] sc1\n\t"
      "global_load_dwordx4 v[140:143], v179, s[64:65] sc1\n\t"
      "global_load_dwordx4 v[144:147], v180, s[64:65] sc1\n\t"
      "global_load_dwordx4 v[148:151], v181, s[64:65] sc1\n\t"
      "global_load_dwordx4 v[152:155], v182, s[64:65] sc1\n\t"
      "global_load_dwordx4 v[156:159], v183, s[64:65] sc1\n\t"
      "s_waitcnt vmcnt(0)\n\t"
      "v_max3_u32 v234, v128, v129, v130\n\t"
      "v_max3_u32 v234, v234, v131, v132\n\t"
      "v_max3_u32 v234, v234, v133, v134\n\t"
      "v_max3_u32 v234, v234, v135, v136\n\t"
      "v_max3_u32 v234, v234, v137, v138\n\t"
      "v_max3_u32 v234, v234, v139, v140\n\t"
      "v_max3_u32 v234, v234, v141, v142\n\t"
      "v_max3_u32 v234, v234, v143, v144\n\t"
      "v_max3_u32 v234, v234, v145, v146\n\t"
      "v_max3_u32 v234, v234, v147, v148\n\t"
      "v_max3_u32 v234, v234, v149, v150\n\t"
      "v_max3_u32 v234, v234, v151, v152\n\t"
      "v_max3_u32 v234, v234, v153, v154\n\t"
      "v_max3_u32 v234, v234, v155, v156\n\t"
      "v_max3_u32 v234, v234, v157, v158\n\t"
      "v_max_u32_e32 v234, v234, v159\n\t"
      "v_cmp_eq_u32_e32 vcc, -1, v234\n\t"
      "s_cbranch_vccnz NP6L_%=\n\t"
      "NP6X_%=:\n\t"
      "v_mfma_f32_16x16x32_bf16 v[184:187], v[0:3], v[128:131], 0\n\t"
      "v_mfma_f32_16x16x32_bf16 v[188:191], v[4:7], v[128:131], 0\n\t"
      "v_mfma_f32_16x16x32_bf16 v[192:195], v[8:11], v[128:131], 0\n\t"
      "v_mfma_f32_16x16x32_bf16 v[196:199], v[12:15], v[128:131], 0\n\t"
      "v_mfma_f32_16x16x32_bf16 v[184:187], v[16:19], v[132:135], v[184:187]\n\t"
      "v_mfma_f32_16x16x32_bf16 v[188:191], v[20:23], v[132:135], v[188:191]\n\t"
      "v_mfma_f32_16x16x32_bf16 v[192:195], v[24:27], v[132:135], v[192:195]\n\t"
      "v_mfma_f32_16x16x32_bf16 v[196:199], v[28:31], v[132:135], v[196:199]\n\t"
      "v_mfma_f32_16x16x32_bf16 v[184:187], v[32:35], v[136:139], v[184:187]\n\t"
      "v_mfma_f32_16x16x32_bf16 v[188:191], v[36:39], v[136:139], v[188:191]\n\t"
      "v_mfma_f32_16x16x32_bf16 v[192:195], v[40:43], v[136:139], v[192:195]\n\t"
      "v_mfma_f32_16x16x32_bf16 v[196:199], v[44:47], v[136:139], v[196:199]\n\t"
      "v_mfma_f32_16x16x32_bf16 v[184:187], v[48:51], v[140:143], v[184:187]\n\t"
      "v_mfma_f32_16x16x32_bf16 v[188:191], v[52:55], v[140:143], v[188:191]\n\t"
      "v_mfma_f32_16x16x32_bf16 v[192:195], v[56:59], v[140:143], v[192:195]\n\t"
      "v_mfma_f32_16x16x32_bf16 v[196:199], v[60:63], v[140:143], v[196:199]\n\t"
      "v_mfma_f32_16x16x32_bf16 v[184:187], v[64:67], v[144:147], v[184:187]\n\t"
      "v_mfma_f32_16x16x32_bf16 v[188:191], v[68:71], v[144:147], v[188:191]\n\t"
      "v_mfma_f32_16x16x32_bf16 v[192:195], v[72:75], v[144:147], v[192:195]\n\t"
      "v_mfma_f32_16x16x32_bf16 v[196:199], v[76:79], v[144:147], v[196:199]\n\t"
      "v_mfma_f32_16x16x32_bf16 v[184:187], v[80:83], v[148:151], v[184:187]\n\t"
      "v_mfma_f32_16x16x32_bf16 v[188:191], v[84:87], v[148:151], v[188:191]\n\t"
      "v_mfma_f32_16x16x32_bf16 v[192:195], v[88:91], v[148:151], v[192:195]\n\t"
      "v_mfma_f32_16x16x32_bf16 v[196:199], v[92:95], v[148:151], v[196:199]\n\t"
      "v_mfma_f32_16x16x32_bf16 v[184:187], v[96:99], v[152:155], v[184:187]\n\t"
      "v_mfma_f32_16x16x32_bf16 v[188:191], v[100:103], v[152:155], v[188:191]\n\t"
      "v_mfma_f32_16x16x32_bf16 v[192:195], v[104:107], v[152:155], v[192:195]\n\t"
      "v_mfma_f32_16x16x32_bf16 v[184:187], v[112:115], v[156:159], v[184:187]\n\t"
      "v_mfma_f32_16x16x32_bf16 v[188:191], v[116:119], v[156:159], v[188:191]\n\t"
      "v_mfma_f32_16x16x32_bf16 v[192:195], v[120:123], v[156:159], v[192:195]\n\t"
      "s_waitcnt vmcnt(0)\n\t"
      "s_nop 7\n\t"
      "s_branch NP6B_%=\n\t"
      : "+{s[4:19]}"(s), "+{s[20:21]}"(kr), "+{s22}"(hint), "+{v[240:247]}"(v)
      :
      : "memory", "vcc", "scc",
        "v0", "v1", "v2", "v3", "v4", "v5", "v6", "v7", "v8", "v9", "v10", "v11", "v12", "v13", "v14", "v15", "v16",
        "v17", "v18", "v19", "v20", "v21", "v22", "v23", "v24", "v25", "v26", "v27", "v28", "v29", "v30", "v31", "v32",
        "v33", "v34", "v35", "v36", "v37", "v38", "v39", "v40", "v41", "v42", "v43", "v44", "v45", "v46", "v47", "v48",
        "v49", "v50", "v51", "v52", "v53", "v54", "v55", "v56", "v57", "v58", "v59", "v60", "v61", "v62", "v63", "v64",
        "v65", "v66", "v67", "v68", "v69", "v70", "v71", "v72", "v73", "v74", "v75", "v76", "v77", "v78", "v79", "v80",
        "v81", "v82", "v83", "v84", "v85", "v86", "v87", "v88", "v89", "v90", "v91", "v92", "v93", "v94", "v95", "v96",
        "v97", "v98", "v99", "v100", "v101", "v102", "v103", "v104", "v105", "v106", "v107", "v108", "v109", "v110",
        "v111", "v112", "v113", "v114", "v115", "v116", "v117", "v118", "v119", "v120", "v121", "v122", "v123", "v124",
        "v125", "v126", "v127", "v128", "v129", "v130", "v131", "v132", "v133", "v134", "v135", "v136", "v137", "v138",
        "v139", "v140", "v141", "v142", "v143", "v144", "v145", "v146", "v147", "v148", "v149", "v150", "v151", "v152",
        "v153", "v154", "v155", "v156", "v157", "v158", "v159", "v160", "v161", "v162", "v163", "v164", "v165", "v166",
        "v167", "v168", "v169", "v170", "v171", "v172", "v173", "v174", "v175", "v176", "v177", "v178", "v179", "v180",
        "v181", "v182", "v183", "v184", "v185", "v186", "v187", "v188", "v189", "v190", "v191", "v192", "v193", "v194",
        "v195", "v196", "v197", "v198", "v199", "v200", "v201", "v202", "v203", "v204", "v205", "v206", "v207", "v208",
        "v209", "v210", "v211", "v212", "v213", "v214", "v215", "v216", "v217", "v218", "v219", "v220", "v221", "v222",
        "v223", "v224", "v225", "v226", "v227", "v228", "v229", "v230", "v231", "v232", "v233", "v234", "v235", "v236",
        "v237", "v238", "v239", "v248", "v249", "v250", "v251", "v252", "v253", "s44", "s45", "s46", "s47", "s48",
        "s49", "s50", "s51", "s52", "s53", "s54", "s55", "s56", "s57", "s58", "s59", "s60", "s61", "s62", "s63", "s64",
        "s65", "s66", "s67", "s68", "s69", "s70"
    );
    __builtin_amdgcn_endpgm();
  } else {
#define B_INDICES                                                                                            \
  const int ib = idx - 16, t = 16 * (4 * side + ((xp + XO) & 3)) + ib;                         \
  const int st = 8 * xp + (ib >> 1), rot = 4 * (ib & 1) + 2 * side - (ib & side & 1);                        \
  const int cb0 = 4 * wave - max(wave - 4, 0);               \
  const int k1 = (ib + 2 * xcd) & 15, k0 = k1 - (k1 >= 14 ? 7 : 0)
    u32x16 acc;
    u32x8 v1, s1;
    u32x4 s3;
    unsigned bits = (wave >> 2) | (wave == 0 ? 16 : 0), id = blockIdx.x, wv = wave;
    {
      LANE_BITS(threadIdx.x & 63);
      B_INDICES;
      v1[0] = (8 * (wave & 3) + lr) * 896 + (wave >> 2) * 128 + ((lc ^ swz_tr(8 * wave + lr)) << 4) + 4096;
      v1[1] = B_X + (4 * g + q4) * 896 + cb0 * 32 + p4 * 8;
      v1[2] = B_PS + (4 * g + q4) * 64 + wave * 8;
      v1[3] = B_PS + (4 * g + p4) * 64 + q4 * 16;
      v1[4] = (t * 16 + 4 * g + p4) * 16;
      v1[5] = lr * 4096 + ((lc ^ swz_tr(lr)) << 4);
      v1[6] = lane * 16;
      v1[7] = WS_HN + (7 * (wave & 1) + 2 * (wave >> 2) + (g & 1)) * 131072 + (t >> 4) * 16384 + (wave & 2) * 4096 + (t & 15) * 512 + (g >> 1) * 256 + c * 16;
      put(s1, 0, (u64)(wo + k0 * 32 * 896 - 4096));
      put(s1, 2, (u64)(ws + WS_ST));
      put(s1, 4, 0);
      put(s1, 6, (u64)(w2 + st * 128 + (wave * 224 + rot * 32) * 4096));
      s3[0] = (wave & 3) * B_UNIT + (wave >> 2) * 1024;
      s3[1] = (wave >> 2) - 1;
      s3[2] = 13 - k0;
      s3[3] = wave * 4096;
    asm volatile(
      "s_waitcnt lgkmcnt(0)\n\ts_nop 4\n\t"
      ".p2align 3\n\t"
      "s_load_dwordx2 s[40:41], s[6:7], 0x20000\n\t"
      "v_mov_b32_e32 v248, -1\n\t"
      "v_mov_b32_e32 v249, -1\n\t"
      "v_mov_b32_e32 v250, -1\n\t"
      "v_mov_b32_e32 v251, -1\n\t"
      "v_add_u32_e32 v221, 4294965504, v224\n\t"
      "v_add_u32_e32 v222, 512, v224\n\t"
      "v_add_u32_e32 v223, 4294966016, v224\n\t"
      "s_bitcmp1_b32 s16, 0\n\t"
      "s_cselect_b64 s[46:47], -1, 0\n\t"
      "s_mov_b64 vcc, s[46:47]\n\t"
      "s_waitcnt vmcnt(3)\n\t"
      "s_barrier\n\t"
      "ds_read_b64_tr_b16 v[204:205], v225\n\t"
      "ds_read_b64_tr_b16 v[206:207], v225 offset:32\n\t"
      "ds_read_b64_tr_b16 v[208:209], v225 offset:64\n\t"
      "ds_read_b64_tr_b16 v[210:211], v225 offset:96\n\t"
      "s_waitcnt lgkmcnt(0)\n\t"
      "s_add_u32 s40, s40, 1\n\t"
      "s_addc_u32 s41, s41, 0\n\t"
      "s_mul_i32 s48, s40, 0x9e3779b9\n\t"
      "s_mul_hi_u32 s9, s40, 0x7f4a7c15\n\t"
      "s_add_u32 s9, s9, s48\n\t"
      "s_mul_i32 s48, s41, 0x7f4a7c15\n\t"
      "s_add_u32 s9, s9, s48\n\t"
      "s_mul_i32 s8, s40, 0x7f4a7c15\n\t"
      "s_add_u32 s8, s8, 0xd192ed03\n\t"
      "s_addc_u32 s9, s9, 0xd1b54a32\n\t"
      "v_mov_b32_e32 v188, s8\n\t"
      "v_mov_b32_e32 v189, s9\n\t"
      "v_mov_b32_e32 v190, s40\n\t"
      "v_mov_b32_e32 v191, s41\n\t"
      "v_mov_b32_e32 v192, 163600\n\t"
      "s_mov_b64 exec, 1\n\t"
      "ds_write_b128 v192, v[188:191]\n\t"
      "s_mov_b64 exec, -1\n\t"
      "v_and_b32_e32 v210, s13, v210\n\t"
      "v_and_b32_e32 v211, s13, v211\n\t"
      "v_lshlrev_b32_e32 v160, 16, v204\n\t"
      "v_and_b32_e32 v161, 0xffff0000, v204\n\t"
      "v_lshlrev_b32_e32 v162, 16, v205\n\t"
      "v_and_b32_e32 v163, 0xffff0000, v205\n\t"
      "v_lshlrev_b32_e32 v164, 16, v206\n\t"
      "v_and_b32_e32 v165, 0xffff0000, v206\n\t"
      "v_lshlrev_b32_e32 v166, 16, v207\n\t"
      "v_and_b32_e32 v167, 0xffff0000, v207\n\t"
      "v_lshlrev_b32_e32 v168, 16, v208\n\t"
      "v_and_b32_e32 v169, 0xffff0000, v208\n\t"
      "v_lshlrev_b32_e32 v170, 16, v209\n\t"
      "v_and_b32_e32 v171, 0xffff0000, v209\n\t"
      "v_lshlrev_b32_e32 v172, 16, v210\n\t"
      "v_and_b32_e32 v173, 0xffff0000, v210\n\t"
      "v_lshlrev_b32_e32 v174, 16, v211\n\t"
      "v_and_b32_e32 v175, 0xffff0000, v211\n\t"
      "v_pk_add_f32 v[188:189], v[160:161], v[164:165]\n\t"
      "v_pk_mul_f32 v[192:193], v[160:161], v[160:161]\n\t"
      "v_pk_add_f32 v[190:191], v[162:163], v[166:167]\n\t"
      "v_pk_mul_f32 v[194:195], v[162:163], v[162:163]\n\t"
      "v_pk_fma_f32 v[192:193], v[164:165], v[164:165], v[192:193]\n\t"
      "v_pk_fma_f32 v[194:195], v[166:167], v[166:167], v[194:195]\n\t"
      "v_pk_add_f32 v[188:189], v[188:189], v[168:169]\n\t"
      "v_pk_fma_f32 v[192:193], v[168:169], v[168:169], v[192:193]\n\t"
      "v_pk_add_f32 v[190:191], v[190:191], v[170:171]\n\t"
      "v_pk_fma_f32 v[194:195], v[170:171], v[170:171], v[194:195]\n\t"
      "s_cbranch_vccnz SB0_%=\n\t"
      "s_nop 0\n\t"
      "v_pk_add_f32 v[188:189], v[188:189], v[172:173]\n\t"
      "v_pk_fma_f32 v[192:193], v[172:173], v[172:173], v[192:193]\n\t"
      "v_pk_add_f32 v[190:191], v[190:191], v[174:175]\n\t"
      "v_pk_fma_f32 v[194:195], v[174:175], v[174:175], v[194:195]\n\t"
      "SB0_%=:\n\t"
      "v_add_f32_dpp v196, v188, v188 row_ror:8 row_mask:0xf bank_mask:0x3\n\t"
      "v_add_f32_dpp v197, v189, v189 row_ror:8 row_mask:0xf bank_mask:0x3\n\t"
      "v_add_f32_dpp v198, v192, v192 row_ror:8 row_mask:0xf bank_mask:0x3\n\t"
      "v_add_f32_dpp v199, v193, v193 row_ror:8 row_mask:0xf bank_mask:0x3\n\t"
      "v_add_f32_dpp v196, v190, v190 row_ror:8 row_mask:0xf bank_mask:0xc\n\t"
      "v_add_f32_dpp v197, v191, v191 row_ror:8 row_mask:0xf bank_mask:0xc\n\t"
      "v_add_f32_dpp v198, v194, v194 row_ror:8 row_mask:0xf bank_mask:0xc\n\t"
      "v_add_f32_dpp v199, v195, v195 row_ror:8 row_mask:0xf bank_mask:0xc\n\t"
      "v_add_f32_dpp v188, v196, v196 row_shl:4 row_mask:0xf bank_mask:0x5\n\t"
      "v_add_f32_dpp v189, v198, v198 row_shl:4 row_mask:0xf bank_mask:0x5\n\t"
      "s_nop 0\n\t"
      "v_add_f32_dpp v188, v197, v197 row_shr:4 row_mask:0xf bank_mask:0xa\n\t"
      "v_add_f32_dpp v189, v199, v199 row_shr:4 row_mask:0xf bank_mask:0xa\n\t"
      "s_nop 0\n\t"
      "v_add_f32_dpp v188, v188, v188 quad_perm:[1,0,3,2] row_mask:0xf bank_mask:0xf\n\t"
      "v_add_f32_dpp v189, v189, v189 quad_perm:[1,0,3,2] row_mask:0xf bank_mask:0xf\n\t"
      "s_nop 0\n\t"
      "v_add_f32_dpp v188, v188, v188 quad_perm:[2,3,0,1] row_mask:0xf bank_mask:0xf\n\t"
      "v_add_f32_dpp v189, v189, v189 quad_perm:[2,3,0,1] row_mask:0xf bank_mask:0xf\n\t"
      "ds_write_b64 v226, v[188:189]\n\t"
      "s_waitcnt lgkmcnt(0)\n\t"
      "s_barrier\n\t"
      "ds_read_b128 v[188:191], v227\n\t"
      "s_waitcnt lgkmcnt(0)\n\t"
      "v_pk_add_f32 v[188:189], v[188:189], v[190:191]\n\t"
      "s_nop 1\n\t"
      "v_add_f32_dpp v188, v188, v188 row_ror:4 row_mask:0xf bank_mask:0xf\n\t"
      "v_add_f32_dpp v189, v189, v189 row_ror:4 row_mask:0xf bank_mask:0xf\n\t"
      "s_nop 0\n\t"
      "v_add_f32_dpp v188, v188, v188 row_ror:8 row_mask:0xf bank_mask:0xf\n\t"
      "v_add_f32_dpp v189, v189, v189 row_ror:8 row_mask:0xf bank_mask:0xf\n\t"
      "v_mul_f32_e32 v188, 0x3b124925, v188\n\t"
      "v_mul_f32_e32 v189, 0x3b124925, v189\n\t"
      "v_fma_f32 v189, -v188, v188, v189\n\t"
      "v_max_f32_e32 v189, 0, v189\n\t"
      "s_bitcmp1_b32 s16, 4\n\t"
      "s_cbranch_scc0 GW_%=\n\t"
      "v_add_f32_e32 v189, 0x3727c5ac, v189\n\t"
      "v_rsq_f32_e32 v189, v189\n\t"
      "v_xor_b32_e32 v188, 0x80000000, v188\n\t"
      "v_xor_b32_e32 v190, s8, v188\n\t"
      "v_xor_b32_e32 v191, s9, v189\n\t"
      "s_mov_b32 s48, 0xf000f\n\t"
      "s_mov_b32 s49, 0xf000f\n\t"
      "s_mov_b64 exec, s[48:49]\n\t"
      "global_store_dwordx4 v228, v[188:191], s[6:7] sc1\n\t"
      "s_mov_b64 exec, -1\n\t"
      "GW_%=:\n\t"
      "s_add_u32 m0, s12, 0\n\t"
      "s_nop 0\n\t"
      "global_load_lds_dwordx4 v224, s[4:5]\n\t"
      "global_load_lds_dwordx4 v221, s[4:5] offset:2048\n\t"
      "s_add_u32 m0, s12, 4096\n\t"
      "s_nop 0\n\t"
      "global_load_lds_dwordx4 v222, s[4:5]\n\t"
      "s_cbranch_vccnz WR0_%=\n\t"
      "global_load_lds_dwordx4 v223, s[4:5] offset:2048\n\t"
      "WR0_%=:\n\t"
      "s_sub_u32 s14, s14, 1\n\t"
      "s_cselect_b32 s48, -13, 1\n\t"
      "s_cselect_b32 s49, -1, 0\n\t"
      "s_mul_i32 s48, s48, 28672\n\t"
      "s_add_u32 s4, s4, s48\n\t"
      "s_addc_u32 s5, s5, s49\n\t"
      "s_add_u32 m0, s12, 28672\n\t"
      "s_nop 0\n\t"
      "global_load_lds_dwordx4 v224, s[4:5]\n\t"
      "global_load_lds_dwordx4 v221, s[4:5] offset:2048\n\t"
      "s_add_u32 m0, s12, 32768\n\t"
      "s_nop 0\n\t"
      "global_load_lds_dwordx4 v222, s[4:5]\n\t"
      "s_cbranch_vccnz WR1_%=\n\t"
      "global_load_lds_dwordx4 v223, s[4:5] offset:2048\n\t"
      "WR1_%=:\n\t"
      "s_sub_u32 s14, s14, 1\n\t"
      "s_cselect_b32 s48, -13, 1\n\t"
      "s_cselect_b32 s49, -1, 0\n\t"
      "s_mul_i32 s48, s48, 28672\n\t"
      "s_add_u32 s4, s4, s48\n\t"
      "s_addc_u32 s5, s5, s49\n\t"
      "s_waitcnt lgkmcnt(0)\n\t"
      "s_nop 1\n\t"
      : "+{s[4:11]}"(s1), "+{s[12:15]}"(s3), "+{s16}"(bits), "+{s17}"(id), "+{s18}"(wv), "+{v[224:231]}"(v1), "={v[160:175]}"(acc)
      :
      : "memory", "vcc", "scc",
        "v176", "v177", "v178", "v179", "v180", "v181", "v182", "v183", "v184", "v185", "v186", "v187", "v188", "v189",
        "v190", "v191", "v192", "v193", "v194", "v195", "v196", "v197", "v198", "v199", "v200", "v201", "v202", "v203",
        "v204", "v205", "v206", "v207", "v208", "v209", "v210", "v211", "v212", "v213", "v214", "v215", "v216", "v217",
        "v218", "v219", "v220", "v221", "v222", "v223", "v248", "v249", "v250", "v251", "s40", "s41", "s42", "s43",
        "s44", "s45", "s46", "s47", "s48", "s49", "s50", "s51"
    );
    }
    PIN();
    {
    const int wave = wv & 0x3ffffff, xcd = id & 7, idx = id >> 3, side = xcd >> 2, xp = xcd & 3;
    B_INDICES;
    const int dg = 2 * side + (ib & 1), blk0 = 7 * dg, rn = (rot + W2E) % 7;
    bits |= (2 - ((wave >> 1) & 2)) | (((t + wave - 1) >> 26) & 32) | ((((wave - 7) & (t - 112)) >> 25) & 64);
    u32x4 vo, v3;
    u64 par = ST_PW + xcd * 128;
    {
      LANE_BITS(v1[6] >> 4);
      u32x8 v;
      u32x4 s;
      const unsigned tv = t + (lane >> 6), wv = wave + (lane >> 6), sv = (tv >> 6) * 8192;
      const unsigned ab = g * B_UNIT + q4 * 128 + (p4 & 1) * 8, al = ((p4 >> 1) ^ swz_tr(8 * g + q4)) << 4;
      const unsigned oi = B_X + c * B_OP, b0 = 112 * wv + 8 * g - 64 * k0;
#pragma unroll
      for (int b = 0; b < 4; ++b) {
        const unsigned cb = 4 * wv - (wv >> 2) * (wv & 3) + b;
        v[b] = ab + (cb >> 2) * 1024 + (((cb & 3) << 5) ^ al);
        const unsigned by = b0 + 56 * (b >> 1) + 32 * (b & 1);
        vo[b] = oi + min(by, by + 896);
      }
      v[4] = oi + 16 * g;
      v[5] = WS_G + tv * 16384 + wv * 1024 + lane * 16;
      v[6] = v1[7] + 4 * 131072;
      v[7] = v[5] + 8192;
      v3[0] = (wv * 224 + 96 + 64 * (side ^ (lane >> 5)) + (tv & 15) * 4 + (lr & 3)) * 4096 + lc * 128 + xp * 1024;
      v3[1] = ST_H3 + (lc & 3) * 32768 + (lc >> 2) * 8192 + 2 * sv + (tv & 63) * 128;
      v3[2] = ST_H2 + sv + (tv & 32) * 512 + (tv & 24) * 256 + wv * 256 + (tv & 4) * 32 + (lane & 1) * 64;
      v3[3] = ST_CK + sv + (tv & 56) * 2048 + (tv & 4) * 1024 + wv * 512 + (g >> 1) * 256 + (tv & 3) * 64 + c * 4;
      put(s, 0, (u64)w2);
      put(s, 2, (u64)(ws + (unsigned)(WS_O + (t & 63) * 32768 + (wave >> 2) * 16384 + side * 8192 + (wave & 3) * 2048)));
    asm volatile(
      "s_waitcnt lgkmcnt(0)\n\ts_nop 4\n\t"
      ".p2align 3\n\t"
      "s_nop 0\n\t"
      "v_mov_b32_e32 v248, -1\n\t"
      "v_mov_b32_e32 v249, -1\n\t"
      "v_mov_b32_e32 v250, -1\n\t"
      "v_mov_b32_e64 v251, -1\n\t"
      "v_add_u32_e32 v221, 4294965504, v224\n\t"
      "v_add_u32_e32 v222, 512, v224\n\t"
      "v_add_u32_e32 v223, 4294966016, v224\n\t"
      "s_bitcmp1_b32 s16, 0\n\t"
      "s_cselect_b64 s[46:47], -1, 0\n\t"
      "s_mov_b64 vcc, s[46:47]\n\t"
      "s_mov_b32 s44, 0xffffffff\n\t"
      "s_mov_b32 s45, 0xffff\n\t"
      "s_or_b32 s42, s13, 0xffff\n\t"
      "s_or_b32 s43, s13, 0xffff\n\t"
      "v_xor_b32_e64 v176, 64, v229\n\t"
      "v_add_u32_e32 v177, 63488, v229\n\t"
      "v_add_u32_e32 v178, 95232, v176\n\t"
      "v_add_u32_e32 v176, 31744, v176\n\t"
      "s_add_u32 m0, s15, 130560\n\t"
      "s_nop 0\n\t"
      "global_load_lds_dwordx4 v229, s[10:11]\n\t"
      "global_load_lds_dwordx4 v176, s[10:11] offset:1024\n\t"
      "global_load_lds_dwordx4 v177, s[10:11] offset:2048\n\t"
      "global_load_lds_dwordx4 v178, s[10:11] offset:3072\n\t"
      "s_add_u32 s10, s10, 131072\n\t"
      "s_addc_u32 s11, s11, 0\n\t"
      "s_cbranch_vccnz SN3_%=\n\t"
      "global_store_dwordx4 v231, v[248:251], s[6:7] sc1\n\t"
      "SN3_%=:\n\t"
      "s_cbranch_vccnz WW0_%=\n\t"
      "s_waitcnt vmcnt(8)\n\t"
      "s_branch WX0_%=\n\t"
      "WW0_%=:\n\t"
      "s_waitcnt vmcnt(7)\n\t"
      "WX0_%=:\n\t"
      "s_waitcnt lgkmcnt(0)\n\t"
      "s_barrier\n\t"
      "ds_read_b64_tr_b16 v[0:1], v232\n\t"
      "ds_read_b64_tr_b16 v[2:3], v232 offset:512\n\t"
      "ds_read_b64_tr_b16 v[4:5], v233\n\t"
      "ds_read_b64_tr_b16 v[6:7], v233 offset:512\n\t"
      "ds_read_b64_tr_b16 v[8:9], v234\n\t"
      "ds_read_b64_tr_b16 v[10:11], v234 offset:512\n\t"
      "ds_read_b64_tr_b16 v[12:13], v235\n\t"
      "ds_read_b64_tr_b16 v[14:15], v235 offset:512\n\t"
      "s_add_u32 m0, s12, 57344\n\t"
      "s_nop 0\n\t"
      "global_load_lds_dwordx4 v224, s[4:5]\n\t"
      "global_load_lds_dwordx4 v221, s[4:5] offset:2048\n\t"
      "s_add_u32 m0, s12, 61440\n\t"
      "s_nop 0\n\t"
      "global_load_lds_dwordx4 v222, s[4:5]\n\t"
      "s_cbranch_vccnz WR2_%=\n\t"
      "global_load_lds_dwordx4 v223, s[4:5] offset:2048\n\t"
      "WR2_%=:\n\t"
      "s_sub_u32 s14, s14, 1\n\t"
      "s_cselect_b32 s48, -13, 1\n\t"
      "s_cselect_b32 s49, -1, 0\n\t"
      "s_mul_i32 s48, s48, 28672\n\t"
      "s_add_u32 s4, s4, s48\n\t"
      "s_addc_u32 s5, s5, s49\n\t"
      "s_cbranch_vccnz WW1_%=\n\t"
      "s_waitcnt vmcnt(8)\n\t"
      "s_branch WX1_%=\n\t"
      "WW1_%=:\n\t"
      "s_waitcnt vmcnt(7)\n\t"
      "WX1_%=:\n\t"
      "s_waitcnt lgkmcnt(0)\n\t"
      "s_barrier\n\t"
      "ds_read_b64_tr_b16 v[16:17], v232 offset:28672\n\t"
      "ds_read_b64_tr_b16 v[18:19], v232 offset:29184\n\t"
      "ds_read_b64_tr_b16 v[20:21], v233 offset:28672\n\t"
      "ds_read_b64_tr_b16 v[22:23], v233 offset:29184\n\t"
      "ds_read_b64_tr_b16 v[24:25], v234 offset:28672\n\t"
      "ds_read_b64_tr_b16 v[26:27], v234 offset:29184\n\t"
      "ds_read_b64_tr_b16 v[28:29], v235 offset:28672\n\t"
      "ds_read_b64_tr_b16 v[30:31], v235 offset:29184\n\t"
      "s_add_u32 m0, s12, 86016\n\t"
      "s_nop 0\n\t"
      "global_load_lds_dwordx4 v224, s[4:5]\n\t"
      "global_load_lds_dwordx4 v221, s[4:5] offset:2048\n\t"
      "s_add_u32 m0, s12, 90112\n\t"
      "s_nop 0\n\t"
      "global_load_lds_dwordx4 v222, s[4:5]\n\t"
      "s_cbranch_vccnz WR3_%=\n\t"
      "global_load_lds_dwordx4 v223, s[4:5] offset:2048\n\t"
      "WR3_%=:\n\t"
      "s_sub_u32 s14, s14, 1\n\t"
      "s_cselect_b32 s48, -13, 1\n\t"
      "s_cselect_b32 s49, -1, 0\n\t"
      "s_mul_i32 s48, s48, 28672\n\t"
      "s_add_u32 s4, s4, s48\n\t"
      "s_addc_u32 s5, s5, s49\n\t"
      "v_add_u32_e32 v232, 57344, v232\n\t"
      "v_add_u32_e32 v233, 57344, v233\n\t"
      "v_add_u32_e32 v234, 57344, v234\n\t"
      "v_add_u32_e32 v235, 57344, v235\n\t"
      "s_cbranch_vccnz WW2_%=\n\t"
      "s_waitcnt vmcnt(4)\n\t"
      "s_branch WX2_%=\n\t"
      "WW2_%=:\n\t"
      "s_waitcnt vmcnt(3)\n\t"
      "WX2_%=:\n\t"
      "s_waitcnt lgkmcnt(0)\n\t"
      "s_barrier\n\t"
      "ds_read_b64_tr_b16 v[32:33], v232\n\t"
      "ds_read_b64_tr_b16 v[34:35], v232 offset:512\n\t"
      "ds_read_b64_tr_b16 v[36:37], v233\n\t"
      "ds_read_b64_tr_b16 v[38:39], v233 offset:512\n\t"
      "ds_read_b64_tr_b16 v[40:41], v234\n\t"
      "ds_read_b64_tr_b16 v[42:43], v234 offset:512\n\t"
      "ds_read_b64_tr_b16 v[44:45], v235\n\t"
      "ds_read_b64_tr_b16 v[46:47], v235 offset:512\n\t"
      "s_add_u32 m0, s12, 0\n\t"
      "s_nop 0\n\t"
      "global_load_lds_dwordx4 v224, s[4:5]\n\t"
      "global_load_lds_dwordx4 v221, s[4:5] offset:2048\n\t"
      "s_add_u32 m0, s12, 4096\n\t"
      "s_nop 0\n\t"
      "global_load_lds_dwordx4 v222, s[4:5]\n\t"
      "s_cbranch_vccnz WR4_%=\n\t"
      "global_load_lds_dwordx4 v223, s[4:5] offset:2048\n\t"
      "WR4_%=:\n\t"
      "s_sub_u32 s14, s14, 1\n\t"
      "s_cselect_b32 s48, -13, 1\n\t"
      "s_cselect_b32 s49, -1, 0\n\t"
      "s_mul_i32 s48, s48, 28672\n\t"
      "s_add_u32 s4, s4, s48\n\t"
      "s_addc_u32 s5, s5, s49\n\t"
      "s_cbranch_vccnz WW3_%=\n\t"
      "s_waitcnt vmcnt(4)\n\t"
      "s_branch WX3_%=\n\t"
      "WW3_%=:\n\t"
      "s_waitcnt vmcnt(3)\n\t"
      "WX3_%=:\n\t"
      "s_waitcnt lgkmcnt(0)\n\t"
      "s_barrier\n\t"
      "ds_read_b64_tr_b16 v[48:49], v232 offset:28672\n\t"
      "ds_read_b64_tr_b16 v[50:51], v232 offset:29184\n\t"
      "ds_read_b64_tr_b16 v[52:53], v233 offset:28672\n\t"
      "ds_read_b64_tr_b16 v[54:55], v233 offset:29184\n\t"
      "ds_read_b64_tr_b16 v[56:57], v234 offset:28672\n\t"
      "ds_read_b64_tr_b16 v[58:59], v234 offset:29184\n\t"
      "ds_read_b64_tr_b16 v[60:61], v235 offset:28672\n\t"
      "ds_read_b64_tr_b16 v[62:63], v235 offset:29184\n\t"
      "s_add_u32 m0, s12, 28672\n\t"
      "s_nop 0\n\t"
      "global_load_lds_dwordx4 v224, s[4:5]\n\t"
      "global_load_lds_dwordx4 v221, s[4:5] offset:2048\n\t"
      "s_add_u32 m0, s12, 32768\n\t"
      "s_nop 0\n\t"
      "global_load_lds_dwordx4 v222, s[4:5]\n\t"
      "s_cbranch_vccnz WR5_%=\n\t"
      "global_load_lds_dwordx4 v223, s[4:5] offset:2048\n\t"
      "WR5_%=:\n\t"
      "s_sub_u32 s14, s14, 1\n\t"
      "s_cselect_b32 s48, -13, 1\n\t"
      "s_cselect_b32 s49, -1, 0\n\t"
      "s_mul_i32 s48, s48, 28672\n\t"
      "s_add_u32 s4, s4, s48\n\t"
      "s_addc_u32 s5, s5, s49\n\t"
      "v_add_u32_e32 v232, 4294909952, v232\n\t"
      "v_add_u32_e32 v233, 4294909952, v233\n\t"
      "v_add_u32_e32 v234, 4294909952, v234\n\t"
      "v_add_u32_e32 v235, 4294909952, v235\n\t"
      "s_cbranch_vccnz WW4_%=\n\t"
      "s_waitcnt vmcnt(4)\n\t"
      "s_branch WX4_%=\n\t"
      "WW4_%=:\n\t"
      "s_waitcnt vmcnt(3)\n\t"
      "WX4_%=:\n\t"
      "s_waitcnt lgkmcnt(0)\n\t"
      "s_barrier\n\t"
      "ds_read_b64_tr_b16 v[64:65], v232\n\t"
      "ds_read_b64_tr_b16 v[66:67], v232 offset:512\n\t"
      "ds_read_b64_tr_b16 v[68:69], v233\n\t"
      "ds_read_b64_tr_b16 v[70:71], v233 offset:512\n\t"
      "ds_read_b64_tr_b16 v[72:73], v234\n\t"
      "ds_read_b64_tr_b16 v[74:75], v234 offset:512\n\t"
      "ds_read_b64_tr_b16 v[76:77], v235\n\t"
      "ds_read_b64_tr_b16 v[78:79], v235 offset:512\n\t"
      "s_add_u32 m0, s12, 57344\n\t"
      "s_nop 0\n\t"
      "global_load_lds_dwordx4 v224, s[4:5]\n\t"
      "global_load_lds_dwordx4 v221, s[4:5] offset:2048\n\t"
      "s_add_u32 m0, s12, 61440\n\t"
      "s_nop 0\n\t"
      "global_load_lds_dwordx4 v222, s[4:5]\n\t"
      "s_cbranch_vccnz WR6_%=\n\t"
      "global_load_lds_dwordx4 v223, s[4:5] offset:2048\n\t"
      "WR6_%=:\n\t"
      "s_sub_u32 s14, s14, 1\n\t"
      "s_cselect_b32 s48, -13, 1\n\t"
      "s_cselect_b32 s49, -1, 0\n\t"
      "s_mul_i32 s48, s48, 28672\n\t"
      "s_add_u32 s4, s4, s48\n\t"
      "s_addc_u32 s5, s5, s49\n\t"
      "s_cbranch_vccnz WW5_%=\n\t"
      "s_waitcnt vmcnt(4)\n\t"
      "s_branch WX5_%=\n\t"
      "WW5_%=:\n\t"
      "s_waitcnt vmcnt(3)\n\t"
      "WX5_%=:\n\t"
      "s_waitcnt lgkmcnt(0)\n\t"
      "s_barrier\n\t"
      "ds_read_b64_tr_b16 v[80:81], v232 offset:28672\n\t"
      "ds_read_b64_tr_b16 v[82:83], v232 offset:29184\n\t"
      "ds_read_b64_tr_b16 v[84:85], v233 offset:28672\n\t"
      "ds_read_b64_tr_b16 v[86:87], v233 offset:29184\n\t"
      "ds_read_b64_tr_b16 v[88:89], v234 offset:28672\n\t"
      "ds_read_b64_tr_b16 v[90:91], v234 offset:29184\n\t"
      "ds_read_b64_tr_b16 v[92:93], v235 offset:28672\n\t"
      "ds_read_b64_tr_b16 v[94:95], v235 offset:29184\n\t"
      "s_add_u32 m0, s12, 86016\n\t"
      "s_nop 0\n\t"
      "global_load_lds_dwordx4 v224, s[4:5]\n\t"
      "global_load_lds_dwordx4 v221, s[4:5] offset:2048\n\t"
      "s_add_u32 m0, s12, 90112\n\t"
      "s_nop 0\n\t"
      "global_load_lds_dwordx4 v222, s[4:5]\n\t"
      "s_cbranch_vccnz WR7_%=\n\t"
      "global_load_lds_dwordx4 v223, s[4:5] offset:2048\n\t"
      "WR7_%=:\n\t"
      "s_sub_u32 s14, s14, 1\n\t"
      "s_cselect_b32 s48, -13, 1\n\t"
      "s_cselect_b32 s49, -1, 0\n\t"
      "s_mul_i32 s48, s48, 28672\n\t"
      "s_add_u32 s4, s4, s48\n\t"
      "s_addc_u32 s5, s5, s49\n\t"
      "v_add_u32_e32 v232, 57344, v232\n\t"
      "v_add_u32_e32 v233, 57344, v233\n\t"
      "v_add_u32_e32 v234, 57344, v234\n\t"
      "v_add_u32_e32 v235, 57344, v235\n\t"
      "s_cbranch_vccnz WW6_%=\n\t"
      "s_waitcnt vmcnt(4)\n\t"
      "s_branch WX6_%=\n\t"
      "WW6_%=:\n\t"
      "s_waitcnt vmcnt(3)\n\t"
      "WX6_%=:\n\t"
      "s_waitcnt lgkmcnt(0)\n\t"
      "s_barrier\n\t"
      "ds_read_b64_tr_b16 v[96:97], v232\n\t"
      "ds_read_b64_tr_b16 v[98:99], v232 offset:512\n\t"
      "ds_read_b64_tr_b16 v[100:101], v233\n\t"
      "ds_read_b64_tr_b16 v[102:103], v233 offset:512\n\t"
      "ds_read_b64_tr_b16 v[104:105], v234\n\t"
      "ds_read_b64_tr_b16 v[106:107], v234 offset:512\n\t"
      "ds_read_b64_tr_b16 v[108:109], v235\n\t"
      "ds_read_b64_tr_b16 v[110:111], v235 offset:512\n\t"
      "s_add_u32 m0, s12, 0\n\t"
      "s_nop 0\n\t"
      "global_load_lds_dwordx4 v224, s[4:5]\n\t"
      "global_load_lds_dwordx4 v221, s[4:5] offset:2048\n\t"
      "s_add_u32 m0, s12, 4096\n\t"
      "s_nop 0\n\t"
      "global_load_lds_dwordx4 v222, s[4:5]\n\t"
      "s_cbranch_vccnz WR8_%=\n\t"
      "global_load_lds_dwordx4 v223, s[4:5] offset:2048\n\t"
      "WR8_%=:\n\t"
      "s_sub_u32 s14, s14, 1\n\t"
      "s_cselect_b32 s48, -13, 1\n\t"
      "s_cselect_b32 s49, -1, 0\n\t"
      "s_mul_i32 s48, s48, 28672\n\t"
      "s_add_u32 s4, s4, s48\n\t"
      "s_addc_u32 s5, s5, s49\n\t"
      "s_cbranch_vccnz WW7_%=\n\t"
      "s_waitcnt vmcnt(4)\n\t"
      "s_branch WX7_%=\n\t"
      "WW7_%=:\n\t"
      "s_waitcnt vmcnt(3)\n\t"
      "WX7_%=:\n\t"
      "s_waitcnt lgkmcnt(0)\n\t"
      "s_barrier\n\t"
      "ds_read_b64_tr_b16 v[112:113], v232 offset:28672\n\t"
      "ds_read_b64_tr_b16 v[114:115], v232 offset:29184\n\t"
      "ds_read_b64_tr_b16 v[116:117], v233 offset:28672\n\t"
      "ds_read_b64_tr_b16 v[118:119], v233 offset:29184\n\t"
      "ds_read_b64_tr_b16 v[120:121], v234 offset:28672\n\t"
      "ds_read_b64_tr_b16 v[122:123], v234 offset:29184\n\t"
      "ds_read_b64_tr_b16 v[124:125], v235 offset:28672\n\t"
      "ds_read_b64_tr_b16 v[126:127], v235 offset:29184\n\t"
      "s_add_u32 m0, s12, 28672\n\t"
      "s_nop 0\n\t"
      "global_load_lds_dwordx4 v224, s[4:5]\n\t"
      "global_load_lds_dwordx4 v221, s[4:5] offset:2048\n\t"
      "s_add_u32 m0, s12, 32768\n\t"
      "s_nop 0\n\t"
      "global_load_lds_dwordx4 v222, s[4:5]\n\t"
      "s_cbranch_vccnz WR9_%=\n\t"
      "global_load_lds_dwordx4 v223, s[4:5] offset:2048\n\t"
      "WR9_%=:\n\t"
      "s_sub_u32 s14, s14, 1\n\t"
      "s_cselect_b32 s48, -13, 1\n\t"
      "s_cselect_b32 s49, -1, 0\n\t"
      "s_mul_i32 s48, s48, 28672\n\t"
      "s_add_u32 s4, s4, s48\n\t"
      "s_addc_u32 s5, s5, s49\n\t"
      "s_nop 0\n\t"
      "v_add_u32_e32 v232, 4294909952, v232\n\t"
      "v_add_u32_e32 v233, 4294909952, v233\n\t"
      "v_add_u32_e32 v234, 4294909952, v234\n\t"
      "v_add_u32_e32 v235, 4294909952, v235\n\t"
      "s_cbranch_vccnz WW8_%=\n\t"
      "s_waitcnt vmcnt(4)\n\t"
      "s_branch WX8_%=\n\t"
      "WW8_%=:\n\t"
      "s_waitcnt vmcnt(3)\n\t"
      "WX8_%=:\n\t"
      "s_waitcnt lgkmcnt(0)\n\t"
      "s_barrier\n\t"
      "ds_read_b64_tr_b16 v[128:129], v232\n\t"
      "ds_read_b64_tr_b16 v[130:131], v232 offset:512\n\t"
      "ds_read_b64_tr_b16 v[132:133], v233\n\t"
      "ds_read_b64_tr_b16 v[134:135], v233 offset:512\n\t"
      "ds_read_b64_tr_b16 v[136:137], v234\n\t"
      "ds_read_b64_tr_b16 v[138:139], v234 offset:512\n\t"
      "ds_read_b64_tr_b16 v[140:141], v235\n\t"
      "ds_read_b64_tr_b16 v[142:143], v235 offset:512\n\t"
      "s_add_u32 m0, s12, 57344\n\t"
      "s_nop 0\n\t"
      "global_load_lds_dwordx4 v224, s[4:5]\n\t"
      "global_load_lds_dwordx4 v221, s[4:5] offset:2048\n\t"
      "s_add_u32 m0, s12, 61440\n\t"
      "s_nop 0\n\t"
      "global_load_lds_dwordx4 v222, s[4:5]\n\t"
      "s_cbranch_vccnz WR10_%=\n\t"
      "global_load_lds_dwordx4 v223, s[4:5] offset:2048\n\t"
      "WR10_%=:\n\t"
      "s_sub_u32 s14, s14, 1\n\t"
      "s_cselect_b32 s48, -13, 1\n\t"
      "s_cselect_b32 s49, -1, 0\n\t"
      "s_mul_i32 s48, s48, 28672\n\t"
      "s_add_u32 s4, s4, s48\n\t"
      "s_addc_u32 s5, s5, s49\n\t"
      "s_cbranch_vccnz WW9_%=\n\t"
      "s_waitcnt vmcnt(4)\n\t"
      "s_branch WX9_%=\n\t"
      "WW9_%=:\n\t"
      "s_waitcnt vmcnt(3)\n\t"
      "WX9_%=:\n\t"
      "s_waitcnt lgkmcnt(0)\n\t"
      "s_barrier\n\t"
      "ds_read_b64_tr_b16 v[144:145], v232 offset:28672\n\t"
      "ds_read_b64_tr_b16 v[146:147], v232 offset:29184\n\t"
      "ds_read_b64_tr_b16 v[148:149], v233 offset:28672\n\t"
      "ds_read_b64_tr_b16 v[150:151], v233 offset:29184\n\t"
      "ds_read_b64_tr_b16 v[152:153], v234 offset:28672\n\t"
      "ds_read_b64_tr_b16 v[154:155], v234 offset:29184\n\t"
      "ds_read_b64_tr_b16 v[156:157], v235 offset:28672\n\t"
      "ds_read_b64_tr_b16 v[158:159], v235 offset:29184\n\t"
      "s_add_u32 m0, s12, 86016\n\t"
      "s_nop 0\n\t"
      "global_load_lds_dwordx4 v224, s[4:5]\n\t"
      "global_load_lds_dwordx4 v221, s[4:5] offset:2048\n\t"
      "s_add_u32 m0, s12, 90112\n\t"
      "s_nop 0\n\t"
      "global_load_lds_dwordx4 v222, s[4:5]\n\t"
      "s_cbranch_vccnz WR11_%=\n\t"
      "global_load_lds_dwordx4 v223, s[4:5] offset:2048\n\t"
      "WR11_%=:\n\t"
      "s_sub_u32 s14, s14, 1\n\t"
      "s_cselect_b32 s48, -13, 1\n\t"
      "s_cselect_b32 s49, -1, 0\n\t"
      "s_mul_i32 s48, s48, 28672\n\t"
      "s_add_u32 s4, s4, s48\n\t"
      "s_addc_u32 s5, s5, s49\n\t"
      "v_add_u32_e32 v232, 57344, v232\n\t"
      "v_add_u32_e32 v233, 57344, v233\n\t"
      "v_add_u32_e32 v234, 57344, v234\n\t"
      "v_add_u32_e32 v235, 57344, v235\n\t"
      "v_min_u32_e32 v184, 32, v230\n\t"
      "v_add_u32_e32 v184, s28, v184\n\t"
      "global_load_dwordx4 v[180:183], v184, s[6:7] sc1\n\t"
      "s_cbranch_vccnz WW10_%=\n\t"
      "s_waitcnt vmcnt(5)\n\t"
      "s_branch WX10_%=\n\t"
      "WW10_%=:\n\t"
      "s_waitcnt vmcnt(4)\n\t"
      "WX10_%=:\n\t"
      "s_waitcnt lgkmcnt(0)\n\t"
      "s_barrier\n\t"
      "ds_read_b64_tr_b16 v[188:189], v232\n\t"
      "ds_read_b64_tr_b16 v[190:191], v232 offset:512\n\t"
      "ds_read_b64_tr_b16 v[192:193], v233\n\t"
      "ds_read_b64_tr_b16 v[194:195], v233 offset:512\n\t"
      "ds_read_b64_tr_b16 v[196:197], v234\n\t"
      "ds_read_b64_tr_b16 v[198:199], v234 offset:512\n\t"
      "ds_read_b64_tr_b16 v[200:201], v235\n\t"
      "ds_read_b64_tr_b16 v[202:203], v235 offset:512\n\t"
      "s_add_u32 m0, s12, 0\n\t"
      "s_nop 0\n\t"
      "global_load_lds_dwordx4 v224, s[4:5]\n\t"
      "global_load_lds_dwordx4 v221, s[4:5] offset:2048\n\t"
      "s_add_u32 m0, s12, 4096\n\t"
      "s_nop 0\n\t"
      "global_load_lds_dwordx4 v222, s[4:5]\n\t"
      "s_cbranch_vccnz WR12_%=\n\t"
      "global_load_lds_dwordx4 v223, s[4:5] offset:2048\n\t"
      "WR12_%=:\n\t"
      "s_sub_u32 s14, s14, 1\n\t"
      "s_cselect_b32 s48, -13, 1\n\t"
      "s_cselect_b32 s49, -1, 0\n\t"
      "s_mul_i32 s48, s48, 28672\n\t"
      "s_add_u32 s4, s4, s48\n\t"
      "s_addc_u32 s5, s5, s49\n\t"
      "s_cbranch_vccnz WW11_%=\n\t"
      "s_waitcnt vmcnt(5)\n\t"
      "s_branch WX11_%=\n\t"
      "WW11_%=:\n\t"
      "s_waitcnt vmcnt(4)\n\t"
      "WX11_%=:\n\t"
      "s_waitcnt lgkmcnt(0)\n\t"
      "s_barrier\n\t"
      "ds_read_b64_tr_b16 v[204:205], v232 offset:28672\n\t"
      "ds_read_b64_tr_b16 v[206:207], v232 offset:29184\n\t"
      "ds_read_b64_tr_b16 v[208:209], v233 offset:28672\n\t"
      "ds_read_b64_tr_b16 v[210:211], v233 offset:29184\n\t"
      "ds_read_b64_tr_b16 v[212:213], v234 offset:28672\n\t"
      "ds_read_b64_tr_b16 v[214:215], v234 offset:29184\n\t"
      "ds_read_b64_tr_b16 v[216:217], v235 offset:28672\n\t"
      "ds_read_b64_tr_b16 v[218:219], v235 offset:29184\n\t"
      "s_add_u32 m0, s12, 28672\n\t"
      "s_nop 0\n\t"
      "global_load_lds_dwordx4 v224, s[4:5]\n\t"
      "global_load_lds_dwordx4 v221, s[4:5] offset:2048\n\t"
      "s_add_u32 m0, s12, 32768\n\t"
      "s_nop 0\n\t"
      "global_load_lds_dwordx4 v222, s[4:5]\n\t"
      "s_cbranch_vccnz WR13_%=\n\t"
      "global_load_lds_dwordx4 v223, s[4:5] offset:2048\n\t"
      "WR13_%=:\n\t"
      "v_add_u32_e32 v232, 4294909952, v232\n\t"
      "v_add_u32_e32 v233, 4294909952, v233\n\t"
      "v_add_u32_e32 v234, 4294909952, v234\n\t"
      "v_add_u32_e32 v235, 4294909952, v235\n\t"
      "s_waitcnt vmcnt(6)\n\t"
      "v_xor_b32_e32 v182, v182, v180\n\t"
      "v_xor_b32_e64 v183, v183, v181\n\t"
      "v_cmp_ne_u64_e32 vcc, s[8:9], v[182:183]\n\t"
      "s_cbranch_vccnz PWR_%=\n\t"
      "PWB_%=:\n\t"
      "v_readlane_b32 s48, v180, 0\n\t"
      "v_readlane_b32 s28, v180, 1\n\t"
      "v_readlane_b32 s29, v180, 2\n\t"
      "v_mov_b32_e32 v220, s8\n\t"
      "v_mov_b32_e32 v221, s9\n\t"
      "s_xor_b32 s22, s22, s48\n\t"
      "v_xor_b32_e32 v231, s28, v231\n\t"
      "v_xor_b32_e32 v238, s28, v238\n\t"
      "v_xor_b32_e32 v246, s28, v246\n\t"
      "v_xor_b32_e32 v247, s28, v247\n\t"
      "v_xor_b32_e32 v245, s29, v245\n\t"
      "s_waitcnt lgkmcnt(0)\n\t"
      "s_waitcnt vmcnt(0)\n\t"
      "s_mov_b32 s51, 0\n\t"
      "s_branch QE_%=\n\t"
      ".p2align 6\n\t"
      "QL_%=:\n\t"
      "s_sleep 2\n\t"
      "s_add_u32 s51, s51, 1\n\t"
      "s_cmp_lt_u32 s51, 262144\n\t"
      "s_cbranch_scc0 QX_%=\n\t"
      "QE_%=:\n\t"
      "s_mov_b64 exec, 3\n\t"
      "v_mov_b64_e32 v[176:177], 0\n\t"
      "global_atomic_or_x2 v[176:177], v246, v[176:177], s[6:7] sc0 sc1\n\t"
      "s_waitcnt vmcnt(0)\n\t"
      "v_cmp_ne_u64_e32 vcc, s[8:9], v[176:177]\n\t"
      "s_mov_b64 exec, -1\n\t"
      "s_cbranch_vccnz QL_%=\n\t"
      "QX_%=:\n\t"
      "s_mov_b32 s51, 0\n\t"
      "s_branch OE_%=\n\t"
      "OL_%=:\n\t"
      "s_sleep 2\n\t"
      "s_add_u32 s51, s51, 1\n\t"
      "s_cmp_lt_u32 s51, 262144\n\t"
      "s_cbranch_scc0 OX_%=\n\t"
      "OE_%=:\n\t"
      "global_load_dwordx4 v[176:179], v230, s[22:23] sc1\n\t"
      "global_load_dwordx4 v[180:183], v230, s[22:23] offset:1024 sc1\n\t"
      "global_load_dword v186, v247, s[6:7] sc1\n\t"
      "s_waitcnt vmcnt(2)\n\t"
      "ds_write_b64 v240, v[176:177]\n\t"
      "s_mov_b64 exec, s[44:45]\n\t"
      "ds_write_b64 v241, v[178:179]\n\t"
      "s_mov_b64 exec, -1\n\t"
      "v_add3_u32 v184, v176, v177, v178\n\t"
      "v_add_u32_e32 v184, v184, v179\n\t"
      "s_waitcnt vmcnt(1)\n\t"
      "ds_write_b64 v242, v[180:181]\n\t"
      "s_mov_b64 exec, s[44:45]\n\t"
      "ds_write_b64 v243, v[182:183]\n\t"
      "s_mov_b64 exec, -1\n\t"
      "v_add3_u32 v185, v180, v181, v182\n\t"
      "v_add_u32_e32 v185, v185, v183\n\t"
      "s_nop 1\n\t"
      "v_permlane32_swap_b32_e32 v184, v185\n\t"
      "v_add_u32_e64 v184, v184, v185\n\t"
      "v_mov_b32_e32 v185, v184\n\t"
      "s_nop 1\n\t"
      "v_permlane16_swap_b32_e32 v184, v185\n\t"
      "v_add3_u32 v184, v184, v185, s8\n\t"
      "s_waitcnt vmcnt(0) lgkmcnt(0)\n\t"
      "v_cmp_ne_u32_e32 vcc, v184, v186\n\t"
      "s_cbranch_vccnz OL_%=\n\t"
      "OX_%=:\n\t"
      "s_mov_b64 vcc, s[46:47]\n\t"
      "s_waitcnt lgkmcnt(0)\n\t"
      "s_barrier\n\t"
      "ds_read_b128 v[176:179], v236\n\t"
      "ds_read_b128 v[180:183], v236 offset:64\n\t"
      "ds_read_b128 v[184:187], v236 offset:128\n\t"
      "s_waitcnt lgkmcnt(2)\n\t"
      "v_mfma_f32_16x16x32_bf16 v[160:163], v[176:179], v[0:3], v[160:163]\n\t"
      "v_mfma_f32_16x16x32_bf16 v[164:167], v[176:179], v[4:7], v[164:167]\n\t"
      "v_mfma_f32_16x16x32_bf16 v[168:171], v[176:179], v[8:11], v[168:171]\n\t"
      "s_cbranch_vccnz M0_%=\n\t"
      "v_mfma_f32_16x16x32_bf16 v[172:175], v[176:179], v[12:15], v[172:175]\n\t"
      "M0_%=:\n\t"
      "s_cbranch_vccz SN4_%=\n\t"
      "global_store_dwordx4 v231, v[248:251], s[6:7] sc1\n\t"
      "SN4_%=:\n\t"
      "ds_read_b128 v[176:179], v236 offset:192\n\t"
      "s_waitcnt lgkmcnt(2)\n\t"
      "v_mfma_f32_16x16x32_bf16 v[160:163], v[180:183], v[16:19], v[160:163]\n\t"
      "v_mfma_f32_16x16x32_bf16 v[164:167], v[180:183], v[20:23], v[164:167]\n\t"
      "v_mfma_f32_16x16x32_bf16 v[168:171], v[180:183], v[24:27], v[168:171]\n\t"
      "s_cbranch_vccnz M1_%=\n\t"
      "v_mfma_f32_16x16x32_bf16 v[172:175], v[180:183], v[28:31], v[172:175]\n\t"
      "M1_%=:\n\t"
      "s_mov_b64 exec, s[42:43]\n\t"
      "global_store_dwordx4 v238, v[248:251], s[6:7] sc1\n\t"
      "s_mov_b64 exec, -1\n\t"
      "ds_read_b128 v[180:183], v236 offset:256\n\t"
      "s_waitcnt lgkmcnt(2)\n\t"
      "v_mfma_f32_16x16x32_bf16 v[160:163], v[184:187], v[32:35], v[160:163]\n\t"
      "v_mfma_f32_16x16x32_bf16 v[164:167], v[184:187], v[36:39], v[164:167]\n\t"
      "v_mfma_f32_16x16x32_bf16 v[168:171], v[184:187], v[40:43], v[168:171]\n\t"
      "s_cbranch_vccnz M2_%=\n\t"
      "v_mfma_f32_16x16x32_bf16 v[172:175], v[184:187], v[44:47], v[172:175]\n\t"
      "M2_%=:\n\t"
      "ds_read_b64_tr_b16 v[0:1], v232\n\t"
      "ds_read_b64_tr_b16 v[2:3], v232 offset:512\n\t"
      "s_bitcmp1_b32 s16, 6\n\t"
      "s_cbranch_scc0 SG2_%=\n\t"
      "global_store_dwordx4 v237, v[248:251], s[6:7] sc1\n\t"
      "global_store_dwordx4 v239, v[248:251], s[6:7] sc1\n\t"
      "SG2_%=:\n\t"
      "ds_read_b128 v[184:187], v236 offset:320\n\t"
      "s_waitcnt lgkmcnt(4)\n\t"
      "v_mfma_f32_16x16x32_bf16 v[160:163], v[176:179], v[48:51], v[160:163]\n\t"
      "v_mfma_f32_16x16x32_bf16 v[164:167], v[176:179], v[52:55], v[164:167]\n\t"
      "v_mfma_f32_16x16x32_bf16 v[168:171], v[176:179], v[56:59], v[168:171]\n\t"
      "s_cbranch_vccnz M3_%=\n\t"
      "v_mfma_f32_16x16x32_bf16 v[172:175], v[176:179], v[60:63], v[172:175]\n\t"
      "M3_%=:\n\t"
      "ds_read_b64_tr_b16 v[4:5], v233\n\t"
      "ds_read_b64_tr_b16 v[6:7], v233 offset:512\n\t"
      "ds_read_b128 v[176:179], v236 offset:384\n\t"
      "s_waitcnt lgkmcnt(6)\n\t"
      "v_mfma_f32_16x16x32_bf16 v[160:163], v[180:183], v[64:67], v[160:163]\n\t"
      "v_mfma_f32_16x16x32_bf16 v[164:167], v[180:183], v[68:71], v[164:167]\n\t"
      "v_mfma_f32_16x16x32_bf16 v[168:171], v[180:183], v[72:75], v[168:171]\n\t"
      "s_cbranch_vccnz M4_%=\n\t"
      "v_mfma_f32_16x16x32_bf16 v[172:175], v[180:183], v[76:79], v[172:175]\n\t"
      "M4_%=:\n\t"
      "ds_read_b64_tr_b16 v[8:9], v234\n\t"
      "ds_read_b64_tr_b16 v[10:11], v234 offset:512\n\t"
      "ds_read_b128 v[180:183], v236 offset:448\n\t"
      "s_waitcnt lgkmcnt(6)\n\t"
      "v_mfma_f32_16x16x32_bf16 v[160:163], v[184:187], v[80:83], v[160:163]\n\t"
      "v_mfma_f32_16x16x32_bf16 v[164:167], v[184:187], v[84:87], v[164:167]\n\t"
      "v_mfma_f32_16x16x32_bf16 v[168:171], v[184:187], v[88:91], v[168:171]\n\t"
      "s_cbranch_vccnz M5_%=\n\t"
      "v_mfma_f32_16x16x32_bf16 v[172:175], v[184:187], v[92:95], v[172:175]\n\t"
      "M5_%=:\n\t"
      "ds_read_b64_tr_b16 v[12:13], v235\n\t"
      "ds_read_b64_tr_b16 v[14:15], v235 offset:512\n\t"
      "s_mov_b32 exec_lo, 0xffff\n\t"
      "s_mov_b32 exec_hi, 0x0\n\t"
      "s_mov_b32 m0, 163328\n\t"
      "s_nop 0\n\t"
      "global_load_lds_dword v244, s[20:21]\n\t"
      "s_mov_b64 exec, -1\n\t"
      "ds_read_b128 v[184:187], v236 offset:512\n\t"
      "s_waitcnt lgkmcnt(6)\n\t"
      "v_mfma_f32_16x16x32_bf16 v[160:163], v[176:179], v[96:99], v[160:163]\n\t"
      "v_mfma_f32_16x16x32_bf16 v[164:167], v[176:179], v[100:103], v[164:167]\n\t"
      "v_mfma_f32_16x16x32_bf16 v[168:171], v[176:179], v[104:107], v[168:171]\n\t"
      "s_cbranch_vccnz M6_%=\n\t"
      "v_mfma_f32_16x16x32_bf16 v[172:175], v[176:179], v[108:111], v[172:175]\n\t"
      "M6_%=:\n\t"
      "ds_read_b64_tr_b16 v[16:17], v232 offset:28672\n\t"
      "ds_read_b64_tr_b16 v[18:19], v232 offset:29184\n\t"
      "ds_read_b128 v[176:179], v236 offset:576\n\t"
      "s_waitcnt lgkmcnt(6)\n\t"
      "v_mfma_f32_16x16x32_bf16 v[160:163], v[180:183], v[112:115], v[160:163]\n\t"
      "v_mfma_f32_16x16x32_bf16 v[164:167], v[180:183], v[116:119], v[164:167]\n\t"
      "v_mfma_f32_16x16x32_bf16 v[168:171], v[180:183], v[120:123], v[168:171]\n\t"
      "s_cbranch_vccnz M7_%=\n\t"
      "v_mfma_f32_16x16x32_bf16 v[172:175], v[180:183], v[124:127], v[172:175]\n\t"
      "M7_%=:\n\t"
      "ds_read_b64_tr_b16 v[20:21], v233 offset:28672\n\t"
      "ds_read_b64_tr_b16 v[22:23], v233 offset:29184\n\t"
      "ds_read_b128 v[180:183], v236 offset:640\n\t"
      "s_waitcnt lgkmcnt(6)\n\t"
      "v_mfma_f32_16x16x32_bf16 v[160:163], v[184:187], v[128:131], v[160:163]\n\t"
      "v_mfma_f32_16x16x32_bf16 v[164:167], v[184:187], v[132:135], v[164:167]\n\t"
      "v_mfma_f32_16x16x32_bf16 v[168:171], v[184:187], v[136:139], v[168:171]\n\t"
      "s_cbranch_vccnz M8_%=\n\t"
      "v_mfma_f32_16x16x32_bf16 v[172:175], v[184:187], v[140:143], v[172:175]\n\t"
      "M8_%=:\n\t"
      "ds_read_b64_tr_b16 v[24:25], v234 offset:28672\n\t"
      "ds_read_b64_tr_b16 v[26:27], v234 offset:29184\n\t"
      "ds_read_b128 v[184:187], v236 offset:704\n\t"
      "s_waitcnt lgkmcnt(6)\n\t"
      "v_mfma_f32_16x16x32_bf16 v[160:163], v[176:179], v[144:147], v[160:163]\n\t"
      "v_mfma_f32_16x16x32_bf16 v[164:167], v[176:179], v[148:151], v[164:167]\n\t"
      "v_mfma_f32_16x16x32_bf16 v[168:171], v[176:179], v[152:155], v[168:171]\n\t"
      "s_cbranch_vccnz M9_%=\n\t"
      "v_mfma_f32_16x16x32_bf16 v[172:175], v[176:179], v[156:159], v[172:175]\n\t"
      "M9_%=:\n\t"
      "ds_read_b64_tr_b16 v[28:29], v235 offset:28672\n\t"
      "ds_read_b64_tr_b16 v[30:31], v235 offset:29184\n\t"
      "ds_read_b128 v[176:179], v236 offset:768\n\t"
      "s_waitcnt lgkmcnt(6)\n\t"
      "v_mfma_f32_16x16x32_bf16 v[160:163], v[180:183], v[188:191], v[160:163]\n\t"
      "v_mfma_f32_16x16x32_bf16 v[164:167], v[180:183], v[192:195], v[164:167]\n\t"
      "v_mfma_f32_16x16x32_bf16 v[168:171], v[180:183], v[196:199], v[168:171]\n\t"
      "s_cbranch_vccnz M10_%=\n\t"
      "v_mfma_f32_16x16x32_bf16 v[172:175], v[180:183], v[200:203], v[172:175]\n\t"
      "M10_%=:\n\t"
      "ds_read_b128 v[180:183], v236 offset:832\n\t"
      "s_waitcnt lgkmcnt(4)\n\t"
      "v_mfma_f32_16x16x32_bf16 v[160:163], v[184:187], v[204:207], v[160:163]\n\t"
      "v_mfma_f32_16x16x32_bf16 v[164:167], v[184:187], v[208:211], v[164:167]\n\t"
      "v_mfma_f32_16x16x32_bf16 v[168:171], v[184:187], v[212:215], v[168:171]\n\t"
      "s_cbranch_vccnz M11_%=\n\t"
      "v_mfma_f32_16x16x32_bf16 v[172:175], v[184:187], v[216:219], v[172:175]\n\t"
      "M11_%=:\n\t"
      "s_waitcnt lgkmcnt(1)\n\t"
      "v_mfma_f32_16x16x32_bf16 v[160:163], v[176:179], v[0:3], v[160:163]\n\t"
      "v_mfma_f32_16x16x32_bf16 v[164:167], v[176:179], v[4:7], v[164:167]\n\t"
      "v_mfma_f32_16x16x32_bf16 v[168:171], v[176:179], v[8:11], v[168:171]\n\t"
      "s_cbranch_vccnz M12_%=\n\t"
      "v_mfma_f32_16x16x32_bf16 v[172:175], v[176:179], v[12:15], v[172:175]\n\t"
      "M12_%=:\n\t"
      "s_waitcnt lgkmcnt(0)\n\t"
      "v_mfma_f32_16x16x32_bf16 v[160:163], v[180:183], v[16:19], v[160:163]\n\t"
      "v_mfma_f32_16x16x32_bf16 v[164:167], v[180:183], v[20:23], v[164:167]\n\t"
      "v_mfma_f32_16x16x32_bf16 v[168:171], v[180:183], v[24:27], v[168:171]\n\t"
      "s_cbranch_vccnz M13_%=\n\t"
      "v_mfma_f32_16x16x32_bf16 v[172:175], v[180:183], v[28:31], v[172:175]\n\t"
      "M13_%=:\n\t"
      "s_nop 5\n\t"
      "v_pk_add_f32 v[188:189], v[160:161], v[164:165]\n\t"
      "v_pk_mul_f32 v[192:193], v[160:161], v[160:161]\n\t"
      "v_pk_add_f32 v[190:191], v[162:163], v[166:167]\n\t"
      "v_pk_mul_f32 v[194:195], v[162:163], v[162:163]\n\t"
      "v_pk_fma_f32 v[192:193], v[164:165], v[164:165], v[192:193]\n\t"
      "v_pk_fma_f32 v[194:195], v[166:167], v[166:167], v[194:195]\n\t"
      "v_pk_add_f32 v[188:189], v[188:189], v[168:169]\n\t"
      "v_pk_fma_f32 v[192:193], v[168:169], v[168:169], v[192:193]\n\t"
      "v_pk_add_f32 v[190:191], v[190:191], v[170:171]\n\t"
      "v_pk_fma_f32 v[194:195], v[170:171], v[170:171], v[194:195]\n\t"
      "s_cbranch_vccnz SB1_%=\n\t"
      "s_nop 0\n\t"
      "v_pk_add_f32 v[188:189], v[188:189], v[172:173]\n\t"
      "v_pk_fma_f32 v[192:193], v[172:173], v[172:173], v[192:193]\n\t"
      "v_pk_add_f32 v[190:191], v[190:191], v[174:175]\n\t"
      "v_pk_fma_f32 v[194:195], v[174:175], v[174:175], v[194:195]\n\t"
      "SB1_%=:\n\t"
      "v_add_f32_dpp v196, v188, v188 row_ror:8 row_mask:0xf bank_mask:0x3\n\t"
      "v_add_f32_dpp v197, v189, v189 row_ror:8 row_mask:0xf bank_mask:0x3\n\t"
      "v_add_f32_dpp v198, v192, v192 row_ror:8 row_mask:0xf bank_mask:0x3\n\t"
      "v_add_f32_dpp v199, v193, v193 row_ror:8 row_mask:0xf bank_mask:0x3\n\t"
      "v_add_f32_dpp v196, v190, v190 row_ror:8 row_mask:0xf bank_mask:0xc\n\t"
      "v_add_f32_dpp v197, v191, v191 row_ror:8 row_mask:0xf bank_mask:0xc\n\t"
      "v_add_f32_dpp v198, v194, v194 row_ror:8 row_mask:0xf bank_mask:0xc\n\t"
      "v_add_f32_dpp v199, v195, v195 row_ror:8 row_mask:0xf bank_mask:0xc\n\t"
      "v_add_f32_dpp v188, v196, v196 row_shl:4 row_mask:0xf bank_mask:0x5\n\t"
      "v_add_f32_dpp v189, v198, v198 row_shl:4 row_mask:0xf bank_mask:0x5\n\t"
      "s_nop 0\n\t"
      "v_add_f32_dpp v188, v197, v197 row_shr:4 row_mask:0xf bank_mask:0xa\n\t"
      "v_add_f32_dpp v189, v199, v199 row_shr:4 row_mask:0xf bank_mask:0xa\n\t"
      "s_nop 0\n\t"
      "v_add_f32_dpp v188, v188, v188 quad_perm:[1,0,3,2] row_mask:0xf bank_mask:0xf\n\t"
      "v_add_f32_dpp v189, v189, v189 quad_perm:[1,0,3,2] row_mask:0xf bank_mask:0xf\n\t"
      "s_nop 0\n\t"
      "v_add_f32_dpp v188, v188, v188 quad_perm:[2,3,0,1] row_mask:0xf bank_mask:0xf\n\t"
      "v_add_f32_dpp v189, v189, v189 quad_perm:[2,3,0,1] row_mask:0xf bank_mask:0xf\n\t"
      "ds_write_b64 v226, v[188:189]\n\t"
      "s_bitcmp1_b32 s16, 4\n\t"
      "s_cbranch_scc0 HI_%=\n\t"
      "s_mov_b64 exec, 255\n\t"
      "global_store_dwordx2 v245, v[220:221], s[6:7] sc1\n\t"
      "s_mov_b64 exec, -1\n\t"
      "HI_%=:\n\t"
      "s_waitcnt vmcnt(1)\n\t"
      "s_waitcnt lgkmcnt(0)\n\t"
      "s_barrier\n\t"
      "ds_read_b128 v[188:191], v227\n\t"
      "s_waitcnt lgkmcnt(0)\n\t"
      "v_pk_add_f32 v[188:189], v[188:189], v[190:191]\n\t"
      "s_nop 1\n\t"
      "v_add_f32_dpp v188, v188, v188 row_ror:4 row_mask:0xf bank_mask:0xf\n\t"
      "v_add_f32_dpp v189, v189, v189 row_ror:4 row_mask:0xf bank_mask:0xf\n\t"
      "s_nop 0\n\t"
      "v_add_f32_dpp v188, v188, v188 row_ror:8 row_mask:0xf bank_mask:0xf\n\t"
      "v_add_f32_dpp v189, v189, v189 row_ror:8 row_mask:0xf bank_mask:0xf\n\t"
      "v_mul_f32_e32 v188, 0x3b124925, v188\n\t"
      "v_mul_f32_e32 v189, 0x3b124925, v189\n\t"
      "v_fma_f32 v189, -v188, v188, v189\n\t"
      "v_max_f32_e64 v189, 0, v189\n\t"
      "v_add_f32_e32 v190, 0x3727c5ac, v189\n\t"
      "v_fmac_f32_e32 v189, 0x3727c5ac, v190\n\t"
      "v_rsq_f32_e64 v191, v189\n\t"
      "s_nop 0\n\t"
      "v_mul_f32_e64 v190, -v188, v191\n\t"
      "v_mov_b32_dpp v192, v191 quad_perm:[0,0,0,0] row_mask:0xf bank_mask:0xf\n\t"
      "s_nop 0\n\t"
      "v_mov_b32_dpp v196, v190 quad_perm:[0,0,0,0] row_mask:0xf bank_mask:0xf\n\t"
      "v_mov_b32_dpp v193, v191 quad_perm:[1,1,1,1] row_mask:0xf bank_mask:0xf\n\t"
      "v_mov_b32_dpp v197, v190 quad_perm:[1,1,1,1] row_mask:0xf bank_mask:0xf\n\t"
      "v_mov_b32_dpp v194, v191 quad_perm:[2,2,2,2] row_mask:0xf bank_mask:0xf\n\t"
      "v_mov_b32_dpp v198, v190 quad_perm:[2,2,2,2] row_mask:0xf bank_mask:0xf\n\t"
      "v_mov_b32_dpp v195, v191 quad_perm:[3,3,3,3] row_mask:0xf bank_mask:0xf\n\t"
      "v_mov_b32_dpp v199, v190 quad_perm:[3,3,3,3] row_mask:0xf bank_mask:0xf\n\t"
      "v_pk_fma_f32 v[160:161], v[160:161], v[192:193], v[196:197]\n\t"
      "v_pk_fma_f32 v[162:163], v[162:163], v[194:195], v[198:199]\n\t"
      "v_pk_fma_f32 v[164:165], v[164:165], v[192:193], v[196:197]\n\t"
      "v_pk_fma_f32 v[166:167], v[166:167], v[194:195], v[198:199]\n\t"
      "v_cvt_pk_bf16_f32 v212, v160, v161\n\t"
      "v_cvt_pk_bf16_f32 v213, v162, v163\n\t"
      "v_cvt_pk_bf16_f32 v214, v164, v165\n\t"
      "v_cvt_pk_bf16_f32 v215, v166, v167\n\t"
      "v_min_u32_e32 v212, -2, v212\n\t"
      "v_min_u32_e32 v213, -2, v213\n\t"
      "v_min_u32_e64 v214, -2, v214\n\t"
      "v_min_u32_e32 v215, -2, v215\n\t"
      "s_nop 0\n\t"
      "v_permlane16_swap_b32_e32 v212, v214\n\t"
      "v_permlane16_swap_b32_e32 v213, v215\n\t"
      "global_store_dwordx4 v231, v[212:215], s[6:7] sc1\n\t"
      "v_pk_fma_f32 v[168:169], v[168:169], v[192:193], v[196:197]\n\t"
      "v_pk_fma_f32 v[170:171], v[170:171], v[194:195], v[198:199]\n\t"
      "v_pk_fma_f32 v[172:173], v[172:173], v[192:193], v[196:197]\n\t"
      "v_pk_fma_f32 v[174:175], v[174:175], v[194:195], v[198:199]\n\t"
      "v_cvt_pk_bf16_f32 v216, v168, v169\n\t"
      "v_cvt_pk_bf16_f32 v217, v170, v171\n\t"
      "v_cvt_pk_bf16_f32 v218, v172, v173\n\t"
      "v_cvt_pk_bf16_f32 v219, v174, v175\n\t"
      "v_min_u32_e32 v216, -2, v216\n\t"
      "v_min_u32_e32 v217, -2, v217\n\t"
      "v_min_u32_e32 v218, -2, v218\n\t"
      "v_min_u32_e32 v219, -2, v219\n\t"
      "s_nop 0\n\t"
      "v_permlane16_swap_b32_e32 v216, v218\n\t"
      "v_permlane16_swap_b32_e32 v217, v219\n\t"
      "s_mov_b64 exec, s[42:43]\n\t"
      "global_store_dwordx4 v238, v[216:219], s[6:7] sc1\n\t"
      "s_mov_b64 exec, -1\n\t"
      "s_nop 1\n\t"
      "v_mov_b32_e32 v240, v188\n\t"
      "v_mul_f32_e32 v241, v189, v191\n\t"
      "v_add_u32_e32 v246, 65536, v228\n\t"
      "v_xor_b32_e32 v242, s8, v188\n\t"
      "v_xor_b32_e32 v243, s9, v241\n\t"
      "s_nop 1\n\t"
      "s_branch BFX_%=\n\t"
      "PWR_%=:\n\t"
      "s_mov_b32 s51, 0\n\t"
      "PWL_%=:\n\t"
      "s_add_u32 s51, s51, 1\n\t"
      "s_cmp_lt_u32 s51, 262144\n\t"
      "s_cbranch_scc0 PWX_%=\n\t"
      "s_sleep 1\n\t"
      "v_min_u32_e32 v184, 32, v230\n\t"
      "v_add_u32_e32 v184, s28, v184\n\t"
      "global_load_dwordx4 v[180:183], v184, s[6:7] sc1\n\t"
      "s_waitcnt vmcnt(0)\n\t"
      "v_xor_b32_e32 v182, v182, v180\n\t"
      "v_xor_b32_e32 v183, v183, v181\n\t"
      "v_cmp_ne_u64_e32 vcc, s[8:9], v[182:183]\n\t"
      "s_cbranch_vccnz PWL_%=\n\t"
      "PWX_%=:\n\t"
      "v_and_b32_e32 v180, 0x2000, v180\n\t"
      "s_nop 1\n\t"
      "s_branch PWB_%=\n\t"
      "BFX_%=:\n\t"
      : "+{s[4:11]}"(s1), "+{s[12:15]}"(s3), "+{s16}"(bits), "+{s[20:23]}"(s), "+{s[28:29]}"(par), "+{v[160:175]}"(acc), "+{v[224:231]}"(v1), "+{v[232:239]}"(v), "+{v[240:243]}"(vo), "+{v[244:247]}"(v3)
      :
      : "memory", "vcc", "scc",
        "v0", "v1", "v2", "v3", "v4", "v5", "v6", "v7", "v8", "v9", "v10", "v11", "v12", "v13", "v14", "v15", "v16",
        "v17", "v18", "v19", "v20", "v21", "v22", "v23", "v24", "v25", "v26", "v27", "v28", "v29", "v30", "v31", "v32",
        "v33", "v34", "v35", "v36", "v37", "v38", "v39", "v40", "v41", "v42", "v43", "v44", "v45", "v46", "v47", "v48",
        "v49", "v50", "v51", "v52", "v53", "v54", "v55", "v56", "v57", "v58", "v59", "v60", "v61", "v62", "v63", "v64",
        "v65", "v66", "v67", "v68", "v69", "v70", "v71", "v72", "v73", "v74", "v75", "v76", "v77", "v78", "v79", "v80",
        "v81", "v82", "v83", "v84", "v85", "v86", "v87", "v88", "v89", "v90", "v91", "v92", "v93", "v94", "v95", "v96",
        "v97", "v98", "v99", "v100", "v101", "v102", "v103", "v104", "v105", "v106", "v107", "v108", "v109", "v110",
        "v111", "v112", "v113", "v114", "v115", "v116", "v117", "v118", "v119", "v120", "v121", "v122", "v123", "v124",
        "v125", "v126", "v127", "v128", "v129", "v130", "v131", "v132", "v133", "v134", "v135", "v136", "v137", "v138",
        "v139", "v140", "v141", "v142", "v143", "v144", "v145", "v146", "v147", "v148", "v149", "v150", "v151", "v152",
        "v153", "v154", "v155", "v156", "v157", "v158", "v159", "v176", "v177", "v178", "v179", "v180", "v181", "v182",
        "v183", "v184", "v185", "v186", "v187", "v188", "v189", "v190", "v191", "v192", "v193", "v194", "v195", "v196",
        "v197", "v198", "v199", "v200", "v201", "v202", "v203", "v204", "v205", "v206", "v207", "v208", "v209", "v210",
        "v211", "v212", "v213", "v214", "v215", "v216", "v217", "v218", "v219", "v220", "v221", "v222", "v223", "v248",
        "v249", "v250", "v251", "s40", "s41", "s42", "s43", "s44", "s45", "s46", "s47", "s48", "s49", "s50", "s51"
    );
    }
    unsigned vq = v3[2];
    PIN();
    LANE_BITS(__builtin_amdgcn_mbcnt_hi(~0u, __builtin_amdgcn_mbcnt_lo(~0u, 0u)));
    u32x8 v, s2;
    u32x16 s;
    v[0] = lane * 16;
    u64 kr = (u64)(6 - rn) | ((u64)rot << 32);
    v[1] = lr * 4096 + ((lc ^ swz_tr(lr)) << 4);
    v[2] = wave * 4096 + g * 1024 + q4 * 128 + (((2 * (wave & 3) + (p4 >> 1)) ^ swz_tr(8 * g + q4)) << 4) + (p4 & 1) * 8;
    v[3] = wave * 4096 + (wave & 3) * 1024 + ((lane * 16) ^ (g << 6));
    v[4] = (wave & 3) * 1024 + (((p4 * 16 + (lane >> 2)) * 16) ^ (p4 << 6));
    v[5] = (lane >> 2) * 896 + p4 * 8;
    v[6] = c * 2;
    const unsigned mh = (unsigned)par, mg = (unsigned)(par >> 32);
    v[7] = ST_H4 + xp * 16384 + ((side * 8192) ^ mg) + ((ib & 1) * 28 + ((7 * wave) >> 1) + p4) * 128;
    put(s, 0, (u64)(w2 + st * 128 + (wave * 224 + rn * 32) * 4096));
    put(s, 2, (u64)(ws + WS_G + (unit(blk0, wave, 0) ^ mg)));
    put(s, 4, (u64)(out + (64 * st + 16 * (wave & 3)) * 896 + dg * 128));
    unsigned od = (16 - dg) * 32;
    put(s, 6, (u64)(b2 + (64 * st + 16 * (wave & 3)) * 2));
    put(s, 8, (u64)(ws + WS_ST));
    s[12] = bits;
    s[13] = wave * G_WV;
    put(s2, 0, (u64)(ws + WS_HN + (unit(blk0, st >> 2, 0) ^ mh) + (2 * (st & 3) + ((wave >> 1) & 1)) * 1024));
    s2[2] = ST_FL + (64 * st + 16 * (wave & 3)) * 16;
    s2[3] = UNI(wave & 1 ? ~0u : 0u);
    put(s2, 4, (u64)(ws + WS_EP));
    asm volatile(
      "s_waitcnt lgkmcnt(0)\n\ts_nop 4\n\t"
      ".p2align 3\n\t"
      "s_lshr_b32 s31, s17, 2\n\t"
      "v_mov_b32_e32 v112, 163600\n\t"
      "ds_read_b128 v[112:115], v112\n\t"
      "v_xor_b32_e32 v248, 64, v241\n\t"
      "v_add_u32_e32 v249, 63488, v241\n\t"
      "v_add_u32_e32 v250, 95232, v248\n\t"
      "v_add_u32_e32 v248, 31744, v248\n\t"
      "v_xor_b32_e64 v251, 32, v242\n\t"
      "v_xor_b32_e32 v252, 64, v242\n\t"
      "v_xor_b32_e32 v253, 96, v242\n\t"
      "s_bitcmp1_b32 s16, 4\n\t"
      "s_cbranch_scc0 GW2_%=\n\t"
      "s_mov_b32 s48, 0xf000f\n\t"
      "s_mov_b32 s49, 0xf000f\n\t"
      "s_mov_b64 exec, s[48:49]\n\t"
      "global_store_dwordx4 v140, v[136:139], s[12:13] sc1\n\t"
      "s_mov_b64 exec, -1\n\t"
      "GW2_%=:\n\t"
      "s_add_u32 m0, s31, 66048\n\t"
      "s_nop 0\n\t"
      "global_load_lds_dwordx4 v241, s[4:5]\n\t"
      "global_load_lds_dwordx4 v248, s[4:5] offset:1024\n\t"
      "global_load_lds_dwordx4 v249, s[4:5] offset:2048\n\t"
      "global_load_lds_dwordx4 v250, s[4:5] offset:3072\n\t"
      "s_sub_u32 s28, s28, 1\n\t"
      "s_cselect_b32 s48, -6, 1\n\t"
      "s_cselect_b32 s49, -1, 0\n\t"
      "s_mul_i32 s48, s48, 131072\n\t"
      "s_add_u32 s4, s4, s48\n\t"
      "s_addc_u32 s5, s5, s49\n\t"
      "global_load_ushort v236, v246, s[10:11]\n\t"
      "s_waitcnt lgkmcnt(0)\n\t"
      "v_readfirstlane_b32 s14, v112\n\t"
      "v_readfirstlane_b32 s15, v113\n\t"
      "v_readfirstlane_b32 s26, v114\n\t"
      "v_readfirstlane_b32 s27, v115\n\t"
      "s_add_u32 s52, s6, 0\n\t"
      "s_addc_u32 s53, s7, 0\n\t"
      "s_add_u32 s54, s6, 131072\n\t"
      "s_addc_u32 s55, s7, 0\n\t"
      "s_add_u32 s56, s6, 262144\n\t"
      "s_addc_u32 s57, s7, 0\n\t"
      "s_add_u32 s58, s6, 393216\n\t"
      "s_addc_u32 s59, s7, 0\n\t"
      "s_add_u32 s60, s6, 524288\n\t"
      "s_addc_u32 s61, s7, 0\n\t"
      "s_add_u32 s62, s6, 655360\n\t"
      "s_addc_u32 s63, s7, 0\n\t"
      "s_add_u32 s64, s6, 786432\n\t"
      "s_addc_u32 s65, s7, 0\n\t"
      "s_add_u32 s48, s29, 0\n\t"
      "s_cmp_ge_u32 s48, 7\n\t"
      "s_cselect_b32 s49, 7, 0\n\t"
      "s_sub_u32 s48, s48, s49\n\t"
      "v_lshl_add_u32 v224, s48, 10, v240\n\t"
      "s_add_u32 s48, s29, 1\n\t"
      "s_cmp_ge_u32 s48, 7\n\t"
      "s_cselect_b32 s49, 7, 0\n\t"
      "s_sub_u32 s48, s48, s49\n\t"
      "v_lshl_add_u32 v225, s48, 10, v240\n\t"
      "s_add_u32 s48, s29, 2\n\t"
      "s_cmp_ge_u32 s48, 7\n\t"
      "s_cselect_b32 s49, 7, 0\n\t"
      "s_sub_u32 s48, s48, s49\n\t"
      "v_lshl_add_u32 v226, s48, 10, v240\n\t"
      "s_add_u32 s48, s29, 3\n\t"
      "s_cmp_ge_u32 s48, 7\n\t"
      "s_cselect_b32 s49, 7, 0\n\t"
      "s_sub_u32 s48, s48, s49\n\t"
      "v_lshl_add_u32 v227, s48, 10, v240\n\t"
      "s_add_u32 s48, s29, 4\n\t"
      "s_cmp_ge_u32 s48, 7\n\t"
      "s_cselect_b32 s49, 7, 0\n\t"
      "s_sub_u32 s48, s48, s49\n\t"
      "v_lshl_add_u32 v228, s48, 10, v240\n\t"
      "s_add_u32 s48, s29, 5\n\t"
      "s_cmp_ge_u32 s48, 7\n\t"
      "s_cselect_b32 s49, 7, 0\n\t"
      "s_sub_u32 s48, s48, s49\n\t"
      "v_lshl_add_u32 v229, s48, 10, v240\n\t"
      "s_add_u32 s48, s29, 6\n\t"
      "s_cmp_ge_u32 s48, 7\n\t"
      "s_cselect_b32 s49, 7, 0\n\t"
      "s_sub_u32 s48, s48, s49\n\t"
      "v_lshl_add_u32 v230, s48, 10, v240\n\t"
      "v_xor_b32_e32 v220, 1024, v243\n\t"
      "v_xor_b32_e32 v221, 2048, v243\n\t"
      "v_xor_b32_e32 v222, 3072, v243\n\t"
      "s_add_u32 s66, s8, s30\n\t"
      "s_addc_u32 s67, s9, 0\n\t"
      "v_lshrrev_b32_e32 v112, 4, v240\n\t"
      "v_and_b32_e32 v113, 15, v112\n\t"
      "v_lshrrev_b32_e32 v112, 4, v112\n\t"
      "s_lshr_b32 s48, s17, 10\n\t"
      "s_and_b32 s48, s48, 16\n\t"
      "v_lshlrev_b32_e32 v112, 3, v112\n\t"
      "v_sub_u32_e32 v112, v113, v112\n\t"
      "v_add_u32_e32 v112, s48, v112\n\t"
      "v_mov_b32_e32 v114, 0xffff\n\t"
      "v_mov_b32_e32 v115, 0xffff0000\n\t"
      "v_cmp_eq_u32_e32 vcc, 0, v112\n\t"
      "s_nop 1\n\t"
      "v_cndmask_b32_e32 v216, 0, v114, vcc\n\t"
      "v_cmp_eq_u32_e32 vcc, 1, v112\n\t"
      "s_nop 1\n\t"
      "v_cndmask_b32_e32 v216, v216, v115, vcc\n\t"
      "v_cmp_eq_u32_e32 vcc, 2, v112\n\t"
      "s_nop 1\n\t"
      "v_cndmask_b32_e32 v217, 0, v114, vcc\n\t"
      "v_cmp_eq_u32_e32 vcc, 3, v112\n\t"
      "s_nop 1\n\t"
      "v_cndmask_b32_e32 v217, v217, v115, vcc\n\t"
      "v_cmp_eq_u32_e32 vcc, 4, v112\n\t"
      "s_nop 1\n\t"
      "v_cndmask_b32_e32 v218, 0, v114, vcc\n\t"
      "v_cmp_eq_u32_e32 vcc, 5, v112\n\t"
      "s_nop 1\n\t"
      "v_cndmask_b32_e32 v218, v218, v115, vcc\n\t"
      "v_cmp_eq_u32_e32 vcc, 6, v112\n\t"
      "s_nop 1\n\t"
      "v_cndmask_b32_e32 v219, 0, v114, vcc\n\t"
      "v_cmp_eq_u32_e32 vcc, 7, v112\n\t"
      "s_nop 1\n\t"
      "v_cndmask_b32_e32 v219, v219, v115, vcc\n\t"
      "v_and_b32_e32 v116, 0xf0, v240\n\t"
      "v_add_u32_e32 v116, s22, v116\n\t"
      "v_mov_b64_e32 v[184:185], 0\n\t"
      "v_mov_b64_e32 v[186:187], 0\n\t"
      "v_mov_b64_e32 v[188:189], 0\n\t"
      "v_mov_b64_e32 v[190:191], 0\n\t"
      "v_mov_b64_e32 v[192:193], 0\n\t"
      "v_mov_b64_e32 v[194:195], 0\n\t"
      "v_mov_b64_e32 v[196:197], 0\n\t"
      "v_mov_b64_e32 v[198:199], 0\n\t"
      "v_mov_b64_e32 v[200:201], 0\n\t"
      "v_mov_b64_e32 v[202:203], 0\n\t"
      "v_mov_b64_e32 v[204:205], 0\n\t"
      "v_mov_b64_e32 v[206:207], 0\n\t"
      "v_mov_b64_e32 v[208:209], 0\n\t"
      "v_mov_b64_e32 v[210:211], 0\n\t"
      "s_waitcnt vmcnt(5)\n\t"
      "v_add_u32_e32 v242, 66048, v242\n\t"
      "v_add_u32_e32 v251, 66048, v251\n\t"
      "v_add_u32_e32 v252, 66048, v252\n\t"
      "v_add_u32_e32 v253, 66048, v253\n\t"
      "ds_read_b64_tr_b16 v[0:1], v242 offset:64512\n\t"
      "ds_read_b64_tr_b16 v[2:3], v242 offset:65024\n\t"
      "ds_read_b64_tr_b16 v[4:5], v251 offset:64512\n\t"
      "ds_read_b64_tr_b16 v[6:7], v251 offset:65024\n\t"
      "ds_read_b64_tr_b16 v[8:9], v252 offset:64512\n\t"
      "ds_read_b64_tr_b16 v[10:11], v252 offset:65024\n\t"
      "ds_read_b64_tr_b16 v[12:13], v253 offset:64512\n\t"
      "ds_read_b64_tr_b16 v[14:15], v253 offset:65024\n\t"
      "s_waitcnt vmcnt(1)\n\t"
      "s_add_u32 m0, s31, 0\n\t"
      "s_nop 0\n\t"
      "global_load_lds_dwordx4 v241, s[4:5]\n\t"
      "global_load_lds_dwordx4 v248, s[4:5] offset:1024\n\t"
      "global_load_lds_dwordx4 v249, s[4:5] offset:2048\n\t"
      "global_load_lds_dwordx4 v250, s[4:5] offset:3072\n\t"
      "s_sub_u32 s28, s28, 1\n\t"
      "s_cselect_b32 s48, -6, 1\n\t"
      "s_cselect_b32 s49, -1, 0\n\t"
      "s_mul_i32 s48, s48, 131072\n\t"
      "s_add_u32 s4, s4, s48\n\t"
      "s_addc_u32 s5, s5, s49\n\t"
      "ds_read_b64_tr_b16 v[16:17], v242\n\t"
      "ds_read_b64_tr_b16 v[18:19], v242 offset:512\n\t"
      "ds_read_b64_tr_b16 v[20:21], v251\n\t"
      "ds_read_b64_tr_b16 v[22:23], v251 offset:512\n\t"
      "ds_read_b64_tr_b16 v[24:25], v252\n\t"
      "ds_read_b64_tr_b16 v[26:27], v252 offset:512\n\t"
      "ds_read_b64_tr_b16 v[28:29], v253\n\t"
      "ds_read_b64_tr_b16 v[30:31], v253 offset:512\n\t"
      "s_waitcnt vmcnt(0)\n\t"
      "s_add_u32 m0, s31, 32768\n\t"
      "s_nop 0\n\t"
      "global_load_lds_dwordx4 v241, s[4:5]\n\t"
      "global_load_lds_dwordx4 v248, s[4:5] offset:1024\n\t"
      "global_load_lds_dwordx4 v249, s[4:5] offset:2048\n\t"
      "global_load_lds_dwordx4 v250, s[4:5] offset:3072\n\t"
      "s_sub_u32 s28, s28, 1\n\t"
      "s_cselect_b32 s48, -6, 1\n\t"
      "s_cselect_b32 s49, -1, 0\n\t"
      "s_mul_i32 s48, s48, 131072\n\t"
      "s_add_u32 s4, s4, s48\n\t"
      "s_addc_u32 s5, s5, s49\n\t"
      "s_nop 0\n\t"
      "v_add_u32_e32 v242, 4294901248, v242\n\t"
      "v_add_u32_e32 v251, 4294901248, v251\n\t"
      "v_add_u32_e32 v252, 4294901248, v252\n\t"
      "v_add_u32_e32 v253, 4294901248, v253\n\t"
      "ds_read_b64_tr_b16 v[32:33], v242\n\t"
      "ds_read_b64_tr_b16 v[34:35], v242 offset:512\n\t"
      "ds_read_b64_tr_b16 v[36:37], v251\n\t"
      "ds_read_b64_tr_b16 v[38:39], v251 offset:512\n\t"
      "ds_read_b64_tr_b16 v[40:41], v252\n\t"
      "ds_read_b64_tr_b16 v[42:43], v252 offset:512\n\t"
      "ds_read_b64_tr_b16 v[44:45], v253\n\t"
      "ds_read_b64_tr_b16 v[46:47], v253 offset:512\n\t"
      "s_waitcnt vmcnt(0)\n\t"
      "s_waitcnt lgkmcnt(14)\n\t"
      "s_add_u32 m0, s31, 130560\n\t"
      "s_nop 0\n\t"
      "global_load_lds_dwordx4 v241, s[4:5]\n\t"
      "global_load_lds_dwordx4 v248, s[4:5] offset:1024\n\t"
      "global_load_lds_dwordx4 v249, s[4:5] offset:2048\n\t"
      "global_load_lds_dwordx4 v250, s[4:5] offset:3072\n\t"
      "s_sub_u32 s28, s28, 1\n\t"
      "s_cselect_b32 s48, -6, 1\n\t"
      "s_cselect_b32 s49, -1, 0\n\t"
      "s_mul_i32 s48, s48, 131072\n\t"
      "s_add_u32 s4, s4, s48\n\t"
      "s_addc_u32 s5, s5, s49\n\t"
      "ds_read_b64_tr_b16 v[48:49], v242 offset:32768\n\t"
      "ds_read_b64_tr_b16 v[50:51], v242 offset:33280\n\t"
      "ds_read_b64_tr_b16 v[52:53], v251 offset:32768\n\t"
      "ds_read_b64_tr_b16 v[54:55], v251 offset:33280\n\t"
      "ds_read_b64_tr_b16 v[56:57], v252 offset:32768\n\t"
      "ds_read_b64_tr_b16 v[58:59], v252 offset:33280\n\t"
      "ds_read_b64_tr_b16 v[60:61], v253 offset:32768\n\t"
      "ds_read_b64_tr_b16 v[62:63], v253 offset:33280\n\t"
      "s_bitcmp1_b32 s16, 1\n\t"
      "s_cbranch_scc0 HWQ_%=\n\t"
      "global_load_dwordx4 v[112:115], v116, s[12:13] sc1\n\t"
      "HWQ_%=:\n\t"
      "s_waitcnt vmcnt(0)\n\t"
      "s_waitcnt lgkmcnt(14)\n\t"
      "s_add_u32 m0, s31, 66048\n\t"
      "s_nop 0\n\t"
      "global_load_lds_dwordx4 v241, s[4:5]\n\t"
      "global_load_lds_dwordx4 v248, s[4:5] offset:1024\n\t"
      "global_load_lds_dwordx4 v249, s[4:5] offset:2048\n\t"
      "global_load_lds_dwordx4 v250, s[4:5] offset:3072\n\t"
      "s_sub_u32 s28, s28, 1\n\t"
      "s_cselect_b32 s48, -6, 1\n\t"
      "s_cselect_b32 s49, -1, 0\n\t"
      "s_mul_i32 s48, s48, 131072\n\t"
      "s_add_u32 s4, s4, s48\n\t"
      "s_addc_u32 s5, s5, s49\n\t"
      "v_add_u32_e32 v242, 66048, v242\n\t"
      "v_add_u32_e32 v251, 66048, v251\n\t"
      "v_add_u32_e32 v252, 66048, v252\n\t"
      "v_add_u32_e32 v253, 66048, v253\n\t"
      "ds_read_b64_tr_b16 v[64:65], v242 offset:64512\n\t"
      "ds_read_b64_tr_b16 v[66:67], v242 offset:65024\n\t"
      "ds_read_b64_tr_b16 v[68:69], v251 offset:64512\n\t"
      "ds_read_b64_tr_b16 v[70:71], v251 offset:65024\n\t"
      "ds_read_b64_tr_b16 v[72:73], v252 offset:64512\n\t"
      "ds_read_b64_tr_b16 v[74:75], v252 offset:65024\n\t"
      "ds_read_b64_tr_b16 v[76:77], v253 offset:64512\n\t"
      "ds_read_b64_tr_b16 v[78:79], v253 offset:65024\n\t"
      "s_waitcnt vmcnt(0)\n\t"
      "s_waitcnt lgkmcnt(14)\n\t"
      "s_add_u32 m0, s31, 0\n\t"
      "s_nop 0\n\t"
      "global_load_lds_dwordx4 v241, s[4:5]\n\t"
      "global_load_lds_dwordx4 v248, s[4:5] offset:1024\n\t"
      "global_load_lds_dwordx4 v249, s[4:5] offset:2048\n\t"
      "global_load_lds_dwordx4 v250, s[4:5] offset:3072\n\t"
      "ds_read_b64_tr_b16 v[80:81], v242\n\t"
      "ds_read_b64_tr_b16 v[82:83], v242 offset:512\n\t"
      "ds_read_b64_tr_b16 v[84:85], v251\n\t"
      "ds_read_b64_tr_b16 v[86:87], v251 offset:512\n\t"
      "ds_read_b64_tr_b16 v[88:89], v252\n\t"
      "ds_read_b64_tr_b16 v[90:91], v252 offset:512\n\t"
      "ds_read_b64_tr_b16 v[92:93], v253\n\t"
      "ds_read_b64_tr_b16 v[94:95], v253 offset:512\n\t"
      "s_bitcmp1_b32 s16, 1\n\t"
      "s_cbranch_scc0 HWH_%=\n\t"
      "v_xor_b32_e32 v114, v114, v112\n\t"
      "v_xor_b32_e32 v115, v115, v113\n\t"
      "v_cmp_ne_u64_e32 vcc, s[14:15], v[114:115]\n\t"
      "s_cbranch_vccnz HWR_%=\n\t"
      "HWB_%=:\n\t"
      "s_mov_b32 exec_hi, s23\n\t"
      "s_not_b32 exec_lo, s23\n\t"
      "s_add_u32 s68, s20, 0\n\t"
      "s_addc_u32 s69, s21, 0\n\t"
      "global_load_dwordx4 v[184:187], v240, s[68:69] sc1\n\t"
      "s_add_u32 s68, s20, 131072\n\t"
      "s_addc_u32 s69, s21, 0\n\t"
      "global_load_dwordx4 v[188:191], v240, s[68:69] sc1\n\t"
      "s_add_u32 s68, s20, 262144\n\t"
      "s_addc_u32 s69, s21, 0\n\t"
      "global_load_dwordx4 v[192:195], v240, s[68:69] sc1\n\t"
      "s_add_u32 s68, s20, 393216\n\t"
      "s_addc_u32 s69, s21, 0\n\t"
      "global_load_dwordx4 v[196:199], v240, s[68:69] sc1\n\t"
      "s_add_u32 s68, s20, 524288\n\t"
      "s_addc_u32 s69, s21, 0\n\t"
      "global_load_dwordx4 v[200:203], v240, s[68:69] sc1\n\t"
      "s_add_u32 s68, s20, 655360\n\t"
      "s_addc_u32 s69, s21, 0\n\t"
      "global_load_dwordx4 v[204:207], v240, s[68:69] sc1\n\t"
      "s_add_u32 s68, s20, 786432\n\t"
      "s_addc_u32 s69, s21, 0\n\t"
      "global_load_dwordx4 v[208:211], v240, s[68:69] sc1\n\t"
      "s_mov_b64 exec, -1\n\t"
      "v_lshlrev_b32_e32 v236, 16, v236\n\t"
      "v_add_f32_e32 v236, v236, v112\n\t"
      "v_cvt_pk_bf16_f32 v114, v113, v113\n\t"
      "v_lshlrev_b32_e32 v115, 16, v114\n\t"
      "v_sub_f32_e32 v115, v113, v115\n\t"
      "v_cvt_pk_bf16_f32 v115, v115, v115\n\t"
      "v_mov_b32_e32 v237, v236\n\t"
      "v_mov_b32_e32 v238, v236\n\t"
      "v_mov_b32_e32 v239, v236\n\t"
      "v_and_b32_e32 v120, v216, v115\n\t"
      "v_and_b32_e32 v216, v216, v114\n\t"
      "v_and_b32_e32 v121, v217, v115\n\t"
      "v_and_b32_e32 v217, v217, v114\n\t"
      "v_and_b32_e32 v122, v218, v115\n\t"
      "v_and_b32_e32 v218, v218, v114\n\t"
      "v_and_b32_e64 v123, v219, v115\n\t"
      "v_and_b32_e32 v219, v219, v114\n\t"
      "HWH_%=:\n\t"
      "s_waitcnt vmcnt(0)\n\t"
      "v_add_u32_e32 v242, 4294901248, v242\n\t"
      "v_add_u32_e32 v251, 4294901248, v251\n\t"
      "v_add_u32_e32 v252, 4294901248, v252\n\t"
      "v_add_u32_e32 v253, 4294901248, v253\n\t"
      "ds_read_b64_tr_b16 v[96:97], v242\n\t"
      "ds_read_b64_tr_b16 v[98:99], v242 offset:512\n\t"
      "ds_read_b64_tr_b16 v[100:101], v251\n\t"
      "ds_read_b64_tr_b16 v[102:103], v251 offset:512\n\t"
      "ds_read_b64_tr_b16 v[104:105], v252\n\t"
      "ds_read_b64_tr_b16 v[106:107], v252 offset:512\n\t"
      "ds_read_b64_tr_b16 v[108:109], v253\n\t"
      "ds_read_b64_tr_b16 v[110:111], v253 offset:512\n\t"
      "s_mov_b64 exec, 15\n\t"
      "v_mov_b64_e32 v[232:233], 0\n\t"
      "global_atomic_or_x2 v[232:233], v247, v[232:233], s[12:13] sc0 sc1\n\t"
      "s_mov_b64 exec, -1\n\t"
      "s_bitcmp1_b32 s16, 1\n\t"
      "s_cbranch_scc0 HWE_%=\n\t"
      "s_waitcnt vmcnt(1)\n\t"
      "v_max3_u32 v234, v184, v185, v186\n\t"
      "v_max3_u32 v234, v234, v187, v188\n\t"
      "v_max3_u32 v234, v234, v189, v190\n\t"
      "v_max3_u32 v234, v234, v191, v192\n\t"
      "v_max3_u32 v234, v234, v193, v194\n\t"
      "v_max3_u32 v234, v234, v195, v196\n\t"
      "v_max3_u32 v234, v234, v197, v198\n\t"
      "v_max3_u32 v234, v234, v199, v200\n\t"
      "v_max3_u32 v234, v234, v201, v202\n\t"
      "v_max3_u32 v234, v234, v203, v204\n\t"
      "v_max3_u32 v234, v234, v205, v206\n\t"
      "v_max3_u32 v234, v234, v207, v208\n\t"
      "v_max3_u32 v234, v234, v209, v210\n\t"
      "v_max_u32_e32 v234, v234, v211\n\t"
      "v_cmp_eq_u32_e32 vcc, -1, v234\n\t"
      "s_cbranch_vccnz HXR_%=\n\t"
      "HXB_%=:\n\t"
      "v_mfma_f32_16x16x32_bf16 v[156:159], v[184:187], v[120:123], v[236:239]\n\t"
      "v_mfma_f32_16x16x32_bf16 v[160:163], v[188:191], v[120:123], v[236:239]\n\t"
      "v_mfma_f32_16x16x32_bf16 v[164:167], v[192:195], v[120:123], v[236:239]\n\t"
      "v_mfma_f32_16x16x32_bf16 v[168:171], v[196:199], v[120:123], v[236:239]\n\t"
      "v_mfma_f32_16x16x32_bf16 v[172:175], v[200:203], v[120:123], v[236:239]\n\t"
      "v_mfma_f32_16x16x32_bf16 v[176:179], v[204:207], v[120:123], v[236:239]\n\t"
      "v_mfma_f32_16x16x32_bf16 v[180:183], v[208:211], v[120:123], v[236:239]\n\t"
      "v_mfma_f32_16x16x32_bf16 v[156:159], v[184:187], v[216:219], v[156:159]\n\t"
      "v_mfma_f32_16x16x32_bf16 v[160:163], v[188:191], v[216:219], v[160:163]\n\t"
      "v_mfma_f32_16x16x32_bf16 v[164:167], v[192:195], v[216:219], v[164:167]\n\t"
      "v_mfma_f32_16x16x32_bf16 v[168:171], v[196:199], v[216:219], v[168:171]\n\t"
      "v_mfma_f32_16x16x32_bf16 v[172:175], v[200:203], v[216:219], v[172:175]\n\t"
      "v_mfma_f32_16x16x32_bf16 v[176:179], v[204:207], v[216:219], v[176:179]\n\t"
      "v_mfma_f32_16x16x32_bf16 v[180:183], v[208:211], v[216:219], v[180:183]\n\t"
      "HWE_%=:\n\t"
      "s_waitcnt lgkmcnt(0)\n\t"
      "s_barrier\n\t"
      "s_mov_b64 exec, 15\n\t"
      "s_waitcnt vmcnt(0)\n\t"
      "v_subrev_u32_e32 v232, s14, v232\n\t"
      "v_cmp_ne_u32_e64 s[48:49], s15, v233\n\t"
      "v_subrev_u32_e32 v232, 0, v232\n\t"
      "v_cmp_lt_u32_e32 vcc, 7, v232\n\t"
      "s_mov_b64 exec, -1\n\t"
      "s_or_b64 vcc, vcc, s[48:49]\n\t"
      "s_cbranch_vccz PRY_%=\n\t"
      "s_mov_b32 s51, 0\n\t"
      "s_branch PRE_%=\n\t"
      "PRL_%=:\n\t"
      "s_sleep 4\n\t"
      "s_add_u32 s51, s51, 1\n\t"
      "s_cmp_lt_u32 s51, 262144\n\t"
      "s_cbranch_scc0 PRX_%=\n\t"
      "PRE_%=:\n\t"
      "s_mov_b64 exec, 15\n\t"
      "v_mov_b64_e32 v[232:233], 0\n\t"
      "global_atomic_or_x2 v[232:233], v247, v[232:233], s[12:13] sc0 sc1\n\t"
      "s_waitcnt vmcnt(0)\n\t"
      "v_subrev_u32_e32 v232, s14, v232\n\t"
      "v_cmp_ne_u32_e64 s[48:49], s15, v233\n\t"
      "v_subrev_u32_e32 v232, 0, v232\n\t"
      "v_cmp_lt_u32_e32 vcc, 7, v232\n\t"
      "s_mov_b64 exec, -1\n\t"
      "s_or_b64 vcc, vcc, s[48:49]\n\t"
      "s_cbranch_vccnz PRL_%=\n\t"
      "PRX_%=:\n\t"
      "PRY_%=:\n\t"
      "global_load_dwordx4 v[128:131], v224, s[52:53] sc1\n\t"
      "global_load_dwordx4 v[132:135], v225, s[52:53] sc1\n\t"
      "global_load_dwordx4 v[136:139], v226, s[52:53] sc1\n\t"
      "global_load_dwordx4 v[140:143], v227, s[52:53] sc1\n\t"
      "global_load_dwordx4 v[144:147], v228, s[52:53] sc1\n\t"
      "global_load_dwordx4 v[148:151], v229, s[52:53] sc1\n\t"
      "global_load_dwordx4 v[152:155], v230, s[52:53] sc1\n\t"
      "s_cmp_ge_u32 s17, 65536\n\t"
      "s_cbranch_scc1 GQ_%=\n\t"
      ".p2align 6\n\t"
      "s_setprio 1\n\t"
      "s_waitcnt vmcnt(6)\n\t"
      "v_max3_u32 v234, v128, v129, v130\n\t"
      "v_max_u32_e32 v234, v234, v131\n\t"
      "v_cmp_eq_u32_e32 vcc, -1, v234\n\t"
      "s_cbranch_vccnz FP0R_%=\n\t"
      "FP0B_%=:\n\t"
      "v_mfma_f32_16x16x32_bf16 v[184:187], v[128:131], v[0:3], v[156:159]\n\t"
      "v_mfma_f32_16x16x32_bf16 v[188:191], v[128:131], v[4:7], 0\n\t"
      "v_mfma_f32_16x16x32_bf16 v[192:195], v[128:131], v[8:11], 0\n\t"
      "v_mfma_f32_16x16x32_bf16 v[196:199], v[128:131], v[12:15], 0\n\t"
      "global_load_dwordx4 v[128:131], v224, s[54:55] sc1\n\t"
      "s_waitcnt vmcnt(6)\n\t"
      "v_max3_u32 v234, v132, v133, v134\n\t"
      "v_max_u32_e32 v234, v234, v135\n\t"
      "v_cmp_eq_u32_e32 vcc, -1, v234\n\t"
      "s_cbranch_vccnz FP1R_%=\n\t"
      "FP1B_%=:\n\t"
      "v_mfma_f32_16x16x32_bf16 v[184:187], v[132:135], v[16:19], v[184:187]\n\t"
      "v_mfma_f32_16x16x32_bf16 v[188:191], v[132:135], v[20:23], v[188:191]\n\t"
      "v_mfma_f32_16x16x32_bf16 v[192:195], v[132:135], v[24:27], v[192:195]\n\t"
      "v_mfma_f32_16x16x32_bf16 v[196:199], v[132:135], v[28:31], v[196:199]\n\t"
      "global_load_dwordx4 v[132:135], v225, s[54:55] sc1\n\t"
      "s_waitcnt vmcnt(6)\n\t"
      "v_max3_u32 v234, v136, v137, v138\n\t"
      "v_max_u32_e32 v234, v234, v139\n\t"
      "v_cmp_eq_u32_e32 vcc, -1, v234\n\t"
      "s_cbranch_vccnz FP2R_%=\n\t"
      "FP2B_%=:\n\t"
      "v_mfma_f32_16x16x32_bf16 v[184:187], v[136:139], v[32:35], v[184:187]\n\t"
      "v_mfma_f32_16x16x32_bf16 v[188:191], v[136:139], v[36:39], v[188:191]\n\t"
      "v_mfma_f32_16x16x32_bf16 v[192:195], v[136:139], v[40:43], v[192:195]\n\t"
      "v_mfma_f32_16x16x32_bf16 v[196:199], v[136:139], v[44:47], v[196:199]\n\t"
      "global_load_dwordx4 v[136:139], v226, s[54:55] sc1\n\t"
      "s_waitcnt vmcnt(6)\n\t"
      "v_max3_u32 v234, v140, v141, v142\n\t"
      "v_max_u32_e32 v234, v234, v143\n\t"
      "v_cmp_eq_u32_e32 vcc, -1, v234\n\t"
      "s_cbranch_vccnz FP3R_%=\n\t"
      "FP3B_%=:\n\t"
      "v_mfma_f32_16x16x32_bf16 v[184:187], v[140:143], v[48:51], v[184:187]\n\t"
      "v_mfma_f32_16x16x32_bf16 v[188:191], v[140:143], v[52:55], v[188:191]\n\t"
      "v_mfma_f32_16x16x32_bf16 v[192:195], v[140:143], v[56:59], v[192:195]\n\t"
      "v_mfma_f32_16x16x32_bf16 v[196:199], v[140:143], v[60:63], v[196:199]\n\t"
      "global_load_dwordx4 v[140:143], v227, s[54:55] sc1\n\t"
      "s_waitcnt vmcnt(6)\n\t"
      "v_max3_u32 v234, v144, v145, v146\n\t"
      "v_max_u32_e32 v234, v234, v147\n\t"
      "v_cmp_eq_u32_e32 vcc, -1, v234\n\t"
      "s_cbranch_vccnz FP4R_%=\n\t"
      "FP4B_%=:\n\t"
      "v_mfma_f32_16x16x32_bf16 v[184:187], v[144:147], v[64:67], v[184:187]\n\t"
      "v_mfma_f32_16x16x32_bf16 v[188:191], v[144:147], v[68:71], v[188:191]\n\t"
      "v_mfma_f32_16x16x32_bf16 v[192:195], v[144:147], v[72:75], v[192:195]\n\t"
      "v_mfma_f32_16x16x32_bf16 v[196:199], v[144:147], v[76:79], v[196:199]\n\t"
      "global_load_dwordx4 v[144:147], v228, s[54:55] sc1\n\t"
      "s_waitcnt vmcnt(6)\n\t"
      "v_max3_u32 v234, v148, v149, v150\n\t"
      "v_max_u32_e32 v234, v234, v151\n\t"
      "v_cmp_eq_u32_e32 vcc, -1, v234\n\t"
      "s_cbranch_vccnz FP5R_%=\n\t"
      "FP5B_%=:\n\t"
      "v_mfma_f32_16x16x32_bf16 v[184:187], v[148:151], v[80:83], v[184:187]\n\t"
      "v_mfma_f32_16x16x32_bf16 v[188:191], v[148:151], v[84:87], v[188:191]\n\t"
      "v_mfma_f32_16x16x32_bf16 v[192:195], v[148:151], v[88:91], v[192:195]\n\t"
      "v_mfma_f32_16x16x32_bf16 v[196:199], v[148:151], v[92:95], v[196:199]\n\t"
      "global_load_dwordx4 v[148:151], v229, s[54:55] sc1\n\t"
      "s_waitcnt vmcnt(6)\n\t"
      "v_max3_u32 v234, v152, v153, v154\n\t"
      "v_max_u32_e32 v234, v234, v155\n\t"
      "v_cmp_eq_u32_e32 vcc, -1, v234\n\t"
      "s_cbranch_vccnz FP6R_%=\n\t"
      "FP6B_%=:\n\t"
      "v_mfma_f32_16x16x32_bf16 v[184:187], v[152:155], v[96:99], v[184:187]\n\t"
      "s_waitcnt vmcnt(5)\n\t"
      "v_cmp_eq_u32_e32 vcc, -1, v128\n\t"
      "v_mfma_f32_16x16x32_bf16 v[188:191], v[152:155], v[100:103], v[188:191]\n\t"
      "v_mfma_f32_16x16x32_bf16 v[192:195], v[152:155], v[104:107], v[192:195]\n\t"
      "s_cbranch_vccnz FP7R_%=\n\t"
      "FP7B_%=:\n\t"
      "v_mfma_f32_16x16x32_bf16 v[196:199], v[152:155], v[108:111], v[196:199]\n\t"
      "global_load_dwordx4 v[152:155], v230, s[54:55] sc1\n\t"
      "s_nop 0\n\t"
      "ds_write_b128 v243, v[184:187]\n\t"
      "s_nop 1\n\t"
      "ds_write_b128 v220, v[188:191]\n\t"
      "ds_write_b128 v221, v[192:195]\n\t"
      "s_nop 0\n\t"
      "ds_write_b128 v222, v[196:199]\n\t"
      "s_waitcnt lgkmcnt(0)\n\t"
      "s_barrier\n\t"
      "ds_read_b128 v[112:115], v244\n\t"
      "ds_read_b128 v[116:119], v244 offset:4096\n\t"
      "ds_read_b128 v[120:123], v244 offset:8192\n\t"
      "ds_read_b128 v[124:127], v244 offset:12288\n\t"
      "ds_read_b128 v[184:187], v244 offset:16384\n\t"
      "ds_read_b128 v[188:191], v244 offset:20480\n\t"
      "ds_read_b128 v[192:195], v244 offset:24576\n\t"
      "ds_read_b128 v[196:199], v244 offset:28672\n\t"
      "v_mfma_f32_16x16x32_bf16 v[200:203], v[128:131], v[0:3], v[160:163]\n\t"
      "s_waitcnt vmcnt(5)\n\t"
      "v_cmp_eq_u32_e32 vcc, -1, v132\n\t"
      "v_mfma_f32_16x16x32_bf16 v[204:207], v[128:131], v[4:7], 0\n\t"
      "s_waitcnt lgkmcnt(6)\n\t"
      "v_add_f32_e32 v112, v112, v116\n\t"
      "v_mfma_f32_16x16x32_bf16 v[208:211], v[128:131], v[8:11], 0\n\t"
      "s_cbranch_vccnz FP8R_%=\n\t"
      "FP8B_%=:\n\t"
      "v_add_f32_e32 v113, v113, v117\n\t"
      "v_add_f32_e32 v114, v114, v118\n\t"
      "v_mfma_f32_16x16x32_bf16 v[212:215], v[128:131], v[12:15], 0\n\t"
      "global_load_dwordx4 v[128:131], v224, s[56:57] sc1\n\t"
      "v_add_f32_e32 v115, v115, v119\n\t"
      "v_mfma_f32_16x16x32_bf16 v[200:203], v[132:135], v[16:19], v[200:203]\n\t"
      "s_waitcnt vmcnt(5)\n\t"
      "v_cmp_eq_u32_e32 vcc, -1, v136\n\t"
      "s_waitcnt lgkmcnt(4)\n\t"
      "v_add_f32_e32 v120, v120, v124\n\t"
      "v_mfma_f32_16x16x32_bf16 v[204:207], v[132:135], v[20:23], v[204:207]\n\t"
      "v_add_f32_e32 v121, v121, v125\n\t"
      "v_add_f32_e32 v122, v122, v126\n\t"
      "v_mfma_f32_16x16x32_bf16 v[208:211], v[132:135], v[24:27], v[208:211]\n\t"
      "s_cbranch_vccnz FP9R_%=\n\t"
      "FP9B_%=:\n\t"
      "v_add_f32_e32 v123, v123, v127\n\t"
      "s_waitcnt lgkmcnt(2)\n\t"
      "v_add_f32_e32 v184, v184, v188\n\t"
      "v_mfma_f32_16x16x32_bf16 v[212:215], v[132:135], v[28:31], v[212:215]\n\t"
      "global_load_dwordx4 v[132:135], v225, s[56:57] sc1\n\t"
      "v_add_f32_e32 v185, v185, v189\n\t"
      "v_mfma_f32_16x16x32_bf16 v[200:203], v[136:139], v[32:35], v[200:203]\n\t"
      "s_waitcnt vmcnt(5)\n\t"
      "v_cmp_eq_u32_e32 vcc, -1, v140\n\t"
      "v_add_f32_e32 v186, v186, v190\n\t"
      "v_mfma_f32_16x16x32_bf16 v[204:207], v[136:139], v[36:39], v[204:207]\n\t"
      "v_add_f32_e32 v187, v187, v191\n\t"
      "v_add_f32_e32 v112, v112, v120\n\t"
      "v_mfma_f32_16x16x32_bf16 v[208:211], v[136:139], v[40:43], v[208:211]\n\t"
      "s_cbranch_vccnz FP10R_%=\n\t"
      "FP10B_%=:\n\t"
      "v_add_f32_e32 v113, v113, v121\n\t"
      "v_add_f32_e32 v114, v114, v122\n\t"
      "v_mfma_f32_16x16x32_bf16 v[212:215], v[136:139], v[44:47], v[212:215]\n\t"
      "global_load_dwordx4 v[136:139], v226, s[56:57] sc1\n\t"
      "v_add_f32_e32 v115, v115, v123\n\t"
      "v_mfma_f32_16x16x32_bf16 v[200:203], v[140:143], v[48:51], v[200:203]\n\t"
      "s_waitcnt vmcnt(5)\n\t"
      "v_cmp_eq_u32_e32 vcc, -1, v144\n\t"
      "s_waitcnt lgkmcnt(0)\n\t"
      "v_add_f32_e32 v192, v192, v196\n\t"
      "v_mfma_f32_16x16x32_bf16 v[204:207], v[140:143], v[52:55], v[204:207]\n\t"
      "v_add_f32_e32 v193, v193, v197\n\t"
      "v_add_f32_e32 v194, v194, v198\n\t"
      "v_mfma_f32_16x16x32_bf16 v[208:211], v[140:143], v[56:59], v[208:211]\n\t"
      "s_cbranch_vccnz FP11R_%=\n\t"
      "FP11B_%=:\n\t"
      "v_add_f32_e32 v195, v195, v199\n\t"
      "v_add_f32_e32 v184, v184, v192\n\t"
      "v_mfma_f32_16x16x32_bf16 v[212:215], v[140:143], v[60:63], v[212:215]\n\t"
      "global_load_dwordx4 v[140:143], v227, s[56:57] sc1\n\t"
      "v_add_f32_e32 v185, v185, v193\n\t"
      "v_mfma_f32_16x16x32_bf16 v[200:203], v[144:147], v[64:67], v[200:203]\n\t"
      "s_waitcnt vmcnt(5)\n\t"
      "v_cmp_eq_u32_e32 vcc, -1, v148\n\t"
      "v_add_f32_e32 v186, v186, v194\n\t"
      "v_mfma_f32_16x16x32_bf16 v[204:207], v[144:147], v[68:71], v[204:207]\n\t"
      "v_add_f32_e32 v187, v187, v195\n\t"
      "v_add_f32_e32 v112, v112, v184\n\t"
      "v_mfma_f32_16x16x32_bf16 v[208:211], v[144:147], v[72:75], v[208:211]\n\t"
      "s_cbranch_vccnz FP12R_%=\n\t"
      "FP12B_%=:\n\t"
      "v_add_f32_e32 v113, v113, v185\n\t"
      "v_add_f32_e32 v114, v114, v186\n\t"
      "v_mfma_f32_16x16x32_bf16 v[212:215], v[144:147], v[76:79], v[212:215]\n\t"
      "global_load_dwordx4 v[144:147], v228, s[56:57] sc1\n\t"
      "v_add_f32_e32 v115, v115, v187\n\t"
      "v_mfma_f32_16x16x32_bf16 v[200:203], v[148:151], v[80:83], v[200:203]\n\t"
      "s_waitcnt vmcnt(5)\n\t"
      "v_cmp_eq_u32_e32 vcc, -1, v152\n\t"
      "v_cvt_pk_bf16_f32 v116, v112, v113\n\t"
      "v_mfma_f32_16x16x32_bf16 v[204:207], v[148:151], v[84:87], v[204:207]\n\t"
      "v_cvt_pk_bf16_f32 v117, v114, v115\n\t"
      "global_store_dwordx2 v245, v[116:117], s[8:9]\n\t"
      "v_mfma_f32_16x16x32_bf16 v[208:211], v[148:151], v[88:91], v[208:211]\n\t"
      "s_cbranch_vccnz FP13R_%=\n\t"
      "FP13B_%=:\n\t"
      "v_mfma_f32_16x16x32_bf16 v[212:215], v[148:151], v[92:95], v[212:215]\n\t"
      "global_load_dwordx4 v[148:151], v229, s[56:57] sc1\n\t"
      "v_mfma_f32_16x16x32_bf16 v[200:203], v[152:155], v[96:99], v[200:203]\n\t"
      "s_waitcnt vmcnt(6)\n\t"
      "v_cmp_eq_u32_e32 vcc, -1, v128\n\t"
      "v_mfma_f32_16x16x32_bf16 v[204:207], v[152:155], v[100:103], v[204:207]\n\t"
      "v_mfma_f32_16x16x32_bf16 v[208:211], v[152:155], v[104:107], v[208:211]\n\t"
      "s_cbranch_vccnz FP14R_%=\n\t"
      "FP14B_%=:\n\t"
      "v_mfma_f32_16x16x32_bf16 v[212:215], v[152:155], v[108:111], v[212:215]\n\t"
      "global_load_dwordx4 v[152:155], v230, s[56:57] sc1\n\t"
      "s_nop 0\n\t"
      "v_cmp_u_f32_e32 vcc, v200, v201\n\t"
      "v_cmp_u_f32_e64 s[48:49], v202, v203\n\t"
      "s_nop 0\n\t"
      "s_or_b64 vcc, vcc, s[48:49]\n\t"
      "s_cbranch_scc1 NP1R_%=\n\t"
      "NP1B_%=:\n\t"
      "ds_write_b128 v243, v[200:203] offset:32768\n\t"
      "ds_write_b128 v220, v[204:207] offset:32768\n\t"
      "ds_write_b128 v221, v[208:211] offset:32768\n\t"
      "ds_write_b128 v222, v[212:215] offset:32768\n\t"
      "s_waitcnt lgkmcnt(0)\n\t"
      "s_barrier\n\t"
      "ds_read_b128 v[112:115], v244 offset:32768\n\t"
      "ds_read_b128 v[116:119], v244 offset:36864\n\t"
      "ds_read_b128 v[120:123], v244 offset:40960\n\t"
      "ds_read_b128 v[124:127], v244 offset:45056\n\t"
      "ds_read_b128 v[200:203], v244 offset:49152\n\t"
      "ds_read_b128 v[204:207], v244 offset:53248\n\t"
      "ds_read_b128 v[208:211], v244 offset:57344\n\t"
      "ds_read_b128 v[212:215], v244 offset:61440\n\t"
      "v_mfma_f32_16x16x32_bf16 v[184:187], v[128:131], v[0:3], v[164:167]\n\t"
      "s_waitcnt vmcnt(6)\n\t"
      "v_cmp_eq_u32_e32 vcc, -1, v132\n\t"
      "v_mfma_f32_16x16x32_bf16 v[188:191], v[128:131], v[4:7], 0\n\t"
      "s_waitcnt lgkmcnt(6)\n\t"
      "v_add_f32_e32 v112, v112, v116\n\t"
      "v_mfma_f32_16x16x32_bf16 v[192:195], v[128:131], v[8:11], 0\n\t"
      "s_cbranch_vccnz FP15R_%=\n\t"
      "FP15B_%=:\n\t"
      "v_add_f32_e32 v113, v113, v117\n\t"
      "v_add_f32_e32 v114, v114, v118\n\t"
      "v_mfma_f32_16x16x32_bf16 v[196:199], v[128:131], v[12:15], 0\n\t"
      "global_load_dwordx4 v[128:131], v224, s[58:59] sc1\n\t"
      "v_add_f32_e32 v115, v115, v119\n\t"
      "v_mfma_f32_16x16x32_bf16 v[184:187], v[132:135], v[16:19], v[184:187]\n\t"
      "s_waitcnt vmcnt(6)\n\t"
      "v_cmp_eq_u32_e32 vcc, -1, v136\n\t"
      "s_waitcnt lgkmcnt(4)\n\t"
      "v_add_f32_e32 v120, v120, v124\n\t"
      "v_mfma_f32_16x16x32_bf16 v[188:191], v[132:135], v[20:23], v[188:191]\n\t"
      "v_add_f32_e32 v121, v121, v125\n\t"
      "v_add_f32_e32 v122, v122, v126\n\t"
      "v_mfma_f32_16x16x32_bf16 v[192:195], v[132:135], v[24:27], v[192:195]\n\t"
      "s_cbranch_vccnz FP16R_%=\n\t"
      "FP16B_%=:\n\t"
      "v_add_f32_e32 v123, v123, v127\n\t"
      "s_waitcnt lgkmcnt(2)\n\t"
      "v_add_f32_e32 v200, v200, v204\n\t"
      "v_mfma_f32_16x16x32_bf16 v[196:199], v[132:135], v[28:31], v[196:199]\n\t"
      "global_load_dwordx4 v[132:135], v225, s[58:59] sc1\n\t"
      "v_add_f32_e32 v201, v201, v205\n\t"
      "v_mfma_f32_16x16x32_bf16 v[184:187], v[136:139], v[32:35], v[184:187]\n\t"
      "s_waitcnt vmcnt(6)\n\t"
      "v_cmp_eq_u32_e32 vcc, -1, v140\n\t"
      "v_add_f32_e32 v202, v202, v206\n\t"
      "v_mfma_f32_16x16x32_bf16 v[188:191], v[136:139], v[36:39], v[188:191]\n\t"
      "v_add_f32_e32 v203, v203, v207\n\t"
      "v_add_f32_e32 v112, v112, v120\n\t"
      "v_mfma_f32_16x16x32_bf16 v[192:195], v[136:139], v[40:43], v[192:195]\n\t"
      "s_cbranch_vccnz FP17R_%=\n\t"
      "FP17B_%=:\n\t"
      "v_add_f32_e32 v113, v113, v121\n\t"
      "v_add_f32_e32 v114, v114, v122\n\t"
      "v_mfma_f32_16x16x32_bf16 v[196:199], v[136:139], v[44:47], v[196:199]\n\t"
      "global_load_dwordx4 v[136:139], v226, s[58:59] sc1\n\t"
      "v_add_f32_e32 v115, v115, v123\n\t"
      "v_mfma_f32_16x16x32_bf16 v[184:187], v[140:143], v[48:51], v[184:187]\n\t"
      "s_waitcnt vmcnt(6)\n\t"
      "v_cmp_eq_u32_e32 vcc, -1, v144\n\t"
      "s_waitcnt lgkmcnt(0)\n\t"
      "v_add_f32_e32 v208, v208, v212\n\t"
      "v_mfma_f32_16x16x32_bf16 v[188:191], v[140:143], v[52:55], v[188:191]\n\t"
      "v_add_f32_e32 v209, v209, v213\n\t"
      "v_add_f32_e32 v210, v210, v214\n\t"
      "v_mfma_f32_16x16x32_bf16 v[192:195], v[140:143], v[56:59], v[192:195]\n\t"
      "s_cbranch_vccnz FP18R_%=\n\t"
      "FP18B_%=:\n\t"
      "v_add_f32_e32 v211, v211, v215\n\t"
      "v_add_f32_e32 v200, v200, v208\n\t"
      "v_mfma_f32_16x16x32_bf16 v[196:199], v[140:143], v[60:63], v[196:199]\n\t"
      "global_load_dwordx4 v[140:143], v227, s[58:59] sc1\n\t"
      "v_add_f32_e32 v201, v201, v209\n\t"
      "v_mfma_f32_16x16x32_bf16 v[184:187], v[144:147], v[64:67], v[184:187]\n\t"
      "s_waitcnt vmcnt(5)\n\t"
      "v_cmp_eq_u32_e32 vcc, -1, v148\n\t"
      "v_add_f32_e32 v202, v202, v210\n\t"
      "v_mfma_f32_16x16x32_bf16 v[188:191], v[144:147], v[68:71], v[188:191]\n\t"
      "v_add_f32_e32 v203, v203, v211\n\t"
      "v_add_f32_e32 v112, v112, v200\n\t"
      "v_mfma_f32_16x16x32_bf16 v[192:195], v[144:147], v[72:75], v[192:195]\n\t"
      "s_cbranch_vccnz FP19R_%=\n\t"
      "FP19B_%=:\n\t"
      "v_add_f32_e32 v113, v113, v201\n\t"
      "v_add_f32_e32 v114, v114, v202\n\t"
      "v_mfma_f32_16x16x32_bf16 v[196:199], v[144:147], v[76:79], v[196:199]\n\t"
      "global_load_dwordx4 v[144:147], v228, s[58:59] sc1\n\t"
      "v_add_f32_e32 v115, v115, v203\n\t"
      "v_mfma_f32_16x16x32_bf16 v[184:187], v[148:151], v[80:83], v[184:187]\n\t"
      "s_waitcnt vmcnt(5)\n\t"
      "v_cmp_eq_u32_e32 vcc, -1, v152\n\t"
      "v_cvt_pk_bf16_f32 v116, v112, v113\n\t"
      "v_mfma_f32_16x16x32_bf16 v[188:191], v[148:151], v[84:87], v[188:191]\n\t"
      "v_cvt_pk_bf16_f32 v117, v114, v115\n\t"
      "global_store_dwordx2 v245, v[116:117], s[8:9] offset:32\n\t"
      "v_mfma_f32_16x16x32_bf16 v[192:195], v[148:151], v[88:91], v[192:195]\n\t"
      "s_cbranch_vccnz FP20R_%=\n\t"
      "FP20B_%=:\n\t"
      "v_mfma_f32_16x16x32_bf16 v[196:199], v[148:151], v[92:95], v[196:199]\n\t"
      "global_load_dwordx4 v[148:151], v229, s[58:59] sc1\n\t"
      "v_mfma_f32_16x16x32_bf16 v[184:187], v[152:155], v[96:99], v[184:187]\n\t"
      "s_waitcnt vmcnt(6)\n\t"
      "v_cmp_eq_u32_e32 vcc, -1, v128\n\t"
      "v_mfma_f32_16x16x32_bf16 v[188:191], v[152:155], v[100:103], v[188:191]\n\t"
      "v_mfma_f32_16x16x32_bf16 v[192:195], v[152:155], v[104:107], v[192:195]\n\t"
      "s_cbranch_vccnz FP21R_%=\n\t"
      "FP21B_%=:\n\t"
      "v_mfma_f32_16x16x32_bf16 v[196:199], v[152:155], v[108:111], v[196:199]\n\t"
      "global_load_dwordx4 v[152:155], v230, s[58:59] sc1\n\t"
      "s_nop 0\n\t"
      "v_cmp_u_f32_e32 vcc, v184, v185\n\t"
      "v_cmp_u_f32_e64 s[48:49], v186, v187\n\t"
      "s_nop 0\n\t"
      "s_or_b64 vcc, vcc, s[48:49]\n\t"
      "s_cbranch_scc1 NP2R_%=\n\t"
      "NP2B_%=:\n\t"
      "ds_write_b128 v243, v[184:187]\n\t"
      "ds_write_b128 v220, v[188:191]\n\t"
      "ds_write_b128 v221, v[192:195]\n\t"
      "ds_write_b128 v222, v[196:199]\n\t"
      "s_waitcnt lgkmcnt(0)\n\t"
      "s_barrier\n\t"
      "ds_read_b128 v[112:115], v244\n\t"
      "ds_read_b128 v[116:119], v244 offset:4096\n\t"
      "ds_read_b128 v[120:123], v244 offset:8192\n\t"
      "ds_read_b128 v[124:127], v244 offset:12288\n\t"
      "ds_read_b128 v[184:187], v244 offset:16384\n\t"
      "ds_read_b128 v[188:191], v244 offset:20480\n\t"
      "ds_read_b128 v[192:195], v244 offset:24576\n\t"
      "ds_read_b128 v[196:199], v244 offset:28672\n\t"
      "v_mfma_f32_16x16x32_bf16 v[200:203], v[128:131], v[0:3], v[168:171]\n\t"
      "s_waitcnt vmcnt(6)\n\t"
      "v_cmp_eq_u32_e32 vcc, -1, v132\n\t"
      "v_mfma_f32_16x16x32_bf16 v[204:207], v[128:131], v[4:7], 0\n\t"
      "s_waitcnt lgkmcnt(6)\n\t"
      "v_add_f32_e32 v112, v112, v116\n\t"
      "v_mfma_f32_16x16x32_bf16 v[208:211], v[128:131], v[8:11], 0\n\t"
      "s_cbranch_vccnz FP22R_%=\n\t"
      "FP22B_%=:\n\t"
      "v_add_f32_e32 v113, v113, v117\n\t"
      "v_add_f32_e32 v114, v114, v118\n\t"
      "v_mfma_f32_16x16x32_bf16 v[212:215], v[128:131], v[12:15], 0\n\t"
      "global_load_dwordx4 v[128:131], v224, s[60:61] sc1\n\t"
      "v_add_f32_e32 v115, v115, v119\n\t"
      "v_mfma_f32_16x16x32_bf16 v[200:203], v[132:135], v[16:19], v[200:203]\n\t"
      "s_waitcnt vmcnt(6)\n\t"
      "v_cmp_eq_u32_e32 vcc, -1, v136\n\t"
      "s_waitcnt lgkmcnt(4)\n\t"
      "v_add_f32_e32 v120, v120, v124\n\t"
      "v_mfma_f32_16x16x32_bf16 v[204:207], v[132:135], v[20:23], v[204:207]\n\t"
      "v_add_f32_e32 v121, v121, v125\n\t"
      "v_add_f32_e32 v122, v122, v126\n\t"
      "v_mfma_f32_16x16x32_bf16 v[208:211], v[132:135], v[24:27], v[208:211]\n\t"
      "s_cbranch_vccnz FP23R_%=\n\t"
      "FP23B_%=:\n\t"
      "v_add_f32_e32 v123, v123, v127\n\t"
      "s_waitcnt lgkmcnt(2)\n\t"
      "v_add_f32_e32 v184, v184, v188\n\t"
      "v_mfma_f32_16x16x32_bf16 v[212:215], v[132:135], v[28:31], v[212:215]\n\t"
      "global_load_dwordx4 v[132:135], v225, s[60:61] sc1\n\t"
      "v_add_f32_e32 v185, v185, v189\n\t"
      "v_mfma_f32_16x16x32_bf16 v[200:203], v[136:139], v[32:35], v[200:203]\n\t"
      "s_waitcnt vmcnt(6)\n\t"
      "v_cmp_eq_u32_e32 vcc, -1, v140\n\t"
      "v_add_f32_e32 v186, v186, v190\n\t"
      "v_mfma_f32_16x16x32_bf16 v[204:207], v[136:139], v[36:39], v[204:207]\n\t"
      "v_add_f32_e32 v187, v187, v191\n\t"
      "v_add_f32_e32 v112, v112, v120\n\t"
      "v_mfma_f32_16x16x32_bf16 v[208:211], v[136:139], v[40:43], v[208:211]\n\t"
      "s_cbranch_vccnz FP24R_%=\n\t"
      "FP24B_%=:\n\t"
      "v_add_f32_e32 v113, v113, v121\n\t"
      "v_add_f32_e32 v114, v114, v122\n\t"
      "v_mfma_f32_16x16x32_bf16 v[212:215], v[136:139], v[44:47], v[212:215]\n\t"
      "global_load_dwordx4 v[136:139], v226, s[60:61] sc1\n\t"
      "v_add_f32_e32 v115, v115, v123\n\t"
      "v_mfma_f32_16x16x32_bf16 v[200:203], v[140:143], v[48:51], v[200:203]\n\t"
      "s_waitcnt vmcnt(6)\n\t"
      "v_cmp_eq_u32_e32 vcc, -1, v144\n\t"
      "s_waitcnt lgkmcnt(0)\n\t"
      "v_add_f32_e32 v192, v192, v196\n\t"
      "v_mfma_f32_16x16x32_bf16 v[204:207], v[140:143], v[52:55], v[204:207]\n\t"
      "v_add_f32_e32 v193, v193, v197\n\t"
      "v_add_f32_e32 v194, v194, v198\n\t"
      "v_mfma_f32_16x16x32_bf16 v[208:211], v[140:143], v[56:59], v[208:211]\n\t"
      "s_cbranch_vccnz FP25R_%=\n\t"
      "FP25B_%=:\n\t"
      "v_add_f32_e32 v195, v195, v199\n\t"
      "v_add_f32_e32 v184, v184, v192\n\t"
      "v_mfma_f32_16x16x32_bf16 v[212:215], v[140:143], v[60:63], v[212:215]\n\t"
      "global_load_dwordx4 v[140:143], v227, s[60:61] sc1\n\t"
      "v_add_f32_e32 v185, v185, v193\n\t"
      "v_mfma_f32_16x16x32_bf16 v[200:203], v[144:147], v[64:67], v[200:203]\n\t"
      "s_waitcnt vmcnt(5)\n\t"
      "v_cmp_eq_u32_e32 vcc, -1, v148\n\t"
      "v_add_f32_e32 v186, v186, v194\n\t"
      "v_mfma_f32_16x16x32_bf16 v[204:207], v[144:147], v[68:71], v[204:207]\n\t"
      "v_add_f32_e32 v187, v187, v195\n\t"
      "v_add_f32_e32 v112, v112, v184\n\t"
      "v_mfma_f32_16x16x32_bf16 v[208:211], v[144:147], v[72:75], v[208:211]\n\t"
      "s_cbranch_vccnz FP26R_%=\n\t"
      "FP26B_%=:\n\t"
      "v_add_f32_e32 v113, v113, v185\n\t"
      "v_add_f32_e32 v114, v114, v186\n\t"
      "v_mfma_f32_16x16x32_bf16 v[212:215], v[144:147], v[76:79], v[212:215]\n\t"
      "global_load_dwordx4 v[144:147], v228, s[60:61] sc1\n\t"
      "v_add_f32_e32 v115, v115, v187\n\t"
      "v_mfma_f32_16x16x32_bf16 v[200:203], v[148:151], v[80:83], v[200:203]\n\t"
      "s_waitcnt vmcnt(5)\n\t"
      "v_cmp_eq_u32_e32 vcc, -1, v152\n\t"
      "v_cvt_pk_bf16_f32 v116, v112, v113\n\t"
      "v_mfma_f32_16x16x32_bf16 v[204:207], v[148:151], v[84:87], v[204:207]\n\t"
      "v_cvt_pk_bf16_f32 v117, v114, v115\n\t"
      "global_store_dwordx2 v245, v[116:117], s[66:67]\n\t"
      "v_mfma_f32_16x16x32_bf16 v[208:211], v[148:151], v[88:91], v[208:211]\n\t"
      "s_cbranch_vccnz FP27R_%=\n\t"
      "FP27B_%=:\n\t"
      "v_mfma_f32_16x16x32_bf16 v[212:215], v[148:151], v[92:95], v[212:215]\n\t"
      "global_load_dwordx4 v[148:151], v229, s[60:61] sc1\n\t"
      "v_mfma_f32_16x16x32_bf16 v[200:203], v[152:155], v[96:99], v[200:203]\n\t"
      "s_waitcnt vmcnt(6)\n\t"
      "v_cmp_eq_u32_e32 vcc, -1, v128\n\t"
      "v_mfma_f32_16x16x32_bf16 v[204:207], v[152:155], v[100:103], v[204:207]\n\t"
      "v_mfma_f32_16x16x32_bf16 v[208:211], v[152:155], v[104:107], v[208:211]\n\t"
      "s_cbranch_vccnz FP28R_%=\n\t"
      "FP28B_%=:\n\t"
      "v_mfma_f32_16x16x32_bf16 v[212:215], v[152:155], v[108:111], v[212:215]\n\t"
      "global_load_dwordx4 v[152:155], v230, s[60:61] sc1\n\t"
      "s_nop 0\n\t"
      "v_cmp_u_f32_e32 vcc, v200, v201\n\t"
      "v_cmp_u_f32_e64 s[48:49], v202, v203\n\t"
      "s_nop 0\n\t"
      "s_or_b64 vcc, vcc, s[48:49]\n\t"
      "s_cbranch_scc1 NP3R_%=\n\t"
      "NP3B_%=:\n\t"
      "ds_write_b128 v243, v[200:203] offset:32768\n\t"
      "ds_write_b128 v220, v[204:207] offset:32768\n\t"
      "ds_write_b128 v221, v[208:211] offset:32768\n\t"
      "ds_write_b128 v222, v[212:215] offset:32768\n\t"
      "s_waitcnt lgkmcnt(0)\n\t"
      "s_barrier\n\t"
      "ds_read_b128 v[112:115], v244 offset:32768\n\t"
      "ds_read_b128 v[116:119], v244 offset:36864\n\t"
      "ds_read_b128 v[120:123], v244 offset:40960\n\t"
      "ds_read_b128 v[124:127], v244 offset:45056\n\t"
      "ds_read_b128 v[200:203], v244 offset:49152\n\t"
      "ds_read_b128 v[204:207], v244 offset:53248\n\t"
      "ds_read_b128 v[208:211], v244 offset:57344\n\t"
      "ds_read_b128 v[212:215], v244 offset:61440\n\t"
      "v_mfma_f32_16x16x32_bf16 v[184:187], v[128:131], v[0:3], v[172:175]\n\t"
      "s_waitcnt vmcnt(6)\n\t"
      "v_cmp_eq_u32_e32 vcc, -1, v132\n\t"
      "v_mfma_f32_16x16x32_bf16 v[188:191], v[128:131], v[4:7], 0\n\t"
      "s_waitcnt lgkmcnt(6)\n\t"
      "v_add_f32_e32 v112, v112, v116\n\t"
      "v_mfma_f32_16x16x32_bf16 v[192:195], v[128:131], v[8:11], 0\n\t"
      "s_cbranch_vccnz FP29R_%=\n\t"
      "FP29B_%=:\n\t"
      "v_add_f32_e32 v113, v113, v117\n\t"
      "v_add_f32_e32 v114, v114, v118\n\t"
      "v_mfma_f32_16x16x32_bf16 v[196:199], v[128:131], v[12:15], 0\n\t"
      "global_load_dwordx4 v[128:131], v224, s[62:63] sc1\n\t"
      "v_add_f32_e32 v115, v115, v119\n\t"
      "v_mfma_f32_16x16x32_bf16 v[184:187], v[132:135], v[16:19], v[184:187]\n\t"
      "s_waitcnt vmcnt(6)\n\t"
      "v_cmp_eq_u32_e32 vcc, -1, v136\n\t"
      "s_waitcnt lgkmcnt(4)\n\t"
      "v_add_f32_e32 v120, v120, v124\n\t"
      "v_mfma_f32_16x16x32_bf16 v[188:191], v[132:135], v[20:23], v[188:191]\n\t"
      "v_add_f32_e32 v121, v121, v125\n\t"
      "v_add_f32_e32 v122, v122, v126\n\t"
      "v_mfma_f32_16x16x32_bf16 v[192:195], v[132:135], v[24:27], v[192:195]\n\t"
      "s_cbranch_vccnz FP30R_%=\n\t"
      "FP30B_%=:\n\t"
      "v_add_f32_e32 v123, v123, v127\n\t"
      "s_waitcnt lgkmcnt(2)\n\t"
      "v_add_f32_e32 v200, v200, v204\n\t"
      "v_mfma_f32_16x16x32_bf16 v[196:199], v[132:135], v[28:31], v[196:199]\n\t"
      "global_load_dwordx4 v[132:135], v225, s[62:63] sc1\n\t"
      "v_add_f32_e32 v201, v201, v205\n\t"
      "v_mfma_f32_16x16x32_bf16 v[184:187], v[136:139], v[32:35], v[184:187]\n\t"
      "s_waitcnt vmcnt(6)\n\t"
      "v_cmp_eq_u32_e32 vcc, -1, v140\n\t"
      "v_add_f32_e32 v202, v202, v206\n\t"
      "v_mfma_f32_16x16x32_bf16 v[188:191], v[136:139], v[36:39], v[188:191]\n\t"
      "v_add_f32_e32 v203, v203, v207\n\t"
      "v_add_f32_e32 v112, v112, v120\n\t"
      "v_mfma_f32_16x16x32_bf16 v[192:195], v[136:139], v[40:43], v[192:195]\n\t"
      "s_cbranch_vccnz FP31R_%=\n\t"
      "FP31B_%=:\n\t"
      "v_add_f32_e32 v113, v113, v121\n\t"
      "v_add_f32_e32 v114, v114, v122\n\t"
      "v_mfma_f32_16x16x32_bf16 v[196:199], v[136:139], v[44:47], v[196:199]\n\t"
      "global_load_dwordx4 v[136:139], v226, s[62:63] sc1\n\t"
      "v_add_f32_e32 v115, v115, v123\n\t"
      "v_mfma_f32_16x16x32_bf16 v[184:187], v[140:143], v[48:51], v[184:187]\n\t"
      "s_waitcnt vmcnt(6)\n\t"
      "v_cmp_eq_u32_e32 vcc, -1, v144\n\t"
      "s_waitcnt lgkmcnt(0)\n\t"
      "v_add_f32_e32 v208, v208, v212\n\t"
      "v_mfma_f32_16x16x32_bf16 v[188:191], v[140:143], v[52:55], v[188:191]\n\t"
      "v_add_f32_e32 v209, v209, v213\n\t"
      "v_add_f32_e32 v210, v210, v214\n\t"
      "v_mfma_f32_16x16x32_bf16 v[192:195], v[140:143], v[56:59], v[192:195]\n\t"
      "s_cbranch_vccnz FP32R_%=\n\t"
      "FP32B_%=:\n\t"
      "v_add_f32_e32 v211, v211, v215\n\t"
      "v_add_f32_e32 v200, v200, v208\n\t"
      "v_mfma_f32_16x16x32_bf16 v[196:199], v[140:143], v[60:63], v[196:199]\n\t"
      "global_load_dwordx4 v[140:143], v227, s[62:63] sc1\n\t"
      "v_add_f32_e32 v201, v201, v209\n\t"
      "v_mfma_f32_16x16x32_bf16 v[184:187], v[144:147], v[64:67], v[184:187]\n\t"
      "s_waitcnt vmcnt(5)\n\t"
      "v_cmp_eq_u32_e32 vcc, -1, v148\n\t"
      "v_add_f32_e32 v202, v202, v210\n\t"
      "v_mfma_f32_16x16x32_bf16 v[188:191], v[144:147], v[68:71], v[188:191]\n\t"
      "v_add_f32_e32 v203, v203, v211\n\t"
      "v_add_f32_e32 v112, v112, v200\n\t"
      "v_mfma_f32_16x16x32_bf16 v[192:195], v[144:147], v[72:75], v[192:195]\n\t"
      "s_cbranch_vccnz FP33R_%=\n\t"
      "FP33B_%=:\n\t"
      "v_add_f32_e32 v113, v113, v201\n\t"
      "v_add_f32_e32 v114, v114, v202\n\t"
      "v_mfma_f32_16x16x32_bf16 v[196:199], v[144:147], v[76:79], v[196:199]\n\t"
      "global_load_dwordx4 v[144:147], v228, s[62:63] sc1\n\t"
      "v_add_f32_e32 v115, v115, v203\n\t"
      "v_mfma_f32_16x16x32_bf16 v[184:187], v[148:151], v[80:83], v[184:187]\n\t"
      "s_waitcnt vmcnt(5)\n\t"
      "v_cmp_eq_u32_e32 vcc, -1, v152\n\t"
      "v_cvt_pk_bf16_f32 v116, v112, v113\n\t"
      "v_mfma_f32_16x16x32_bf16 v[188:191], v[148:151], v[84:87], v[188:191]\n\t"
      "v_cvt_pk_bf16_f32 v117, v114, v115\n\t"
      "global_store_dwordx2 v245, v[116:117], s[66:67] offset:32\n\t"
      "v_mfma_f32_16x16x32_bf16 v[192:195], v[148:151], v[88:91], v[192:195]\n\t"
      "s_cbranch_vccnz FP34R_%=\n\t"
      "FP34B_%=:\n\t"
      "v_mfma_f32_16x16x32_bf16 v[196:199], v[148:151], v[92:95], v[196:199]\n\t"
      "global_load_dwordx4 v[148:151], v229, s[62:63] sc1\n\t"
      "v_mfma_f32_16x16x32_bf16 v[184:187], v[152:155], v[96:99], v[184:187]\n\t"
      "s_waitcnt vmcnt(6)\n\t"
      "v_cmp_eq_u32_e32 vcc, -1, v128\n\t"
      "v_mfma_f32_16x16x32_bf16 v[188:191], v[152:155], v[100:103], v[188:191]\n\t"
      "v_mfma_f32_16x16x32_bf16 v[192:195], v[152:155], v[104:107], v[192:195]\n\t"
      "s_cbranch_vccnz FP35R_%=\n\t"
      "FP35B_%=:\n\t"
      "v_mfma_f32_16x16x32_bf16 v[196:199], v[152:155], v[108:111], v[196:199]\n\t"
      "global_load_dwordx4 v[152:155], v230, s[62:63] sc1\n\t"
      "s_nop 0\n\t"
      "v_cmp_u_f32_e32 vcc, v184, v185\n\t"
      "v_cmp_u_f32_e64 s[48:49], v186, v187\n\t"
      "s_nop 0\n\t"
      "s_or_b64 vcc, vcc, s[48:49]\n\t"
      "s_cbranch_scc1 NP4R_%=\n\t"
      "NP4B_%=:\n\t"
      "ds_write_b128 v243, v[184:187]\n\t"
      "ds_write_b128 v220, v[188:191]\n\t"
      "ds_write_b128 v221, v[192:195]\n\t"
      "ds_write_b128 v222, v[196:199]\n\t"
      "s_waitcnt lgkmcnt(0)\n\t"
      "s_barrier\n\t"
      "ds_read_b128 v[112:115], v244\n\t"
      "ds_read_b128 v[116:119], v244 offset:4096\n\t"
      "ds_read_b128 v[120:123], v244 offset:8192\n\t"
      "ds_read_b128 v[124:127], v244 offset:12288\n\t"
      "ds_read_b128 v[184:187], v244 offset:16384\n\t"
      "ds_read_b128 v[188:191], v244 offset:20480\n\t"
      "ds_read_b128 v[192:195], v244 offset:24576\n\t"
      "ds_read_b128 v[196:199], v244 offset:28672\n\t"
      "v_mfma_f32_16x16x32_bf16 v[200:203], v[128:131], v[0:3], v[176:179]\n\t"
      "s_waitcnt vmcnt(6)\n\t"
      "v_cmp_eq_u32_e32 vcc, -1, v132\n\t"
      "v_mfma_f32_16x16x32_bf16 v[204:207], v[128:131], v[4:7], 0\n\t"
      "s_waitcnt lgkmcnt(6)\n\t"
      "v_add_f32_e32 v112, v112, v116\n\t"
      "v_mfma_f32_16x16x32_bf16 v[208:211], v[128:131], v[8:11], 0\n\t"
      "s_cbranch_vccnz FP36R_%=\n\t"
      "FP36B_%=:\n\t"
      "v_add_f32_e32 v113, v113, v117\n\t"
      "v_add_f32_e32 v114, v114, v118\n\t"
      "v_mfma_f32_16x16x32_bf16 v[212:215], v[128:131], v[12:15], 0\n\t"
      "global_load_dwordx4 v[128:131], v224, s[64:65] sc1\n\t"
      "v_add_f32_e32 v115, v115, v119\n\t"
      "v_mfma_f32_16x16x32_bf16 v[200:203], v[132:135], v[16:19], v[200:203]\n\t"
      "s_waitcnt vmcnt(6)\n\t"
      "v_cmp_eq_u32_e32 vcc, -1, v136\n\t"
      "s_waitcnt lgkmcnt(4)\n\t"
      "v_add_f32_e32 v120, v120, v124\n\t"
      "v_mfma_f32_16x16x32_bf16 v[204:207], v[132:135], v[20:23], v[204:207]\n\t"
      "v_add_f32_e32 v121, v121, v125\n\t"
      "v_add_f32_e32 v122, v122, v126\n\t"
      "v_mfma_f32_16x16x32_bf16 v[208:211], v[132:135], v[24:27], v[208:211]\n\t"
      "s_cbranch_vccnz FP37R_%=\n\t"
      "FP37B_%=:\n\t"
      "v_add_f32_e32 v123, v123, v127\n\t"
      "s_waitcnt lgkmcnt(2)\n\t"
      "v_add_f32_e32 v184, v184, v188\n\t"
      "v_mfma_f32_16x16x32_bf16 v[212:215], v[132:135], v[28:31], v[212:215]\n\t"
      "global_load_dwordx4 v[132:135], v225, s[64:65] sc1\n\t"
      "v_add_f32_e32 v185, v185, v189\n\t"
      "v_mfma_f32_16x16x32_bf16 v[200:203], v[136:139], v[32:35], v[200:203]\n\t"
      "s_waitcnt vmcnt(6)\n\t"
      "v_cmp_eq_u32_e32 vcc, -1, v140\n\t"
      "v_add_f32_e32 v186, v186, v190\n\t"
      "v_mfma_f32_16x16x32_bf16 v[204:207], v[136:139], v[36:39], v[204:207]\n\t"
      "v_add_f32_e32 v187, v187, v191\n\t"
      "v_add_f32_e32 v112, v112, v120\n\t"
      "v_mfma_f32_16x16x32_bf16 v[208:211], v[136:139], v[40:43], v[208:211]\n\t"
      "s_cbranch_vccnz FP38R_%=\n\t"
      "FP38B_%=:\n\t"
      "v_add_f32_e32 v113, v113, v121\n\t"
      "v_add_f32_e32 v114, v114, v122\n\t"
      "v_mfma_f32_16x16x32_bf16 v[212:215], v[136:139], v[44:47], v[212:215]\n\t"
      "global_load_dwordx4 v[136:139], v226, s[64:65] sc1\n\t"
      "v_add_f32_e32 v115, v115, v123\n\t"
      "v_mfma_f32_16x16x32_bf16 v[200:203], v[140:143], v[48:51], v[200:203]\n\t"
      "s_waitcnt vmcnt(6)\n\t"
      "v_cmp_eq_u32_e32 vcc, -1, v144\n\t"
      "s_waitcnt lgkmcnt(0)\n\t"
      "v_add_f32_e32 v192, v192, v196\n\t"
      "v_mfma_f32_16x16x32_bf16 v[204:207], v[140:143], v[52:55], v[204:207]\n\t"
      "v_add_f32_e32 v193, v193, v197\n\t"
      "v_add_f32_e32 v194, v194, v198\n\t"
      "v_mfma_f32_16x16x32_bf16 v[208:211], v[140:143], v[56:59], v[208:211]\n\t"
      "s_cbranch_vccnz FP39R_%=\n\t"
      "FP39B_%=:\n\t"
      "v_add_f32_e32 v195, v195, v199\n\t"
      "v_add_f32_e32 v184, v184, v192\n\t"
      "v_mfma_f32_16x16x32_bf16 v[212:215], v[140:143], v[60:63], v[212:215]\n\t"
      "global_load_dwordx4 v[140:143], v227, s[64:65] sc1\n\t"
      "v_add_f32_e32 v185, v185, v193\n\t"
      "v_mfma_f32_16x16x32_bf16 v[200:203], v[144:147], v[64:67], v[200:203]\n\t"
      "s_waitcnt vmcnt(5)\n\t"
      "v_cmp_eq_u32_e32 vcc, -1, v148\n\t"
      "v_add_f32_e32 v186, v186, v194\n\t"
      "v_mfma_f32_16x16x32_bf16 v[204:207], v[144:147], v[68:71], v[204:207]\n\t"
      "v_add_f32_e32 v187, v187, v195\n\t"
      "v_add_f32_e32 v112, v112, v184\n\t"
      "v_mfma_f32_16x16x32_bf16 v[208:211], v[144:147], v[72:75], v[208:211]\n\t"
      "s_cbranch_vccnz FP40R_%=\n\t"
      "FP40B_%=:\n\t"
      "v_add_f32_e32 v113, v113, v185\n\t"
      "v_add_f32_e32 v114, v114, v186\n\t"
      "v_mfma_f32_16x16x32_bf16 v[212:215], v[144:147], v[76:79], v[212:215]\n\t"
      "global_load_dwordx4 v[144:147], v228, s[64:65] sc1\n\t"
      "v_add_f32_e32 v115, v115, v187\n\t"
      "v_mfma_f32_16x16x32_bf16 v[200:203], v[148:151], v[80:83], v[200:203]\n\t"
      "s_waitcnt vmcnt(5)\n\t"
      "v_cmp_eq_u32_e32 vcc, -1, v152\n\t"
      "v_cvt_pk_bf16_f32 v116, v112, v113\n\t"
      "v_mfma_f32_16x16x32_bf16 v[204:207], v[148:151], v[84:87], v[204:207]\n\t"
      "v_cvt_pk_bf16_f32 v117, v114, v115\n\t"
      "global_store_dwordx2 v245, v[116:117], s[8:9] offset:64\n\t"
      "v_mfma_f32_16x16x32_bf16 v[208:211], v[148:151], v[88:91], v[208:211]\n\t"
      "s_cbranch_vccnz FP41R_%=\n\t"
      "FP41B_%=:\n\t"
      "v_mfma_f32_16x16x32_bf16 v[212:215], v[148:151], v[92:95], v[212:215]\n\t"
      "global_load_dwordx4 v[148:151], v229, s[64:65] sc1\n\t"
      "v_mfma_f32_16x16x32_bf16 v[200:203], v[152:155], v[96:99], v[200:203]\n\t"
      "s_waitcnt vmcnt(6)\n\t"
      "v_cmp_eq_u32_e32 vcc, -1, v128\n\t"
      "v_mfma_f32_16x16x32_bf16 v[204:207], v[152:155], v[100:103], v[204:207]\n\t"
      "v_mfma_f32_16x16x32_bf16 v[208:211], v[152:155], v[104:107], v[208:211]\n\t"
      "s_cbranch_vccnz FP42R_%=\n\t"
      "FP42B_%=:\n\t"
      "v_mfma_f32_16x16x32_bf16 v[212:215], v[152:155], v[108:111], v[212:215]\n\t"
      "global_load_dwordx4 v[152:155], v230, s[64:65] sc1\n\t"
      "s_nop 0\n\t"
      "v_cmp_u_f32_e32 vcc, v200, v201\n\t"
      "v_cmp_u_f32_e64 s[48:49], v202, v203\n\t"
      "s_nop 0\n\t"
      "s_or_b64 vcc, vcc, s[48:49]\n\t"
      "s_cbranch_scc1 NP5R_%=\n\t"
      "NP5B_%=:\n\t"
      "ds_write_b128 v243, v[200:203] offset:32768\n\t"
      "ds_write_b128 v220, v[204:207] offset:32768\n\t"
      "ds_write_b128 v221, v[208:211] offset:32768\n\t"
      "ds_write_b128 v222, v[212:215] offset:32768\n\t"
      "s_waitcnt lgkmcnt(0)\n\t"
      "s_barrier\n\t"
      "ds_read_b128 v[112:115], v244 offset:32768\n\t"
      "ds_read_b128 v[116:119], v244 offset:36864\n\t"
      "ds_read_b128 v[120:123], v244 offset:40960\n\t"
      "ds_read_b128 v[124:127], v244 offset:45056\n\t"
      "ds_read_b128 v[200:203], v244 offset:49152\n\t"
      "ds_read_b128 v[204:207], v244 offset:53248\n\t"
      "ds_read_b128 v[208:211], v244 offset:57344\n\t"
      "ds_read_b128 v[212:215], v244 offset:61440\n\t"
      "v_mfma_f32_16x16x32_bf16 v[184:187], v[128:131], v[0:3], v[180:183]\n\t"
      "s_waitcnt vmcnt(6)\n\t"
      "v_cmp_eq_u32_e32 vcc, -1, v132\n\t"
      "v_mfma_f32_16x16x32_bf16 v[188:191], v[128:131], v[4:7], 0\n\t"
      "v_mfma_f32_16x16x32_bf16 v[192:195], v[128:131], v[8:11], 0\n\t"
      "s_cbranch_vccnz FP43R_%=\n\t"
      "FP43B_%=:\n\t"
      "v_mfma_f32_16x16x32_bf16 v[196:199], v[128:131], v[12:15], 0\n\t"
      "v_mfma_f32_16x16x32_bf16 v[184:187], v[132:135], v[16:19], v[184:187]\n\t"
      "s_waitcnt vmcnt(5)\n\t"
      "v_cmp_eq_u32_e32 vcc, -1, v136\n\t"
      "s_waitcnt lgkmcnt(6)\n\t"
      "v_add_f32_e32 v112, v112, v116\n\t"
      "v_mfma_f32_16x16x32_bf16 v[188:191], v[132:135], v[20:23], v[188:191]\n\t"
      "v_add_f32_e32 v113, v113, v117\n\t"
      "v_add_f32_e32 v114, v114, v118\n\t"
      "v_mfma_f32_16x16x32_bf16 v[192:195], v[132:135], v[24:27], v[192:195]\n\t"
      "s_cbranch_vccnz FP44R_%=\n\t"
      "FP44B_%=:\n\t"
      "v_add_f32_e32 v115, v115, v119\n\t"
      "s_waitcnt lgkmcnt(4)\n\t"
      "v_add_f32_e32 v120, v120, v124\n\t"
      "v_mfma_f32_16x16x32_bf16 v[196:199], v[132:135], v[28:31], v[196:199]\n\t"
      "v_add_f32_e32 v121, v121, v125\n\t"
      "v_add_f32_e32 v122, v122, v126\n\t"
      "v_mfma_f32_16x16x32_bf16 v[184:187], v[136:139], v[32:35], v[184:187]\n\t"
      "s_waitcnt vmcnt(4)\n\t"
      "v_cmp_eq_u32_e32 vcc, -1, v140\n\t"
      "v_add_f32_e32 v123, v123, v127\n\t"
      "v_mfma_f32_16x16x32_bf16 v[188:191], v[136:139], v[36:39], v[188:191]\n\t"
      "s_waitcnt lgkmcnt(2)\n\t"
      "v_add_f32_e32 v200, v200, v204\n\t"
      "v_add_f32_e32 v201, v201, v205\n\t"
      "v_mfma_f32_16x16x32_bf16 v[192:195], v[136:139], v[40:43], v[192:195]\n\t"
      "s_cbranch_vccnz FP45R_%=\n\t"
      "FP45B_%=:\n\t"
      "v_add_f32_e32 v202, v202, v206\n\t"
      "v_add_f32_e32 v203, v203, v207\n\t"
      "v_mfma_f32_16x16x32_bf16 v[196:199], v[136:139], v[44:47], v[196:199]\n\t"
      "v_add_f32_e32 v112, v112, v120\n\t"
      "v_add_f32_e32 v113, v113, v121\n\t"
      "v_mfma_f32_16x16x32_bf16 v[184:187], v[140:143], v[48:51], v[184:187]\n\t"
      "s_waitcnt vmcnt(3)\n\t"
      "v_cmp_eq_u32_e32 vcc, -1, v144\n\t"
      "v_add_f32_e32 v114, v114, v122\n\t"
      "v_mfma_f32_16x16x32_bf16 v[188:191], v[140:143], v[52:55], v[188:191]\n\t"
      "v_add_f32_e32 v115, v115, v123\n\t"
      "s_waitcnt lgkmcnt(0)\n\t"
      "v_add_f32_e32 v208, v208, v212\n\t"
      "v_mfma_f32_16x16x32_bf16 v[192:195], v[140:143], v[56:59], v[192:195]\n\t"
      "s_cbranch_vccnz FP46R_%=\n\t"
      "FP46B_%=:\n\t"
      "v_add_f32_e32 v209, v209, v213\n\t"
      "v_add_f32_e32 v210, v210, v214\n\t"
      "v_mfma_f32_16x16x32_bf16 v[196:199], v[140:143], v[60:63], v[196:199]\n\t"
      "v_add_f32_e32 v211, v211, v215\n\t"
      "v_add_f32_e32 v200, v200, v208\n\t"
      "v_mfma_f32_16x16x32_bf16 v[184:187], v[144:147], v[64:67], v[184:187]\n\t"
      "s_waitcnt vmcnt(1)\n\t"
      "v_cmp_eq_u32_e32 vcc, -1, v148\n\t"
      "v_add_f32_e32 v201, v201, v209\n\t"
      "v_mfma_f32_16x16x32_bf16 v[188:191], v[144:147], v[68:71], v[188:191]\n\t"
      "v_add_f32_e32 v202, v202, v210\n\t"
      "v_add_f32_e32 v203, v203, v211\n\t"
      "v_mfma_f32_16x16x32_bf16 v[192:195], v[144:147], v[72:75], v[192:195]\n\t"
      "s_cbranch_vccnz FP47R_%=\n\t"
      "FP47B_%=:\n\t"
      "v_add_f32_e32 v112, v112, v200\n\t"
      "v_add_f32_e32 v113, v113, v201\n\t"
      "v_mfma_f32_16x16x32_bf16 v[196:199], v[144:147], v[76:79], v[196:199]\n\t"
      "v_add_f32_e32 v114, v114, v202\n\t"
      "v_add_f32_e32 v115, v115, v203\n\t"
      "v_mfma_f32_16x16x32_bf16 v[184:187], v[148:151], v[80:83], v[184:187]\n\t"
      "s_waitcnt vmcnt(0)\n\t"
      "v_cmp_eq_u32_e32 vcc, -1, v152\n\t"
      "v_cvt_pk_bf16_f32 v116, v112, v113\n\t"
      "v_mfma_f32_16x16x32_bf16 v[188:191], v[148:151], v[84:87], v[188:191]\n\t"
      "v_cvt_pk_bf16_f32 v117, v114, v115\n\t"
      "global_store_dwordx2 v245, v[116:117], s[8:9] offset:96\n\t"
      "v_mfma_f32_16x16x32_bf16 v[192:195], v[148:151], v[88:91], v[192:195]\n\t"
      "s_cbranch_vccnz FP48R_%=\n\t"
      "FP48B_%=:\n\t"
      "v_mfma_f32_16x16x32_bf16 v[196:199], v[148:151], v[92:95], v[196:199]\n\t"
      "v_mfma_f32_16x16x32_bf16 v[184:187], v[152:155], v[96:99], v[184:187]\n\t"
      "v_mfma_f32_16x16x32_bf16 v[188:191], v[152:155], v[100:103], v[188:191]\n\t"
      "v_mfma_f32_16x16x32_bf16 v[192:195], v[152:155], v[104:107], v[192:195]\n\t"
      "v_mfma_f32_16x16x32_bf16 v[196:199], v[152:155], v[108:111], v[196:199]\n\t"
      "s_nop 4\n\t"
      "v_cmp_u_f32_e32 vcc, v184, v185\n\t"
      "v_cmp_u_f32_e64 s[48:49], v186, v187\n\t"
      "s_nop 0\n\t"
      "s_or_b64 vcc, vcc, s[48:49]\n\t"
      "s_cbranch_scc1 NP6R_%=\n\t"
      "NP6B_%=:\n\t"
      "ds_write_b128 v243, v[184:187]\n\t"
      "ds_write_b128 v220, v[188:191]\n\t"
      "ds_write_b128 v221, v[192:195]\n\t"
      "ds_write_b128 v222, v[196:199]\n\t"
      "s_waitcnt lgkmcnt(0)\n\t"
      "s_barrier\n\t"
      "ds_read_b128 v[112:115], v244\n\t"
      "ds_read_b128 v[116:119], v244 offset:4096\n\t"
      "ds_read_b128 v[120:123], v244 offset:8192\n\t"
      "ds_read_b128 v[124:127], v244 offset:12288\n\t"
      "ds_read_b128 v[184:187], v244 offset:16384\n\t"
      "ds_read_b128 v[188:191], v244 offset:20480\n\t"
      "ds_read_b128 v[192:195], v244 offset:24576\n\t"
      "ds_read_b128 v[196:199], v244 offset:28672\n\t"
      "s_waitcnt lgkmcnt(6)\n\t"
      "v_add_f32_e32 v112, v112, v116\n\t"
      "v_add_f32_e32 v113, v113, v117\n\t"
      "v_add_f32_e32 v114, v114, v118\n\t"
      "v_add_f32_e32 v115, v115, v119\n\t"
      "s_waitcnt lgkmcnt(4)\n\t"
      "v_add_f32_e32 v120, v120, v124\n\t"
      "v_add_f32_e32 v121, v121, v125\n\t"
      "v_add_f32_e32 v122, v122, v126\n\t"
      "v_add_f32_e32 v123, v123, v127\n\t"
      "s_waitcnt lgkmcnt(2)\n\t"
      "v_add_f32_e32 v184, v184, v188\n\t"
      "v_add_f32_e32 v185, v185, v189\n\t"
      "v_add_f32_e32 v186, v186, v190\n\t"
      "v_add_f32_e32 v187, v187, v191\n\t"
      "v_add_f32_e32 v112, v112, v120\n\t"
      "v_add_f32_e32 v113, v113, v121\n\t"
      "v_add_f32_e32 v114, v114, v122\n\t"
      "v_add_f32_e32 v115, v115, v123\n\t"
      "s_waitcnt lgkmcnt(0)\n\t"
      "v_add_f32_e32 v192, v192, v196\n\t"
      "v_add_f32_e32 v193, v193, v197\n\t"
      "v_add_f32_e32 v194, v194, v198\n\t"
      "v_add_f32_e32 v195, v195, v199\n\t"
      "v_add_f32_e32 v184, v184, v192\n\t"
      "v_add_f32_e32 v185, v185, v193\n\t"
      "v_add_f32_e32 v186, v186, v194\n\t"
      "v_add_f32_e32 v187, v187, v195\n\t"
      "v_add_f32_e32 v112, v112, v184\n\t"
      "v_add_f32_e32 v113, v113, v185\n\t"
      "v_add_f32_e32 v114, v114, v186\n\t"
      "v_add_f32_e32 v115, v115, v187\n\t"
      "v_cvt_pk_bf16_f32 v116, v112, v113\n\t"
      "v_cvt_pk_bf16_f32 v117, v114, v115\n\t"
      "global_store_dwordx2 v245, v[116:117], s[66:67] offset:64\n\t"
      "GX_%=:\n\t"
      "s_bitcmp1_b32 s16, 5\n\t"
      "s_cbranch_scc0 EP_%=\n\t"
      "v_mov_b32_e32 v0, s26\n\t"
      "v_mov_b32_e32 v1, s27\n\t"
      "v_mov_b32_e32 v2, 0\n\t"
      "s_mov_b64 exec, 1\n\t"
      "global_store_dwordx2 v2, v[0:1], s[24:25] sc1\n\t"
      "s_mov_b64 exec, -1\n\t"
      "EP_%=:\n\t"
      "s_endpgm\n\t"
      ".p2align 6\n\t"
      "GQ_%=:\n\t"
      "s_waitcnt vmcnt(6)\n\t"
      "v_max3_u32 v234, v128, v129, v130\n\t"
      "v_max_u32_e32 v234, v234, v131\n\t"
      "v_cmp_eq_u32_e32 vcc, -1, v234\n\t"
      "s_cbranch_vccnz FQ0R_%=\n\t"
      "FQ0B_%=:\n\t"
      "v_mfma_f32_16x16x32_bf16 v[184:187], v[128:131], v[0:3], 0\n\t"
      "v_mfma_f32_16x16x32_bf16 v[188:191], v[128:131], v[4:7], 0\n\t"
      "v_mfma_f32_16x16x32_bf16 v[192:195], v[128:131], v[8:11], 0\n\t"
      "v_mfma_f32_16x16x32_bf16 v[196:199], v[128:131], v[12:15], 0\n\t"
      "global_load_dwordx4 v[128:131], v224, s[54:55] sc1\n\t"
      "s_waitcnt vmcnt(6)\n\t"
      "v_max3_u32 v234, v132, v133, v134\n\t"
      "v_max_u32_e32 v234, v234, v135\n\t"
      "v_cmp_eq_u32_e32 vcc, -1, v234\n\t"
      "s_cbranch_vccnz FQ1R_%=\n\t"
      "FQ1B_%=:\n\t"
      "v_mfma_f32_16x16x32_bf16 v[184:187], v[132:135], v[16:19], v[184:187]\n\t"
      "v_mfma_f32_16x16x32_bf16 v[188:191], v[132:135], v[20:23], v[188:191]\n\t"
      "v_mfma_f32_16x16x32_bf16 v[192:195], v[132:135], v[24:27], v[192:195]\n\t"
      "v_mfma_f32_16x16x32_bf16 v[196:199], v[132:135], v[28:31], v[196:199]\n\t"
      "global_load_dwordx4 v[132:135], v225, s[54:55] sc1\n\t"
      "s_waitcnt vmcnt(6)\n\t"
      "v_max3_u32 v234, v136, v137, v138\n\t"
      "v_max_u32_e32 v234, v234, v139\n\t"
      "v_cmp_eq_u32_e32 vcc, -1, v234\n\t"
      "s_cbranch_vccnz FQ2R_%=\n\t"
      "FQ2B_%=:\n\t"
      "v_mfma_f32_16x16x32_bf16 v[184:187], v[136:139], v[32:35], v[184:187]\n\t"
      "v_mfma_f32_16x16x32_bf16 v[188:191], v[136:139], v[36:39], v[188:191]\n\t"
      "v_mfma_f32_16x16x32_bf16 v[192:195], v[136:139], v[40:43], v[192:195]\n\t"
      "v_mfma_f32_16x16x32_bf16 v[196:199], v[136:139], v[44:47], v[196:199]\n\t"
      "global_load_dwordx4 v[136:139], v226, s[54:55] sc1\n\t"
      "s_waitcnt vmcnt(6)\n\t"
      "v_max3_u32 v234, v140, v141, v142\n\t"
      "v_max_u32_e32 v234, v234, v143\n\t"
      "v_cmp_eq_u32_e32 vcc, -1, v234\n\t"
      "s_cbranch_vccnz FQ3R_%=\n\t"
      "FQ3B_%=:\n\t"
      "v_mfma_f32_16x16x32_bf16 v[184:187], v[140:143], v[48:51], v[184:187]\n\t"
      "v_mfma_f32_16x16x32_bf16 v[188:191], v[140:143], v[52:55], v[188:191]\n\t"
      "v_mfma_f32_16x16x32_bf16 v[192:195], v[140:143], v[56:59], v[192:195]\n\t"
      "v_mfma_f32_16x16x32_bf16 v[196:199], v[140:143], v[60:63], v[196:199]\n\t"
      "global_load_dwordx4 v[140:143], v227, s[54:55] sc1\n\t"
      "s_waitcnt vmcnt(6)\n\t"
      "v_max3_u32 v234, v144, v145, v146\n\t"
      "v_max_u32_e32 v234, v234, v147\n\t"
      "v_cmp_eq_u32_e32 vcc, -1, v234\n\t"
      "s_cbranch_vccnz FQ4R_%=\n\t"
      "FQ4B_%=:\n\t"
      "v_mfma_f32_16x16x32_bf16 v[184:187], v[144:147], v[64:67], v[184:187]\n\t"
      "v_mfma_f32_16x16x32_bf16 v[188:191], v[144:147], v[68:71], v[188:191]\n\t"
      "v_mfma_f32_16x16x32_bf16 v[192:195], v[144:147], v[72:75], v[192:195]\n\t"
      "v_mfma_f32_16x16x32_bf16 v[196:199], v[144:147], v[76:79], v[196:199]\n\t"
      "global_load_dwordx4 v[144:147], v228, s[54:55] sc1\n\t"
      "s_waitcnt vmcnt(6)\n\t"
      "v_max3_u32 v234, v148, v149, v150\n\t"
      "v_max_u32_e32 v234, v234, v151\n\t"
      "v_cmp_eq_u32_e32 vcc, -1, v234\n\t"
      "s_cbranch_vccnz FQ5R_%=\n\t"
      "FQ5B_%=:\n\t"
      "v_mfma_f32_16x16x32_bf16 v[184:187], v[148:151], v[80:83], v[184:187]\n\t"
      "v_mfma_f32_16x16x32_bf16 v[188:191], v[148:151], v[84:87], v[188:191]\n\t"
      "v_mfma_f32_16x16x32_bf16 v[192:195], v[148:151], v[88:91], v[192:195]\n\t"
      "v_mfma_f32_16x16x32_bf16 v[196:199], v[148:151], v[92:95], v[196:199]\n\t"
      "global_load_dwordx4 v[148:151], v229, s[54:55] sc1\n\t"
      "s_waitcnt vmcnt(6)\n\t"
      "v_max3_u32 v234, v152, v153, v154\n\t"
      "v_max_u32_e32 v234, v234, v155\n\t"
      "v_cmp_eq_u32_e32 vcc, -1, v234\n\t"
      "s_cbranch_vccnz FQ6R_%=\n\t"
      "FQ6B_%=:\n\t"
      "v_mfma_f32_16x16x32_bf16 v[184:187], v[152:155], v[96:99], v[184:187]\n\t"
      "s_waitcnt vmcnt(5)\n\t"
      "v_cmp_eq_u32_e32 vcc, -1, v128\n\t"
      "v_mfma_f32_16x16x32_bf16 v[188:191], v[152:155], v[100:103], v[188:191]\n\t"
      "v_mfma_f32_16x16x32_bf16 v[192:195], v[152:155], v[104:107], v[192:195]\n\t"
      "s_cbranch_vccnz FQ7R_%=\n\t"
      "FQ7B_%=:\n\t"
      "v_mfma_f32_16x16x32_bf16 v[196:199], v[152:155], v[108:111], v[196:199]\n\t"
      "global_load_dwordx4 v[152:155], v230, s[54:55] sc1\n\t"
      "s_nop 0\n\t"
      "ds_write_b128 v243, v[184:187]\n\t"
      "s_nop 1\n\t"
      "ds_write_b128 v220, v[188:191]\n\t"
      "ds_write_b128 v221, v[192:195]\n\t"
      "s_nop 0\n\t"
      "ds_write_b128 v222, v[196:199]\n\t"
      "v_mfma_f32_16x16x32_bf16 v[200:203], v[128:131], v[0:3], 0\n\t"
      "s_waitcnt vmcnt(5)\n\t"
      "v_cmp_eq_u32_e32 vcc, -1, v132\n\t"
      "v_mfma_f32_16x16x32_bf16 v[204:207], v[128:131], v[4:7], 0\n\t"
      "v_mfma_f32_16x16x32_bf16 v[208:211], v[128:131], v[8:11], 0\n\t"
      "s_cbranch_vccnz FQ8R_%=\n\t"
      "FQ8B_%=:\n\t"
      "v_mfma_f32_16x16x32_bf16 v[212:215], v[128:131], v[12:15], 0\n\t"
      "global_load_dwordx4 v[128:131], v224, s[56:57] sc1\n\t"
      "v_mfma_f32_16x16x32_bf16 v[200:203], v[132:135], v[16:19], v[200:203]\n\t"
      "s_waitcnt vmcnt(5)\n\t"
      "v_cmp_eq_u32_e32 vcc, -1, v136\n\t"
      "v_mfma_f32_16x16x32_bf16 v[204:207], v[132:135], v[20:23], v[204:207]\n\t"
      "v_mfma_f32_16x16x32_bf16 v[208:211], v[132:135], v[24:27], v[208:211]\n\t"
      "s_cbranch_vccnz FQ9R_%=\n\t"
      "FQ9B_%=:\n\t"
      "v_mfma_f32_16x16x32_bf16 v[212:215], v[132:135], v[28:31], v[212:215]\n\t"
      "global_load_dwordx4 v[132:135], v225, s[56:57] sc1\n\t"
      "v_mfma_f32_16x16x32_bf16 v[200:203], v[136:139], v[32:35], v[200:203]\n\t"
      "s_waitcnt vmcnt(5)\n\t"
      "v_cmp_eq_u32_e32 vcc, -1, v140\n\t"
      "v_mfma_f32_16x16x32_bf16 v[204:207], v[136:139], v[36:39], v[204:207]\n\t"
      "v_mfma_f32_16x16x32_bf16 v[208:211], v[136:139], v[40:43], v[208:211]\n\t"
      "s_cbranch_vccnz FQ10R_%=\n\t"
      "FQ10B_%=:\n\t"
      "v_mfma_f32_16x16x32_bf16 v[212:215], v[136:139], v[44:47], v[212:215]\n\t"
      "global_load_dwordx4 v[136:139], v226, s[56:57] sc1\n\t"
      "v_mfma_f32_16x16x32_bf16 v[200:203], v[140:143], v[48:51], v[200:203]\n\t"
      "s_waitcnt vmcnt(5)\n\t"
      "v_cmp_eq_u32_e32 vcc, -1, v144\n\t"
      "v_mfma_f32_16x16x32_bf16 v[204:207], v[140:143], v[52:55], v[204:207]\n\t"
      "v_mfma_f32_16x16x32_bf16 v[208:211], v[140:143], v[56:59], v[208:211]\n\t"
      "s_cbranch_vccnz FQ11R_%=\n\t"
      "FQ11B_%=:\n\t"
      "v_mfma_f32_16x16x32_bf16 v[212:215], v[140:143], v[60:63], v[212:215]\n\t"
      "global_load_dwordx4 v[140:143], v227, s[56:57] sc1\n\t"
      "v_mfma_f32_16x16x32_bf16 v[200:203], v[144:147], v[64:67], v[200:203]\n\t"
      "s_waitcnt vmcnt(5)\n\t"
      "v_cmp_eq_u32_e32 vcc, -1, v148\n\t"
      "v_mfma_f32_16x16x32_bf16 v[204:207], v[144:147], v[68:71], v[204:207]\n\t"
      "v_mfma_f32_16x16x32_bf16 v[208:211], v[144:147], v[72:75], v[208:211]\n\t"
      "s_cbranch_vccnz FQ12R_%=\n\t"
      "FQ12B_%=:\n\t"
      "v_mfma_f32_16x16x32_bf16 v[212:215], v[144:147], v[76:79], v[212:215]\n\t"
      "global_load_dwordx4 v[144:147], v228, s[56:57] sc1\n\t"
      "s_waitcnt lgkmcnt(0)\n\t"
      "s_barrier\n\t"
      "s_setprio 2\n\t"
      "v_mfma_f32_16x16x32_bf16 v[200:203], v[148:151], v[80:83], v[200:203]\n\t"
      "s_waitcnt vmcnt(5)\n\t"
      "v_cmp_eq_u32_e32 vcc, -1, v152\n\t"
      "v_mfma_f32_16x16x32_bf16 v[204:207], v[148:151], v[84:87], v[204:207]\n\t"
      "v_mfma_f32_16x16x32_bf16 v[208:211], v[148:151], v[88:91], v[208:211]\n\t"
      "s_cbranch_vccnz FQ13R_%=\n\t"
      "FQ13B_%=:\n\t"
      "v_mfma_f32_16x16x32_bf16 v[212:215], v[148:151], v[92:95], v[212:215]\n\t"
      "global_load_dwordx4 v[148:151], v229, s[56:57] sc1\n\t"
      "v_mfma_f32_16x16x32_bf16 v[200:203], v[152:155], v[96:99], v[200:203]\n\t"
      "s_waitcnt vmcnt(5)\n\t"
      "v_cmp_eq_u32_e32 vcc, -1, v128\n\t"
      "v_mfma_f32_16x16x32_bf16 v[204:207], v[152:155], v[100:103], v[204:207]\n\t"
      "v_mfma_f32_16x16x32_bf16 v[208:211], v[152:155], v[104:107], v[208:211]\n\t"
      "s_cbranch_vccnz FQ14R_%=\n\t"
      "FQ14B_%=:\n\t"
      "v_mfma_f32_16x16x32_bf16 v[212:215], v[152:155], v[108:111], v[212:215]\n\t"
      "global_load_dwordx4 v[152:155], v230, s[56:57] sc1\n\t"
      "s_setprio 0\n\t"
      "v_cmp_u_f32_e32 vcc, v200, v201\n\t"
      "v_cmp_u_f32_e64 s[48:49], v202, v203\n\t"
      "s_nop 0\n\t"
      "s_or_b64 vcc, vcc, s[48:49]\n\t"
      "s_cbranch_scc1 NQ1R_%=\n\t"
      "NQ1B_%=:\n\t"
      "ds_write_b128 v243, v[200:203] offset:32768\n\t"
      "ds_write_b128 v220, v[204:207] offset:32768\n\t"
      "ds_write_b128 v221, v[208:211] offset:32768\n\t"
      "ds_write_b128 v222, v[212:215] offset:32768\n\t"
      "v_mfma_f32_16x16x32_bf16 v[184:187], v[128:131], v[0:3], 0\n\t"
      "s_waitcnt vmcnt(5)\n\t"
      "v_cmp_eq_u32_e32 vcc, -1, v132\n\t"
      "v_mfma_f32_16x16x32_bf16 v[188:191], v[128:131], v[4:7], 0\n\t"
      "v_mfma_f32_16x16x32_bf16 v[192:195], v[128:131], v[8:11], 0\n\t"
      "s_cbranch_vccnz FQ15R_%=\n\t"
      "FQ15B_%=:\n\t"
      "v_mfma_f32_16x16x32_bf16 v[196:199], v[128:131], v[12:15], 0\n\t"
      "global_load_dwordx4 v[128:131], v224, s[58:59] sc1\n\t"
      "v_mfma_f32_16x16x32_bf16 v[184:187], v[132:135], v[16:19], v[184:187]\n\t"
      "s_waitcnt vmcnt(5)\n\t"
      "v_cmp_eq_u32_e32 vcc, -1, v136\n\t"
      "v_mfma_f32_16x16x32_bf16 v[188:191], v[132:135], v[20:23], v[188:191]\n\t"
      "v_mfma_f32_16x16x32_bf16 v[192:195], v[132:135], v[24:27], v[192:195]\n\t"
      "s_cbranch_vccnz FQ16R_%=\n\t"
      "FQ16B_%=:\n\t"
      "v_mfma_f32_16x16x32_bf16 v[196:199], v[132:135], v[28:31], v[196:199]\n\t"
      "global_load_dwordx4 v[132:135], v225, s[58:59] sc1\n\t"
      "v_mfma_f32_16x16x32_bf16 v[184:187], v[136:139], v[32:35], v[184:187]\n\t"
      "s_waitcnt vmcnt(5)\n\t"
      "v_cmp_eq_u32_e32 vcc, -1, v140\n\t"
      "v_mfma_f32_16x16x32_bf16 v[188:191], v[136:139], v[36:39], v[188:191]\n\t"
      "v_mfma_f32_16x16x32_bf16 v[192:195], v[136:139], v[40:43], v[192:195]\n\t"
      "s_cbranch_vccnz FQ17R_%=\n\t"
      "FQ17B_%=:\n\t"
      "v_mfma_f32_16x16x32_bf16 v[196:199], v[136:139], v[44:47], v[196:199]\n\t"
      "global_load_dwordx4 v[136:139], v226, s[58:59] sc1\n\t"
      "v_mfma_f32_16x16x32_bf16 v[184:187], v[140:143], v[48:51], v[184:187]\n\t"
      "s_waitcnt vmcnt(5)\n\t"
      "v_cmp_eq_u32_e32 vcc, -1, v144\n\t"
      "v_mfma_f32_16x16x32_bf16 v[188:191], v[140:143], v[52:55], v[188:191]\n\t"
      "v_mfma_f32_16x16x32_bf16 v[192:195], v[140:143], v[56:59], v[192:195]\n\t"
      "s_cbranch_vccnz FQ18R_%=\n\t"
      "FQ18B_%=:\n\t"
      "v_mfma_f32_16x16x32_bf16 v[196:199], v[140:143], v[60:63], v[196:199]\n\t"
      "global_load_dwordx4 v[140:143], v227, s[58:59] sc1\n\t"
      "v_mfma_f32_16x16x32_bf16 v[184:187], v[144:147], v[64:67], v[184:187]\n\t"
      "s_waitcnt vmcnt(5)\n\t"
      "v_cmp_eq_u32_e32 vcc, -1, v148\n\t"
      "v_mfma_f32_16x16x32_bf16 v[188:191], v[144:147], v[68:71], v[188:191]\n\t"
      "v_mfma_f32_16x16x32_bf16 v[192:195], v[144:147], v[72:75], v[192:195]\n\t"
      "s_cbranch_vccnz FQ19R_%=\n\t"
      "FQ19B_%=:\n\t"
      "v_mfma_f32_16x16x32_bf16 v[196:199], v[144:147], v[76:79], v[196:199]\n\t"
      "global_load_dwordx4 v[144:147], v228, s[58:59] sc1\n\t"
      "s_waitcnt lgkmcnt(0)\n\t"
      "s_barrier\n\t"
      "s_setprio 2\n\t"
      "v_mfma_f32_16x16x32_bf16 v[184:187], v[148:151], v[80:83], v[184:187]\n\t"
      "s_waitcnt vmcnt(5)\n\t"
      "v_cmp_eq_u32_e32 vcc, -1, v152\n\t"
      "v_mfma_f32_16x16x32_bf16 v[188:191], v[148:151], v[84:87], v[188:191]\n\t"
      "v_mfma_f32_16x16x32_bf16 v[192:195], v[148:151], v[88:91], v[192:195]\n\t"
      "s_cbranch_vccnz FQ20R_%=\n\t"
      "FQ20B_%=:\n\t"
      "v_mfma_f32_16x16x32_bf16 v[196:199], v[148:151], v[92:95], v[196:199]\n\t"
      "global_load_dwordx4 v[148:151], v229, s[58:59] sc1\n\t"
      "v_mfma_f32_16x16x32_bf16 v[184:187], v[152:155], v[96:99], v[184:187]\n\t"
      "s_waitcnt vmcnt(5)\n\t"
      "v_cmp_eq_u32_e32 vcc, -1, v128\n\t"
      "v_mfma_f32_16x16x32_bf16 v[188:191], v[152:155], v[100:103], v[188:191]\n\t"
      "v_mfma_f32_16x16x32_bf16 v[192:195], v[152:155], v[104:107], v[192:195]\n\t"
      "s_cbranch_vccnz FQ21R_%=\n\t"
      "FQ21B_%=:\n\t"
      "v_mfma_f32_16x16x32_bf16 v[196:199], v[152:155], v[108:111], v[196:199]\n\t"
      "global_load_dwordx4 v[152:155], v230, s[58:59] sc1\n\t"
      "s_setprio 0\n\t"
      "v_cmp_u_f32_e32 vcc, v184, v185\n\t"
      "v_cmp_u_f32_e64 s[48:49], v186, v187\n\t"
      "s_nop 0\n\t"
      "s_or_b64 vcc, vcc, s[48:49]\n\t"
      "s_cbranch_scc1 NQ2R_%=\n\t"
      "NQ2B_%=:\n\t"
      "ds_write_b128 v243, v[184:187]\n\t"
      "ds_write_b128 v220, v[188:191]\n\t"
      "ds_write_b128 v221, v[192:195]\n\t"
      "ds_write_b128 v222, v[196:199]\n\t"
      "v_mfma_f32_16x16x32_bf16 v[200:203], v[128:131], v[0:3], 0\n\t"
      "s_waitcnt vmcnt(5)\n\t"
      "v_cmp_eq_u32_e32 vcc, -1, v132\n\t"
      "v_mfma_f32_16x16x32_bf16 v[204:207], v[128:131], v[4:7], 0\n\t"
      "v_mfma_f32_16x16x32_bf16 v[208:211], v[128:131], v[8:11], 0\n\t"
      "s_cbranch_vccnz FQ22R_%=\n\t"
      "FQ22B_%=:\n\t"
      "v_mfma_f32_16x16x32_bf16 v[212:215], v[128:131], v[12:15], 0\n\t"
      "global_load_dwordx4 v[128:131], v224, s[60:61] sc1\n\t"
      "v_mfma_f32_16x16x32_bf16 v[200:203], v[132:135], v[16:19], v[200:203]\n\t"
      "s_waitcnt vmcnt(5)\n\t"
      "v_cmp_eq_u32_e32 vcc, -1, v136\n\t"
      "v_mfma_f32_16x16x32_bf16 v[204:207], v[132:135], v[20:23], v[204:207]\n\t"
      "v_mfma_f32_16x16x32_bf16 v[208:211], v[132:135], v[24:27], v[208:211]\n\t"
      "s_cbranch_vccnz FQ23R_%=\n\t"
      "FQ23B_%=:\n\t"
      "v_mfma_f32_16x16x32_bf16 v[212:215], v[132:135], v[28:31], v[212:215]\n\t"
      "global_load_dwordx4 v[132:135], v225, s[60:61] sc1\n\t"
      "v_mfma_f32_16x16x32_bf16 v[200:203], v[136:139], v[32:35], v[200:203]\n\t"
      "s_waitcnt vmcnt(5)\n\t"
      "v_cmp_eq_u32_e32 vcc, -1, v140\n\t"
      "v_mfma_f32_16x16x32_bf16 v[204:207], v[136:139], v[36:39], v[204:207]\n\t"
      "v_mfma_f32_16x16x32_bf16 v[208:211], v[136:139], v[40:43], v[208:211]\n\t"
      "s_cbranch_vccnz FQ24R_%=\n\t"
      "FQ24B_%=:\n\t"
      "v_mfma_f32_16x16x32_bf16 v[212:215], v[136:139], v[44:47], v[212:215]\n\t"
      "global_load_dwordx4 v[136:139], v226, s[60:61] sc1\n\t"
      "v_mfma_f32_16x16x32_bf16 v[200:203], v[140:143], v[48:51], v[200:203]\n\t"
      "s_waitcnt vmcnt(5)\n\t"
      "v_cmp_eq_u32_e32 vcc, -1, v144\n\t"
      "v_mfma_f32_16x16x32_bf16 v[204:207], v[140:143], v[52:55], v[204:207]\n\t"
      "v_mfma_f32_16x16x32_bf16 v[208:211], v[140:143], v[56:59], v[208:211]\n\t"
      "s_cbranch_vccnz FQ25R_%=\n\t"
      "FQ25B_%=:\n\t"
      "v_mfma_f32_16x16x32_bf16 v[212:215], v[140:143], v[60:63], v[212:215]\n\t"
      "global_load_dwordx4 v[140:143], v227, s[60:61] sc1\n\t"
      "v_mfma_f32_16x16x32_bf16 v[200:203], v[144:147], v[64:67], v[200:203]\n\t"
      "s_waitcnt vmcnt(5)\n\t"
      "v_cmp_eq_u32_e32 vcc, -1, v148\n\t"
      "v_mfma_f32_16x16x32_bf16 v[204:207], v[144:147], v[68:71], v[204:207]\n\t"
      "v_mfma_f32_16x16x32_bf16 v[208:211], v[144:147], v[72:75], v[208:211]\n\t"
      "s_cbranch_vccnz FQ26R_%=\n\t"
      "FQ26B_%=:\n\t"
      "v_mfma_f32_16x16x32_bf16 v[212:215], v[144:147], v[76:79], v[212:215]\n\t"
      "global_load_dwordx4 v[144:147], v228, s[60:61] sc1\n\t"
      "s_waitcnt lgkmcnt(0)\n\t"
      "s_barrier\n\t"
      "s_setprio 2\n\t"
      "v_mfma_f32_16x16x32_bf16 v[200:203], v[148:151], v[80:83], v[200:203]\n\t"
      "s_waitcnt vmcnt(5)\n\t"
      "v_cmp_eq_u32_e32 vcc, -1, v152\n\t"
      "v_mfma_f32_16x16x32_bf16 v[204:207], v[148:151], v[84:87], v[204:207]\n\t"
      "v_mfma_f32_16x16x32_bf16 v[208:211], v[148:151], v[88:91], v[208:211]\n\t"
      "s_cbranch_vccnz FQ27R_%=\n\t"
      "FQ27B_%=:\n\t"
      "v_mfma_f32_16x16x32_bf16 v[212:215], v[148:151], v[92:95], v[212:215]\n\t"
      "global_load_dwordx4 v[148:151], v229, s[60:61] sc1\n\t"
      "v_mfma_f32_16x16x32_bf16 v[200:203], v[152:155], v[96:99], v[200:203]\n\t"
      "s_waitcnt vmcnt(5)\n\t"
      "v_cmp_eq_u32_e32 vcc, -1, v128\n\t"
      "v_mfma_f32_16x16x32_bf16 v[204:207], v[152:155], v[100:103], v[204:207]\n\t"
      "v_mfma_f32_16x16x32_bf16 v[208:211], v[152:155], v[104:107], v[208:211]\n\t"
      "s_cbranch_vccnz FQ28R_%=\n\t"
      "FQ28B_%=:\n\t"
      "v_mfma_f32_16x16x32_bf16 v[212:215], v[152:155], v[108:111], v[212:215]\n\t"
      "global_load_dwordx4 v[152:155], v230, s[60:61] sc1\n\t"
      "s_setprio 0\n\t"
      "v_cmp_u_f32_e32 vcc, v200, v201\n\t"
      "v_cmp_u_f32_e64 s[48:49], v202, v203\n\t"
      "s_nop 0\n\t"
      "s_or_b64 vcc, vcc, s[48:49]\n\t"
      "s_cbranch_scc1 NQ3R_%=\n\t"
      "NQ3B_%=:\n\t"
      "ds_write_b128 v243, v[200:203] offset:32768\n\t"
      "ds_write_b128 v220, v[204:207] offset:32768\n\t"
      "ds_write_b128 v221, v[208:211] offset:32768\n\t"
      "ds_write_b128 v222, v[212:215] offset:32768\n\t"
      "v_mfma_f32_16x16x32_bf16 v[184:187], v[128:131], v[0:3], 0\n\t"
      "s_waitcnt vmcnt(5)\n\t"
      "v_cmp_eq_u32_e32 vcc, -1, v132\n\t"
      "v_mfma_f32_16x16x32_bf16 v[188:191], v[128:131], v[4:7], 0\n\t"
      "v_mfma_f32_16x16x32_bf16 v[192:195], v[128:131], v[8:11], 0\n\t"
      "s_cbranch_vccnz FQ29R_%=\n\t"
      "FQ29B_%=:\n\t"
      "v_mfma_f32_16x16x32_bf16 v[196:199], v[128:131], v[12:15], 0\n\t"
      "global_load_dwordx4 v[128:131], v224, s[62:63] sc1\n\t"
      "v_mfma_f32_16x16x32_bf16 v[184:187], v[132:135], v[16:19], v[184:187]\n\t"
      "s_waitcnt vmcnt(5)\n\t"
      "v_cmp_eq_u32_e32 vcc, -1, v136\n\t"
      "v_mfma_f32_16x16x32_bf16 v[188:191], v[132:135], v[20:23], v[188:191]\n\t"
      "v_mfma_f32_16x16x32_bf16 v[192:195], v[132:135], v[24:27], v[192:195]\n\t"
      "s_cbranch_vccnz FQ30R_%=\n\t"
      "FQ30B_%=:\n\t"
      "v_mfma_f32_16x16x32_bf16 v[196:199], v[132:135], v[28:31], v[196:199]\n\t"
      "global_load_dwordx4 v[132:135], v225, s[62:63] sc1\n\t"
      "v_mfma_f32_16x16x32_bf16 v[184:187], v[136:139], v[32:35], v[184:187]\n\t"
      "s_waitcnt vmcnt(5)\n\t"
      "v_cmp_eq_u32_e32 vcc, -1, v140\n\t"
      "v_mfma_f32_16x16x32_bf16 v[188:191], v[136:139], v[36:39], v[188:191]\n\t"
      "v_mfma_f32_16x16x32_bf16 v[192:195], v[136:139], v[40:43], v[192:195]\n\t"
      "s_cbranch_vccnz FQ31R_%=\n\t"
      "FQ31B_%=:\n\t"
      "v_mfma_f32_16x16x32_bf16 v[196:199], v[136:139], v[44:47], v[196:199]\n\t"
      "global_load_dwordx4 v[136:139], v226, s[62:63] sc1\n\t"
      "v_mfma_f32_16x16x32_bf16 v[184:187], v[140:143], v[48:51], v[184:187]\n\t"
      "s_waitcnt vmcnt(5)\n\t"
      "v_cmp_eq_u32_e32 vcc, -1, v144\n\t"
      "v_mfma_f32_16x16x32_bf16 v[188:191], v[140:143], v[52:55], v[188:191]\n\t"
      "v_mfma_f32_16x16x32_bf16 v[192:195], v[140:143], v[56:59], v[192:195]\n\t"
      "s_cbranch_vccnz FQ32R_%=\n\t"
      "FQ32B_%=:\n\t"
      "v_mfma_f32_16x16x32_bf16 v[196:199], v[140:143], v[60:63], v[196:199]\n\t"
      "global_load_dwordx4 v[140:143], v227, s[62:63] sc1\n\t"
      "v_mfma_f32_16x16x32_bf16 v[184:187], v[144:147], v[64:67], v[184:187]\n\t"
      "s_waitcnt vmcnt(5)\n\t"
      "v_cmp_eq_u32_e32 vcc, -1, v148\n\t"
      "v_mfma_f32_16x16x32_bf16 v[188:191], v[144:147], v[68:71], v[188:191]\n\t"
      "v_mfma_f32_16x16x32_bf16 v[192:195], v[144:147], v[72:75], v[192:195]\n\t"
      "s_cbranch_vccnz FQ33R_%=\n\t"
      "FQ33B_%=:\n\t"
      "v_mfma_f32_16x16x32_bf16 v[196:199], v[144:147], v[76:79], v[196:199]\n\t"
      "global_load_dwordx4 v[144:147], v228, s[62:63] sc1\n\t"
      "s_waitcnt lgkmcnt(0)\n\t"
      "s_barrier\n\t"
      "s_setprio 2\n\t"
      "v_mfma_f32_16x16x32_bf16 v[184:187], v[148:151], v[80:83], v[184:187]\n\t"
      "s_waitcnt vmcnt(5)\n\t"
      "v_cmp_eq_u32_e32 vcc, -1, v152\n\t"
      "v_mfma_f32_16x16x32_bf16 v[188:191], v[148:151], v[84:87], v[188:191]\n\t"
      "v_mfma_f32_16x16x32_bf16 v[192:195], v[148:151], v[88:91], v[192:195]\n\t"
      "s_cbranch_vccnz FQ34R_%=\n\t"
      "FQ34B_%=:\n\t"
      "v_mfma_f32_16x16x32_bf16 v[196:199], v[148:151], v[92:95], v[196:199]\n\t"
      "global_load_dwordx4 v[148:151], v229, s[62:63] sc1\n\t"
      "v_mfma_f32_16x16x32_bf16 v[184:187], v[152:155], v[96:99], v[184:187]\n\t"
      "s_waitcnt vmcnt(5)\n\t"
      "v_cmp_eq_u32_e32 vcc, -1, v128\n\t"
      "v_mfma_f32_16x16x32_bf16 v[188:191], v[152:155], v[100:103], v[188:191]\n\t"
      "v_mfma_f32_16x16x32_bf16 v[192:195], v[152:155], v[104:107], v[192:195]\n\t"
      "s_cbranch_vccnz FQ35R_%=\n\t"
      "FQ35B_%=:\n\t"
      "v_mfma_f32_16x16x32_bf16 v[196:199], v[152:155], v[108:111], v[196:199]\n\t"
      "global_load_dwordx4 v[152:155], v230, s[62:63] sc1\n\t"
      "s_setprio 0\n\t"
      "v_cmp_u_f32_e32 vcc, v184, v185\n\t"
      "v_cmp_u_f32_e64 s[48:49], v186, v187\n\t"
      "s_nop 0\n\t"
      "s_or_b64 vcc, vcc, s[48:49]\n\t"
      "s_cbranch_scc1 NQ4R_%=\n\t"
      "NQ4B_%=:\n\t"
      "ds_write_b128 v243, v[184:187]\n\t"
      "ds_write_b128 v220, v[188:191]\n\t"
      "ds_write_b128 v221, v[192:195]\n\t"
      "ds_write_b128 v222, v[196:199]\n\t"
      "v_mfma_f32_16x16x32_bf16 v[200:203], v[128:131], v[0:3], 0\n\t"
      "s_waitcnt vmcnt(5)\n\t"
      "v_cmp_eq_u32_e32 vcc, -1, v132\n\t"
      "v_mfma_f32_16x16x32_bf16 v[204:207], v[128:131], v[4:7], 0\n\t"
      "v_mfma_f32_16x16x32_bf16 v[208:211], v[128:131], v[8:11], 0\n\t"
      "s_cbranch_vccnz FQ36R_%=\n\t"
      "FQ36B_%=:\n\t"
      "v_mfma_f32_16x16x32_bf16 v[212:215], v[128:131], v[12:15], 0\n\t"
      "global_load_dwordx4 v[128:131], v224, s[64:65] sc1\n\t"
      "v_mfma_f32_16x16x32_bf16 v[200:203], v[132:135], v[16:19], v[200:203]\n\t"
      "s_waitcnt vmcnt(5)\n\t"
      "v_cmp_eq_u32_e32 vcc, -1, v136\n\t"
      "v_mfma_f32_16x16x32_bf16 v[204:207], v[132:135], v[20:23], v[204:207]\n\t"
      "v_mfma_f32_16x16x32_bf16 v[208:211], v[132:135], v[24:27], v[208:211]\n\t"
      "s_cbranch_vccnz FQ37R_%=\n\t"
      "FQ37B_%=:\n\t"
      "v_mfma_f32_16x16x32_bf16 v[212:215], v[132:135], v[28:31], v[212:215]\n\t"
      "global_load_dwordx4 v[132:135], v225, s[64:65] sc1\n\t"
      "v_mfma_f32_16x16x32_bf16 v[200:203], v[136:139], v[32:35], v[200:203]\n\t"
      "s_waitcnt vmcnt(5)\n\t"
      "v_cmp_eq_u32_e32 vcc, -1, v140\n\t"
      "v_mfma_f32_16x16x32_bf16 v[204:207], v[136:139], v[36:39], v[204:207]\n\t"
      "v_mfma_f32_16x16x32_bf16 v[208:211], v[136:139], v[40:43], v[208:211]\n\t"
      "s_cbranch_vccnz FQ38R_%=\n\t"
      "FQ38B_%=:\n\t"
      "v_mfma_f32_16x16x32_bf16 v[212:215], v[136:139], v[44:47], v[212:215]\n\t"
      "global_load_dwordx4 v[136:139], v226, s[64:65] sc1\n\t"
      "v_mfma_f32_16x16x32_bf16 v[200:203], v[140:143], v[48:51], v[200:203]\n\t"
      "s_waitcnt vmcnt(5)\n\t"
      "v_cmp_eq_u32_e32 vcc, -1, v144\n\t"
      "v_mfma_f32_16x16x32_bf16 v[204:207], v[140:143], v[52:55], v[204:207]\n\t"
      "v_mfma_f32_16x16x32_bf16 v[208:211], v[140:143], v[56:59], v[208:211]\n\t"
      "s_cbranch_vccnz FQ39R_%=\n\t"
      "FQ39B_%=:\n\t"
      "v_mfma_f32_16x16x32_bf16 v[212:215], v[140:143], v[60:63], v[212:215]\n\t"
      "global_load_dwordx4 v[140:143], v227, s[64:65] sc1\n\t"
      "v_mfma_f32_16x16x32_bf16 v[200:203], v[144:147], v[64:67], v[200:203]\n\t"
      "s_waitcnt vmcnt(5)\n\t"
      "v_cmp_eq_u32_e32 vcc, -1, v148\n\t"
      "v_mfma_f32_16x16x32_bf16 v[204:207], v[144:147], v[68:71], v[204:207]\n\t"
      "v_mfma_f32_16x16x32_bf16 v[208:211], v[144:147], v[72:75], v[208:211]\n\t"
      "s_cbranch_vccnz FQ40R_%=\n\t"
      "FQ40B_%=:\n\t"
      "v_mfma_f32_16x16x32_bf16 v[212:215], v[144:147], v[76:79], v[212:215]\n\t"
      "global_load_dwordx4 v[144:147], v228, s[64:65] sc1\n\t"
      "s_waitcnt lgkmcnt(0)\n\t"
      "s_barrier\n\t"
      "s_setprio 2\n\t"
      "v_mfma_f32_16x16x32_bf16 v[200:203], v[148:151], v[80:83], v[200:203]\n\t"
      "s_waitcnt vmcnt(5)\n\t"
      "v_cmp_eq_u32_e32 vcc, -1, v152\n\t"
      "v_mfma_f32_16x16x32_bf16 v[204:207], v[148:151], v[84:87], v[204:207]\n\t"
      "v_mfma_f32_16x16x32_bf16 v[208:211], v[148:151], v[88:91], v[208:211]\n\t"
      "s_cbranch_vccnz FQ41R_%=\n\t"
      "FQ41B_%=:\n\t"
      "v_mfma_f32_16x16x32_bf16 v[212:215], v[148:151], v[92:95], v[212:215]\n\t"
      "global_load_dwordx4 v[148:151], v229, s[64:65] sc1\n\t"
      "v_mfma_f32_16x16x32_bf16 v[200:203], v[152:155], v[96:99], v[200:203]\n\t"
      "s_waitcnt vmcnt(5)\n\t"
      "v_cmp_eq_u32_e32 vcc, -1, v128\n\t"
      "v_mfma_f32_16x16x32_bf16 v[204:207], v[152:155], v[100:103], v[204:207]\n\t"
      "v_mfma_f32_16x16x32_bf16 v[208:211], v[152:155], v[104:107], v[208:211]\n\t"
      "s_cbranch_vccnz FQ42R_%=\n\t"
      "FQ42B_%=:\n\t"
      "v_mfma_f32_16x16x32_bf16 v[212:215], v[152:155], v[108:111], v[212:215]\n\t"
      "global_load_dwordx4 v[152:155], v230, s[64:65] sc1\n\t"
      "s_setprio 0\n\t"
      "v_cmp_u_f32_e32 vcc, v200, v201\n\t"
      "v_cmp_u_f32_e64 s[48:49], v202, v203\n\t"
      "s_nop 0\n\t"
      "s_or_b64 vcc, vcc, s[48:49]\n\t"
      "s_cbranch_scc1 NQ5R_%=\n\t"
      "NQ5B_%=:\n\t"
      "ds_write_b128 v243, v[200:203] offset:32768\n\t"
      "ds_write_b128 v220, v[204:207] offset:32768\n\t"
      "ds_write_b128 v221, v[208:211] offset:32768\n\t"
      "ds_write_b128 v222, v[212:215] offset:32768\n\t"
      "v_mfma_f32_16x16x32_bf16 v[184:187], v[128:131], v[0:3], 0\n\t"
      "s_waitcnt vmcnt(5)\n\t"
      "v_cmp_eq_u32_e32 vcc, -1, v132\n\t"
      "v_mfma_f32_16x16x32_bf16 v[188:191], v[128:131], v[4:7], 0\n\t"
      "v_mfma_f32_16x16x32_bf16 v[192:195], v[128:131], v[8:11], 0\n\t"
      "s_cbranch_vccnz FQ43R_%=\n\t"
      "FQ43B_%=:\n\t"
      "v_mfma_f32_16x16x32_bf16 v[196:199], v[128:131], v[12:15], 0\n\t"
      "v_mfma_f32_16x16x32_bf16 v[184:187], v[132:135], v[16:19], v[184:187]\n\t"
      "s_waitcnt vmcnt(4)\n\t"
      "v_cmp_eq_u32_e32 vcc, -1, v136\n\t"
      "v_mfma_f32_16x16x32_bf16 v[188:191], v[132:135], v[20:23], v[188:191]\n\t"
      "v_mfma_f32_16x16x32_bf16 v[192:195], v[132:135], v[24:27], v[192:195]\n\t"
      "s_cbranch_vccnz FQ44R_%=\n\t"
      "FQ44B_%=:\n\t"
      "v_mfma_f32_16x16x32_bf16 v[196:199], v[132:135], v[28:31], v[196:199]\n\t"
      "v_mfma_f32_16x16x32_bf16 v[184:187], v[136:139], v[32:35], v[184:187]\n\t"
      "s_waitcnt vmcnt(3)\n\t"
      "v_cmp_eq_u32_e32 vcc, -1, v140\n\t"
      "v_mfma_f32_16x16x32_bf16 v[188:191], v[136:139], v[36:39], v[188:191]\n\t"
      "v_mfma_f32_16x16x32_bf16 v[192:195], v[136:139], v[40:43], v[192:195]\n\t"
      "s_cbranch_vccnz FQ45R_%=\n\t"
      "FQ45B_%=:\n\t"
      "v_mfma_f32_16x16x32_bf16 v[196:199], v[136:139], v[44:47], v[196:199]\n\t"
      "v_mfma_f32_16x16x32_bf16 v[184:187], v[140:143], v[48:51], v[184:187]\n\t"
      "s_waitcnt vmcnt(2)\n\t"
      "v_cmp_eq_u32_e32 vcc, -1, v144\n\t"
      "v_mfma_f32_16x16x32_bf16 v[188:191], v[140:143], v[52:55], v[188:191]\n\t"
      "v_mfma_f32_16x16x32_bf16 v[192:195], v[140:143], v[56:59], v[192:195]\n\t"
      "s_cbranch_vccnz FQ46R_%=\n\t"
      "FQ46B_%=:\n\t"
      "v_mfma_f32_16x16x32_bf16 v[196:199], v[140:143], v[60:63], v[196:199]\n\t"
      "v_mfma_f32_16x16x32_bf16 v[184:187], v[144:147], v[64:67], v[184:187]\n\t"
      "s_waitcnt vmcnt(1)\n\t"
      "v_cmp_eq_u32_e32 vcc, -1, v148\n\t"
      "v_mfma_f32_16x16x32_bf16 v[188:191], v[144:147], v[68:71], v[188:191]\n\t"
      "v_mfma_f32_16x16x32_bf16 v[192:195], v[144:147], v[72:75], v[192:195]\n\t"
      "s_cbranch_vccnz FQ47R_%=\n\t"
      "FQ47B_%=:\n\t"
      "v_mfma_f32_16x16x32_bf16 v[196:199], v[144:147], v[76:79], v[196:199]\n\t"
      "s_waitcnt lgkmcnt(0)\n\t"
      "s_barrier\n\t"
      "s_setprio 2\n\t"
      "v_mfma_f32_16x16x32_bf16 v[184:187], v[148:151], v[80:83], v[184:187]\n\t"
      "s_waitcnt vmcnt(0)\n\t"
      "v_cmp_eq_u32_e32 vcc, -1, v152\n\t"
      "v_mfma_f32_16x16x32_bf16 v[188:191], v[148:151], v[84:87], v[188:191]\n\t"
      "v_mfma_f32_16x16x32_bf16 v[192:195], v[148:151], v[88:91], v[192:195]\n\t"
      "s_cbranch_vccnz FQ48R_%=\n\t"
      "FQ48B_%=:\n\t"
      "v_mfma_f32_16x16x32_bf16 v[196:199], v[148:151], v[92:95], v[196:199]\n\t"
      "v_mfma_f32_16x16x32_bf16 v[184:187], v[152:155], v[96:99], v[184:187]\n\t"
      "v_mfma_f32_16x16x32_bf16 v[188:191], v[152:155], v[100:103], v[188:191]\n\t"
      "v_mfma_f32_16x16x32_bf16 v[192:195], v[152:155], v[104:107], v[192:195]\n\t"
      "v_mfma_f32_16x16x32_bf16 v[196:199], v[152:155], v[108:111], v[196:199]\n\t"
      "s_nop 4\n\t"
      "v_cmp_u_f32_e32 vcc, v184, v185\n\t"
      "v_cmp_u_f32_e64 s[48:49], v186, v187\n\t"
      "s_nop 0\n\t"
      "s_or_b64 vcc, vcc, s[48:49]\n\t"
      "s_cbranch_scc1 NQ6R_%=\n\t"
      "NQ6B_%=:\n\t"
      "ds_write_b128 v243, v[184:187]\n\t"
      "ds_write_b128 v220, v[188:191]\n\t"
      "ds_write_b128 v221, v[192:195]\n\t"
      "ds_write_b128 v222, v[196:199]\n\t"
      "s_waitcnt lgkmcnt(0)\n\t"
      "s_barrier\n\t"
      "s_branch GX_%=\n\t"
      "HWR_%=:\n\t"
      "s_mov_b32 s51, 0\n\t"
      "HWL_%=:\n\t"
      "s_add_u32 s51, s51, 1\n\t"
      "s_cmp_lt_u32 s51, 262144\n\t"
      "s_cbranch_scc0 HWX_%=\n\t"
      "s_sleep 2\n\t"
      "global_load_dwordx4 v[112:115], v116, s[12:13] sc1\n\t"
      "s_waitcnt vmcnt(0)\n\t"
      "v_xor_b32_e32 v114, v114, v112\n\t"
      "v_xor_b32_e32 v115, v115, v113\n\t"
      "v_cmp_ne_u64_e32 vcc, s[14:15], v[114:115]\n\t"
      "s_cbranch_vccnz HWL_%=\n\t"
      "HWX_%=:\n\t"
      "s_branch HWB_%=\n\t"
      "HXR_%=:\n\t"
      "s_mov_b32 s51, 0\n\t"
      "HXL_%=:\n\t"
      "s_add_u32 s51, s51, 1\n\t"
      "s_cmp_lt_u32 s51, 262144\n\t"
      "s_cbranch_scc0 HXX_%=\n\t"
      "s_sleep 2\n\t"
      "s_mov_b32 exec_hi, s23\n\t"
      "s_not_b32 exec_lo, s23\n\t"
      "s_add_u32 s68, s20, 0\n\t"
      "s_addc_u32 s69, s21, 0\n\t"
      "global_load_dwordx4 v[184:187], v240, s[68:69] sc1\n\t"
      "s_add_u32 s68, s20, 131072\n\t"
      "s_addc_u32 s69, s21, 0\n\t"
      "global_load_dwordx4 v[188:191], v240, s[68:69] sc1\n\t"
      "s_add_u32 s68, s20, 262144\n\t"
      "s_addc_u32 s69, s21, 0\n\t"
      "global_load_dwordx4 v[192:195], v240, s[68:69] sc1\n\t"
      "s_add_u32 s68, s20, 393216\n\t"
      "s_addc_u32 s69, s21, 0\n\t"
      "global_load_dwordx4 v[196:199], v240, s[68:69] sc1\n\t"
      "s_add_u32 s68, s20, 524288\n\t"
      "s_addc_u32 s69, s21, 0\n\t"
      "global_load_dwordx4 v[200:203], v240, s[68:69] sc1\n\t"
      "s_add_u32 s68, s20, 655360\n\t"
      "s_addc_u32 s69, s21, 0\n\t"
      "global_load_dwordx4 v[204:207], v240, s[68:69] sc1\n\t"
      "s_add_u32 s68, s20, 786432\n\t"
      "s_addc_u32 s69, s21, 0\n\t"
      "global_load_dwordx4 v[208:211], v240, s[68:69] sc1\n\t"
      "s_mov_b64 exec, -1\n\t"
      "s_waitcnt vmcnt(0)\n\t"
      "v_max3_u32 v234, v184, v185, v186\n\t"
      "v_max3_u32 v234, v234, v187, v188\n\t"
      "v_max3_u32 v234, v234, v189, v190\n\t"
      "v_max3_u32 v234, v234, v191, v192\n\t"
      "v_max3_u32 v234, v234, v193, v194\n\t"
      "v_max3_u32 v234, v234, v195, v196\n\t"
      "v_max3_u32 v234, v234, v197, v198\n\t"
      "v_max3_u32 v234, v234, v199, v200\n\t"
      "v_max3_u32 v234, v234, v201, v202\n\t"
      "v_max3_u32 v234, v234, v203, v204\n\t"
      "v_max3_u32 v234, v234, v205, v206\n\t"
      "v_max3_u32 v234, v234, v207, v208\n\t"
      "v_max3_u32 v234, v234, v209, v210\n\t"
      "v_max_u32_e32 v234, v234, v211\n\t"
      "v_cmp_eq_u32_e32 vcc, -1, v234\n\t"
      "s_cbranch_vccnz HXL_%=\n\t"
      "HXX_%=:\n\t"
      "s_nop 1\n\t"
      "s_branch HXB_%=\n\t"
      "FQ0R_%=:\n\t"
      "s_mov_b32 s51, 0\n\t"
      "FQ0L_%=:\n\t"
      "s_add_u32 s51, s51, 1\n\t"
      "s_cmp_lt_u32 s51, 262144\n\t"
      "s_cbranch_scc0 FQ0X_%=\n\t"
      "s_waitcnt vmcnt(0)\n\t"
      "s_sleep 1\n\t"
      "global_load_dwordx4 v[128:131], v224, s[52:53] sc1\n\t"
      "global_load_dwordx4 v[132:135], v225, s[52:53] sc1\n\t"
      "global_load_dwordx4 v[136:139], v226, s[52:53] sc1\n\t"
      "global_load_dwordx4 v[140:143], v227, s[52:53] sc1\n\t"
      "global_load_dwordx4 v[144:147], v228, s[52:53] sc1\n\t"
      "global_load_dwordx4 v[148:151], v229, s[52:53] sc1\n\t"
      "global_load_dwordx4 v[152:155], v230, s[52:53] sc1\n\t"
      "s_waitcnt vmcnt(6)\n\t"
      "v_max3_u32 v234, v128, v129, v130\n\t"
      "v_max_u32_e32 v234, v234, v131\n\t"
      "v_cmp_eq_u32_e32 vcc, -1, v234\n\t"
      "s_cbranch_vccnz FQ0L_%=\n\t"
      "FQ0X_%=:\n\t"
      "s_branch FQ0B_%=\n\t"
      "FQ1R_%=:\n\t"
      "s_mov_b32 s51, 0\n\t"
      "FQ1L_%=:\n\t"
      "s_add_u32 s51, s51, 1\n\t"
      "s_cmp_lt_u32 s51, 262144\n\t"
      "s_cbranch_scc0 FQ1X_%=\n\t"
      "s_waitcnt vmcnt(0)\n\t"
      "s_sleep 1\n\t"
      "global_load_dwordx4 v[132:135], v225, s[52:53] sc1\n\t"
      "global_load_dwordx4 v[136:139], v226, s[52:53] sc1\n\t"
      "global_load_dwordx4 v[140:143], v227, s[52:53] sc1\n\t"
      "global_load_dwordx4 v[144:147], v228, s[52:53] sc1\n\t"
      "global_load_dwordx4 v[148:151], v229, s[52:53] sc1\n\t"
      "global_load_dwordx4 v[152:155], v230, s[52:53] sc1\n\t"
      "global_load_dwordx4 v[128:131], v224, s[54:55] sc1\n\t"
      "s_waitcnt vmcnt(6)\n\t"
      "v_max3_u32 v234, v132, v133, v134\n\t"
      "v_max_u32_e32 v234, v234, v135\n\t"
      "v_cmp_eq_u32_e32 vcc, -1, v234\n\t"
      "s_cbranch_vccnz FQ1L_%=\n\t"
      "FQ1X_%=:\n\t"
      "s_branch FQ1B_%=\n\t"
      "FQ2R_%=:\n\t"
      "s_mov_b32 s51, 0\n\t"
      "FQ2L_%=:\n\t"
      "s_add_u32 s51, s51, 1\n\t"
      "s_cmp_lt_u32 s51, 262144\n\t"
      "s_cbranch_scc0 FQ2X_%=\n\t"
      "s_waitcnt vmcnt(0)\n\t"
      "s_sleep 1\n\t"
      "global_load_dwordx4 v[136:139], v226, s[52:53] sc1\n\t"
      "global_load_dwordx4 v[140:143], v227, s[52:53] sc1\n\t"
      "global_load_dwordx4 v[144:147], v228, s[52:53] sc1\n\t"
      "global_load_dwordx4 v[148:151], v229, s[52:53] sc1\n\t"
      "global_load_dwordx4 v[152:155], v230, s[52:53] sc1\n\t"
      "global_load_dwordx4 v[128:131], v224, s[54:55] sc1\n\t"
      "global_load_dwordx4 v[132:135], v225, s[54:55] sc1\n\t"
      "s_waitcnt vmcnt(6)\n\t"
      "v_max3_u32 v234, v136, v137, v138\n\t"
      "v_max_u32_e32 v234, v234, v139\n\t"
      "v_cmp_eq_u32_e32 vcc, -1, v234\n\t"
      "s_cbranch_vccnz FQ2L_%=\n\t"
      "FQ2X_%=:\n\t"
      "s_branch FQ2B_%=\n\t"
      "FQ3R_%=:\n\t"
      "s_mov_b32 s51, 0\n\t"
      "FQ3L_%=:\n\t"
      "s_add_u32 s51, s51, 1\n\t"
      "s_cmp_lt_u32 s51, 262144\n\t"
      "s_cbranch_scc0 FQ3X_%=\n\t"
      "s_waitcnt vmcnt(0)\n\t"
      "s_sleep 1\n\t"
      "global_load_dwordx4 v[140:143], v227, s[52:53] sc1\n\t"
      "global_load_dwordx4 v[144:147], v228, s[52:53] sc1\n\t"
      "global_load_dwordx4 v[148:151], v229, s[52:53] sc1\n\t"
      "global_load_dwordx4 v[152:155], v230, s[52:53] sc1\n\t"
      "global_load_dwordx4 v[128:131], v224, s[54:55] sc1\n\t"
      "global_load_dwordx4 v[132:135], v225, s[54:55] sc1\n\t"
      "global_load_dwordx4 v[136:139], v226, s[54:55] sc1\n\t"
      "s_waitcnt vmcnt(6)\n\t"
      "v_max3_u32 v234, v140, v141, v142\n\t"
      "v_max_u32_e32 v234, v234, v143\n\t"
      "v_cmp_eq_u32_e32 vcc, -1, v234\n\t"
      "s_cbranch_vccnz FQ3L_%=\n\t"
      "FQ3X_%=:\n\t"
      "s_branch FQ3B_%=\n\t"
      "FQ4R_%=:\n\t"
      "s_mov_b32 s51, 0\n\t"
      "FQ4L_%=:\n\t"
      "s_add_u32 s51, s51, 1\n\t"
      "s_cmp_lt_u32 s51, 262144\n\t"
      "s_cbranch_scc0 FQ4X_%=\n\t"
      "s_waitcnt vmcnt(0)\n\t"
      "s_sleep 1\n\t"
      "global_load_dwordx4 v[144:147], v228, s[52:53] sc1\n\t"
      "global_load_dwordx4 v[148:151], v229, s[52:53] sc1\n\t"
      "global_load_dwordx4 v[152:155], v230, s[52:53] sc1\n\t"
      "global_load_dwordx4 v[128:131], v224, s[54:55] sc1\n\t"
      "global_load_dwordx4 v[132:135], v225, s[54:55] sc1\n\t"
      "global_load_dwordx4 v[136:139], v226, s[54:55] sc1\n\t"
      "global_load_dwordx4 v[140:143], v227, s[54:55] sc1\n\t"
      "s_waitcnt vmcnt(6)\n\t"
      "v_max3_u32 v234, v144, v145, v146\n\t"
      "v_max_u32_e32 v234, v234, v147\n\t"
      "v_cmp_eq_u32_e32 vcc, -1, v234\n\t"
      "s_cbranch_vccnz FQ4L_%=\n\t"
      "FQ4X_%=:\n\t"
      "s_branch FQ4B_%=\n\t"
      "FQ5R_%=:\n\t"
      "s_mov_b32 s51, 0\n\t"
      "FQ5L_%=:\n\t"
      "s_add_u32 s51, s51, 1\n\t"
      "s_cmp_lt_u32 s51, 262144\n\t"
      "s_cbranch_scc0 FQ5X_%=\n\t"
      "s_waitcnt vmcnt(0)\n\t"
      "s_sleep 1\n\t"
      "global_load_dwordx4 v[148:151], v229, s[52:53] sc1\n\t"
      "global_load_dwordx4 v[152:155], v230, s[52:53] sc1\n\t"
      "global_load_dwordx4 v[128:131], v224, s[54:55] sc1\n\t"
      "global_load_dwordx4 v[132:135], v225, s[54:55] sc1\n\t"
      "global_load_dwordx4 v[136:139], v226, s[54:55] sc1\n\t"
      "global_load_dwordx4 v[140:143], v227, s[54:55] sc1\n\t"
      "global_load_dwordx4 v[144:147], v228, s[54:55] sc1\n\t"
      "s_waitcnt vmcnt(6)\n\t"
      "v_max3_u32 v234, v148, v149, v150\n\t"
      "v_max_u32_e32 v234, v234, v151\n\t"
      "v_cmp_eq_u32_e32 vcc, -1, v234\n\t"
      "s_cbranch_vccnz FQ5L_%=\n\t"
      "FQ5X_%=:\n\t"
      "s_branch FQ5B_%=\n\t"
      "FQ6R_%=:\n\t"
      "s_mov_b32 s51, 0\n\t"
      "FQ6L_%=:\n\t"
      "s_add_u32 s51, s51, 1\n\t"
      "s_cmp_lt_u32 s51, 262144\n\t"
      "s_cbranch_scc0 FQ6X_%=\n\t"
      "s_waitcnt vmcnt(0)\n\t"
      "s_sleep 1\n\t"
      "global_load_dwordx4 v[152:155], v230, s[52:53] sc1\n\t"
      "global_load_dwordx4 v[128:131], v224, s[54:55] sc1\n\t"
      "global_load_dwordx4 v[132:135], v225, s[54:55] sc1\n\t"
      "global_load_dwordx4 v[136:139], v226, s[54:55] sc1\n\t"
      "global_load_dwordx4 v[140:143], v227, s[54:55] sc1\n\t"
      "global_load_dwordx4 v[144:147], v228, s[54:55] sc1\n\t"
      "global_load_dwordx4 v[148:151], v229, s[54:55] sc1\n\t"
      "s_waitcnt vmcnt(6)\n\t"
      "v_max3_u32 v234, v152, v153, v154\n\t"
      "v_max_u32_e32 v234, v234, v155\n\t"
      "v_cmp_eq_u32_e32 vcc, -1, v234\n\t"
      "s_cbranch_vccnz FQ6L_%=\n\t"
      "FQ6X_%=:\n\t"
      "s_branch FQ6B_%=\n\t"
      "FQ7R_%=:\n\t"
      "s_mov_b32 s51, 0\n\t"
      "FQ7L_%=:\n\t"
      "s_add_u32 s51, s51, 1\n\t"
      "s_cmp_lt_u32 s51, 262144\n\t"
      "s_cbranch_scc0 FQ7X_%=\n\t"
      "s_waitcnt vmcnt(0)\n\t"
      "s_sleep 4\n\t"
      "global_load_dwordx4 v[128:131], v224, s[54:55] sc1\n\t"
      "global_load_dwordx4 v[132:135], v225, s[54:55] sc1\n\t"
      "global_load_dwordx4 v[136:139], v226, s[54:55] sc1\n\t"
      "global_load_dwordx4 v[140:143], v227, s[54:55] sc1\n\t"
      "global_load_dwordx4 v[144:147], v228, s[54:55] sc1\n\t"
      "global_load_dwordx4 v[148:151], v229, s[54:55] sc1\n\t"
      "s_waitcnt vmcnt(5)\n\t"
      "v_max3_u32 v234, v128, v129, v130\n\t"
      "v_max_u32_e32 v234, v234, v131\n\t"
      "v_cmp_eq_u32_e32 vcc, -1, v234\n\t"
      "s_cbranch_vccnz FQ7L_%=\n\t"
      "FQ7X_%=:\n\t"
      "s_branch FQ7B_%=\n\t"
      "FQ8R_%=:\n\t"
      "s_mov_b32 s51, 0\n\t"
      "FQ8L_%=:\n\t"
      "s_add_u32 s51, s51, 1\n\t"
      "s_cmp_lt_u32 s51, 262144\n\t"
      "s_cbranch_scc0 FQ8X_%=\n\t"
      "s_waitcnt vmcnt(0)\n\t"
      "s_sleep 4\n\t"
      "global_load_dwordx4 v[132:135], v225, s[54:55] sc1\n\t"
      "global_load_dwordx4 v[136:139], v226, s[54:55] sc1\n\t"
      "global_load_dwordx4 v[140:143], v227, s[54:55] sc1\n\t"
      "global_load_dwordx4 v[144:147], v228, s[54:55] sc1\n\t"
      "global_load_dwordx4 v[148:151], v229, s[54:55] sc1\n\t"
      "global_load_dwordx4 v[152:155], v230, s[54:55] sc1\n\t"
      "s_waitcnt vmcnt(5)\n\t"
      "v_max3_u32 v234, v132, v133, v134\n\t"
      "v_max_u32_e32 v234, v234, v135\n\t"
      "v_cmp_eq_u32_e32 vcc, -1, v234\n\t"
      "s_cbranch_vccnz FQ8L_%=\n\t"
      "FQ8X_%=:\n\t"
      "s_branch FQ8B_%=\n\t"
      "FQ9R_%=:\n\t"
      "s_mov_b32 s51, 0\n\t"
      "FQ9L_%=:\n\t"
      "s_add_u32 s51, s51, 1\n\t"
      "s_cmp_lt_u32 s51, 262144\n\t"
      "s_cbranch_scc0 FQ9X_%=\n\t"
      "s_waitcnt vmcnt(0)\n\t"
      "s_sleep 4\n\t"
      "global_load_dwordx4 v[136:139], v226, s[54:55] sc1\n\t"
      "global_load_dwordx4 v[140:143], v227, s[54:55] sc1\n\t"
      "global_load_dwordx4 v[144:147], v228, s[54:55] sc1\n\t"
      "global_load_dwordx4 v[148:151], v229, s[54:55] sc1\n\t"
      "global_load_dwordx4 v[152:155], v230, s[54:55] sc1\n\t"
      "global_load_dwordx4 v[128:131], v224, s[56:57] sc1\n\t"
      "s_waitcnt vmcnt(5)\n\t"
      "v_max3_u32 v234, v136, v137, v138\n\t"
      "v_max_u32_e32 v234, v234, v139\n\t"
      "v_cmp_eq_u32_e32 vcc, -1, v234\n\t"
      "s_cbranch_vccnz FQ9L_%=\n\t"
      "FQ9X_%=:\n\t"
      "s_branch FQ9B_%=\n\t"
      "FQ10R_%=:\n\t"
      "s_mov_b32 s51, 0\n\t"
      "FQ10L_%=:\n\t"
      "s_add_u32 s51, s51, 1\n\t"
      "s_cmp_lt_u32 s51, 262144\n\t"
      "s_cbranch_scc0 FQ10X_%=\n\t"
      "s_waitcnt vmcnt(0)\n\t"
      "s_sleep 4\n\t"
      "global_load_dwordx4 v[140:143], v227, s[54:55] sc1\n\t"
      "global_load_dwordx4 v[144:147], v228, s[54:55] sc1\n\t"
      "global_load_dwordx4 v[148:151], v229, s[54:55] sc1\n\t"
      "global_load_dwordx4 v[152:155], v230, s[54:55] sc1\n\t"
      "global_load_dwordx4 v[128:131], v224, s[56:57] sc1\n\t"
      "global_load_dwordx4 v[132:135], v225, s[56:57] sc1\n\t"
      "s_waitcnt vmcnt(5)\n\t"
      "v_max3_u32 v234, v140, v141, v142\n\t"
      "v_max_u32_e32 v234, v234, v143\n\t"
      "v_cmp_eq_u32_e32 vcc, -1, v234\n\t"
      "s_cbranch_vccnz FQ10L_%=\n\t"
      "FQ10X_%=:\n\t"
      "s_branch FQ10B_%=\n\t"
      "FQ11R_%=:\n\t"
      "s_mov_b32 s51, 0\n\t"
      "FQ11L_%=:\n\t"
      "s_add_u32 s51, s51, 1\n\t"
      "s_cmp_lt_u32 s51, 262144\n\t"
      "s_cbranch_scc0 FQ11X_%=\n\t"
      "s_waitcnt vmcnt(0)\n\t"
      "s_sleep 4\n\t"
      "global_load_dwordx4 v[144:147], v228, s[54:55] sc1\n\t"
      "global_load_dwordx4 v[148:151], v229, s[54:55] sc1\n\t"
      "global_load_dwordx4 v[152:155], v230, s[54:55] sc1\n\t"
      "global_load_dwordx4 v[128:131], v224, s[56:57] sc1\n\t"
      "global_load_dwordx4 v[132:135], v225, s[56:57] sc1\n\t"
      "global_load_dwordx4 v[136:139], v226, s[56:57] sc1\n\t"
      "s_waitcnt vmcnt(5)\n\t"
      "v_max3_u32 v234, v144, v145, v146\n\t"
      "v_max_u32_e32 v234, v234, v147\n\t"
      "v_cmp_eq_u32_e32 vcc, -1, v234\n\t"
      "s_cbranch_vccnz FQ11L_%=\n\t"
      "FQ11X_%=:\n\t"
      "s_branch FQ11B_%=\n\t"
      "FQ12R_%=:\n\t"
      "s_mov_b32 s51, 0\n\t"
      "FQ12L_%=:\n\t"
      "s_add_u32 s51, s51, 1\n\t"
      "s_cmp_lt_u32 s51, 262144\n\t"
      "s_cbranch_scc0 FQ12X_%=\n\t"
      "s_waitcnt vmcnt(0)\n\t"
      "s_sleep 4\n\t"
      "global_load_dwordx4 v[148:151], v229, s[54:55] sc1\n\t"
      "global_load_dwordx4 v[152:155], v230, s[54:55] sc1\n\t"
      "global_load_dwordx4 v[128:131], v224, s[56:57] sc1\n\t"
      "global_load_dwordx4 v[132:135], v225, s[56:57] sc1\n\t"
      "global_load_dwordx4 v[136:139], v226, s[56:57] sc1\n\t"
      "global_load_dwordx4 v[140:143], v227, s[56:57] sc1\n\t"
      "s_waitcnt vmcnt(5)\n\t"
      "v_max3_u32 v234, v148, v149, v150\n\t"
      "v_max_u32_e32 v234, v234, v151\n\t"
      "v_cmp_eq_u32_e32 vcc, -1, v234\n\t"
      "s_cbranch_vccnz FQ12L_%=\n\t"
      "FQ12X_%=:\n\t"
      "s_branch FQ12B_%=\n\t"
      "FQ13R_%=:\n\t"
      "s_mov_b32 s51, 0\n\t"
      "FQ13L_%=:\n\t"
      "s_add_u32 s51, s51, 1\n\t"
      "s_cmp_lt_u32 s51, 262144\n\t"
      "s_cbranch_scc0 FQ13X_%=\n\t"
      "s_waitcnt vmcnt(0)\n\t"
      "s_sleep 4\n\t"
      "global_load_dwordx4 v[152:155], v230, s[54:55] sc1\n\t"
      "global_load_dwordx4 v[128:131], v224, s[56:57] sc1\n\t"
      "global_load_dwordx4 v[132:135], v225, s[56:57] sc1\n\t"
      "global_load_dwordx4 v[136:139], v226, s[56:57] sc1\n\t"
      "global_load_dwordx4 v[140:143], v227, s[56:57] sc1\n\t"
      "global_load_dwordx4 v[144:147], v228, s[56:57] sc1\n\t"
      "s_waitcnt vmcnt(5)\n\t"
      "v_max3_u32 v234, v152, v153, v154\n\t"
      "v_max_u32_e32 v234, v234, v155\n\t"
      "v_cmp_eq_u32_e32 vcc, -1, v234\n\t"
      "s_cbranch_vccnz FQ13L_%=\n\t"
      "FQ13X_%=:\n\t"
      "s_branch FQ13B_%=\n\t"
      "FQ14R_%=:\n\t"
      "s_mov_b32 s51, 0\n\t"
      "FQ14L_%=:\n\t"
      "s_add_u32 s51, s51, 1\n\t"
      "s_cmp_lt_u32 s51, 262144\n\t"
      "s_cbranch_scc0 FQ14X_%=\n\t"
      "s_waitcnt vmcnt(0)\n\t"
      "s_sleep 4\n\t"
      "global_load_dwordx4 v[128:131], v224, s[56:57] sc1\n\t"
      "global_load_dwordx4 v[132:135], v225, s[56:57] sc1\n\t"
      "global_load_dwordx4 v[136:139], v226, s[56:57] sc1\n\t"
      "global_load_dwordx4 v[140:143], v227, s[56:57] sc1\n\t"
      "global_load_dwordx4 v[144:147], v228, s[56:57] sc1\n\t"
      "global_load_dwordx4 v[148:151], v229, s[56:57] sc1\n\t"
      "s_waitcnt vmcnt(5)\n\t"
      "v_max3_u32 v234, v128, v129, v130\n\t"
      "v_max_u32_e32 v234, v234, v131\n\t"
      "v_cmp_eq_u32_e32 vcc, -1, v234\n\t"
      "s_cbranch_vccnz FQ14L_%=\n\t"
      "FQ14X_%=:\n\t"
      "s_branch FQ14B_%=\n\t"
      "NQ1R_%=:\n\t"
      "s_mov_b32 s51, 0\n\t"
      "NQ1L_%=:\n\t"
      "s_add_u32 s51, s51, 1\n\t"
      "s_cmp_lt_u32 s51, 262144\n\t"
      "s_cbranch_scc0 NQ1X_%=\n\t"
      "s_waitcnt vmcnt(0)\n\t"
      "s_sleep 1\n\t"
      "global_load_dwordx4 v[128:131], v224, s[54:55] sc1\n\t"
      "global_load_dwordx4 v[132:135], v225, s[54:55] sc1\n\t"
      "global_load_dwordx4 v[136:139], v226, s[54:55] sc1\n\t"
      "global_load_dwordx4 v[140:143], v227, s[54:55] sc1\n\t"
      "global_load_dwordx4 v[144:147], v228, s[54:55] sc1\n\t"
      "global_load_dwordx4 v[148:151], v229, s[54:55] sc1\n\t"
      "global_load_dwordx4 v[152:155], v230, s[54:55] sc1\n\t"
      "s_waitcnt vmcnt(0)\n\t"
      "s_nop 0\n\t"
      "v_max3_u32 v234, v128, v129, v130\n\t"
      "v_max3_u32 v234, v234, v131, v132\n\t"
      "v_max3_u32 v234, v234, v133, v134\n\t"
      "v_max3_u32 v234, v234, v135, v136\n\t"
      "v_max3_u32 v234, v234, v137, v138\n\t"
      "v_max3_u32 v234, v234, v139, v140\n\t"
      "v_max3_u32 v234, v234, v141, v142\n\t"
      "v_max3_u32 v234, v234, v143, v144\n\t"
      "v_max3_u32 v234, v234, v145, v146\n\t"
      "v_max3_u32 v234, v234, v147, v148\n\t"
      "v_max3_u32 v234, v234, v149, v150\n\t"
      "v_max3_u32 v234, v234, v151, v152\n\t"
      "v_max3_u32 v234, v234, v153, v154\n\t"
      "v_max_u32_e32 v234, v234, v155\n\t"
      "v_cmp_eq_u32_e32 vcc, -1, v234\n\t"
      "s_cbranch_vccnz NQ1L_%=\n\t"
      "NQ1X_%=:\n\t"
      "v_mfma_f32_16x16x32_bf16 v[200:203], v[128:131], v[0:3], 0\n\t"
      "v_mfma_f32_16x16x32_bf16 v[204:207], v[128:131], v[4:7], 0\n\t"
      "v_mfma_f32_16x16x32_bf16 v[208:211], v[128:131], v[8:11], 0\n\t"
      "v_mfma_f32_16x16x32_bf16 v[212:215], v[128:131], v[12:15], 0\n\t"
      "v_mfma_f32_16x16x32_bf16 v[200:203], v[132:135], v[16:19], v[200:203]\n\t"
      "v_mfma_f32_16x16x32_bf16 v[204:207], v[132:135], v[20:23], v[204:207]\n\t"
      "v_mfma_f32_16x16x32_bf16 v[208:211], v[132:135], v[24:27], v[208:211]\n\t"
      "v_mfma_f32_16x16x32_bf16 v[212:215], v[132:135], v[28:31], v[212:215]\n\t"
      "v_mfma_f32_16x16x32_bf16 v[200:203], v[136:139], v[32:35], v[200:203]\n\t"
      "v_mfma_f32_16x16x32_bf16 v[204:207], v[136:139], v[36:39], v[204:207]\n\t"
      "v_mfma_f32_16x16x32_bf16 v[208:211], v[136:139], v[40:43], v[208:211]\n\t"
      "v_mfma_f32_16x16x32_bf16 v[212:215], v[136:139], v[44:47], v[212:215]\n\t"
      "v_mfma_f32_16x16x32_bf16 v[200:203], v[140:143], v[48:51], v[200:203]\n\t"
      "v_mfma_f32_16x16x32_bf16 v[204:207], v[140:143], v[52:55], v[204:207]\n\t"
      "v_mfma_f32_16x16x32_bf16 v[208:211], v[140:143], v[56:59], v[208:211]\n\t"
      "v_mfma_f32_16x16x32_bf16 v[212:215], v[140:143], v[60:63], v[212:215]\n\t"
      "v_mfma_f32_16x16x32_bf16 v[200:203], v[144:147], v[64:67], v[200:203]\n\t"
      "v_mfma_f32_16x16x32_bf16 v[204:207], v[144:147], v[68:71], v[204:207]\n\t"
      "v_mfma_f32_16x16x32_bf16 v[208:211], v[144:147], v[72:75], v[208:211]\n\t"
      "v_mfma_f32_16x16x32_bf16 v[212:215], v[144:147], v[76:79], v[212:215]\n\t"
      "v_mfma_f32_16x16x32_bf16 v[200:203], v[148:151], v[80:83], v[200:203]\n\t"
      "v_mfma_f32_16x16x32_bf16 v[204:207], v[148:151], v[84:87], v[204:207]\n\t"
      "v_mfma_f32_16x16x32_bf16 v[208:211], v[148:151], v[88:91], v[208:211]\n\t"
      "v_mfma_f32_16x16x32_bf16 v[212:215], v[148:151], v[92:95], v[212:215]\n\t"
      "v_mfma_f32_16x16x32_bf16 v[200:203], v[152:155], v[96:99], v[200:203]\n\t"
      "v_mfma_f32_16x16x32_bf16 v[204:207], v[152:155], v[100:103], v[204:207]\n\t"
      "v_mfma_f32_16x16x32_bf16 v[208:211], v[152:155], v[104:107], v[208:211]\n\t"
      "v_mfma_f32_16x16x32_bf16 v[212:215], v[152:155], v[108:111], v[212:215]\n\t"
      "global_load_dwordx4 v[128:131], v224, s[56:57] sc1\n\t"
      "global_load_dwordx4 v[132:135], v225, s[56:57] sc1\n\t"
      "global_load_dwordx4 v[136:139], v226, s[56:57] sc1\n\t"
      "global_load_dwordx4 v[140:143], v227, s[56:57] sc1\n\t"
      "global_load_dwordx4 v[144:147], v228, s[56:57] sc1\n\t"
      "global_load_dwordx4 v[148:151], v229, s[56:57] sc1\n\t"
      "global_load_dwordx4 v[152:155], v230, s[56:57] sc1\n\t"
      "s_waitcnt vmcnt(0)\n\t"
      "s_nop 7\n\t"
      "s_branch NQ1B_%=\n\t"
      "FQ15R_%=:\n\t"
      "s_mov_b32 s51, 0\n\t"
      "FQ15L_%=:\n\t"
      "s_add_u32 s51, s51, 1\n\t"
      "s_cmp_lt_u32 s51, 262144\n\t"
      "s_cbranch_scc0 FQ15X_%=\n\t"
      "s_waitcnt vmcnt(0)\n\t"
      "s_sleep 4\n\t"
      "global_load_dwordx4 v[132:135], v225, s[56:57] sc1\n\t"
      "global_load_dwordx4 v[136:139], v226, s[56:57] sc1\n\t"
      "global_load_dwordx4 v[140:143], v227, s[56:57] sc1\n\t"
      "global_load_dwordx4 v[144:147], v228, s[56:57] sc1\n\t"
      "global_load_dwordx4 v[148:151], v229, s[56:57] sc1\n\t"
      "global_load_dwordx4 v[152:155], v230, s[56:57] sc1\n\t"
      "s_waitcnt vmcnt(5)\n\t"
      "v_max3_u32 v234, v132, v133, v134\n\t"
      "v_max_u32_e32 v234, v234, v135\n\t"
      "v_cmp_eq_u32_e32 vcc, -1, v234\n\t"
      "s_cbranch_vccnz FQ15L_%=\n\t"
      "FQ15X_%=:\n\t"
      "s_branch FQ15B_%=\n\t"
      "FQ16R_%=:\n\t"
      "s_mov_b32 s51, 0\n\t"
      "FQ16L_%=:\n\t"
      "s_add_u32 s51, s51, 1\n\t"
      "s_cmp_lt_u32 s51, 262144\n\t"
      "s_cbranch_scc0 FQ16X_%=\n\t"
      "s_waitcnt vmcnt(0)\n\t"
      "s_sleep 4\n\t"
      "global_load_dwordx4 v[136:139], v226, s[56:57] sc1\n\t"
      "global_load_dwordx4 v[140:143], v227, s[56:57] sc1\n\t"
      "global_load_dwordx4 v[144:147], v228, s[56:57] sc1\n\t"
      "global_load_dwordx4 v[148:151], v229, s[56:57] sc1\n\t"
      "global_load_dwordx4 v[152:155], v230, s[56:57] sc1\n\t"
      "global_load_dwordx4 v[128:131], v224, s[58:59] sc1\n\t"
      "s_waitcnt vmcnt(5)\n\t"
      "v_max3_u32 v234, v136, v137, v138\n\t"
      "v_max_u32_e32 v234, v234, v139\n\t"
      "v_cmp_eq_u32_e32 vcc, -1, v234\n\t"
      "s_cbranch_vccnz FQ16L_%=\n\t"
      "FQ16X_%=:\n\t"
      "s_branch FQ16B_%=\n\t"
      "FQ17R_%=:\n\t"
      "s_mov_b32 s51, 0\n\t"
      "FQ17L_%=:\n\t"
      "s_add_u32 s51, s51, 1\n\t"
      "s_cmp_lt_u32 s51, 262144\n\t"
      "s_cbranch_scc0 FQ17X_%=\n\t"
      "s_waitcnt vmcnt(0)\n\t"
      "s_sleep 4\n\t"
      "global_load_dwordx4 v[140:143], v227, s[56:57] sc1\n\t"
      "global_load_dwordx4 v[144:147], v228, s[56:57] sc1\n\t"
      "global_load_dwordx4 v[148:151], v229, s[56:57] sc1\n\t"
      "global_load_dwordx4 v[152:155], v230, s[56:57] sc1\n\t"
      "global_load_dwordx4 v[128:131], v224, s[58:59] sc1\n\t"
      "global_load_dwordx4 v[132:135], v225, s[58:59] sc1\n\t"
      "s_waitcnt vmcnt(5)\n\t"
      "v_max3_u32 v234, v140, v141, v142\n\t"
      "v_max_u32_e32 v234, v234, v143\n\t"
      "v_cmp_eq_u32_e32 vcc, -1, v234\n\t"
      "s_cbranch_vccnz FQ17L_%=\n\t"
      "FQ17X_%=:\n\t"
      "s_branch FQ17B_%=\n\t"
      "FQ18R_%=:\n\t"
      "s_mov_b32 s51, 0\n\t"
      "FQ18L_%=:\n\t"
      "s_add_u32 s51, s51, 1\n\t"
      "s_cmp_lt_u32 s51, 262144\n\t"
      "s_cbranch_scc0 FQ18X_%=\n\t"
      "s_waitcnt vmcnt(0)\n\t"
      "s_sleep 4\n\t"
      "global_load_dwordx4 v[144:147], v228, s[56:57] sc1\n\t"
      "global_load_dwordx4 v[148:151], v229, s[56:57] sc1\n\t"
      "global_load_dwordx4 v[152:155], v230, s[56:57] sc1\n\t"
      "global_load_dwordx4 v[128:131], v224, s[58:59] sc1\n\t"
      "global_load_dwordx4 v[132:135], v225, s[58:59] sc1\n\t"
      "global_load_dwordx4 v[136:139], v226, s[58:59] sc1\n\t"
      "s_waitcnt vmcnt(5)\n\t"
      "v_max3_u32 v234, v144, v145, v146\n\t"
      "v_max_u32_e32 v234, v234, v147\n\t"
      "v_cmp_eq_u32_e32 vcc, -1, v234\n\t"
      "s_cbranch_vccnz FQ18L_%=\n\t"
      "FQ18X_%=:\n\t"
      "s_branch FQ18B_%=\n\t"
      "FQ19R_%=:\n\t"
      "s_mov_b32 s51, 0\n\t"
      "FQ19L_%=:\n\t"
      "s_add_u32 s51, s51, 1\n\t"
      "s_cmp_lt_u32 s51, 262144\n\t"
      "s_cbranch_scc0 FQ19X_%=\n\t"
      "s_waitcnt vmcnt(0)\n\t"
      "s_sleep 4\n\t"
      "global_load_dwordx4 v[148:151], v229, s[56:57] sc1\n\t"
      "global_load_dwordx4 v[152:155], v230, s[56:57] sc1\n\t"
      "global_load_dwordx4 v[128:131], v224, s[58:59] sc1\n\t"
      "global_load_dwordx4 v[132:135], v225, s[58:59] sc1\n\t"
      "global_load_dwordx4 v[136:139], v226, s[58:59] sc1\n\t"
      "global_load_dwordx4 v[140:143], v227, s[58:59] sc1\n\t"
      "s_waitcnt vmcnt(5)\n\t"
      "v_max3_u32 v234, v148, v149, v150\n\t"
      "v_max_u32_e32 v234, v234, v151\n\t"
      "v_cmp_eq_u32_e32 vcc, -1, v234\n\t"
      "s_cbranch_vccnz FQ19L_%=\n\t"
      "FQ19X_%=:\n\t"
      "s_branch FQ19B_%=\n\t"
      "FQ20R_%=:\n\t"
      "s_mov_b32 s51, 0\n\t"
      "FQ20L_%=:\n\t"
      "s_add_u32 s51, s51, 1\n\t"
      "s_cmp_lt_u32 s51, 262144\n\t"
      "s_cbranch_scc0 FQ20X_%=\n\t"
      "s_waitcnt vmcnt(0)\n\t"
      "s_sleep 4\n\t"
      "global_load_dwordx4 v[152:155], v230, s[56:57] sc1\n\t"
      "global_load_dwordx4 v[128:131], v224, s[58:59] sc1\n\t"
      "global_load_dwordx4 v[132:135], v225, s[58:59] sc1\n\t"
      "global_load_dwordx4 v[136:139], v226, s[58:59] sc1\n\t"
      "global_load_dwordx4 v[140:143], v227, s[58:59] sc1\n\t"
      "global_load_dwordx4 v[144:147], v228, s[58:59] sc1\n\t"
      "s_waitcnt vmcnt(5)\n\t"
      "v_max3_u32 v234, v152, v153, v154\n\t"
      "v_max_u32_e32 v234, v234, v155\n\t"
      "v_cmp_eq_u32_e32 vcc, -1, v234\n\t"
      "s_cbranch_vccnz FQ20L_%=\n\t"
      "FQ20X_%=:\n\t"
      "s_branch FQ20B_%=\n\t"
      "FQ21R_%=:\n\t"
      "s_mov_b32 s51, 0\n\t"
      "FQ21L_%=:\n\t"
      "s_add_u32 s51, s51, 1\n\t"
      "s_cmp_lt_u32 s51, 262144\n\t"
      "s_cbranch_scc0 FQ21X_%=\n\t"
      "s_waitcnt vmcnt(0)\n\t"
      "s_sleep 4\n\t"
      "global_load_dwordx4 v[128:131], v224, s[58:59] sc1\n\t"
      "global_load_dwordx4 v[132:135], v225, s[58:59] sc1\n\t"
      "global_load_dwordx4 v[136:139], v226, s[58:59] sc1\n\t"
      "global_load_dwordx4 v[140:143], v227, s[58:59] sc1\n\t"
      "global_load_dwordx4 v[144:147], v228, s[58:59] sc1\n\t"
      "global_load_dwordx4 v[148:151], v229, s[58:59] sc1\n\t"
      "s_waitcnt vmcnt(5)\n\t"
      "v_max3_u32 v234, v128, v129, v130\n\t"
      "v_max_u32_e32 v234, v234, v131\n\t"
      "v_cmp_eq_u32_e32 vcc, -1, v234\n\t"
      "s_cbranch_vccnz FQ21L_%=\n\t"
      "FQ21X_%=:\n\t"
      "s_branch FQ21B_%=\n\t"
      "NQ2R_%=:\n\t"
      "s_mov_b32 s51, 0\n\t"
      "NQ2L_%=:\n\t"
      "s_add_u32 s51, s51, 1\n\t"
      "s_cmp_lt_u32 s51, 262144\n\t"
      "s_cbranch_scc0 NQ2X_%=\n\t"
      "s_waitcnt vmcnt(0)\n\t"
      "s_sleep 1\n\t"
      "global_load_dwordx4 v[128:131], v224, s[56:57] sc1\n\t"
      "global_load_dwordx4 v[132:135], v225, s[56:57] sc1\n\t"
      "global_load_dwordx4 v[136:139], v226, s[56:57] sc1\n\t"
      "global_load_dwordx4 v[140:143], v227, s[56:57] sc1\n\t"
      "global_load_dwordx4 v[144:147], v228, s[56:57] sc1\n\t"
      "global_load_dwordx4 v[148:151], v229, s[56:57] sc1\n\t"
      "global_load_dwordx4 v[152:155], v230, s[56:57] sc1\n\t"
      "s_waitcnt vmcnt(0)\n\t"
      "v_max3_u32 v234, v128, v129, v130\n\t"
      "v_max3_u32 v234, v234, v131, v132\n\t"
      "v_max3_u32 v234, v234, v133, v134\n\t"
      "v_max3_u32 v234, v234, v135, v136\n\t"
      "v_max3_u32 v234, v234, v137, v138\n\t"
      "v_max3_u32 v234, v234, v139, v140\n\t"
      "v_max3_u32 v234, v234, v141, v142\n\t"
      "v_max3_u32 v234, v234, v143, v144\n\t"
      "v_max3_u32 v234, v234, v145, v146\n\t"
      "v_max3_u32 v234, v234, v147, v148\n\t"
      "v_max3_u32 v234, v234, v149, v150\n\t"
      "v_max3_u32 v234, v234, v151, v152\n\t"
      "v_max3_u32 v234, v234, v153, v154\n\t"
      "v_max_u32_e32 v234, v234, v155\n\t"
      "v_cmp_eq_u32_e32 vcc, -1, v234\n\t"
      "s_cbranch_vccnz NQ2L_%=\n\t"
      "NQ2X_%=:\n\t"
      "v_mfma_f32_16x16x32_bf16 v[184:187], v[128:131], v[0:3], 0\n\t"
      "v_mfma_f32_16x16x32_bf16 v[188:191], v[128:131], v[4:7], 0\n\t"
      "v_mfma_f32_16x16x32_bf16 v[192:195], v[128:131], v[8:11], 0\n\t"
      "v_mfma_f32_16x16x32_bf16 v[196:199], v[128:131], v[12:15], 0\n\t"
      "v_mfma_f32_16x16x32_bf16 v[184:187], v[132:135], v[16:19], v[184:187]\n\t"
      "v_mfma_f32_16x16x32_bf16 v[188:191], v[132:135], v[20:23], v[188:191]\n\t"
      "v_mfma_f32_16x16x32_bf16 v[192:195], v[132:135], v[24:27], v[192:195]\n\t"
      "v_mfma_f32_16x16x32_bf16 v[196:199], v[132:135], v[28:31], v[196:199]\n\t"
      "v_mfma_f32_16x16x32_bf16 v[184:187], v[136:139], v[32:35], v[184:187]\n\t"
      "v_mfma_f32_16x16x32_bf16 v[188:191], v[136:139], v[36:39], v[188:191]\n\t"
      "v_mfma_f32_16x16x32_bf16 v[192:195], v[136:139], v[40:43], v[192:195]\n\t"
      "v_mfma_f32_16x16x32_bf16 v[196:199], v[136:139], v[44:47], v[196:199]\n\t"
      "v_mfma_f32_16x16x32_bf16 v[184:187], v[140:143], v[48:51], v[184:187]\n\t"
      "v_mfma_f32_16x16x32_bf16 v[188:191], v[140:143], v[52:55], v[188:191]\n\t"
      "v_mfma_f32_16x16x32_bf16 v[192:195], v[140:143], v[56:59], v[192:195]\n\t"
      "v_mfma_f32_16x16x32_bf16 v[196:199], v[140:143], v[60:63], v[196:199]\n\t"
      "v_mfma_f32_16x16x32_bf16 v[184:187], v[144:147], v[64:67], v[184:187]\n\t"
      "v_mfma_f32_16x16x32_bf16 v[188:191], v[144:147], v[68:71], v[188:191]\n\t"
      "v_mfma_f32_16x16x32_bf16 v[192:195], v[144:147], v[72:75], v[192:195]\n\t"
      "v_mfma_f32_16x16x32_bf16 v[196:199], v[144:147], v[76:79], v[196:199]\n\t"
      "v_mfma_f32_16x16x32_bf16 v[184:187], v[148:151], v[80:83], v[184:187]\n\t"
      "v_mfma_f32_16x16x32_bf16 v[188:191], v[148:151], v[84:87], v[188:191]\n\t"
      "v_mfma_f32_16x16x32_bf16 v[192:195], v[148:151], v[88:91], v[192:195]\n\t"
      "v_mfma_f32_16x16x32_bf16 v[196:199], v[148:151], v[92:95], v[196:199]\n\t"
      "v_mfma_f32_16x16x32_bf16 v[184:187], v[152:155], v[96:99], v[184:187]\n\t"
      "v_mfma_f32_16x16x32_bf16 v[188:191], v[152:155], v[100:103], v[188:191]\n\t"
      "v_mfma_f32_16x16x32_bf16 v[192:195], v[152:155], v[104:107], v[192:195]\n\t"
      "v_mfma_f32_16x16x32_bf16 v[196:199], v[152:155], v[108:111], v[196:199]\n\t"
      "global_load_dwordx4 v[128:131], v224, s[58:59] sc1\n\t"
      "global_load_dwordx4 v[132:135], v225, s[58:59] sc1\n\t"
      "global_load_dwordx4 v[136:139], v226, s[58:59] sc1\n\t"
      "global_load_dwordx4 v[140:143], v227, s[58:59] sc1\n\t"
      "global_load_dwordx4 v[144:147], v228, s[58:59] sc1\n\t"
      "global_load_dwordx4 v[148:151], v229, s[58:59] sc1\n\t"
      "global_load_dwordx4 v[152:155], v230, s[58:59] sc1\n\t"
      "s_waitcnt vmcnt(0)\n\t"
      "s_nop 7\n\t"
      "s_branch NQ2B_%=\n\t"
      "FQ22R_%=:\n\t"
      "s_mov_b32 s51, 0\n\t"
      "FQ22L_%=:\n\t"
      "s_add_u32 s51, s51, 1\n\t"
      "s_cmp_lt_u32 s51, 262144\n\t"
      "s_cbranch_scc0 FQ22X_%=\n\t"
      "s_waitcnt vmcnt(0)\n\t"
      "s_sleep 4\n\t"
      "global_load_dwordx4 v[132:135], v225, s[58:59] sc1\n\t"
      "global_load_dwordx4 v[136:139], v226, s[58:59] sc1\n\t"
      "global_load_dwordx4 v[140:143], v227, s[58:59] sc1\n\t"
      "global_load_dwordx4 v[144:147], v228, s[58:59] sc1\n\t"
      "global_load_dwordx4 v[148:151], v229, s[58:59] sc1\n\t"
      "global_load_dwordx4 v[152:155], v230, s[58:59] sc1\n\t"
      "s_waitcnt vmcnt(5)\n\t"
      "v_max3_u32 v234, v132, v133, v134\n\t"
      "v_max_u32_e32 v234, v234, v135\n\t"
      "v_cmp_eq_u32_e32 vcc, -1, v234\n\t"
      "s_cbranch_vccnz FQ22L_%=\n\t"
      "FQ22X_%=:\n\t"
      "s_branch FQ22B_%=\n\t"
      "FQ23R_%=:\n\t"
      "s_mov_b32 s51, 0\n\t"
      "FQ23L_%=:\n\t"
      "s_add_u32 s51, s51, 1\n\t"
      "s_cmp_lt_u32 s51, 262144\n\t"
      "s_cbranch_scc0 FQ23X_%=\n\t"
      "s_waitcnt vmcnt(0)\n\t"
      "s_sleep 4\n\t"
      "global_load_dwordx4 v[136:139], v226, s[58:59] sc1\n\t"
      "global_load_dwordx4 v[140:143], v227, s[58:59] sc1\n\t"
      "global_load_dwordx4 v[144:147], v228, s[58:59] sc1\n\t"
      "global_load_dwordx4 v[148:151], v229, s[58:59] sc1\n\t"
      "global_load_dwordx4 v[152:155], v230, s[58:59] sc1\n\t"
      "global_load_dwordx4 v[128:131], v224, s[60:61] sc1\n\t"
      "s_waitcnt vmcnt(5)\n\t"
      "v_max3_u32 v234, v136, v137, v138\n\t"
      "v_max_u32_e32 v234, v234, v139\n\t"
      "v_cmp_eq_u32_e32 vcc, -1, v234\n\t"
      "s_cbranch_vccnz FQ23L_%=\n\t"
      "FQ23X_%=:\n\t"
      "s_branch FQ23B_%=\n\t"
      "FQ24R_%=:\n\t"
      "s_mov_b32 s51, 0\n\t"
      "FQ24L_%=:\n\t"
      "s_add_u32 s51, s51, 1\n\t"
      "s_cmp_lt_u32 s51, 262144\n\t"
      "s_cbranch_scc0 FQ24X_%=\n\t"
      "s_waitcnt vmcnt(0)\n\t"
      "s_sleep 4\n\t"
      "global_load_dwordx4 v[140:143], v227, s[58:59] sc1\n\t"
      "global_load_dwordx4 v[144:147], v228, s[58:59] sc1\n\t"
      "global_load_dwordx4 v[148:151], v229, s[58:59] sc1\n\t"
      "global_load_dwordx4 v[152:155], v230, s[58:59] sc1\n\t"
      "global_load_dwordx4 v[128:131], v224, s[60:61] sc1\n\t"
      "global_load_dwordx4 v[132:135], v225, s[60:61] sc1\n\t"
      "s_waitcnt vmcnt(5)\n\t"
      "v_max3_u32 v234, v140, v141, v142\n\t"
      "v_max_u32_e32 v234, v234, v143\n\t"
      "v_cmp_eq_u32_e32 vcc, -1, v234\n\t"
      "s_cbranch_vccnz FQ24L_%=\n\t"
      "FQ24X_%=:\n\t"
      "s_branch FQ24B_%=\n\t"
      "FQ25R_%=:\n\t"
      "s_mov_b32 s51, 0\n\t"
      "FQ25L_%=:\n\t"
      "s_add_u32 s51, s51, 1\n\t"
      "s_cmp_lt_u32 s51, 262144\n\t"
      "s_cbranch_scc0 FQ25X_%=\n\t"
      "s_waitcnt vmcnt(0)\n\t"
      "s_sleep 4\n\t"
      "global_load_dwordx4 v[144:147], v228, s[58:59] sc1\n\t"
      "global_load_dwordx4 v[148:151], v229, s[58:59] sc1\n\t"
      "global_load_dwordx4 v[152:155], v230, s[58:59] sc1\n\t"
      "global_load_dwordx4 v[128:131], v224, s[60:61] sc1\n\t"
      "global_load_dwordx4 v[132:135], v225, s[60:61] sc1\n\t"
      "global_load_dwordx4 v[136:139], v226, s[60:61] sc1\n\t"
      "s_waitcnt vmcnt(5)\n\t"
      "v_max3_u32 v234, v144, v145, v146\n\t"
      "v_max_u32_e32 v234, v234, v147\n\t"
      "v_cmp_eq_u32_e32 vcc, -1, v234\n\t"
      "s_cbranch_vccnz FQ25L_%=\n\t"
      "FQ25X_%=:\n\t"
      "s_branch FQ25B_%=\n\t"
      "FQ26R_%=:\n\t"
      "s_mov_b32 s51, 0\n\t"
      "FQ26L_%=:\n\t"
      "s_add_u32 s51, s51, 1\n\t"
      "s_cmp_lt_u32 s51, 262144\n\t"
      "s_cbranch_scc0 FQ26X_%=\n\t"
      "s_waitcnt vmcnt(0)\n\t"
      "s_sleep 4\n\t"
      "global_load_dwordx4 v[148:151], v229, s[58:59] sc1\n\t"
      "global_load_dwordx4 v[152:155], v230, s[58:59] sc1\n\t"
      "global_load_dwordx4 v[128:131], v224, s[60:61] sc1\n\t"
      "global_load_dwordx4 v[132:135], v225, s[60:61] sc1\n\t"
      "global_load_dwordx4 v[136:139], v226, s[60:61] sc1\n\t"
      "global_load_dwordx4 v[140:143], v227, s[60:61] sc1\n\t"
      "s_waitcnt vmcnt(5)\n\t"
      "v_max3_u32 v234, v148, v149, v150\n\t"
      "v_max_u32_e32 v234, v234, v151\n\t"
      "v_cmp_eq_u32_e32 vcc, -1, v234\n\t"
      "s_cbranch_vccnz FQ26L_%=\n\t"
      "FQ26X_%=:\n\t"
      "s_branch FQ26B_%=\n\t"
      "FQ27R_%=:\n\t"
      "s_mov_b32 s51, 0\n\t"
      "FQ27L_%=:\n\t"
      "s_add_u32 s51, s51, 1\n\t"
      "s_cmp_lt_u32 s51, 262144\n\t"
      "s_cbranch_scc0 FQ27X_%=\n\t"
      "s_waitcnt vmcnt(0)\n\t"
      "s_sleep 4\n\t"
      "global_load_dwordx4 v[152:155], v230, s[58:59] sc1\n\t"
      "global_load_dwordx4 v[128:131], v224, s[60:61] sc1\n\t"
      "global_load_dwordx4 v[132:135], v225, s[60:61] sc1\n\t"
      "global_load_dwordx4 v[136:139], v226, s[60:61] sc1\n\t"
      "global_load_dwordx4 v[140:143], v227, s[60:61] sc1\n\t"
      "global_load_dwordx4 v[144:147], v228, s[60:61] sc1\n\t"
      "s_waitcnt vmcnt(5)\n\t"
      "v_max3_u32 v234, v152, v153, v154\n\t"
      "v_max_u32_e32 v234, v234, v155\n\t"
      "v_cmp_eq_u32_e32 vcc, -1, v234\n\t"
      "s_cbranch_vccnz FQ27L_%=\n\t"
      "FQ27X_%=:\n\t"
      "s_branch FQ27B_%=\n\t"
      "FQ28R_%=:\n\t"
      "s_mov_b32 s51, 0\n\t"
      "FQ28L_%=:\n\t"
      "s_add_u32 s51, s51, 1\n\t"
      "s_cmp_lt_u32 s51, 262144\n\t"
      "s_cbranch_scc0 FQ28X_%=\n\t"
      "s_waitcnt vmcnt(0)\n\t"
      "s_sleep 4\n\t"
      "global_load_dwordx4 v[128:131], v224, s[60:61] sc1\n\t"
      "global_load_dwordx4 v[132:135], v225, s[60:61] sc1\n\t"
      "global_load_dwordx4 v[136:139], v226, s[60:61] sc1\n\t"
      "global_load_dwordx4 v[140:143], v227, s[60:61] sc1\n\t"
      "global_load_dwordx4 v[144:147], v228, s[60:61] sc1\n\t"
      "global_load_dwordx4 v[148:151], v229, s[60:61] sc1\n\t"
      "s_waitcnt vmcnt(5)\n\t"
      "v_max3_u32 v234, v128, v129, v130\n\t"
      "v_max_u32_e32 v234, v234, v131\n\t"
      "v_cmp_eq_u32_e32 vcc, -1, v234\n\t"
      "s_cbranch_vccnz FQ28L_%=\n\t"
      "FQ28X_%=:\n\t"
      "s_branch FQ28B_%=\n\t"
      "NQ3R_%=:\n\t"
      "s_mov_b32 s51, 0\n\t"
      "NQ3L_%=:\n\t"
      "s_add_u32 s51, s51, 1\n\t"
      "s_cmp_lt_u32 s51, 262144\n\t"
      "s_cbranch_scc0 NQ3X_%=\n\t"
      "s_waitcnt vmcnt(0)\n\t"
      "s_sleep 1\n\t"
      "global_load_dwordx4 v[128:131], v224, s[58:59] sc1\n\t"
      "global_load_dwordx4 v[132:135], v225, s[58:59] sc1\n\t"
      "global_load_dwordx4 v[136:139], v226, s[58:59] sc1\n\t"
      "global_load_dwordx4 v[140:143], v227, s[58:59] sc1\n\t"
      "global_load_dwordx4 v[144:147], v228, s[58:59] sc1\n\t"
      "global_load_dwordx4 v[148:151], v229, s[58:59] sc1\n\t"
      "global_load_dwordx4 v[152:155], v230, s[58:59] sc1\n\t"
      "s_waitcnt vmcnt(0)\n\t"
      "v_max3_u32 v234, v128, v129, v130\n\t"
      "v_max3_u32 v234, v234, v131, v132\n\t"
      "v_max3_u32 v234, v234, v133, v134\n\t"
      "v_max3_u32 v234, v234, v135, v136\n\t"
      "v_max3_u32 v234, v234, v137, v138\n\t"
      "v_max3_u32 v234, v234, v139, v140\n\t"
      "v_max3_u32 v234, v234, v141, v142\n\t"
      "v_max3_u32 v234, v234, v143, v144\n\t"
      "v_max3_u32 v234, v234, v145, v146\n\t"
      "v_max3_u32 v234, v234, v147, v148\n\t"
      "v_max3_u32 v234, v234, v149, v150\n\t"
      "v_max3_u32 v234, v234, v151, v152\n\t"
      "v_max3_u32 v234, v234, v153, v154\n\t"
      "v_max_u32_e32 v234, v234, v155\n\t"
      "v_cmp_eq_u32_e32 vcc, -1, v234\n\t"
      "s_cbranch_vccnz NQ3L_%=\n\t"
      "NQ3X_%=:\n\t"
      "v_mfma_f32_16x16x32_bf16 v[200:203], v[128:131], v[0:3], 0\n\t"
      "v_mfma_f32_16x16x32_bf16 v[204:207], v[128:131], v[4:7], 0\n\t"
      "v_mfma_f32_16x16x32_bf16 v[208:211], v[128:131], v[8:11], 0\n\t"
      "v_mfma_f32_16x16x32_bf16 v[212:215], v[128:131], v[12:15], 0\n\t"
      "v_mfma_f32_16x16x32_bf16 v[200:203], v[132:135], v[16:19], v[200:203]\n\t"
      "v_mfma_f32_16x16x32_bf16 v[204:207], v[132:135], v[20:23], v[204:207]\n\t"
      "v_mfma_f32_16x16x32_bf16 v[208:211], v[132:135], v[24:27], v[208:211]\n\t"
      "v_mfma_f32_16x16x32_bf16 v[212:215], v[132:135], v[28:31], v[212:215]\n\t"
      "v_mfma_f32_16x16x32_bf16 v[200:203], v[136:139], v[32:35], v[200:203]\n\t"
      "v_mfma_f32_16x16x32_bf16 v[204:207], v[136:139], v[36:39], v[204:207]\n\t"
      "v_mfma_f32_16x16x32_bf16 v[208:211], v[136:139], v[40:43], v[208:211]\n\t"
      "v_mfma_f32_16x16x32_bf16 v[212:215], v[136:139], v[44:47], v[212:215]\n\t"
      "v_mfma_f32_16x16x32_bf16 v[200:203], v[140:143], v[48:51], v[200:203]\n\t"
      "v_mfma_f32_16x16x32_bf16 v[204:207], v[140:143], v[52:55], v[204:207]\n\t"
      "v_mfma_f32_16x16x32_bf16 v[208:211], v[140:143], v[56:59], v[208:211]\n\t"
      "v_mfma_f32_16x16x32_bf16 v[212:215], v[140:143], v[60:63], v[212:215]\n\t"
      "v_mfma_f32_16x16x32_bf16 v[200:203], v[144:147], v[64:67], v[200:203]\n\t"
      "v_mfma_f32_16x16x32_bf16 v[204:207], v[144:147], v[68:71], v[204:207]\n\t"
      "v_mfma_f32_16x16x32_bf16 v[208:211], v[144:147], v[72:75], v[208:211]\n\t"
      "v_mfma_f32_16x16x32_bf16 v[212:215], v[144:147], v[76:79], v[212:215]\n\t"
      "v_mfma_f32_16x16x32_bf16 v[200:203], v[148:151], v[80:83], v[200:203]\n\t"
      "v_mfma_f32_16x16x32_bf16 v[204:207], v[148:151], v[84:87], v[204:207]\n\t"
      "v_mfma_f32_16x16x32_bf16 v[208:211], v[148:151], v[88:91], v[208:211]\n\t"
      "v_mfma_f32_16x16x32_bf16 v[212:215], v[148:151], v[92:95], v[212:215]\n\t"
      "v_mfma_f32_16x16x32_bf16 v[200:203], v[152:155], v[96:99], v[200:203]\n\t"
      "v_mfma_f32_16x16x32_bf16 v[204:207], v[152:155], v[100:103], v[204:207]\n\t"
      "v_mfma_f32_16x16x32_bf16 v[208:211], v[152:155], v[104:107], v[208:211]\n\t"
      "v_mfma_f32_16x16x32_bf16 v[212:215], v[152:155], v[108:111], v[212:215]\n\t"
      "global_load_dwordx4 v[128:131], v224, s[60:61] sc1\n\t"
      "global_load_dwordx4 v[132:135], v225, s[60:61] sc1\n\t"
      "global_load_dwordx4 v[136:139], v226, s[60:61] sc1\n\t"
      "global_load_dwordx4 v[140:143], v227, s[60:61] sc1\n\t"
      "global_load_dwordx4 v[144:147], v228, s[60:61] sc1\n\t"
      "global_load_dwordx4 v[148:151], v229, s[60:61] sc1\n\t"
      "global_load_dwordx4 v[152:155], v230, s[60:61] sc1\n\t"
      "s_waitcnt vmcnt(0)\n\t"
      "s_nop 7\n\t"
      "s_branch NQ3B_%=\n\t"
      "FQ29R_%=:\n\t"
      "s_mov_b32 s51, 0\n\t"
      "FQ29L_%=:\n\t"
      "s_add_u32 s51, s51, 1\n\t"
      "s_cmp_lt_u32 s51, 262144\n\t"
      "s_cbranch_scc0 FQ29X_%=\n\t"
      "s_waitcnt vmcnt(0)\n\t"
      "s_sleep 4\n\t"
      "global_load_dwordx4 v[132:135], v225, s[60:61] sc1\n\t"
      "global_load_dwordx4 v[136:139], v226, s[60:61] sc1\n\t"
      "global_load_dwordx4 v[140:143], v227, s[60:61] sc1\n\t"
      "global_load_dwordx4 v[144:147], v228, s[60:61] sc1\n\t"
      "global_load_dwordx4 v[148:151], v229, s[60:61] sc1\n\t"
      "global_load_dwordx4 v[152:155], v230, s[60:61] sc1\n\t"
      "s_waitcnt vmcnt(5)\n\t"
      "v_max3_u32 v234, v132, v133, v134\n\t"
      "v_max_u32_e32 v234, v234, v135\n\t"
      "v_cmp_eq_u32_e32 vcc, -1, v234\n\t"
      "s_cbranch_vccnz FQ29L_%=\n\t"
      "FQ29X_%=:\n\t"
      "s_branch FQ29B_%=\n\t"
      "FQ30R_%=:\n\t"
      "s_mov_b32 s51, 0\n\t"
      "FQ30L_%=:\n\t"
      "s_add_u32 s51, s51, 1\n\t"
      "s_cmp_lt_u32 s51, 262144\n\t"
      "s_cbranch_scc0 FQ30X_%=\n\t"
      "s_waitcnt vmcnt(0)\n\t"
      "s_sleep 4\n\t"
      "global_load_dwordx4 v[136:139], v226, s[60:61] sc1\n\t"
      "global_load_dwordx4 v[140:143], v227, s[60:61] sc1\n\t"
      "global_load_dwordx4 v[144:147], v228, s[60:61] sc1\n\t"
      "global_load_dwordx4 v[148:151], v229, s[60:61] sc1\n\t"
      "global_load_dwordx4 v[152:155], v230, s[60:61] sc1\n\t"
      "global_load_dwordx4 v[128:131], v224, s[62:63] sc1\n\t"
      "s_waitcnt vmcnt(5)\n\t"
      "v_max3_u32 v234, v136, v137, v138\n\t"
      "v_max_u32_e32 v234, v234, v139\n\t"
      "v_cmp_eq_u32_e32 vcc, -1, v234\n\t"
      "s_cbranch_vccnz FQ30L_%=\n\t"
      "FQ30X_%=:\n\t"
      "s_branch FQ30B_%=\n\t"
      "FQ31R_%=:\n\t"
      "s_mov_b32 s51, 0\n\t"
      "FQ31L_%=:\n\t"
      "s_add_u32 s51, s51, 1\n\t"
      "s_cmp_lt_u32 s51, 262144\n\t"
      "s_cbranch_scc0 FQ31X_%=\n\t"
      "s_waitcnt vmcnt(0)\n\t"
      "s_sleep 4\n\t"
      "global_load_dwordx4 v[140:143], v227, s[60:61] sc1\n\t"
      "global_load_dwordx4 v[144:147], v228, s[60:61] sc1\n\t"
      "global_load_dwordx4 v[148:151], v229, s[60:61] sc1\n\t"
      "global_load_dwordx4 v[152:155], v230, s[60:61] sc1\n\t"
      "global_load_dwordx4 v[128:131], v224, s[62:63] sc1\n\t"
      "global_load_dwordx4 v[132:135], v225, s[62:63] sc1\n\t"
      "s_waitcnt vmcnt(5)\n\t"
      "v_max3_u32 v234, v140, v141, v142\n\t"
      "v_max_u32_e32 v234, v234, v143\n\t"
      "v_cmp_eq_u32_e32 vcc, -1, v234\n\t"
      "s_cbranch_vccnz FQ31L_%=\n\t"
      "FQ31X_%=:\n\t"
      "s_branch FQ31B_%=\n\t"
      "FQ32R_%=:\n\t"
      "s_mov_b32 s51, 0\n\t"
      "FQ32L_%=:\n\t"
      "s_add_u32 s51, s51, 1\n\t"
      "s_cmp_lt_u32 s51, 262144\n\t"
      "s_cbranch_scc0 FQ32X_%=\n\t"
      "s_waitcnt vmcnt(0)\n\t"
      "s_sleep 4\n\t"
      "global_load_dwordx4 v[144:147], v228, s[60:61] sc1\n\t"
      "global_load_dwordx4 v[148:151], v229, s[60:61] sc1\n\t"
      "global_load_dwordx4 v[152:155], v230, s[60:61] sc1\n\t"
      "global_load_dwordx4 v[128:131], v224, s[62:63] sc1\n\t"
      "global_load_dwordx4 v[132:135], v225, s[62:63] sc1\n\t"
      "global_load_dwordx4 v[136:139], v226, s[62:63] sc1\n\t"
      "s_waitcnt vmcnt(5)\n\t"
      "v_max3_u32 v234, v144, v145, v146\n\t"
      "v_max_u32_e32 v234, v234, v147\n\t"
      "v_cmp_eq_u32_e32 vcc, -1, v234\n\t"
      "s_cbranch_vccnz FQ32L_%=\n\t"
      "FQ32X_%=:\n\t"
      "s_branch FQ32B_%=\n\t"
      "FQ33R_%=:\n\t"
      "s_mov_b32 s51, 0\n\t"
      "FQ33L_%=:\n\t"
      "s_add_u32 s51, s51, 1\n\t"
      "s_cmp_lt_u32 s51, 262144\n\t"
      "s_cbranch_scc0 FQ33X_%=\n\t"
      "s_waitcnt vmcnt(0)\n\t"
      "s_sleep 4\n\t"
      "global_load_dwordx4 v[148:151], v229, s[60:61] sc1\n\t"
      "global_load_dwordx4 v[152:155], v230, s[60:61] sc1\n\t"
      "global_load_dwordx4 v[128:131], v224, s[62:63] sc1\n\t"
      "global_load_dwordx4 v[132:135], v225, s[62:63] sc1\n\t"
      "global_load_dwordx4 v[136:139], v226, s[62:63] sc1\n\t"
      "global_load_dwordx4 v[140:143], v227, s[62:63] sc1\n\t"
      "s_waitcnt vmcnt(5)\n\t"
      "v_max3_u32 v234, v148, v149, v150\n\t"
      "v_max_u32_e32 v234, v234, v151\n\t"
      "v_cmp_eq_u32_e32 vcc, -1, v234\n\t"
      "s_cbranch_vccnz FQ33L_%=\n\t"
      "FQ33X_%=:\n\t"
      "s_branch FQ33B_%=\n\t"
      "FQ34R_%=:\n\t"
      "s_mov_b32 s51, 0\n\t"
      "FQ34L_%=:\n\t"
      "s_add_u32 s51, s51, 1\n\t"
      "s_cmp_lt_u32 s51, 262144\n\t"
      "s_cbranch_scc0 FQ34X_%=\n\t"
      "s_waitcnt vmcnt(0)\n\t"
      "s_sleep 4\n\t"
      "global_load_dwordx4 v[152:155], v230, s[60:61] sc1\n\t"
      "global_load_dwordx4 v[128:131], v224, s[62:63] sc1\n\t"
      "global_load_dwordx4 v[132:135], v225, s[62:63] sc1\n\t"
      "global_load_dwordx4 v[136:139], v226, s[62:63] sc1\n\t"
      "global_load_dwordx4 v[140:143], v227, s[62:63] sc1\n\t"
      "global_load_dwordx4 v[144:147], v228, s[62:63] sc1\n\t"
      "s_waitcnt vmcnt(5)\n\t"
      "v_max3_u32 v234, v152, v153, v154\n\t"
      "v_max_u32_e32 v234, v234, v155\n\t"
      "v_cmp_eq_u32_e32 vcc, -1, v234\n\t"
      "s_cbranch_vccnz FQ34L_%=\n\t"
      "FQ34X_%=:\n\t"
      "s_branch FQ34B_%=\n\t"
      "FQ35R_%=:\n\t"
      "s_mov_b32 s51, 0\n\t"
      "FQ35L_%=:\n\t"
      "s_add_u32 s51, s51, 1\n\t"
      "s_cmp_lt_u32 s51, 262144\n\t"
      "s_cbranch_scc0 FQ35X_%=\n\t"
      "s_waitcnt vmcnt(0)\n\t"
      "s_sleep 4\n\t"
      "global_load_dwordx4 v[128:131], v224, s[62:63] sc1\n\t"
      "global_load_dwordx4 v[132:135], v225, s[62:63] sc1\n\t"
      "global_load_dwordx4 v[136:139], v226, s[62:63] sc1\n\t"
      "global_load_dwordx4 v[140:143], v227, s[62:63] sc1\n\t"
      "global_load_dwordx4 v[144:147], v228, s[62:63] sc1\n\t"
      "global_load_dwordx4 v[148:151], v229, s[62:63] sc1\n\t"
      "s_waitcnt vmcnt(5)\n\t"
      "v_max3_u32 v234, v128, v129, v130\n\t"
      "v_max_u32_e32 v234, v234, v131\n\t"
      "v_cmp_eq_u32_e32 vcc, -1, v234\n\t"
      "s_cbranch_vccnz FQ35L_%=\n\t"
      "FQ35X_%=:\n\t"
      "s_branch FQ35B_%=\n\t"
      "NQ4R_%=:\n\t"
      "s_mov_b32 s51, 0\n\t"
      "NQ4L_%=:\n\t"
      "s_add_u32 s51, s51, 1\n\t"
      "s_cmp_lt_u32 s51, 262144\n\t"
      "s_cbranch_scc0 NQ4X_%=\n\t"
      "s_waitcnt vmcnt(0)\n\t"
      "s_sleep 1\n\t"
      "global_load_dwordx4 v[128:131], v224, s[60:61] sc1\n\t"
      "global_load_dwordx4 v[132:135], v225, s[60:61] sc1\n\t"
      "global_load_dwordx4 v[136:139], v226, s[60:61] sc1\n\t"
      "global_load_dwordx4 v[140:143], v227, s[60:61] sc1\n\t"
      "global_load_dwordx4 v[144:147], v228, s[60:61] sc1\n\t"
      "global_load_dwordx4 v[148:151], v229, s[60:61] sc1\n\t"
      "global_load_dwordx4 v[152:155], v230, s[60:61] sc1\n\t"
      "s_waitcnt vmcnt(0)\n\t"
      "v_max3_u32 v234, v128, v129, v130\n\t"
      "v_max3_u32 v234, v234, v131, v132\n\t"
      "v_max3_u32 v234, v234, v133, v134\n\t"
      "v_max3_u32 v234, v234, v135, v136\n\t"
      "v_max3_u32 v234, v234, v137, v138\n\t"
      "v_max3_u32 v234, v234, v139, v140\n\t"
      "v_max3_u32 v234, v234, v141, v142\n\t"
      "v_max3_u32 v234, v234, v143, v144\n\t"
      "v_max3_u32 v234, v234, v145, v146\n\t"
      "v_max3_u32 v234, v234, v147, v148\n\t"
      "v_max3_u32 v234, v234, v149, v150\n\t"
      "v_max3_u32 v234, v234, v151, v152\n\t"
      "v_max3_u32 v234, v234, v153, v154\n\t"
      "v_max_u32_e32 v234, v234, v155\n\t"
      "v_cmp_eq_u32_e32 vcc, -1, v234\n\t"
      "s_cbranch_vccnz NQ4L_%=\n\t"
      "NQ4X_%=:\n\t"
      "v_mfma_f32_16x16x32_bf16 v[184:187], v[128:131], v[0:3], 0\n\t"
      "v_mfma_f32_16x16x32_bf16 v[188:191], v[128:131], v[4:7], 0\n\t"
      "v_mfma_f32_16x16x32_bf16 v[192:195], v[128:131], v[8:11], 0\n\t"
      "v_mfma_f32_16x16x32_bf16 v[196:199], v[128:131], v[12:15], 0\n\t"
      "v_mfma_f32_16x16x32_bf16 v[184:187], v[132:135], v[16:19], v[184:187]\n\t"
      "v_mfma_f32_16x16x32_bf16 v[188:191], v[132:135], v[20:23], v[188:191]\n\t"
      "v_mfma_f32_16x16x32_bf16 v[192:195], v[132:135], v[24:27], v[192:195]\n\t"
      "v_mfma_f32_16x16x32_bf16 v[196:199], v[132:135], v[28:31], v[196:199]\n\t"
      "v_mfma_f32_16x16x32_bf16 v[184:187], v[136:139], v[32:35], v[184:187]\n\t"
      "v_mfma_f32_16x16x32_bf16 v[188:191], v[136:139], v[36:39], v[188:191]\n\t"
      "v_mfma_f32_16x16x32_bf16 v[192:195], v[136:139], v[40:43], v[192:195]\n\t"
      "v_mfma_f32_16x16x32_bf16 v[196:199], v[136:139], v[44:47], v[196:199]\n\t"
      "v_mfma_f32_16x16x32_bf16 v[184:187], v[140:143], v[48:51], v[184:187]\n\t"
      "v_mfma_f32_16x16x32_bf16 v[188:191], v[140:143], v[52:55], v[188:191]\n\t"
      "v_mfma_f32_16x16x32_bf16 v[192:195], v[140:143], v[56:59], v[192:195]\n\t"
      "v_mfma_f32_16x16x32_bf16 v[196:199], v[140:143], v[60:63], v[196:199]\n\t"
      "v_mfma_f32_16x16x32_bf16 v[184:187], v[144:147], v[64:67], v[184:187]\n\t"
      "v_mfma_f32_16x16x32_bf16 v[188:191], v[144:147], v[68:71], v[188:191]\n\t"
      "v_mfma_f32_16x16x32_bf16 v[192:195], v[144:147], v[72:75], v[192:195]\n\t"
      "v_mfma_f32_16x16x32_bf16 v[196:199], v[144:147], v[76:79], v[196:199]\n\t"
      "v_mfma_f32_16x16x32_bf16 v[184:187], v[148:151], v[80:83], v[184:187]\n\t"
      "v_mfma_f32_16x16x32_bf16 v[188:191], v[148:151], v[84:87], v[188:191]\n\t"
      "v_mfma_f32_16x16x32_bf16 v[192:195], v[148:151], v[88:91], v[192:195]\n\t"
      "v_mfma_f32_16x16x32_bf16 v[196:199], v[148:151], v[92:95], v[196:199]\n\t"
      "v_mfma_f32_16x16x32_bf16 v[184:187], v[152:155], v[96:99], v[184:187]\n\t"
      "v_mfma_f32_16x16x32_bf16 v[188:191], v[152:155], v[100:103], v[188:191]\n\t"
      "v_mfma_f32_16x16x32_bf16 v[192:195], v[152:155], v[104:107], v[192:195]\n\t"
      "v_mfma_f32_16x16x32_bf16 v[196:199], v[152:155], v[108:111], v[196:199]\n\t"
      "global_load_dwordx4 v[128:131], v224, s[62:63] sc1\n\t"
      "global_load_dwordx4 v[132:135], v225, s[62:63] sc1\n\t"
      "global_load_dwordx4 v[136:139], v226, s[62:63] sc1\n\t"
      "global_load_dwordx4 v[140:143], v227, s[62:63] sc1\n\t"
      "global_load_dwordx4 v[144:147], v228, s[62:63] sc1\n\t"
      "global_load_dwordx4 v[148:151], v229, s[62:63] sc1\n\t"
      "global_load_dwordx4 v[152:155], v230, s[62:63] sc1\n\t"
      "s_waitcnt vmcnt(0)\n\t"
      "s_nop 7\n\t"
      "s_branch NQ4B_%=\n\t"
      "FQ36R_%=:\n\t"
      "s_mov_b32 s51, 0\n\t"
      "FQ36L_%=:\n\t"
      "s_add_u32 s51, s51, 1\n\t"
      "s_cmp_lt_u32 s51, 262144\n\t"
      "s_cbranch_scc0 FQ36X_%=\n\t"
      "s_waitcnt vmcnt(0)\n\t"
      "s_sleep 4\n\t"
      "global_load_dwordx4 v[132:135], v225, s[62:63] sc1\n\t"
      "global_load_dwordx4 v[136:139], v226, s[62:63] sc1\n\t"
      "global_load_dwordx4 v[140:143], v227, s[62:63] sc1\n\t"
      "global_load_dwordx4 v[144:147], v228, s[62:63] sc1\n\t"
      "global_load_dwordx4 v[148:151], v229, s[62:63] sc1\n\t"
      "global_load_dwordx4 v[152:155], v230, s[62:63] sc1\n\t"
      "s_waitcnt vmcnt(5)\n\t"
      "v_max3_u32 v234, v132, v133, v134\n\t"
      "v_max_u32_e32 v234, v234, v135\n\t"
      "v_cmp_eq_u32_e32 vcc, -1, v234\n\t"
      "s_cbranch_vccnz FQ36L_%=\n\t"
      "FQ36X_%=:\n\t"
      "s_branch FQ36B_%=\n\t"
      "FQ37R_%=:\n\t"
      "s_mov_b32 s51, 0\n\t"
      "FQ37L_%=:\n\t"
      "s_add_u32 s51, s51, 1\n\t"
      "s_cmp_lt_u32 s51, 262144\n\t"
      "s_cbranch_scc0 FQ37X_%=\n\t"
      "s_waitcnt vmcnt(0)\n\t"
      "s_sleep 4\n\t"
      "global_load_dwordx4 v[136:139], v226, s[62:63] sc1\n\t"
      "global_load_dwordx4 v[140:143], v227, s[62:63] sc1\n\t"
      "global_load_dwordx4 v[144:147], v228, s[62:63] sc1\n\t"
      "global_load_dwordx4 v[148:151], v229, s[62:63] sc1\n\t"
      "global_load_dwordx4 v[152:155], v230, s[62:63] sc1\n\t"
      "global_load_dwordx4 v[128:131], v224, s[64:65] sc1\n\t"
      "s_waitcnt vmcnt(5)\n\t"
      "v_max3_u32 v234, v136, v137, v138\n\t"
      "v_max_u32_e32 v234, v234, v139\n\t"
      "v_cmp_eq_u32_e32 vcc, -1, v234\n\t"
      "s_cbranch_vccnz FQ37L_%=\n\t"
      "FQ37X_%=:\n\t"
      "s_branch FQ37B_%=\n\t"
      "FQ38R_%=:\n\t"
      "s_mov_b32 s51, 0\n\t"
      "FQ38L_%=:\n\t"
      "s_add_u32 s51, s51, 1\n\t"
      "s_cmp_lt_u32 s51, 262144\n\t"
      "s_cbranch_scc0 FQ38X_%=\n\t"
      "s_waitcnt vmcnt(0)\n\t"
      "s_sleep 4\n\t"
      "global_load_dwordx4 v[140:143], v227, s[62:63] sc1\n\t"
      "global_load_dwordx4 v[144:147], v228, s[62:63] sc1\n\t"
      "global_load_dwordx4 v[148:151], v229, s[62:63] sc1\n\t"
      "global_load_dwordx4 v[152:155], v230, s[62:63] sc1\n\t"
      "global_load_dwordx4 v[128:131], v224, s[64:65] sc1\n\t"
      "global_load_dwordx4 v[132:135], v225, s[64:65] sc1\n\t"
      "s_waitcnt vmcnt(5)\n\t"
      "v_max3_u32 v234, v140, v141, v142\n\t"
      "v_max_u32_e32 v234, v234, v143\n\t"
      "v_cmp_eq_u32_e32 vcc, -1, v234\n\t"
      "s_cbranch_vccnz FQ38L_%=\n\t"
      "FQ38X_%=:\n\t"
      "s_branch FQ38B_%=\n\t"
      "FQ39R_%=:\n\t"
      "s_mov_b32 s51, 0\n\t"
      "FQ39L_%=:\n\t"
      "s_add_u32 s51, s51, 1\n\t"
      "s_cmp_lt_u32 s51, 262144\n\t"
      "s_cbranch_scc0 FQ39X_%=\n\t"
      "s_waitcnt vmcnt(0)\n\t"
      "s_sleep 4\n\t"
      "global_load_dwordx4 v[144:147], v228, s[62:63] sc1\n\t"
      "global_load_dwordx4 v[148:151], v229, s[62:63] sc1\n\t"
      "global_load_dwordx4 v[152:155], v230, s[62:63] sc1\n\t"
      "global_load_dwordx4 v[128:131], v224, s[64:65] sc1\n\t"
      "global_load_dwordx4 v[132:135], v225, s[64:65] sc1\n\t"
      "global_load_dwordx4 v[136:139], v226, s[64:65] sc1\n\t"
      "s_waitcnt vmcnt(5)\n\t"
      "v_max3_u32 v234, v144, v145, v146\n\t"
      "v_max_u32_e32 v234, v234, v147\n\t"
      "v_cmp_eq_u32_e32 vcc, -1, v234\n\t"
      "s_cbranch_vccnz FQ39L_%=\n\t"
      "FQ39X_%=:\n\t"
      "s_branch FQ39B_%=\n\t"
      "FQ40R_%=:\n\t"
      "s_mov_b32 s51, 0\n\t"
      "FQ40L_%=:\n\t"
      "s_add_u32 s51, s51, 1\n\t"
      "s_cmp_lt_u32 s51, 262144\n\t"
      "s_cbranch_scc0 FQ40X_%=\n\t"
      "s_waitcnt vmcnt(0)\n\t"
      "s_sleep 4\n\t"
      "global_load_dwordx4 v[148:151], v229, s[62:63] sc1\n\t"
      "global_load_dwordx4 v[152:155], v230, s[62:63] sc1\n\t"
      "global_load_dwordx4 v[128:131], v224, s[64:65] sc1\n\t"
      "global_load_dwordx4 v[132:135], v225, s[64:65] sc1\n\t"
      "global_load_dwordx4 v[136:139], v226, s[64:65] sc1\n\t"
      "global_load_dwordx4 v[140:143], v227, s[64:65] sc1\n\t"
      "s_waitcnt vmcnt(5)\n\t"
      "v_max3_u32 v234, v148, v149, v150\n\t"
      "v_max_u32_e32 v234, v234, v151\n\t"
      "v_cmp_eq_u32_e32 vcc, -1, v234\n\t"
      "s_cbranch_vccnz FQ40L_%=\n\t"
      "FQ40X_%=:\n\t"
      "s_branch FQ40B_%=\n\t"
      "FQ41R_%=:\n\t"
      "s_mov_b32 s51, 0\n\t"
      "FQ41L_%=:\n\t"
      "s_add_u32 s51, s51, 1\n\t"
      "s_cmp_lt_u32 s51, 262144\n\t"
      "s_cbranch_scc0 FQ41X_%=\n\t"
      "s_waitcnt vmcnt(0)\n\t"
      "s_sleep 4\n\t"
      "global_load_dwordx4 v[152:155], v230, s[62:63] sc1\n\t"
      "global_load_dwordx4 v[128:131], v224, s[64:65] sc1\n\t"
      "global_load_dwordx4 v[132:135], v225, s[64:65] sc1\n\t"
      "global_load_dwordx4 v[136:139], v226, s[64:65] sc1\n\t"
      "global_load_dwordx4 v[140:143], v227, s[64:65] sc1\n\t"
      "global_load_dwordx4 v[144:147], v228, s[64:65] sc1\n\t"
      "s_waitcnt vmcnt(5)\n\t"
      "v_max3_u32 v234, v152, v153, v154\n\t"
      "v_max_u32_e32 v234, v234, v155\n\t"
      "v_cmp_eq_u32_e32 vcc, -1, v234\n\t"
      "s_cbranch_vccnz FQ41L_%=\n\t"
      "FQ41X_%=:\n\t"
      "s_branch FQ41B_%=\n\t"
      "FQ42R_%=:\n\t"
      "s_mov_b32 s51, 0\n\t"
      "FQ42L_%=:\n\t"
      "s_add_u32 s51, s51, 1\n\t"
      "s_cmp_lt_u32 s51, 262144\n\t"
      "s_cbranch_scc0 FQ42X_%=\n\t"
      "s_sleep 1\n\t"
      "s_mov_b64 exec, 15\n\t"
      "v_mov_b64_e32 v[232:233], 0\n\t"
      "global_atomic_or_x2 v[232:233], v247, v[232:233], s[12:13] sc0 sc1\n\t"
      "s_waitcnt vmcnt(0)\n\t"
      "v_subrev_u32_e32 v232, s14, v232\n\t"
      "v_cmp_ne_u32_e64 s[48:49], s15, v233\n\t"
      "v_subrev_u32_e32 v232, 6, v232\n\t"
      "v_cmp_lt_u32_e32 vcc, 7, v232\n\t"
      "s_mov_b64 exec, -1\n\t"
      "s_or_b64 vcc, vcc, s[48:49]\n\t"
      "s_cbranch_vccnz FQ42L_%=\n\t"
      "FQ42X_%=:\n\t"
      "FQ42DR_%=:\n\t"
      "s_mov_b32 s51, 0\n\t"
      "FQ42DL_%=:\n\t"
      "s_add_u32 s51, s51, 1\n\t"
      "s_cmp_lt_u32 s51, 262144\n\t"
      "s_cbranch_scc0 FQ42DX_%=\n\t"
      "s_waitcnt vmcnt(0)\n\t"
      "s_sleep 1\n\t"
      "global_load_dwordx4 v[128:131], v224, s[64:65] sc1\n\t"
      "global_load_dwordx4 v[132:135], v225, s[64:65] sc1\n\t"
      "global_load_dwordx4 v[136:139], v226, s[64:65] sc1\n\t"
      "global_load_dwordx4 v[140:143], v227, s[64:65] sc1\n\t"
      "global_load_dwordx4 v[144:147], v228, s[64:65] sc1\n\t"
      "global_load_dwordx4 v[148:151], v229, s[64:65] sc1\n\t"
      "s_waitcnt vmcnt(5)\n\t"
      "v_max3_u32 v234, v128, v129, v130\n\t"
      "v_max_u32_e32 v234, v234, v131\n\t"
      "v_cmp_eq_u32_e32 vcc, -1, v234\n\t"
      "s_cbranch_vccnz FQ42DL_%=\n\t"
      "FQ42DX_%=:\n\t"
      "s_branch FQ42B_%=\n\t"
      "NQ5R_%=:\n\t"
      "s_mov_b32 s51, 0\n\t"
      "NQ5L_%=:\n\t"
      "s_add_u32 s51, s51, 1\n\t"
      "s_cmp_lt_u32 s51, 262144\n\t"
      "s_cbranch_scc0 NQ5X_%=\n\t"
      "s_waitcnt vmcnt(0)\n\t"
      "s_sleep 1\n\t"
      "global_load_dwordx4 v[128:131], v224, s[62:63] sc1\n\t"
      "global_load_dwordx4 v[132:135], v225, s[62:63] sc1\n\t"
      "global_load_dwordx4 v[136:139], v226, s[62:63] sc1\n\t"
      "global_load_dwordx4 v[140:143], v227, s[62:63] sc1\n\t"
      "global_load_dwordx4 v[144:147], v228, s[62:63] sc1\n\t"
      "global_load_dwordx4 v[148:151], v229, s[62:63] sc1\n\t"
      "global_load_dwordx4 v[152:155], v230, s[62:63] sc1\n\t"
      "s_waitcnt vmcnt(0)\n\t"
      "s_nop 0\n\t"
      "v_max3_u32 v234, v128, v129, v130\n\t"
      "v_max3_u32 v234, v234, v131, v132\n\t"
      "v_max3_u32 v234, v234, v133, v134\n\t"
      "v_max3_u32 v234, v234, v135, v136\n\t"
      "v_max3_u32 v234, v234, v137, v138\n\t"
      "v_max3_u32 v234, v234, v139, v140\n\t"
      "v_max3_u32 v234, v234, v141, v142\n\t"
      "v_max3_u32 v234, v234, v143, v144\n\t"
      "v_max3_u32 v234, v234, v145, v146\n\t"
      "v_max3_u32 v234, v234, v147, v148\n\t"
      "v_max3_u32 v234, v234, v149, v150\n\t"
      "v_max3_u32 v234, v234, v151, v152\n\t"
      "v_max3_u32 v234, v234, v153, v154\n\t"
      "v_max_u32_e32 v234, v234, v155\n\t"
      "v_cmp_eq_u32_e32 vcc, -1, v234\n\t"
      "s_cbranch_vccnz NQ5L_%=\n\t"
      "NQ5X_%=:\n\t"
      "v_mfma_f32_16x16x32_bf16 v[200:203], v[128:131], v[0:3], 0\n\t"
      "v_mfma_f32_16x16x32_bf16 v[204:207], v[128:131], v[4:7], 0\n\t"
      "v_mfma_f32_16x16x32_bf16 v[208:211], v[128:131], v[8:11], 0\n\t"
      "v_mfma_f32_16x16x32_bf16 v[212:215], v[128:131], v[12:15], 0\n\t"
      "v_mfma_f32_16x16x32_bf16 v[200:203], v[132:135], v[16:19], v[200:203]\n\t"
      "v_mfma_f32_16x16x32_bf16 v[204:207], v[132:135], v[20:23], v[204:207]\n\t"
      "v_mfma_f32_16x16x32_bf16 v[208:211], v[132:135], v[24:27], v[208:211]\n\t"
      "v_mfma_f32_16x16x32_bf16 v[212:215], v[132:135], v[28:31], v[212:215]\n\t"
      "v_mfma_f32_16x16x32_bf16 v[200:203], v[136:139], v[32:35], v[200:203]\n\t"
      "v_mfma_f32_16x16x32_bf16 v[204:207], v[136:139], v[36:39], v[204:207]\n\t"
      "v_mfma_f32_16x16x32_bf16 v[208:211], v[136:139], v[40:43], v[208:211]\n\t"
      "v_mfma_f32_16x16x32_bf16 v[212:215], v[136:139], v[44:47], v[212:215]\n\t"
      "v_mfma_f32_16x16x32_bf16 v[200:203], v[140:143], v[48:51], v[200:203]\n\t"
      "v_mfma_f32_16x16x32_bf16 v[204:207], v[140:143], v[52:55], v[204:207]\n\t"
      "v_mfma_f32_16x16x32_bf16 v[208:211], v[140:143], v[56:59], v[208:211]\n\t"
      "v_mfma_f32_16x16x32_bf16 v[212:215], v[140:143], v[60:63], v[212:215]\n\t"
      "v_mfma_f32_16x16x32_bf16 v[200:203], v[144:147], v[64:67], v[200:203]\n\t"
      "v_mfma_f32_16x16x32_bf16 v[204:207], v[144:147], v[68:71], v[204:207]\n\t"
      "v_mfma_f32_16x16x32_bf16 v[208:211], v[144:147], v[72:75], v[208:211]\n\t"
      "v_mfma_f32_16x16x32_bf16 v[212:215], v[144:147], v[76:79], v[212:215]\n\t"
      "v_mfma_f32_16x16x32_bf16 v[200:203], v[148:151], v[80:83], v[200:203]\n\t"
      "v_mfma_f32_16x16x32_bf16 v[204:207], v[148:151], v[84:87], v[204:207]\n\t"
      "v_mfma_f32_16x16x32_bf16 v[208:211], v[148:151], v[88:91], v[208:211]\n\t"
      "v_mfma_f32_16x16x32_bf16 v[212:215], v[148:151], v[92:95], v[212:215]\n\t"
      "v_mfma_f32_16x16x32_bf16 v[200:203], v[152:155], v[96:99], v[200:203]\n\t"
      "v_mfma_f32_16x16x32_bf16 v[204:207], v[152:155], v[100:103], v[204:207]\n\t"
      "v_mfma_f32_16x16x32_bf16 v[208:211], v[152:155], v[104:107], v[208:211]\n\t"
      "v_mfma_f32_16x16x32_bf16 v[212:215], v[152:155], v[108:111], v[212:215]\n\t"
      "global_load_dwordx4 v[128:131], v224, s[64:65] sc1\n\t"
      "global_load_dwordx4 v[132:135], v225, s[64:65] sc1\n\t"
      "global_load_dwordx4 v[136:139], v226, s[64:65] sc1\n\t"
      "global_load_dwordx4 v[140:143], v227, s[64:65] sc1\n\t"
      "global_load_dwordx4 v[144:147], v228, s[64:65] sc1\n\t"
      "global_load_dwordx4 v[148:151], v229, s[64:65] sc1\n\t"
      "global_load_dwordx4 v[152:155], v230, s[64:65] sc1\n\t"
      "s_waitcnt vmcnt(0)\n\t"
      "s_nop 7\n\t"
      "s_branch NQ5B_%=\n\t"
      "FQ43R_%=:\n\t"
      "s_mov_b32 s51, 0\n\t"
      "FQ43L_%=:\n\t"
      "s_add_u32 s51, s51, 1\n\t"
      "s_cmp_lt_u32 s51, 262144\n\t"
      "s_cbranch_scc0 FQ43X_%=\n\t"
      "s_sleep 1\n\t"
      "s_mov_b64 exec, 15\n\t"
      "v_mov_b64_e32 v[232:233], 0\n\t"
      "global_atomic_or_x2 v[232:233], v247, v[232:233], s[12:13] sc0 sc1\n\t"
      "s_waitcnt vmcnt(0)\n\t"
      "v_subrev_u32_e32 v232, s14, v232\n\t"
      "v_cmp_ne_u32_e64 s[48:49], s15, v233\n\t"
      "v_subrev_u32_e32 v232, 6, v232\n\t"
      "v_cmp_lt_u32_e32 vcc, 7, v232\n\t"
      "s_mov_b64 exec, -1\n\t"
      "s_or_b64 vcc, vcc, s[48:49]\n\t"
      "s_cbranch_vccnz FQ43L_%=\n\t"
      "FQ43X_%=:\n\t"
      "FQ43DR_%=:\n\t"
      "s_mov_b32 s51, 0\n\t"
      "FQ43DL_%=:\n\t"
      "s_add_u32 s51, s51, 1\n\t"
      "s_cmp_lt_u32 s51, 262144\n\t"
      "s_cbranch_scc0 FQ43DX_%=\n\t"
      "s_waitcnt vmcnt(0)\n\t"
      "s_sleep 1\n\t"
      "global_load_dwordx4 v[132:135], v225, s[64:65] sc1\n\t"
      "global_load_dwordx4 v[136:139], v226, s[64:65] sc1\n\t"
      "global_load_dwordx4 v[140:143], v227, s[64:65] sc1\n\t"
      "global_load_dwordx4 v[144:147], v228, s[64:65] sc1\n\t"
      "global_load_dwordx4 v[148:151], v229, s[64:65] sc1\n\t"
      "global_load_dwordx4 v[152:155], v230, s[64:65] sc1\n\t"
      "s_waitcnt vmcnt(5)\n\t"
      "v_max3_u32 v234, v132, v133, v134\n\t"
      "v_max_u32_e32 v234, v234, v135\n\t"
      "v_cmp_eq_u32_e32 vcc, -1, v234\n\t"
      "s_cbranch_vccnz FQ43DL_%=\n\t"
      "FQ43DX_%=:\n\t"
      "s_branch FQ43B_%=\n\t"
      "FQ44R_%=:\n\t"
      "s_mov_b32 s51, 0\n\t"
      "FQ44L_%=:\n\t"
      "s_add_u32 s51, s51, 1\n\t"
      "s_cmp_lt_u32 s51, 262144\n\t"
      "s_cbranch_scc0 FQ44X_%=\n\t"
      "s_sleep 1\n\t"
      "s_mov_b64 exec, 15\n\t"
      "v_mov_b64_e32 v[232:233], 0\n\t"
      "global_atomic_or_x2 v[232:233], v247, v[232:233], s[12:13] sc0 sc1\n\t"
      "s_waitcnt vmcnt(0)\n\t"
      "v_subrev_u32_e32 v232, s14, v232\n\t"
      "v_cmp_ne_u32_e64 s[48:49], s15, v233\n\t"
      "v_subrev_u32_e32 v232, 6, v232\n\t"
      "v_cmp_lt_u32_e32 vcc, 7, v232\n\t"
      "s_mov_b64 exec, -1\n\t"
      "s_or_b64 vcc, vcc, s[48:49]\n\t"
      "s_cbranch_vccnz FQ44L_%=\n\t"
      "FQ44X_%=:\n\t"
      "FQ44DR_%=:\n\t"
      "s_mov_b32 s51, 0\n\t"
      "FQ44DL_%=:\n\t"
      "s_add_u32 s51, s51, 1\n\t"
      "s_cmp_lt_u32 s51, 262144\n\t"
      "s_cbranch_scc0 FQ44DX_%=\n\t"
      "s_waitcnt vmcnt(0)\n\t"
      "s_sleep 1\n\t"
      "global_load_dwordx4 v[136:139], v226, s[64:65] sc1\n\t"
      "global_load_dwordx4 v[140:143], v227, s[64:65] sc1\n\t"
      "global_load_dwordx4 v[144:147], v228, s[64:65] sc1\n\t"
      "global_load_dwordx4 v[148:151], v229, s[64:65] sc1\n\t"
      "global_load_dwordx4 v[152:155], v230, s[64:65] sc1\n\t"
      "s_waitcnt vmcnt(4)\n\t"
      "v_max3_u32 v234, v136, v137, v138\n\t"
      "v_max_u32_e32 v234, v234, v139\n\t"
      "v_cmp_eq_u32_e32 vcc, -1, v234\n\t"
      "s_cbranch_vccnz FQ44DL_%=\n\t"
      "FQ44DX_%=:\n\t"
      "s_branch FQ44B_%=\n\t"
      "FQ45R_%=:\n\t"
      "s_mov_b32 s51, 0\n\t"
      "FQ45L_%=:\n\t"
      "s_add_u32 s51, s51, 1\n\t"
      "s_cmp_lt_u32 s51, 262144\n\t"
      "s_cbranch_scc0 FQ45X_%=\n\t"
      "s_sleep 1\n\t"
      "s_mov_b64 exec, 15\n\t"
      "v_mov_b64_e32 v[232:233], 0\n\t"
      "global_atomic_or_x2 v[232:233], v247, v[232:233], s[12:13] sc0 sc1\n\t"
      "s_waitcnt vmcnt(0)\n\t"
      "v_subrev_u32_e32 v232, s14, v232\n\t"
      "v_cmp_ne_u32_e64 s[48:49], s15, v233\n\t"
      "v_subrev_u32_e32 v232, 6, v232\n\t"
      "v_cmp_lt_u32_e32 vcc, 7, v232\n\t"
      "s_mov_b64 exec, -1\n\t"
      "s_or_b64 vcc, vcc, s[48:49]\n\t"
      "s_cbranch_vccnz FQ45L_%=\n\t"
      "FQ45X_%=:\n\t"
      "FQ45DR_%=:\n\t"
      "s_mov_b32 s51, 0\n\t"
      "FQ45DL_%=:\n\t"
      "s_add_u32 s51, s51, 1\n\t"
      "s_cmp_lt_u32 s51, 262144\n\t"
      "s_cbranch_scc0 FQ45DX_%=\n\t"
      "s_waitcnt vmcnt(0)\n\t"
      "s_sleep 1\n\t"
      "global_load_dwordx4 v[140:143], v227, s[64:65] sc1\n\t"
      "global_load_dwordx4 v[144:147], v228, s[64:65] sc1\n\t"
      "global_load_dwordx4 v[148:151], v229, s[64:65] sc1\n\t"
      "global_load_dwordx4 v[152:155], v230, s[64:65] sc1\n\t"
      "s_waitcnt vmcnt(3)\n\t"
      "v_max3_u32 v234, v140, v141, v142\n\t"
      "v_max_u32_e32 v234, v234, v143\n\t"
      "v_cmp_eq_u32_e32 vcc, -1, v234\n\t"
      "s_cbranch_vccnz FQ45DL_%=\n\t"
      "FQ45DX_%=:\n\t"
      "s_branch FQ45B_%=\n\t"
      "FQ46R_%=:\n\t"
      "s_mov_b32 s51, 0\n\t"
      "FQ46L_%=:\n\t"
      "s_add_u32 s51, s51, 1\n\t"
      "s_cmp_lt_u32 s51, 262144\n\t"
      "s_cbranch_scc0 FQ46X_%=\n\t"
      "s_sleep 1\n\t"
      "s_mov_b64 exec, 15\n\t"
      "v_mov_b64_e32 v[232:233], 0\n\t"
      "global_atomic_or_x2 v[232:233], v247, v[232:233], s[12:13] sc0 sc1\n\t"
      "s_waitcnt vmcnt(0)\n\t"
      "v_subrev_u32_e32 v232, s14, v232\n\t"
      "v_cmp_ne_u32_e64 s[48:49], s15, v233\n\t"
      "v_subrev_u32_e32 v232, 6, v232\n\t"
      "v_cmp_lt_u32_e32 vcc, 7, v232\n\t"
      "s_mov_b64 exec, -1\n\t"
      "s_or_b64 vcc, vcc, s[48:49]\n\t"
      "s_cbranch_vccnz FQ46L_%=\n\t"
      "FQ46X_%=:\n\t"
      "FQ46DR_%=:\n\t"
      "s_mov_b32 s51, 0\n\t"
      "FQ46DL_%=:\n\t"
      "s_add_u32 s51, s51, 1\n\t"
      "s_cmp_lt_u32 s51, 262144\n\t"
      "s_cbranch_scc0 FQ46DX_%=\n\t"
      "s_waitcnt vmcnt(0)\n\t"
      "s_sleep 1\n\t"
      "global_load_dwordx4 v[144:147], v228, s[64:65] sc1\n\t"
      "global_load_dwordx4 v[148:151], v229, s[64:65] sc1\n\t"
      "global_load_dwordx4 v[152:155], v230, s[64:65] sc1\n\t"
      "s_waitcnt vmcnt(2)\n\t"
      "v_max3_u32 v234, v144, v145, v146\n\t"
      "v_max_u32_e32 v234, v234, v147\n\t"
      "v_cmp_eq_u32_e32 vcc, -1, v234\n\t"
      "s_cbranch_vccnz FQ46DL_%=\n\t"
      "FQ46DX_%=:\n\t"
      "s_branch FQ46B_%=\n\t"
      "FQ47R_%=:\n\t"
      "s_mov_b32 s51, 0\n\t"
      "FQ47L_%=:\n\t"
      "s_add_u32 s51, s51, 1\n\t"
      "s_cmp_lt_u32 s51, 262144\n\t"
      "s_cbranch_scc0 FQ47X_%=\n\t"
      "s_sleep 1\n\t"
      "s_mov_b64 exec, 15\n\t"
      "v_mov_b64_e32 v[232:233], 0\n\t"
      "global_atomic_or_x2 v[232:233], v247, v[232:233], s[12:13] sc0 sc1\n\t"
      "s_waitcnt vmcnt(0)\n\t"
      "v_subrev_u32_e32 v232, s14, v232\n\t"
      "v_cmp_ne_u32_e64 s[48:49], s15, v233\n\t"
      "v_subrev_u32_e32 v232, 6, v232\n\t"
      "v_cmp_lt_u32_e32 vcc, 7, v232\n\t"
      "s_mov_b64 exec, -1\n\t"
      "s_or_b64 vcc, vcc, s[48:49]\n\t"
      "s_cbranch_vccnz FQ47L_%=\n\t"
      "FQ47X_%=:\n\t"
      "FQ47DR_%=:\n\t"
      "s_mov_b32 s51, 0\n\t"
      "FQ47DL_%=:\n\t"
      "s_add_u32 s51, s51, 1\n\t"
      "s_cmp_lt_u32 s51, 262144\n\t"
      "s_cbranch_scc0 FQ47DX_%=\n\t"
      "s_waitcnt vmcnt(0)\n\t"
      "s_sleep 1\n\t"
      "global_load_dwordx4 v[148:151], v229, s[64:65] sc1\n\t"
      "global_load_dwordx4 v[152:155], v230, s[64:65] sc1\n\t"
      "s_waitcnt vmcnt(1)\n\t"
      "v_max3_u32 v234, v148, v149, v150\n\t"
      "v_max_u32_e32 v234, v234, v151\n\t"
      "v_cmp_eq_u32_e32 vcc, -1, v234\n\t"
      "s_cbranch_vccnz FQ47DL_%=\n\t"
      "FQ47DX_%=:\n\t"
      "s_branch FQ47B_%=\n\t"
      "FQ48R_%=:\n\t"
      "s_mov_b32 s51, 0\n\t"
      "FQ48L_%=:\n\t"
      "s_add_u32 s51, s51, 1\n\t"
      "s_cmp_lt_u32 s51, 262144\n\t"
      "s_cbranch_scc0 FQ48X_%=\n\t"
      "s_sleep 1\n\t"
      "s_mov_b64 exec, 15\n\t"
      "v_mov_b64_e32 v[232:233], 0\n\t"
      "global_atomic_or_x2 v[232:233], v247, v[232:233], s[12:13] sc0 sc1\n\t"
      "s_waitcnt vmcnt(0)\n\t"
      "v_subrev_u32_e32 v232, s14, v232\n\t"
      "v_cmp_ne_u32_e64 s[48:49], s15, v233\n\t"
      "v_subrev_u32_e32 v232, 6, v232\n\t"
      "v_cmp_lt_u32_e32 vcc, 7, v232\n\t"
      "s_mov_b64 exec, -1\n\t"
      "s_or_b64 vcc, vcc, s[48:49]\n\t"
      "s_cbranch_vccnz FQ48L_%=\n\t"
      "FQ48X_%=:\n\t"
      "FQ48DR_%=:\n\t"
      "s_mov_b32 s51, 0\n\t"
      "FQ48DL_%=:\n\t"
      "s_add_u32 s51, s51, 1\n\t"
      "s_cmp_lt_u32 s51, 262144\n\t"
      "s_cbranch_scc0 FQ48DX_%=\n\t"
      "s_waitcnt vmcnt(0)\n\t"
      "s_sleep 1\n\t"
      "global_load_dwordx4 v[152:155], v230, s[64:65] sc1\n\t"
      "s_waitcnt vmcnt(0)\n\t"
      "v_max3_u32 v234, v152, v153, v154\n\t"
      "v_max_u32_e32 v234, v234, v155\n\t"
      "v_cmp_eq_u32_e32 vcc, -1, v234\n\t"
      "s_cbranch_vccnz FQ48DL_%=\n\t"
      "FQ48DX_%=:\n\t"
      "s_branch FQ48B_%=\n\t"
      "NQ6R_%=:\n\t"
      "s_mov_b32 s51, 0\n\t"
      "NQ6L_%=:\n\t"
      "s_add_u32 s51, s51, 1\n\t"
      "s_cmp_lt_u32 s51, 262144\n\t"
      "s_cbranch_scc0 NQ6X_%=\n\t"
      "s_waitcnt vmcnt(0)\n\t"
      "s_sleep 1\n\t"
      "global_load_dwordx4 v[128:131], v224, s[64:65] sc1\n\t"
      "global_load_dwordx4 v[132:135], v225, s[64:65] sc1\n\t"
      "global_load_dwordx4 v[136:139], v226, s[64:65] sc1\n\t"
      "global_load_dwordx4 v[140:143], v227, s[64:65] sc1\n\t"
      "global_load_dwordx4 v[144:147], v228, s[64:65] sc1\n\t"
      "global_load_dwordx4 v[148:151], v229, s[64:65] sc1\n\t"
      "global_load_dwordx4 v[152:155], v230, s[64:65] sc1\n\t"
      "s_waitcnt vmcnt(0)\n\t"
      "v_max3_u32 v234, v128, v129, v130\n\t"
      "v_max3_u32 v234, v234, v131, v132\n\t"
      "v_max3_u32 v234, v234, v133, v134\n\t"
      "v_max3_u32 v234, v234, v135, v136\n\t"
      "v_max3_u32 v234, v234, v137, v138\n\t"
      "v_max3_u32 v234, v234, v139, v140\n\t"
      "v_max3_u32 v234, v234, v141, v142\n\t"
      "v_max3_u32 v234, v234, v143, v144\n\t"
      "v_max3_u32 v234, v234, v145, v146\n\t"
      "v_max3_u32 v234, v234, v147, v148\n\t"
      "v_max3_u32 v234, v234, v149, v150\n\t"
      "v_max3_u32 v234, v234, v151, v152\n\t"
      "v_max3_u32 v234, v234, v153, v154\n\t"
      "v_max_u32_e32 v234, v234, v155\n\t"
      "v_cmp_eq_u32_e32 vcc, -1, v234\n\t"
      "s_cbranch_vccnz NQ6L_%=\n\t"
      "NQ6X_%=:\n\t"
      "v_mfma_f32_16x16x32_bf16 v[184:187], v[128:131], v[0:3], 0\n\t"
      "v_mfma_f32_16x16x32_bf16 v[188:191], v[128:131], v[4:7], 0\n\t"
      "v_mfma_f32_16x16x32_bf16 v[192:195], v[128:131], v[8:11], 0\n\t"
      "v_mfma_f32_16x16x32_bf16 v[196:199], v[128:131], v[12:15], 0\n\t"
      "v_mfma_f32_16x16x32_bf16 v[184:187], v[132:135], v[16:19], v[184:187]\n\t"
      "v_mfma_f32_16x16x32_bf16 v[188:191], v[132:135], v[20:23], v[188:191]\n\t"
      "v_mfma_f32_16x16x32_bf16 v[192:195], v[132:135], v[24:27], v[192:195]\n\t"
      "v_mfma_f32_16x16x32_bf16 v[196:199], v[132:135], v[28:31], v[196:199]\n\t"
      "v_mfma_f32_16x16x32_bf16 v[184:187], v[136:139], v[32:35], v[184:187]\n\t"
      "v_mfma_f32_16x16x32_bf16 v[188:191], v[136:139], v[36:39], v[188:191]\n\t"
      "v_mfma_f32_16x16x32_bf16 v[192:195], v[136:139], v[40:43], v[192:195]\n\t"
      "v_mfma_f32_16x16x32_bf16 v[196:199], v[136:139], v[44:47], v[196:199]\n\t"
      "v_mfma_f32_16x16x32_bf16 v[184:187], v[140:143], v[48:51], v[184:187]\n\t"
      "v_mfma_f32_16x16x32_bf16 v[188:191], v[140:143], v[52:55], v[188:191]\n\t"
      "v_mfma_f32_16x16x32_bf16 v[192:195], v[140:143], v[56:59], v[192:195]\n\t"
      "v_mfma_f32_16x16x32_bf16 v[196:199], v[140:143], v[60:63], v[196:199]\n\t"
      "v_mfma_f32_16x16x32_bf16 v[184:187], v[144:147], v[64:67], v[184:187]\n\t"
      "v_mfma_f32_16x16x32_bf16 v[188:191], v[144:147], v[68:71], v[188:191]\n\t"
      "v_mfma_f32_16x16x32_bf16 v[192:195], v[144:147], v[72:75], v[192:195]\n\t"
      "v_mfma_f32_16x16x32_bf16 v[196:199], v[144:147], v[76:79], v[196:199]\n\t"
      "v_mfma_f32_16x16x32_bf16 v[184:187], v[148:151], v[80:83], v[184:187]\n\t"
      "v_mfma_f32_16x16x32_bf16 v[188:191], v[148:151], v[84:87], v[188:191]\n\t"
      "v_mfma_f32_16x16x32_bf16 v[192:195], v[148:151], v[88:91], v[192:195]\n\t"
      "v_mfma_f32_16x16x32_bf16 v[196:199], v[148:151], v[92:95], v[196:199]\n\t"
      "v_mfma_f32_16x16x32_bf16 v[184:187], v[152:155], v[96:99], v[184:187]\n\t"
      "v_mfma_f32_16x16x32_bf16 v[188:191], v[152:155], v[100:103], v[188:191]\n\t"
      "v_mfma_f32_16x16x32_bf16 v[192:195], v[152:155], v[104:107], v[192:195]\n\t"
      "v_mfma_f32_16x16x32_bf16 v[196:199], v[152:155], v[108:111], v[196:199]\n\t"
      "s_waitcnt vmcnt(0)\n\t"
      "s_nop 7\n\t"
      "s_branch NQ6B_%=\n\t"
      "FP0R_%=:\n\t"
      "s_mov_b32 s51, 0\n\t"
      "FP0L_%=:\n\t"
      "s_add_u32 s51, s51, 1\n\t"
      "s_cmp_lt_u32 s51, 262144\n\t"
      "s_cbranch_scc0 FP0X_%=\n\t"
      "s_waitcnt vmcnt(0)\n\t"
      "s_sleep 1\n\t"
      "global_load_dwordx4 v[128:131], v224, s[52:53] sc1\n\t"
      "global_load_dwordx4 v[132:135], v225, s[52:53] sc1\n\t"
      "global_load_dwordx4 v[136:139], v226, s[52:53] sc1\n\t"
      "global_load_dwordx4 v[140:143], v227, s[52:53] sc1\n\t"
      "global_load_dwordx4 v[144:147], v228, s[52:53] sc1\n\t"
      "global_load_dwordx4 v[148:151], v229, s[52:53] sc1\n\t"
      "global_load_dwordx4 v[152:155], v230, s[52:53] sc1\n\t"
      "s_waitcnt vmcnt(6)\n\t"
      "v_max3_u32 v234, v128, v129, v130\n\t"
      "v_max_u32_e32 v234, v234, v131\n\t"
      "v_cmp_eq_u32_e32 vcc, -1, v234\n\t"
      "s_cbranch_vccnz FP0L_%=\n\t"
      "FP0X_%=:\n\t"
      "s_branch FP0B_%=\n\t"
      "FP1R_%=:\n\t"
      "s_mov_b32 s51, 0\n\t"
      "FP1L_%=:\n\t"
      "s_add_u32 s51, s51, 1\n\t"
      "s_cmp_lt_u32 s51, 262144\n\t"
      "s_cbranch_scc0 FP1X_%=\n\t"
      "s_waitcnt vmcnt(0)\n\t"
      "s_sleep 1\n\t"
      "global_load_dwordx4 v[132:135], v225, s[52:53] sc1\n\t"
      "global_load_dwordx4 v[136:139], v226, s[52:53] sc1\n\t"
      "global_load_dwordx4 v[140:143], v227, s[52:53] sc1\n\t"
      "global_load_dwordx4 v[144:147], v228, s[52:53] sc1\n\t"
      "global_load_dwordx4 v[148:151], v229, s[52:53] sc1\n\t"
      "global_load_dwordx4 v[152:155], v230, s[52:53] sc1\n\t"
      "global_load_dwordx4 v[128:131], v224, s[54:55] sc1\n\t"
      "s_waitcnt vmcnt(6)\n\t"
      "v_max3_u32 v234, v132, v133, v134\n\t"
      "v_max_u32_e32 v234, v234, v135\n\t"
      "v_cmp_eq_u32_e32 vcc, -1, v234\n\t"
      "s_cbranch_vccnz FP1L_%=\n\t"
      "FP1X_%=:\n\t"
      "s_branch FP1B_%=\n\t"
      "FP2R_%=:\n\t"
      "s_mov_b32 s51, 0\n\t"
      "FP2L_%=:\n\t"
      "s_add_u32 s51, s51, 1\n\t"
      "s_cmp_lt_u32 s51, 262144\n\t"
      "s_cbranch_scc0 FP2X_%=\n\t"
      "s_waitcnt vmcnt(0)\n\t"
      "s_sleep 1\n\t"
      "global_load_dwordx4 v[136:139], v226, s[52:53] sc1\n\t"
      "global_load_dwordx4 v[140:143], v227, s[52:53] sc1\n\t"
      "global_load_dwordx4 v[144:147], v228, s[52:53] sc1\n\t"
      "global_load_dwordx4 v[148:151], v229, s[52:53] sc1\n\t"
      "global_load_dwordx4 v[152:155], v230, s[52:53] sc1\n\t"
      "global_load_dwordx4 v[128:131], v224, s[54:55] sc1\n\t"
      "global_load_dwordx4 v[132:135], v225, s[54:55] sc1\n\t"
      "s_waitcnt vmcnt(6)\n\t"
      "v_max3_u32 v234, v136, v137, v138\n\t"
      "v_max_u32_e32 v234, v234, v139\n\t"
      "v_cmp_eq_u32_e32 vcc, -1, v234\n\t"
      "s_cbranch_vccnz FP2L_%=\n\t"
      "FP2X_%=:\n\t"
      "s_branch FP2B_%=\n\t"
      "FP3R_%=:\n\t"
      "s_mov_b32 s51, 0\n\t"
      "FP3L_%=:\n\t"
      "s_add_u32 s51, s51, 1\n\t"
      "s_cmp_lt_u32 s51, 262144\n\t"
      "s_cbranch_scc0 FP3X_%=\n\t"
      "s_waitcnt vmcnt(0)\n\t"
      "s_sleep 1\n\t"
      "global_load_dwordx4 v[140:143], v227, s[52:53] sc1\n\t"
      "global_load_dwordx4 v[144:147], v228, s[52:53] sc1\n\t"
      "global_load_dwordx4 v[148:151], v229, s[52:53] sc1\n\t"
      "global_load_dwordx4 v[152:155], v230, s[52:53] sc1\n\t"
      "global_load_dwordx4 v[128:131], v224, s[54:55] sc1\n\t"
      "global_load_dwordx4 v[132:135], v225, s[54:55] sc1\n\t"
      "global_load_dwordx4 v[136:139], v226, s[54:55] sc1\n\t"
      "s_waitcnt vmcnt(6)\n\t"
      "v_max3_u32 v234, v140, v141, v142\n\t"
      "v_max_u32_e32 v234, v234, v143\n\t"
      "v_cmp_eq_u32_e32 vcc, -1, v234\n\t"
      "s_cbranch_vccnz FP3L_%=\n\t"
      "FP3X_%=:\n\t"
      "s_branch FP3B_%=\n\t"
      "FP4R_%=:\n\t"
      "s_mov_b32 s51, 0\n\t"
      "FP4L_%=:\n\t"
      "s_add_u32 s51, s51, 1\n\t"
      "s_cmp_lt_u32 s51, 262144\n\t"
      "s_cbranch_scc0 FP4X_%=\n\t"
      "s_waitcnt vmcnt(0)\n\t"
      "s_sleep 1\n\t"
      "global_load_dwordx4 v[144:147], v228, s[52:53] sc1\n\t"
      "global_load_dwordx4 v[148:151], v229, s[52:53] sc1\n\t"
      "global_load_dwordx4 v[152:155], v230, s[52:53] sc1\n\t"
      "global_load_dwordx4 v[128:131], v224, s[54:55] sc1\n\t"
      "global_load_dwordx4 v[132:135], v225, s[54:55] sc1\n\t"
      "global_load_dwordx4 v[136:139], v226, s[54:55] sc1\n\t"
      "global_load_dwordx4 v[140:143], v227, s[54:55] sc1\n\t"
      "s_waitcnt vmcnt(6)\n\t"
      "v_max3_u32 v234, v144, v145, v146\n\t"
      "v_max_u32_e32 v234, v234, v147\n\t"
      "v_cmp_eq_u32_e32 vcc, -1, v234\n\t"
      "s_cbranch_vccnz FP4L_%=\n\t"
      "FP4X_%=:\n\t"
      "s_branch FP4B_%=\n\t"
      "FP5R_%=:\n\t"
      "s_mov_b32 s51, 0\n\t"
      "FP5L_%=:\n\t"
      "s_add_u32 s51, s51, 1\n\t"
      "s_cmp_lt_u32 s51, 262144\n\t"
      "s_cbranch_scc0 FP5X_%=\n\t"
      "s_waitcnt vmcnt(0)\n\t"
      "s_sleep 1\n\t"
      "global_load_dwordx4 v[148:151], v229, s[52:53] sc1\n\t"
      "global_load_dwordx4 v[152:155], v230, s[52:53] sc1\n\t"
      "global_load_dwordx4 v[128:131], v224, s[54:55] sc1\n\t"
      "global_load_dwordx4 v[132:135], v225, s[54:55] sc1\n\t"
      "global_load_dwordx4 v[136:139], v226, s[54:55] sc1\n\t"
      "global_load_dwordx4 v[140:143], v227, s[54:55] sc1\n\t"
      "global_load_dwordx4 v[144:147], v228, s[54:55] sc1\n\t"
      "s_waitcnt vmcnt(6)\n\t"
      "v_max3_u32 v234, v148, v149, v150\n\t"
      "v_max_u32_e32 v234, v234, v151\n\t"
      "v_cmp_eq_u32_e32 vcc, -1, v234\n\t"
      "s_cbranch_vccnz FP5L_%=\n\t"
      "FP5X_%=:\n\t"
      "s_branch FP5B_%=\n\t"
      "FP6R_%=:\n\t"
      "s_mov_b32 s51, 0\n\t"
      "FP6L_%=:\n\t"
      "s_add_u32 s51, s51, 1\n\t"
      "s_cmp_lt_u32 s51, 262144\n\t"
      "s_cbranch_scc0 FP6X_%=\n\t"
      "s_waitcnt vmcnt(0)\n\t"
      "s_sleep 1\n\t"
      "global_load_dwordx4 v[152:155], v230, s[52:53] sc1\n\t"
      "global_load_dwordx4 v[128:131], v224, s[54:55] sc1\n\t"
      "global_load_dwordx4 v[132:135], v225, s[54:55] sc1\n\t"
      "global_load_dwordx4 v[136:139], v226, s[54:55] sc1\n\t"
      "global_load_dwordx4 v[140:143], v227, s[54:55] sc1\n\t"
      "global_load_dwordx4 v[144:147], v228, s[54:55] sc1\n\t"
      "global_load_dwordx4 v[148:151], v229, s[54:55] sc1\n\t"
      "s_waitcnt vmcnt(6)\n\t"
      "v_max3_u32 v234, v152, v153, v154\n\t"
      "v_max_u32_e32 v234, v234, v155\n\t"
      "v_cmp_eq_u32_e32 vcc, -1, v234\n\t"
      "s_cbranch_vccnz FP6L_%=\n\t"
      "FP6X_%=:\n\t"
      "s_branch FP6B_%=\n\t"
      "FP7R_%=:\n\t"
      "s_mov_b32 s51, 0\n\t"
      "FP7L_%=:\n\t"
      "s_add_u32 s51, s51, 1\n\t"
      "s_cmp_lt_u32 s51, 262144\n\t"
      "s_cbranch_scc0 FP7X_%=\n\t"
      "s_waitcnt vmcnt(0)\n\t"
      "s_sleep 4\n\t"
      "global_load_dwordx4 v[128:131], v224, s[54:55] sc1\n\t"
      "global_load_dwordx4 v[132:135], v225, s[54:55] sc1\n\t"
      "global_load_dwordx4 v[136:139], v226, s[54:55] sc1\n\t"
      "global_load_dwordx4 v[140:143], v227, s[54:55] sc1\n\t"
      "global_load_dwordx4 v[144:147], v228, s[54:55] sc1\n\t"
      "global_load_dwordx4 v[148:151], v229, s[54:55] sc1\n\t"
      "s_waitcnt vmcnt(5)\n\t"
      "v_max3_u32 v234, v128, v129, v130\n\t"
      "v_max_u32_e32 v234, v234, v131\n\t"
      "v_cmp_eq_u32_e32 vcc, -1, v234\n\t"
      "s_cbranch_vccnz FP7L_%=\n\t"
      "FP7X_%=:\n\t"
      "s_branch FP7B_%=\n\t"
      "FP8R_%=:\n\t"
      "s_mov_b32 s51, 0\n\t"
      "FP8L_%=:\n\t"
      "s_add_u32 s51, s51, 1\n\t"
      "s_cmp_lt_u32 s51, 262144\n\t"
      "s_cbranch_scc0 FP8X_%=\n\t"
      "s_waitcnt vmcnt(0)\n\t"
      "s_sleep 4\n\t"
      "global_load_dwordx4 v[132:135], v225, s[54:55] sc1\n\t"
      "global_load_dwordx4 v[136:139], v226, s[54:55] sc1\n\t"
      "global_load_dwordx4 v[140:143], v227, s[54:55] sc1\n\t"
      "global_load_dwordx4 v[144:147], v228, s[54:55] sc1\n\t"
      "global_load_dwordx4 v[148:151], v229, s[54:55] sc1\n\t"
      "global_load_dwordx4 v[152:155], v230, s[54:55] sc1\n\t"
      "s_waitcnt vmcnt(5)\n\t"
      "v_max3_u32 v234, v132, v133, v134\n\t"
      "v_max_u32_e32 v234, v234, v135\n\t"
      "v_cmp_eq_u32_e32 vcc, -1, v234\n\t"
      "s_cbranch_vccnz FP8L_%=\n\t"
      "FP8X_%=:\n\t"
      "s_branch FP8B_%=\n\t"
      "FP9R_%=:\n\t"
      "s_mov_b32 s51, 0\n\t"
      "FP9L_%=:\n\t"
      "s_add_u32 s51, s51, 1\n\t"
      "s_cmp_lt_u32 s51, 262144\n\t"
      "s_cbranch_scc0 FP9X_%=\n\t"
      "s_waitcnt vmcnt(0)\n\t"
      "s_sleep 4\n\t"
      "global_load_dwordx4 v[136:139], v226, s[54:55] sc1\n\t"
      "global_load_dwordx4 v[140:143], v227, s[54:55] sc1\n\t"
      "global_load_dwordx4 v[144:147], v228, s[54:55] sc1\n\t"
      "global_load_dwordx4 v[148:151], v229, s[54:55] sc1\n\t"
      "global_load_dwordx4 v[152:155], v230, s[54:55] sc1\n\t"
      "global_load_dwordx4 v[128:131], v224, s[56:57] sc1\n\t"
      "s_waitcnt vmcnt(5)\n\t"
      "v_max3_u32 v234, v136, v137, v138\n\t"
      "v_max_u32_e32 v234, v234, v139\n\t"
      "v_cmp_eq_u32_e32 vcc, -1, v234\n\t"
      "s_cbranch_vccnz FP9L_%=\n\t"
      "FP9X_%=:\n\t"
      "s_branch FP9B_%=\n\t"
      "FP10R_%=:\n\t"
      "s_mov_b32 s51, 0\n\t"
      "FP10L_%=:\n\t"
      "s_add_u32 s51, s51, 1\n\t"
      "s_cmp_lt_u32 s51, 262144\n\t"
      "s_cbranch_scc0 FP10X_%=\n\t"
      "s_waitcnt vmcnt(0)\n\t"
      "s_sleep 4\n\t"
      "global_load_dwordx4 v[140:143], v227, s[54:55] sc1\n\t"
      "global_load_dwordx4 v[144:147], v228, s[54:55] sc1\n\t"
      "global_load_dwordx4 v[148:151], v229, s[54:55] sc1\n\t"
      "global_load_dwordx4 v[152:155], v230, s[54:55] sc1\n\t"
      "global_load_dwordx4 v[128:131], v224, s[56:57] sc1\n\t"
      "global_load_dwordx4 v[132:135], v225, s[56:57] sc1\n\t"
      "s_waitcnt vmcnt(5)\n\t"
      "v_max3_u32 v234, v140, v141, v142\n\t"
      "v_max_u32_e32 v234, v234, v143\n\t"
      "v_cmp_eq_u32_e32 vcc, -1, v234\n\t"
      "s_cbranch_vccnz FP10L_%=\n\t"
      "FP10X_%=:\n\t"
      "s_branch FP10B_%=\n\t"
      "FP11R_%=:\n\t"
      "s_mov_b32 s51, 0\n\t"
      "FP11L_%=:\n\t"
      "s_add_u32 s51, s51, 1\n\t"
      "s_cmp_lt_u32 s51, 262144\n\t"
      "s_cbranch_scc0 FP11X_%=\n\t"
      "s_waitcnt vmcnt(0)\n\t"
      "s_sleep 4\n\t"
      "global_load_dwordx4 v[144:147], v228, s[54:55] sc1\n\t"
      "global_load_dwordx4 v[148:151], v229, s[54:55] sc1\n\t"
      "global_load_dwordx4 v[152:155], v230, s[54:55] sc1\n\t"
      "global_load_dwordx4 v[128:131], v224, s[56:57] sc1\n\t"
      "global_load_dwordx4 v[132:135], v225, s[56:57] sc1\n\t"
      "global_load_dwordx4 v[136:139], v226, s[56:57] sc1\n\t"
      "s_waitcnt vmcnt(5)\n\t"
      "v_max3_u32 v234, v144, v145, v146\n\t"
      "v_max_u32_e32 v234, v234, v147\n\t"
      "v_cmp_eq_u32_e32 vcc, -1, v234\n\t"
      "s_cbranch_vccnz FP11L_%=\n\t"
      "FP11X_%=:\n\t"
      "s_branch FP11B_%=\n\t"
      "FP12R_%=:\n\t"
      "s_mov_b32 s51, 0\n\t"
      "FP12L_%=:\n\t"
      "s_add_u32 s51, s51, 1\n\t"
      "s_cmp_lt_u32 s51, 262144\n\t"
      "s_cbranch_scc0 FP12X_%=\n\t"
      "s_waitcnt vmcnt(0)\n\t"
      "s_sleep 4\n\t"
      "global_load_dwordx4 v[148:151], v229, s[54:55] sc1\n\t"
      "global_load_dwordx4 v[152:155], v230, s[54:55] sc1\n\t"
      "global_load_dwordx4 v[128:131], v224, s[56:57] sc1\n\t"
      "global_load_dwordx4 v[132:135], v225, s[56:57] sc1\n\t"
      "global_load_dwordx4 v[136:139], v226, s[56:57] sc1\n\t"
      "global_load_dwordx4 v[140:143], v227, s[56:57] sc1\n\t"
      "s_waitcnt vmcnt(5)\n\t"
      "v_max3_u32 v234, v148, v149, v150\n\t"
      "v_max_u32_e32 v234, v234, v151\n\t"
      "v_cmp_eq_u32_e32 vcc, -1, v234\n\t"
      "s_cbranch_vccnz FP12L_%=\n\t"
      "FP12X_%=:\n\t"
      "s_branch FP12B_%=\n\t"
      "FP13R_%=:\n\t"
      "s_mov_b32 s51, 0\n\t"
      "FP13L_%=:\n\t"
      "s_add_u32 s51, s51, 1\n\t"
      "s_cmp_lt_u32 s51, 262144\n\t"
      "s_cbranch_scc0 FP13X_%=\n\t"
      "s_waitcnt vmcnt(0)\n\t"
      "s_sleep 4\n\t"
      "global_load_dwordx4 v[152:155], v230, s[54:55] sc1\n\t"
      "global_load_dwordx4 v[128:131], v224, s[56:57] sc1\n\t"
      "global_load_dwordx4 v[132:135], v225, s[56:57] sc1\n\t"
      "global_load_dwordx4 v[136:139], v226, s[56:57] sc1\n\t"
      "global_load_dwordx4 v[140:143], v227, s[56:57] sc1\n\t"
      "global_load_dwordx4 v[144:147], v228, s[56:57] sc1\n\t"
      "s_waitcnt vmcnt(5)\n\t"
      "v_max3_u32 v234, v152, v153, v154\n\t"
      "v_max_u32_e32 v234, v234, v155\n\t"
      "v_cmp_eq_u32_e32 vcc, -1, v234\n\t"
      "s_cbranch_vccnz FP13L_%=\n\t"
      "FP13X_%=:\n\t"
      "s_waitcnt vmcnt(0)\n\t"
      "s_branch FP13B_%=\n\t"
      "FP14R_%=:\n\t"
      "s_mov_b32 s51, 0\n\t"
      "FP14L_%=:\n\t"
      "s_add_u32 s51, s51, 1\n\t"
      "s_cmp_lt_u32 s51, 262144\n\t"
      "s_cbranch_scc0 FP14X_%=\n\t"
      "s_waitcnt vmcnt(0)\n\t"
      "s_sleep 4\n\t"
      "global_load_dwordx4 v[128:131], v224, s[56:57] sc1\n\t"
      "global_load_dwordx4 v[132:135], v225, s[56:57] sc1\n\t"
      "global_load_dwordx4 v[136:139], v226, s[56:57] sc1\n\t"
      "global_load_dwordx4 v[140:143], v227, s[56:57] sc1\n\t"
      "global_load_dwordx4 v[144:147], v228, s[56:57] sc1\n\t"
      "global_load_dwordx4 v[148:151], v229, s[56:57] sc1\n\t"
      "s_waitcnt vmcnt(5)\n\t"
      "v_max3_u32 v234, v128, v129, v130\n\t"
      "v_max_u32_e32 v234, v234, v131\n\t"
      "v_cmp_eq_u32_e32 vcc, -1, v234\n\t"
      "s_cbranch_vccnz FP14L_%=\n\t"
      "FP14X_%=:\n\t"
      "s_waitcnt vmcnt(0)\n\t"
      "s_branch FP14B_%=\n\t"
      "NP1R_%=:\n\t"
      "s_mov_b32 s51, 0\n\t"
      "NP1L_%=:\n\t"
      "s_add_u32 s51, s51, 1\n\t"
      "s_cmp_lt_u32 s51, 262144\n\t"
      "s_cbranch_scc0 NP1X_%=\n\t"
      "s_waitcnt vmcnt(0)\n\t"
      "s_sleep 1\n\t"
      "global_load_dwordx4 v[128:131], v224, s[54:55] sc1\n\t"
      "global_load_dwordx4 v[132:135], v225, s[54:55] sc1\n\t"
      "global_load_dwordx4 v[136:139], v226, s[54:55] sc1\n\t"
      "global_load_dwordx4 v[140:143], v227, s[54:55] sc1\n\t"
      "global_load_dwordx4 v[144:147], v228, s[54:55] sc1\n\t"
      "global_load_dwordx4 v[148:151], v229, s[54:55] sc1\n\t"
      "global_load_dwordx4 v[152:155], v230, s[54:55] sc1\n\t"
      "s_waitcnt vmcnt(0)\n\t"
      "v_max3_u32 v234, v128, v129, v130\n\t"
      "v_max3_u32 v234, v234, v131, v132\n\t"
      "v_max3_u32 v234, v234, v133, v134\n\t"
      "v_max3_u32 v234, v234, v135, v136\n\t"
      "v_max3_u32 v234, v234, v137, v138\n\t"
      "v_max3_u32 v234, v234, v139, v140\n\t"
      "v_max3_u32 v234, v234, v141, v142\n\t"
      "v_max3_u32 v234, v234, v143, v144\n\t"
      "v_max3_u32 v234, v234, v145, v146\n\t"
      "v_max3_u32 v234, v234, v147, v148\n\t"
      "v_max3_u32 v234, v234, v149, v150\n\t"
      "v_max3_u32 v234, v234, v151, v152\n\t"
      "v_max3_u32 v234, v234, v153, v154\n\t"
      "v_max_u32_e32 v234, v234, v155\n\t"
      "v_cmp_eq_u32_e32 vcc, -1, v234\n\t"
      "s_cbranch_vccnz NP1L_%=\n\t"
      "NP1X_%=:\n\t"
      "v_mfma_f32_16x16x32_bf16 v[200:203], v[128:131], v[0:3], v[160:163]\n\t"
      "v_mfma_f32_16x16x32_bf16 v[204:207], v[128:131], v[4:7], 0\n\t"
      "v_mfma_f32_16x16x32_bf16 v[208:211], v[128:131], v[8:11], 0\n\t"
      "v_mfma_f32_16x16x32_bf16 v[212:215], v[128:131], v[12:15], 0\n\t"
      "v_mfma_f32_16x16x32_bf16 v[200:203], v[132:135], v[16:19], v[200:203]\n\t"
      "v_mfma_f32_16x16x32_bf16 v[204:207], v[132:135], v[20:23], v[204:207]\n\t"
      "v_mfma_f32_16x16x32_bf16 v[208:211], v[132:135], v[24:27], v[208:211]\n\t"
      "v_mfma_f32_16x16x32_bf16 v[212:215], v[132:135], v[28:31], v[212:215]\n\t"
      "v_mfma_f32_16x16x32_bf16 v[200:203], v[136:139], v[32:35], v[200:203]\n\t"
      "v_mfma_f32_16x16x32_bf16 v[204:207], v[136:139], v[36:39], v[204:207]\n\t"
      "v_mfma_f32_16x16x32_bf16 v[208:211], v[136:139], v[40:43], v[208:211]\n\t"
      "v_mfma_f32_16x16x32_bf16 v[212:215], v[136:139], v[44:47], v[212:215]\n\t"
      "v_mfma_f32_16x16x32_bf16 v[200:203], v[140:143], v[48:51], v[200:203]\n\t"
      "v_mfma_f32_16x16x32_bf16 v[204:207], v[140:143], v[52:55], v[204:207]\n\t"
      "v_mfma_f32_16x16x32_bf16 v[208:211], v[140:143], v[56:59], v[208:211]\n\t"
      "v_mfma_f32_16x16x32_bf16 v[212:215], v[140:143], v[60:63], v[212:215]\n\t"
      "v_mfma_f32_16x16x32_bf16 v[200:203], v[144:147], v[64:67], v[200:203]\n\t"
      "v_mfma_f32_16x16x32_bf16 v[204:207], v[144:147], v[68:71], v[204:207]\n\t"
      "v_mfma_f32_16x16x32_bf16 v[208:211], v[144:147], v[72:75], v[208:211]\n\t"
      "v_mfma_f32_16x16x32_bf16 v[212:215], v[144:147], v[76:79], v[212:215]\n\t"
      "v_mfma_f32_16x16x32_bf16 v[200:203], v[148:151], v[80:83], v[200:203]\n\t"
      "v_mfma_f32_16x16x32_bf16 v[204:207], v[148:151], v[84:87], v[204:207]\n\t"
      "v_mfma_f32_16x16x32_bf16 v[208:211], v[148:151], v[88:91], v[208:211]\n\t"
      "v_mfma_f32_16x16x32_bf16 v[212:215], v[148:151], v[92:95], v[212:215]\n\t"
      "v_mfma_f32_16x16x32_bf16 v[200:203], v[152:155], v[96:99], v[200:203]\n\t"
      "v_mfma_f32_16x16x32_bf16 v[204:207], v[152:155], v[100:103], v[204:207]\n\t"
      "v_mfma_f32_16x16x32_bf16 v[208:211], v[152:155], v[104:107], v[208:211]\n\t"
      "v_mfma_f32_16x16x32_bf16 v[212:215], v[152:155], v[108:111], v[212:215]\n\t"
      "global_load_dwordx4 v[128:131], v224, s[56:57] sc1\n\t"
      "global_load_dwordx4 v[132:135], v225, s[56:57] sc1\n\t"
      "global_load_dwordx4 v[136:139], v226, s[56:57] sc1\n\t"
      "global_load_dwordx4 v[140:143], v227, s[56:57] sc1\n\t"
      "global_load_dwordx4 v[144:147], v228, s[56:57] sc1\n\t"
      "global_load_dwordx4 v[148:151], v229, s[56:57] sc1\n\t"
      "global_load_dwordx4 v[152:155], v230, s[56:57] sc1\n\t"
      "s_waitcnt vmcnt(0)\n\t"
      "s_nop 7\n\t"
      "s_branch NP1B_%=\n\t"
      "FP15R_%=:\n\t"
      "s_mov_b32 s51, 0\n\t"
      "FP15L_%=:\n\t"
      "s_add_u32 s51, s51, 1\n\t"
      "s_cmp_lt_u32 s51, 262144\n\t"
      "s_cbranch_scc0 FP15X_%=\n\t"
      "s_waitcnt vmcnt(0)\n\t"
      "s_sleep 4\n\t"
      "global_load_dwordx4 v[132:135], v225, s[56:57] sc1\n\t"
      "global_load_dwordx4 v[136:139], v226, s[56:57] sc1\n\t"
      "global_load_dwordx4 v[140:143], v227, s[56:57] sc1\n\t"
      "global_load_dwordx4 v[144:147], v228, s[56:57] sc1\n\t"
      "global_load_dwordx4 v[148:151], v229, s[56:57] sc1\n\t"
      "global_load_dwordx4 v[152:155], v230, s[56:57] sc1\n\t"
      "s_waitcnt vmcnt(5)\n\t"
      "v_max3_u32 v234, v132, v133, v134\n\t"
      "v_max_u32_e32 v234, v234, v135\n\t"
      "v_cmp_eq_u32_e32 vcc, -1, v234\n\t"
      "s_cbranch_vccnz FP15L_%=\n\t"
      "FP15X_%=:\n\t"
      "s_waitcnt vmcnt(0)\n\t"
      "s_branch FP15B_%=\n\t"
      "FP16R_%=:\n\t"
      "s_mov_b32 s51, 0\n\t"
      "FP16L_%=:\n\t"
      "s_add_u32 s51, s51, 1\n\t"
      "s_cmp_lt_u32 s51, 262144\n\t"
      "s_cbranch_scc0 FP16X_%=\n\t"
      "s_waitcnt vmcnt(0)\n\t"
      "s_sleep 4\n\t"
      "global_load_dwordx4 v[136:139], v226, s[56:57] sc1\n\t"
      "global_load_dwordx4 v[140:143], v227, s[56:57] sc1\n\t"
      "global_load_dwordx4 v[144:147], v228, s[56:57] sc1\n\t"
      "global_load_dwordx4 v[148:151], v229, s[56:57] sc1\n\t"
      "global_load_dwordx4 v[152:155], v230, s[56:57] sc1\n\t"
      "global_load_dwordx4 v[128:131], v224, s[58:59] sc1\n\t"
      "s_waitcnt vmcnt(5)\n\t"
      "v_max3_u32 v234, v136, v137, v138\n\t"
      "v_max_u32_e32 v234, v234, v139\n\t"
      "v_cmp_eq_u32_e32 vcc, -1, v234\n\t"
      "s_cbranch_vccnz FP16L_%=\n\t"
      "FP16X_%=:\n\t"
      "s_waitcnt vmcnt(0)\n\t"
      "s_branch FP16B_%=\n\t"
      "FP17R_%=:\n\t"
      "s_mov_b32 s51, 0\n\t"
      "FP17L_%=:\n\t"
      "s_add_u32 s51, s51, 1\n\t"
      "s_cmp_lt_u32 s51, 262144\n\t"
      "s_cbranch_scc0 FP17X_%=\n\t"
      "s_waitcnt vmcnt(0)\n\t"
      "s_sleep 4\n\t"
      "global_load_dwordx4 v[140:143], v227, s[56:57] sc1\n\t"
      "global_load_dwordx4 v[144:147], v228, s[56:57] sc1\n\t"
      "global_load_dwordx4 v[148:151], v229, s[56:57] sc1\n\t"
      "global_load_dwordx4 v[152:155], v230, s[56:57] sc1\n\t"
      "global_load_dwordx4 v[128:131], v224, s[58:59] sc1\n\t"
      "global_load_dwordx4 v[132:135], v225, s[58:59] sc1\n\t"
      "s_waitcnt vmcnt(5)\n\t"
      "v_max3_u32 v234, v140, v141, v142\n\t"
      "v_max_u32_e32 v234, v234, v143\n\t"
      "v_cmp_eq_u32_e32 vcc, -1, v234\n\t"
      "s_cbranch_vccnz FP17L_%=\n\t"
      "FP17X_%=:\n\t"
      "s_waitcnt vmcnt(0)\n\t"
      "s_branch FP17B_%=\n\t"
      "FP18R_%=:\n\t"
      "s_mov_b32 s51, 0\n\t"
      "FP18L_%=:\n\t"
      "s_add_u32 s51, s51, 1\n\t"
      "s_cmp_lt_u32 s51, 262144\n\t"
      "s_cbranch_scc0 FP18X_%=\n\t"
      "s_waitcnt vmcnt(0)\n\t"
      "s_sleep 4\n\t"
      "global_load_dwordx4 v[144:147], v228, s[56:57] sc1\n\t"
      "global_load_dwordx4 v[148:151], v229, s[56:57] sc1\n\t"
      "global_load_dwordx4 v[152:155], v230, s[56:57] sc1\n\t"
      "global_load_dwordx4 v[128:131], v224, s[58:59] sc1\n\t"
      "global_load_dwordx4 v[132:135], v225, s[58:59] sc1\n\t"
      "global_load_dwordx4 v[136:139], v226, s[58:59] sc1\n\t"
      "s_waitcnt vmcnt(5)\n\t"
      "v_max3_u32 v234, v144, v145, v146\n\t"
      "v_max_u32_e32 v234, v234, v147\n\t"
      "v_cmp_eq_u32_e32 vcc, -1, v234\n\t"
      "s_cbranch_vccnz FP18L_%=\n\t"
      "FP18X_%=:\n\t"
      "s_waitcnt vmcnt(0)\n\t"
      "s_branch FP18B_%=\n\t"
      "FP19R_%=:\n\t"
      "s_mov_b32 s51, 0\n\t"
      "FP19L_%=:\n\t"
      "s_add_u32 s51, s51, 1\n\t"
      "s_cmp_lt_u32 s51, 262144\n\t"
      "s_cbranch_scc0 FP19X_%=\n\t"
      "s_waitcnt vmcnt(0)\n\t"
      "s_sleep 4\n\t"
      "global_load_dwordx4 v[148:151], v229, s[56:57] sc1\n\t"
      "global_load_dwordx4 v[152:155], v230, s[56:57] sc1\n\t"
      "global_load_dwordx4 v[128:131], v224, s[58:59] sc1\n\t"
      "global_load_dwordx4 v[132:135], v225, s[58:59] sc1\n\t"
      "global_load_dwordx4 v[136:139], v226, s[58:59] sc1\n\t"
      "global_load_dwordx4 v[140:143], v227, s[58:59] sc1\n\t"
      "s_waitcnt vmcnt(5)\n\t"
      "v_max3_u32 v234, v148, v149, v150\n\t"
      "v_max_u32_e32 v234, v234, v151\n\t"
      "v_cmp_eq_u32_e32 vcc, -1, v234\n\t"
      "s_cbranch_vccnz FP19L_%=\n\t"
      "FP19X_%=:\n\t"
      "s_branch FP19B_%=\n\t"
      "FP20R_%=:\n\t"
      "s_mov_b32 s51, 0\n\t"
      "FP20L_%=:\n\t"
      "s_add_u32 s51, s51, 1\n\t"
      "s_cmp_lt_u32 s51, 262144\n\t"
      "s_cbranch_scc0 FP20X_%=\n\t"
      "s_waitcnt vmcnt(0)\n\t"
      "s_sleep 4\n\t"
      "global_load_dwordx4 v[152:155], v230, s[56:57] sc1\n\t"
      "global_load_dwordx4 v[128:131], v224, s[58:59] sc1\n\t"
      "global_load_dwordx4 v[132:135], v225, s[58:59] sc1\n\t"
      "global_load_dwordx4 v[136:139], v226, s[58:59] sc1\n\t"
      "global_load_dwordx4 v[140:143], v227, s[58:59] sc1\n\t"
      "global_load_dwordx4 v[144:147], v228, s[58:59] sc1\n\t"
      "s_waitcnt vmcnt(5)\n\t"
      "v_max3_u32 v234, v152, v153, v154\n\t"
      "v_max_u32_e32 v234, v234, v155\n\t"
      "v_cmp_eq_u32_e32 vcc, -1, v234\n\t"
      "s_cbranch_vccnz FP20L_%=\n\t"
      "FP20X_%=:\n\t"
      "s_waitcnt vmcnt(0)\n\t"
      "s_branch FP20B_%=\n\t"
      "FP21R_%=:\n\t"
      "s_mov_b32 s51, 0\n\t"
      "FP21L_%=:\n\t"
      "s_add_u32 s51, s51, 1\n\t"
      "s_cmp_lt_u32 s51, 262144\n\t"
      "s_cbranch_scc0 FP21X_%=\n\t"
      "s_waitcnt vmcnt(0)\n\t"
      "s_sleep 4\n\t"
      "global_load_dwordx4 v[128:131], v224, s[58:59] sc1\n\t"
      "global_load_dwordx4 v[132:135], v225, s[58:59] sc1\n\t"
      "global_load_dwordx4 v[136:139], v226, s[58:59] sc1\n\t"
      "global_load_dwordx4 v[140:143], v227, s[58:59] sc1\n\t"
      "global_load_dwordx4 v[144:147], v228, s[58:59] sc1\n\t"
      "global_load_dwordx4 v[148:151], v229, s[58:59] sc1\n\t"
      "s_waitcnt vmcnt(5)\n\t"
      "v_max3_u32 v234, v128, v129, v130\n\t"
      "v_max_u32_e32 v234, v234, v131\n\t"
      "v_cmp_eq_u32_e32 vcc, -1, v234\n\t"
      "s_cbranch_vccnz FP21L_%=\n\t"
      "FP21X_%=:\n\t"
      "s_waitcnt vmcnt(0)\n\t"
      "s_branch FP21B_%=\n\t"
      "NP2R_%=:\n\t"
      "s_mov_b32 s51, 0\n\t"
      "NP2L_%=:\n\t"
      "s_add_u32 s51, s51, 1\n\t"
      "s_cmp_lt_u32 s51, 262144\n\t"
      "s_cbranch_scc0 NP2X_%=\n\t"
      "s_waitcnt vmcnt(0)\n\t"
      "s_sleep 1\n\t"
      "global_load_dwordx4 v[128:131], v224, s[56:57] sc1\n\t"
      "global_load_dwordx4 v[132:135], v225, s[56:57] sc1\n\t"
      "global_load_dwordx4 v[136:139], v226, s[56:57] sc1\n\t"
      "global_load_dwordx4 v[140:143], v227, s[56:57] sc1\n\t"
      "global_load_dwordx4 v[144:147], v228, s[56:57] sc1\n\t"
      "global_load_dwordx4 v[148:151], v229, s[56:57] sc1\n\t"
      "global_load_dwordx4 v[152:155], v230, s[56:57] sc1\n\t"
      "s_waitcnt vmcnt(0)\n\t"
      "v_max3_u32 v234, v128, v129, v130\n\t"
      "v_max3_u32 v234, v234, v131, v132\n\t"
      "v_max3_u32 v234, v234, v133, v134\n\t"
      "v_max3_u32 v234, v234, v135, v136\n\t"
      "v_max3_u32 v234, v234, v137, v138\n\t"
      "v_max3_u32 v234, v234, v139, v140\n\t"
      "v_max3_u32 v234, v234, v141, v142\n\t"
      "v_max3_u32 v234, v234, v143, v144\n\t"
      "v_max3_u32 v234, v234, v145, v146\n\t"
      "v_max3_u32 v234, v234, v147, v148\n\t"
      "v_max3_u32 v234, v234, v149, v150\n\t"
      "v_max3_u32 v234, v234, v151, v152\n\t"
      "v_max3_u32 v234, v234, v153, v154\n\t"
      "v_max_u32_e32 v234, v234, v155\n\t"
      "v_cmp_eq_u32_e32 vcc, -1, v234\n\t"
      "s_cbranch_vccnz NP2L_%=\n\t"
      "NP2X_%=:\n\t"
      "v_mfma_f32_16x16x32_bf16 v[184:187], v[128:131], v[0:3], v[164:167]\n\t"
      "v_mfma_f32_16x16x32_bf16 v[188:191], v[128:131], v[4:7], 0\n\t"
      "v_mfma_f32_16x16x32_bf16 v[192:195], v[128:131], v[8:11], 0\n\t"
      "v_mfma_f32_16x16x32_bf16 v[196:199], v[128:131], v[12:15], 0\n\t"
      "v_mfma_f32_16x16x32_bf16 v[184:187], v[132:135], v[16:19], v[184:187]\n\t"
      "v_mfma_f32_16x16x32_bf16 v[188:191], v[132:135], v[20:23], v[188:191]\n\t"
      "v_mfma_f32_16x16x32_bf16 v[192:195], v[132:135], v[24:27], v[192:195]\n\t"
      "v_mfma_f32_16x16x32_bf16 v[196:199], v[132:135], v[28:31], v[196:199]\n\t"
      "v_mfma_f32_16x16x32_bf16 v[184:187], v[136:139], v[32:35], v[184:187]\n\t"
      "v_mfma_f32_16x16x32_bf16 v[188:191], v[136:139], v[36:39], v[188:191]\n\t"
      "v_mfma_f32_16x16x32_bf16 v[192:195], v[136:139], v[40:43], v[192:195]\n\t"
      "v_mfma_f32_16x16x32_bf16 v[196:199], v[136:139], v[44:47], v[196:199]\n\t"
      "v_mfma_f32_16x16x32_bf16 v[184:187], v[140:143], v[48:51], v[184:187]\n\t"
      "v_mfma_f32_16x16x32_bf16 v[188:191], v[140:143], v[52:55], v[188:191]\n\t"
      "v_mfma_f32_16x16x32_bf16 v[192:195], v[140:143], v[56:59], v[192:195]\n\t"
      "v_mfma_f32_16x16x32_bf16 v[196:199], v[140:143], v[60:63], v[196:199]\n\t"
      "v_mfma_f32_16x16x32_bf16 v[184:187], v[144:147], v[64:67], v[184:187]\n\t"
      "v_mfma_f32_16x16x32_bf16 v[188:191], v[144:147], v[68:71], v[188:191]\n\t"
      "v_mfma_f32_16x16x32_bf16 v[192:195], v[144:147], v[72:75], v[192:195]\n\t"
      "v_mfma_f32_16x16x32_bf16 v[196:199], v[144:147], v[76:79], v[196:199]\n\t"
      "v_mfma_f32_16x16x32_bf16 v[184:187], v[148:151], v[80:83], v[184:187]\n\t"
      "v_mfma_f32_16x16x32_bf16 v[188:191], v[148:151], v[84:87], v[188:191]\n\t"
      "v_mfma_f32_16x16x32_bf16 v[192:195], v[148:151], v[88:91], v[192:195]\n\t"
      "v_mfma_f32_16x16x32_bf16 v[196:199], v[148:151], v[92:95], v[196:199]\n\t"
      "v_mfma_f32_16x16x32_bf16 v[184:187], v[152:155], v[96:99], v[184:187]\n\t"
      "v_mfma_f32_16x16x32_bf16 v[188:191], v[152:155], v[100:103], v[188:191]\n\t"
      "v_mfma_f32_16x16x32_bf16 v[192:195], v[152:155], v[104:107], v[192:195]\n\t"
      "v_mfma_f32_16x16x32_bf16 v[196:199], v[152:155], v[108:111], v[196:199]\n\t"
      "global_load_dwordx4 v[128:131], v224, s[58:59] sc1\n\t"
      "global_load_dwordx4 v[132:135], v225, s[58:59] sc1\n\t"
      "global_load_dwordx4 v[136:139], v226, s[58:59] sc1\n\t"
      "global_load_dwordx4 v[140:143], v227, s[58:59] sc1\n\t"
      "global_load_dwordx4 v[144:147], v228, s[58:59] sc1\n\t"
      "global_load_dwordx4 v[148:151], v229, s[58:59] sc1\n\t"
      "global_load_dwordx4 v[152:155], v230, s[58:59] sc1\n\t"
      "s_waitcnt vmcnt(0)\n\t"
      "s_nop 7\n\t"
      "s_branch NP2B_%=\n\t"
      "FP22R_%=:\n\t"
      "s_mov_b32 s51, 0\n\t"
      "FP22L_%=:\n\t"
      "s_add_u32 s51, s51, 1\n\t"
      "s_cmp_lt_u32 s51, 262144\n\t"
      "s_cbranch_scc0 FP22X_%=\n\t"
      "s_waitcnt vmcnt(0)\n\t"
      "s_sleep 4\n\t"
      "global_load_dwordx4 v[132:135], v225, s[58:59] sc1\n\t"
      "global_load_dwordx4 v[136:139], v226, s[58:59] sc1\n\t"
      "global_load_dwordx4 v[140:143], v227, s[58:59] sc1\n\t"
      "global_load_dwordx4 v[144:147], v228, s[58:59] sc1\n\t"
      "global_load_dwordx4 v[148:151], v229, s[58:59] sc1\n\t"
      "global_load_dwordx4 v[152:155], v230, s[58:59] sc1\n\t"
      "s_waitcnt vmcnt(5)\n\t"
      "v_max3_u32 v234, v132, v133, v134\n\t"
      "v_max_u32_e32 v234, v234, v135\n\t"
      "v_cmp_eq_u32_e32 vcc, -1, v234\n\t"
      "s_cbranch_vccnz FP22L_%=\n\t"
      "FP22X_%=:\n\t"
      "s_waitcnt vmcnt(0)\n\t"
      "s_branch FP22B_%=\n\t"
      "FP23R_%=:\n\t"
      "s_mov_b32 s51, 0\n\t"
      "FP23L_%=:\n\t"
      "s_add_u32 s51, s51, 1\n\t"
      "s_cmp_lt_u32 s51, 262144\n\t"
      "s_cbranch_scc0 FP23X_%=\n\t"
      "s_waitcnt vmcnt(0)\n\t"
      "s_sleep 4\n\t"
      "global_load_dwordx4 v[136:139], v226, s[58:59] sc1\n\t"
      "global_load_dwordx4 v[140:143], v227, s[58:59] sc1\n\t"
      "global_load_dwordx4 v[144:147], v228, s[58:59] sc1\n\t"
      "global_load_dwordx4 v[148:151], v229, s[58:59] sc1\n\t"
      "global_load_dwordx4 v[152:155], v230, s[58:59] sc1\n\t"
      "global_load_dwordx4 v[128:131], v224, s[60:61] sc1\n\t"
      "s_waitcnt vmcnt(5)\n\t"
      "v_max3_u32 v234, v136, v137, v138\n\t"
      "v_max_u32_e32 v234, v234, v139\n\t"
      "v_cmp_eq_u32_e32 vcc, -1, v234\n\t"
      "s_cbranch_vccnz FP23L_%=\n\t"
      "FP23X_%=:\n\t"
      "s_waitcnt vmcnt(0)\n\t"
      "s_branch FP23B_%=\n\t"
      "FP24R_%=:\n\t"
      "s_mov_b32 s51, 0\n\t"
      "FP24L_%=:\n\t"
      "s_add_u32 s51, s51, 1\n\t"
      "s_cmp_lt_u32 s51, 262144\n\t"
      "s_cbranch_scc0 FP24X_%=\n\t"
      "s_waitcnt vmcnt(0)\n\t"
      "s_sleep 4\n\t"
      "global_load_dwordx4 v[140:143], v227, s[58:59] sc1\n\t"
      "global_load_dwordx4 v[144:147], v228, s[58:59] sc1\n\t"
      "global_load_dwordx4 v[148:151], v229, s[58:59] sc1\n\t"
      "global_load_dwordx4 v[152:155], v230, s[58:59] sc1\n\t"
      "global_load_dwordx4 v[128:131], v224, s[60:61] sc1\n\t"
      "global_load_dwordx4 v[132:135], v225, s[60:61] sc1\n\t"
      "s_waitcnt vmcnt(5)\n\t"
      "v_max3_u32 v234, v140, v141, v142\n\t"
      "v_max_u32_e32 v234, v234, v143\n\t"
      "v_cmp_eq_u32_e32 vcc, -1, v234\n\t"
      "s_cbranch_vccnz FP24L_%=\n\t"
      "FP24X_%=:\n\t"
      "s_waitcnt vmcnt(0)\n\t"
      "s_branch FP24B_%=\n\t"
      "FP25R_%=:\n\t"
      "s_mov_b32 s51, 0\n\t"
      "FP25L_%=:\n\t"
      "s_add_u32 s51, s51, 1\n\t"
      "s_cmp_lt_u32 s51, 262144\n\t"
      "s_cbranch_scc0 FP25X_%=\n\t"
      "s_waitcnt vmcnt(0)\n\t"
      "s_sleep 4\n\t"
      "global_load_dwordx4 v[144:147], v228, s[58:59] sc1\n\t"
      "global_load_dwordx4 v[148:151], v229, s[58:59] sc1\n\t"
      "global_load_dwordx4 v[152:155], v230, s[58:59] sc1\n\t"
      "global_load_dwordx4 v[128:131], v224, s[60:61] sc1\n\t"
      "global_load_dwordx4 v[132:135], v225, s[60:61] sc1\n\t"
      "global_load_dwordx4 v[136:139], v226, s[60:61] sc1\n\t"
      "s_waitcnt vmcnt(5)\n\t"
      "v_max3_u32 v234, v144, v145, v146\n\t"
      "v_max_u32_e32 v234, v234, v147\n\t"
      "v_cmp_eq_u32_e32 vcc, -1, v234\n\t"
      "s_cbranch_vccnz FP25L_%=\n\t"
      "FP25X_%=:\n\t"
      "s_waitcnt vmcnt(0)\n\t"
      "s_branch FP25B_%=\n\t"
      "FP26R_%=:\n\t"
      "s_mov_b32 s51, 0\n\t"
      "FP26L_%=:\n\t"
      "s_add_u32 s51, s51, 1\n\t"
      "s_cmp_lt_u32 s51, 262144\n\t"
      "s_cbranch_scc0 FP26X_%=\n\t"
      "s_waitcnt vmcnt(0)\n\t"
      "s_sleep 4\n\t"
      "global_load_dwordx4 v[148:151], v229, s[58:59] sc1\n\t"
      "global_load_dwordx4 v[152:155], v230, s[58:59] sc1\n\t"
      "global_load_dwordx4 v[128:131], v224, s[60:61] sc1\n\t"
      "global_load_dwordx4 v[132:135], v225, s[60:61] sc1\n\t"
      "global_load_dwordx4 v[136:139], v226, s[60:61] sc1\n\t"
      "global_load_dwordx4 v[140:143], v227, s[60:61] sc1\n\t"
      "s_waitcnt vmcnt(5)\n\t"
      "v_max3_u32 v234, v148, v149, v150\n\t"
      "v_max_u32_e32 v234, v234, v151\n\t"
      "v_cmp_eq_u32_e32 vcc, -1, v234\n\t"
      "s_cbranch_vccnz FP26L_%=\n\t"
      "FP26X_%=:\n\t"
      "s_branch FP26B_%=\n\t"
      "FP27R_%=:\n\t"
      "s_mov_b32 s51, 0\n\t"
      "FP27L_%=:\n\t"
      "s_add_u32 s51, s51, 1\n\t"
      "s_cmp_lt_u32 s51, 262144\n\t"
      "s_cbranch_scc0 FP27X_%=\n\t"
      "s_waitcnt vmcnt(0)\n\t"
      "s_sleep 4\n\t"
      "global_load_dwordx4 v[152:155], v230, s[58:59] sc1\n\t"
      "global_load_dwordx4 v[128:131], v224, s[60:61] sc1\n\t"
      "global_load_dwordx4 v[132:135], v225, s[60:61] sc1\n\t"
      "global_load_dwordx4 v[136:139], v226, s[60:61] sc1\n\t"
      "global_load_dwordx4 v[140:143], v227, s[60:61] sc1\n\t"
      "global_load_dwordx4 v[144:147], v228, s[60:61] sc1\n\t"
      "s_waitcnt vmcnt(5)\n\t"
      "v_max3_u32 v234, v152, v153, v154\n\t"
      "v_max_u32_e32 v234, v234, v155\n\t"
      "v_cmp_eq_u32_e32 vcc, -1, v234\n\t"
      "s_cbranch_vccnz FP27L_%=\n\t"
      "FP27X_%=:\n\t"
      "s_waitcnt vmcnt(0)\n\t"
      "s_branch FP27B_%=\n\t"
      "FP28R_%=:\n\t"
      "s_mov_b32 s51, 0\n\t"
      "FP28L_%=:\n\t"
      "s_add_u32 s51, s51, 1\n\t"
      "s_cmp_lt_u32 s51, 262144\n\t"
      "s_cbranch_scc0 FP28X_%=\n\t"
      "s_waitcnt vmcnt(0)\n\t"
      "s_sleep 4\n\t"
      "global_load_dwordx4 v[128:131], v224, s[60:61] sc1\n\t"
      "global_load_dwordx4 v[132:135], v225, s[60:61] sc1\n\t"
      "global_load_dwordx4 v[136:139], v226, s[60:61] sc1\n\t"
      "global_load_dwordx4 v[140:143], v227, s[60:61] sc1\n\t"
      "global_load_dwordx4 v[144:147], v228, s[60:61] sc1\n\t"
      "global_load_dwordx4 v[148:151], v229, s[60:61] sc1\n\t"
      "s_waitcnt vmcnt(5)\n\t"
      "v_max3_u32 v234, v128, v129, v130\n\t"
      "v_max_u32_e32 v234, v234, v131\n\t"
      "v_cmp_eq_u32_e32 vcc, -1, v234\n\t"
      "s_cbranch_vccnz FP28L_%=\n\t"
      "FP28X_%=:\n\t"
      "s_waitcnt vmcnt(0)\n\t"
      "s_branch FP28B_%=\n\t"
      "NP3R_%=:\n\t"
      "s_mov_b32 s51, 0\n\t"
      "NP3L_%=:\n\t"
      "s_add_u32 s51, s51, 1\n\t"
      "s_cmp_lt_u32 s51, 262144\n\t"
      "s_cbranch_scc0 NP3X_%=\n\t"
      "s_waitcnt vmcnt(0)\n\t"
      "s_sleep 1\n\t"
      "global_load_dwordx4 v[128:131], v224, s[58:59] sc1\n\t"
      "global_load_dwordx4 v[132:135], v225, s[58:59] sc1\n\t"
      "global_load_dwordx4 v[136:139], v226, s[58:59] sc1\n\t"
      "global_load_dwordx4 v[140:143], v227, s[58:59] sc1\n\t"
      "global_load_dwordx4 v[144:147], v228, s[58:59] sc1\n\t"
      "global_load_dwordx4 v[148:151], v229, s[58:59] sc1\n\t"
      "global_load_dwordx4 v[152:155], v230, s[58:59] sc1\n\t"
      "s_waitcnt vmcnt(0)\n\t"
      "v_max3_u32 v234, v128, v129, v130\n\t"
      "v_max3_u32 v234, v234, v131, v132\n\t"
      "v_max3_u32 v234, v234, v133, v134\n\t"
      "v_max3_u32 v234, v234, v135, v136\n\t"
      "v_max3_u32 v234, v234, v137, v138\n\t"
      "v_max3_u32 v234, v234, v139, v140\n\t"
      "v_max3_u32 v234, v234, v141, v142\n\t"
      "v_max3_u32 v234, v234, v143, v144\n\t"
      "v_max3_u32 v234, v234, v145, v146\n\t"
      "v_max3_u32 v234, v234, v147, v148\n\t"
      "v_max3_u32 v234, v234, v149, v150\n\t"
      "v_max3_u32 v234, v234, v151, v152\n\t"
      "v_max3_u32 v234, v234, v153, v154\n\t"
      "v_max_u32_e32 v234, v234, v155\n\t"
      "v_cmp_eq_u32_e32 vcc, -1, v234\n\t"
      "s_cbranch_vccnz NP3L_%=\n\t"
      "NP3X_%=:\n\t"
      "v_mfma_f32_16x16x32_bf16 v[200:203], v[128:131], v[0:3], v[168:171]\n\t"
      "v_mfma_f32_16x16x32_bf16 v[204:207], v[128:131], v[4:7], 0\n\t"
      "v_mfma_f32_16x16x32_bf16 v[208:211], v[128:131], v[8:11], 0\n\t"
      "v_mfma_f32_16x16x32_bf16 v[212:215], v[128:131], v[12:15], 0\n\t"
      "v_mfma_f32_16x16x32_bf16 v[200:203], v[132:135], v[16:19], v[200:203]\n\t"
      "v_mfma_f32_16x16x32_bf16 v[204:207], v[132:135], v[20:23], v[204:207]\n\t"
      "v_mfma_f32_16x16x32_bf16 v[208:211], v[132:135], v[24:27], v[208:211]\n\t"
      "v_mfma_f32_16x16x32_bf16 v[212:215], v[132:135], v[28:31], v[212:215]\n\t"
      "v_mfma_f32_16x16x32_bf16 v[200:203], v[136:139], v[32:35], v[200:203]\n\t"
      "v_mfma_f32_16x16x32_bf16 v[204:207], v[136:139], v[36:39], v[204:207]\n\t"
      "v_mfma_f32_16x16x32_bf16 v[208:211], v[136:139], v[40:43], v[208:211]\n\t"
      "v_mfma_f32_16x16x32_bf16 v[212:215], v[136:139], v[44:47], v[212:215]\n\t"
      "v_mfma_f32_16x16x32_bf16 v[200:203], v[140:143], v[48:51], v[200:203]\n\t"
      "v_mfma_f32_16x16x32_bf16 v[204:207], v[140:143], v[52:55], v[204:207]\n\t"
      "v_mfma_f32_16x16x32_bf16 v[208:211], v[140:143], v[56:59], v[208:211]\n\t"
      "v_mfma_f32_16x16x32_bf16 v[212:215], v[140:143], v[60:63], v[212:215]\n\t"
      "v_mfma_f32_16x16x32_bf16 v[200:203], v[144:147], v[64:67], v[200:203]\n\t"
      "v_mfma_f32_16x16x32_bf16 v[204:207], v[144:147], v[68:71], v[204:207]\n\t"
      "v_mfma_f32_16x16x32_bf16 v[208:211], v[144:147], v[72:75], v[208:211]\n\t"
      "v_mfma_f32_16x16x32_bf16 v[212:215], v[144:147], v[76:79], v[212:215]\n\t"
      "v_mfma_f32_16x16x32_bf16 v[200:203], v[148:151], v[80:83], v[200:203]\n\t"
      "v_mfma_f32_16x16x32_bf16 v[204:207], v[148:151], v[84:87], v[204:207]\n\t"
      "v_mfma_f32_16x16x32_bf16 v[208:211], v[148:151], v[88:91], v[208:211]\n\t"
      "v_mfma_f32_16x16x32_bf16 v[212:215], v[148:151], v[92:95], v[212:215]\n\t"
      "v_mfma_f32_16x16x32_bf16 v[200:203], v[152:155], v[96:99], v[200:203]\n\t"
      "v_mfma_f32_16x16x32_bf16 v[204:207], v[152:155], v[100:103], v[204:207]\n\t"
      "v_mfma_f32_16x16x32_bf16 v[208:211], v[152:155], v[104:107], v[208:211]\n\t"
      "v_mfma_f32_16x16x32_bf16 v[212:215], v[152:155], v[108:111], v[212:215]\n\t"
      "global_load_dwordx4 v[128:131], v224, s[60:61] sc1\n\t"
      "global_load_dwordx4 v[132:135], v225, s[60:61] sc1\n\t"
      "global_load_dwordx4 v[136:139], v226, s[60:61] sc1\n\t"
      "global_load_dwordx4 v[140:143], v227, s[60:61] sc1\n\t"
      "global_load_dwordx4 v[144:147], v228, s[60:61] sc1\n\t"
      "global_load_dwordx4 v[148:151], v229, s[60:61] sc1\n\t"
      "global_load_dwordx4 v[152:155], v230, s[60:61] sc1\n\t"
      "s_waitcnt vmcnt(0)\n\t"
      "s_nop 7\n\t"
      "s_branch NP3B_%=\n\t"
      "FP29R_%=:\n\t"
      "s_mov_b32 s51, 0\n\t"
      "FP29L_%=:\n\t"
      "s_add_u32 s51, s51, 1\n\t"
      "s_cmp_lt_u32 s51, 262144\n\t"
      "s_cbranch_scc0 FP29X_%=\n\t"
      "s_waitcnt vmcnt(0)\n\t"
      "s_sleep 4\n\t"
      "global_load_dwordx4 v[132:135], v225, s[60:61] sc1\n\t"
      "global_load_dwordx4 v[136:139], v226, s[60:61] sc1\n\t"
      "global_load_dwordx4 v[140:143], v227, s[60:61] sc1\n\t"
      "global_load_dwordx4 v[144:147], v228, s[60:61] sc1\n\t"
      "global_load_dwordx4 v[148:151], v229, s[60:61] sc1\n\t"
      "global_load_dwordx4 v[152:155], v230, s[60:61] sc1\n\t"
      "s_waitcnt vmcnt(5)\n\t"
      "v_max3_u32 v234, v132, v133, v134\n\t"
      "v_max_u32_e32 v234, v234, v135\n\t"
      "v_cmp_eq_u32_e32 vcc, -1, v234\n\t"
      "s_cbranch_vccnz FP29L_%=\n\t"
      "FP29X_%=:\n\t"
      "s_waitcnt vmcnt(0)\n\t"
      "s_branch FP29B_%=\n\t"
      "FP30R_%=:\n\t"
      "s_mov_b32 s51, 0\n\t"
      "FP30L_%=:\n\t"
      "s_add_u32 s51, s51, 1\n\t"
      "s_cmp_lt_u32 s51, 262144\n\t"
      "s_cbranch_scc0 FP30X_%=\n\t"
      "s_waitcnt vmcnt(0)\n\t"
      "s_sleep 4\n\t"
      "global_load_dwordx4 v[136:139], v226, s[60:61] sc1\n\t"
      "global_load_dwordx4 v[140:143], v227, s[60:61] sc1\n\t"
      "global_load_dwordx4 v[144:147], v228, s[60:61] sc1\n\t"
      "global_load_dwordx4 v[148:151], v229, s[60:61] sc1\n\t"
      "global_load_dwordx4 v[152:155], v230, s[60:61] sc1\n\t"
      "global_load_dwordx4 v[128:131], v224, s[62:63] sc1\n\t"
      "s_waitcnt vmcnt(5)\n\t"
      "v_max3_u32 v234, v136, v137, v138\n\t"
      "v_max_u32_e32 v234, v234, v139\n\t"
      "v_cmp_eq_u32_e32 vcc, -1, v234\n\t"
      "s_cbranch_vccnz FP30L_%=\n\t"
      "FP30X_%=:\n\t"
      "s_waitcnt vmcnt(0)\n\t"
      "s_branch FP30B_%=\n\t"
      "FP31R_%=:\n\t"
      "s_mov_b32 s51, 0\n\t"
      "FP31L_%=:\n\t"
      "s_add_u32 s51, s51, 1\n\t"
      "s_cmp_lt_u32 s51, 262144\n\t"
      "s_cbranch_scc0 FP31X_%=\n\t"
      "s_waitcnt vmcnt(0)\n\t"
      "s_sleep 4\n\t"
      "global_load_dwordx4 v[140:143], v227, s[60:61] sc1\n\t"
      "global_load_dwordx4 v[144:147], v228, s[60:61] sc1\n\t"
      "global_load_dwordx4 v[148:151], v229, s[60:61] sc1\n\t"
      "global_load_dwordx4 v[152:155], v230, s[60:61] sc1\n\t"
      "global_load_dwordx4 v[128:131], v224, s[62:63] sc1\n\t"
      "global_load_dwordx4 v[132:135], v225, s[62:63] sc1\n\t"
      "s_waitcnt vmcnt(5)\n\t"
      "v_max3_u32 v234, v140, v141, v142\n\t"
      "v_max_u32_e32 v234, v234, v143\n\t"
      "v_cmp_eq_u32_e32 vcc, -1, v234\n\t"
      "s_cbranch_vccnz FP31L_%=\n\t"
      "FP31X_%=:\n\t"
      "s_waitcnt vmcnt(0)\n\t"
      "s_branch FP31B_%=\n\t"
      "FP32R_%=:\n\t"
      "s_mov_b32 s51, 0\n\t"
      "FP32L_%=:\n\t"
      "s_add_u32 s51, s51, 1\n\t"
      "s_cmp_lt_u32 s51, 262144\n\t"
      "s_cbranch_scc0 FP32X_%=\n\t"
      "s_waitcnt vmcnt(0)\n\t"
      "s_sleep 4\n\t"
      "global_load_dwordx4 v[144:147], v228, s[60:61] sc1\n\t"
      "global_load_dwordx4 v[148:151], v229, s[60:61] sc1\n\t"
      "global_load_dwordx4 v[152:155], v230, s[60:61] sc1\n\t"
      "global_load_dwordx4 v[128:131], v224, s[62:63] sc1\n\t"
      "global_load_dwordx4 v[132:135], v225, s[62:63] sc1\n\t"
      "global_load_dwordx4 v[136:139], v226, s[62:63] sc1\n\t"
      "s_waitcnt vmcnt(5)\n\t"
      "v_max3_u32 v234, v144, v145, v146\n\t"
      "v_max_u32_e32 v234, v234, v147\n\t"
      "v_cmp_eq_u32_e32 vcc, -1, v234\n\t"
      "s_cbranch_vccnz FP32L_%=\n\t"
      "FP32X_%=:\n\t"
      "s_waitcnt vmcnt(0)\n\t"
      "s_branch FP32B_%=\n\t"
      "FP33R_%=:\n\t"
      "s_mov_b32 s51, 0\n\t"
      "FP33L_%=:\n\t"
      "s_add_u32 s51, s51, 1\n\t"
      "s_cmp_lt_u32 s51, 262144\n\t"
      "s_cbranch_scc0 FP33X_%=\n\t"
      "s_waitcnt vmcnt(0)\n\t"
      "s_sleep 4\n\t"
      "global_load_dwordx4 v[148:151], v229, s[60:61] sc1\n\t"
      "global_load_dwordx4 v[152:155], v230, s[60:61] sc1\n\t"
      "global_load_dwordx4 v[128:131], v224, s[62:63] sc1\n\t"
      "global_load_dwordx4 v[132:135], v225, s[62:63] sc1\n\t"
      "global_load_dwordx4 v[136:139], v226, s[62:63] sc1\n\t"
      "global_load_dwordx4 v[140:143], v227, s[62:63] sc1\n\t"
      "s_waitcnt vmcnt(5)\n\t"
      "v_max3_u32 v234, v148, v149, v150\n\t"
      "v_max_u32_e32 v234, v234, v151\n\t"
      "v_cmp_eq_u32_e32 vcc, -1, v234\n\t"
      "s_cbranch_vccnz FP33L_%=\n\t"
      "FP33X_%=:\n\t"
      "s_branch FP33B_%=\n\t"
      "FP34R_%=:\n\t"
      "s_mov_b32 s51, 0\n\t"
      "FP34L_%=:\n\t"
      "s_add_u32 s51, s51, 1\n\t"
      "s_cmp_lt_u32 s51, 262144\n\t"
      "s_cbranch_scc0 FP34X_%=\n\t"
      "s_waitcnt vmcnt(0)\n\t"
      "s_sleep 4\n\t"
      "global_load_dwordx4 v[152:155], v230, s[60:61] sc1\n\t"
      "global_load_dwordx4 v[128:131], v224, s[62:63] sc1\n\t"
      "global_load_dwordx4 v[132:135], v225, s[62:63] sc1\n\t"
      "global_load_dwordx4 v[136:139], v226, s[62:63] sc1\n\t"
      "global_load_dwordx4 v[140:143], v227, s[62:63] sc1\n\t"
      "global_load_dwordx4 v[144:147], v228, s[62:63] sc1\n\t"
      "s_waitcnt vmcnt(5)\n\t"
      "v_max3_u32 v234, v152, v153, v154\n\t"
      "v_max_u32_e32 v234, v234, v155\n\t"
      "v_cmp_eq_u32_e32 vcc, -1, v234\n\t"
      "s_cbranch_vccnz FP34L_%=\n\t"
      "FP34X_%=:\n\t"
      "s_waitcnt vmcnt(0)\n\t"
      "s_branch FP34B_%=\n\t"
      "FP35R_%=:\n\t"
      "s_mov_b32 s51, 0\n\t"
      "FP35L_%=:\n\t"
      "s_add_u32 s51, s51, 1\n\t"
      "s_cmp_lt_u32 s51, 262144\n\t"
      "s_cbranch_scc0 FP35X_%=\n\t"
      "s_waitcnt vmcnt(0)\n\t"
      "s_sleep 4\n\t"
      "global_load_dwordx4 v[128:131], v224, s[62:63] sc1\n\t"
      "global_load_dwordx4 v[132:135], v225, s[62:63] sc1\n\t"
      "global_load_dwordx4 v[136:139], v226, s[62:63] sc1\n\t"
      "global_load_dwordx4 v[140:143], v227, s[62:63] sc1\n\t"
      "global_load_dwordx4 v[144:147], v228, s[62:63] sc1\n\t"
      "global_load_dwordx4 v[148:151], v229, s[62:63] sc1\n\t"
      "s_waitcnt vmcnt(5)\n\t"
      "v_max3_u32 v234, v128, v129, v130\n\t"
      "v_max_u32_e32 v234, v234, v131\n\t"
      "v_cmp_eq_u32_e32 vcc, -1, v234\n\t"
      "s_cbranch_vccnz FP35L_%=\n\t"
      "FP35X_%=:\n\t"
      "s_waitcnt vmcnt(0)\n\t"
      "s_branch FP35B_%=\n\t"
      "NP4R_%=:\n\t"
      "s_mov_b32 s51, 0\n\t"
      "NP4L_%=:\n\t"
      "s_add_u32 s51, s51, 1\n\t"
      "s_cmp_lt_u32 s51, 262144\n\t"
      "s_cbranch_scc0 NP4X_%=\n\t"
      "s_waitcnt vmcnt(0)\n\t"
      "s_sleep 1\n\t"
      "global_load_dwordx4 v[128:131], v224, s[60:61] sc1\n\t"
      "global_load_dwordx4 v[132:135], v225, s[60:61] sc1\n\t"
      "global_load_dwordx4 v[136:139], v226, s[60:61] sc1\n\t"
      "global_load_dwordx4 v[140:143], v227, s[60:61] sc1\n\t"
      "global_load_dwordx4 v[144:147], v228, s[60:61] sc1\n\t"
      "global_load_dwordx4 v[148:151], v229, s[60:61] sc1\n\t"
      "global_load_dwordx4 v[152:155], v230, s[60:61] sc1\n\t"
      "s_waitcnt vmcnt(0)\n\t"
      "v_max3_u32 v234, v128, v129, v130\n\t"
      "v_max3_u32 v234, v234, v131, v132\n\t"
      "v_max3_u32 v234, v234, v133, v134\n\t"
      "v_max3_u32 v234, v234, v135, v136\n\t"
      "v_max3_u32 v234, v234, v137, v138\n\t"
      "v_max3_u32 v234, v234, v139, v140\n\t"
      "v_max3_u32 v234, v234, v141, v142\n\t"
      "v_max3_u32 v234, v234, v143, v144\n\t"
      "v_max3_u32 v234, v234, v145, v146\n\t"
      "v_max3_u32 v234, v234, v147, v148\n\t"
      "v_max3_u32 v234, v234, v149, v150\n\t"
      "v_max3_u32 v234, v234, v151, v152\n\t"
      "v_max3_u32 v234, v234, v153, v154\n\t"
      "v_max_u32_e32 v234, v234, v155\n\t"
      "v_cmp_eq_u32_e32 vcc, -1, v234\n\t"
      "s_cbranch_vccnz NP4L_%=\n\t"
      "NP4X_%=:\n\t"
      "v_mfma_f32_16x16x32_bf16 v[184:187], v[128:131], v[0:3], v[172:175]\n\t"
      "v_mfma_f32_16x16x32_bf16 v[188:191], v[128:131], v[4:7], 0\n\t"
      "v_mfma_f32_16x16x32_bf16 v[192:195], v[128:131], v[8:11], 0\n\t"
      "v_mfma_f32_16x16x32_bf16 v[196:199], v[128:131], v[12:15], 0\n\t"
      "v_mfma_f32_16x16x32_bf16 v[184:187], v[132:135], v[16:19], v[184:187]\n\t"
      "v_mfma_f32_16x16x32_bf16 v[188:191], v[132:135], v[20:23], v[188:191]\n\t"
      "v_mfma_f32_16x16x32_bf16 v[192:195], v[132:135], v[24:27], v[192:195]\n\t"
      "v_mfma_f32_16x16x32_bf16 v[196:199], v[132:135], v[28:31], v[196:199]\n\t"
      "v_mfma_f32_16x16x32_bf16 v[184:187], v[136:139], v[32:35], v[184:187]\n\t"
      "v_mfma_f32_16x16x32_bf16 v[188:191], v[136:139], v[36:39], v[188:191]\n\t"
      "v_mfma_f32_16x16x32_bf16 v[192:195], v[136:139], v[40:43], v[192:195]\n\t"
      "v_mfma_f32_16x16x32_bf16 v[196:199], v[136:139], v[44:47], v[196:199]\n\t"
      "v_mfma_f32_16x16x32_bf16 v[184:187], v[140:143], v[48:51], v[184:187]\n\t"
      "v_mfma_f32_16x16x32_bf16 v[188:191], v[140:143], v[52:55], v[188:191]\n\t"
      "v_mfma_f32_16x16x32_bf16 v[192:195], v[140:143], v[56:59], v[192:195]\n\t"
      "v_mfma_f32_16x16x32_bf16 v[196:199], v[140:143], v[60:63], v[196:199]\n\t"
      "v_mfma_f32_16x16x32_bf16 v[184:187], v[144:147], v[64:67], v[184:187]\n\t"
      "v_mfma_f32_16x16x32_bf16 v[188:191], v[144:147], v[68:71], v[188:191]\n\t"
      "v_mfma_f32_16x16x32_bf16 v[192:195], v[144:147], v[72:75], v[192:195]\n\t"
      "v_mfma_f32_16x16x32_bf16 v[196:199], v[144:147], v[76:79], v[196:199]\n\t"
      "v_mfma_f32_16x16x32_bf16 v[184:187], v[148:151], v[80:83], v[184:187]\n\t"
      "v_mfma_f32_16x16x32_bf16 v[188:191], v[148:151], v[84:87], v[188:191]\n\t"
      "v_mfma_f32_16x16x32_bf16 v[192:195], v[148:151], v[88:91], v[192:195]\n\t"
      "v_mfma_f32_16x16x32_bf16 v[196:199], v[148:151], v[92:95], v[196:199]\n\t"
      "v_mfma_f32_16x16x32_bf16 v[184:187], v[152:155], v[96:99], v[184:187]\n\t"
      "v_mfma_f32_16x16x32_bf16 v[188:191], v[152:155], v[100:103], v[188:191]\n\t"
      "v_mfma_f32_16x16x32_bf16 v[192:195], v[152:155], v[104:107], v[192:195]\n\t"
      "v_mfma_f32_16x16x32_bf16 v[196:199], v[152:155], v[108:111], v[196:199]\n\t"
      "global_load_dwordx4 v[128:131], v224, s[62:63] sc1\n\t"
      "global_load_dwordx4 v[132:135], v225, s[62:63] sc1\n\t"
      "global_load_dwordx4 v[136:139], v226, s[62:63] sc1\n\t"
      "global_load_dwordx4 v[140:143], v227, s[62:63] sc1\n\t"
      "global_load_dwordx4 v[144:147], v228, s[62:63] sc1\n\t"
      "global_load_dwordx4 v[148:151], v229, s[62:63] sc1\n\t"
      "global_load_dwordx4 v[152:155], v230, s[62:63] sc1\n\t"
      "s_waitcnt vmcnt(0)\n\t"
      "s_nop 7\n\t"
      "s_branch NP4B_%=\n\t"
      "FP36R_%=:\n\t"
      "s_mov_b32 s51, 0\n\t"
      "FP36L_%=:\n\t"
      "s_add_u32 s51, s51, 1\n\t"
      "s_cmp_lt_u32 s51, 262144\n\t"
      "s_cbranch_scc0 FP36X_%=\n\t"
      "s_waitcnt vmcnt(0)\n\t"
      "s_sleep 4\n\t"
      "global_load_dwordx4 v[132:135], v225, s[62:63] sc1\n\t"
      "global_load_dwordx4 v[136:139], v226, s[62:63] sc1\n\t"
      "global_load_dwordx4 v[140:143], v227, s[62:63] sc1\n\t"
      "global_load_dwordx4 v[144:147], v228, s[62:63] sc1\n\t"
      "global_load_dwordx4 v[148:151], v229, s[62:63] sc1\n\t"
      "global_load_dwordx4 v[152:155], v230, s[62:63] sc1\n\t"
      "s_waitcnt vmcnt(5)\n\t"
      "v_max3_u32 v234, v132, v133, v134\n\t"
      "v_max_u32_e32 v234, v234, v135\n\t"
      "v_cmp_eq_u32_e32 vcc, -1, v234\n\t"
      "s_cbranch_vccnz FP36L_%=\n\t"
      "FP36X_%=:\n\t"
      "s_waitcnt vmcnt(0)\n\t"
      "s_branch FP36B_%=\n\t"
      "FP37R_%=:\n\t"
      "s_mov_b32 s51, 0\n\t"
      "FP37L_%=:\n\t"
      "s_add_u32 s51, s51, 1\n\t"
      "s_cmp_lt_u32 s51, 262144\n\t"
      "s_cbranch_scc0 FP37X_%=\n\t"
      "s_waitcnt vmcnt(0)\n\t"
      "s_sleep 4\n\t"
      "global_load_dwordx4 v[136:139], v226, s[62:63] sc1\n\t"
      "global_load_dwordx4 v[140:143], v227, s[62:63] sc1\n\t"
      "global_load_dwordx4 v[144:147], v228, s[62:63] sc1\n\t"
      "global_load_dwordx4 v[148:151], v229, s[62:63] sc1\n\t"
      "global_load_dwordx4 v[152:155], v230, s[62:63] sc1\n\t"
      "global_load_dwordx4 v[128:131], v224, s[64:65] sc1\n\t"
      "s_waitcnt vmcnt(5)\n\t"
      "v_max3_u32 v234, v136, v137, v138\n\t"
      "v_max_u32_e32 v234, v234, v139\n\t"
      "v_cmp_eq_u32_e32 vcc, -1, v234\n\t"
      "s_cbranch_vccnz FP37L_%=\n\t"
      "FP37X_%=:\n\t"
      "s_waitcnt vmcnt(0)\n\t"
      "s_branch FP37B_%=\n\t"
      "FP38R_%=:\n\t"
      "s_mov_b32 s51, 0\n\t"
      "FP38L_%=:\n\t"
      "s_add_u32 s51, s51, 1\n\t"
      "s_cmp_lt_u32 s51, 262144\n\t"
      "s_cbranch_scc0 FP38X_%=\n\t"
      "s_waitcnt vmcnt(0)\n\t"
      "s_sleep 4\n\t"
      "global_load_dwordx4 v[140:143], v227, s[62:63] sc1\n\t"
      "global_load_dwordx4 v[144:147], v228, s[62:63] sc1\n\t"
      "global_load_dwordx4 v[148:151], v229, s[62:63] sc1\n\t"
      "global_load_dwordx4 v[152:155], v230, s[62:63] sc1\n\t"
      "global_load_dwordx4 v[128:131], v224, s[64:65] sc1\n\t"
      "global_load_dwordx4 v[132:135], v225, s[64:65] sc1\n\t"
      "s_waitcnt vmcnt(5)\n\t"
      "v_max3_u32 v234, v140, v141, v142\n\t"
      "v_max_u32_e32 v234, v234, v143\n\t"
      "v_cmp_eq_u32_e32 vcc, -1, v234\n\t"
      "s_cbranch_vccnz FP38L_%=\n\t"
      "FP38X_%=:\n\t"
      "s_waitcnt vmcnt(0)\n\t"
      "s_branch FP38B_%=\n\t"
      "FP39R_%=:\n\t"
      "s_mov_b32 s51, 0\n\t"
      "FP39L_%=:\n\t"
      "s_add_u32 s51, s51, 1\n\t"
      "s_cmp_lt_u32 s51, 262144\n\t"
      "s_cbranch_scc0 FP39X_%=\n\t"
      "s_waitcnt vmcnt(0)\n\t"
      "s_sleep 4\n\t"
      "global_load_dwordx4 v[144:147], v228, s[62:63] sc1\n\t"
      "global_load_dwordx4 v[148:151], v229, s[62:63] sc1\n\t"
      "global_load_dwordx4 v[152:155], v230, s[62:63] sc1\n\t"
      "global_load_dwordx4 v[128:131], v224, s[64:65] sc1\n\t"
      "global_load_dwordx4 v[132:135], v225, s[64:65] sc1\n\t"
      "global_load_dwordx4 v[136:139], v226, s[64:65] sc1\n\t"
      "s_waitcnt vmcnt(5)\n\t"
      "v_max3_u32 v234, v144, v145, v146\n\t"
      "v_max_u32_e32 v234, v234, v147\n\t"
      "v_cmp_eq_u32_e32 vcc, -1, v234\n\t"
      "s_cbranch_vccnz FP39L_%=\n\t"
      "FP39X_%=:\n\t"
      "s_waitcnt vmcnt(0)\n\t"
      "s_branch FP39B_%=\n\t"
      "FP40R_%=:\n\t"
      "s_mov_b32 s51, 0\n\t"
      "FP40L_%=:\n\t"
      "s_add_u32 s51, s51, 1\n\t"
      "s_cmp_lt_u32 s51, 262144\n\t"
      "s_cbranch_scc0 FP40X_%=\n\t"
      "s_waitcnt vmcnt(0)\n\t"
      "s_sleep 4\n\t"
      "global_load_dwordx4 v[148:151], v229, s[62:63] sc1\n\t"
      "global_load_dwordx4 v[152:155], v230, s[62:63] sc1\n\t"
      "global_load_dwordx4 v[128:131], v224, s[64:65] sc1\n\t"
      "global_load_dwordx4 v[132:135], v225, s[64:65] sc1\n\t"
      "global_load_dwordx4 v[136:139], v226, s[64:65] sc1\n\t"
      "global_load_dwordx4 v[140:143], v227, s[64:65] sc1\n\t"
      "s_waitcnt vmcnt(5)\n\t"
      "v_max3_u32 v234, v148, v149, v150\n\t"
      "v_max_u32_e32 v234, v234, v151\n\t"
      "v_cmp_eq_u32_e32 vcc, -1, v234\n\t"
      "s_cbranch_vccnz FP40L_%=\n\t"
      "FP40X_%=:\n\t"
      "s_branch FP40B_%=\n\t"
      "FP41R_%=:\n\t"
      "s_mov_b32 s51, 0\n\t"
      "FP41L_%=:\n\t"
      "s_add_u32 s51, s51, 1\n\t"
      "s_cmp_lt_u32 s51, 262144\n\t"
      "s_cbranch_scc0 FP41X_%=\n\t"
      "s_waitcnt vmcnt(0)\n\t"
      "s_sleep 4\n\t"
      "global_load_dwordx4 v[152:155], v230, s[62:63] sc1\n\t"
      "global_load_dwordx4 v[128:131], v224, s[64:65] sc1\n\t"
      "global_load_dwordx4 v[132:135], v225, s[64:65] sc1\n\t"
      "global_load_dwordx4 v[136:139], v226, s[64:65] sc1\n\t"
      "global_load_dwordx4 v[140:143], v227, s[64:65] sc1\n\t"
      "global_load_dwordx4 v[144:147], v228, s[64:65] sc1\n\t"
      "s_waitcnt vmcnt(5)\n\t"
      "v_max3_u32 v234, v152, v153, v154\n\t"
      "v_max_u32_e32 v234, v234, v155\n\t"
      "v_cmp_eq_u32_e32 vcc, -1, v234\n\t"
      "s_cbranch_vccnz FP41L_%=\n\t"
      "FP41X_%=:\n\t"
      "s_waitcnt vmcnt(0)\n\t"
      "s_branch FP41B_%=\n\t"
      "FP42R_%=:\n\t"
      "s_mov_b32 s51, 0\n\t"
      "FP42L_%=:\n\t"
      "s_add_u32 s51, s51, 1\n\t"
      "s_cmp_lt_u32 s51, 262144\n\t"
      "s_cbranch_scc0 FP42X_%=\n\t"
      "s_sleep 1\n\t"
      "s_mov_b64 exec, 15\n\t"
      "v_mov_b64_e32 v[232:233], 0\n\t"
      "global_atomic_or_x2 v[232:233], v247, v[232:233], s[12:13] sc0 sc1\n\t"
      "s_waitcnt vmcnt(0)\n\t"
      "v_subrev_u32_e32 v232, s14, v232\n\t"
      "v_cmp_ne_u32_e64 s[48:49], s15, v233\n\t"
      "v_subrev_u32_e32 v232, 6, v232\n\t"
      "v_cmp_lt_u32_e32 vcc, 7, v232\n\t"
      "s_mov_b64 exec, -1\n\t"
      "s_or_b64 vcc, vcc, s[48:49]\n\t"
      "s_cbranch_vccnz FP42L_%=\n\t"
      "FP42X_%=:\n\t"
      "FP42DR_%=:\n\t"
      "s_mov_b32 s51, 0\n\t"
      "FP42DL_%=:\n\t"
      "s_add_u32 s51, s51, 1\n\t"
      "s_cmp_lt_u32 s51, 262144\n\t"
      "s_cbranch_scc0 FP42DX_%=\n\t"
      "s_waitcnt vmcnt(0)\n\t"
      "s_sleep 1\n\t"
      "global_load_dwordx4 v[128:131], v224, s[64:65] sc1\n\t"
      "global_load_dwordx4 v[132:135], v225, s[64:65] sc1\n\t"
      "global_load_dwordx4 v[136:139], v226, s[64:65] sc1\n\t"
      "global_load_dwordx4 v[140:143], v227, s[64:65] sc1\n\t"
      "global_load_dwordx4 v[144:147], v228, s[64:65] sc1\n\t"
      "global_load_dwordx4 v[148:151], v229, s[64:65] sc1\n\t"
      "s_waitcnt vmcnt(5)\n\t"
      "v_max3_u32 v234, v128, v129, v130\n\t"
      "v_max_u32_e32 v234, v234, v131\n\t"
      "v_cmp_eq_u32_e32 vcc, -1, v234\n\t"
      "s_cbranch_vccnz FP42DL_%=\n\t"
      "FP42DX_%=:\n\t"
      "s_waitcnt vmcnt(0)\n\t"
      "s_branch FP42B_%=\n\t"
      "NP5R_%=:\n\t"
      "s_mov_b32 s51, 0\n\t"
      "NP5L_%=:\n\t"
      "s_add_u32 s51, s51, 1\n\t"
      "s_cmp_lt_u32 s51, 262144\n\t"
      "s_cbranch_scc0 NP5X_%=\n\t"
      "s_waitcnt vmcnt(0)\n\t"
      "s_sleep 1\n\t"
      "global_load_dwordx4 v[128:131], v224, s[62:63] sc1\n\t"
      "global_load_dwordx4 v[132:135], v225, s[62:63] sc1\n\t"
      "global_load_dwordx4 v[136:139], v226, s[62:63] sc1\n\t"
      "global_load_dwordx4 v[140:143], v227, s[62:63] sc1\n\t"
      "global_load_dwordx4 v[144:147], v228, s[62:63] sc1\n\t"
      "global_load_dwordx4 v[148:151], v229, s[62:63] sc1\n\t"
      "global_load_dwordx4 v[152:155], v230, s[62:63] sc1\n\t"
      "s_waitcnt vmcnt(0)\n\t"
      "s_nop 0\n\t"
      "v_max3_u32 v234, v128, v129, v130\n\t"
      "v_max3_u32 v234, v234, v131, v132\n\t"
      "v_max3_u32 v234, v234, v133, v134\n\t"
      "v_max3_u32 v234, v234, v135, v136\n\t"
      "v_max3_u32 v234, v234, v137, v138\n\t"
      "v_max3_u32 v234, v234, v139, v140\n\t"
      "v_max3_u32 v234, v234, v141, v142\n\t"
      "v_max3_u32 v234, v234, v143, v144\n\t"
      "v_max3_u32 v234, v234, v145, v146\n\t"
      "v_max3_u32 v234, v234, v147, v148\n\t"
      "v_max3_u32 v234, v234, v149, v150\n\t"
      "v_max3_u32 v234, v234, v151, v152\n\t"
      "v_max3_u32 v234, v234, v153, v154\n\t"
      "v_max_u32_e32 v234, v234, v155\n\t"
      "v_cmp_eq_u32_e32 vcc, -1, v234\n\t"
      "s_cbranch_vccnz NP5L_%=\n\t"
      "NP5X_%=:\n\t"
      "v_mfma_f32_16x16x32_bf16 v[200:203], v[128:131], v[0:3], v[176:179]\n\t"
      "v_mfma_f32_16x16x32_bf16 v[204:207], v[128:131], v[4:7], 0\n\t"
      "v_mfma_f32_16x16x32_bf16 v[208:211], v[128:131], v[8:11], 0\n\t"
      "v_mfma_f32_16x16x32_bf16 v[212:215], v[128:131], v[12:15], 0\n\t"
      "v_mfma_f32_16x16x32_bf16 v[200:203], v[132:135], v[16:19], v[200:203]\n\t"
      "v_mfma_f32_16x16x32_bf16 v[204:207], v[132:135], v[20:23], v[204:207]\n\t"
      "v_mfma_f32_16x16x32_bf16 v[208:211], v[132:135], v[24:27], v[208:211]\n\t"
      "v_mfma_f32_16x16x32_bf16 v[212:215], v[132:135], v[28:31], v[212:215]\n\t"
      "v_mfma_f32_16x16x32_bf16 v[200:203], v[136:139], v[32:35], v[200:203]\n\t"
      "v_mfma_f32_16x16x32_bf16 v[204:207], v[136:139], v[36:39], v[204:207]\n\t"
      "v_mfma_f32_16x16x32_bf16 v[208:211], v[136:139], v[40:43], v[208:211]\n\t"
      "v_mfma_f32_16x16x32_bf16 v[212:215], v[136:139], v[44:47], v[212:215]\n\t"
      "v_mfma_f32_16x16x32_bf16 v[200:203], v[140:143], v[48:51], v[200:203]\n\t"
      "v_mfma_f32_16x16x32_bf16 v[204:207], v[140:143], v[52:55], v[204:207]\n\t"
      "v_mfma_f32_16x16x32_bf16 v[208:211], v[140:143], v[56:59], v[208:211]\n\t"
      "v_mfma_f32_16x16x32_bf16 v[212:215], v[140:143], v[60:63], v[212:215]\n\t"
      "v_mfma_f32_16x16x32_bf16 v[200:203], v[144:147], v[64:67], v[200:203]\n\t"
      "v_mfma_f32_16x16x32_bf16 v[204:207], v[144:147], v[68:71], v[204:207]\n\t"
      "v_mfma_f32_16x16x32_bf16 v[208:211], v[144:147], v[72:75], v[208:211]\n\t"
      "v_mfma_f32_16x16x32_bf16 v[212:215], v[144:147], v[76:79], v[212:215]\n\t"
      "v_mfma_f32_16x16x32_bf16 v[200:203], v[148:151], v[80:83], v[200:203]\n\t"
      "v_mfma_f32_16x16x32_bf16 v[204:207], v[148:151], v[84:87], v[204:207]\n\t"
      "v_mfma_f32_16x16x32_bf16 v[208:211], v[148:151], v[88:91], v[208:211]\n\t"
      "v_mfma_f32_16x16x32_bf16 v[212:215], v[148:151], v[92:95], v[212:215]\n\t"
      "v_mfma_f32_16x16x32_bf16 v[200:203], v[152:155], v[96:99], v[200:203]\n\t"
      "v_mfma_f32_16x16x32_bf16 v[204:207], v[152:155], v[100:103], v[204:207]\n\t"
      "v_mfma_f32_16x16x32_bf16 v[208:211], v[152:155], v[104:107], v[208:211]\n\t"
      "v_mfma_f32_16x16x32_bf16 v[212:215], v[152:155], v[108:111], v[212:215]\n\t"
      "global_load_dwordx4 v[128:131], v224, s[64:65] sc1\n\t"
      "global_load_dwordx4 v[132:135], v225, s[64:65] sc1\n\t"
      "global_load_dwordx4 v[136:139], v226, s[64:65] sc1\n\t"
      "global_load_dwordx4 v[140:143], v227, s[64:65] sc1\n\t"
      "global_load_dwordx4 v[144:147], v228, s[64:65] sc1\n\t"
      "global_load_dwordx4 v[148:151], v229, s[64:65] sc1\n\t"
      "global_load_dwordx4 v[152:155], v230, s[64:65] sc1\n\t"
      "s_waitcnt vmcnt(0)\n\t"
      "s_nop 7\n\t"
      "s_branch NP5B_%=\n\t"
      "FP43R_%=:\n\t"
      "s_mov_b32 s51, 0\n\t"
      "FP43L_%=:\n\t"
      "s_add_u32 s51, s51, 1\n\t"
      "s_cmp_lt_u32 s51, 262144\n\t"
      "s_cbranch_scc0 FP43X_%=\n\t"
      "s_sleep 1\n\t"
      "s_mov_b64 exec, 15\n\t"
      "v_mov_b64_e32 v[232:233], 0\n\t"
      "global_atomic_or_x2 v[232:233], v247, v[232:233], s[12:13] sc0 sc1\n\t"
      "s_waitcnt vmcnt(0)\n\t"
      "v_subrev_u32_e32 v232, s14, v232\n\t"
      "v_cmp_ne_u32_e64 s[48:49], s15, v233\n\t"
      "v_subrev_u32_e32 v232, 6, v232\n\t"
      "v_cmp_lt_u32_e32 vcc, 7, v232\n\t"
      "s_mov_b64 exec, -1\n\t"
      "s_or_b64 vcc, vcc, s[48:49]\n\t"
      "s_cbranch_vccnz FP43L_%=\n\t"
      "FP43X_%=:\n\t"
      "FP43DR_%=:\n\t"
      "s_mov_b32 s51, 0\n\t"
      "FP43DL_%=:\n\t"
      "s_add_u32 s51, s51, 1\n\t"
      "s_cmp_lt_u32 s51, 262144\n\t"
      "s_cbranch_scc0 FP43DX_%=\n\t"
      "s_waitcnt vmcnt(0)\n\t"
      "s_sleep 1\n\t"
      "global_load_dwordx4 v[132:135], v225, s[64:65] sc1\n\t"
      "global_load_dwordx4 v[136:139], v226, s[64:65] sc1\n\t"
      "global_load_dwordx4 v[140:143], v227, s[64:65] sc1\n\t"
      "global_load_dwordx4 v[144:147], v228, s[64:65] sc1\n\t"
      "global_load_dwordx4 v[148:151], v229, s[64:65] sc1\n\t"
      "global_load_dwordx4 v[152:155], v230, s[64:65] sc1\n\t"
      "s_waitcnt vmcnt(5)\n\t"
      "v_max3_u32 v234, v132, v133, v134\n\t"
      "v_max_u32_e32 v234, v234, v135\n\t"
      "v_cmp_eq_u32_e32 vcc, -1, v234\n\t"
      "s_cbranch_vccnz FP43DL_%=\n\t"
      "FP43DX_%=:\n\t"
      "s_waitcnt vmcnt(0)\n\t"
      "s_branch FP43B_%=\n\t"
      "FP44R_%=:\n\t"
      "s_mov_b32 s51, 0\n\t"
      "FP44L_%=:\n\t"
      "s_add_u32 s51, s51, 1\n\t"
      "s_cmp_lt_u32 s51, 262144\n\t"
      "s_cbranch_scc0 FP44X_%=\n\t"
      "s_sleep 1\n\t"
      "s_mov_b64 exec, 15\n\t"
      "v_mov_b64_e32 v[232:233], 0\n\t"
      "global_atomic_or_x2 v[232:233], v247, v[232:233], s[12:13] sc0 sc1\n\t"
      "s_waitcnt vmcnt(0)\n\t"
      "v_subrev_u32_e32 v232, s14, v232\n\t"
      "v_cmp_ne_u32_e64 s[48:49], s15, v233\n\t"
      "v_subrev_u32_e32 v232, 6, v232\n\t"
      "v_cmp_lt_u32_e32 vcc, 7, v232\n\t"
      "s_mov_b64 exec, -1\n\t"
      "s_or_b64 vcc, vcc, s[48:49]\n\t"
      "s_cbranch_vccnz FP44L_%=\n\t"
      "FP44X_%=:\n\t"
      "FP44DR_%=:\n\t"
      "s_mov_b32 s51, 0\n\t"
      "FP44DL_%=:\n\t"
      "s_add_u32 s51, s51, 1\n\t"
      "s_cmp_lt_u32 s51, 262144\n\t"
      "s_cbranch_scc0 FP44DX_%=\n\t"
      "s_waitcnt vmcnt(0)\n\t"
      "s_sleep 1\n\t"
      "global_load_dwordx4 v[136:139], v226, s[64:65] sc1\n\t"
      "global_load_dwordx4 v[140:143], v227, s[64:65] sc1\n\t"
      "global_load_dwordx4 v[144:147], v228, s[64:65] sc1\n\t"
      "global_load_dwordx4 v[148:151], v229, s[64:65] sc1\n\t"
      "global_load_dwordx4 v[152:155], v230, s[64:65] sc1\n\t"
      "s_waitcnt vmcnt(4)\n\t"
      "v_max3_u32 v234, v136, v137, v138\n\t"
      "v_max_u32_e32 v234, v234, v139\n\t"
      "v_cmp_eq_u32_e32 vcc, -1, v234\n\t"
      "s_cbranch_vccnz FP44DL_%=\n\t"
      "FP44DX_%=:\n\t"
      "s_waitcnt vmcnt(0)\n\t"
      "s_branch FP44B_%=\n\t"
      "FP45R_%=:\n\t"
      "s_mov_b32 s51, 0\n\t"
      "FP45L_%=:\n\t"
      "s_add_u32 s51, s51, 1\n\t"
      "s_cmp_lt_u32 s51, 262144\n\t"
      "s_cbranch_scc0 FP45X_%=\n\t"
      "s_sleep 1\n\t"
      "s_mov_b64 exec, 15\n\t"
      "v_mov_b64_e32 v[232:233], 0\n\t"
      "global_atomic_or_x2 v[232:233], v247, v[232:233], s[12:13] sc0 sc1\n\t"
      "s_waitcnt vmcnt(0)\n\t"
      "v_subrev_u32_e32 v232, s14, v232\n\t"
      "v_cmp_ne_u32_e64 s[48:49], s15, v233\n\t"
      "v_subrev_u32_e32 v232, 6, v232\n\t"
      "v_cmp_lt_u32_e32 vcc, 7, v232\n\t"
      "s_mov_b64 exec, -1\n\t"
      "s_or_b64 vcc, vcc, s[48:49]\n\t"
      "s_cbranch_vccnz FP45L_%=\n\t"
      "FP45X_%=:\n\t"
      "FP45DR_%=:\n\t"
      "s_mov_b32 s51, 0\n\t"
      "FP45DL_%=:\n\t"
      "s_add_u32 s51, s51, 1\n\t"
      "s_cmp_lt_u32 s51, 262144\n\t"
      "s_cbranch_scc0 FP45DX_%=\n\t"
      "s_waitcnt vmcnt(0)\n\t"
      "s_sleep 1\n\t"
      "global_load_dwordx4 v[140:143], v227, s[64:65] sc1\n\t"
      "global_load_dwordx4 v[144:147], v228, s[64:65] sc1\n\t"
      "global_load_dwordx4 v[148:151], v229, s[64:65] sc1\n\t"
      "global_load_dwordx4 v[152:155], v230, s[64:65] sc1\n\t"
      "s_waitcnt vmcnt(3)\n\t"
      "v_max3_u32 v234, v140, v141, v142\n\t"
      "v_max_u32_e32 v234, v234, v143\n\t"
      "v_cmp_eq_u32_e32 vcc, -1, v234\n\t"
      "s_cbranch_vccnz FP45DL_%=\n\t"
      "FP45DX_%=:\n\t"
      "s_waitcnt vmcnt(0)\n\t"
      "s_branch FP45B_%=\n\t"
      "FP46R_%=:\n\t"
      "s_mov_b32 s51, 0\n\t"
      "FP46L_%=:\n\t"
      "s_add_u32 s51, s51, 1\n\t"
      "s_cmp_lt_u32 s51, 262144\n\t"
      "s_cbranch_scc0 FP46X_%=\n\t"
      "s_sleep 1\n\t"
      "s_mov_b64 exec, 15\n\t"
      "v_mov_b64_e32 v[232:233], 0\n\t"
      "global_atomic_or_x2 v[232:233], v247, v[232:233], s[12:13] sc0 sc1\n\t"
      "s_waitcnt vmcnt(0)\n\t"
      "v_subrev_u32_e32 v232, s14, v232\n\t"
      "v_cmp_ne_u32_e64 s[48:49], s15, v233\n\t"
      "v_subrev_u32_e32 v232, 6, v232\n\t"
      "v_cmp_lt_u32_e32 vcc, 7, v232\n\t"
      "s_mov_b64 exec, -1\n\t"
      "s_or_b64 vcc, vcc, s[48:49]\n\t"
      "s_cbranch_vccnz FP46L_%=\n\t"
      "FP46X_%=:\n\t"
      "FP46DR_%=:\n\t"
      "s_mov_b32 s51, 0\n\t"
      "FP46DL_%=:\n\t"
      "s_add_u32 s51, s51, 1\n\t"
      "s_cmp_lt_u32 s51, 262144\n\t"
      "s_cbranch_scc0 FP46DX_%=\n\t"
      "s_waitcnt vmcnt(0)\n\t"
      "s_sleep 1\n\t"
      "global_load_dwordx4 v[144:147], v228, s[64:65] sc1\n\t"
      "global_load_dwordx4 v[148:151], v229, s[64:65] sc1\n\t"
      "global_load_dwordx4 v[152:155], v230, s[64:65] sc1\n\t"
      "s_waitcnt vmcnt(2)\n\t"
      "v_max3_u32 v234, v144, v145, v146\n\t"
      "v_max_u32_e32 v234, v234, v147\n\t"
      "v_cmp_eq_u32_e32 vcc, -1, v234\n\t"
      "s_cbranch_vccnz FP46DL_%=\n\t"
      "FP46DX_%=:\n\t"
      "s_waitcnt vmcnt(0)\n\t"
      "s_branch FP46B_%=\n\t"
      "FP47R_%=:\n\t"
      "s_mov_b32 s51, 0\n\t"
      "FP47L_%=:\n\t"
      "s_add_u32 s51, s51, 1\n\t"
      "s_cmp_lt_u32 s51, 262144\n\t"
      "s_cbranch_scc0 FP47X_%=\n\t"
      "s_sleep 1\n\t"
      "s_mov_b64 exec, 15\n\t"
      "v_mov_b64_e32 v[232:233], 0\n\t"
      "global_atomic_or_x2 v[232:233], v247, v[232:233], s[12:13] sc0 sc1\n\t"
      "s_waitcnt vmcnt(0)\n\t"
      "v_subrev_u32_e32 v232, s14, v232\n\t"
      "v_cmp_ne_u32_e64 s[48:49], s15, v233\n\t"
      "v_subrev_u32_e32 v232, 6, v232\n\t"
      "v_cmp_lt_u32_e32 vcc, 7, v232\n\t"
      "s_mov_b64 exec, -1\n\t"
      "s_or_b64 vcc, vcc, s[48:49]\n\t"
      "s_cbranch_vccnz FP47L_%=\n\t"
      "FP47X_%=:\n\t"
      "FP47DR_%=:\n\t"
      "s_mov_b32 s51, 0\n\t"
      "FP47DL_%=:\n\t"
      "s_add_u32 s51, s51, 1\n\t"
      "s_cmp_lt_u32 s51, 262144\n\t"
      "s_cbranch_scc0 FP47DX_%=\n\t"
      "s_waitcnt vmcnt(0)\n\t"
      "s_sleep 1\n\t"
      "global_load_dwordx4 v[148:151], v229, s[64:65] sc1\n\t"
      "global_load_dwordx4 v[152:155], v230, s[64:65] sc1\n\t"
      "s_waitcnt vmcnt(1)\n\t"
      "v_max3_u32 v234, v148, v149, v150\n\t"
      "v_max_u32_e32 v234, v234, v151\n\t"
      "v_cmp_eq_u32_e32 vcc, -1, v234\n\t"
      "s_cbranch_vccnz FP47DL_%=\n\t"
      "FP47DX_%=:\n\t"
      "s_branch FP47B_%=\n\t"
      "FP48R_%=:\n\t"
      "s_mov_b32 s51, 0\n\t"
      "FP48L_%=:\n\t"
      "s_add_u32 s51, s51, 1\n\t"
      "s_cmp_lt_u32 s51, 262144\n\t"
      "s_cbranch_scc0 FP48X_%=\n\t"
      "s_sleep 1\n\t"
      "s_mov_b64 exec, 15\n\t"
      "v_mov_b64_e32 v[232:233], 0\n\t"
      "global_atomic_or_x2 v[232:233], v247, v[232:233], s[12:13] sc0 sc1\n\t"
      "s_waitcnt vmcnt(0)\n\t"
      "v_subrev_u32_e32 v232, s14, v232\n\t"
      "v_cmp_ne_u32_e64 s[48:49], s15, v233\n\t"
      "v_subrev_u32_e32 v232, 6, v232\n\t"
      "v_cmp_lt_u32_e32 vcc, 7, v232\n\t"
      "s_mov_b64 exec, -1\n\t"
      "s_or_b64 vcc, vcc, s[48:49]\n\t"
      "s_cbranch_vccnz FP48L_%=\n\t"
      "FP48X_%=:\n\t"
      "FP48DR_%=:\n\t"
      "s_mov_b32 s51, 0\n\t"
      "FP48DL_%=:\n\t"
      "s_add_u32 s51, s51, 1\n\t"
      "s_cmp_lt_u32 s51, 262144\n\t"
      "s_cbranch_scc0 FP48DX_%=\n\t"
      "s_waitcnt vmcnt(0)\n\t"
      "s_sleep 1\n\t"
      "global_load_dwordx4 v[152:155], v230, s[64:65] sc1\n\t"
      "s_waitcnt vmcnt(0)\n\t"
      "v_max3_u32 v234, v152, v153, v154\n\t"
      "v_max_u32_e32 v234, v234, v155\n\t"
      "v_cmp_eq_u32_e32 vcc, -1, v234\n\t"
      "s_cbranch_vccnz FP48DL_%=\n\t"
      "FP48DX_%=:\n\t"
      "s_waitcnt vmcnt(0)\n\t"
      "s_branch FP48B_%=\n\t"
      "NP6R_%=:\n\t"
      "s_mov_b32 s51, 0\n\t"
      "NP6L_%=:\n\t"
      "s_add_u32 s51, s51, 1\n\t"
      "s_cmp_lt_u32 s51, 262144\n\t"
      "s_cbranch_scc0 NP6X_%=\n\t"
      "s_waitcnt vmcnt(0)\n\t"
      "s_sleep 1\n\t"
      "global_load_dwordx4 v[128:131], v224, s[64:65] sc1\n\t"
      "global_load_dwordx4 v[132:135], v225, s[64:65] sc1\n\t"
      "global_load_dwordx4 v[136:139], v226, s[64:65] sc1\n\t"
      "global_load_dwordx4 v[140:143], v227, s[64:65] sc1\n\t"
      "global_load_dwordx4 v[144:147], v228, s[64:65] sc1\n\t"
      "global_load_dwordx4 v[148:151], v229, s[64:65] sc1\n\t"
      "global_load_dwordx4 v[152:155], v230, s[64:65] sc1\n\t"
      "s_waitcnt vmcnt(0)\n\t"
      "s_nop 0\n\t"
      "v_max3_u32 v234, v128, v129, v130\n\t"
      "v_max3_u32 v234, v234, v131, v132\n\t"
      "v_max3_u32 v234, v234, v133, v134\n\t"
      "v_max3_u32 v234, v234, v135, v136\n\t"
      "v_max3_u32 v234, v234, v137, v138\n\t"
      "v_max3_u32 v234, v234, v139, v140\n\t"
      "v_max3_u32 v234, v234, v141, v142\n\t"
      "v_max3_u32 v234, v234, v143, v144\n\t"
      "v_max3_u32 v234, v234, v145, v146\n\t"
      "v_max3_u32 v234, v234, v147, v148\n\t"
      "v_max3_u32 v234, v234, v149, v150\n\t"
      "v_max3_u32 v234, v234, v151, v152\n\t"
      "v_max3_u32 v234, v234, v153, v154\n\t"
      "v_max_u32_e32 v234, v234, v155\n\t"
      "v_cmp_eq_u32_e32 vcc, -1, v234\n\t"
      "s_cbranch_vccnz NP6L_%=\n\t"
      "NP6X_%=:\n\t"
      "v_mfma_f32_16x16x32_bf16 v[184:187], v[128:131], v[0:3], v[180:183]\n\t"
      "v_mfma_f32_16x16x32_bf16 v[188:191], v[128:131], v[4:7], 0\n\t"
      "v_mfma_f32_16x16x32_bf16 v[192:195], v[128:131], v[8:11], 0\n\t"
      "v_mfma_f32_16x16x32_bf16 v[196:199], v[128:131], v[12:15], 0\n\t"
      "v_mfma_f32_16x16x32_bf16 v[184:187], v[132:135], v[16:19], v[184:187]\n\t"
      "v_mfma_f32_16x16x32_bf16 v[188:191], v[132:135], v[20:23], v[188:191]\n\t"
      "v_mfma_f32_16x16x32_bf16 v[192:195], v[132:135], v[24:27], v[192:195]\n\t"
      "v_mfma_f32_16x16x32_bf16 v[196:199], v[132:135], v[28:31], v[196:199]\n\t"
      "v_mfma_f32_16x16x32_bf16 v[184:187], v[136:139], v[32:35], v[184:187]\n\t"
      "v_mfma_f32_16x16x32_bf16 v[188:191], v[136:139], v[36:39], v[188:191]\n\t"
      "v_mfma_f32_16x16x32_bf16 v[192:195], v[136:139], v[40:43], v[192:195]\n\t"
      "v_mfma_f32_16x16x32_bf16 v[196:199], v[136:139], v[44:47], v[196:199]\n\t"
      "v_mfma_f32_16x16x32_bf16 v[184:187], v[140:143], v[48:51], v[184:187]\n\t"
      "v_mfma_f32_16x16x32_bf16 v[188:191], v[140:143], v[52:55], v[188:191]\n\t"
      "v_mfma_f32_16x16x32_bf16 v[192:195], v[140:143], v[56:59], v[192:195]\n\t"
      "v_mfma_f32_16x16x32_bf16 v[196:199], v[140:143], v[60:63], v[196:199]\n\t"
      "v_mfma_f32_16x16x32_bf16 v[184:187], v[144:147], v[64:67], v[184:187]\n\t"
      "v_mfma_f32_16x16x32_bf16 v[188:191], v[144:147], v[68:71], v[188:191]\n\t"
      "v_mfma_f32_16x16x32_bf16 v[192:195], v[144:147], v[72:75], v[192:195]\n\t"
      "v_mfma_f32_16x16x32_bf16 v[196:199], v[144:147], v[76:79], v[196:199]\n\t"
      "v_mfma_f32_16x16x32_bf16 v[184:187], v[148:151], v[80:83], v[184:187]\n\t"
      "v_mfma_f32_16x16x32_bf16 v[188:191], v[148:151], v[84:87], v[188:191]\n\t"
      "v_mfma_f32_16x16x32_bf16 v[192:195], v[148:151], v[88:91], v[192:195]\n\t"
      "v_mfma_f32_16x16x32_bf16 v[196:199], v[148:151], v[92:95], v[196:199]\n\t"
      "v_mfma_f32_16x16x32_bf16 v[184:187], v[152:155], v[96:99], v[184:187]\n\t"
      "v_mfma_f32_16x16x32_bf16 v[188:191], v[152:155], v[100:103], v[188:191]\n\t"
      "v_mfma_f32_16x16x32_bf16 v[192:195], v[152:155], v[104:107], v[192:195]\n\t"
      "v_mfma_f32_16x16x32_bf16 v[196:199], v[152:155], v[108:111], v[196:199]\n\t"
      "s_waitcnt vmcnt(0)\n\t"
      "s_nop 7\n\t"
      "s_branch NP6B_%=\n\t"
      : "+{s[4:19]}"(s), "+{s[20:27]}"(s2), "+{s[28:29]}"(kr), "+{s30}"(od), "+{v[240:247]}"(v), "+{v[136:139]}"(vo), "+{v140}"(vq)
      :
      : "memory", "vcc", "scc",
        "v0", "v1", "v2", "v3", "v4", "v5", "v6", "v7", "v8", "v9", "v10", "v11", "v12", "v13", "v14", "v15", "v16",
        "v17", "v18", "v19", "v20", "v21", "v22", "v23", "v24", "v25", "v26", "v27", "v28", "v29", "v30", "v31", "v32",
        "v33", "v34", "v35", "v36", "v37", "v38", "v39", "v40", "v41", "v42", "v43", "v44", "v45", "v46", "v47", "v48",
        "v49", "v50", "v51", "v52", "v53", "v54", "v55", "v56", "v57", "v58", "v59", "v60", "v61", "v62", "v63", "v64",
        "v65", "v66", "v67", "v68", "v69", "v70", "v71", "v72", "v73", "v74", "v75", "v76", "v77", "v78", "v79", "v80",
        "v81", "v82", "v83", "v84", "v85", "v86", "v87", "v88", "v89", "v90", "v91", "v92", "v93", "v94", "v95", "v96",
        "v97", "v98", "v99", "v100", "v101", "v102", "v103", "v104", "v105", "v106", "v107", "v108", "v109", "v110",
        "v111", "v112", "v113", "v114", "v115", "v116", "v117", "v118", "v119", "v120", "v121", "v122", "v123", "v124",
        "v125", "v126", "v127", "v128", "v129", "v130", "v131", "v132", "v133", "v134", "v135", "v141", "v142", "v143",
        "v144", "v145", "v146", "v147", "v148", "v149", "v150", "v151", "v152", "v153", "v154", "v155", "v156", "v157",
        "v158", "v159", "v160", "v161", "v162", "v163", "v164", "v165", "v166", "v167", "v168", "v169", "v170", "v171",
        "v172", "v173", "v174", "v175", "v176", "v177", "v178", "v179", "v180", "v181", "v182", "v183", "v184", "v185",
        "v186", "v187", "v188", "v189", "v190", "v191", "v192", "v193", "v194", "v195", "v196", "v197", "v198", "v199",
        "v200", "v201", "v202", "v203", "v204", "v205", "v206", "v207", "v208", "v209", "v210", "v211", "v212", "v213",
        "v214", "v215", "v216", "v217", "v218", "v219", "v220", "v221", "v222", "v223", "v224", "v225", "v226", "v227",
        "v228", "v229", "v230", "v231", "v232", "v233", "v234", "v235", "v236", "v237", "v238", "v239", "v248", "v249",
        "v250", "v251", "v252", "v253", "s31", "s44", "s45", "s46", "s47", "s48", "s49", "s50", "s51", "s52", "s53",
        "s54", "s55", "s56", "s57", "s58", "s59", "s60", "s61", "s62", "s63", "s64", "s65", "s66", "s67", "s68", "s69",
        "s70"
    );
    __builtin_amdgcn_endpgm();
    }
  }
}

extern "C" void kernel_launch(void* const* d_in, const int* in_sizes, int n_in, void* d_out, int out_size,
                              void* d_ws, size_t ws_size, hipStream_t stream) {
  (void)hipFuncSetAttribute((const void*)fused_block, hipFuncAttributeMaxDynamicSharedMemorySize, LDS_BYTES);
  fused_block<<<256, 512, LDS_BYTES, stream>>>((const char*)d_in[0], (const char*)d_in[1], (const char*)d_in[2],
                                               (const char*)d_in[4], (const char*)d_in[5], (const char*)d_in[6],
                                               (const char*)d_in[7], (char*)d_out, (char*)d_ws);
}
```

```cpp
#include <hip/hip_runtime.h>
#include <stdint.h>

#define WS_ST 0
#define WS_HN 2097152
#define WS_O 4194304
#define WS_G 6291456
#define ST_FL 65536
#define ST_PW 114688
#define WS_EP 131072
#define PROBE 1835008
#define ST_CK 3981312
#define ST_H2 4112384
#define ST_H3 8175616
#define ST_H4 8306688
#define A_UNIT 5120
#define A_HALO 40960
#define A_SLOT 43008
#define A_CS 131072
#define A_ST 132096
#define A_EX 142336
#define A_FL 154624
#define A_MK 154752
#define B_UNIT 7168
#define B_SLOT 57344
#define B_X 114688
#define B_OP 928
#define B_PS 129536
#define B_WX 130560
#define B_WY 66048
#define G_WV 16384
#define A_W0 49152
#define A_WD 40960
#define G_RED 0
#define G_RED2 32768
#define LDS_KEY 163600
#define LDS_SINK 163328
#define LDS_BYTES 163840
#define XO 0
#define M44 1
#define PRE 2
#define KROT 14
#define KMUL 1
#define HP4 1
#define W2E 1
#define R4 0
#define SP3 1

typedef unsigned short u16;
typedef unsigned long long u64;
typedef __attribute__((ext_vector_type(4))) unsigned int u32x4;
typedef __attribute__((ext_vector_type(8))) unsigned int u32x8;
typedef __attribute__((ext_vector_type(16))) unsigned int u32x16;
#define UNI(x) (unsigned)__builtin_amdgcn_readfirstlane((int)(x))
#define PIN() __builtin_amdgcn_sched_barrier(0)

__device__ __forceinline__ int swz_tr(int row) { return (((row >> 1) & 1) | (((row >> 3) & 1) << 1)) << 1; }
__device__ __forceinline__ int swz_row(int row) { return (row >> 1) & 7; }
__device__ __forceinline__ int unit(int blk, int slice, int par) { return (((blk < 14 ? blk : blk - 14) * 8 + slice) * 2 + ((blk < 14 ? 0 : 1) ^ par)) * 8192; }
__device__ __forceinline__ int part(int n, int side) { return (n >> 13) * 16384 + side * 8192 + (n & 8191); }
template <class T>
__device__ __forceinline__ void put(T& s, int i, u64 p) { s[i] = (unsigned)p; s[i + 1] = (unsigned)(p >> 32); }
template <class T>
__device__ __forceinline__ void put2(T& s, int i, unsigned lo, unsigned hi) { s[i] = UNI(lo); s[i + 1] = UNI(hi); }

__global__ __launch_bounds__(512, 2) void fused_block(const char* __restrict__ x, const char* __restrict__ wqkv,
                                                   const char* __restrict__ wo, const char* __restrict__ w1,
                                                   const char* __restrict__ b1, const char* __restrict__ w2,
                                                   const char* __restrict__ b2, char* __restrict__ out,
                                                   char* __restrict__ ws0) {
  u32x8 f;
  asm volatile(
      "s_load_dwordx4 s[4:7], s[0:1], 0x0\n\t"
      "v_readfirstlane_b32 s3, v0\n\t"
      "s_getpc_b64 s[40:41]\n\t"
      "s_lshr_b32 s12, s2, 3\n\t"
      "s_or_b32 s13, s3, s12\n\t"
      "s_and_b32 s13, s13, 0x1d0\n\t"
      "s_cmp_lg_u32 s13, 16\n\t"
      "s_cbranch_scc1 ET2_%=\n\t"
      "s_and_b32 s12, s12, 7\n\t"
      "s_mov_b32 m0, 163328\n\t"
      "v_lshl_add_u32 v3, s12, 6, v0\n\t"
      "v_min_u32_e32 v3, 463, v3\n\t"
      "v_lshlrev_b32_e32 v3, 7, v3\n\t"
      "global_load_lds_dword v3, s[40:41]\n\t"
      "ET2_%=:\n\t"
      "v_lshrrev_b32_e32 v1, 3, v0\n\t"
      "v_and_b32_e32 v2, 7, v0\n\t"
      "s_and_b32 s17, s2, 7\n\t"
      "s_lshr_b32 s18, s2, 3\n\t"
      "s_lshr_b32 s3, s3, 6\n\t"
      "s_cmpk_gt_u32 s2, 0x7f\n\t"
      "s_cbranch_scc1 EB_%=\n\t"
      "v_and_b32_e32 v3, 2, v1\n\t"
      "v_lshrrev_b32_e32 v4, 1, v1\n\t"
      "s_lshr_b32 s19, s18, 2\n\t"
      "s_and_b32 s12, s17, 6\n\t"
      "s_lshl1_add_u32 s19, s12, s19\n\t"
      "v_and_or_b32 v3, v4, 4, v3\n\t"
      "v_xor_b32_e32 v3, v3, v2\n\t"
      "v_min_u32_e32 v3, 6, v3\n\t"
      "v_mul_u32_u24_e32 v5, 2688, v1\n\t"
      "v_lshl_add_u32 v3, v3, 4, v5\n\t"
      "v_add_u32_e32 v195, 2048, v3\n\t"
      "v_add_u32_e32 v196, 1920, v3\n\t"
      "v_add_u32_e32 v197, 5888, v3\n\t"
      "s_mul_i32 s12, s19, 114688\n\t"
      "v_and_b32_e32 v5, 0x38, v1\n\t"
      "v_add_u32_e32 v5, v5, v1\n\t"
      "v_mul_u32_u24_e32 v5, 896, v5\n\t"
      "s_and_b32 s14, s17, 1\n\t"
      "s_add_u32 s14, s14, 3\n\t"
      "s_and_b32 s13, s18, 3\n\t"
      "s_xor_b32 s13, s13, s14\n\t"
      "s_mul_i32 s13, s13, 112\n\t"
      "v_and_b32_e32 v6, 3, v4\n\t"
      "v_xor_b32_e32 v6, v6, v2\n\t"
      "v_lshl_add_u32 v5, v6, 4, v5\n\t"
      "s_sub_u32 s12, s12, 15360\n\t"
      "s_ashr_i32 s14, s12, 31\n\t"
      "s_sub_u32 s13, s13, 4096\n\t"
      "v_add_u32_e32 v192, 15360, v5\n\t"
      "v_xor_b32_e32 v5, 64, v5\n\t"
      "v_add_u32_e32 v193, 21504, v5\n\t"
      "s_mul_i32 s9, s3, 5120\n\t"
      "s_sub_u32 s3, s3, 4\n\t"
      "s_cbranch_scc0 EH_%=\n\t"
      "s_waitcnt lgkmcnt(0)\n\t"
      "s_add_u32 s4, s4, s12\n\t"
      "s_addc_u32 s5, s5, s14\n\t"
      "s_add_u32 s6, s6, s13\n\t"
      "s_addc_u32 s7, s7, -1\n\t"
      "s_add_u32 m0, s9, 0\n\t"
      "s_nop 0\n\t"
      "global_load_lds_dwordx4 v195, s[6:7] offset:2048\n\t"
      "global_load_lds_dwordx4 v192, s[4:5]\n\t"
      "global_load_lds_dwordx4 v193, s[4:5] offset:1024\n\t"
      "global_load_lds_dwordx4 v196, s[6:7] offset:3072\n\t"
      "s_add_u32 m0, s9, 4096\n\t"
      "s_nop 0\n\t"
      "global_load_lds_dwordx4 v197, s[6:7]\n\t"
      "s_branch EX_%=\n\t"
      "EH_%=:\n\t"
      "s_cmp_eq_u32 s19, 0\n\t"
      "s_cselect_b32 s13, 16, 0\n\t"
      "s_lshl2_add_u32 s13, s3, s13\n\t"
      "v_and_or_b32 v3, v1, 3, s13\n\t"
      "v_bfe_u32 v4, v3, 1, 3\n\t"
      "v_mul_u32_u24_e32 v3, 896, v3\n\t"
      "v_xor_b32_e32 v4, v4, v2\n\t"
      "v_lshl_add_u32 v3, v4, 4, v3\n\t"
      "v_add_u32_e32 v194, 1024, v3\n\t"
      "s_waitcnt lgkmcnt(0)\n\t"
      "s_branch EX_%=\n\t"
      "EB_%=:\n\t"
      "v_and_b32_e32 v3, 63, v0\n\t"
      "v_lshlrev_b32_e32 v3, 4, v3\n\t"
      "s_sub_u32 s18, s18, 16\n\t"
      "s_min_u32 s16, s3, 6\n\t"
      "s_xor_b32 s15, s17, 1\n\t"
      "s_lshl4_add_u32 s15, s15, s18\n\t"
      "s_mul_i32 s15, s15, 14336\n\t"
      "s_lshl_b32 s12, s16, 7\n\t"
      "s_add_u32 s15, s15, s12\n\t"
      "v_mul_u32_u24_e32 v6, 56, v3\n\t"
      "v_add_u32_e32 v6, s15, v6\n\t"
      "s_add_u32 s19, s17, 0\n\t"
      "s_and_b32 s19, s19, 3\n\t"
      "s_and_b32 s12, s17, 4\n\t"
      "s_or_b32 s19, s19, s12\n\t"
      "s_lshl4_add_u32 s19, s19, s18\n\t"
      "s_lshl3_add_u32 s12, s19, s3\n\t"
      "s_lshl_b32 s12, s12, 11\n\t"
      "s_mul_i32 s19, s19, 14336\n\t"
      "s_lshl_b32 s16, s16, 11\n\t"
      "s_add_u32 s19, s19, s16\n\t"
      "s_add_u32 s8, s16, 114688\n\t"
      "v_add_u32_e32 v1, s19, v3\n\t"
      "s_lshl4_add_u32 s16, s3, s18\n\t"
      "v_bfe_u32 v4, v0, 2, 2\n\t"
      "v_mul_u32_u24_e32 v4, 896, v4\n\t"
      "v_and_b32_e32 v5, 48, v3\n\t"
      "v_lshl_add_u32 v4, v5, 3, v4\n\t"
      "s_and_b32 s13, s17, 1\n\t"
      "s_mul_i32 s13, s13, 384\n\t"
      "v_add_u32_e32 v4, s13, v4\n\t"
      "v_lshrrev_b32_e32 v2, 8, v3\n\t"
      "s_mul_i32 s13, s16, 1\n\t"
      "s_mul_i32 s14, s16, 3\n\t"
      "s_add_u32 s14, s14, 128\n\t"
      "v_add_u32_e32 v5, s13, v2\n\t"
      "v_add_u32_e32 v2, s14, v2\n\t"
      "v_min_u32_e32 v2, 447, v2\n\t"
      "s_movk_i32 s13, 2688\n\t"
      "v_mad_u32_u24 v5, v5, s13, v4\n\t"
      "v_mad_u32_u24 v4, v2, s13, v4\n\t"
      "s_mov_b32 m0, 163328\n\t"
      "s_waitcnt lgkmcnt(0)\n\t"
      "s_mov_b32 m0, s8\n\t"
      "s_nop 0\n\t"
      "global_load_lds_dwordx4 v1, s[4:5]\n\t"
      "global_load_lds_dwordx4 v1, s[4:5] offset:1024\n\t"
      "s_mov_b32 m0, 163328\n\t"
      "s_mov_b32 exec_lo, 0xfff\n\t"
      "s_mov_b32 exec_hi, 0x0\n\t"
      "global_load_lds_dword v5, s[6:7]\n\t"
      "s_mov_b64 exec, -1\n\t"
      "s_barrier\n\t"
      "s_mov_b32 exec_lo, 0xfff0fff\n\t"
      "s_mov_b32 exec_hi, 0xfff\n\t"
      "global_load_lds_dword v4, s[6:7]\n\t"
      "s_mov_b64 exec, -1\n\t"
      "s_mov_b64 exec, 0xffff\n\t"
      "global_load_lds_dword v6, s[4:5]\n\t"
      "s_mov_b64 exec, -1\n\t"
      "EX_%=:\n\t"
      : "={v[192:199]}"(f)
      : "{s[0:1]}"(__builtin_amdgcn_kernarg_segment_ptr()), "{s2}"(blockIdx.x), "{v0}"(threadIdx.x)
      : "memory", "scc", "s3", "s4", "s5", "s6", "s7", "s8", "s9", "s10", "s11", "s12", "s13", "s14", "s15", "s16", "s17", "s18", "s19", "v1", "v2",
        "v3", "v4", "v5", "v6", "s40", "s41", "s42", "s43", "s48", "s49", "s50"
      );
  PIN();
  char* const ws = ws0 + (-(unsigned)(uintptr_t)ws0 & ((2u << 20) - 1));
  const int wave = __builtin_amdgcn_readfirstlane(threadIdx.x >> 6);
  const int xcd = blockIdx.x & 7, idx = blockIdx.x >> 3, side = xcd >> 2, xp = xcd & 3;
#define LANE_BITS(l) \
  const int lane = (l), g = lane >> 4, c = lane & 15, q4 = c >> 2, p4 = lane & 3, lr = lane >> 3, lc = lane & 7

  if (__builtin_expect(idx < 16, 1)) {
    const int rt = M44 ? 2 * (xcd & 6) + (idx >> 2) : 2 * xcd + (idx >> 3), hp = M44 ? (idx & 3) ^ (3 + (xcd & 1)) : idx & 7, hh = (wave >> 1) & 1, half = wave & 1;
    const unsigned sta = A_ST + half * 1024;
    if (wave < 4) {
      LANE_BITS(threadIdx.x & 63);
      u32x16 v;
      u32x8 s;
      v[0] = 4 * half * A_UNIT + c * 128 + ((g ^ swz_row(c)) << 4);
      v[1] = A_HALO + c * 128 + ((g ^ swz_row(c)) << 4);
#pragma unroll
      for (int j = 0; j < 2; ++j) {
        const int by = 56 * hh + 32 * j + 8 * p4;
        v[2 + j] = g * A_UNIT + 2048 + q4 * 128 + (((by >> 4) ^ swz_tr(8 * g + q4)) << 4) + (by & 8);
      }
      const unsigned csa = A_CS + hh * 384, exa = A_EX + hh * 3 * 2048;
      v[4] = csa + g * 16;
      v[5] = csa + c * 4;
      v[6] = v[4] + 64 * half;
      v[7] = v[5] + 64 * half;
      v[8] = A_FL + (wave ^ 1) * 8;
      v[9] = sta + g * 16;
      v[10] = sta + c * 4;
      const int key = 4 * g + (c & 1);
      v[11] = sta + 384 + (key <= c ? 128 : 64) + key * 4;
      unsigned vk1 = sta + 384 + (key + 2 <= c ? 128 : 64) + key * 4 + 8;
      v[12] = exa + half * 2048 + lane * 16;
      v[13] = exa + half * 2048 + lane * 8;
      v[14] = exa + lane * 16 + 8 * half;
      v[15] = lane * 16;
      put(s, 0, (u64)(ws + WS_O + part(((8 * (rt & 7) + 4 * half) * 16 + 2 * hp + hh) * 1024, 0)));
      s[2] = half;
      s[3] = UNI(rt == 0 && half == 0 ? 0xff800000u : 0u);
      s[4] = exa + 1024 + 512 * half;
      s[5] = A_FL + 32 + 4 * half;
      put(s, 6, (u64)(ws + WS_ST));
      unsigned hint = ST_H2 + part(((((rt & 7) * 8 + hp) * 2 + half) * 2 + hh) * 64, side);
      unsigned chk = ST_CK + part((((rt & 7) * 2 + half) * 16 + 2 * hp + hh) * 256, side);
    asm volatile(
      "s_waitcnt lgkmcnt(0)\n\ts_nop 4\n\t"
      ".p2align 3\n\t"
      "v_xor_b32_e64 v248, 64, v224\n\t"
      "v_xor_b32_e32 v249, 64, v225\n\t"
      "s_mov_b32 s36, 0xaaaaaaaa\n\t"
      "s_mov_b32 s37, 0xaaaaaaaa\n\t"
      "s_mov_b32 s38, 0xfff0ffff\n\t"
      "s_mov_b32 s39, 0xf000ff00\n\t"
      "s_mov_b32 s40, 0xffc0fffc\n\t"
      "s_mov_b32 s41, 0xc000fc00\n\t"
      "s_mov_b32 s42, 0x0\n\t"
      "s_mov_b32 s43, 0xffff0000\n\t"
      "s_mov_b32 s44, 0x10001\n\t"
      "s_mov_b32 s45, 0x10001\n\t"
      "v_mov_b32_e32 v218, 0x0c0c0c0c\n\t"
      "v_mov_b32_e32 v216, 0xc0c0100\n\t"
      "v_mov_b32_e32 v217, 0x1000c0c\n\t"
      "v_cndmask_b32_e64 v216, v216, v217, s[36:37]\n\t"
      "v_cndmask_b32_e64 v240, v216, v218, s[38:39]\n\t"
      "v_cndmask_b32_e64 v242, v218, v216, s[38:39]\n\t"
      "v_mov_b32_e32 v216, 0xc0c0302\n\t"
      "v_mov_b32_e32 v217, 0x3020c0c\n\t"
      "v_cndmask_b32_e64 v216, v216, v217, s[36:37]\n\t"
      "v_cndmask_b32_e64 v241, v216, v218, s[40:41]\n\t"
      "v_cndmask_b32_e64 v243, v218, v216, s[40:41]\n\t"
      "v_xor_b32_e32 v213, 8, v232\n\t"
      "v_lshrrev_b32_e32 v214, 1, v239\n\t"
      "v_add_u32_e32 v214, s8, v214\n\t"
      "v_mov_b32_e32 v215, s9\n\t"
      "v_mov_b32_e32 v220, 154752\n\t"
      "v_lshrrev_b32_e32 v221, 2, v239\n\t"
      "s_add_u32 s56, s4, 32768\n\t"
      "s_addc_u32 s57, s5, 0\n\t"
      "s_add_u32 s58, s4, 65536\n\t"
      "s_addc_u32 s59, s5, 0\n\t"
      "s_add_u32 s60, s4, 98304\n\t"
      "s_addc_u32 s61, s5, 0\n\t"
      "s_and_b32 s52, s12, 0x2000\n\t"
      "v_mov_b32_e32 v244, s7\n\t"
      "v_mov_b32_e32 v245, s7\n\t"
      "v_mov_b32_e32 v246, s7\n\t"
      "v_mov_b32_e32 v247, s7\n\t"
      "v_mov_b64_e32 v[0:1], 0\n\t"
      "v_mov_b64_e32 v[2:3], 0\n\t"
      "v_mov_b64_e32 v[4:5], 0\n\t"
      "v_mov_b64_e32 v[6:7], 0\n\t"
      "v_mov_b64_e32 v[8:9], 0\n\t"
      "v_mov_b64_e32 v[10:11], 0\n\t"
      "v_mov_b64_e32 v[12:13], 0\n\t"
      "v_mov_b64_e32 v[14:15], 0\n\t"
      "v_mov_b64_e32 v[16:17], 0\n\t"
      "v_mov_b64_e32 v[18:19], 0\n\t"
      "v_mov_b64_e32 v[20:21], 0\n\t"
      "v_mov_b64_e32 v[22:23], 0\n\t"
      "v_mov_b64_e32 v[24:25], 0\n\t"
      "v_mov_b64_e32 v[26:27], 0\n\t"
      "v_mov_b64_e32 v[28:29], 0\n\t"
      "v_mov_b64_e32 v[30:31], 0\n\t"
      "v_mov_b64_e32 v[32:33], 0\n\t"
      "v_mov_b64_e32 v[34:35], 0\n\t"
      "v_mov_b64_e32 v[36:37], 0\n\t"
      "v_mov_b64_e32 v[38:39], 0\n\t"
      "v_mov_b64_e32 v[40:41], 0\n\t"
      "v_mov_b64_e32 v[42:43], 0\n\t"
      "v_mov_b64_e32 v[44:45], 0\n\t"
      "v_mov_b64_e32 v[46:47], 0\n\t"
      "v_mov_b64_e32 v[48:49], 0\n\t"
      "v_mov_b64_e32 v[50:51], 0\n\t"
      "v_mov_b64_e32 v[52:53], 0\n\t"
      "v_mov_b64_e32 v[54:55], 0\n\t"
      "v_mov_b64_e32 v[56:57], 0\n\t"
      "v_mov_b64_e32 v[58:59], 0\n\t"
      "v_mov_b64_e32 v[60:61], 0\n\t"
      "v_mov_b64_e32 v[62:63], 0\n\t"
      "v_mov_b64_e32 v[64:65], 0\n\t"
      "v_mov_b64_e32 v[66:67], 0\n\t"
      "v_mov_b64_e32 v[68:69], 0\n\t"
      "v_mov_b64_e32 v[70:71], 0\n\t"
      "v_mov_b64_e32 v[72:73], 0\n\t"
      "v_mov_b64_e32 v[74:75], 0\n\t"
      "v_mov_b64_e32 v[76:77], 0\n\t"
      "v_mov_b64_e32 v[78:79], 0\n\t"
      "v_mov_b64_e32 v[80:81], 0\n\t"
      "v_mov_b64_e32 v[82:83], 0\n\t"
      "v_mov_b64_e32 v[84:85], 0\n\t"
      "v_mov_b64_e32 v[86:87], 0\n\t"
      "v_mov_b64_e32 v[88:89], 0\n\t"
      "v_mov_b64_e32 v[90:91], 0\n\t"
      "v_mov_b64_e32 v[92:93], 0\n\t"
      "v_mov_b64_e32 v[94:95], 0\n\t"
      "v_mov_b64_e32 v[96:97], 0\n\t"
      "v_mov_b64_e32 v[98:99], 0\n\t"
      "v_mov_b64_e32 v[100:101], 0\n\t"
      "v_mov_b64_e32 v[102:103], 0\n\t"
      "v_mov_b64_e32 v[104:105], 0\n\t"
      "v_mov_b64_e32 v[106:107], 0\n\t"
      "s_mov_b32 s47, 6\n\t"
      "s_cmp_eq_u32 s6, 0\n\t"
      "s_cbranch_scc0 LOOP1_%=\n\t"
      "s_waitcnt vmcnt(0)\n\t"
      "s_barrier\n\t"
      "ds_read_b64_tr_b16 v[108:109], v226\n\t"
      "ds_read_b64_tr_b16 v[110:111], v226 offset:512\n\t"
      "ds_read_b128 v[132:135], v224\n\t"
      "ds_read_b128 v[136:139], v224 offset:5120\n\t"
      "ds_read_b128 v[140:143], v224 offset:10240\n\t"
      "ds_read_b128 v[144:147], v224 offset:15360\n\t"
      "ds_read_b64_tr_b16 v[112:113], v227\n\t"
      "ds_read_b64_tr_b16 v[114:115], v227 offset:512\n\t"
      "ds_read_b128 v[148:151], v225\n\t"
      "ds_read_b64_tr_b16 v[116:117], v226 offset:1024\n\t"
      "ds_read_b64_tr_b16 v[118:119], v226 offset:1536\n\t"
      "ds_read_b64_tr_b16 v[120:121], v227 offset:1024\n\t"
      "ds_read_b64_tr_b16 v[122:123], v227 offset:1536\n\t"
      "ds_read_b64_tr_b16 v[124:125], v226 offset:2048\n\t"
      "ds_read_b64_tr_b16 v[126:127], v226 offset:2560\n\t"
      "ds_read_b64_tr_b16 v[128:129], v227 offset:2048\n\t"
      "ds_read_b64_tr_b16 v[130:131], v227 offset:2560\n\t"
      "s_branch EVEN0_%=\n\t"
      "s_waitcnt lgkmcnt(0)\n\t"
      ".p2align 6\n\t"
      "ODD0_%=:\n\t"
      "s_waitcnt lgkmcnt(0)\n\t"
      "v_mfma_f32_16x16x32_bf16 v[0:3], v[152:155], v[176:179], v[0:3]\n\t"
      "v_add_u32_e32 v224, s46, v224\n\t"
      "v_add_u32_e32 v226, s46, v226\n\t"
      "v_mfma_f32_16x16x32_bf16 v[24:27], v[152:155], v[180:183], v[24:27]\n\t"
      "v_add_u32_e32 v227, s46, v227\n\t"
      "v_add_u32_e32 v225, s46, v225\n\t"
      "v_mfma_f32_16x16x32_bf16 v[48:51], v[152:155], v[184:187], v[48:51]\n\t"
      "s_barrier\n\t"
      "ds_read_b64_tr_b16 v[108:109], v226\n\t"
      "v_mfma_f32_16x16x32_bf16 v[72:75], v[152:155], v[188:191], v[72:75]\n\t"
      "ds_read_b64_tr_b16 v[110:111], v226 offset:512\n\t"
      "v_mfma_f32_16x16x32_bf16 v[4:7], v[156:159], v[176:179], v[4:7]\n\t"
      "ds_read_b128 v[132:135], v224\n\t"
      "v_mfma_f32_16x16x32_bf16 v[28:31], v[156:159], v[180:183], v[28:31]\n\t"
      "ds_read_b128 v[136:139], v224 offset:5120\n\t"
      "v_mfma_f32_16x16x32_bf16 v[52:55], v[156:159], v[184:187], v[52:55]\n\t"
      "ds_read_b128 v[140:143], v224 offset:10240\n\t"
      "v_mfma_f32_16x16x32_bf16 v[76:79], v[156:159], v[188:191], v[76:79]\n\t"
      "ds_read_b128 v[144:147], v224 offset:15360\n\t"
      "v_mfma_f32_16x16x32_bf16 v[96:99], v[152:155], v[192:195], v[96:99]\n\t"
      "v_mfma_f32_16x16x32_bf16 v[8:11], v[160:163], v[176:179], v[8:11]\n\t"
      "ds_read_b64_tr_b16 v[112:113], v227\n\t"
      "v_mfma_f32_16x16x32_bf16 v[32:35], v[160:163], v[180:183], v[32:35]\n\t"
      "ds_read_b64_tr_b16 v[114:115], v227 offset:512\n\t"
      "v_mfma_f32_16x16x32_bf16 v[56:59], v[160:163], v[184:187], v[56:59]\n\t"
      "ds_read_b128 v[148:151], v225\n\t"
      "v_mfma_f32_16x16x32_bf16 v[80:83], v[160:163], v[188:191], v[80:83]\n\t"
      "ds_read_b64_tr_b16 v[116:117], v226 offset:1024\n\t"
      "v_mfma_f32_16x16x32_bf16 v[12:15], v[164:167], v[176:179], v[12:15]\n\t"
      "ds_read_b64_tr_b16 v[118:119], v226 offset:1536\n\t"
      "v_mfma_f32_16x16x32_bf16 v[36:39], v[164:167], v[180:183], v[36:39]\n\t"
      "ds_read_b64_tr_b16 v[120:121], v227 offset:1024\n\t"
      "v_mfma_f32_16x16x32_bf16 v[60:63], v[164:167], v[184:187], v[60:63]\n\t"
      "v_mfma_f32_16x16x32_bf16 v[84:87], v[164:167], v[188:191], v[84:87]\n\t"
      "ds_read_b64_tr_b16 v[122:123], v227 offset:1536\n\t"
      "v_mfma_f32_16x16x32_bf16 v[100:103], v[160:163], v[192:195], v[100:103]\n\t"
      "ds_read_b64_tr_b16 v[124:125], v226 offset:2048\n\t"
      "v_mfma_f32_16x16x32_bf16 v[16:19], v[176:179], v[168:171], v[16:19]\n\t"
      "ds_read_b64_tr_b16 v[126:127], v226 offset:2560\n\t"
      "v_mfma_f32_16x16x32_bf16 v[40:43], v[180:183], v[168:171], v[40:43]\n\t"
      "ds_read_b64_tr_b16 v[128:129], v227 offset:2048\n\t"
      "v_mfma_f32_16x16x32_bf16 v[64:67], v[184:187], v[168:171], v[64:67]\n\t"
      "ds_read_b64_tr_b16 v[130:131], v227 offset:2560\n\t"
      "v_mfma_f32_16x16x32_bf16 v[88:91], v[188:191], v[168:171], v[88:91]\n\t"
      "v_mfma_f32_16x16x32_bf16 v[20:23], v[176:179], v[172:175], v[20:23]\n\t"
      "v_mfma_f32_16x16x32_bf16 v[44:47], v[180:183], v[172:175], v[44:47]\n\t"
      "v_add_u32_e32 v248, s46, v248\n\t"
      "v_add_u32_e32 v249, s46, v249\n\t"
      "v_mfma_f32_16x16x32_bf16 v[68:71], v[184:187], v[172:175], v[68:71]\n\t"
      "v_mfma_f32_16x16x32_bf16 v[92:95], v[188:191], v[172:175], v[92:95]\n\t"
      "v_mfma_f32_16x16x32_bf16 v[104:107], v[192:195], v[168:171], v[104:107]\n\t"
      "EVEN0_%=:\n\t"
      "s_waitcnt lgkmcnt(0)\n\t"
      "v_mfma_f32_16x16x32_bf16 v[0:3], v[108:111], v[132:135], v[0:3]\n\t"
      "ds_read_b64_tr_b16 v[152:153], v226 offset:20480\n\t"
      "v_mfma_f32_16x16x32_bf16 v[24:27], v[108:111], v[136:139], v[24:27]\n\t"
      "ds_read_b64_tr_b16 v[154:155], v226 offset:20992\n\t"
      "v_mfma_f32_16x16x32_bf16 v[48:51], v[108:111], v[140:143], v[48:51]\n\t"
      "ds_read_b128 v[176:179], v248\n\t"
      "v_mfma_f32_16x16x32_bf16 v[72:75], v[108:111], v[144:147], v[72:75]\n\t"
      "ds_read_b128 v[180:183], v248 offset:5120\n\t"
      "v_mfma_f32_16x16x32_bf16 v[4:7], v[112:115], v[132:135], v[4:7]\n\t"
      "v_mfma_f32_16x16x32_bf16 v[28:31], v[112:115], v[136:139], v[28:31]\n\t"
      "ds_read_b128 v[184:187], v248 offset:10240\n\t"
      "v_mfma_f32_16x16x32_bf16 v[52:55], v[112:115], v[140:143], v[52:55]\n\t"
      "ds_read_b128 v[188:191], v248 offset:15360\n\t"
      "v_mfma_f32_16x16x32_bf16 v[76:79], v[112:115], v[144:147], v[76:79]\n\t"
      "ds_read_b64_tr_b16 v[156:157], v227 offset:20480\n\t"
      "v_mfma_f32_16x16x32_bf16 v[96:99], v[108:111], v[148:151], v[96:99]\n\t"
      "v_mfma_f32_16x16x32_bf16 v[8:11], v[116:119], v[132:135], v[8:11]\n\t"
      "ds_read_b64_tr_b16 v[158:159], v227 offset:20992\n\t"
      "v_mfma_f32_16x16x32_bf16 v[32:35], v[116:119], v[136:139], v[32:35]\n\t"
      "ds_read_b128 v[192:195], v249\n\t"
      "v_mfma_f32_16x16x32_bf16 v[56:59], v[116:119], v[140:143], v[56:59]\n\t"
      "ds_read_b64_tr_b16 v[160:161], v226 offset:21504\n\t"
      "v_mfma_f32_16x16x32_bf16 v[80:83], v[116:119], v[144:147], v[80:83]\n\t"
      "ds_read_b64_tr_b16 v[162:163], v226 offset:22016\n\t"
      "v_mfma_f32_16x16x32_bf16 v[12:15], v[120:123], v[132:135], v[12:15]\n\t"
      "v_mfma_f32_16x16x32_bf16 v[36:39], v[120:123], v[136:139], v[36:39]\n\t"
      "ds_read_b64_tr_b16 v[164:165], v227 offset:21504\n\t"
      "v_mfma_f32_16x16x32_bf16 v[60:63], v[120:123], v[140:143], v[60:63]\n\t"
      "ds_read_b64_tr_b16 v[166:167], v227 offset:22016\n\t"
      "v_mfma_f32_16x16x32_bf16 v[84:87], v[120:123], v[144:147], v[84:87]\n\t"
      "ds_read_b64_tr_b16 v[168:169], v226 offset:22528\n\t"
      "v_mfma_f32_16x16x32_bf16 v[100:103], v[116:119], v[148:151], v[100:103]\n\t"
      "v_mfma_f32_16x16x32_bf16 v[16:19], v[132:135], v[124:127], v[16:19]\n\t"
      "ds_read_b64_tr_b16 v[170:171], v226 offset:23040\n\t"
      "v_mfma_f32_16x16x32_bf16 v[40:43], v[136:139], v[124:127], v[40:43]\n\t"
      "ds_read_b64_tr_b16 v[172:173], v227 offset:22528\n\t"
      "v_mfma_f32_16x16x32_bf16 v[64:67], v[140:143], v[124:127], v[64:67]\n\t"
      "ds_read_b64_tr_b16 v[174:175], v227 offset:23040\n\t"
      "s_cmp_eq_u32 s47, 4\n\t"
      "s_cselect_b32 s46, -2, 1\n\t"
      "v_mfma_f32_16x16x32_bf16 v[88:91], v[144:147], v[124:127], v[88:91]\n\t"
      "s_cmp_eq_u32 s47, 1\n\t"
      "s_cselect_b32 s46, -2, s46\n\t"
      "v_mfma_f32_16x16x32_bf16 v[20:23], v[132:135], v[128:131], v[20:23]\n\t"
      "s_mul_i32 s46, s46, 43008\n\t"
      "v_mfma_f32_16x16x32_bf16 v[44:47], v[136:139], v[128:131], v[44:47]\n\t"
      "v_mfma_f32_16x16x32_bf16 v[68:71], v[140:143], v[128:131], v[68:71]\n\t"
      "v_mfma_f32_16x16x32_bf16 v[92:95], v[144:147], v[128:131], v[92:95]\n\t"
      "s_sub_u32 s47, s47, 1\n\t"
      "v_mfma_f32_16x16x32_bf16 v[104:107], v[148:151], v[124:127], v[104:107]\n\t"
      "s_cbranch_scc0 ODD0_%=\n\t"
      "s_waitcnt lgkmcnt(0)\n\t"
      "v_mfma_f32_16x16x32_bf16 v[96:99], v[152:155], v[192:195], v[96:99]\n\t"
      "ds_read_b32 v212, v215\n\t"
      "v_mfma_f32_16x16x32_bf16 v[100:103], v[160:163], v[192:195], v[100:103]\n\t"
      "ds_read2_b32 v[134:135], v234 offset0:80 offset1:80\n\t"
      "v_mfma_f32_16x16x32_bf16 v[104:107], v[192:195], v[168:171], v[104:107]\n\t"
      "ds_read_b128 v[148:151], v233 offset:320\n\t"
      "v_mfma_f32_16x16x32_bf16 v[80:83], v[160:163], v[188:191], v[80:83]\n\t"
      "ds_read2_b32 v[248:249], v234 offset0:0 offset1:0\n\t"
      "v_mfma_f32_16x16x32_bf16 v[84:87], v[164:167], v[188:191], v[84:87]\n\t"
      "ds_read_b128 v[196:199], v233\n\t"
      "v_mfma_f32_16x16x32_bf16 v[88:91], v[188:191], v[168:171], v[88:91]\n\t"
      "ds_read2_b32 v[132:133], v234 offset0:64 offset1:64\n\t"
      "v_mfma_f32_16x16x32_bf16 v[92:95], v[188:191], v[172:175], v[92:95]\n\t"
      "s_mov_b64 exec, s[44:45]\n\t"
      "ds_write_b128 v230, v[96:99]\n\t"
      "ds_write_b128 v230, v[100:103] offset:128\n\t"
      "s_mov_b64 exec, 0xffff\n\t"
      "ds_write_b32 v231, v104 offset:256\n\t"
      "ds_write_b32 v213, v213\n\t"
      "s_mov_b64 exec, -1\n\t"
      "v_mfma_f32_16x16x32_bf16 v[56:59], v[160:163], v[184:187], v[56:59]\n\t"
      "ds_read2_b32 v[130:131], v234 offset0:48 offset1:48\n\t"
      "v_mfma_f32_16x16x32_bf16 v[60:63], v[164:167], v[184:187], v[60:63]\n\t"
      "ds_read2_b32 v[128:129], v234 offset0:32 offset1:32\n\t"
      "v_mfma_f32_16x16x32_bf16 v[64:67], v[184:187], v[168:171], v[64:67]\n\t"
      "s_waitcnt lgkmcnt(0)\n\t"
      "v_mfma_f32_16x16x32_bf16 v[68:71], v[184:187], v[172:175], v[68:71]\n\t"
      "ds_read_b32 v226, v232\n\t"
      "ds_read_b128 v[116:119], v228 offset:128\n\t"
      "v_mfma_f32_16x16x32_bf16 v[32:35], v[160:163], v[180:183], v[32:35]\n\t"
      "ds_read_b128 v[120:123], v228 offset:192\n\t"
      "v_mfma_f32_16x16x32_bf16 v[36:39], v[164:167], v[180:183], v[36:39]\n\t"
      "ds_read2_b32 v[250:251], v229 offset0:64 offset1:64\n\t"
      "v_mfma_f32_16x16x32_bf16 v[40:43], v[180:183], v[168:171], v[40:43]\n\t"
      "ds_read2_b32 v[252:253], v229 offset0:80 offset1:80\n\t"
      "v_mfma_f32_16x16x32_bf16 v[44:47], v[180:183], v[172:175], v[44:47]\n\t"
      "ds_read_b128 v[124:127], v230 offset:128\n\t"
      "v_mfma_f32_16x16x32_bf16 v[8:11], v[160:163], v[176:179], v[8:11]\n\t"
      "ds_read2_b32 v[224:225], v231 offset0:64 offset1:64\n\t"
      "v_mfma_f32_16x16x32_bf16 v[12:15], v[164:167], v[176:179], v[12:15]\n\t"
      "v_mfma_f32_16x16x32_bf16 v[16:19], v[176:179], v[168:171], v[16:19]\n\t"
      "s_waitcnt lgkmcnt(6)\n\t"
      "v_min_u32_e32 v226, v226, v212\n\t"
      "v_cmp_eq_u32_e32 vcc, 0, v226\n\t"
      "v_mfma_f32_16x16x32_bf16 v[20:23], v[176:179], v[172:175], v[20:23]\n\t"
      "LOOPX_%=:\n\t"
      "s_cbranch_vccnz XAR_%=\n\t"
      "XAB_%=:\n\t"
      "v_mfma_f32_16x16x32_bf16 v[72:75], v[152:155], v[188:191], v[72:75]\n\t"
      "s_waitcnt lgkmcnt(4)\n\t"
      "v_fmac_f32_e32 v80, v134, v116\n\t"
      "v_fmac_f32_e32 v81, v134, v117\n\t"
      "v_mfma_f32_16x16x32_bf16 v[76:79], v[156:159], v[188:191], v[76:79]\n\t"
      "v_fmac_f32_e32 v82, v134, v118\n\t"
      "v_fmac_f32_e32 v83, v134, v119\n\t"
      "v_mfma_f32_16x16x32_bf16 v[48:51], v[152:155], v[184:187], v[48:51]\n\t"
      "v_fmac_f32_e32 v84, v134, v120\n\t"
      "v_fmac_f32_e32 v85, v134, v121\n\t"
      "v_mfma_f32_16x16x32_bf16 v[52:55], v[156:159], v[184:187], v[52:55]\n\t"
      "v_fmac_f32_e32 v86, v134, v122\n\t"
      "v_fmac_f32_e32 v87, v134, v123\n\t"
      "v_mfma_f32_16x16x32_bf16 v[24:27], v[152:155], v[180:183], v[24:27]\n\t"
      "v_cvt_pk_bf16_f32 v80, v80, v81\n\t"
      "v_cvt_pk_bf16_f32 v81, v82, v83\n\t"
      "v_mfma_f32_16x16x32_bf16 v[28:31], v[156:159], v[180:183], v[28:31]\n\t"
      "v_cvt_pk_bf16_f32 v82, v84, v85\n\t"
      "v_cvt_pk_bf16_f32 v83, v86, v87\n\t"
      "v_mfma_f32_16x16x32_bf16 v[0:3], v[152:155], v[176:179], v[0:3]\n\t"
      "s_waitcnt lgkmcnt(2)\n\t"
      "v_fmac_f32_e32 v88, v148, v250\n\t"
      "v_fmac_f32_e32 v89, v149, v250\n\t"
      "v_mfma_f32_16x16x32_bf16 v[4:7], v[156:159], v[176:179], v[4:7]\n\t"
      "v_fmac_f32_e32 v90, v150, v250\n\t"
      "v_fmac_f32_e32 v91, v151, v250\n\t"
      "ds_write_b128 v236, v[80:83] offset:2048\n\t"
      "v_fmac_f32_e32 v92, v148, v252\n\t"
      "v_fmac_f32_e32 v93, v149, v252\n\t"
      "v_fmac_f32_e32 v94, v150, v252\n\t"
      "v_fmac_f32_e32 v95, v151, v252\n\t"
      "v_cvt_pk_bf16_f32 v164, v88, v89\n\t"
      "v_cvt_pk_bf16_f32 v165, v90, v91\n\t"
      "v_cvt_pk_bf16_f32 v174, v92, v93\n\t"
      "v_cvt_pk_bf16_f32 v175, v94, v95\n\t"
      "ds_read_b128 v[108:111], v228\n\t"
      "ds_read_b128 v[112:115], v228 offset:64\n\t"
      "s_waitcnt lgkmcnt(3)\n\t"
      "v_pk_fma_f32 v[100:101], v[248:249], v[124:125], v[100:101]\n\t"
      "v_pk_fma_f32 v[104:105], v[196:197], v[224:225], v[104:105]\n\t"
      "v_pk_fma_f32 v[102:103], v[248:249], v[126:127], v[102:103]\n\t"
      "v_pk_fma_f32 v[106:107], v[198:199], v[224:225], v[106:107]\n\t"
      "ds_write_b64 v237, v[164:165] offset:3072\n\t"
      "ds_write_b64 v237, v[174:175] offset:3584\n\t"
      "v_cvt_pk_bf16_f32 v100, v100, v101\n\t"
      "v_cvt_pk_bf16_f32 v101, v102, v103\n\t"
      "v_cvt_pk_bf16_f32 v102, v104, v105\n\t"
      "v_cvt_pk_bf16_f32 v103, v106, v107\n\t"
      "v_pk_fma_f32 v[56:57], v[132:133], v[116:117], v[56:57]\n\t"
      "v_pk_fma_f32 v[58:59], v[132:133], v[118:119], v[58:59]\n\t"
      "v_pk_fma_f32 v[60:61], v[132:133], v[120:121], v[60:61]\n\t"
      "v_pk_fma_f32 v[62:63], v[132:133], v[122:123], v[62:63]\n\t"
      "ds_write_b64 v238, v[100:101]\n\t"
      "ds_write_b64 v214, v[102:103]\n\t"
      "ds_write_b32 v213, v213 offset:4\n\t"
      "v_cvt_pk_bf16_f32 v56, v56, v57\n\t"
      "v_cvt_pk_bf16_f32 v57, v58, v59\n\t"
      "v_cvt_pk_bf16_f32 v58, v60, v61\n\t"
      "v_cvt_pk_bf16_f32 v59, v62, v63\n\t"
      "ds_read_b128 v[144:147], v233 offset:256\n\t"
      "ds_read_b128 v[140:143], v233 offset:192\n\t"
      "ds_read_b128 v[136:139], v233 offset:128\n\t"
      "v_pk_fma_f32 v[32:33], v[130:131], v[116:117], v[32:33]\n\t"
      "v_pk_fma_f32 v[34:35], v[130:131], v[118:119], v[34:35]\n\t"
      "v_pk_fma_f32 v[36:37], v[130:131], v[120:121], v[36:37]\n\t"
      "v_pk_fma_f32 v[38:39], v[130:131], v[122:123], v[38:39]\n\t"
      "v_pk_fma_f32 v[8:9], v[128:129], v[116:117], v[8:9]\n\t"
      "v_pk_fma_f32 v[10:11], v[128:129], v[118:119], v[10:11]\n\t"
      "v_pk_fma_f32 v[12:13], v[128:129], v[120:121], v[12:13]\n\t"
      "v_pk_fma_f32 v[14:15], v[128:129], v[122:123], v[14:15]\n\t"
      "s_waitcnt lgkmcnt(8)\n\t"
      "s_nop 0\n\t"
      "v_pk_fma_f32 v[72:73], v[134:135], v[108:109], v[72:73]\n\t"
      "v_pk_fma_f32 v[74:75], v[134:135], v[110:111], v[74:75]\n\t"
      "v_pk_fma_f32 v[76:77], v[134:135], v[112:113], v[76:77]\n\t"
      "v_pk_fma_f32 v[78:79], v[134:135], v[114:115], v[78:79]\n\t"
      "v_pk_fma_f32 v[48:49], v[132:133], v[108:109], v[48:49]\n\t"
      "v_pk_fma_f32 v[50:51], v[132:133], v[110:111], v[50:51]\n\t"
      "v_pk_fma_f32 v[52:53], v[132:133], v[112:113], v[52:53]\n\t"
      "v_pk_fma_f32 v[54:55], v[132:133], v[114:115], v[54:55]\n\t"
      "s_waitcnt lgkmcnt(3)\n\t"
      "ds_read2_b32 v[204:205], v235 offset0:0 offset1:16\n\t"
      "ds_read2_b32 v[206:207], v235 offset0:32 offset1:48\n\t"
      "v_pk_fma_f32 v[24:25], v[130:131], v[108:109], v[24:25]\n\t"
      "v_pk_fma_f32 v[26:27], v[130:131], v[110:111], v[26:27]\n\t"
      "v_pk_fma_f32 v[28:29], v[130:131], v[112:113], v[28:29]\n\t"
      "v_pk_fma_f32 v[30:31], v[130:131], v[114:115], v[30:31]\n\t"
      "ds_read2_b32 v[208:209], v254 offset0:0 offset1:16\n\t"
      "ds_read2_b32 v[210:211], v254 offset0:32 offset1:48\n\t"
      "v_pk_fma_f32 v[0:1], v[128:129], v[108:109], v[0:1]\n\t"
      "v_pk_fma_f32 v[2:3], v[128:129], v[110:111], v[2:3]\n\t"
      "v_pk_fma_f32 v[4:5], v[128:129], v[112:113], v[4:5]\n\t"
      "v_pk_fma_f32 v[6:7], v[128:129], v[114:115], v[6:7]\n\t"
      "ds_read2_b32 v[200:201], v234 offset0:192 offset1:208\n\t"
      "ds_read2_b32 v[202:203], v234 offset0:224 offset1:240\n\t"
      "ds_read_b32 v226, v232 offset:4\n\t"
      "ds_read_b128 v[152:155], v236\n\t"
      "ds_read_b64 v[156:157], v237 offset:1024\n\t"
      "ds_read_b64 v[166:167], v237 offset:1536\n\t"
      "s_waitcnt lgkmcnt(12)\n\t"
      "v_pk_fma_f32 v[64:65], v[144:145], v[250:251], v[64:65]\n\t"
      "v_pk_fma_f32 v[66:67], v[146:147], v[250:251], v[66:67]\n\t"
      "v_pk_fma_f32 v[68:69], v[144:145], v[252:253], v[68:69]\n\t"
      "v_pk_fma_f32 v[70:71], v[146:147], v[252:253], v[70:71]\n\t"
      "s_waitcnt lgkmcnt(11)\n\t"
      "v_pk_fma_f32 v[40:41], v[140:141], v[250:251], v[40:41]\n\t"
      "v_pk_fma_f32 v[42:43], v[142:143], v[250:251], v[42:43]\n\t"
      "v_pk_fma_f32 v[44:45], v[140:141], v[252:253], v[44:45]\n\t"
      "v_pk_fma_f32 v[46:47], v[142:143], v[252:253], v[46:47]\n\t"
      "s_waitcnt lgkmcnt(10)\n\t"
      "v_pk_fma_f32 v[16:17], v[136:137], v[250:251], v[16:17]\n\t"
      "v_pk_fma_f32 v[18:19], v[138:139], v[250:251], v[18:19]\n\t"
      "v_pk_fma_f32 v[20:21], v[136:137], v[252:253], v[20:21]\n\t"
      "v_pk_fma_f32 v[22:23], v[138:139], v[252:253], v[22:23]\n\t"
      "ds_read_b64 v[250:251], v220 offset:8848\n\t"
      "ds_read_b64 v[196:197], v220\n\t"
      "v_cvt_pk_bf16_f32 v72, v72, v73\n\t"
      "v_cvt_pk_bf16_f32 v73, v74, v75\n\t"
      "v_cvt_pk_bf16_f32 v74, v76, v77\n\t"
      "v_cvt_pk_bf16_f32 v75, v78, v79\n\t"
      "v_cndmask_b32_e64 v74, v74, 0, s[42:43]\n\t"
      "v_cndmask_b32_e64 v75, v75, 0, s[42:43]\n\t"
      "v_cvt_pk_bf16_f32 v48, v48, v49\n\t"
      "v_cvt_pk_bf16_f32 v49, v50, v51\n\t"
      "v_cvt_pk_bf16_f32 v50, v52, v53\n\t"
      "v_cvt_pk_bf16_f32 v51, v54, v55\n\t"
      "v_cndmask_b32_e64 v50, v50, 0, s[42:43]\n\t"
      "v_cndmask_b32_e64 v51, v51, 0, s[42:43]\n\t"
      "v_cvt_pk_bf16_f32 v32, v32, v33\n\t"
      "v_cvt_pk_bf16_f32 v33, v34, v35\n\t"
      "v_cvt_pk_bf16_f32 v34, v36, v37\n\t"
      "v_cvt_pk_bf16_f32 v35, v38, v39\n\t"
      "v_cvt_pk_bf16_f32 v8, v8, v9\n\t"
      "v_cvt_pk_bf16_f32 v9, v10, v11\n\t"
      "v_cvt_pk_bf16_f32 v10, v12, v13\n\t"
      "v_cvt_pk_bf16_f32 v11, v14, v15\n\t"
      "v_mfma_f32_16x16x32_bf16 v[76:79], v[56:59], v[72:75], 0\n\t"
      "v_cvt_pk_bf16_f32 v24, v24, v25\n\t"
      "v_cvt_pk_bf16_f32 v25, v26, v27\n\t"
      "v_mfma_f32_16x16x32_bf16 v[84:87], v[80:83], v[72:75], 0\n\t"
      "v_cvt_pk_bf16_f32 v26, v28, v29\n\t"
      "v_cvt_pk_bf16_f32 v27, v30, v31\n\t"
      "v_mfma_f32_16x16x32_bf16 v[52:55], v[32:35], v[48:51], 0\n\t"
      "v_cndmask_b32_e64 v26, v26, 0, s[42:43]\n\t"
      "v_cndmask_b32_e64 v27, v27, 0, s[42:43]\n\t"
      "v_mfma_f32_16x16x32_bf16 v[60:63], v[56:59], v[48:51], 0\n\t"
      "s_waitcnt lgkmcnt(5)\n\t"
      "v_cmp_eq_u32_e32 vcc, 0, v226\n\t"
      "v_cvt_pk_bf16_f32 v0, v0, v1\n\t"
      "v_mfma_f32_16x16x32_bf16 v[28:31], v[8:11], v[24:27], 0\n\t"
      "v_cvt_pk_bf16_f32 v1, v2, v3\n\t"
      "v_cvt_pk_bf16_f32 v2, v4, v5\n\t"
      "v_mfma_f32_16x16x32_bf16 v[36:39], v[32:35], v[24:27], 0\n\t"
      "v_cvt_pk_bf16_f32 v3, v6, v7\n\t"
      "v_cndmask_b32_e64 v2, v2, 0, s[42:43]\n\t"
      "v_cndmask_b32_e64 v3, v3, 0, s[42:43]\n\t"
      "s_cbranch_vccnz XBR_%=\n\t"
      "XBB_%=:\n\t"
      "v_cvt_pk_bf16_f32 v162, v64, v65\n\t"
      "s_waitcnt lgkmcnt(4)\n\t"
      "v_mfma_f32_16x16x32_bf16 v[4:7], v[152:155], v[0:3], v[244:247]\n\t"
      "v_cvt_pk_bf16_f32 v163, v66, v67\n\t"
      "v_cvt_pk_bf16_f32 v172, v68, v69\n\t"
      "v_mfma_f32_16x16x32_bf16 v[12:15], v[8:11], v[0:3], 0\n\t"
      "v_cvt_pk_bf16_f32 v173, v70, v71\n\t"
      "v_cvt_pk_bf16_f32 v160, v40, v41\n\t"
      "v_cndmask_b32_e64 v76, v76, v77, s[36:37]\n\t"
      "v_cndmask_b32_e64 v84, v84, v85, s[36:37]\n\t"
      "v_cndmask_b32_e64 v78, v78, v79, s[36:37]\n\t"
      "v_cndmask_b32_e64 v86, v86, v87, s[36:37]\n\t"
      "v_cndmask_b32_e64 v179, v76, v84, s[38:39]\n\t"
      "v_cndmask_b32_e64 v183, v78, v86, s[40:41]\n\t"
      "v_cndmask_b32_e64 v52, v52, v53, s[36:37]\n\t"
      "v_cndmask_b32_e64 v60, v60, v61, s[36:37]\n\t"
      "v_cndmask_b32_e64 v54, v54, v55, s[36:37]\n\t"
      "v_cndmask_b32_e64 v62, v62, v63, s[36:37]\n\t"
      "v_cndmask_b32_e64 v178, v52, v60, s[38:39]\n\t"
      "v_cndmask_b32_e64 v182, v54, v62, s[40:41]\n\t"
      "v_cndmask_b32_e64 v28, v28, v29, s[36:37]\n\t"
      "v_cndmask_b32_e64 v36, v36, v37, s[36:37]\n\t"
      "v_cndmask_b32_e64 v30, v30, v31, s[36:37]\n\t"
      "v_cndmask_b32_e64 v38, v38, v39, s[36:37]\n\t"
      "v_cndmask_b32_e64 v177, v28, v36, s[38:39]\n\t"
      "v_cndmask_b32_e64 v181, v30, v38, s[40:41]\n\t"
      "v_cndmask_b32_e64 v4, v4, v5, s[36:37]\n\t"
      "v_cndmask_b32_e64 v12, v12, v13, s[36:37]\n\t"
      "v_cndmask_b32_e64 v6, v6, v7, s[36:37]\n\t"
      "v_cndmask_b32_e64 v14, v14, v15, s[36:37]\n\t"
      "v_cndmask_b32_e64 v176, v4, v12, s[38:39]\n\t"
      "v_cndmask_b32_e64 v180, v6, v14, s[40:41]\n\t"
      "v_pk_mul_f32 v[178:179], v[178:179], v[206:207]\n\t"
      "v_pk_mul_f32 v[182:183], v[182:183], v[210:211]\n\t"
      "v_pk_mul_f32 v[176:177], v[176:177], v[204:205]\n\t"
      "v_pk_mul_f32 v[180:181], v[180:181], v[208:209]\n\t"
      "v_max_f32_e32 v219, v179, v183\n\t"
      "v_max_f32_e32 v217, v177, v181\n\t"
      "v_max_f32_e32 v218, v178, v182\n\t"
      "v_max_f32_e32 v216, v176, v180\n\t"
      "v_cvt_pk_bf16_f32 v161, v42, v43\n\t"
      "v_permlane32_swap_b32_e32 v217, v219\n\t"
      "v_permlane32_swap_b32_e32 v216, v218\n\t"
      "v_max_f32_e32 v217, v217, v219\n\t"
      "v_max_f32_e32 v216, v216, v218\n\t"
      "v_cvt_pk_bf16_f32 v170, v44, v45\n\t"
      "v_cvt_pk_bf16_f32 v171, v46, v47\n\t"
      "v_permlane16_swap_b32_e32 v216, v217\n\t"
      "v_max_f32_e32 v216, v216, v217\n\t"
      "v_mov_b32_e32 v217, v216\n\t"
      "v_cvt_pk_bf16_f32 v158, v16, v17\n\t"
      "s_waitcnt lgkmcnt(0)\n\t"
      "v_xad_u32 v252, s13, v197, v221\n\t"
      "v_permlane16_swap_b32_e32 v216, v217\n\t"
      "v_mov_b32_e32 v218, v216\n\t"
      "v_mov_b32_e64 v219, v217\n\t"
      "v_xad_u32 v196, s52, v196, v239\n\t"
      "v_permlane32_swap_b32_e32 v216, v218\n\t"
      "v_permlane32_swap_b32_e32 v217, v219\n\t"
      "v_pk_add_f32 v[178:179], v[178:179], v[218:219] neg_lo:[0,1] neg_hi:[0,1]\n\t"
      "v_pk_add_f32 v[182:183], v[182:183], v[218:219] neg_lo:[0,1] neg_hi:[0,1]\n\t"
      "v_pk_add_f32 v[176:177], v[176:177], v[216:217] neg_lo:[0,1] neg_hi:[0,1]\n\t"
      "v_pk_add_f32 v[180:181], v[180:181], v[216:217] neg_lo:[0,1] neg_hi:[0,1]\n\t"
      "v_pk_mul_f32 v[178:179], v[178:179], v[202:203]\n\t"
      "v_pk_mul_f32 v[182:183], v[182:183], v[202:203]\n\t"
      "v_pk_mul_f32 v[176:177], v[176:177], v[200:201]\n\t"
      "v_pk_mul_f32 v[180:181], v[180:181], v[200:201]\n\t"
      "v_exp_f32_e32 v179, v179\n\t"
      "v_exp_f32_e32 v183, v183\n\t"
      "v_exp_f32_e32 v178, v178\n\t"
      "v_exp_f32_e32 v182, v182\n\t"
      "v_exp_f32_e32 v177, v177\n\t"
      "v_exp_f32_e32 v181, v181\n\t"
      "v_exp_f32_e32 v176, v176\n\t"
      "v_exp_f32_e32 v180, v180\n\t"
      "v_xor_b32_e64 v253, s12, v197\n\t"
      "s_mov_b64 exec, 1\n\t"
      "global_store_dwordx2 v253, v[250:251], s[10:11] sc1\n\t"
      "s_mov_b64 exec, -1\n\t"
      "v_pk_add_f32 v[218:219], v[178:179], v[182:183]\n\t"
      "v_pk_add_f32 v[216:217], v[176:177], v[180:181]\n\t"
      "v_cvt_pk_bf16_f32 v159, v18, v19\n\t"
      "v_cvt_pk_bf16_f32 v168, v20, v21\n\t"
      "v_permlane32_swap_b32_e32 v217, v219\n\t"
      "v_permlane32_swap_b32_e32 v216, v218\n\t"
      "v_add_f32_e32 v217, v217, v219\n\t"
      "v_add_f32_e32 v216, v216, v218\n\t"
      "v_pk_mul_f32 v[178:179], v[178:179], v[206:207]\n\t"
      "v_pk_mul_f32 v[182:183], v[182:183], v[210:211]\n\t"
      "v_permlane16_swap_b32_e32 v216, v217\n\t"
      "v_add_f32_e32 v216, v216, v217\n\t"
      "v_rcp_f32_e32 v216, v216\n\t"
      "v_pk_mul_f32 v[176:177], v[176:177], v[204:205]\n\t"
      "v_mov_b32_e32 v217, v216\n\t"
      "v_pk_mul_f32 v[180:181], v[180:181], v[208:209]\n\t"
      "v_cvt_pk_bf16_f32 v169, v22, v23\n\t"
      "v_permlane16_swap_b32_e32 v216, v217\n\t"
      "v_mov_b32_e32 v218, v216\n\t"
      "v_mov_b32_e32 v219, v217\n\t"
      "s_nop 0\n\t"
      "v_permlane32_swap_b32_e32 v216, v218\n\t"
      "v_permlane32_swap_b32_e32 v217, v219\n\t"
      "v_pk_mul_f32 v[178:179], v[178:179], v[218:219]\n\t"
      "v_pk_mul_f32 v[182:183], v[182:183], v[218:219]\n\t"
      "v_pk_mul_f32 v[176:177], v[176:177], v[216:217]\n\t"
      "v_pk_mul_f32 v[180:181], v[180:181], v[216:217]\n\t"
      "v_cvt_pk_bf16_f32 v179, v179, v183\n\t"
      "v_cvt_pk_bf16_f32 v178, v178, v182\n\t"
      "v_cvt_pk_bf16_f32 v177, v177, v181\n\t"
      "v_cvt_pk_bf16_f32 v176, v176, v180\n\t"
      "v_perm_b32 v72, v179, v179, v240\n\t"
      "v_perm_b32 v73, v179, v179, v241\n\t"
      "v_perm_b32 v74, v179, v179, v242\n\t"
      "v_perm_b32 v75, v179, v179, v243\n\t"
      "v_perm_b32 v48, v178, v178, v240\n\t"
      "v_perm_b32 v49, v178, v178, v241\n\t"
      "v_mfma_f32_16x16x32_bf16 v[88:91], v[162:165], v[72:75], 0\n\t"
      "v_perm_b32 v50, v178, v178, v242\n\t"
      "v_perm_b32 v51, v178, v178, v243\n\t"
      "v_mfma_f32_16x16x32_bf16 v[92:95], v[172:175], v[72:75], 0\n\t"
      "v_perm_b32 v24, v177, v177, v240\n\t"
      "v_perm_b32 v25, v177, v177, v241\n\t"
      "v_mfma_f32_16x16x32_bf16 v[64:67], v[160:163], v[48:51], 0\n\t"
      "v_perm_b32 v26, v177, v177, v242\n\t"
      "v_perm_b32 v27, v177, v177, v243\n\t"
      "v_mfma_f32_16x16x32_bf16 v[68:71], v[170:173], v[48:51], 0\n\t"
      "v_perm_b32 v0, v176, v176, v240\n\t"
      "v_perm_b32 v1, v176, v176, v241\n\t"
      "v_mfma_f32_16x16x32_bf16 v[40:43], v[158:161], v[24:27], 0\n\t"
      "v_perm_b32 v2, v176, v176, v242\n\t"
      "v_perm_b32 v3, v176, v176, v243\n\t"
      "v_mfma_f32_16x16x32_bf16 v[44:47], v[168:171], v[24:27], 0\n\t"
      "s_nop 0\n\t"
      "v_mfma_f32_16x16x32_bf16 v[16:19], v[156:159], v[0:3], 0\n\t"
      "v_cvt_pk_bf16_f32 v80, v88, v89\n\t"
      "v_cvt_pk_bf16_f32 v81, v90, v91\n\t"
      "v_mfma_f32_16x16x32_bf16 v[20:23], v[166:169], v[0:3], 0\n\t"
      "v_cvt_pk_bf16_f32 v82, v92, v93\n\t"
      "v_cvt_pk_bf16_f32 v83, v94, v95\n\t"
      "global_store_dwordx4 v196, v[80:83], s[60:61] sc1\n\t"
      "v_add3_u32 v219, v80, v81, v82\n\t"
      "v_add_u32_e32 v219, v219, v83\n\t"
      "v_cvt_pk_bf16_f32 v56, v64, v65\n\t"
      "v_cvt_pk_bf16_f32 v57, v66, v67\n\t"
      "v_cvt_pk_bf16_f32 v58, v68, v69\n\t"
      "v_cvt_pk_bf16_f32 v59, v70, v71\n\t"
      "global_store_dwordx4 v196, v[56:59], s[58:59] sc1\n\t"
      "v_add3_u32 v218, v56, v57, v58\n\t"
      "v_add_u32_e64 v218, v218, v59\n\t"
      "v_cvt_pk_bf16_f32 v32, v40, v41\n\t"
      "v_cvt_pk_bf16_f32 v33, v42, v43\n\t"
      "v_cvt_pk_bf16_f32 v34, v44, v45\n\t"
      "v_cvt_pk_bf16_f32 v35, v46, v47\n\t"
      "global_store_dwordx4 v196, v[32:35], s[56:57] sc1\n\t"
      "v_add3_u32 v217, v32, v33, v34\n\t"
      "v_add_u32_e64 v217, v217, v35\n\t"
      "v_cvt_pk_bf16_f32 v8, v16, v17\n\t"
      "v_cvt_pk_bf16_f32 v9, v18, v19\n\t"
      "v_cvt_pk_bf16_f32 v10, v20, v21\n\t"
      "v_cvt_pk_bf16_f32 v11, v22, v23\n\t"
      "global_store_dwordx4 v196, v[8:11], s[4:5] sc1\n\t"
      "v_add3_u32 v216, v8, v9, v10\n\t"
      "v_add_u32_e32 v216, v216, v11\n\t"
      "v_permlane32_swap_b32_e32 v217, v219\n\t"
      "v_add_u32_e32 v217, v217, v219\n\t"
      "v_permlane32_swap_b32_e32 v216, v218\n\t"
      "v_add_u32_e64 v216, v216, v218\n\t"
      "s_nop 1\n\t"
      "v_permlane16_swap_b32_e32 v216, v217\n\t"
      "v_add3_u32 v216, v216, v217, v250\n\t"
      "global_store_dword v252, v216, s[10:11] sc1\n\t"
      "s_barrier\n\t"
      "s_nop 1\n\t"
      "s_branch CEND_%=\n\t"
      "LOOP1_%=:\n\t"
      "s_waitcnt vmcnt(0)\n\t"
      "s_barrier\n\t"
      "ds_read_b64_tr_b16 v[108:109], v226\n\t"
      "ds_read_b64_tr_b16 v[110:111], v226 offset:512\n\t"
      "ds_read_b128 v[132:135], v224\n\t"
      "ds_read_b128 v[136:139], v224 offset:5120\n\t"
      "ds_read_b128 v[140:143], v224 offset:10240\n\t"
      "ds_read_b128 v[144:147], v224 offset:15360\n\t"
      "ds_read_b64_tr_b16 v[112:113], v227\n\t"
      "ds_read_b64_tr_b16 v[114:115], v227 offset:512\n\t"
      "ds_read_b128 v[148:151], v225\n\t"
      "ds_read_b64_tr_b16 v[116:117], v226 offset:1024\n\t"
      "ds_read_b64_tr_b16 v[118:119], v226 offset:1536\n\t"
      "ds_read_b64_tr_b16 v[120:121], v227 offset:1024\n\t"
      "ds_read_b64_tr_b16 v[122:123], v227 offset:1536\n\t"
      "ds_read_b64_tr_b16 v[124:125], v226 offset:2048\n\t"
      "ds_read_b64_tr_b16 v[126:127], v226 offset:2560\n\t"
      "ds_read_b64_tr_b16 v[128:129], v227 offset:2048\n\t"
      "ds_read_b64_tr_b16 v[130:131], v227 offset:2560\n\t"
      "s_branch EVEN1_%=\n\t"
      "s_waitcnt lgkmcnt(0)\n\t"
      ".p2align 6\n\t"
      "ODD1_%=:\n\t"
      "s_waitcnt lgkmcnt(0)\n\t"
      "v_mfma_f32_16x16x32_bf16 v[0:3], v[152:155], v[176:179], v[0:3]\n\t"
      "v_add_u32_e32 v224, s46, v224\n\t"
      "v_add_u32_e32 v226, s46, v226\n\t"
      "v_mfma_f32_16x16x32_bf16 v[24:27], v[152:155], v[180:183], v[24:27]\n\t"
      "v_add_u32_e32 v227, s46, v227\n\t"
      "v_add_u32_e32 v225, s46, v225\n\t"
      "v_mfma_f32_16x16x32_bf16 v[48:51], v[152:155], v[184:187], v[48:51]\n\t"
      "s_barrier\n\t"
      "ds_read_b64_tr_b16 v[108:109], v226\n\t"
      "v_mfma_f32_16x16x32_bf16 v[72:75], v[152:155], v[188:191], v[72:75]\n\t"
      "ds_read_b64_tr_b16 v[110:111], v226 offset:512\n\t"
      "v_mfma_f32_16x16x32_bf16 v[4:7], v[156:159], v[176:179], v[4:7]\n\t"
      "ds_read_b128 v[132:135], v224\n\t"
      "v_mfma_f32_16x16x32_bf16 v[28:31], v[156:159], v[180:183], v[28:31]\n\t"
      "ds_read_b128 v[136:139], v224 offset:5120\n\t"
      "v_mfma_f32_16x16x32_bf16 v[52:55], v[156:159], v[184:187], v[52:55]\n\t"
      "ds_read_b128 v[140:143], v224 offset:10240\n\t"
      "v_mfma_f32_16x16x32_bf16 v[76:79], v[156:159], v[188:191], v[76:79]\n\t"
      "ds_read_b128 v[144:147], v224 offset:15360\n\t"
      "v_mfma_f32_16x16x32_bf16 v[96:99], v[156:159], v[192:195], v[96:99]\n\t"
      "v_mfma_f32_16x16x32_bf16 v[8:11], v[160:163], v[176:179], v[8:11]\n\t"
      "ds_read_b64_tr_b16 v[112:113], v227\n\t"
      "v_mfma_f32_16x16x32_bf16 v[32:35], v[160:163], v[180:183], v[32:35]\n\t"
      "ds_read_b64_tr_b16 v[114:115], v227 offset:512\n\t"
      "v_mfma_f32_16x16x32_bf16 v[56:59], v[160:163], v[184:187], v[56:59]\n\t"
      "ds_read_b128 v[148:151], v225\n\t"
      "v_mfma_f32_16x16x32_bf16 v[80:83], v[160:163], v[188:191], v[80:83]\n\t"
      "ds_read_b64_tr_b16 v[116:117], v226 offset:1024\n\t"
      "v_mfma_f32_16x16x32_bf16 v[12:15], v[164:167], v[176:179], v[12:15]\n\t"
      "ds_read_b64_tr_b16 v[118:119], v226 offset:1536\n\t"
      "v_mfma_f32_16x16x32_bf16 v[36:39], v[164:167], v[180:183], v[36:39]\n\t"
      "ds_read_b64_tr_b16 v[120:121], v227 offset:1024\n\t"
      "v_mfma_f32_16x16x32_bf16 v[60:63], v[164:167], v[184:187], v[60:63]\n\t"
      "v_mfma_f32_16x16x32_bf16 v[84:87], v[164:167], v[188:191], v[84:87]\n\t"
      "ds_read_b64_tr_b16 v[122:123], v227 offset:1536\n\t"
      "v_mfma_f32_16x16x32_bf16 v[100:103], v[164:167], v[192:195], v[100:103]\n\t"
      "ds_read_b64_tr_b16 v[124:125], v226 offset:2048\n\t"
      "v_mfma_f32_16x16x32_bf16 v[16:19], v[176:179], v[168:171], v[16:19]\n\t"
      "ds_read_b64_tr_b16 v[126:127], v226 offset:2560\n\t"
      "v_mfma_f32_16x16x32_bf16 v[40:43], v[180:183], v[168:171], v[40:43]\n\t"
      "ds_read_b64_tr_b16 v[128:129], v227 offset:2048\n\t"
      "v_mfma_f32_16x16x32_bf16 v[64:67], v[184:187], v[168:171], v[64:67]\n\t"
      "ds_read_b64_tr_b16 v[130:131], v227 offset:2560\n\t"
      "v_mfma_f32_16x16x32_bf16 v[88:91], v[188:191], v[168:171], v[88:91]\n\t"
      "v_mfma_f32_16x16x32_bf16 v[20:23], v[176:179], v[172:175], v[20:23]\n\t"
      "v_mfma_f32_16x16x32_bf16 v[44:47], v[180:183], v[172:175], v[44:47]\n\t"
      "v_add_u32_e32 v248, s46, v248\n\t"
      "v_add_u32_e32 v249, s46, v249\n\t"
      "v_mfma_f32_16x16x32_bf16 v[68:71], v[184:187], v[172:175], v[68:71]\n\t"
      "v_mfma_f32_16x16x32_bf16 v[92:95], v[188:191], v[172:175], v[92:95]\n\t"
      "v_mfma_f32_16x16x32_bf16 v[104:107], v[192:195], v[172:175], v[104:107]\n\t"
      "EVEN1_%=:\n\t"
      "s_waitcnt lgkmcnt(0)\n\t"
      "v_mfma_f32_16x16x32_bf16 v[0:3], v[108:111], v[132:135], v[0:3]\n\t"
      "ds_read_b64_tr_b16 v[152:153], v226 offset:20480\n\t"
      "v_mfma_f32_16x16x32_bf16 v[24:27], v[108:111], v[136:139], v[24:27]\n\t"
      "ds_read_b64_tr_b16 v[154:155], v226 offset:20992\n\t"
      "v_mfma_f32_16x16x32_bf16 v[48:51], v[108:111], v[140:143], v[48:51]\n\t"
      "ds_read_b128 v[176:179], v248\n\t"
      "v_mfma_f32_16x16x32_bf16 v[72:75], v[108:111], v[144:147], v[72:75]\n\t"
      "ds_read_b128 v[180:183], v248 offset:5120\n\t"
      "v_mfma_f32_16x16x32_bf16 v[4:7], v[112:115], v[132:135], v[4:7]\n\t"
      "v_mfma_f32_16x16x32_bf16 v[28:31], v[112:115], v[136:139], v[28:31]\n\t"
      "ds_read_b128 v[184:187], v248 offset:10240\n\t"
      "v_mfma_f32_16x16x32_bf16 v[52:55], v[112:115], v[140:143], v[52:55]\n\t"
      "ds_read_b128 v[188:191], v248 offset:15360\n\t"
      "v_mfma_f32_16x16x32_bf16 v[76:79], v[112:115], v[144:147], v[76:79]\n\t"
      "ds_read_b64_tr_b16 v[156:157], v227 offset:20480\n\t"
      "v_mfma_f32_16x16x32_bf16 v[96:99], v[112:115], v[148:151], v[96:99]\n\t"
      "v_mfma_f32_16x16x32_bf16 v[8:11], v[116:119], v[132:135], v[8:11]\n\t"
      "ds_read_b64_tr_b16 v[158:159], v227 offset:20992\n\t"
      "v_mfma_f32_16x16x32_bf16 v[32:35], v[116:119], v[136:139], v[32:35]\n\t"
      "ds_read_b128 v[192:195], v249\n\t"
      "v_mfma_f32_16x16x32_bf16 v[56:59], v[116:119], v[140:143], v[56:59]\n\t"
      "ds_read_b64_tr_b16 v[160:161], v226 offset:21504\n\t"
      "v_mfma_f32_16x16x32_bf16 v[80:83], v[116:119], v[144:147], v[80:83]\n\t"
      "ds_read_b64_tr_b16 v[162:163], v226 offset:22016\n\t"
      "v_mfma_f32_16x16x32_bf16 v[12:15], v[120:123], v[132:135], v[12:15]\n\t"
      "v_mfma_f32_16x16x32_bf16 v[36:39], v[120:123], v[136:139], v[36:39]\n\t"
      "ds_read_b64_tr_b16 v[164:165], v227 offset:21504\n\t"
      "v_mfma_f32_16x16x32_bf16 v[60:63], v[120:123], v[140:143], v[60:63]\n\t"
      "ds_read_b64_tr_b16 v[166:167], v227 offset:22016\n\t"
      "v_mfma_f32_16x16x32_bf16 v[84:87], v[120:123], v[144:147], v[84:87]\n\t"
      "ds_read_b64_tr_b16 v[168:169], v226 offset:22528\n\t"
      "v_mfma_f32_16x16x32_bf16 v[100:103], v[120:123], v[148:151], v[100:103]\n\t"
      "v_mfma_f32_16x16x32_bf16 v[16:19], v[132:135], v[124:127], v[16:19]\n\t"
      "ds_read_b64_tr_b16 v[170:171], v226 offset:23040\n\t"
      "v_mfma_f32_16x16x32_bf16 v[40:43], v[136:139], v[124:127], v[40:43]\n\t"
      "ds_read_b64_tr_b16 v[172:173], v227 offset:22528\n\t"
      "v_mfma_f32_16x16x32_bf16 v[64:67], v[140:143], v[124:127], v[64:67]\n\t"
      "ds_read_b64_tr_b16 v[174:175], v227 offset:23040\n\t"
      "s_cmp_eq_u32 s47, 4\n\t"
      "s_cselect_b32 s46, -2, 1\n\t"
      "v_mfma_f32_16x16x32_bf16 v[88:91], v[144:147], v[124:127], v[88:91]\n\t"
      "s_cmp_eq_u32 s47, 1\n\t"
      "s_cselect_b32 s46, -2, s46\n\t"
      "v_mfma_f32_16x16x32_bf16 v[20:23], v[132:135], v[128:131], v[20:23]\n\t"
      "s_mul_i32 s46, s46, 43008\n\t"
      "v_mfma_f32_16x16x32_bf16 v[44:47], v[136:139], v[128:131], v[44:47]\n\t"
      "v_mfma_f32_16x16x32_bf16 v[68:71], v[140:143], v[128:131], v[68:71]\n\t"
      "v_mfma_f32_16x16x32_bf16 v[92:95], v[144:147], v[128:131], v[92:95]\n\t"
      "s_sub_u32 s47, s47, 1\n\t"
      "v_mfma_f32_16x16x32_bf16 v[104:107], v[148:151], v[128:131], v[104:107]\n\t"
      "s_cbranch_scc0 ODD1_%=\n\t"
      "s_waitcnt lgkmcnt(0)\n\t"
      "v_mfma_f32_16x16x32_bf16 v[96:99], v[156:159], v[192:195], v[96:99]\n\t"
      "ds_read_b32 v212, v215\n\t"
      "v_mfma_f32_16x16x32_bf16 v[100:103], v[164:167], v[192:195], v[100:103]\n\t"
      "ds_read2_b32 v[134:135], v234 offset0:80 offset1:80\n\t"
      "v_mfma_f32_16x16x32_bf16 v[104:107], v[192:195], v[172:175], v[104:107]\n\t"
      "ds_read_b128 v[148:151], v233 offset:320\n\t"
      "v_mfma_f32_16x16x32_bf16 v[80:83], v[160:163], v[188:191], v[80:83]\n\t"
      "ds_read2_b32 v[248:249], v234 offset0:0 offset1:0\n\t"
      "v_mfma_f32_16x16x32_bf16 v[84:87], v[164:167], v[188:191], v[84:87]\n\t"
      "ds_read_b128 v[196:199], v233\n\t"
      "v_mfma_f32_16x16x32_bf16 v[88:91], v[188:191], v[168:171], v[88:91]\n\t"
      "ds_read2_b32 v[132:133], v234 offset0:64 offset1:64\n\t"
      "v_mfma_f32_16x16x32_bf16 v[92:95], v[188:191], v[172:175], v[92:95]\n\t"
      "s_mov_b64 exec, s[44:45]\n\t"
      "ds_write_b128 v230, v[96:99]\n\t"
      "ds_write_b128 v230, v[100:103] offset:128\n\t"
      "s_mov_b64 exec, 0xffff\n\t"
      "ds_write_b32 v231, v104 offset:256\n\t"
      "ds_write_b32 v213, v213\n\t"
      "s_mov_b64 exec, -1\n\t"
      "v_mfma_f32_16x16x32_bf16 v[56:59], v[160:163], v[184:187], v[56:59]\n\t"
      "ds_read2_b32 v[130:131], v234 offset0:48 offset1:48\n\t"
      "v_mfma_f32_16x16x32_bf16 v[60:63], v[164:167], v[184:187], v[60:63]\n\t"
      "ds_read2_b32 v[128:129], v234 offset0:32 offset1:32\n\t"
      "v_mfma_f32_16x16x32_bf16 v[64:67], v[184:187], v[168:171], v[64:67]\n\t"
      "s_waitcnt lgkmcnt(0)\n\t"
      "v_mfma_f32_16x16x32_bf16 v[68:71], v[184:187], v[172:175], v[68:71]\n\t"
      "ds_read_b32 v226, v232\n\t"
      "ds_read_b128 v[116:119], v228 offset:128\n\t"
      "v_mfma_f32_16x16x32_bf16 v[32:35], v[160:163], v[180:183], v[32:35]\n\t"
      "ds_read_b128 v[120:123], v228 offset:192\n\t"
      "v_mfma_f32_16x16x32_bf16 v[36:39], v[164:167], v[180:183], v[36:39]\n\t"
      "ds_read2_b32 v[250:251], v229 offset0:64 offset1:64\n\t"
      "v_mfma_f32_16x16x32_bf16 v[40:43], v[180:183], v[168:171], v[40:43]\n\t"
      "ds_read2_b32 v[252:253], v229 offset0:80 offset1:80\n\t"
      "v_mfma_f32_16x16x32_bf16 v[44:47], v[180:183], v[172:175], v[44:47]\n\t"
      "ds_read_b128 v[124:127], v230 offset:128\n\t"
      "v_mfma_f32_16x16x32_bf16 v[8:11], v[160:163], v[176:179], v[8:11]\n\t"
      "ds_read2_b32 v[224:225], v231 offset0:64 offset1:64\n\t"
      "v_mfma_f32_16x16x32_bf16 v[12:15], v[164:167], v[176:179], v[12:15]\n\t"
      "v_mfma_f32_16x16x32_bf16 v[16:19], v[176:179], v[168:171], v[16:19]\n\t"
      "s_waitcnt lgkmcnt(6)\n\t"
      "v_min_u32_e32 v226, v226, v212\n\t"
      "v_cmp_eq_u32_e32 vcc, 0, v226\n\t"
      "v_mfma_f32_16x16x32_bf16 v[20:23], v[176:179], v[172:175], v[20:23]\n\t"
      "s_branch LOOPX_%=\n\t"
      "XAR_%=:\n\t"
      "s_mov_b32 s51, 0\n\t"
      "XAL_%=:\n\t"
      "s_add_u32 s51, s51, 1\n\t"
      "s_cmp_lt_u32 s51, 262144\n\t"
      "s_cbranch_scc0 XAX_%=\n\t"
      "s_waitcnt lgkmcnt(0)\n\t"
      "ds_read_b32 v212, v215\n\t"
      "ds_read2_b32 v[134:135], v234 offset0:80 offset1:80\n\t"
      "ds_read_b128 v[148:151], v233 offset:320\n\t"
      "ds_read2_b32 v[248:249], v234 offset0:0 offset1:0\n\t"
      "ds_read_b128 v[196:199], v233\n\t"
      "ds_read2_b32 v[132:133], v234 offset0:64 offset1:64\n\t"
      "ds_read2_b32 v[130:131], v234 offset0:48 offset1:48\n\t"
      "ds_read2_b32 v[128:129], v234 offset0:32 offset1:32\n\t"
      "s_waitcnt lgkmcnt(0)\n\t"
      "ds_read_b32 v226, v232\n\t"
      "ds_read_b128 v[116:119], v228 offset:128\n\t"
      "ds_read_b128 v[120:123], v228 offset:192\n\t"
      "ds_read2_b32 v[250:251], v229 offset0:64 offset1:64\n\t"
      "ds_read2_b32 v[252:253], v229 offset0:80 offset1:80\n\t"
      "ds_read_b128 v[124:127], v230 offset:128\n\t"
      "ds_read2_b32 v[224:225], v231 offset0:64 offset1:64\n\t"
      "s_waitcnt lgkmcnt(0)\n\t"
      "v_min_u32_e32 v226, v226, v212\n\t"
      "v_cmp_eq_u32_e32 vcc, 0, v226\n\t"
      "s_cbranch_vccnz XAL_%=\n\t"
      "XAX_%=:\n\t"
      "s_branch XAB_%=\n\t"
      "XBR_%=:\n\t"
      "s_mov_b32 s51, 0\n\t"
      "XBL_%=:\n\t"
      "s_add_u32 s51, s51, 1\n\t"
      "s_cmp_lt_u32 s51, 262144\n\t"
      "s_cbranch_scc0 XBX_%=\n\t"
      "s_waitcnt lgkmcnt(0)\n\t"
      "ds_read_b32 v226, v232 offset:4\n\t"
      "ds_read_b128 v[152:155], v236\n\t"
      "ds_read_b64 v[156:157], v237 offset:1024\n\t"
      "ds_read_b64 v[166:167], v237 offset:1536\n\t"
      "s_waitcnt lgkmcnt(0)\n\t"
      "v_cmp_eq_u32_e32 vcc, 0, v226\n\t"
      "s_cbranch_vccnz XBL_%=\n\t"
      "XBX_%=:\n\t"
      "s_branch XBB_%=\n\t"
      "CEND_%=:\n\t"
      : "+{s[4:11]}"(s), "+{s12}"(hint), "+{s13}"(chk), "+{v[224:239]}"(v), "+{v254}"(vk1)
      :
      : "memory", "vcc", "scc",
        "v0", "v1", "v2", "v3", "v4", "v5", "v6", "v7", "v8", "v9", "v10", "v11", "v12", "v13", "v14", "v15", "v16",
        "v17", "v18", "v19", "v20", "v21", "v22", "v23", "v24", "v25", "v26", "v27", "v28", "v29", "v30", "v31", "v32",
        "v33", "v34", "v35", "v36", "v37", "v38", "v39", "v40", "v41", "v42", "v43", "v44", "v45", "v46", "v47", "v48",
        "v49", "v50", "v51", "v52", "v53", "v54", "v55", "v56", "v57", "v58", "v59", "v60", "v61", "v62", "v63", "v64",
        "v65", "v66", "v67", "v68", "v69", "v70", "v71", "v72", "v73", "v74", "v75", "v76", "v77", "v78", "v79", "v80",
        "v81", "v82", "v83", "v84", "v85", "v86", "v87", "v88", "v89", "v90", "v91", "v92", "v93", "v94", "v95", "v96",
        "v97", "v98", "v99", "v100", "v101", "v102", "v103", "v104", "v105", "v106", "v107", "v108", "v109", "v110",
        "v111", "v112", "v113", "v114", "v115", "v116", "v117", "v118", "v119", "v120", "v121", "v122", "v123", "v124",
        "v125", "v126", "v127", "v128", "v129", "v130", "v131", "v132", "v133", "v134", "v135", "v136", "v137", "v138",
        "v139", "v140", "v141", "v142", "v143", "v144", "v145", "v146", "v147", "v148", "v149", "v150", "v151", "v152",
        "v153", "v154", "v155", "v156", "v157", "v158", "v159", "v160", "v161", "v162", "v163", "v164", "v165", "v166",
        "v167", "v168", "v169", "v170", "v171", "v172", "v173", "v174", "v175", "v176", "v177", "v178", "v179", "v180",
        "v181", "v182", "v183", "v184", "v185", "v186", "v187", "v188", "v189", "v190", "v191", "v192", "v193", "v194",
        "v195", "v196", "v197", "v198", "v199", "v200", "v201", "v202", "v203", "v204", "v205", "v206", "v207", "v208",
        "v209", "v210", "v211", "v212", "v213", "v214", "v215", "v216", "v217", "v218", "v219", "v220", "v221", "v222",
        "v223", "v240", "v241", "v242", "v243", "v244", "v245", "v246", "v247", "v248", "v249", "v250", "v251", "v252",
        "v253", "s36", "s37", "s38", "s39", "s40", "s41", "s42", "s43", "s44", "s45", "s46", "s47", "s48", "s49", "s50",
        "s51", "s52", "s53", "s54", "s55", "s56", "s57", "s58", "s59", "s60", "s61"
    );
    } else {
      LANE_BITS(threadIdx.x & 63);
      u32x8 v;
      u32x16 s;
      v[0] = ((8 * rt + 4 * half + g) * 16 + c) * 16;
      const int hi = -(lane >> 5), m3 = min(c, 2);
      v[1] = (((max(8 * rt + ((g & 1) ? 0 : 4 * half) - 1, 0) * 16 + c) * 16) & ~hi) | ((ST_PW + xcd * 128 + m3 * 16) & hi);
      v[2] = sta + 128 + lane * 4;
      v[3] = ((sta + ((g & 1) ? 0 : 64) + c * 4) & ~hi) | ((A_MK + m3 * 4) & hi);
      v[4] = lr * A_SLOT + A_HALO + lc * 16;
      v[6] = (side * 1024 + (idx * 4 + wave - 4) * 8 + lr) * 3584 + (7 * xp + min(lc, 6)) * 128;
      v[7] = v[6] + (1 - 2 * side) * 1024 * 3584;
      u64 tw = (u64)w1;
      unsigned vn = (((wave ^ (4 * side)) * 256 + (idx & 1) * 128 + lr) * 28 + 7 * xp + min(idx >> 1, 6)) * 128 + ((lc ^ swz_tr(lr)) << 4);
      s[4] = UNI(idx == 15 && xcd < 3 && wave == 4 ? (xcd == 0 ? WS_O : xcd == 1 ? WS_HN + 16 : WS_G + 32) + PROBE : 0);
      v[5] = lane * (8192 + 640);
      put(s, 0, (u64)(x + (rt * 128 * 896 + 128) - (16 * 896 + 1024)));
      put(s, 2, (u64)(wqkv + (hp * 112 + 64 * 2688) - 4096));
      s[5] = wave * A_UNIT;
      put2(s, 6, wave > 4 ? 0xffffffffu : 0xffffff00u, 0u);
      s[8] = A_HALO + (wave - 4) * 512;
      s[9] = A_FL + 32 + 4 * half;
      put(s, 10, (u64)(ws + WS_ST));
    asm volatile(
      "s_waitcnt lgkmcnt(0)\n\ts_nop 4\n\t"
      ".p2align 3\n\t"
      "s_load_dwordx2 s[18:19], s[14:15], 0x20000\n\t"
      "s_sub_u32 s40, s4, 57344\n\t"
      "s_subb_u32 s41, s5, 0\n\t"
      "s_sub_u32 s42, s6, 86016\n\t"
      "s_subb_u32 s43, s7, 0\n\t"
      "v_mov_b32_e32 v0, 0x3f803f80\n\t"
      "v_mov_b32_e32 v1, 0x3f803f80\n\t"
      "v_mov_b32_e32 v2, 0x3f803f80\n\t"
      "v_mov_b32_e32 v3, 0x3f803f80\n\t"
      "s_mov_b64 exec, 0xffffff\n\t"
      "ds_write_b128 v204, v[0:3]\n\t"
      "s_mov_b64 exec, -1\n\t"
      "v_mov_b32_e32 v32, 154624\n\t"
      "v_mov_b32_e32 v33, s13\n\t"
      "v_mov_b32_e32 v36, 0\n\t"
      "v_mov_b32_e32 v37, 0\n\t"
      "v_mov_b32_e32 v38, 0\n\t"
      "v_mov_b32_e32 v39, 0\n\t"
      "ds_write_b128 v32, v[36:39]\n\t"
      "ds_write_b128 v32, v[36:39] offset:16\n\t"
      "ds_write_b128 v32, v[36:39] offset:32\n\t"
      "v_xor_b32_e64 v21, 64, v20\n\t"
      "v_add_u32_e32 v22, 55296, v20\n\t"
      "v_add_u32_e32 v23, 82944, v21\n\t"
      "v_add_u32_e32 v21, 27648, v21\n\t"
      "s_sub_u32 s4, s4, 128\n\t"
      "s_subb_u32 s5, s5, 0\n\t"
      "s_sub_u32 s6, s6, 172032\n\t"
      "s_subb_u32 s7, s7, 0\n\t"
      "s_add_u32 m0, s9, 0\n\t"
      "s_nop 0\n\t"
      "global_load_lds_dwordx4 v195, s[6:7] offset:2048\n\t"
      "global_load_lds_dwordx4 v192, s[4:5]\n\t"
      "global_load_lds_dwordx4 v193, s[4:5] offset:1024\n\t"
      "global_load_lds_dwordx4 v196, s[6:7] offset:3072\n\t"
      "s_add_u32 m0, s9, 4096\n\t"
      "s_nop 0\n\t"
      "global_load_lds_dwordx4 v197, s[6:7]\n\t"
      "s_add_u32 m0, s12, 0\n\t"
      "s_mov_b64 exec, s[10:11]\n\t"
      "global_load_lds_dwordx4 v194, s[4:5]\n\t"
      "s_mov_b64 exec, -1\n\t"
      "s_add_u32 s4, s4, 128\n\t"
      "s_addc_u32 s5, s5, 0\n\t"
      "s_add_u32 s6, s6, 172032\n\t"
      "s_addc_u32 s7, s7, 0\n\t"
      "s_waitcnt lgkmcnt(0)\n\t"
      "s_add_u32 s18, s18, 1\n\t"
      "s_addc_u32 s19, s19, 0\n\t"
      "s_mul_i32 s48, s18, 0x9e3779b9\n\t"
      "s_mul_hi_u32 s17, s18, 0x7f4a7c15\n\t"
      "s_add_u32 s17, s17, s48\n\t"
      "s_mul_i32 s48, s19, 0x7f4a7c15\n\t"
      "s_add_u32 s17, s17, s48\n\t"
      "s_mul_i32 s16, s18, 0x7f4a7c15\n\t"
      "s_add_u32 s16, s16, 0xd192ed03\n\t"
      "s_addc_u32 s17, s17, 0xd1b54a32\n\t"
      "v_mov_b32_e32 v12, s16\n\t"
      "v_mov_b32_e32 v13, s17\n\t"
      "v_mov_b32_e32 v14, s18\n\t"
      "v_mov_b32_e32 v15, s19\n\t"
      "v_mov_b32_e32 v16, 163600\n\t"
      "s_mov_b64 exec, 1\n\t"
      "ds_write_b128 v16, v[12:15]\n\t"
      "s_mov_b64 exec, -1\n\t"
      "s_and_b32 s47, s8, 48\n\t"
      "s_add_u32 s47, s47, 114688\n\t"
      "v_mbcnt_lo_u32_b32 v28, -1, 0\n\t"
      "v_mov_b32_e32 v25, 0\n\t"
      "v_mov_b32_e32 v26, s16\n\t"
      "v_mov_b32_e32 v27, s17\n\t"
      "v_lshl_add_u32 v28, v28, 7, s47\n\t"
      "s_cmp_eq_u32 s8, 0\n\t"
      "s_cbranch_scc1 PRt_%=\n\t"
      "s_add_u32 s52, s14, s8\n\t"
      "s_addc_u32 s53, s15, 0\n\t"
      "s_mov_b32 s54, 0\n\t"
      "s_mov_b64 s[56:57], -1\n\t"
      "s_load_dword s50, s[52:53], 0x1f80\n\t"
      "PRt_%=:\n\t"
      "s_cmp_eq_u32 s8, 0\n\t"
      "s_cbranch_scc1 PRu_%=\n\t"
      "s_mov_b64 exec, 3\n\t"
      "global_store_dword v205, v0, s[52:53] sc1\n\t"
      "s_mov_b64 exec, -1\n\t"
      "PRu_%=:\n\t"
      "s_add_u32 m0, s9, 22528\n\t"
      "s_nop 0\n\t"
      "global_load_lds_dwordx4 v195, s[42:43] offset:2048\n\t"
      "global_load_lds_dwordx4 v192, s[40:41]\n\t"
      "global_load_lds_dwordx4 v193, s[40:41] offset:1024\n\t"
      "global_load_lds_dwordx4 v196, s[42:43] offset:3072\n\t"
      "s_add_u32 m0, s9, 26624\n\t"
      "s_nop 0\n\t"
      "global_load_lds_dwordx4 v197, s[42:43]\n\t"
      "s_add_u32 m0, s9, 43008\n\t"
      "s_nop 0\n\t"
      "global_load_lds_dwordx4 v195, s[6:7] offset:2048\n\t"
      "global_load_lds_dwordx4 v192, s[4:5]\n\t"
      "global_load_lds_dwordx4 v193, s[4:5] offset:1024\n\t"
      "global_load_lds_dwordx4 v196, s[6:7] offset:3072\n\t"
      "s_add_u32 m0, s9, 47104\n\t"
      "s_nop 0\n\t"
      "global_load_lds_dwordx4 v197, s[6:7]\n\t"
      "s_add_u32 m0, s12, 43008\n\t"
      "s_mov_b64 exec, s[10:11]\n\t"
      "global_load_lds_dwordx4 v194, s[4:5]\n\t"
      "s_mov_b64 exec, -1\n\t"
      "s_waitcnt vmcnt(11) lgkmcnt(0)\n\t"
      "s_barrier\n\t"
      "s_cmp_eq_u32 s8, 0\n\t"
      "s_cbranch_scc1 PRC0_%=\n\t"
      "s_mov_b32 s55, 8832\n\t"
      "s_call_b64 s[44:45], PRB_%=\n\t"
      "PRC0_%=:\n\t"
      "s_add_u32 s4, s4, 128\n\t"
      "s_addc_u32 s5, s5, 0\n\t"
      "s_add_u32 s6, s6, 172032\n\t"
      "s_addc_u32 s7, s7, 0\n\t"
      "s_add_u32 s40, s40, 128\n\t"
      "s_addc_u32 s41, s41, 0\n\t"
      "s_add_u32 s42, s42, 172032\n\t"
      "s_addc_u32 s43, s43, 0\n\t"
      "s_add_u32 m0, s9, 65536\n\t"
      "s_nop 0\n\t"
      "global_load_lds_dwordx4 v195, s[42:43] offset:2048\n\t"
      "global_load_lds_dwordx4 v192, s[40:41]\n\t"
      "global_load_lds_dwordx4 v193, s[40:41] offset:1024\n\t"
      "global_load_lds_dwordx4 v196, s[42:43] offset:3072\n\t"
      "s_add_u32 m0, s9, 69632\n\t"
      "s_nop 0\n\t"
      "global_load_lds_dwordx4 v197, s[42:43]\n\t"
      "s_add_u32 m0, s9, 86016\n\t"
      "s_nop 0\n\t"
      "global_load_lds_dwordx4 v195, s[6:7] offset:2048\n\t"
      "global_load_lds_dwordx4 v192, s[4:5]\n\t"
      "global_load_lds_dwordx4 v193, s[4:5] offset:1024\n\t"
      "global_load_lds_dwordx4 v196, s[6:7] offset:3072\n\t"
      "s_add_u32 m0, s9, 90112\n\t"
      "s_nop 0\n\t"
      "global_load_lds_dwordx4 v197, s[6:7]\n\t"
      "s_add_u32 m0, s12, 86016\n\t"
      "s_mov_b64 exec, s[10:11]\n\t"
      "global_load_lds_dwordx4 v194, s[4:5]\n\t"
      "s_mov_b64 exec, -1\n\t"
      "s_waitcnt vmcnt(11)\n\t"
      "s_barrier\n\t"
      "s_cmp_eq_u32 s8, 0\n\t"
      "s_cbranch_scc1 PRC1_%=\n\t"
      "s_mov_b32 s55, 17664\n\t"
      "s_call_b64 s[44:45], PRB_%=\n\t"
      "PRC1_%=:\n\t"
      "s_add_u32 s4, s4, 128\n\t"
      "s_addc_u32 s5, s5, 0\n\t"
      "s_add_u32 s6, s6, 172032\n\t"
      "s_addc_u32 s7, s7, 0\n\t"
      "s_add_u32 s40, s40, 128\n\t"
      "s_addc_u32 s41, s41, 0\n\t"
      "s_add_u32 s42, s42, 172032\n\t"
      "s_addc_u32 s43, s43, 0\n\t"
      "s_add_u32 m0, s9, 4294946816\n\t"
      "s_nop 0\n\t"
      "global_load_lds_dwordx4 v195, s[42:43] offset:2048\n\t"
      "global_load_lds_dwordx4 v192, s[40:41]\n\t"
      "global_load_lds_dwordx4 v193, s[40:41] offset:1024\n\t"
      "global_load_lds_dwordx4 v196, s[42:43] offset:3072\n\t"
      "s_add_u32 m0, s9, 4294950912\n\t"
      "s_nop 0\n\t"
      "global_load_lds_dwordx4 v197, s[42:43]\n\t"
      "s_add_u32 m0, s9, 0\n\t"
      "s_nop 0\n\t"
      "global_load_lds_dwordx4 v195, s[6:7] offset:2048\n\t"
      "global_load_lds_dwordx4 v192, s[4:5]\n\t"
      "global_load_lds_dwordx4 v193, s[4:5] offset:1024\n\t"
      "global_load_lds_dwordx4 v196, s[6:7] offset:3072\n\t"
      "s_add_u32 m0, s9, 4096\n\t"
      "s_nop 0\n\t"
      "global_load_lds_dwordx4 v197, s[6:7]\n\t"
      "s_add_u32 m0, s12, 0\n\t"
      "s_mov_b64 exec, s[10:11]\n\t"
      "global_load_lds_dwordx4 v194, s[4:5]\n\t"
      "s_mov_b64 exec, -1\n\t"
      "s_cmp_eq_u32 s8, 0\n\t"
      "s_cbranch_scc1 PRS_%=\n\t"
      "s_cmp_lt_u32 s57, s56\n\t"
      "s_cselect_b32 s46, 0x2000, 0\n\t"
      "v_mov_b32_e32 v24, s46\n\t"
      "v_xor_b32_e32 v26, s46, v26\n\t"
      "s_mov_b64 exec, 0xff\n\t"
      "global_store_dwordx4 v28, v[24:27], s[14:15] sc1\n\t"
      "s_mov_b64 exec, -1\n\t"
      "PRS_%=:\n\t"
      "s_waitcnt vmcnt(11)\n\t"
      "s_barrier\n\t"
      "s_add_u32 s4, s4, 128\n\t"
      "s_addc_u32 s5, s5, 0\n\t"
      "s_add_u32 s6, s6, 172032\n\t"
      "s_addc_u32 s7, s7, 0\n\t"
      "s_add_u32 s40, s40, 128\n\t"
      "s_addc_u32 s41, s41, 0\n\t"
      "s_add_u32 s42, s42, 172032\n\t"
      "s_addc_u32 s43, s43, 0\n\t"
      "s_add_u32 m0, s9, 22528\n\t"
      "s_nop 0\n\t"
      "global_load_lds_dwordx4 v195, s[42:43] offset:2048\n\t"
      "global_load_lds_dwordx4 v192, s[40:41]\n\t"
      "global_load_lds_dwordx4 v193, s[40:41] offset:1024\n\t"
      "global_load_lds_dwordx4 v196, s[42:43] offset:3072\n\t"
      "s_add_u32 m0, s9, 26624\n\t"
      "s_nop 0\n\t"
      "global_load_lds_dwordx4 v197, s[42:43]\n\t"
      "s_add_u32 m0, s9, 43008\n\t"
      "s_nop 0\n\t"
      "global_load_lds_dwordx4 v195, s[6:7] offset:2048\n\t"
      "global_load_lds_dwordx4 v192, s[4:5]\n\t"
      "global_load_lds_dwordx4 v193, s[4:5] offset:1024\n\t"
      "global_load_lds_dwordx4 v196, s[6:7] offset:3072\n\t"
      "s_add_u32 m0, s9, 47104\n\t"
      "s_nop 0\n\t"
      "global_load_lds_dwordx4 v197, s[6:7]\n\t"
      "s_add_u32 m0, s12, 43008\n\t"
      "s_mov_b64 exec, s[10:11]\n\t"
      "global_load_lds_dwordx4 v194, s[4:5]\n\t"
      "s_mov_b64 exec, -1\n\t"
      "s_waitcnt vmcnt(11)\n\t"
      "s_barrier\n\t"
      "s_add_u32 s4, s4, 128\n\t"
      "s_addc_u32 s5, s5, 0\n\t"
      "s_add_u32 s6, s6, 172032\n\t"
      "s_addc_u32 s7, s7, 0\n\t"
      "s_add_u32 s40, s40, 128\n\t"
      "s_addc_u32 s41, s41, 0\n\t"
      "s_add_u32 s42, s42, 172032\n\t"
      "s_addc_u32 s43, s43, 0\n\t"
      "s_add_u32 m0, s9, 65536\n\t"
      "s_nop 0\n\t"
      "global_load_lds_dwordx4 v195, s[42:43] offset:2048\n\t"
      "global_load_lds_dwordx4 v192, s[40:41]\n\t"
      "global_load_lds_dwordx4 v193, s[40:41] offset:1024\n\t"
      "global_load_lds_dwordx4 v196, s[42:43] offset:3072\n\t"
      "s_add_u32 m0, s9, 69632\n\t"
      "s_nop 0\n\t"
      "global_load_lds_dwordx4 v197, s[42:43]\n\t"
      "s_add_u32 m0, s9, 86016\n\t"
      "s_nop 0\n\t"
      "global_load_lds_dwordx4 v195, s[6:7] offset:2048\n\t"
      "global_load_lds_dwordx4 v192, s[4:5]\n\t"
      "global_load_lds_dwordx4 v193, s[4:5] offset:1024\n\t"
      "global_load_lds_dwordx4 v196, s[6:7] offset:3072\n\t"
      "s_add_u32 m0, s9, 90112\n\t"
      "s_nop 0\n\t"
      "global_load_lds_dwordx4 v197, s[6:7]\n\t"
      "s_add_u32 m0, s12, 86016\n\t"
      "s_mov_b64 exec, s[10:11]\n\t"
      "global_load_lds_dwordx4 v194, s[4:5]\n\t"
      "s_mov_b64 exec, -1\n\t"
      "global_load_dwordx4 v[4:7], v200, s[14:15] sc1\n\t"
      "global_load_dwordx4 v[8:11], v201, s[14:15] sc1\n\t"
      "s_waitcnt vmcnt(13)\n\t"
      "s_barrier\n\t"
      "s_add_u32 s4, s4, 128\n\t"
      "s_addc_u32 s5, s5, 0\n\t"
      "s_add_u32 s6, s6, 172032\n\t"
      "s_addc_u32 s7, s7, 0\n\t"
      "s_add_u32 s40, s40, 128\n\t"
      "s_addc_u32 s41, s41, 0\n\t"
      "s_add_u32 s42, s42, 172032\n\t"
      "s_addc_u32 s43, s43, 0\n\t"
      "s_add_u32 m0, s9, 4294946816\n\t"
      "s_nop 0\n\t"
      "global_load_lds_dwordx4 v195, s[42:43] offset:2048\n\t"
      "global_load_lds_dwordx4 v192, s[40:41]\n\t"
      "global_load_lds_dwordx4 v193, s[40:41] offset:1024\n\t"
      "global_load_lds_dwordx4 v196, s[42:43] offset:3072\n\t"
      "s_add_u32 m0, s9, 4294950912\n\t"
      "s_nop 0\n\t"
      "global_load_lds_dwordx4 v197, s[42:43]\n\t"
      "s_add_u32 m0, s9, 0\n\t"
      "s_nop 0\n\t"
      "global_load_lds_dwordx4 v195, s[6:7] offset:2048\n\t"
      "global_load_lds_dwordx4 v192, s[4:5]\n\t"
      "global_load_lds_dwordx4 v193, s[4:5] offset:1024\n\t"
      "global_load_lds_dwordx4 v196, s[6:7] offset:3072\n\t"
      "s_add_u32 m0, s9, 4096\n\t"
      "s_nop 0\n\t"
      "global_load_lds_dwordx4 v197, s[6:7]\n\t"
      "s_add_u32 m0, s12, 0\n\t"
      "s_mov_b64 exec, s[10:11]\n\t"
      "global_load_lds_dwordx4 v194, s[4:5]\n\t"
      "s_mov_b64 exec, -1\n\t"
      "s_waitcnt vmcnt(13)\n\t"
      "s_barrier\n\t"
      "s_mov_b32 m0, 163328\n\t"
      "s_add_u32 s44, s20, 0\n\t"
      "s_addc_u32 s45, s21, 0\n\t"
      "global_load_lds_dword v206, s[44:45]\n\t"
      "s_mov_b32 m0, 163328\n\t"
      "s_add_u32 s44, s20, 1835008\n\t"
      "s_addc_u32 s45, s21, 0\n\t"
      "global_load_lds_dword v206, s[44:45]\n\t"
      "s_waitcnt vmcnt(2)\n\t"
      "s_barrier\n\t"
      "v_xor_b32_e32 v6, v6, v4\n\t"
      "v_xor_b32_e32 v7, v7, v5\n\t"
      "v_xor_b32_e32 v10, v10, v8\n\t"
      "v_xor_b32_e32 v11, v11, v9\n\t"
      "v_cmp_ne_u64_e64 s[48:49], s[16:17], v[6:7]\n\t"
      "v_cmp_ne_u64_e32 vcc, s[16:17], v[10:11]\n\t"
      "s_nop 0\n\t"
      "s_or_b64 vcc, vcc, s[48:49]\n\t"
      "s_cbranch_scc1 GRR_%=\n\t"
      "GRB_%=:\n\t"
      "v_mul_f32_e32 v6, 0x3e8b97f6, v5\n\t"
      "ds_write2_b32 v202, v4, v5 offset0:0 offset1:96\n\t"
      "ds_write2_b32 v203, v8, v9 offset0:0 offset1:96\n\t"
      "ds_write_b32 v202, v6 offset:640\n\t"
      "s_mov_b64 exec, 1\n\t"
      "ds_write_b32 v33, v33\n\t"
      "s_mov_b64 exec, -1\n\t"
      "s_sub_u32 s46, s12, 38912\n\t"
      "s_lshl_b32 s46, s46, 5\n\t"
      "s_add_u32 m0, s46, 0\n\t"
      "s_add_u32 s44, s20, 0\n\t"
      "s_addc_u32 s45, s21, 0\n\t"
      "global_load_lds_dwordx4 v20, s[44:45]\n\t"
      "global_load_lds_dwordx4 v21, s[44:45] offset:1024\n\t"
      "global_load_lds_dwordx4 v22, s[44:45] offset:2048\n\t"
      "global_load_lds_dwordx4 v23, s[44:45] offset:3072\n\t"
      "s_add_u32 m0, s46, 4096\n\t"
      "s_add_u32 s44, s20, 114688\n\t"
      "s_addc_u32 s45, s21, 0\n\t"
      "global_load_lds_dwordx4 v20, s[44:45]\n\t"
      "global_load_lds_dwordx4 v21, s[44:45] offset:1024\n\t"
      "global_load_lds_dwordx4 v22, s[44:45] offset:2048\n\t"
      "global_load_lds_dwordx4 v23, s[44:45] offset:3072\n\t"
      "s_waitcnt lgkmcnt(0)\n\t"
      "s_branch HEND_%=\n\t"
      ".p2align 6\n\t"
      "PRB_%=:\n\t"
      "s_waitcnt lgkmcnt(0)\n\t"
      "s_memtime s[58:59]\n\t"
      "s_waitcnt lgkmcnt(0)\n\t"
      "s_load_dword s50, s[52:53], s54\n\t"
      "s_waitcnt lgkmcnt(0)\n\t"
      "s_memtime s[46:47]\n\t"
      "s_add_u32 s54, s54, 8832\n\t"
      "s_waitcnt lgkmcnt(0)\n\t"
      "s_sub_u32 s46, s46, s58\n\t"
      "s_min_u32 s46, s56, s46\n\t"
      "s_mov_b32 s56, s57\n\t"
      "s_mov_b32 s57, s46\n\t"
      "s_cmp_lt_u32 s54, s55\n\t"
      "s_cbranch_scc1 PRB_%=\n\t"
      "s_setpc_b64 s[44:45]\n\t"
      "GRR_%=:\n\t"
      "s_mov_b32 s51, 0\n\t"
      "GRL_%=:\n\t"
      "s_add_u32 s51, s51, 1\n\t"
      "s_cmp_lt_u32 s51, 262144\n\t"
      "s_cbranch_scc0 GRX_%=\n\t"
      "s_sleep 2\n\t"
      "global_load_dwordx4 v[4:7], v200, s[14:15] sc1\n\t"
      "global_load_dwordx4 v[8:11], v201, s[14:15] sc1\n\t"
      "s_waitcnt vmcnt(0)\n\t"
      "v_xor_b32_e32 v6, v6, v4\n\t"
      "v_xor_b32_e32 v7, v7, v5\n\t"
      "v_xor_b32_e32 v10, v10, v8\n\t"
      "v_xor_b32_e32 v11, v11, v9\n\t"
      "v_cmp_ne_u64_e64 s[48:49], s[16:17], v[6:7]\n\t"
      "v_cmp_ne_u64_e32 vcc, s[16:17], v[10:11]\n\t"
      "s_nop 0\n\t"
      "s_or_b64 vcc, vcc, s[48:49]\n\t"
      "s_cbranch_scc1 GRL_%=\n\t"
      "GRX_%=:\n\t"
      "s_mov_b32 exec_lo, 0\n\t"
      "v_and_b32_e32 v8, 0x2000, v8\n\t"
      "s_mov_b32 exec_lo, -1\n\t"
      "s_branch GRB_%=\n\t"
      "HEND_%=:\n\t"
      : "+{s[4:19]}"(s), "+{s[20:21]}"(tw), "+{v[192:199]}"(f), "+{v[200:207]}"(v), "+{v20}"(vn)
      :
      : "memory", "vcc", "scc",
        "v0", "v1", "v2", "v3", "v4", "v5", "v6", "v7", "v8", "v9", "v10", "v11", "v12", "v13", "v14", "v15", "v16",
        "v17", "v18", "v19", "v21", "v22", "v23", "v24", "v25", "v26", "v27", "v28", "v29", "v30", "v31", "v32", "v33",
        "v34", "v35", "v36", "v37", "v38", "v39", "s40", "s41", "s42", "s43", "s44", "s45", "s46", "s47", "s48", "s49",
        "s50", "s51", "s52", "s53", "s54", "s55", "s56", "s57", "s58", "s59"
    );
    }
    PIN();
    LANE_BITS(__builtin_amdgcn_mbcnt_hi(~0u, __builtin_amdgcn_mbcnt_lo(~0u, 0u)));
    const bool has3 = idx < 14;
    const int ft = has3 ? 7 * xp + (idx >> 1) : 0, blk0 = 7 * (2 * side + (idx & 1));
    u32x8 v;
    u32x16 s;
    v[0] = lane * 16;
    const int rot = 4 * (idx & 1), kg = 2 * ft + ((wave >> 1) & 1), ks = PRE ? wave ^ (4 * side) : wave, rn = rot + (wave >> 2) * PRE;
    u64 kr = (u64)(7 - rn) | ((u64)rot << 32);
    v[1] = has3 ? lr * 3584 + ((lc ^ swz_tr(lr)) << 4) : SP3 ? (min(lane, 55) + 96 * (idx & 1)) * 4096 + 384 + xp * 1024 : (lr + 8 * side) * 4096 + lc * 128 + xp * 1024;
    v[2] = wave * G_WV + g * 1024 + q4 * 128 + (((2 * (wave & 3) + (p4 >> 1)) ^ swz_tr(8 * g + q4)) << 4) + (p4 & 1) * 8;
    v[3] = wave * 4096 + (wave & 3) * 1024 + ((lane * 16) ^ ((g & 1) << 7));
    const int gg = 2 * (lane >> 5) + (lane & 1);
    v[4] = (wave & 3) * 1024 + (((gg * 16 + ((lane >> 1) & 15)) * 16) ^ ((gg & 1) << 7));
    v[5] = (wave & 1) * 512 + lane * 8;
    v[6] = g * 8;
    v[7] = ST_H3 + part(xp * 16384 + (ks * 16 + c) * 128, side);
    put(s, 0, has3 ? (u64)(w1 + ft * 128 + (ks * 256 + rn * 32) * 3584) : (u64)(w2 + (wave * 224 + (idx & 1) * 16) * 4096));
    put(s, 2, (u64)(ws + WS_HN + unit(blk0, ks, 0)));
    put(s, 4, (u64)(ws + WS_G + unit(blk0, kg / 7, 0) + kg % 7 * 1024));
    put(s, 6, (u64)(b1 + (64 * ft + 16 * (wave & 3)) * 2));
    put(s, 8, (u64)(ws + WS_ST));
    unsigned hint = ST_H4 + side * 8192 + ((idx & 1) * 28 + ft) * 128;
    s[12] = UNI((has3 ? 4 : 0) |  (ks == 4 * side + ((xp + XO) & 3) ? 16 : 0));
    s[13] = wave * G_WV;
    asm volatile(
      "s_waitcnt lgkmcnt(0)\n\ts_nop 4\n\t"
      ".p2align 3\n\t"
      "s_bitcmp0_b32 s16, 2\n\t"
      "s_cbranch_scc1 ASP_%=\n\t"
      "v_mov_b32_e32 v216, 163600\n\t"
      "ds_read_b128 v[216:219], v216\n\t"
      "v_mov_b32_e32 v220, 154756\n\t"
      "ds_read2_b32 v[220:221], v220 offset0:0 offset1:1\n\t"
      "v_xor_b32_e64 v248, 64, v241\n\t"
      "v_add_u32_e32 v249, 55296, v241\n\t"
      "v_add_u32_e32 v250, 82944, v248\n\t"
      "v_add_u32_e32 v248, 27648, v248\n\t"
      "v_xor_b32_e64 v251, 32, v242\n\t"
      "v_xor_b32_e32 v252, 64, v242\n\t"
      "v_xor_b32_e32 v253, 96, v242\n\t"
      "s_cmp_ge_u32 s17, 65536\n\t"
      "s_cbranch_scc1 WPR_%=\n\t"
      "s_add_u32 m0, s17, 0\n\t"
      "s_nop 0\n\t"
      "global_load_lds_dwordx4 v241, s[4:5]\n\t"
      "global_load_lds_dwordx4 v248, s[4:5] offset:1024\n\t"
      "global_load_lds_dwordx4 v249, s[4:5] offset:2048\n\t"
      "global_load_lds_dwordx4 v250, s[4:5] offset:3072\n\t"
      "s_sub_u32 s20, s20, 1\n\t"
      "s_cselect_b32 s48, -7, 1\n\t"
      "s_cselect_b32 s49, -1, 0\n\t"
      "s_mul_i32 s48, s48, 114688\n\t"
      "s_add_u32 s4, s4, s48\n\t"
      "s_addc_u32 s5, s5, s49\n\t"
      "s_add_u32 m0, s17, 4096\n\t"
      "s_nop 0\n\t"
      "global_load_lds_dwordx4 v241, s[4:5]\n\t"
      "global_load_lds_dwordx4 v248, s[4:5] offset:1024\n\t"
      "global_load_lds_dwordx4 v249, s[4:5] offset:2048\n\t"
      "global_load_lds_dwordx4 v250, s[4:5] offset:3072\n\t"
      "s_sub_u32 s20, s20, 1\n\t"
      "s_cselect_b32 s48, -7, 1\n\t"
      "s_cselect_b32 s49, -1, 0\n\t"
      "s_mul_i32 s48, s48, 114688\n\t"
      "s_add_u32 s4, s4, s48\n\t"
      "s_addc_u32 s5, s5, s49\n\t"
      "WPR_%=:\n\t"
      "global_load_dwordx2 v[238:239], v246, s[10:11]\n\t"
      "s_waitcnt lgkmcnt(0)\n\t"
      "v_readfirstlane_b32 s14, v216\n\t"
      "v_readfirstlane_b32 s15, v217\n\t"
      "v_readfirstlane_b32 s46, v218\n\t"
      "v_readfirstlane_b32 s47, v219\n\t"
      "v_readfirstlane_b32 s48, v220\n\t"
      "v_readfirstlane_b32 s49, v221\n\t"
      "v_xor_b32_e32 v247, v247, v221\n\t"
      "s_nop 0\n\t"
      "s_xor_b32 s6, s6, s48\n\t"
      "s_xor_b32 s8, s8, s49\n\t"
      "s_xor_b32 s22, s22, s49\n\t"
      "s_add_u32 s52, s6, 0\n\t"
      "s_addc_u32 s53, s7, 0\n\t"
      "s_add_u32 s54, s6, 131072\n\t"
      "s_addc_u32 s55, s7, 0\n\t"
      "s_add_u32 s56, s6, 262144\n\t"
      "s_addc_u32 s57, s7, 0\n\t"
      "s_add_u32 s58, s6, 393216\n\t"
      "s_addc_u32 s59, s7, 0\n\t"
      "s_add_u32 s60, s6, 524288\n\t"
      "s_addc_u32 s61, s7, 0\n\t"
      "s_add_u32 s62, s6, 655360\n\t"
      "s_addc_u32 s63, s7, 0\n\t"
      "s_add_u32 s64, s6, 786432\n\t"
      "s_addc_u32 s65, s7, 0\n\t"
      "s_add_u32 s48, s21, 0\n\t"
      "s_cmp_ge_u32 s48, 8\n\t"
      "s_cselect_b32 s49, 8, 0\n\t"
      "s_sub_u32 s48, s48, s49\n\t"
      "v_lshl_add_u32 v176, s48, 10, v240\n\t"
      "s_add_u32 s48, s21, 1\n\t"
      "s_cmp_ge_u32 s48, 8\n\t"
      "s_cselect_b32 s49, 8, 0\n\t"
      "s_sub_u32 s48, s48, s49\n\t"
      "v_lshl_add_u32 v177, s48, 10, v240\n\t"
      "s_add_u32 s48, s21, 2\n\t"
      "s_cmp_ge_u32 s48, 8\n\t"
      "s_cselect_b32 s49, 8, 0\n\t"
      "s_sub_u32 s48, s48, s49\n\t"
      "v_lshl_add_u32 v178, s48, 10, v240\n\t"
      "s_add_u32 s48, s21, 3\n\t"
      "s_cmp_ge_u32 s48, 8\n\t"
      "s_cselect_b32 s49, 8, 0\n\t"
      "s_sub_u32 s48, s48, s49\n\t"
      "v_lshl_add_u32 v179, s48, 10, v240\n\t"
      "s_add_u32 s48, s21, 4\n\t"
      "s_cmp_ge_u32 s48, 8\n\t"
      "s_cselect_b32 s49, 8, 0\n\t"
      "s_sub_u32 s48, s48, s49\n\t"
      "v_lshl_add_u32 v180, s48, 10, v240\n\t"
      "s_add_u32 s48, s21, 5\n\t"
      "s_cmp_ge_u32 s48, 8\n\t"
      "s_cselect_b32 s49, 8, 0\n\t"
      "s_sub_u32 s48, s48, s49\n\t"
      "v_lshl_add_u32 v181, s48, 10, v240\n\t"
      "s_add_u32 s48, s21, 6\n\t"
      "s_cmp_ge_u32 s48, 8\n\t"
      "s_cselect_b32 s49, 8, 0\n\t"
      "s_sub_u32 s48, s48, s49\n\t"
      "v_lshl_add_u32 v182, s48, 10, v240\n\t"
      "s_add_u32 s48, s21, 7\n\t"
      "s_cmp_ge_u32 s48, 8\n\t"
      "s_cselect_b32 s49, 8, 0\n\t"
      "s_sub_u32 s48, s48, s49\n\t"
      "v_lshl_add_u32 v183, s48, 10, v240\n\t"
      "s_cmp_ge_u32 s17, 65536\n\t"
      "s_cbranch_scc0 BOH_%=\n\t"
      "s_barrier\n\t"
      "BOH_%=:\n\t"
      "s_waitcnt vmcnt(5)\n\t"
      "s_add_u32 m0, s17, 8192\n\t"
      "s_nop 0\n\t"
      "global_load_lds_dwordx4 v241, s[4:5]\n\t"
      "global_load_lds_dwordx4 v248, s[4:5] offset:1024\n\t"
      "global_load_lds_dwordx4 v249, s[4:5] offset:2048\n\t"
      "global_load_lds_dwordx4 v250, s[4:5] offset:3072\n\t"
      "s_sub_u32 s20, s20, 1\n\t"
      "s_cselect_b32 s48, -7, 1\n\t"
      "s_cselect_b32 s49, -1, 0\n\t"
      "s_mul_i32 s48, s48, 114688\n\t"
      "s_add_u32 s4, s4, s48\n\t"
      "s_addc_u32 s5, s5, s49\n\t"
      "ds_read_b64_tr_b16 v[0:1], v242\n\t"
      "ds_read_b64_tr_b16 v[2:3], v242 offset:512\n\t"
      "ds_read_b64_tr_b16 v[4:5], v251\n\t"
      "ds_read_b64_tr_b16 v[6:7], v251 offset:512\n\t"
      "ds_read_b64_tr_b16 v[8:9], v252\n\t"
      "ds_read_b64_tr_b16 v[10:11], v252 offset:512\n\t"
      "ds_read_b64_tr_b16 v[12:13], v253\n\t"
      "ds_read_b64_tr_b16 v[14:15], v253 offset:512\n\t"
      "s_waitcnt vmcnt(5)\n\t"
      "s_add_u32 m0, s17, 12288\n\t"
      "s_nop 0\n\t"
      "global_load_lds_dwordx4 v241, s[4:5]\n\t"
      "global_load_lds_dwordx4 v248, s[4:5] offset:1024\n\t"
      "global_load_lds_dwordx4 v249, s[4:5] offset:2048\n\t"
      "global_load_lds_dwordx4 v250, s[4:5] offset:3072\n\t"
      "s_sub_u32 s20, s20, 1\n\t"
      "s_cselect_b32 s48, -7, 1\n\t"
      "s_cselect_b32 s49, -1, 0\n\t"
      "s_mul_i32 s48, s48, 114688\n\t"
      "s_add_u32 s4, s4, s48\n\t"
      "s_addc_u32 s5, s5, s49\n\t"
      "ds_read_b64_tr_b16 v[16:17], v242 offset:4096\n\t"
      "ds_read_b64_tr_b16 v[18:19], v242 offset:4608\n\t"
      "ds_read_b64_tr_b16 v[20:21], v251 offset:4096\n\t"
      "ds_read_b64_tr_b16 v[22:23], v251 offset:4608\n\t"
      "ds_read_b64_tr_b16 v[24:25], v252 offset:4096\n\t"
      "ds_read_b64_tr_b16 v[26:27], v252 offset:4608\n\t"
      "ds_read_b64_tr_b16 v[28:29], v253 offset:4096\n\t"
      "ds_read_b64_tr_b16 v[30:31], v253 offset:4608\n\t"
      "s_waitcnt vmcnt(8)\n\t"
      "v_xor_b32_e32 v164, 1024, v243\n\t"
      "v_xor_b32_e32 v165, 2048, v243\n\t"
      "v_xor_b32_e32 v166, 3072, v243\n\t"
      "v_lshlrev_b32_e32 v236, 16, v238\n\t"
      "v_and_b32_e32 v237, 0xffff0000, v238\n\t"
      "v_lshlrev_b32_e32 v238, 16, v239\n\t"
      "v_and_b32_e32 v239, 0xffff0000, v239\n\t"
      "v_mov_b32_e32 v235, 0xbdd2d3e8\n\t"
      "s_waitcnt vmcnt(4)\n\t"
      "s_waitcnt lgkmcnt(8)\n\t"
      "s_add_u32 m0, s17, 0\n\t"
      "s_nop 0\n\t"
      "global_load_lds_dwordx4 v241, s[4:5]\n\t"
      "global_load_lds_dwordx4 v248, s[4:5] offset:1024\n\t"
      "global_load_lds_dwordx4 v249, s[4:5] offset:2048\n\t"
      "global_load_lds_dwordx4 v250, s[4:5] offset:3072\n\t"
      "s_sub_u32 s20, s20, 1\n\t"
      "s_cselect_b32 s48, -7, 1\n\t"
      "s_cselect_b32 s49, -1, 0\n\t"
      "s_mul_i32 s48, s48, 114688\n\t"
      "s_add_u32 s4, s4, s48\n\t"
      "s_addc_u32 s5, s5, s49\n\t"
      "ds_read_b64_tr_b16 v[32:33], v242 offset:8192\n\t"
      "ds_read_b64_tr_b16 v[34:35], v242 offset:8704\n\t"
      "ds_read_b64_tr_b16 v[36:37], v251 offset:8192\n\t"
      "ds_read_b64_tr_b16 v[38:39], v251 offset:8704\n\t"
      "ds_read_b64_tr_b16 v[40:41], v252 offset:8192\n\t"
      "ds_read_b64_tr_b16 v[42:43], v252 offset:8704\n\t"
      "ds_read_b64_tr_b16 v[44:45], v253 offset:8192\n\t"
      "ds_read_b64_tr_b16 v[46:47], v253 offset:8704\n\t"
      "s_waitcnt vmcnt(4)\n\t"
      "s_waitcnt lgkmcnt(8)\n\t"
      "s_add_u32 m0, s17, 4096\n\t"
      "s_nop 0\n\t"
      "global_load_lds_dwordx4 v241, s[4:5]\n\t"
      "global_load_lds_dwordx4 v248, s[4:5] offset:1024\n\t"
      "global_load_lds_dwordx4 v249, s[4:5] offset:2048\n\t"
      "global_load_lds_dwordx4 v250, s[4:5] offset:3072\n\t"
      "s_sub_u32 s20, s20, 1\n\t"
      "s_cselect_b32 s48, -7, 1\n\t"
      "s_cselect_b32 s49, -1, 0\n\t"
      "s_mul_i32 s48, s48, 114688\n\t"
      "s_add_u32 s4, s4, s48\n\t"
      "s_addc_u32 s5, s5, s49\n\t"
      "ds_read_b64_tr_b16 v[48:49], v242 offset:12288\n\t"
      "ds_read_b64_tr_b16 v[50:51], v242 offset:12800\n\t"
      "ds_read_b64_tr_b16 v[52:53], v251 offset:12288\n\t"
      "ds_read_b64_tr_b16 v[54:55], v251 offset:12800\n\t"
      "ds_read_b64_tr_b16 v[56:57], v252 offset:12288\n\t"
      "ds_read_b64_tr_b16 v[58:59], v252 offset:12800\n\t"
      "ds_read_b64_tr_b16 v[60:61], v253 offset:12288\n\t"
      "ds_read_b64_tr_b16 v[62:63], v253 offset:12800\n\t"
      "s_waitcnt vmcnt(4)\n\t"
      "s_waitcnt lgkmcnt(8)\n\t"
      "s_add_u32 m0, s17, 8192\n\t"
      "s_nop 0\n\t"
      "global_load_lds_dwordx4 v241, s[4:5]\n\t"
      "global_load_lds_dwordx4 v248, s[4:5] offset:1024\n\t"
      "global_load_lds_dwordx4 v249, s[4:5] offset:2048\n\t"
      "global_load_lds_dwordx4 v250, s[4:5] offset:3072\n\t"
      "s_sub_u32 s20, s20, 1\n\t"
      "s_cselect_b32 s48, -7, 1\n\t"
      "s_cselect_b32 s49, -1, 0\n\t"
      "s_mul_i32 s48, s48, 114688\n\t"
      "s_add_u32 s4, s4, s48\n\t"
      "s_addc_u32 s5, s5, s49\n\t"
      "ds_read_b64_tr_b16 v[64:65], v242\n\t"
      "ds_read_b64_tr_b16 v[66:67], v242 offset:512\n\t"
      "ds_read_b64_tr_b16 v[68:69], v251\n\t"
      "ds_read_b64_tr_b16 v[70:71], v251 offset:512\n\t"
      "ds_read_b64_tr_b16 v[72:73], v252\n\t"
      "ds_read_b64_tr_b16 v[74:75], v252 offset:512\n\t"
      "ds_read_b64_tr_b16 v[76:77], v253\n\t"
      "ds_read_b64_tr_b16 v[78:79], v253 offset:512\n\t"
      "s_waitcnt vmcnt(4)\n\t"
      "s_waitcnt lgkmcnt(8)\n\t"
      "s_add_u32 m0, s17, 12288\n\t"
      "s_nop 0\n\t"
      "global_load_lds_dwordx4 v241, s[4:5]\n\t"
      "global_load_lds_dwordx4 v248, s[4:5] offset:1024\n\t"
      "global_load_lds_dwordx4 v249, s[4:5] offset:2048\n\t"
      "global_load_lds_dwordx4 v250, s[4:5] offset:3072\n\t"
      "ds_read_b64_tr_b16 v[80:81], v242 offset:4096\n\t"
      "ds_read_b64_tr_b16 v[82:83], v242 offset:4608\n\t"
      "ds_read_b64_tr_b16 v[84:85], v251 offset:4096\n\t"
      "ds_read_b64_tr_b16 v[86:87], v251 offset:4608\n\t"
      "ds_read_b64_tr_b16 v[88:89], v252 offset:4096\n\t"
      "ds_read_b64_tr_b16 v[90:91], v252 offset:4608\n\t"
      "ds_read_b64_tr_b16 v[92:93], v253 offset:4096\n\t"
      "ds_read_b64_tr_b16 v[94:95], v253 offset:4608\n\t"
      "s_waitcnt vmcnt(4)\n\t"
      "ds_read_b64_tr_b16 v[96:97], v242 offset:8192\n\t"
      "ds_read_b64_tr_b16 v[98:99], v242 offset:8704\n\t"
      "ds_read_b64_tr_b16 v[100:101], v251 offset:8192\n\t"
      "ds_read_b64_tr_b16 v[102:103], v251 offset:8704\n\t"
      "ds_read_b64_tr_b16 v[104:105], v252 offset:8192\n\t"
      "ds_read_b64_tr_b16 v[106:107], v252 offset:8704\n\t"
      "ds_read_b64_tr_b16 v[108:109], v253 offset:8192\n\t"
      "ds_read_b64_tr_b16 v[110:111], v253 offset:8704\n\t"
      "s_waitcnt vmcnt(0)\n\t"
      "ds_read_b64_tr_b16 v[112:113], v242 offset:12288\n\t"
      "ds_read_b64_tr_b16 v[114:115], v242 offset:12800\n\t"
      "ds_read_b64_tr_b16 v[116:117], v251 offset:12288\n\t"
      "ds_read_b64_tr_b16 v[118:119], v251 offset:12800\n\t"
      "ds_read_b64_tr_b16 v[120:121], v252 offset:12288\n\t"
      "ds_read_b64_tr_b16 v[122:123], v252 offset:12800\n\t"
      "ds_read_b64_tr_b16 v[124:125], v253 offset:12288\n\t"
      "ds_read_b64_tr_b16 v[126:127], v253 offset:12800\n\t"
      "s_waitcnt lgkmcnt(0)\n\t"
      "s_barrier\n\t"
      "s_mov_b32 s51, 0\n\t"
      "s_branch PRE_%=\n\t"
      "PRL_%=:\n\t"
      "s_sleep 4\n\t"
      "s_add_u32 s51, s51, 1\n\t"
      "s_cmp_lt_u32 s51, 262144\n\t"
      "s_cbranch_scc0 PRX_%=\n\t"
      "PRE_%=:\n\t"
      "s_mov_b64 exec, 65535\n\t"
      "s_bitcmp1_b32 s16, 4\n\t"
      "s_cbranch_scc1 PM_%=\n\t"
      "global_load_dwordx2 v[232:233], v247, s[12:13] sc1\n\t"
      "s_waitcnt vmcnt(0)\n\t"
      "PM_%=:\n\t"
      "s_cbranch_scc0 PN_%=\n\t"
      "v_mov_b64_e32 v[232:233], 0\n\t"
      "global_atomic_or_x2 v[232:233], v247, v[232:233], s[12:13] sc0 sc1\n\t"
      "s_waitcnt vmcnt(0)\n\t"
      "PN_%=:\n\t"
      "v_subrev_u32_e32 v232, s14, v232\n\t"
      "v_cmp_ne_u32_e64 s[48:49], s15, v233\n\t"
      "v_subrev_u32_e32 v232, 0, v232\n\t"
      "v_cmp_lt_u32_e32 vcc, 7, v232\n\t"
      "s_mov_b64 exec, -1\n\t"
      "s_or_b64 vcc, vcc, s[48:49]\n\t"
      "s_cbranch_vccnz PRL_%=\n\t"
      "PRX_%=:\n\t"
      "PRY_%=:\n\t"
      "global_load_dwordx4 v[128:131], v176, s[52:53] sc1\n\t"
      "global_load_dwordx4 v[132:135], v177, s[52:53] sc1\n\t"
      "global_load_dwordx4 v[136:139], v178, s[52:53] sc1\n\t"
      "global_load_dwordx4 v[140:143], v179, s[52:53] sc1\n\t"
      "global_load_dwordx4 v[144:147], v180, s[52:53] sc1\n\t"
      "global_load_dwordx4 v[148:151], v181, s[52:53] sc1\n\t"
      "global_load_dwordx4 v[152:155], v182, s[52:53] sc1\n\t"
      "global_load_dwordx4 v[156:159], v183, s[52:53] sc1\n\t"
      "s_nop 7\n\t"
      "s_cmp_ge_u32 s17, 65536\n\t"
      "s_cbranch_scc1 GQ_%=\n\t"
      ".p2align 6\n\t"
      "s_setprio 1\n\t"
      "v_mov_b32_e32 v232, s14\n\t"
      "v_mov_b32_e32 v233, s15\n\t"
      "v_lshl_add_u32 v247, v240, 10, s22\n\t"
      "s_waitcnt vmcnt(7)\n\t"
      "v_max3_u32 v234, v128, v129, v130\n\t"
      "v_max_u32_e32 v234, v234, v131\n\t"
      "v_cmp_eq_u32_e32 vcc, -1, v234\n\t"
      "s_cbranch_vccnz FP0R_%=\n\t"
      "FP0B_%=:\n\t"
      "v_mfma_f32_16x16x32_bf16 v[184:187], v[0:3], v[128:131], 0\n\t"
      "s_waitcnt vmcnt(6)\n\t"
      "v_max3_u32 v234, v132, v133, v134\n\t"
      "v_max_u32_e32 v234, v234, v135\n\t"
      "v_cmp_eq_u32_e32 vcc, -1, v234\n\t"
      "s_cbranch_vccnz FP1R_%=\n\t"
      "FP1B_%=:\n\t"
      "v_mfma_f32_16x16x32_bf16 v[184:187], v[16:19], v[132:135], v[184:187]\n\t"
      "v_mfma_f32_16x16x32_bf16 v[188:191], v[4:7], v[128:131], 0\n\t"
      "v_mfma_f32_16x16x32_bf16 v[188:191], v[20:23], v[132:135], v[188:191]\n\t"
      "v_mfma_f32_16x16x32_bf16 v[192:195], v[8:11], v[128:131], 0\n\t"
      "v_mfma_f32_16x16x32_bf16 v[192:195], v[24:27], v[132:135], v[192:195]\n\t"
      "v_mfma_f32_16x16x32_bf16 v[196:199], v[12:15], v[128:131], 0\n\t"
      "global_load_dwordx4 v[128:131], v176, s[54:55] sc1\n\t"
      "v_mfma_f32_16x16x32_bf16 v[196:199], v[28:31], v[132:135], v[196:199]\n\t"
      "global_load_dwordx4 v[132:135], v177, s[54:55] sc1\n\t"
      "s_waitcnt vmcnt(7)\n\t"
      "v_max3_u32 v234, v136, v137, v138\n\t"
      "v_max_u32_e32 v234, v234, v139\n\t"
      "v_cmp_eq_u32_e32 vcc, -1, v234\n\t"
      "s_cbranch_vccnz FP2R_%=\n\t"
      "FP2B_%=:\n\t"
      "v_mfma_f32_16x16x32_bf16 v[184:187], v[32:35], v[136:139], v[184:187]\n\t"
      "s_waitcnt vmcnt(6)\n\t"
      "v_max3_u32 v234, v140, v141, v142\n\t"
      "v_max_u32_e32 v234, v234, v143\n\t"
      "v_cmp_eq_u32_e32 vcc, -1, v234\n\t"
      "s_cbranch_vccnz FP3R_%=\n\t"
      "FP3B_%=:\n\t"
      "v_mfma_f32_16x16x32_bf16 v[184:187], v[48:51], v[140:143], v[184:187]\n\t"
      "v_mfma_f32_16x16x32_bf16 v[188:191], v[36:39], v[136:139], v[188:191]\n\t"
      "v_mfma_f32_16x16x32_bf16 v[188:191], v[52:55], v[140:143], v[188:191]\n\t"
      "v_mfma_f32_16x16x32_bf16 v[192:195], v[40:43], v[136:139], v[192:195]\n\t"
      "v_mfma_f32_16x16x32_bf16 v[192:195], v[56:59], v[140:143], v[192:195]\n\t"
      "v_mfma_f32_16x16x32_bf16 v[196:199], v[44:47], v[136:139], v[196:199]\n\t"
      "global_load_dwordx4 v[136:139], v178, s[54:55] sc1\n\t"
      "v_mfma_f32_16x16x32_bf16 v[196:199], v[60:63], v[140:143], v[196:199]\n\t"
      "global_load_dwordx4 v[140:143], v179, s[54:55] sc1\n\t"
      "s_waitcnt vmcnt(7)\n\t"
      "v_max3_u32 v234, v144, v145, v146\n\t"
      "v_max_u32_e32 v234, v234, v147\n\t"
      "v_cmp_eq_u32_e32 vcc, -1, v234\n\t"
      "s_cbranch_vccnz FP4R_%=\n\t"
      "FP4B_%=:\n\t"
      "v_mfma_f32_16x16x32_bf16 v[184:187], v[64:67], v[144:147], v[184:187]\n\t"
      "s_waitcnt vmcnt(6)\n\t"
      "v_max3_u32 v234, v148, v149, v150\n\t"
      "v_max_u32_e32 v234, v234, v151\n\t"
      "v_cmp_eq_u32_e32 vcc, -1, v234\n\t"
      "s_cbranch_vccnz FP5R_%=\n\t"
      "FP5B_%=:\n\t"
      "v_mfma_f32_16x16x32_bf16 v[184:187], v[80:83], v[148:151], v[184:187]\n\t"
      "v_mfma_f32_16x16x32_bf16 v[188:191], v[68:71], v[144:147], v[188:191]\n\t"
      "v_mfma_f32_16x16x32_bf16 v[188:191], v[84:87], v[148:151], v[188:191]\n\t"
      "v_mfma_f32_16x16x32_bf16 v[192:195], v[72:75], v[144:147], v[192:195]\n\t"
      "v_mfma_f32_16x16x32_bf16 v[192:195], v[88:91], v[148:151], v[192:195]\n\t"
      "v_mfma_f32_16x16x32_bf16 v[196:199], v[76:79], v[144:147], v[196:199]\n\t"
      "global_load_dwordx4 v[144:147], v180, s[54:55] sc1\n\t"
      "v_mfma_f32_16x16x32_bf16 v[196:199], v[92:95], v[148:151], v[196:199]\n\t"
      "global_load_dwordx4 v[148:151], v181, s[54:55] sc1\n\t"
      "s_waitcnt vmcnt(7)\n\t"
      "v_max3_u32 v234, v152, v153, v154\n\t"
      "v_max_u32_e32 v234, v234, v155\n\t"
      "v_cmp_eq_u32_e32 vcc, -1, v234\n\t"
      "s_cbranch_vccnz FP6R_%=\n\t"
      "FP6B_%=:\n\t"
      "v_mfma_f32_16x16x32_bf16 v[184:187], v[96:99], v[152:155], v[184:187]\n\t"
      "s_waitcnt vmcnt(6)\n\t"
      "v_max3_u32 v234, v156, v157, v158\n\t"
      "v_max_u32_e32 v234, v234, v159\n\t"
      "v_cmp_eq_u32_e32 vcc, -1, v234\n\t"
      "s_cbranch_vccnz FP7R_%=\n\t"
      "FP7B_%=:\n\t"
      "v_mfma_f32_16x16x32_bf16 v[184:187], v[112:115], v[156:159], v[184:187]\n\t"
      "v_mfma_f32_16x16x32_bf16 v[188:191], v[100:103], v[152:155], v[188:191]\n\t"
      "v_mfma_f32_16x16x32_bf16 v[188:191], v[116:119], v[156:159], v[188:191]\n\t"
      "v_mfma_f32_16x16x32_bf16 v[192:195], v[104:107], v[152:155], v[192:195]\n\t"
      "v_mfma_f32_16x16x32_bf16 v[192:195], v[120:123], v[156:159], v[192:195]\n\t"
      "s_nop 3\n\t"
      "ds_write_b128 v243, v[184:187]\n\t"
      "v_mfma_f32_16x16x32_bf16 v[196:199], v[108:111], v[152:155], v[196:199]\n\t"
      "global_load_dwordx4 v[152:155], v182, s[54:55] sc1\n\t"
      "v_mfma_f32_16x16x32_bf16 v[196:199], v[124:127], v[156:159], v[196:199]\n\t"
      "global_load_dwordx4 v[156:159], v183, s[54:55] sc1\n\t"
      "ds_write_b128 v164, v[188:191]\n\t"
      "ds_write_b128 v165, v[192:195]\n\t"
      "s_nop 4\n\t"
      "ds_write_b128 v166, v[196:199]\n\t"
      "s_waitcnt lgkmcnt(0)\n\t"
      "s_barrier\n\t"
      "ds_read_b128 v[216:219], v244\n\t"
      "ds_read_b128 v[220:223], v244 offset:4096\n\t"
      "ds_read_b128 v[224:227], v244 offset:8192\n\t"
      "ds_read_b128 v[228:231], v244 offset:12288\n\t"
      "ds_read_b128 v[184:187], v244 offset:16384\n\t"
      "ds_read_b128 v[188:191], v244 offset:20480\n\t"
      "ds_read_b128 v[192:195], v244 offset:24576\n\t"
      "ds_read_b128 v[196:199], v244 offset:28672\n\t"
      "s_waitcnt lgkmcnt(6)\n\t"
      "v_add_f32_e32 v216, v216, v220\n\t"
      "v_add_f32_e32 v217, v217, v221\n\t"
      "v_add_f32_e32 v218, v218, v222\n\t"
      "v_add_f32_e32 v219, v219, v223\n\t"
      "s_waitcnt lgkmcnt(4)\n\t"
      "v_add_f32_e32 v224, v224, v228\n\t"
      "v_add_f32_e32 v225, v225, v229\n\t"
      "v_add_f32_e32 v226, v226, v230\n\t"
      "v_add_f32_e32 v227, v227, v231\n\t"
      "s_waitcnt lgkmcnt(2)\n\t"
      "v_add_f32_e32 v184, v184, v188\n\t"
      "v_add_f32_e32 v185, v185, v189\n\t"
      "v_add_f32_e32 v186, v186, v190\n\t"
      "v_add_f32_e32 v187, v187, v191\n\t"
      "v_add_f32_e32 v216, v216, v224\n\t"
      "v_add_f32_e32 v217, v217, v225\n\t"
      "v_add_f32_e32 v218, v218, v226\n\t"
      "v_add_f32_e32 v219, v219, v227\n\t"
      "s_waitcnt lgkmcnt(0)\n\t"
      "v_add_f32_e32 v192, v192, v196\n\t"
      "v_add_f32_e32 v193, v193, v197\n\t"
      "v_add_f32_e32 v194, v194, v198\n\t"
      "v_add_f32_e32 v195, v195, v199\n\t"
      "v_add_f32_e32 v184, v184, v192\n\t"
      "v_add_f32_e32 v185, v185, v193\n\t"
      "v_add_f32_e32 v186, v186, v194\n\t"
      "v_add_f32_e32 v187, v187, v195\n\t"
      "v_add_f32_e32 v216, v216, v184\n\t"
      "s_waitcnt vmcnt(7)\n\t"
      "v_mfma_f32_16x16x32_bf16 v[200:203], v[0:3], v[128:131], 0\n\t"
      "v_add_f32_e32 v217, v217, v185\n\t"
      "s_waitcnt vmcnt(6)\n\t"
      "v_mfma_f32_16x16x32_bf16 v[200:203], v[16:19], v[132:135], v[200:203]\n\t"
      "v_add_f32_e32 v218, v218, v186\n\t"
      "v_add_f32_e32 v219, v219, v187\n\t"
      "v_mfma_f32_16x16x32_bf16 v[204:207], v[4:7], v[128:131], 0\n\t"
      "v_mul_f32_e32 v220, v216, v216\n\t"
      "v_mul_f32_e32 v221, v217, v217\n\t"
      "v_mfma_f32_16x16x32_bf16 v[204:207], v[20:23], v[132:135], v[204:207]\n\t"
      "v_mul_f32_e32 v222, v218, v218\n\t"
      "v_mul_f32_e32 v223, v219, v219\n\t"
      "v_mfma_f32_16x16x32_bf16 v[208:211], v[8:11], v[128:131], 0\n\t"
      "v_fmaak_f32 v220, v235, v220, 0xc0135761\n\t"
      "v_fmaak_f32 v221, v235, v221, 0xc0135761\n\t"
      "v_mfma_f32_16x16x32_bf16 v[208:211], v[24:27], v[132:135], v[208:211]\n\t"
      "v_fmaak_f32 v222, v235, v222, 0xc0135761\n\t"
      "v_fmaak_f32 v223, v235, v223, 0xc0135761\n\t"
      "v_mfma_f32_16x16x32_bf16 v[212:215], v[12:15], v[128:131], 0\n\t"
      "global_load_dwordx4 v[128:131], v176, s[56:57] sc1\n\t"
      "v_mul_f32_e32 v220, v216, v220\n\t"
      "v_mfma_f32_16x16x32_bf16 v[212:215], v[28:31], v[132:135], v[212:215]\n\t"
      "global_load_dwordx4 v[132:135], v177, s[56:57] sc1\n\t"
      "s_waitcnt vmcnt(7)\n\t"
      "v_mfma_f32_16x16x32_bf16 v[200:203], v[32:35], v[136:139], v[200:203]\n\t"
      "v_mul_f32_e32 v221, v217, v221\n\t"
      "s_waitcnt vmcnt(6)\n\t"
      "v_mfma_f32_16x16x32_bf16 v[200:203], v[48:51], v[140:143], v[200:203]\n\t"
      "v_mul_f32_e32 v222, v218, v222\n\t"
      "v_mul_f32_e32 v223, v219, v223\n\t"
      "v_mfma_f32_16x16x32_bf16 v[204:207], v[36:39], v[136:139], v[204:207]\n\t"
      "v_exp_f32_e32 v220, v220\n\t"
      "v_mfma_f32_16x16x32_bf16 v[204:207], v[52:55], v[140:143], v[204:207]\n\t"
      "v_exp_f32_e32 v221, v221\n\t"
      "v_mfma_f32_16x16x32_bf16 v[208:211], v[40:43], v[136:139], v[208:211]\n\t"
      "v_exp_f32_e32 v222, v222\n\t"
      "v_mfma_f32_16x16x32_bf16 v[208:211], v[56:59], v[140:143], v[208:211]\n\t"
      "v_exp_f32_e32 v223, v223\n\t"
      "v_mfma_f32_16x16x32_bf16 v[212:215], v[44:47], v[136:139], v[212:215]\n\t"
      "global_load_dwordx4 v[136:139], v178, s[56:57] sc1\n\t"
      "v_add_f32_e32 v220, 1.0, v220\n\t"
      "v_mfma_f32_16x16x32_bf16 v[212:215], v[60:63], v[140:143], v[212:215]\n\t"
      "global_load_dwordx4 v[140:143], v179, s[56:57] sc1\n\t"
      "s_waitcnt vmcnt(7)\n\t"
      "v_mfma_f32_16x16x32_bf16 v[200:203], v[64:67], v[144:147], v[200:203]\n\t"
      "v_add_f32_e32 v221, 1.0, v221\n\t"
      "s_waitcnt vmcnt(6)\n\t"
      "v_mfma_f32_16x16x32_bf16 v[200:203], v[80:83], v[148:151], v[200:203]\n\t"
      "v_add_f32_e32 v222, 1.0, v222\n\t"
      "v_add_f32_e32 v223, 1.0, v223\n\t"
      "v_mfma_f32_16x16x32_bf16 v[204:207], v[68:71], v[144:147], v[204:207]\n\t"
      "v_rcp_f32_e32 v220, v220\n\t"
      "v_mfma_f32_16x16x32_bf16 v[204:207], v[84:87], v[148:151], v[204:207]\n\t"
      "v_rcp_f32_e32 v221, v221\n\t"
      "v_mfma_f32_16x16x32_bf16 v[208:211], v[72:75], v[144:147], v[208:211]\n\t"
      "v_rcp_f32_e32 v222, v222\n\t"
      "v_mfma_f32_16x16x32_bf16 v[208:211], v[88:91], v[148:151], v[208:211]\n\t"
      "v_rcp_f32_e32 v223, v223\n\t"
      "v_mfma_f32_16x16x32_bf16 v[212:215], v[76:79], v[144:147], v[212:215]\n\t"
      "global_load_dwordx4 v[144:147], v180, s[56:57] sc1\n\t"
      "v_mul_f32_e32 v216, v216, v220\n\t"
      "v_mfma_f32_16x16x32_bf16 v[212:215], v[92:95], v[148:151], v[212:215]\n\t"
      "global_load_dwordx4 v[148:151], v181, s[56:57] sc1\n\t"
      "s_waitcnt vmcnt(7)\n\t"
      "v_mfma_f32_16x16x32_bf16 v[200:203], v[96:99], v[152:155], v[200:203]\n\t"
      "v_mul_f32_e32 v217, v217, v221\n\t"
      "s_waitcnt vmcnt(6)\n\t"
      "v_mfma_f32_16x16x32_bf16 v[200:203], v[112:115], v[156:159], v[200:203]\n\t"
      "v_mul_f32_e32 v218, v218, v222\n\t"
      "v_mul_f32_e32 v219, v219, v223\n\t"
      "v_mfma_f32_16x16x32_bf16 v[204:207], v[100:103], v[152:155], v[204:207]\n\t"
      "v_cvt_pk_bf16_f32 v220, v216, v217\n\t"
      "v_cvt_pk_bf16_f32 v221, v218, v219\n\t"
      "v_mfma_f32_16x16x32_bf16 v[204:207], v[116:119], v[156:159], v[204:207]\n\t"
      "s_nop 1\n\t"
      "v_cmp_u_f32_e32 vcc, v200, v200\n\t"
      "v_min_u32_e32 v220, -2, v220\n\t"
      "v_mfma_f32_16x16x32_bf16 v[208:211], v[104:107], v[152:155], v[208:211]\n\t"
      "v_min_u32_e32 v221, -2, v221\n\t"
      "global_store_dwordx2 v245, v[220:221], s[8:9] sc1\n\t"
      "s_add_u32 s8, s8, 131072\n\t"
      "s_addc_u32 s9, s9, 0\n\t"
      "v_mfma_f32_16x16x32_bf16 v[208:211], v[120:123], v[156:159], v[208:211]\n\t"
      "s_cbranch_vccnz NP1R_%=\n\t"
      "NP1B_%=:\n\t"
      "ds_write_b128 v243, v[200:203] offset:32768\n\t"
      "v_mfma_f32_16x16x32_bf16 v[212:215], v[108:111], v[152:155], v[212:215]\n\t"
      "global_load_dwordx4 v[152:155], v182, s[56:57] sc1\n\t"
      "v_mfma_f32_16x16x32_bf16 v[212:215], v[124:127], v[156:159], v[212:215]\n\t"
      "global_load_dwordx4 v[156:159], v183, s[56:57] sc1\n\t"
      "ds_write_b128 v164, v[204:207] offset:32768\n\t"
      "s_nop 0\n\t"
      "ds_write_b128 v165, v[208:211] offset:32768\n\t"
      "s_nop 3\n\t"
      "ds_write_b128 v166, v[212:215] offset:32768\n\t"
      "s_waitcnt lgkmcnt(0)\n\t"
      "s_barrier\n\t"
      "ds_read_b128 v[216:219], v244 offset:32768\n\t"
      "ds_read_b128 v[220:223], v244 offset:36864\n\t"
      "ds_read_b128 v[224:227], v244 offset:40960\n\t"
      "ds_read_b128 v[228:231], v244 offset:45056\n\t"
      "ds_read_b128 v[200:203], v244 offset:49152\n\t"
      "ds_read_b128 v[204:207], v244 offset:53248\n\t"
      "ds_read_b128 v[208:211], v244 offset:57344\n\t"
      "ds_read_b128 v[212:215], v244 offset:61440\n\t"
      "s_waitcnt lgkmcnt(6)\n\t"
      "v_add_f32_e32 v216, v216, v220\n\t"
      "v_add_f32_e32 v217, v217, v221\n\t"
      "v_add_f32_e32 v218, v218, v222\n\t"
      "v_add_f32_e32 v219, v219, v223\n\t"
      "s_waitcnt lgkmcnt(4)\n\t"
      "v_add_f32_e32 v224, v224, v228\n\t"
      "v_add_f32_e32 v225, v225, v229\n\t"
      "v_add_f32_e32 v226, v226, v230\n\t"
      "v_add_f32_e32 v227, v227, v231\n\t"
      "s_waitcnt lgkmcnt(2)\n\t"
      "v_add_f32_e32 v200, v200, v204\n\t"
      "v_add_f32_e32 v201, v201, v205\n\t"
      "v_add_f32_e32 v202, v202, v206\n\t"
      "v_add_f32_e32 v203, v203, v207\n\t"
      "v_add_f32_e32 v216, v216, v224\n\t"
      "v_add_f32_e32 v217, v217, v225\n\t"
      "v_add_f32_e32 v218, v218, v226\n\t"
      "v_add_f32_e32 v219, v219, v227\n\t"
      "s_waitcnt lgkmcnt(0)\n\t"
      "v_add_f32_e32 v208, v208, v212\n\t"
      "v_add_f32_e32 v209, v209, v213\n\t"
      "v_add_f32_e32 v210, v210, v214\n\t"
      "v_add_f32_e32 v211, v211, v215\n\t"
      "v_add_f32_e32 v200, v200, v208\n\t"
      "v_add_f32_e32 v201, v201, v209\n\t"
      "v_add_f32_e32 v202, v202, v210\n\t"
      "v_add_f32_e32 v203, v203, v211\n\t"
      "v_add_f32_e32 v216, v216, v200\n\t"
      "s_waitcnt vmcnt(8)\n\t"
      "v_mfma_f32_16x16x32_bf16 v[184:187], v[0:3], v[128:131], 0\n\t"
      "v_add_f32_e32 v217, v217, v201\n\t"
      "s_cmp_eq_u32 s17, 0\n\t"
      "s_cbranch_scc0 HI0_%=\n\t"
      "s_mov_b64 exec, 0xf\n\t"
      "global_store_dwordx2 v247, v[232:233], s[12:13] sc1\n\t"
      "s_mov_b64 exec, -1\n\t"
      "s_nop 0\n\t"
      "v_add_u32_e32 v232, 6, v232\n\t"
      "HI0_%=:\n\t"
      "s_waitcnt vmcnt(7)\n\t"
      "v_mfma_f32_16x16x32_bf16 v[184:187], v[16:19], v[132:135], v[184:187]\n\t"
      "v_add_f32_e32 v218, v218, v202\n\t"
      "v_add_f32_e32 v219, v219, v203\n\t"
      "v_mfma_f32_16x16x32_bf16 v[188:191], v[4:7], v[128:131], 0\n\t"
      "v_mul_f32_e32 v220, v216, v216\n\t"
      "v_mul_f32_e32 v221, v217, v217\n\t"
      "v_mfma_f32_16x16x32_bf16 v[188:191], v[20:23], v[132:135], v[188:191]\n\t"
      "v_mul_f32_e32 v222, v218, v218\n\t"
      "v_mul_f32_e32 v223, v219, v219\n\t"
      "v_mfma_f32_16x16x32_bf16 v[192:195], v[8:11], v[128:131], 0\n\t"
      "v_fmaak_f32 v220, v235, v220, 0xc0135761\n\t"
      "v_fmaak_f32 v221, v235, v221, 0xc0135761\n\t"
      "v_mfma_f32_16x16x32_bf16 v[192:195], v[24:27], v[132:135], v[192:195]\n\t"
      "v_fmaak_f32 v222, v235, v222, 0xc0135761\n\t"
      "v_fmaak_f32 v223, v235, v223, 0xc0135761\n\t"
      "v_mfma_f32_16x16x32_bf16 v[196:199], v[12:15], v[128:131], 0\n\t"
      "global_load_dwordx4 v[128:131], v176, s[58:59] sc1\n\t"
      "v_mul_f32_e32 v220, v216, v220\n\t"
      "v_mfma_f32_16x16x32_bf16 v[196:199], v[28:31], v[132:135], v[196:199]\n\t"
      "global_load_dwordx4 v[132:135], v177, s[58:59] sc1\n\t"
      "s_waitcnt vmcnt(8)\n\t"
      "v_mfma_f32_16x16x32_bf16 v[184:187], v[32:35], v[136:139], v[184:187]\n\t"
      "v_mul_f32_e32 v221, v217, v221\n\t"
      "s_waitcnt vmcnt(7)\n\t"
      "v_mfma_f32_16x16x32_bf16 v[184:187], v[48:51], v[140:143], v[184:187]\n\t"
      "v_mul_f32_e32 v222, v218, v222\n\t"
      "v_mul_f32_e32 v223, v219, v223\n\t"
      "v_mfma_f32_16x16x32_bf16 v[188:191], v[36:39], v[136:139], v[188:191]\n\t"
      "v_exp_f32_e32 v220, v220\n\t"
      "v_mfma_f32_16x16x32_bf16 v[188:191], v[52:55], v[140:143], v[188:191]\n\t"
      "v_exp_f32_e32 v221, v221\n\t"
      "v_mfma_f32_16x16x32_bf16 v[192:195], v[40:43], v[136:139], v[192:195]\n\t"
      "v_exp_f32_e32 v222, v222\n\t"
      "v_mfma_f32_16x16x32_bf16 v[192:195], v[56:59], v[140:143], v[192:195]\n\t"
      "v_exp_f32_e32 v223, v223\n\t"
      "v_mfma_f32_16x16x32_bf16 v[196:199], v[44:47], v[136:139], v[196:199]\n\t"
      "global_load_dwordx4 v[136:139], v178, s[58:59] sc1\n\t"
      "v_add_f32_e32 v220, 1.0, v220\n\t"
      "v_mfma_f32_16x16x32_bf16 v[196:199], v[60:63], v[140:143], v[196:199]\n\t"
      "global_load_dwordx4 v[140:143], v179, s[58:59] sc1\n\t"
      "s_waitcnt vmcnt(8)\n\t"
      "v_mfma_f32_16x16x32_bf16 v[184:187], v[64:67], v[144:147], v[184:187]\n\t"
      "v_add_f32_e32 v221, 1.0, v221\n\t"
      "s_waitcnt vmcnt(7)\n\t"
      "v_mfma_f32_16x16x32_bf16 v[184:187], v[80:83], v[148:151], v[184:187]\n\t"
      "v_add_f32_e32 v222, 1.0, v222\n\t"
      "v_add_f32_e32 v223, 1.0, v223\n\t"
      "v_mfma_f32_16x16x32_bf16 v[188:191], v[68:71], v[144:147], v[188:191]\n\t"
      "v_rcp_f32_e32 v220, v220\n\t"
      "v_mfma_f32_16x16x32_bf16 v[188:191], v[84:87], v[148:151], v[188:191]\n\t"
      "v_rcp_f32_e32 v221, v221\n\t"
      "v_mfma_f32_16x16x32_bf16 v[192:195], v[72:75], v[144:147], v[192:195]\n\t"
      "v_rcp_f32_e32 v222, v222\n\t"
      "v_mfma_f32_16x16x32_bf16 v[192:195], v[88:91], v[148:151], v[192:195]\n\t"
      "v_rcp_f32_e32 v223, v223\n\t"
      "v_mfma_f32_16x16x32_bf16 v[196:199], v[76:79], v[144:147], v[196:199]\n\t"
      "global_load_dwordx4 v[144:147], v180, s[58:59] sc1\n\t"
      "v_mul_f32_e32 v216, v216, v220\n\t"
      "v_mfma_f32_16x16x32_bf16 v[196:199], v[92:95], v[148:151], v[196:199]\n\t"
      "global_load_dwordx4 v[148:151], v181, s[58:59] sc1\n\t"
      "s_waitcnt vmcnt(7)\n\t"
      "v_mfma_f32_16x16x32_bf16 v[184:187], v[96:99], v[152:155], v[184:187]\n\t"
      "v_mul_f32_e32 v217, v217, v221\n\t"
      "s_waitcnt vmcnt(6)\n\t"
      "v_mfma_f32_16x16x32_bf16 v[184:187], v[112:115], v[156:159], v[184:187]\n\t"
      "v_mul_f32_e32 v218, v218, v222\n\t"
      "v_mul_f32_e32 v219, v219, v223\n\t"
      "v_mfma_f32_16x16x32_bf16 v[188:191], v[100:103], v[152:155], v[188:191]\n\t"
      "v_cvt_pk_bf16_f32 v220, v216, v217\n\t"
      "v_cvt_pk_bf16_f32 v221, v218, v219\n\t"
      "v_mfma_f32_16x16x32_bf16 v[188:191], v[116:119], v[156:159], v[188:191]\n\t"
      "s_nop 1\n\t"
      "v_cmp_u_f32_e32 vcc, v184, v184\n\t"
      "v_min_u32_e32 v220, -2, v220\n\t"
      "v_mfma_f32_16x16x32_bf16 v[192:195], v[104:107], v[152:155], v[192:195]\n\t"
      "v_min_u32_e32 v221, -2, v221\n\t"
      "global_store_dwordx2 v245, v[220:221], s[8:9] sc1\n\t"
      "s_add_u32 s8, s8, 131072\n\t"
      "s_addc_u32 s9, s9, 0\n\t"
      "v_mfma_f32_16x16x32_bf16 v[192:195], v[120:123], v[156:159], v[192:195]\n\t"
      "s_cbranch_vccnz NP2R_%=\n\t"
      "NP2B_%=:\n\t"
      "ds_write_b128 v243, v[184:187]\n\t"
      "v_mfma_f32_16x16x32_bf16 v[196:199], v[108:111], v[152:155], v[196:199]\n\t"
      "global_load_dwordx4 v[152:155], v182, s[58:59] sc1\n\t"
      "v_mfma_f32_16x16x32_bf16 v[196:199], v[124:127], v[156:159], v[196:199]\n\t"
      "global_load_dwordx4 v[156:159], v183, s[58:59] sc1\n\t"
      "ds_write_b128 v164, v[188:191]\n\t"
      "s_nop 0\n\t"
      "ds_write_b128 v165, v[192:195]\n\t"
      "s_nop 3\n\t"
      "ds_write_b128 v166, v[196:199]\n\t"
      "s_waitcnt lgkmcnt(0)\n\t"
      "s_barrier\n\t"
      "ds_read_b128 v[216:219], v244\n\t"
      "ds_read_b128 v[220:223], v244 offset:4096\n\t"
      "ds_read_b128 v[224:227], v244 offset:8192\n\t"
      "ds_read_b128 v[228:231], v244 offset:12288\n\t"
      "ds_read_b128 v[184:187], v244 offset:16384\n\t"
      "ds_read_b128 v[188:191], v244 offset:20480\n\t"
      "ds_read_b128 v[192:195], v244 offset:24576\n\t"
      "ds_read_b128 v[196:199], v244 offset:28672\n\t"
      "s_waitcnt lgkmcnt(6)\n\t"
      "v_add_f32_e32 v216, v216, v220\n\t"
      "v_add_f32_e32 v217, v217, v221\n\t"
      "v_add_f32_e32 v218, v218, v222\n\t"
      "v_add_f32_e32 v219, v219, v223\n\t"
      "s_waitcnt lgkmcnt(4)\n\t"
      "v_add_f32_e32 v224, v224, v228\n\t"
      "v_add_f32_e32 v225, v225, v229\n\t"
      "v_add_f32_e32 v226, v226, v230\n\t"
      "v_add_f32_e32 v227, v227, v231\n\t"
      "s_waitcnt lgkmcnt(2)\n\t"
      "v_add_f32_e32 v184, v184, v188\n\t"
      "v_add_f32_e32 v185, v185, v189\n\t"
      "v_add_f32_e32 v186, v186, v190\n\t"
      "v_add_f32_e32 v187, v187, v191\n\t"
      "v_add_f32_e32 v216, v216, v224\n\t"
      "v_add_f32_e32 v217, v217, v225\n\t"
      "v_add_f32_e32 v218, v218, v226\n\t"
      "v_add_f32_e32 v219, v219, v227\n\t"
      "s_waitcnt lgkmcnt(0)\n\t"
      "v_add_f32_e32 v192, v192, v196\n\t"
      "v_add_f32_e32 v193, v193, v197\n\t"
      "v_add_f32_e32 v194, v194, v198\n\t"
      "v_add_f32_e32 v195, v195, v199\n\t"
      "v_add_f32_e32 v184, v184, v192\n\t"
      "v_add_f32_e32 v185, v185, v193\n\t"
      "v_add_f32_e32 v186, v186, v194\n\t"
      "v_add_f32_e32 v187, v187, v195\n\t"
      "v_add_f32_e32 v216, v216, v184\n\t"
      "s_waitcnt vmcnt(8)\n\t"
      "v_mfma_f32_16x16x32_bf16 v[200:203], v[0:3], v[128:131], 0\n\t"
      "v_add_f32_e32 v217, v217, v185\n\t"
      "s_waitcnt vmcnt(7)\n\t"
      "v_mfma_f32_16x16x32_bf16 v[200:203], v[16:19], v[132:135], v[200:203]\n\t"
      "v_add_f32_e32 v218, v218, v186\n\t"
      "v_add_f32_e32 v219, v219, v187\n\t"
      "v_mfma_f32_16x16x32_bf16 v[204:207], v[4:7], v[128:131], 0\n\t"
      "v_mul_f32_e32 v220, v216, v216\n\t"
      "v_mul_f32_e32 v221, v217, v217\n\t"
      "v_mfma_f32_16x16x32_bf16 v[204:207], v[20:23], v[132:135], v[204:207]\n\t"
      "v_mul_f32_e32 v222, v218, v218\n\t"
      "v_mul_f32_e32 v223, v219, v219\n\t"
      "v_mfma_f32_16x16x32_bf16 v[208:211], v[8:11], v[128:131], 0\n\t"
      "v_fmaak_f32 v220, v235, v220, 0xc0135761\n\t"
      "v_fmaak_f32 v221, v235, v221, 0xc0135761\n\t"
      "v_mfma_f32_16x16x32_bf16 v[208:211], v[24:27], v[132:135], v[208:211]\n\t"
      "v_fmaak_f32 v222, v235, v222, 0xc0135761\n\t"
      "v_fmaak_f32 v223, v235, v223, 0xc0135761\n\t"
      "v_mfma_f32_16x16x32_bf16 v[212:215], v[12:15], v[128:131], 0\n\t"
      "global_load_dwordx4 v[128:131], v176, s[60:61] sc1\n\t"
      "v_mul_f32_e32 v220, v216, v220\n\t"
      "v_mfma_f32_16x16x32_bf16 v[212:215], v[28:31], v[132:135], v[212:215]\n\t"
      "global_load_dwordx4 v[132:135], v177, s[60:61] sc1\n\t"
      "s_waitcnt vmcnt(8)\n\t"
      "v_mfma_f32_16x16x32_bf16 v[200:203], v[32:35], v[136:139], v[200:203]\n\t"
      "v_mul_f32_e32 v221, v217, v221\n\t"
      "s_waitcnt vmcnt(7)\n\t"
      "v_mfma_f32_16x16x32_bf16 v[200:203], v[48:51], v[140:143], v[200:203]\n\t"
      "v_mul_f32_e32 v222, v218, v222\n\t"
      "v_mul_f32_e32 v223, v219, v223\n\t"
      "v_mfma_f32_16x16x32_bf16 v[204:207], v[36:39], v[136:139], v[204:207]\n\t"
      "v_exp_f32_e32 v220, v220\n\t"
      "v_mfma_f32_16x16x32_bf16 v[204:207], v[52:55], v[140:143], v[204:207]\n\t"
      "v_exp_f32_e32 v221, v221\n\t"
      "v_mfma_f32_16x16x32_bf16 v[208:211], v[40:43], v[136:139], v[208:211]\n\t"
      "v_exp_f32_e32 v222, v222\n\t"
      "v_mfma_f32_16x16x32_bf16 v[208:211], v[56:59], v[140:143], v[208:211]\n\t"
      "v_exp_f32_e32 v223, v223\n\t"
      "v_mfma_f32_16x16x32_bf16 v[212:215], v[44:47], v[136:139], v[212:215]\n\t"
      "global_load_dwordx4 v[136:139], v178, s[60:61] sc1\n\t"
      "v_add_f32_e32 v220, 1.0, v220\n\t"
      "v_mfma_f32_16x16x32_bf16 v[212:215], v[60:63], v[140:143], v[212:215]\n\t"
      "global_load_dwordx4 v[140:143], v179, s[60:61] sc1\n\t"
      "s_waitcnt vmcnt(8)\n\t"
      "v_mfma_f32_16x16x32_bf16 v[200:203], v[64:67], v[144:147], v[200:203]\n\t"
      "v_add_f32_e32 v221, 1.0, v221\n\t"
      "s_waitcnt vmcnt(7)\n\t"
      "v_mfma_f32_16x16x32_bf16 v[200:203], v[80:83], v[148:151], v[200:203]\n\t"
      "v_add_f32_e32 v222, 1.0, v222\n\t"
      "v_add_f32_e32 v223, 1.0, v223\n\t"
      "v_mfma_f32_16x16x32_bf16 v[204:207], v[68:71], v[144:147], v[204:207]\n\t"
      "v_rcp_f32_e32 v220, v220\n\t"
      "v_mfma_f32_16x16x32_bf16 v[204:207], v[84:87], v[148:151], v[204:207]\n\t"
      "v_rcp_f32_e32 v221, v221\n\t"
      "v_mfma_f32_16x16x32_bf16 v[208:211], v[72:75], v[144:147], v[208:211]\n\t"
      "v_rcp_f32_e32 v222, v222\n\t"
      "v_mfma_f32_16x16x32_bf16 v[208:211], v[88:91], v[148:151], v[208:211]\n\t"
      "v_rcp_f32_e32 v223, v223\n\t"
      "v_mfma_f32_16x16x32_bf16 v[212:215], v[76:79], v[144:147], v[212:215]\n\t"
      "global_load_dwordx4 v[144:147], v180, s[60:61] sc1\n\t"
      "v_mul_f32_e32 v216, v216, v220\n\t"
      "v_mfma_f32_16x16x32_bf16 v[212:215], v[92:95], v[148:151], v[212:215]\n\t"
      "global_load_dwordx4 v[148:151], v181, s[60:61] sc1\n\t"
      "s_waitcnt vmcnt(7)\n\t"
      "v_mfma_f32_16x16x32_bf16 v[200:203], v[96:99], v[152:155], v[200:203]\n\t"
      "v_mul_f32_e32 v217, v217, v221\n\t"
      "s_waitcnt vmcnt(6)\n\t"
      "v_mfma_f32_16x16x32_bf16 v[200:203], v[112:115], v[156:159], v[200:203]\n\t"
      "v_mul_f32_e32 v218, v218, v222\n\t"
      "v_mul_f32_e32 v219, v219, v223\n\t"
      "v_mfma_f32_16x16x32_bf16 v[204:207], v[100:103], v[152:155], v[204:207]\n\t"
      "v_cvt_pk_bf16_f32 v220, v216, v217\n\t"
      "v_cvt_pk_bf16_f32 v221, v218, v219\n\t"
      "v_mfma_f32_16x16x32_bf16 v[204:207], v[116:119], v[156:159], v[204:207]\n\t"
      "s_nop 1\n\t"
      "v_cmp_u_f32_e32 vcc, v200, v200\n\t"
      "v_min_u32_e32 v220, -2, v220\n\t"
      "v_mfma_f32_16x16x32_bf16 v[208:211], v[104:107], v[152:155], v[208:211]\n\t"
      "v_min_u32_e32 v221, -2, v221\n\t"
      "global_store_dwordx2 v245, v[220:221], s[8:9] sc1\n\t"
      "s_add_u32 s8, s8, 131072\n\t"
      "s_addc_u32 s9, s9, 0\n\t"
      "v_mfma_f32_16x16x32_bf16 v[208:211], v[120:123], v[156:159], v[208:211]\n\t"
      "s_cbranch_vccnz NP3R_%=\n\t"
      "NP3B_%=:\n\t"
      "ds_write_b128 v243, v[200:203] offset:32768\n\t"
      "v_mfma_f32_16x16x32_bf16 v[212:215], v[108:111], v[152:155], v[212:215]\n\t"
      "global_load_dwordx4 v[152:155], v182, s[60:61] sc1\n\t"
      "v_mfma_f32_16x16x32_bf16 v[212:215], v[124:127], v[156:159], v[212:215]\n\t"
      "global_load_dwordx4 v[156:159], v183, s[60:61] sc1\n\t"
      "ds_write_b128 v164, v[204:207] offset:32768\n\t"
      "s_nop 0\n\t"
      "ds_write_b128 v165, v[208:211] offset:32768\n\t"
      "s_nop 3\n\t"
      "ds_write_b128 v166, v[212:215] offset:32768\n\t"
      "s_waitcnt lgkmcnt(0)\n\t"
      "s_barrier\n\t"
      "ds_read_b128 v[216:219], v244 offset:32768\n\t"
      "ds_read_b128 v[220:223], v244 offset:36864\n\t"
      "ds_read_b128 v[224:227], v244 offset:40960\n\t"
      "ds_read_b128 v[228:231], v244 offset:45056\n\t"
      "ds_read_b128 v[200:203], v244 offset:49152\n\t"
      "ds_read_b128 v[204:207], v244 offset:53248\n\t"
      "ds_read_b128 v[208:211], v244 offset:57344\n\t"
      "ds_read_b128 v[212:215], v244 offset:61440\n\t"
      "s_waitcnt lgkmcnt(6)\n\t"
      "v_add_f32_e32 v216, v216, v220\n\t"
      "v_add_f32_e32 v217, v217, v221\n\t"
      "v_add_f32_e32 v218, v218, v222\n\t"
      "v_add_f32_e32 v219, v219, v223\n\t"
      "s_waitcnt lgkmcnt(4)\n\t"
      "v_add_f32_e32 v224, v224, v228\n\t"
      "v_add_f32_e32 v225, v225, v229\n\t"
      "v_add_f32_e32 v226, v226, v230\n\t"
      "v_add_f32_e32 v227, v227, v231\n\t"
      "s_waitcnt lgkmcnt(2)\n\t"
      "v_add_f32_e32 v200, v200, v204\n\t"
      "v_add_f32_e32 v201, v201, v205\n\t"
      "v_add_f32_e32 v202, v202, v206\n\t"
      "v_add_f32_e32 v203, v203, v207\n\t"
      "v_add_f32_e32 v216, v216, v224\n\t"
      "v_add_f32_e32 v217, v217, v225\n\t"
      "v_add_f32_e32 v218, v218, v226\n\t"
      "v_add_f32_e32 v219, v219, v227\n\t"
      "s_waitcnt lgkmcnt(0)\n\t"
      "v_add_f32_e32 v208, v208, v212\n\t"
      "v_add_f32_e32 v209, v209, v213\n\t"
      "v_add_f32_e32 v210, v210, v214\n\t"
      "v_add_f32_e32 v211, v211, v215\n\t"
      "v_add_f32_e32 v200, v200, v208\n\t"
      "v_add_f32_e32 v201, v201, v209\n\t"
      "v_add_f32_e32 v202, v202, v210\n\t"
      "v_add_f32_e32 v203, v203, v211\n\t"
      "v_add_f32_e32 v216, v216, v200\n\t"
      "s_waitcnt vmcnt(8)\n\t"
      "v_mfma_f32_16x16x32_bf16 v[184:187], v[0:3], v[128:131], 0\n\t"
      "v_add_f32_e32 v217, v217, v201\n\t"
      "s_waitcnt vmcnt(7)\n\t"
      "v_mfma_f32_16x16x32_bf16 v[184:187], v[16:19], v[132:135], v[184:187]\n\t"
      "v_add_f32_e32 v218, v218, v202\n\t"
      "v_add_f32_e32 v219, v219, v203\n\t"
      "v_mfma_f32_16x16x32_bf16 v[188:191], v[4:7], v[128:131], 0\n\t"
      "v_mul_f32_e32 v220, v216, v216\n\t"
      "v_mul_f32_e32 v221, v217, v217\n\t"
      "v_mfma_f32_16x16x32_bf16 v[188:191], v[20:23], v[132:135], v[188:191]\n\t"
      "v_mul_f32_e32 v222, v218, v218\n\t"
      "v_mul_f32_e32 v223, v219, v219\n\t"
      "v_mfma_f32_16x16x32_bf16 v[192:195], v[8:11], v[128:131], 0\n\t"
      "v_fmaak_f32 v220, v235, v220, 0xc0135761\n\t"
      "v_fmaak_f32 v221, v235, v221, 0xc0135761\n\t"
      "v_mfma_f32_16x16x32_bf16 v[192:195], v[24:27], v[132:135], v[192:195]\n\t"
      "v_fmaak_f32 v222, v235, v222, 0xc0135761\n\t"
      "v_fmaak_f32 v223, v235, v223, 0xc0135761\n\t"
      "v_mfma_f32_16x16x32_bf16 v[196:199], v[12:15], v[128:131], 0\n\t"
      "global_load_dwordx4 v[128:131], v176, s[62:63] sc1\n\t"
      "v_mul_f32_e32 v220, v216, v220\n\t"
      "v_mfma_f32_16x16x32_bf16 v[196:199], v[28:31], v[132:135], v[196:199]\n\t"
      "global_load_dwordx4 v[132:135], v177, s[62:63] sc1\n\t"
      "s_waitcnt vmcnt(8)\n\t"
      "v_mfma_f32_16x16x32_bf16 v[184:187], v[32:35], v[136:139], v[184:187]\n\t"
      "v_mul_f32_e32 v221, v217, v221\n\t"
      "s_waitcnt vmcnt(7)\n\t"
      "v_mfma_f32_16x16x32_bf16 v[184:187], v[48:51], v[140:143], v[184:187]\n\t"
      "v_mul_f32_e32 v222, v218, v222\n\t"
      "v_mul_f32_e32 v223, v219, v223\n\t"
      "v_mfma_f32_16x16x32_bf16 v[188:191], v[36:39], v[136:139], v[188:191]\n\t"
      "v_exp_f32_e32 v220, v220\n\t"
      "v_mfma_f32_16x16x32_bf16 v[188:191], v[52:55], v[140:143], v[188:191]\n\t"
      "v_exp_f32_e32 v221, v221\n\t"
      "v_mfma_f32_16x16x32_bf16 v[192:195], v[40:43], v[136:139], v[192:195]\n\t"
      "v_exp_f32_e32 v222, v222\n\t"
      "v_mfma_f32_16x16x32_bf16 v[192:195], v[56:59], v[140:143], v[192:195]\n\t"
      "v_exp_f32_e32 v223, v223\n\t"
      "v_mfma_f32_16x16x32_bf16 v[196:199], v[44:47], v[136:139], v[196:199]\n\t"
      "global_load_dwordx4 v[136:139], v178, s[62:63] sc1\n\t"
      "v_add_f32_e32 v220, 1.0, v220\n\t"
      "v_mfma_f32_16x16x32_bf16 v[196:199], v[60:63], v[140:143], v[196:199]\n\t"
      "global_load_dwordx4 v[140:143], v179, s[62:63] sc1\n\t"
      "s_waitcnt vmcnt(8)\n\t"
      "v_mfma_f32_16x16x32_bf16 v[184:187], v[64:67], v[144:147], v[184:187]\n\t"
      "v_add_f32_e32 v221, 1.0, v221\n\t"
      "s_waitcnt vmcnt(7)\n\t"
      "v_mfma_f32_16x16x32_bf16 v[184:187], v[80:83], v[148:151], v[184:187]\n\t"
      "v_add_f32_e32 v222, 1.0, v222\n\t"
      "v_add_f32_e32 v223, 1.0, v223\n\t"
      "v_mfma_f32_16x16x32_bf16 v[188:191], v[68:71], v[144:147], v[188:191]\n\t"
      "v_rcp_f32_e32 v220, v220\n\t"
      "v_mfma_f32_16x16x32_bf16 v[188:191], v[84:87], v[148:151], v[188:191]\n\t"
      "v_rcp_f32_e32 v221, v221\n\t"
      "v_mfma_f32_16x16x32_bf16 v[192:195], v[72:75], v[144:147], v[192:195]\n\t"
      "v_rcp_f32_e32 v222, v222\n\t"
      "v_mfma_f32_16x16x32_bf16 v[192:195], v[88:91], v[148:151], v[192:195]\n\t"
      "v_rcp_f32_e32 v223, v223\n\t"
      "v_mfma_f32_16x16x32_bf16 v[196:199], v[76:79], v[144:147], v[196:199]\n\t"
      "global_load_dwordx4 v[144:147], v180, s[62:63] sc1\n\t"
      "v_mul_f32_e32 v216, v216, v220\n\t"
      "v_mfma_f32_16x16x32_bf16 v[196:199], v[92:95], v[148:151], v[196:199]\n\t"
      "global_load_dwordx4 v[148:151], v181, s[62:63] sc1\n\t"
      "s_waitcnt vmcnt(7)\n\t"
      "v_mfma_f32_16x16x32_bf16 v[184:187], v[96:99], v[152:155], v[184:187]\n\t"
      "v_mul_f32_e32 v217, v217, v221\n\t"
      "s_waitcnt vmcnt(6)\n\t"
      "v_mfma_f32_16x16x32_bf16 v[184:187], v[112:115], v[156:159], v[184:187]\n\t"
      "v_mul_f32_e32 v218, v218, v222\n\t"
      "v_mul_f32_e32 v219, v219, v223\n\t"
      "v_mfma_f32_16x16x32_bf16 v[188:191], v[100:103], v[152:155], v[188:191]\n\t"
      "v_cvt_pk_bf16_f32 v220, v216, v217\n\t"
      "v_cvt_pk_bf16_f32 v221, v218, v219\n\t"
      "v_mfma_f32_16x16x32_bf16 v[188:191], v[116:119], v[156:159], v[188:191]\n\t"
      "s_nop 1\n\t"
      "v_cmp_u_f32_e32 vcc, v184, v184\n\t"
      "v_min_u32_e32 v220, -2, v220\n\t"
      "v_mfma_f32_16x16x32_bf16 v[192:195], v[104:107], v[152:155], v[192:195]\n\t"
      "v_min_u32_e32 v221, -2, v221\n\t"
      "global_store_dwordx2 v245, v[220:221], s[8:9] sc1\n\t"
      "s_add_u32 s8, s8, 131072\n\t"
      "s_addc_u32 s9, s9, 0\n\t"
      "v_mfma_f32_16x16x32_bf16 v[192:195], v[120:123], v[156:159], v[192:195]\n\t"
      "s_cbranch_vccnz NP4R_%=\n\t"
      "NP4B_%=:\n\t"
      "ds_write_b128 v243, v[184:187]\n\t"
      "v_mfma_f32_16x16x32_bf16 v[196:199], v[108:111], v[152:155], v[196:199]\n\t"
      "global_load_dwordx4 v[152:155], v182, s[62:63] sc1\n\t"
      "v_mfma_f32_16x16x32_bf16 v[196:199], v[124:127], v[156:159], v[196:199]\n\t"
      "global_load_dwordx4 v[156:159], v183, s[62:63] sc1\n\t"
      "ds_write_b128 v164, v[188:191]\n\t"
      "s_nop 0\n\t"
      "ds_write_b128 v165, v[192:195]\n\t"
      "s_nop 3\n\t"
      "ds_write_b128 v166, v[196:199]\n\t"
      "s_waitcnt lgkmcnt(0)\n\t"
      "s_barrier\n\t"
      "ds_read_b128 v[216:219], v244\n\t"
      "ds_read_b128 v[220:223], v244 offset:4096\n\t"
      "ds_read_b128 v[224:227], v244 offset:8192\n\t"
      "ds_read_b128 v[228:231], v244 offset:12288\n\t"
      "ds_read_b128 v[184:187], v244 offset:16384\n\t"
      "ds_read_b128 v[188:191], v244 offset:20480\n\t"
      "ds_read_b128 v[192:195], v244 offset:24576\n\t"
      "ds_read_b128 v[196:199], v244 offset:28672\n\t"
      "s_waitcnt lgkmcnt(6)\n\t"
      "v_add_f32_e32 v216, v216, v220\n\t"
      "v_add_f32_e32 v217, v217, v221\n\t"
      "v_add_f32_e32 v218, v218, v222\n\t"
      "v_add_f32_e32 v219, v219, v223\n\t"
      "s_waitcnt lgkmcnt(4)\n\t"
      "v_add_f32_e32 v224, v224, v228\n\t"
      "v_add_f32_e32 v225, v225, v229\n\t"
      "v_add_f32_e32 v226, v226, v230\n\t"
      "v_add_f32_e32 v227, v227, v231\n\t"
      "s_waitcnt lgkmcnt(2)\n\t"
      "v_add_f32_e32 v184, v184, v188\n\t"
      "v_add_f32_e32 v185, v185, v189\n\t"
      "v_add_f32_e32 v186, v186, v190\n\t"
      "v_add_f32_e32 v187, v187, v191\n\t"
      "v_add_f32_e32 v216, v216, v224\n\t"
      "v_add_f32_e32 v217, v217, v225\n\t"
      "v_add_f32_e32 v218, v218, v226\n\t"
      "v_add_f32_e32 v219, v219, v227\n\t"
      "s_waitcnt lgkmcnt(0)\n\t"
      "v_add_f32_e32 v192, v192, v196\n\t"
      "v_add_f32_e32 v193, v193, v197\n\t"
      "v_add_f32_e32 v194, v194, v198\n\t"
      "v_add_f32_e32 v195, v195, v199\n\t"
      "v_add_f32_e32 v184, v184, v192\n\t"
      "v_add_f32_e32 v185, v185, v193\n\t"
      "v_add_f32_e32 v186, v186, v194\n\t"
      "v_add_f32_e32 v187, v187, v195\n\t"
      "v_add_f32_e32 v216, v216, v184\n\t"
      "s_waitcnt vmcnt(8)\n\t"
      "v_mfma_f32_16x16x32_bf16 v[200:203], v[0:3], v[128:131], 0\n\t"
      "v_add_f32_e32 v217, v217, v185\n\t"
      "s_waitcnt vmcnt(7)\n\t"
      "v_mfma_f32_16x16x32_bf16 v[200:203], v[16:19], v[132:135], v[200:203]\n\t"
      "v_add_f32_e32 v218, v218, v186\n\t"
      "v_add_f32_e32 v219, v219, v187\n\t"
      "v_mfma_f32_16x16x32_bf16 v[204:207], v[4:7], v[128:131], 0\n\t"
      "v_mul_f32_e32 v220, v216, v216\n\t"
      "v_mul_f32_e32 v221, v217, v217\n\t"
      "v_mfma_f32_16x16x32_bf16 v[204:207], v[20:23], v[132:135], v[204:207]\n\t"
      "v_mul_f32_e32 v222, v218, v218\n\t"
      "v_mul_f32_e32 v223, v219, v219\n\t"
      "v_mfma_f32_16x16x32_bf16 v[208:211], v[8:11], v[128:131], 0\n\t"
      "v_fmaak_f32 v220, v235, v220, 0xc0135761\n\t"
      "v_fmaak_f32 v221, v235, v221, 0xc0135761\n\t"
      "v_mfma_f32_16x16x32_bf16 v[208:211], v[24:27], v[132:135], v[208:211]\n\t"
      "v_fmaak_f32 v222, v235, v222, 0xc0135761\n\t"
      "v_fmaak_f32 v223, v235, v223, 0xc0135761\n\t"
      "v_mfma_f32_16x16x32_bf16 v[212:215], v[12:15], v[128:131], 0\n\t"
      "global_load_dwordx4 v[128:131], v176, s[64:65] sc1\n\t"
      "v_mul_f32_e32 v220, v216, v220\n\t"
      "v_mfma_f32_16x16x32_bf16 v[212:215], v[28:31], v[132:135], v[212:215]\n\t"
      "global_load_dwordx4 v[132:135], v177, s[64:65] sc1\n\t"
      "s_waitcnt vmcnt(8)\n\t"
      "v_mfma_f32_16x16x32_bf16 v[200:203], v[32:35], v[136:139], v[200:203]\n\t"
      "v_mul_f32_e32 v221, v217, v221\n\t"
      "s_waitcnt vmcnt(7)\n\t"
      "v_mfma_f32_16x16x32_bf16 v[200:203], v[48:51], v[140:143], v[200:203]\n\t"
      "v_mul_f32_e32 v222, v218, v222\n\t"
      "v_mul_f32_e32 v223, v219, v223\n\t"
      "v_mfma_f32_16x16x32_bf16 v[204:207], v[36:39], v[136:139], v[204:207]\n\t"
      "v_exp_f32_e32 v220, v220\n\t"
      "v_mfma_f32_16x16x32_bf16 v[204:207], v[52:55], v[140:143], v[204:207]\n\t"
      "v_exp_f32_e32 v221, v221\n\t"
      "v_mfma_f32_16x16x32_bf16 v[208:211], v[40:43], v[136:139], v[208:211]\n\t"
      "v_exp_f32_e32 v222, v222\n\t"
      "v_mfma_f32_16x16x32_bf16 v[208:211], v[56:59], v[140:143], v[208:211]\n\t"
      "v_exp_f32_e32 v223, v223\n\t"
      "v_mfma_f32_16x16x32_bf16 v[212:215], v[44:47], v[136:139], v[212:215]\n\t"
      "global_load_dwordx4 v[136:139], v178, s[64:65] sc1\n\t"
      "v_add_f32_e32 v220, 1.0, v220\n\t"
      "v_mfma_f32_16x16x32_bf16 v[212:215], v[60:63], v[140:143], v[212:215]\n\t"
      "global_load_dwordx4 v[140:143], v179, s[64:65] sc1\n\t"
      "s_waitcnt vmcnt(8)\n\t"
      "v_mfma_f32_16x16x32_bf16 v[200:203], v[64:67], v[144:147], v[200:203]\n\t"
      "v_add_f32_e32 v221, 1.0, v221\n\t"
      "s_waitcnt vmcnt(7)\n\t"
      "v_mfma_f32_16x16x32_bf16 v[200:203], v[80:83], v[148:151], v[200:203]\n\t"
      "v_add_f32_e32 v222, 1.0, v222\n\t"
      "v_add_f32_e32 v223, 1.0, v223\n\t"
      "v_mfma_f32_16x16x32_bf16 v[204:207], v[68:71], v[144:147], v[204:207]\n\t"
      "v_rcp_f32_e32 v220, v220\n\t"
      "v_mfma_f32_16x16x32_bf16 v[204:207], v[84:87], v[148:151], v[204:207]\n\t"
      "v_rcp_f32_e32 v221, v221\n\t"
      "v_mfma_f32_16x16x32_bf16 v[208:211], v[72:75], v[144:147], v[208:211]\n\t"
      "v_rcp_f32_e32 v222, v222\n\t"
      "v_mfma_f32_16x16x32_bf16 v[208:211], v[88:91], v[148:151], v[208:211]\n\t"
      "v_rcp_f32_e32 v223, v223\n\t"
      "v_mfma_f32_16x16x32_bf16 v[212:215], v[76:79], v[144:147], v[212:215]\n\t"
      "global_load_dwordx4 v[144:147], v180, s[64:65] sc1\n\t"
      "v_mul_f32_e32 v216, v216, v220\n\t"
      "v_mfma_f32_16x16x32_bf16 v[212:215], v[92:95], v[148:151], v[212:215]\n\t"
      "global_load_dwordx4 v[148:151], v181, s[64:65] sc1\n\t"
      "s_waitcnt vmcnt(7)\n\t"
      "v_mfma_f32_16x16x32_bf16 v[200:203], v[96:99], v[152:155], v[200:203]\n\t"
      "v_mul_f32_e32 v217, v217, v221\n\t"
      "s_waitcnt vmcnt(6)\n\t"
      "v_mfma_f32_16x16x32_bf16 v[200:203], v[112:115], v[156:159], v[200:203]\n\t"
      "v_mul_f32_e32 v218, v218, v222\n\t"
      "v_mul_f32_e32 v219, v219, v223\n\t"
      "v_mfma_f32_16x16x32_bf16 v[204:207], v[100:103], v[152:155], v[204:207]\n\t"
      "v_cvt_pk_bf16_f32 v220, v216, v217\n\t"
      "v_cvt_pk_bf16_f32 v221, v218, v219\n\t"
      "v_mfma_f32_16x16x32_bf16 v[204:207], v[116:119], v[156:159], v[204:207]\n\t"
      "s_nop 1\n\t"
      "v_cmp_u_f32_e32 vcc, v200, v200\n\t"
      "v_min_u32_e32 v220, -2, v220\n\t"
      "v_mfma_f32_16x16x32_bf16 v[208:211], v[104:107], v[152:155], v[208:211]\n\t"
      "v_min_u32_e32 v221, -2, v221\n\t"
      "global_store_dwordx2 v245, v[220:221], s[8:9] sc1\n\t"
      "s_add_u32 s8, s8, 131072\n\t"
      "s_addc_u32 s9, s9, 0\n\t"
      "v_mfma_f32_16x16x32_bf16 v[208:211], v[120:123], v[156:159], v[208:211]\n\t"
      "s_cbranch_vccnz NP5R_%=\n\t"
      "NP5B_%=:\n\t"
      "ds_write_b128 v243, v[200:203] offset:32768\n\t"
      "v_mfma_f32_16x16x32_bf16 v[212:215], v[108:111], v[152:155], v[212:215]\n\t"
      "global_load_dwordx4 v[152:155], v182, s[64:65] sc1\n\t"
      "v_mfma_f32_16x16x32_bf16 v[212:215], v[124:127], v[156:159], v[212:215]\n\t"
      "global_load_dwordx4 v[156:159], v183, s[64:65] sc1\n\t"
      "ds_write_b128 v164, v[204:207] offset:32768\n\t"
      "s_nop 0\n\t"
      "ds_write_b128 v165, v[208:211] offset:32768\n\t"
      "s_nop 3\n\t"
      "ds_write_b128 v166, v[212:215] offset:32768\n\t"
      "s_waitcnt lgkmcnt(0)\n\t"
      "s_barrier\n\t"
      "ds_read_b128 v[216:219], v244 offset:32768\n\t"
      "ds_read_b128 v[220:223], v244 offset:36864\n\t"
      "ds_read_b128 v[224:227], v244 offset:40960\n\t"
      "ds_read_b128 v[228:231], v244 offset:45056\n\t"
      "ds_read_b128 v[200:203], v244 offset:49152\n\t"
      "ds_read_b128 v[204:207], v244 offset:53248\n\t"
      "ds_read_b128 v[208:211], v244 offset:57344\n\t"
      "ds_read_b128 v[212:215], v244 offset:61440\n\t"
      "s_waitcnt lgkmcnt(6)\n\t"
      "v_add_f32_e32 v216, v216, v220\n\t"
      "v_add_f32_e32 v217, v217, v221\n\t"
      "v_add_f32_e32 v218, v218, v222\n\t"
      "v_add_f32_e32 v219, v219, v223\n\t"
      "s_waitcnt lgkmcnt(4)\n\t"
      "v_add_f32_e32 v224, v224, v228\n\t"
      "v_add_f32_e32 v225, v225, v229\n\t"
      "v_add_f32_e32 v226, v226, v230\n\t"
      "v_add_f32_e32 v227, v227, v231\n\t"
      "s_waitcnt lgkmcnt(2)\n\t"
      "v_add_f32_e32 v200, v200, v204\n\t"
      "v_add_f32_e32 v201, v201, v205\n\t"
      "v_add_f32_e32 v202, v202, v206\n\t"
      "v_add_f32_e32 v203, v203, v207\n\t"
      "v_add_f32_e32 v216, v216, v224\n\t"
      "v_add_f32_e32 v217, v217, v225\n\t"
      "v_add_f32_e32 v218, v218, v226\n\t"
      "v_add_f32_e32 v219, v219, v227\n\t"
      "s_waitcnt lgkmcnt(0)\n\t"
      "v_add_f32_e32 v208, v208, v212\n\t"
      "v_add_f32_e32 v209, v209, v213\n\t"
      "v_add_f32_e32 v210, v210, v214\n\t"
      "v_add_f32_e32 v211, v211, v215\n\t"
      "v_add_f32_e32 v200, v200, v208\n\t"
      "s_waitcnt vmcnt(8)\n\t"
      "v_mfma_f32_16x16x32_bf16 v[184:187], v[0:3], v[128:131], 0\n\t"
      "v_add_f32_e32 v201, v201, v209\n\t"
      "s_waitcnt vmcnt(7)\n\t"
      "v_mfma_f32_16x16x32_bf16 v[184:187], v[16:19], v[132:135], v[184:187]\n\t"
      "v_add_f32_e32 v202, v202, v210\n\t"
      "v_add_f32_e32 v203, v203, v211\n\t"
      "v_mfma_f32_16x16x32_bf16 v[188:191], v[4:7], v[128:131], 0\n\t"
      "v_add_f32_e32 v216, v216, v200\n\t"
      "v_add_f32_e32 v217, v217, v201\n\t"
      "v_mfma_f32_16x16x32_bf16 v[188:191], v[20:23], v[132:135], v[188:191]\n\t"
      "v_add_f32_e32 v218, v218, v202\n\t"
      "v_add_f32_e32 v219, v219, v203\n\t"
      "v_mfma_f32_16x16x32_bf16 v[192:195], v[8:11], v[128:131], 0\n\t"
      "v_mul_f32_e32 v220, v216, v216\n\t"
      "v_mul_f32_e32 v221, v217, v217\n\t"
      "v_mfma_f32_16x16x32_bf16 v[192:195], v[24:27], v[132:135], v[192:195]\n\t"
      "v_mul_f32_e32 v222, v218, v218\n\t"
      "v_mul_f32_e32 v223, v219, v219\n\t"
      "v_mfma_f32_16x16x32_bf16 v[196:199], v[12:15], v[128:131], 0\n\t"
      "v_fmaak_f32 v220, v235, v220, 0xc0135761\n\t"
      "v_fmaak_f32 v221, v235, v221, 0xc0135761\n\t"
      "v_mfma_f32_16x16x32_bf16 v[196:199], v[28:31], v[132:135], v[196:199]\n\t"
      "v_fmaak_f32 v222, v235, v222, 0xc0135761\n\t"
      "s_waitcnt vmcnt(6)\n\t"
      "v_mfma_f32_16x16x32_bf16 v[184:187], v[32:35], v[136:139], v[184:187]\n\t"
      "v_fmaak_f32 v223, v235, v223, 0xc0135761\n\t"
      "s_waitcnt vmcnt(5)\n\t"
      "v_mfma_f32_16x16x32_bf16 v[184:187], v[48:51], v[140:143], v[184:187]\n\t"
      "v_mul_f32_e32 v220, v216, v220\n\t"
      "v_mul_f32_e32 v221, v217, v221\n\t"
      "v_mfma_f32_16x16x32_bf16 v[188:191], v[36:39], v[136:139], v[188:191]\n\t"
      "v_mul_f32_e32 v222, v218, v222\n\t"
      "v_mul_f32_e32 v223, v219, v223\n\t"
      "v_mfma_f32_16x16x32_bf16 v[188:191], v[52:55], v[140:143], v[188:191]\n\t"
      "v_exp_f32_e32 v220, v220\n\t"
      "v_mfma_f32_16x16x32_bf16 v[192:195], v[40:43], v[136:139], v[192:195]\n\t"
      "v_exp_f32_e32 v221, v221\n\t"
      "v_mfma_f32_16x16x32_bf16 v[192:195], v[56:59], v[140:143], v[192:195]\n\t"
      "v_exp_f32_e32 v222, v222\n\t"
      "v_mfma_f32_16x16x32_bf16 v[196:199], v[44:47], v[136:139], v[196:199]\n\t"
      "v_exp_f32_e32 v223, v223\n\t"
      "v_mfma_f32_16x16x32_bf16 v[196:199], v[60:63], v[140:143], v[196:199]\n\t"
      "s_waitcnt vmcnt(4)\n\t"
      "v_mfma_f32_16x16x32_bf16 v[184:187], v[64:67], v[144:147], v[184:187]\n\t"
      "s_waitcnt vmcnt(3)\n\t"
      "v_mfma_f32_16x16x32_bf16 v[184:187], v[80:83], v[148:151], v[184:187]\n\t"
      "v_add_f32_e32 v220, 1.0, v220\n\t"
      "v_add_f32_e32 v221, 1.0, v221\n\t"
      "v_mfma_f32_16x16x32_bf16 v[188:191], v[68:71], v[144:147], v[188:191]\n\t"
      "v_add_f32_e32 v222, 1.0, v222\n\t"
      "v_add_f32_e32 v223, 1.0, v223\n\t"
      "v_mfma_f32_16x16x32_bf16 v[188:191], v[84:87], v[148:151], v[188:191]\n\t"
      "v_rcp_f32_e32 v220, v220\n\t"
      "v_mfma_f32_16x16x32_bf16 v[192:195], v[72:75], v[144:147], v[192:195]\n\t"
      "v_rcp_f32_e32 v221, v221\n\t"
      "v_mfma_f32_16x16x32_bf16 v[192:195], v[88:91], v[148:151], v[192:195]\n\t"
      "v_rcp_f32_e32 v222, v222\n\t"
      "v_mfma_f32_16x16x32_bf16 v[196:199], v[76:79], v[144:147], v[196:199]\n\t"
      "v_rcp_f32_e32 v223, v223\n\t"
      "v_mfma_f32_16x16x32_bf16 v[196:199], v[92:95], v[148:151], v[196:199]\n\t"
      "v_mul_f32_e32 v216, v216, v220\n\t"
      "s_waitcnt vmcnt(1)\n\t"
      "v_mfma_f32_16x16x32_bf16 v[184:187], v[96:99], v[152:155], v[184:187]\n\t"
      "v_mul_f32_e32 v217, v217, v221\n\t"
      "s_waitcnt vmcnt(0)\n\t"
      "v_mfma_f32_16x16x32_bf16 v[184:187], v[112:115], v[156:159], v[184:187]\n\t"
      "v_mul_f32_e32 v218, v218, v222\n\t"
      "v_mul_f32_e32 v219, v219, v223\n\t"
      "v_mfma_f32_16x16x32_bf16 v[188:191], v[100:103], v[152:155], v[188:191]\n\t"
      "v_cvt_pk_bf16_f32 v220, v216, v217\n\t"
      "v_cvt_pk_bf16_f32 v221, v218, v219\n\t"
      "v_mfma_f32_16x16x32_bf16 v[188:191], v[116:119], v[156:159], v[188:191]\n\t"
      "s_nop 1\n\t"
      "v_cmp_u_f32_e32 vcc, v184, v184\n\t"
      "v_min_u32_e32 v220, -2, v220\n\t"
      "v_mfma_f32_16x16x32_bf16 v[192:195], v[104:107], v[152:155], v[192:195]\n\t"
      "v_min_u32_e32 v221, -2, v221\n\t"
      "global_store_dwordx2 v245, v[220:221], s[8:9] sc1\n\t"
      "s_add_u32 s8, s8, 131072\n\t"
      "s_addc_u32 s9, s9, 0\n\t"
      "v_mfma_f32_16x16x32_bf16 v[192:195], v[120:123], v[156:159], v[192:195]\n\t"
      "s_cbranch_vccnz NP6R_%=\n\t"
      "NP6B_%=:\n\t"
      "ds_write_b128 v243, v[184:187]\n\t"
      "v_mfma_f32_16x16x32_bf16 v[196:199], v[108:111], v[152:155], v[196:199]\n\t"
      "v_mfma_f32_16x16x32_bf16 v[196:199], v[124:127], v[156:159], v[196:199]\n\t"
      "ds_write_b128 v164, v[188:191]\n\t"
      "s_nop 2\n\t"
      "ds_write_b128 v165, v[192:195]\n\t"
      "s_nop 2\n\t"
      "ds_write_b128 v166, v[196:199]\n\t"
      "s_waitcnt lgkmcnt(0)\n\t"
      "s_barrier\n\t"
      "ds_read_b128 v[216:219], v244\n\t"
      "ds_read_b128 v[220:223], v244 offset:4096\n\t"
      "ds_read_b128 v[224:227], v244 offset:8192\n\t"
      "ds_read_b128 v[228:231], v244 offset:12288\n\t"
      "ds_read_b128 v[184:187], v244 offset:16384\n\t"
      "ds_read_b128 v[188:191], v244 offset:20480\n\t"
      "ds_read_b128 v[192:195], v244 offset:24576\n\t"
      "ds_read_b128 v[196:199], v244 offset:28672\n\t"
      "s_waitcnt lgkmcnt(6)\n\t"
      "v_add_f32_e32 v216, v216, v220\n\t"
      "v_add_f32_e32 v217, v217, v221\n\t"
      "v_add_f32_e32 v218, v218, v222\n\t"
      "v_add_f32_e32 v219, v219, v223\n\t"
      "s_waitcnt lgkmcnt(4)\n\t"
      "v_add_f32_e32 v224, v224, v228\n\t"
      "v_add_f32_e32 v225, v225, v229\n\t"
      "v_add_f32_e32 v226, v226, v230\n\t"
      "v_add_f32_e32 v227, v227, v231\n\t"
      "s_waitcnt lgkmcnt(2)\n\t"
      "v_add_f32_e32 v184, v184, v188\n\t"
      "v_add_f32_e32 v185, v185, v189\n\t"
      "v_add_f32_e32 v186, v186, v190\n\t"
      "v_add_f32_e32 v187, v187, v191\n\t"
      "v_add_f32_e32 v216, v216, v224\n\t"
      "v_add_f32_e32 v217, v217, v225\n\t"
      "v_add_f32_e32 v218, v218, v226\n\t"
      "v_add_f32_e32 v219, v219, v227\n\t"
      "s_waitcnt lgkmcnt(0)\n\t"
      "v_add_f32_e32 v192, v192, v196\n\t"
      "v_add_f32_e32 v193, v193, v197\n\t"
      "v_add_f32_e32 v194, v194, v198\n\t"
      "v_add_f32_e32 v195, v195, v199\n\t"
      "v_add_f32_e32 v184, v184, v192\n\t"
      "v_add_f32_e32 v185, v185, v193\n\t"
      "v_add_f32_e32 v186, v186, v194\n\t"
      "v_add_f32_e32 v187, v187, v195\n\t"
      "v_add_f32_e32 v216, v216, v184\n\t"
      "v_add_f32_e32 v217, v217, v185\n\t"
      "v_add_f32_e32 v218, v218, v186\n\t"
      "v_add_f32_e32 v219, v219, v187\n\t"
      "v_mul_f32_e32 v220, v216, v216\n\t"
      "v_mul_f32_e32 v221, v217, v217\n\t"
      "v_mul_f32_e32 v222, v218, v218\n\t"
      "v_mul_f32_e64 v223, v219, v219\n\t"
      "v_fmaak_f32 v220, v235, v220, 0xc0135761\n\t"
      "v_fmaak_f32 v221, v235, v221, 0xc0135761\n\t"
      "v_fmaak_f32 v222, v235, v222, 0xc0135761\n\t"
      "v_fmaak_f32 v223, v235, v223, 0xc0135761\n\t"
      "v_mul_f32_e32 v220, v216, v220\n\t"
      "v_mul_f32_e32 v221, v217, v221\n\t"
      "v_mul_f32_e32 v222, v218, v222\n\t"
      "v_mul_f32_e32 v223, v219, v223\n\t"
      "v_exp_f32_e32 v220, v220\n\t"
      "v_exp_f32_e32 v221, v221\n\t"
      "v_exp_f32_e32 v222, v222\n\t"
      "v_exp_f32_e32 v223, v223\n\t"
      "v_add_f32_e32 v220, 1.0, v220\n\t"
      "v_add_f32_e32 v221, 1.0, v221\n\t"
      "v_add_f32_e32 v222, 1.0, v222\n\t"
      "v_add_f32_e32 v223, 1.0, v223\n\t"
      "v_rcp_f32_e32 v220, v220\n\t"
      "v_rcp_f32_e32 v221, v221\n\t"
      "v_rcp_f32_e32 v222, v222\n\t"
      "v_rcp_f32_e32 v223, v223\n\t"
      "v_mul_f32_e32 v216, v216, v220\n\t"
      "v_mul_f32_e32 v217, v217, v221\n\t"
      "v_mul_f32_e32 v218, v218, v222\n\t"
      "v_mul_f32_e32 v219, v219, v223\n\t"
      "v_cvt_pk_bf16_f32 v220, v216, v217\n\t"
      "v_cvt_pk_bf16_f32 v221, v218, v219\n\t"
      "v_min_u32_e32 v220, -2, v220\n\t"
      "v_min_u32_e32 v221, -2, v221\n\t"
      "s_cmp_eq_u32 s17, 0\n\t"
      "s_cbranch_scc0 HI6_%=\n\t"
      "s_mov_b64 exec, 0xf\n\t"
      "global_store_dwordx2 v247, v[232:233], s[12:13] sc1\n\t"
      "s_mov_b64 exec, -1\n\t"
      "HI6_%=:\n\t"
      "global_store_dwordx2 v245, v[220:221], s[8:9] sc1\n\t"
      "GX_%=:\n\t"
      "AEND_%=:\n\t"
      "s_endpgm\n\t"
      ".p2align 6\n\t"
      "GQ_%=:\n\t"
      "s_waitcnt vmcnt(7)\n\t"
      "v_max3_u32 v234, v128, v129, v130\n\t"
      "v_max_u32_e32 v234, v234, v131\n\t"
      "v_cmp_eq_u32_e32 vcc, -1, v234\n\t"
      "s_cbranch_vccnz FQ0R_%=\n\t"
      "FQ0B_%=:\n\t"
      "v_mfma_f32_16x16x32_bf16 v[184:187], v[0:3], v[128:131], v[236:239]\n\t"
      "s_waitcnt vmcnt(6)\n\t"
      "v_max3_u32 v234, v132, v133, v134\n\t"
      "v_max_u32_e32 v234, v234, v135\n\t"
      "v_cmp_eq_u32_e32 vcc, -1, v234\n\t"
      "s_cbranch_vccnz FQ1R_%=\n\t"
      "FQ1B_%=:\n\t"
      "v_mfma_f32_16x16x32_bf16 v[184:187], v[16:19], v[132:135], v[184:187]\n\t"
      "v_mfma_f32_16x16x32_bf16 v[188:191], v[4:7], v[128:131], 0\n\t"
      "v_mfma_f32_16x16x32_bf16 v[188:191], v[20:23], v[132:135], v[188:191]\n\t"
      "v_mfma_f32_16x16x32_bf16 v[192:195], v[8:11], v[128:131], 0\n\t"
      "v_mfma_f32_16x16x32_bf16 v[192:195], v[24:27], v[132:135], v[192:195]\n\t"
      "v_mfma_f32_16x16x32_bf16 v[196:199], v[12:15], v[128:131], 0\n\t"
      "global_load_dwordx4 v[128:131], v176, s[54:55] sc1\n\t"
      "v_mfma_f32_16x16x32_bf16 v[196:199], v[28:31], v[132:135], v[196:199]\n\t"
      "global_load_dwordx4 v[132:135], v177, s[54:55] sc1\n\t"
      "s_waitcnt vmcnt(7)\n\t"
      "v_max3_u32 v234, v136, v137, v138\n\t"
      "v_max_u32_e32 v234, v234, v139\n\t"
      "v_cmp_eq_u32_e32 vcc, -1, v234\n\t"
      "s_cbranch_vccnz FQ2R_%=\n\t"
      "FQ2B_%=:\n\t"
      "v_mfma_f32_16x16x32_bf16 v[184:187], v[32:35], v[136:139], v[184:187]\n\t"
      "s_waitcnt vmcnt(6)\n\t"
      "v_max3_u32 v234, v140, v141, v142\n\t"
      "v_max_u32_e32 v234, v234, v143\n\t"
      "v_cmp_eq_u32_e32 vcc, -1, v234\n\t"
      "s_cbranch_vccnz FQ3R_%=\n\t"
      "FQ3B_%=:\n\t"
      "v_mfma_f32_16x16x32_bf16 v[184:187], v[48:51], v[140:143], v[184:187]\n\t"
      "v_mfma_f32_16x16x32_bf16 v[188:191], v[36:39], v[136:139], v[188:191]\n\t"
      "v_mfma_f32_16x16x32_bf16 v[188:191], v[52:55], v[140:143], v[188:191]\n\t"
      "v_mfma_f32_16x16x32_bf16 v[192:195], v[40:43], v[136:139], v[192:195]\n\t"
      "v_mfma_f32_16x16x32_bf16 v[192:195], v[56:59], v[140:143], v[192:195]\n\t"
      "v_mfma_f32_16x16x32_bf16 v[196:199], v[44:47], v[136:139], v[196:199]\n\t"
      "global_load_dwordx4 v[136:139], v178, s[54:55] sc1\n\t"
      "v_mfma_f32_16x16x32_bf16 v[196:199], v[60:63], v[140:143], v[196:199]\n\t"
      "global_load_dwordx4 v[140:143], v179, s[54:55] sc1\n\t"
      "s_waitcnt vmcnt(7)\n\t"
      "v_max3_u32 v234, v144, v145, v146\n\t"
      "v_max_u32_e32 v234, v234, v147\n\t"
      "v_cmp_eq_u32_e32 vcc, -1, v234\n\t"
      "s_cbranch_vccnz FQ4R_%=\n\t"
      "FQ4B_%=:\n\t"
      "v_mfma_f32_16x16x32_bf16 v[184:187], v[64:67], v[144:147], v[184:187]\n\t"
      "s_waitcnt vmcnt(6)\n\t"
      "v_max3_u32 v234, v148, v149, v150\n\t"
      "v_max_u32_e32 v234, v234, v151\n\t"
      "v_cmp_eq_u32_e32 vcc, -1, v234\n\t"
      "s_cbranch_vccnz FQ5R_%=\n\t"
      "FQ5B_%=:\n\t"
      "v_mfma_f32_16x16x32_bf16 v[184:187], v[80:83], v[148:151], v[184:187]\n\t"
      "v_mfma_f32_16x16x32_bf16 v[188:191], v[68:71], v[144:147], v[188:191]\n\t"
      "v_mfma_f32_16x16x32_bf16 v[188:191], v[84:87], v[148:151], v[188:191]\n\t"
      "v_mfma_f32_16x16x32_bf16 v[192:195], v[72:75], v[144:147], v[192:195]\n\t"
      "v_mfma_f32_16x16x32_bf16 v[192:195], v[88:91], v[148:151], v[192:195]\n\t"
      "v_mfma_f32_16x16x32_bf16 v[196:199], v[76:79], v[144:147], v[196:199]\n\t"
      "global_load_dwordx4 v[144:147], v180, s[54:55] sc1\n\t"
      "v_mfma_f32_16x16x32_bf16 v[196:199], v[92:95], v[148:151], v[196:199]\n\t"
      "global_load_dwordx4 v[148:151], v181, s[54:55] sc1\n\t"
      "s_waitcnt vmcnt(7)\n\t"
      "v_max3_u32 v234, v152, v153, v154\n\t"
      "v_max_u32_e32 v234, v234, v155\n\t"
      "v_cmp_eq_u32_e32 vcc, -1, v234\n\t"
      "s_cbranch_vccnz FQ6R_%=\n\t"
      "FQ6B_%=:\n\t"
      "v_mfma_f32_16x16x32_bf16 v[184:187], v[96:99], v[152:155], v[184:187]\n\t"
      "s_waitcnt vmcnt(6)\n\t"
      "v_max3_u32 v234, v156, v157, v158\n\t"
      "v_max_u32_e32 v234, v234, v159\n\t"
      "v_cmp_eq_u32_e32 vcc, -1, v234\n\t"
      "s_cbranch_vccnz FQ7R_%=\n\t"
      "FQ7B_%=:\n\t"
      "v_mfma_f32_16x16x32_bf16 v[184:187], v[112:115], v[156:159], v[184:187]\n\t"
      "v_mfma_f32_16x16x32_bf16 v[188:191], v[100:103], v[152:155], v[188:191]\n\t"
      "v_mfma_f32_16x16x32_bf16 v[188:191], v[116:119], v[156:159], v[188:191]\n\t"
      "v_mfma_f32_16x16x32_bf16 v[192:195], v[104:107], v[152:155], v[192:195]\n\t"
      "v_mfma_f32_16x16x32_bf16 v[192:195], v[120:123], v[156:159], v[192:195]\n\t"
      "v_mfma_f32_16x16x32_bf16 v[196:199], v[108:111], v[152:155], v[196:199]\n\t"
      "global_load_dwordx4 v[152:155], v182, s[54:55] sc1\n\t"
      "v_mfma_f32_16x16x32_bf16 v[196:199], v[124:127], v[156:159], v[196:199]\n\t"
      "global_load_dwordx4 v[156:159], v183, s[54:55] sc1\n\t"
      "ds_write_b128 v243, v[184:187]\n\t"
      "s_nop 0\n\t"
      "ds_write_b128 v164, v[188:191]\n\t"
      "s_nop 0\n\t"
      "ds_write_b128 v165, v[192:195]\n\t"
      "s_nop 1\n\t"
      "ds_write_b128 v166, v[196:199]\n\t"
      "s_waitcnt vmcnt(7)\n\t"
      "v_mfma_f32_16x16x32_bf16 v[200:203], v[0:3], v[128:131], v[236:239]\n\t"
      "s_waitcnt vmcnt(6)\n\t"
      "v_mfma_f32_16x16x32_bf16 v[200:203], v[16:19], v[132:135], v[200:203]\n\t"
      "v_mfma_f32_16x16x32_bf16 v[204:207], v[4:7], v[128:131], 0\n\t"
      "v_mfma_f32_16x16x32_bf16 v[204:207], v[20:23], v[132:135], v[204:207]\n\t"
      "v_mfma_f32_16x16x32_bf16 v[208:211], v[8:11], v[128:131], 0\n\t"
      "v_mfma_f32_16x16x32_bf16 v[208:211], v[24:27], v[132:135], v[208:211]\n\t"
      "v_mfma_f32_16x16x32_bf16 v[212:215], v[12:15], v[128:131], 0\n\t"
      "global_load_dwordx4 v[128:131], v176, s[56:57] sc1\n\t"
      "v_mfma_f32_16x16x32_bf16 v[212:215], v[28:31], v[132:135], v[212:215]\n\t"
      "global_load_dwordx4 v[132:135], v177, s[56:57] sc1\n\t"
      "s_waitcnt vmcnt(7)\n\t"
      "v_mfma_f32_16x16x32_bf16 v[200:203], v[32:35], v[136:139], v[200:203]\n\t"
      "s_waitcnt vmcnt(6)\n\t"
      "v_mfma_f32_16x16x32_bf16 v[200:203], v[48:51], v[140:143], v[200:203]\n\t"
      "v_mfma_f32_16x16x32_bf16 v[204:207], v[36:39], v[136:139], v[204:207]\n\t"
      "v_mfma_f32_16x16x32_bf16 v[204:207], v[52:55], v[140:143], v[204:207]\n\t"
      "v_mfma_f32_16x16x32_bf16 v[208:211], v[40:43], v[136:139], v[208:211]\n\t"
      "v_mfma_f32_16x16x32_bf16 v[208:211], v[56:59], v[140:143], v[208:211]\n\t"
      "v_mfma_f32_16x16x32_bf16 v[212:215], v[44:47], v[136:139], v[212:215]\n\t"
      "global_load_dwordx4 v[136:139], v178, s[56:57] sc1\n\t"
      "v_mfma_f32_16x16x32_bf16 v[212:215], v[60:63], v[140:143], v[212:215]\n\t"
      "global_load_dwordx4 v[140:143], v179, s[56:57] sc1\n\t"
      "s_waitcnt vmcnt(7)\n\t"
      "v_mfma_f32_16x16x32_bf16 v[200:203], v[64:67], v[144:147], v[200:203]\n\t"
      "s_waitcnt vmcnt(6)\n\t"
      "v_mfma_f32_16x16x32_bf16 v[200:203], v[80:83], v[148:151], v[200:203]\n\t"
      "v_mfma_f32_16x16x32_bf16 v[204:207], v[68:71], v[144:147], v[204:207]\n\t"
      "v_mfma_f32_16x16x32_bf16 v[204:207], v[84:87], v[148:151], v[204:207]\n\t"
      "v_mfma_f32_16x16x32_bf16 v[208:211], v[72:75], v[144:147], v[208:211]\n\t"
      "v_mfma_f32_16x16x32_bf16 v[208:211], v[88:91], v[148:151], v[208:211]\n\t"
      "s_waitcnt lgkmcnt(0)\n\t"
      "s_barrier\n\t"
      "s_setprio 2\n\t"
      "v_mfma_f32_16x16x32_bf16 v[212:215], v[76:79], v[144:147], v[212:215]\n\t"
      "global_load_dwordx4 v[144:147], v180, s[56:57] sc1\n\t"
      "v_mfma_f32_16x16x32_bf16 v[212:215], v[92:95], v[148:151], v[212:215]\n\t"
      "global_load_dwordx4 v[148:151], v181, s[56:57] sc1\n\t"
      "s_waitcnt vmcnt(7)\n\t"
      "v_mfma_f32_16x16x32_bf16 v[200:203], v[96:99], v[152:155], v[200:203]\n\t"
      "s_waitcnt vmcnt(6)\n\t"
      "v_mfma_f32_16x16x32_bf16 v[200:203], v[112:115], v[156:159], v[200:203]\n\t"
      "v_mfma_f32_16x16x32_bf16 v[204:207], v[100:103], v[152:155], v[204:207]\n\t"
      "v_mfma_f32_16x16x32_bf16 v[204:207], v[116:119], v[156:159], v[204:207]\n\t"
      "v_mfma_f32_16x16x32_bf16 v[208:211], v[104:107], v[152:155], v[208:211]\n\t"
      "v_mfma_f32_16x16x32_bf16 v[208:211], v[120:123], v[156:159], v[208:211]\n\t"
      "v_mfma_f32_16x16x32_bf16 v[212:215], v[108:111], v[152:155], v[212:215]\n\t"
      "global_load_dwordx4 v[152:155], v182, s[56:57] sc1\n\t"
      "v_mfma_f32_16x16x32_bf16 v[212:215], v[124:127], v[156:159], v[212:215]\n\t"
      "global_load_dwordx4 v[156:159], v183, s[56:57] sc1\n\t"
      "s_setprio 0\n\t"
      "v_cmp_u_f32_e32 vcc, v200, v200\n\t"
      "s_cbranch_vccnz NQ1R_%=\n\t"
      "NQ1B_%=:\n\t"
      "ds_write_b128 v243, v[200:203] offset:32768\n\t"
      "ds_write_b128 v164, v[204:207] offset:32768\n\t"
      "ds_write_b128 v165, v[208:211] offset:32768\n\t"
      "s_nop 0\n\t"
      "ds_write_b128 v166, v[212:215] offset:32768\n\t"
      "s_waitcnt vmcnt(7)\n\t"
      "v_mfma_f32_16x16x32_bf16 v[184:187], v[0:3], v[128:131], v[236:239]\n\t"
      "s_waitcnt vmcnt(6)\n\t"
      "v_mfma_f32_16x16x32_bf16 v[184:187], v[16:19], v[132:135], v[184:187]\n\t"
      "v_mfma_f32_16x16x32_bf16 v[188:191], v[4:7], v[128:131], 0\n\t"
      "v_mfma_f32_16x16x32_bf16 v[188:191], v[20:23], v[132:135], v[188:191]\n\t"
      "v_mfma_f32_16x16x32_bf16 v[192:195], v[8:11], v[128:131], 0\n\t"
      "v_mfma_f32_16x16x32_bf16 v[192:195], v[24:27], v[132:135], v[192:195]\n\t"
      "v_mfma_f32_16x16x32_bf16 v[196:199], v[12:15], v[128:131], 0\n\t"
      "global_load_dwordx4 v[128:131], v176, s[58:59] sc1\n\t"
      "v_mfma_f32_16x16x32_bf16 v[196:199], v[28:31], v[132:135], v[196:199]\n\t"
      "global_load_dwordx4 v[132:135], v177, s[58:59] sc1\n\t"
      "s_waitcnt vmcnt(7)\n\t"
      "v_mfma_f32_16x16x32_bf16 v[184:187], v[32:35], v[136:139], v[184:187]\n\t"
      "s_waitcnt vmcnt(6)\n\t"
      "v_mfma_f32_16x16x32_bf16 v[184:187], v[48:51], v[140:143], v[184:187]\n\t"
      "v_mfma_f32_16x16x32_bf16 v[188:191], v[36:39], v[136:139], v[188:191]\n\t"
      "v_mfma_f32_16x16x32_bf16 v[188:191], v[52:55], v[140:143], v[188:191]\n\t"
      "v_mfma_f32_16x16x32_bf16 v[192:195], v[40:43], v[136:139], v[192:195]\n\t"
      "v_mfma_f32_16x16x32_bf16 v[192:195], v[56:59], v[140:143], v[192:195]\n\t"
      "v_mfma_f32_16x16x32_bf16 v[196:199], v[44:47], v[136:139], v[196:199]\n\t"
      "global_load_dwordx4 v[136:139], v178, s[58:59] sc1\n\t"
      "v_mfma_f32_16x16x32_bf16 v[196:199], v[60:63], v[140:143], v[196:199]\n\t"
      "global_load_dwordx4 v[140:143], v179, s[58:59] sc1\n\t"
      "s_waitcnt vmcnt(7)\n\t"
      "v_mfma_f32_16x16x32_bf16 v[184:187], v[64:67], v[144:147], v[184:187]\n\t"
      "s_waitcnt vmcnt(6)\n\t"
      "v_mfma_f32_16x16x32_bf16 v[184:187], v[80:83], v[148:151], v[184:187]\n\t"
      "v_mfma_f32_16x16x32_bf16 v[188:191], v[68:71], v[144:147], v[188:191]\n\t"
      "v_mfma_f32_16x16x32_bf16 v[188:191], v[84:87], v[148:151], v[188:191]\n\t"
      "v_mfma_f32_16x16x32_bf16 v[192:195], v[72:75], v[144:147], v[192:195]\n\t"
      "v_mfma_f32_16x16x32_bf16 v[192:195], v[88:91], v[148:151], v[192:195]\n\t"
      "s_waitcnt lgkmcnt(0)\n\t"
      "s_barrier\n\t"
      "s_setprio 2\n\t"
      "v_mfma_f32_16x16x32_bf16 v[196:199], v[76:79], v[144:147], v[196:199]\n\t"
      "global_load_dwordx4 v[144:147], v180, s[58:59] sc1\n\t"
      "v_mfma_f32_16x16x32_bf16 v[196:199], v[92:95], v[148:151], v[196:199]\n\t"
      "global_load_dwordx4 v[148:151], v181, s[58:59] sc1\n\t"
      "s_waitcnt vmcnt(7)\n\t"
      "v_mfma_f32_16x16x32_bf16 v[184:187], v[96:99], v[152:155], v[184:187]\n\t"
      "s_waitcnt vmcnt(6)\n\t"
      "v_mfma_f32_16x16x32_bf16 v[184:187], v[112:115], v[156:159], v[184:187]\n\t"
      "v_mfma_f32_16x16x32_bf16 v[188:191], v[100:103], v[152:155], v[188:191]\n\t"
      "v_mfma_f32_16x16x32_bf16 v[188:191], v[116:119], v[156:159], v[188:191]\n\t"
      "v_mfma_f32_16x16x32_bf16 v[192:195], v[104:107], v[152:155], v[192:195]\n\t"
      "v_mfma_f32_16x16x32_bf16 v[192:195], v[120:123], v[156:159], v[192:195]\n\t"
      "v_mfma_f32_16x16x32_bf16 v[196:199], v[108:111], v[152:155], v[196:199]\n\t"
      "global_load_dwordx4 v[152:155], v182, s[58:59] sc1\n\t"
      "v_mfma_f32_16x16x32_bf16 v[196:199], v[124:127], v[156:159], v[196:199]\n\t"
      "global_load_dwordx4 v[156:159], v183, s[58:59] sc1\n\t"
      "s_setprio 0\n\t"
      "v_cmp_u_f32_e32 vcc, v184, v184\n\t"
      "s_cbranch_vccnz NQ2R_%=\n\t"
      "NQ2B_%=:\n\t"
      "ds_write_b128 v243, v[184:187]\n\t"
      "ds_write_b128 v164, v[188:191]\n\t"
      "ds_write_b128 v165, v[192:195]\n\t"
      "s_nop 0\n\t"
      "ds_write_b128 v166, v[196:199]\n\t"
      "s_waitcnt vmcnt(7)\n\t"
      "v_mfma_f32_16x16x32_bf16 v[200:203], v[0:3], v[128:131], v[236:239]\n\t"
      "s_waitcnt vmcnt(6)\n\t"
      "v_mfma_f32_16x16x32_bf16 v[200:203], v[16:19], v[132:135], v[200:203]\n\t"
      "v_mfma_f32_16x16x32_bf16 v[204:207], v[4:7], v[128:131], 0\n\t"
      "v_mfma_f32_16x16x32_bf16 v[204:207], v[20:23], v[132:135], v[204:207]\n\t"
      "v_mfma_f32_16x16x32_bf16 v[208:211], v[8:11], v[128:131], 0\n\t"
      "v_mfma_f32_16x16x32_bf16 v[208:211], v[24:27], v[132:135], v[208:211]\n\t"
      "v_mfma_f32_16x16x32_bf16 v[212:215], v[12:15], v[128:131], 0\n\t"
      "global_load_dwordx4 v[128:131], v176, s[60:61] sc1\n\t"
      "v_mfma_f32_16x16x32_bf16 v[212:215], v[28:31], v[132:135], v[212:215]\n\t"
      "global_load_dwordx4 v[132:135], v177, s[60:61] sc1\n\t"
      "s_waitcnt vmcnt(7)\n\t"
      "v_mfma_f32_16x16x32_bf16 v[200:203], v[32:35], v[136:139], v[200:203]\n\t"
      "s_waitcnt vmcnt(6)\n\t"
      "v_mfma_f32_16x16x32_bf16 v[200:203], v[48:51], v[140:143], v[200:203]\n\t"
      "v_mfma_f32_16x16x32_bf16 v[204:207], v[36:39], v[136:139], v[204:207]\n\t"
      "v_mfma_f32_16x16x32_bf16 v[204:207], v[52:55], v[140:143], v[204:207]\n\t"
      "v_mfma_f32_16x16x32_bf16 v[208:211], v[40:43], v[136:139], v[208:211]\n\t"
      "v_mfma_f32_16x16x32_bf16 v[208:211], v[56:59], v[140:143], v[208:211]\n\t"
      "v_mfma_f32_16x16x32_bf16 v[212:215], v[44:47], v[136:139], v[212:215]\n\t"
      "global_load_dwordx4 v[136:139], v178, s[60:61] sc1\n\t"
      "v_mfma_f32_16x16x32_bf16 v[212:215], v[60:63], v[140:143], v[212:215]\n\t"
      "global_load_dwordx4 v[140:143], v179, s[60:61] sc1\n\t"
      "s_waitcnt vmcnt(7)\n\t"
      "v_mfma_f32_16x16x32_bf16 v[200:203], v[64:67], v[144:147], v[200:203]\n\t"
      "s_waitcnt vmcnt(6)\n\t"
      "v_mfma_f32_16x16x32_bf16 v[200:203], v[80:83], v[148:151], v[200:203]\n\t"
      "v_mfma_f32_16x16x32_bf16 v[204:207], v[68:71], v[144:147], v[204:207]\n\t"
      "v_mfma_f32_16x16x32_bf16 v[204:207], v[84:87], v[148:151], v[204:207]\n\t"
      "v_mfma_f32_16x16x32_bf16 v[208:211], v[72:75], v[144:147], v[208:211]\n\t"
      "v_mfma_f32_16x16x32_bf16 v[208:211], v[88:91], v[148:151], v[208:211]\n\t"
      "s_waitcnt lgkmcnt(0)\n\t"
      "s_barrier\n\t"
      "s_setprio 2\n\t"
      "v_mfma_f32_16x16x32_bf16 v[212:215], v[76:79], v[144:147], v[212:215]\n\t"
      "global_load_dwordx4 v[144:147], v180, s[60:61] sc1\n\t"
      "v_mfma_f32_16x16x32_bf16 v[212:215], v[92:95], v[148:151], v[212:215]\n\t"
      "global_load_dwordx4 v[148:151], v181, s[60:61] sc1\n\t"
      "s_waitcnt vmcnt(7)\n\t"
      "v_mfma_f32_16x16x32_bf16 v[200:203], v[96:99], v[152:155], v[200:203]\n\t"
      "s_waitcnt vmcnt(6)\n\t"
      "v_mfma_f32_16x16x32_bf16 v[200:203], v[112:115], v[156:159], v[200:203]\n\t"
      "v_mfma_f32_16x16x32_bf16 v[204:207], v[100:103], v[152:155], v[204:207]\n\t"
      "v_mfma_f32_16x16x32_bf16 v[204:207], v[116:119], v[156:159], v[204:207]\n\t"
      "v_mfma_f32_16x16x32_bf16 v[208:211], v[104:107], v[152:155], v[208:211]\n\t"
      "v_mfma_f32_16x16x32_bf16 v[208:211], v[120:123], v[156:159], v[208:211]\n\t"
      "v_mfma_f32_16x16x32_bf16 v[212:215], v[108:111], v[152:155], v[212:215]\n\t"
      "global_load_dwordx4 v[152:155], v182, s[60:61] sc1\n\t"
      "v_mfma_f32_16x16x32_bf16 v[212:215], v[124:127], v[156:159], v[212:215]\n\t"
      "global_load_dwordx4 v[156:159], v183, s[60:61] sc1\n\t"
      "s_setprio 0\n\t"
      "v_cmp_u_f32_e32 vcc, v200, v200\n\t"
      "s_cbranch_vccnz NQ3R_%=\n\t"
      "NQ3B_%=:\n\t"
      "ds_write_b128 v243, v[200:203] offset:32768\n\t"
      "ds_write_b128 v164, v[204:207] offset:32768\n\t"
      "ds_write_b128 v165, v[208:211] offset:32768\n\t"
      "s_nop 0\n\t"
      "ds_write_b128 v166, v[212:215] offset:32768\n\t"
      "s_waitcnt vmcnt(7)\n\t"
      "v_mfma_f32_16x16x32_bf16 v[184:187], v[0:3], v[128:131], v[236:239]\n\t"
      "s_waitcnt vmcnt(6)\n\t"
      "v_mfma_f32_16x16x32_bf16 v[184:187], v[16:19], v[132:135], v[184:187]\n\t"
      "v_mfma_f32_16x16x32_bf16 v[188:191], v[4:7], v[128:131], 0\n\t"
      "v_mfma_f32_16x16x32_bf16 v[188:191], v[20:23], v[132:135], v[188:191]\n\t"
      "v_mfma_f32_16x16x32_bf16 v[192:195], v[8:11], v[128:131], 0\n\t"
      "v_mfma_f32_16x16x32_bf16 v[192:195], v[24:27], v[132:135], v[192:195]\n\t"
      "v_mfma_f32_16x16x32_bf16 v[196:199], v[12:15], v[128:131], 0\n\t"
      "global_load_dwordx4 v[128:131], v176, s[62:63] sc1\n\t"
      "v_mfma_f32_16x16x32_bf16 v[196:199], v[28:31], v[132:135], v[196:199]\n\t"
      "global_load_dwordx4 v[132:135], v177, s[62:63] sc1\n\t"
      "s_waitcnt vmcnt(7)\n\t"
      "v_mfma_f32_16x16x32_bf16 v[184:187], v[32:35], v[136:139], v[184:187]\n\t"
      "s_waitcnt vmcnt(6)\n\t"
      "v_mfma_f32_16x16x32_bf16 v[184:187], v[48:51], v[140:143], v[184:187]\n\t"
      "v_mfma_f32_16x16x32_bf16 v[188:191], v[36:39], v[136:139], v[188:191]\n\t"
      "v_mfma_f32_16x16x32_bf16 v[188:191], v[52:55], v[140:143], v[188:191]\n\t"
      "v_mfma_f32_16x16x32_bf16 v[192:195], v[40:43], v[136:139], v[192:195]\n\t"
      "v_mfma_f32_16x16x32_bf16 v[192:195], v[56:59], v[140:143], v[192:195]\n\t"
      "v_mfma_f32_16x16x32_bf16 v[196:199], v[44:47], v[136:139], v[196:199]\n\t"
      "global_load_dwordx4 v[136:139], v178, s[62:63] sc1\n\t"
      "v_mfma_f32_16x16x32_bf16 v[196:199], v[60:63], v[140:143], v[196:199]\n\t"
      "global_load_dwordx4 v[140:143], v179, s[62:63] sc1\n\t"
      "s_waitcnt vmcnt(7)\n\t"
      "v_mfma_f32_16x16x32_bf16 v[184:187], v[64:67], v[144:147], v[184:187]\n\t"
      "s_waitcnt vmcnt(6)\n\t"
      "v_mfma_f32_16x16x32_bf16 v[184:187], v[80:83], v[148:151], v[184:187]\n\t"
      "v_mfma_f32_16x16x32_bf16 v[188:191], v[68:71], v[144:147], v[188:191]\n\t"
      "v_mfma_f32_16x16x32_bf16 v[188:191], v[84:87], v[148:151], v[188:191]\n\t"
      "v_mfma_f32_16x16x32_bf16 v[192:195], v[72:75], v[144:147], v[192:195]\n\t"
      "v_mfma_f32_16x16x32_bf16 v[192:195], v[88:91], v[148:151], v[192:195]\n\t"
      "s_waitcnt lgkmcnt(0)\n\t"
      "s_barrier\n\t"
      "s_setprio 2\n\t"
      "v_mfma_f32_16x16x32_bf16 v[196:199], v[76:79], v[144:147], v[196:199]\n\t"
      "global_load_dwordx4 v[144:147], v180, s[62:63] sc1\n\t"
      "v_mfma_f32_16x16x32_bf16 v[196:199], v[92:95], v[148:151], v[196:199]\n\t"
      "global_load_dwordx4 v[148:151], v181, s[62:63] sc1\n\t"
      "s_waitcnt vmcnt(7)\n\t"
      "v_mfma_f32_16x16x32_bf16 v[184:187], v[96:99], v[152:155], v[184:187]\n\t"
      "s_waitcnt vmcnt(6)\n\t"
      "v_mfma_f32_16x16x32_bf16 v[184:187], v[112:115], v[156:159], v[184:187]\n\t"
      "v_mfma_f32_16x16x32_bf16 v[188:191], v[100:103], v[152:155], v[188:191]\n\t"
      "v_mfma_f32_16x16x32_bf16 v[188:191], v[116:119], v[156:159], v[188:191]\n\t"
      "v_mfma_f32_16x16x32_bf16 v[192:195], v[104:107], v[152:155], v[192:195]\n\t"
      "v_mfma_f32_16x16x32_bf16 v[192:195], v[120:123], v[156:159], v[192:195]\n\t"
      "v_mfma_f32_16x16x32_bf16 v[196:199], v[108:111], v[152:155], v[196:199]\n\t"
      "global_load_dwordx4 v[152:155], v182, s[62:63] sc1\n\t"
      "v_mfma_f32_16x16x32_bf16 v[196:199], v[124:127], v[156:159], v[196:199]\n\t"
      "global_load_dwordx4 v[156:159], v183, s[62:63] sc1\n\t"
      "s_setprio 0\n\t"
      "v_cmp_u_f32_e32 vcc, v184, v184\n\t"
      "s_cbranch_vccnz NQ4R_%=\n\t"
      "NQ4B_%=:\n\t"
      "ds_write_b128 v243, v[184:187]\n\t"
      "ds_write_b128 v164, v[188:191]\n\t"
      "ds_write_b128 v165, v[192:195]\n\t"
      "s_nop 0\n\t"
      "ds_write_b128 v166, v[196:199]\n\t"
      "s_waitcnt vmcnt(7)\n\t"
      "v_mfma_f32_16x16x32_bf16 v[200:203], v[0:3], v[128:131], v[236:239]\n\t"
      "s_waitcnt vmcnt(6)\n\t"
      "v_mfma_f32_16x16x32_bf16 v[200:203], v[16:19], v[132:135], v[200:203]\n\t"
      "v_mfma_f32_16x16x32_bf16 v[204:207], v[4:7], v[128:131], 0\n\t"
      "v_mfma_f32_16x16x32_bf16 v[204:207], v[20:23], v[132:135], v[204:207]\n\t"
      "v_mfma_f32_16x16x32_bf16 v[208:211], v[8:11], v[128:131], 0\n\t"
      "v_mfma_f32_16x16x32_bf16 v[208:211], v[24:27], v[132:135], v[208:211]\n\t"
      "v_mfma_f32_16x16x32_bf16 v[212:215], v[12:15], v[128:131], 0\n\t"
      "global_load_dwordx4 v[128:131], v176, s[64:65] sc1\n\t"
      "v_mfma_f32_16x16x32_bf16 v[212:215], v[28:31], v[132:135], v[212:215]\n\t"
      "global_load_dwordx4 v[132:135], v177, s[64:65] sc1\n\t"
      "s_waitcnt vmcnt(7)\n\t"
      "v_mfma_f32_16x16x32_bf16 v[200:203], v[32:35], v[136:139], v[200:203]\n\t"
      "s_waitcnt vmcnt(6)\n\t"
      "v_mfma_f32_16x16x32_bf16 v[200:203], v[48:51], v[140:143], v[200:203]\n\t"
      "v_mfma_f32_16x16x32_bf16 v[204:207], v[36:39], v[136:139], v[204:207]\n\t"
      "v_mfma_f32_16x16x32_bf16 v[204:207], v[52:55], v[140:143], v[204:207]\n\t"
      "v_mfma_f32_16x16x32_bf16 v[208:211], v[40:43], v[136:139], v[208:211]\n\t"
      "v_mfma_f32_16x16x32_bf16 v[208:211], v[56:59], v[140:143], v[208:211]\n\t"
      "v_mfma_f32_16x16x32_bf16 v[212:215], v[44:47], v[136:139], v[212:215]\n\t"
      "global_load_dwordx4 v[136:139], v178, s[64:65] sc1\n\t"
      "v_mfma_f32_16x16x32_bf16 v[212:215], v[60:63], v[140:143], v[212:215]\n\t"
      "global_load_dwordx4 v[140:143], v179, s[64:65] sc1\n\t"
      "s_waitcnt vmcnt(7)\n\t"
      "v_mfma_f32_16x16x32_bf16 v[200:203], v[64:67], v[144:147], v[200:203]\n\t"
      "s_waitcnt vmcnt(6)\n\t"
      "v_mfma_f32_16x16x32_bf16 v[200:203], v[80:83], v[148:151], v[200:203]\n\t"
      "v_mfma_f32_16x16x32_bf16 v[204:207], v[68:71], v[144:147], v[204:207]\n\t"
      "v_mfma_f32_16x16x32_bf16 v[204:207], v[84:87], v[148:151], v[204:207]\n\t"
      "v_mfma_f32_16x16x32_bf16 v[208:211], v[72:75], v[144:147], v[208:211]\n\t"
      "v_mfma_f32_16x16x32_bf16 v[208:211], v[88:91], v[148:151], v[208:211]\n\t"
      "s_waitcnt lgkmcnt(0)\n\t"
      "s_barrier\n\t"
      "s_setprio 2\n\t"
      "v_mfma_f32_16x16x32_bf16 v[212:215], v[76:79], v[144:147], v[212:215]\n\t"
      "global_load_dwordx4 v[144:147], v180, s[64:65] sc1\n\t"
      "v_mfma_f32_16x16x32_bf16 v[212:215], v[92:95], v[148:151], v[212:215]\n\t"
      "global_load_dwordx4 v[148:151], v181, s[64:65] sc1\n\t"
      "s_waitcnt vmcnt(7)\n\t"
      "v_mfma_f32_16x16x32_bf16 v[200:203], v[96:99], v[152:155], v[200:203]\n\t"
      "s_waitcnt vmcnt(6)\n\t"
      "v_mfma_f32_16x16x32_bf16 v[200:203], v[112:115], v[156:159], v[200:203]\n\t"
      "v_mfma_f32_16x16x32_bf16 v[204:207], v[100:103], v[152:155], v[204:207]\n\t"
      "v_mfma_f32_16x16x32_bf16 v[204:207], v[116:119], v[156:159], v[204:207]\n\t"
      "v_mfma_f32_16x16x32_bf16 v[208:211], v[104:107], v[152:155], v[208:211]\n\t"
      "v_mfma_f32_16x16x32_bf16 v[208:211], v[120:123], v[156:159], v[208:211]\n\t"
      "v_mfma_f32_16x16x32_bf16 v[212:215], v[108:111], v[152:155], v[212:215]\n\t"
      "global_load_dwordx4 v[152:155], v182, s[64:65] sc1\n\t"
      "v_mfma_f32_16x16x32_bf16 v[212:215], v[124:127], v[156:159], v[212:215]\n\t"
      "global_load_dwordx4 v[156:159], v183, s[64:65] sc1\n\t"
      "s_setprio 0\n\t"
      "v_cmp_u_f32_e32 vcc, v200, v200\n\t"
      "s_cbranch_vccnz NQ5R_%=\n\t"
      "NQ5B_%=:\n\t"
      "ds_write_b128 v243, v[200:203] offset:32768\n\t"
      "ds_write_b128 v164, v[204:207] offset:32768\n\t"
      "ds_write_b128 v165, v[208:211] offset:32768\n\t"
      "s_nop 0\n\t"
      "ds_write_b128 v166, v[212:215] offset:32768\n\t"
      "s_waitcnt vmcnt(7)\n\t"
      "v_mfma_f32_16x16x32_bf16 v[184:187], v[0:3], v[128:131], v[236:239]\n\t"
      "s_waitcnt vmcnt(6)\n\t"
      "v_mfma_f32_16x16x32_bf16 v[184:187], v[16:19], v[132:135], v[184:187]\n\t"
      "v_mfma_f32_16x16x32_bf16 v[188:191], v[4:7], v[128:131], 0\n\t"
      "v_mfma_f32_16x16x32_bf16 v[188:191], v[20:23], v[132:135], v[188:191]\n\t"
      "v_mfma_f32_16x16x32_bf16 v[192:195], v[8:11], v[128:131], 0\n\t"
      "v_mfma_f32_16x16x32_bf16 v[192:195], v[24:27], v[132:135], v[192:195]\n\t"
      "v_mfma_f32_16x16x32_bf16 v[196:199], v[12:15], v[128:131], 0\n\t"
      "v_mfma_f32_16x16x32_bf16 v[196:199], v[28:31], v[132:135], v[196:199]\n\t"
      "s_waitcnt vmcnt(5)\n\t"
      "v_mfma_f32_16x16x32_bf16 v[184:187], v[32:35], v[136:139], v[184:187]\n\t"
      "s_waitcnt vmcnt(4)\n\t"
      "v_mfma_f32_16x16x32_bf16 v[184:187], v[48:51], v[140:143], v[184:187]\n\t"
      "v_mfma_f32_16x16x32_bf16 v[188:191], v[36:39], v[136:139], v[188:191]\n\t"
      "v_mfma_f32_16x16x32_bf16 v[188:191], v[52:55], v[140:143], v[188:191]\n\t"
      "v_mfma_f32_16x16x32_bf16 v[192:195], v[40:43], v[136:139], v[192:195]\n\t"
      "v_mfma_f32_16x16x32_bf16 v[192:195], v[56:59], v[140:143], v[192:195]\n\t"
      "v_mfma_f32_16x16x32_bf16 v[196:199], v[44:47], v[136:139], v[196:199]\n\t"
      "v_mfma_f32_16x16x32_bf16 v[196:199], v[60:63], v[140:143], v[196:199]\n\t"
      "s_waitcnt vmcnt(3)\n\t"
      "v_mfma_f32_16x16x32_bf16 v[184:187], v[64:67], v[144:147], v[184:187]\n\t"
      "s_waitcnt vmcnt(2)\n\t"
      "v_mfma_f32_16x16x32_bf16 v[184:187], v[80:83], v[148:151], v[184:187]\n\t"
      "v_mfma_f32_16x16x32_bf16 v[188:191], v[68:71], v[144:147], v[188:191]\n\t"
      "v_mfma_f32_16x16x32_bf16 v[188:191], v[84:87], v[148:151], v[188:191]\n\t"
      "v_mfma_f32_16x16x32_bf16 v[192:195], v[72:75], v[144:147], v[192:195]\n\t"
      "v_mfma_f32_16x16x32_bf16 v[192:195], v[88:91], v[148:151], v[192:195]\n\t"
      "s_waitcnt lgkmcnt(0)\n\t"
      "s_barrier\n\t"
      "s_setprio 2\n\t"
      "v_mfma_f32_16x16x32_bf16 v[196:199], v[76:79], v[144:147], v[196:199]\n\t"
      "v_mfma_f32_16x16x32_bf16 v[196:199], v[92:95], v[148:151], v[196:199]\n\t"
      "s_waitcnt vmcnt(1)\n\t"
      "v_mfma_f32_16x16x32_bf16 v[184:187], v[96:99], v[152:155], v[184:187]\n\t"
      "s_waitcnt vmcnt(0)\n\t"
      "v_mfma_f32_16x16x32_bf16 v[184:187], v[112:115], v[156:159], v[184:187]\n\t"
      "v_mfma_f32_16x16x32_bf16 v[188:191], v[100:103], v[152:155], v[188:191]\n\t"
      "v_mfma_f32_16x16x32_bf16 v[188:191], v[116:119], v[156:159], v[188:191]\n\t"
      "v_mfma_f32_16x16x32_bf16 v[192:195], v[104:107], v[152:155], v[192:195]\n\t"
      "v_mfma_f32_16x16x32_bf16 v[192:195], v[120:123], v[156:159], v[192:195]\n\t"
      "v_mfma_f32_16x16x32_bf16 v[196:199], v[108:111], v[152:155], v[196:199]\n\t"
      "v_mfma_f32_16x16x32_bf16 v[196:199], v[124:127], v[156:159], v[196:199]\n\t"
      "s_nop 1\n\t"
      "v_cmp_u_f32_e32 vcc, v184, v184\n\t"
      "s_cbranch_vccnz NQ6R_%=\n\t"
      "NQ6B_%=:\n\t"
      "ds_write_b128 v243, v[184:187]\n\t"
      "ds_write_b128 v164, v[188:191]\n\t"
      "ds_write_b128 v165, v[192:195]\n\t"
      "s_nop 0\n\t"
      "ds_write_b128 v166, v[196:199]\n\t"
      "s_waitcnt lgkmcnt(0)\n\t"
      "s_barrier\n\t"
      "s_branch GX_%=\n\t"
      "ASP_%=:\n\t"
      "s_mov_b32 m0, 163328\n\t"
      "s_cmp_ge_u32 s17, 65536\n\t"
      "s_cbranch_scc0 BOT_%=\n\t"
      "s_barrier\n\t"
      "BOT_%=:\n\t"
      "global_load_lds_dword v241, s[4:5]\n\t"
      "s_add_u32 s4, s4, 229376\n\t"
      "s_addc_u32 s5, s5, 0\n\t"
      "global_load_lds_dword v241, s[4:5]\n\t"
      "s_add_u32 s4, s4, 229376\n\t"
      "s_addc_u32 s5, s5, 0\n\t"
      "s_waitcnt vmcnt(0)\n\t"
      "s_sleep 1\n\t"
      "s_getpc_b64 s[44:45]\n\t"
      "s_add_u32 s44, s44, _Z11fused_blockPKcS0_S0_S0_S0_S0_S0_PcS1_@rel32@lo+4\n\t"
      "s_addc_u32 s45, s45, _Z11fused_blockPKcS0_S0_S0_S0_S0_S0_PcS1_@rel32@hi+12\n\t"
      "s_lshr_b32 s46, s17, 8\n\t"
      "v_lshrrev_b32_e32 v0, 4, v240\n\t"
      "v_add_u32_e32 v0, s46, v0\n\t"
      "v_min_u32_e32 v0, 463, v0\n\t"
      "v_lshlrev_b32_e32 v0, 7, v0\n\t"
      "global_load_lds_dword v0, s[44:45] sc1\n\t"
      "s_branch AEND_%=\n\t"
      "FQ0R_%=:\n\t"
      "s_mov_b32 s51, 0\n\t"
      "FQ0L_%=:\n\t"
      "s_add_u32 s51, s51, 1\n\t"
      "s_cmp_lt_u32 s51, 262144\n\t"
      "s_cbranch_scc0 FQ0X_%=\n\t"
      "s_waitcnt vmcnt(0)\n\t"
      "s_sleep 1\n\t"
      "global_load_dwordx4 v[128:131], v176, s[52:53] sc1\n\t"
      "global_load_dwordx4 v[132:135], v177, s[52:53] sc1\n\t"
      "global_load_dwordx4 v[136:139], v178, s[52:53] sc1\n\t"
      "global_load_dwordx4 v[140:143], v179, s[52:53] sc1\n\t"
      "global_load_dwordx4 v[144:147], v180, s[52:53] sc1\n\t"
      "global_load_dwordx4 v[148:151], v181, s[52:53] sc1\n\t"
      "global_load_dwordx4 v[152:155], v182, s[52:53] sc1\n\t"
      "global_load_dwordx4 v[156:159], v183, s[52:53] sc1\n\t"
      "s_waitcnt vmcnt(7)\n\t"
      "v_max3_u32 v234, v128, v129, v130\n\t"
      "v_max_u32_e32 v234, v234, v131\n\t"
      "v_cmp_eq_u32_e32 vcc, -1, v234\n\t"
      "s_cbranch_vccnz FQ0L_%=\n\t"
      "FQ0X_%=:\n\t"
      "s_branch FQ0B_%=\n\t"
      "FQ1R_%=:\n\t"
      "s_mov_b32 s51, 0\n\t"
      "FQ1L_%=:\n\t"
      "s_add_u32 s51, s51, 1\n\t"
      "s_cmp_lt_u32 s51, 262144\n\t"
      "s_cbranch_scc0 FQ1X_%=\n\t"
      "s_waitcnt vmcnt(0)\n\t"
      "s_sleep 1\n\t"
      "global_load_dwordx4 v[132:135], v177, s[52:53] sc1\n\t"
      "global_load_dwordx4 v[136:139], v178, s[52:53] sc1\n\t"
      "global_load_dwordx4 v[140:143], v179, s[52:53] sc1\n\t"
      "global_load_dwordx4 v[144:147], v180, s[52:53] sc1\n\t"
      "global_load_dwordx4 v[148:151], v181, s[52:53] sc1\n\t"
      "global_load_dwordx4 v[152:155], v182, s[52:53] sc1\n\t"
      "global_load_dwordx4 v[156:159], v183, s[52:53] sc1\n\t"
      "s_waitcnt vmcnt(6)\n\t"
      "v_max3_u32 v234, v132, v133, v134\n\t"
      "v_max_u32_e32 v234, v234, v135\n\t"
      "v_cmp_eq_u32_e32 vcc, -1, v234\n\t"
      "s_cbranch_vccnz FQ1L_%=\n\t"
      "FQ1X_%=:\n\t"
      "s_branch FQ1B_%=\n\t"
      "FQ2R_%=:\n\t"
      "s_mov_b32 s51, 0\n\t"
      "FQ2L_%=:\n\t"
      "s_add_u32 s51, s51, 1\n\t"
      "s_cmp_lt_u32 s51, 262144\n\t"
      "s_cbranch_scc0 FQ2X_%=\n\t"
      "s_waitcnt vmcnt(0)\n\t"
      "s_sleep 1\n\t"
      "global_load_dwordx4 v[136:139], v178, s[52:53] sc1\n\t"
      "global_load_dwordx4 v[140:143], v179, s[52:53] sc1\n\t"
      "global_load_dwordx4 v[144:147], v180, s[52:53] sc1\n\t"
      "global_load_dwordx4 v[148:151], v181, s[52:53] sc1\n\t"
      "global_load_dwordx4 v[152:155], v182, s[52:53] sc1\n\t"
      "global_load_dwordx4 v[156:159], v183, s[52:53] sc1\n\t"
      "global_load_dwordx4 v[128:131], v176, s[54:55] sc1\n\t"
      "global_load_dwordx4 v[132:135], v177, s[54:55] sc1\n\t"
      "s_waitcnt vmcnt(7)\n\t"
      "v_max3_u32 v234, v136, v137, v138\n\t"
      "v_max_u32_e32 v234, v234, v139\n\t"
      "v_cmp_eq_u32_e32 vcc, -1, v234\n\t"
      "s_cbranch_vccnz FQ2L_%=\n\t"
      "FQ2X_%=:\n\t"
      "s_branch FQ2B_%=\n\t"
      "FQ3R_%=:\n\t"
      "s_mov_b32 s51, 0\n\t"
      "FQ3L_%=:\n\t"
      "s_add_u32 s51, s51, 1\n\t"
      "s_cmp_lt_u32 s51, 262144\n\t"
      "s_cbranch_scc0 FQ3X_%=\n\t"
      "s_waitcnt vmcnt(0)\n\t"
      "s_sleep 1\n\t"
      "global_load_dwordx4 v[140:143], v179, s[52:53] sc1\n\t"
      "global_load_dwordx4 v[144:147], v180, s[52:53] sc1\n\t"
      "global_load_dwordx4 v[148:151], v181, s[52:53] sc1\n\t"
      "global_load_dwordx4 v[152:155], v182, s[52:53] sc1\n\t"
      "global_load_dwordx4 v[156:159], v183, s[52:53] sc1\n\t"
      "global_load_dwordx4 v[128:131], v176, s[54:55] sc1\n\t"
      "global_load_dwordx4 v[132:135], v177, s[54:55] sc1\n\t"
      "s_waitcnt vmcnt(6)\n\t"
      "v_max3_u32 v234, v140, v141, v142\n\t"
      "v_max_u32_e32 v234, v234, v143\n\t"
      "v_cmp_eq_u32_e32 vcc, -1, v234\n\t"
      "s_cbranch_vccnz FQ3L_%=\n\t"
      "FQ3X_%=:\n\t"
      "s_branch FQ3B_%=\n\t"
      "FQ4R_%=:\n\t"
      "s_mov_b32 s51, 0\n\t"
      "FQ4L_%=:\n\t"
      "s_add_u32 s51, s51, 1\n\t"
      "s_cmp_lt_u32 s51, 262144\n\t"
      "s_cbranch_scc0 FQ4X_%=\n\t"
      "s_waitcnt vmcnt(0)\n\t"
      "s_sleep 1\n\t"
      "global_load_dwordx4 v[144:147], v180, s[52:53] sc1\n\t"
      "global_load_dwordx4 v[148:151], v181, s[52:53] sc1\n\t"
      "global_load_dwordx4 v[152:155], v182, s[52:53] sc1\n\t"
      "global_load_dwordx4 v[156:159], v183, s[52:53] sc1\n\t"
      "global_load_dwordx4 v[128:131], v176, s[54:55] sc1\n\t"
      "global_load_dwordx4 v[132:135], v177, s[54:55] sc1\n\t"
      "global_load_dwordx4 v[136:139], v178, s[54:55] sc1\n\t"
      "global_load_dwordx4 v[140:143], v179, s[54:55] sc1\n\t"
      "s_waitcnt vmcnt(7)\n\t"
      "v_max3_u32 v234, v144, v145, v146\n\t"
      "v_max_u32_e32 v234, v234, v147\n\t"
      "v_cmp_eq_u32_e32 vcc, -1, v234\n\t"
      "s_cbranch_vccnz FQ4L_%=\n\t"
      "FQ4X_%=:\n\t"
      "s_branch FQ4B_%=\n\t"
      "FQ5R_%=:\n\t"
      "s_mov_b32 s51, 0\n\t"
      "FQ5L_%=:\n\t"
      "s_add_u32 s51, s51, 1\n\t"
      "s_cmp_lt_u32 s51, 262144\n\t"
      "s_cbranch_scc0 FQ5X_%=\n\t"
      "s_waitcnt vmcnt(0)\n\t"
      "s_sleep 1\n\t"
      "global_load_dwordx4 v[148:151], v181, s[52:53] sc1\n\t"
      "global_load_dwordx4 v[152:155], v182, s[52:53] sc1\n\t"
      "global_load_dwordx4 v[156:159], v183, s[52:53] sc1\n\t"
      "global_load_dwordx4 v[128:131], v176, s[54:55] sc1\n\t"
      "global_load_dwordx4 v[132:135], v177, s[54:55] sc1\n\t"
      "global_load_dwordx4 v[136:139], v178, s[54:55] sc1\n\t"
      "global_load_dwordx4 v[140:143], v179, s[54:55] sc1\n\t"
      "s_waitcnt vmcnt(6)\n\t"
      "v_max3_u32 v234, v148, v149, v150\n\t"
      "v_max_u32_e32 v234, v234, v151\n\t"
      "v_cmp_eq_u32_e32 vcc, -1, v234\n\t"
      "s_cbranch_vccnz FQ5L_%=\n\t"
      "FQ5X_%=:\n\t"
      "s_branch FQ5B_%=\n\t"
      "FQ6R_%=:\n\t"
      "s_mov_b32 s51, 0\n\t"
      "FQ6L_%=:\n\t"
      "s_add_u32 s51, s51, 1\n\t"
      "s_cmp_lt_u32 s51, 262144\n\t"
      "s_cbranch_scc0 FQ6X_%=\n\t"
      "s_waitcnt vmcnt(0)\n\t"
      "s_sleep 1\n\t"
      "global_load_dwordx4 v[152:155], v182, s[52:53] sc1\n\t"
      "global_load_dwordx4 v[156:159], v183, s[52:53] sc1\n\t"
      "global_load_dwordx4 v[128:131], v176, s[54:55] sc1\n\t"
      "global_load_dwordx4 v[132:135], v177, s[54:55] sc1\n\t"
      "global_load_dwordx4 v[136:139], v178, s[54:55] sc1\n\t"
      "global_load_dwordx4 v[140:143], v179, s[54:55] sc1\n\t"
      "global_load_dwordx4 v[144:147], v180, s[54:55] sc1\n\t"
      "global_load_dwordx4 v[148:151], v181, s[54:55] sc1\n\t"
      "s_waitcnt vmcnt(7)\n\t"
      "v_max3_u32 v234, v152, v153, v154\n\t"
      "v_max_u32_e32 v234, v234, v155\n\t"
      "v_cmp_eq_u32_e32 vcc, -1, v234\n\t"
      "s_cbranch_vccnz FQ6L_%=\n\t"
      "FQ6X_%=:\n\t"
      "s_branch FQ6B_%=\n\t"
      "FQ7R_%=:\n\t"
      "s_mov_b32 s51, 0\n\t"
      "FQ7L_%=:\n\t"
      "s_add_u32 s51, s51, 1\n\t"
      "s_cmp_lt_u32 s51, 262144\n\t"
      "s_cbranch_scc0 FQ7X_%=\n\t"
      "s_waitcnt vmcnt(0)\n\t"
      "s_sleep 1\n\t"
      "global_load_dwordx4 v[156:159], v183, s[52:53] sc1\n\t"
      "global_load_dwordx4 v[128:131], v176, s[54:55] sc1\n\t"
      "global_load_dwordx4 v[132:135], v177, s[54:55] sc1\n\t"
      "global_load_dwordx4 v[136:139], v178, s[54:55] sc1\n\t"
      "global_load_dwordx4 v[140:143], v179, s[54:55] sc1\n\t"
      "global_load_dwordx4 v[144:147], v180, s[54:55] sc1\n\t"
      "global_load_dwordx4 v[148:151], v181, s[54:55] sc1\n\t"
      "s_waitcnt vmcnt(6)\n\t"
      "v_max3_u32 v234, v156, v157, v158\n\t"
      "v_max_u32_e32 v234, v234, v159\n\t"
      "v_cmp_eq_u32_e32 vcc, -1, v234\n\t"
      "s_cbranch_vccnz FQ7L_%=\n\t"
      "FQ7X_%=:\n\t"
      "s_branch FQ7B_%=\n\t"
      "NQ1R_%=:\n\t"
      "s_mov_b32 s51, 0\n\t"
      "NQ1L_%=:\n\t"
      "s_add_u32 s51, s51, 1\n\t"
      "s_cmp_lt_u32 s51, 262144\n\t"
      "s_cbranch_scc0 NQ1X_%=\n\t"
      "s_waitcnt vmcnt(0)\n\t"
      "s_sleep 1\n\t"
      "global_load_dwordx4 v[128:131], v176, s[54:55] sc1\n\t"
      "global_load_dwordx4 v[132:135], v177, s[54:55] sc1\n\t"
      "global_load_dwordx4 v[136:139], v178, s[54:55] sc1\n\t"
      "global_load_dwordx4 v[140:143], v179, s[54:55] sc1\n\t"
      "global_load_dwordx4 v[144:147], v180, s[54:55] sc1\n\t"
      "global_load_dwordx4 v[148:151], v181, s[54:55] sc1\n\t"
      "global_load_dwordx4 v[152:155], v182, s[54:55] sc1\n\t"
      "global_load_dwordx4 v[156:159], v183, s[54:55] sc1\n\t"
      "s_waitcnt vmcnt(0)\n\t"
      "v_max3_u32 v234, v128, v129, v130\n\t"
      "v_max3_u32 v234, v234, v131, v132\n\t"
      "v_max3_u32 v234, v234, v133, v134\n\t"
      "v_max3_u32 v234, v234, v135, v136\n\t"
      "v_max3_u32 v234, v234, v137, v138\n\t"
      "v_max3_u32 v234, v234, v139, v140\n\t"
      "v_max3_u32 v234, v234, v141, v142\n\t"
      "v_max3_u32 v234, v234, v143, v144\n\t"
      "v_max3_u32 v234, v234, v145, v146\n\t"
      "v_max3_u32 v234, v234, v147, v148\n\t"
      "v_max3_u32 v234, v234, v149, v150\n\t"
      "v_max3_u32 v234, v234, v151, v152\n\t"
      "v_max3_u32 v234, v234, v153, v154\n\t"
      "v_max3_u32 v234, v234, v155, v156\n\t"
      "v_max3_u32 v234, v234, v157, v158\n\t"
      "v_max_u32_e32 v234, v234, v159\n\t"
      "v_cmp_eq_u32_e32 vcc, -1, v234\n\t"
      "s_cbranch_vccnz NQ1L_%=\n\t"
      "NQ1X_%=:\n\t"
      "v_mfma_f32_16x16x32_bf16 v[200:203], v[0:3], v[128:131], v[236:239]\n\t"
      "v_mfma_f32_16x16x32_bf16 v[204:207], v[4:7], v[128:131], 0\n\t"
      "v_mfma_f32_16x16x32_bf16 v[208:211], v[8:11], v[128:131], 0\n\t"
      "v_mfma_f32_16x16x32_bf16 v[212:215], v[12:15], v[128:131], 0\n\t"
      "v_mfma_f32_16x16x32_bf16 v[200:203], v[16:19], v[132:135], v[200:203]\n\t"
      "v_mfma_f32_16x16x32_bf16 v[204:207], v[20:23], v[132:135], v[204:207]\n\t"
      "v_mfma_f32_16x16x32_bf16 v[208:211], v[24:27], v[132:135], v[208:211]\n\t"
      "v_mfma_f32_16x16x32_bf16 v[212:215], v[28:31], v[132:135], v[212:215]\n\t"
      "v_mfma_f32_16x16x32_bf16 v[200:203], v[32:35], v[136:139], v[200:203]\n\t"
      "v_mfma_f32_16x16x32_bf16 v[204:207], v[36:39], v[136:139], v[204:207]\n\t"
      "v_mfma_f32_16x16x32_bf16 v[208:211], v[40:43], v[136:139], v[208:211]\n\t"
      "v_mfma_f32_16x16x32_bf16 v[212:215], v[44:47], v[136:139], v[212:215]\n\t"
      "v_mfma_f32_16x16x32_bf16 v[200:203], v[48:51], v[140:143], v[200:203]\n\t"
      "v_mfma_f32_16x16x32_bf16 v[204:207], v[52:55], v[140:143], v[204:207]\n\t"
      "v_mfma_f32_16x16x32_bf16 v[208:211], v[56:59], v[140:143], v[208:211]\n\t"
      "v_mfma_f32_16x16x32_bf16 v[212:215], v[60:63], v[140:143], v[212:215]\n\t"
      "v_mfma_f32_16x16x32_bf16 v[200:203], v[64:67], v[144:147], v[200:203]\n\t"
      "v_mfma_f32_16x16x32_bf16 v[204:207], v[68:71], v[144:147], v[204:207]\n\t"
      "v_mfma_f32_16x16x32_bf16 v[208:211], v[72:75], v[144:147], v[208:211]\n\t"
      "v_mfma_f32_16x16x32_bf16 v[212:215], v[76:79], v[144:147], v[212:215]\n\t"
      "v_mfma_f32_16x16x32_bf16 v[200:203], v[80:83], v[148:151], v[200:203]\n\t"
      "v_mfma_f32_16x16x32_bf16 v[204:207], v[84:87], v[148:151], v[204:207]\n\t"
      "v_mfma_f32_16x16x32_bf16 v[208:211], v[88:91], v[148:151], v[208:211]\n\t"
      "v_mfma_f32_16x16x32_bf16 v[212:215], v[92:95], v[148:151], v[212:215]\n\t"
      "v_mfma_f32_16x16x32_bf16 v[200:203], v[96:99], v[152:155], v[200:203]\n\t"
      "v_mfma_f32_16x16x32_bf16 v[204:207], v[100:103], v[152:155], v[204:207]\n\t"
      "v_mfma_f32_16x16x32_bf16 v[208:211], v[104:107], v[152:155], v[208:211]\n\t"
      "v_mfma_f32_16x16x32_bf16 v[212:215], v[108:111], v[152:155], v[212:215]\n\t"
      "v_mfma_f32_16x16x32_bf16 v[200:203], v[112:115], v[156:159], v[200:203]\n\t"
      "v_mfma_f32_16x16x32_bf16 v[204:207], v[116:119], v[156:159], v[204:207]\n\t"
      "v_mfma_f32_16x16x32_bf16 v[208:211], v[120:123], v[156:159], v[208:211]\n\t"
      "v_mfma_f32_16x16x32_bf16 v[212:215], v[124:127], v[156:159], v[212:215]\n\t"
      "global_load_dwordx4 v[128:131], v176, s[56:57] sc1\n\t"
      "global_load_dwordx4 v[132:135], v177, s[56:57] sc1\n\t"
      "global_load_dwordx4 v[136:139], v178, s[56:57] sc1\n\t"
      "global_load_dwordx4 v[140:143], v179, s[56:57] sc1\n\t"
      "global_load_dwordx4 v[144:147], v180, s[56:57] sc1\n\t"
      "global_load_dwordx4 v[148:151], v181, s[56:57] sc1\n\t"
      "global_load_dwordx4 v[152:155], v182, s[56:57] sc1\n\t"
      "global_load_dwordx4 v[156:159], v183, s[56:57] sc1\n\t"
      "s_waitcnt vmcnt(0)\n\t"
      "s_nop 7\n\t"
      "s_branch NQ1B_%=\n\t"
      "NQ2R_%=:\n\t"
      "s_mov_b32 s51, 0\n\t"
      "NQ2L_%=:\n\t"
      "s_add_u32 s51, s51, 1\n\t"
      "s_cmp_lt_u32 s51, 262144\n\t"
      "s_cbranch_scc0 NQ2X_%=\n\t"
      "s_waitcnt vmcnt(0)\n\t"
      "s_sleep 1\n\t"
      "global_load_dwordx4 v[128:131], v176, s[56:57] sc1\n\t"
      "global_load_dwordx4 v[132:135], v177, s[56:57] sc1\n\t"
      "global_load_dwordx4 v[136:139], v178, s[56:57] sc1\n\t"
      "global_load_dwordx4 v[140:143], v179, s[56:57] sc1\n\t"
      "global_load_dwordx4 v[144:147], v180, s[56:57] sc1\n\t"
      "global_load_dwordx4 v[148:151], v181, s[56:57] sc1\n\t"
      "global_load_dwordx4 v[152:155], v182, s[56:57] sc1\n\t"
      "global_load_dwordx4 v[156:159], v183, s[56:57] sc1\n\t"
      "s_waitcnt vmcnt(0)\n\t"
      "v_max3_u32 v234, v128, v129, v130\n\t"
      "v_max3_u32 v234, v234, v131, v132\n\t"
      "v_max3_u32 v234, v234, v133, v134\n\t"
      "v_max3_u32 v234, v234, v135, v136\n\t"
      "v_max3_u32 v234, v234, v137, v138\n\t"
      "v_max3_u32 v234, v234, v139, v140\n\t"
      "v_max3_u32 v234, v234, v141, v142\n\t"
      "v_max3_u32 v234, v234, v143, v144\n\t"
      "v_max3_u32 v234, v234, v145, v146\n\t"
      "v_max3_u32 v234, v234, v147, v148\n\t"
      "v_max3_u32 v234, v234, v149, v150\n\t"
      "v_max3_u32 v234, v234, v151, v152\n\t"
      "v_max3_u32 v234, v234, v153, v154\n\t"
      "v_max3_u32 v234, v234, v155, v156\n\t"
      "v_max3_u32 v234, v234, v157, v158\n\t"
      "v_max_u32_e32 v234, v234, v159\n\t"
      "v_cmp_eq_u32_e32 vcc, -1, v234\n\t"
      "s_cbranch_vccnz NQ2L_%=\n\t"
      "NQ2X_%=:\n\t"
      "v_mfma_f32_16x16x32_bf16 v[184:187], v[0:3], v[128:131], v[236:239]\n\t"
      "v_mfma_f32_16x16x32_bf16 v[188:191], v[4:7], v[128:131], 0\n\t"
      "v_mfma_f32_16x16x32_bf16 v[192:195], v[8:11], v[128:131], 0\n\t"
      "v_mfma_f32_16x16x32_bf16 v[196:199], v[12:15], v[128:131], 0\n\t"
      "v_mfma_f32_16x16x32_bf16 v[184:187], v[16:19], v[132:135], v[184:187]\n\t"
      "v_mfma_f32_16x16x32_bf16 v[188:191], v[20:23], v[132:135], v[188:191]\n\t"
      "v_mfma_f32_16x16x32_bf16 v[192:195], v[24:27], v[132:135], v[192:195]\n\t"
      "v_mfma_f32_16x16x32_bf16 v[196:199], v[28:31], v[132:135], v[196:199]\n\t"
      "v_mfma_f32_16x16x32_bf16 v[184:187], v[32:35], v[136:139], v[184:187]\n\t"
      "v_mfma_f32_16x16x32_bf16 v[188:191], v[36:39], v[136:139], v[188:191]\n\t"
      "v_mfma_f32_16x16x32_bf16 v[192:195], v[40:43], v[136:139], v[192:195]\n\t"
      "v_mfma_f32_16x16x32_bf16 v[196:199], v[44:47], v[136:139], v[196:199]\n\t"
      "v_mfma_f32_16x16x32_bf16 v[184:187], v[48:51], v[140:143], v[184:187]\n\t"
      "v_mfma_f32_16x16x32_bf16 v[188:191], v[52:55], v[140:143], v[188:191]\n\t"
      "v_mfma_f32_16x16x32_bf16 v[192:195], v[56:59], v[140:143], v[192:195]\n\t"
      "v_mfma_f32_16x16x32_bf16 v[196:199], v[60:63], v[140:143], v[196:199]\n\t"
      "v_mfma_f32_16x16x32_bf16 v[184:187], v[64:67], v[144:147], v[184:187]\n\t"
      "v_mfma_f32_16x16x32_bf16 v[188:191], v[68:71], v[144:147], v[188:191]\n\t"
      "v_mfma_f32_16x16x32_bf16 v[192:195], v[72:75], v[144:147], v[192:195]\n\t"
      "v_mfma_f32_16x16x32_bf16 v[196:199], v[76:79], v[144:147], v[196:199]\n\t"
      "v_mfma_f32_16x16x32_bf16 v[184:187], v[80:83], v[148:151], v[184:187]\n\t"
      "v_mfma_f32_16x16x32_bf16 v[188:191], v[84:87], v[148:151], v[188:191]\n\t"
      "v_mfma_f32_16x16x32_bf16 v[192:195], v[88:91], v[148:151], v[192:195]\n\t"
      "v_mfma_f32_16x16x32_bf16 v[196:199], v[92:95], v[148:151], v[196:199]\n\t"
      "v_mfma_f32_16x16x32_bf16 v[184:187], v[96:99], v[152:155], v[184:187]\n\t"
      "v_mfma_f32_16x16x32_bf16 v[188:191], v[100:103], v[152:155], v[188:191]\n\t"
      "v_mfma_f32_16x16x32_bf16 v[192:195], v[104:107], v[152:155], v[192:195]\n\t"
      "v_mfma_f32_16x16x32_bf16 v[196:199], v[108:111], v[152:155], v[196:199]\n\t"
      "v_mfma_f32_16x16x32_bf16 v[184:187], v[112:115], v[156:159], v[184:187]\n\t"
      "v_mfma_f32_16x16x32_bf16 v[188:191], v[116:119], v[156:159], v[188:191]\n\t"
      "v_mfma_f32_16x16x32_bf16 v[192:195], v[120:123], v[156:159], v[192:195]\n\t"
      "v_mfma_f32_16x16x32_bf16 v[196:199], v[124:127], v[156:159], v[196:199]\n\t"
      "global_load_dwordx4 v[128:131], v176, s[58:59] sc1\n\t"
      "global_load_dwordx4 v[132:135], v177, s[58:59] sc1\n\t"
      "global_load_dwordx4 v[136:139], v178, s[58:59] sc1\n\t"
      "global_load_dwordx4 v[140:143], v179, s[58:59] sc1\n\t"
      "global_load_dwordx4 v[144:147], v180, s[58:59] sc1\n\t"
      "global_load_dwordx4 v[148:151], v181, s[58:59] sc1\n\t"
      "global_load_dwordx4 v[152:155], v182, s[58:59] sc1\n\t"
      "global_load_dwordx4 v[156:159], v183, s[58:59] sc1\n\t"
      "s_waitcnt vmcnt(0)\n\t"
      "s_nop 7\n\t"
      "s_branch NQ2B_%=\n\t"
      "NQ3R_%=:\n\t"
      "s_mov_b32 s51, 0\n\t"
      "NQ3L_%=:\n\t"
      "s_add_u32 s51, s51, 1\n\t"
      "s_cmp_lt_u32 s51, 262144\n\t"
      "s_cbranch_scc0 NQ3X_%=\n\t"
      "s_waitcnt vmcnt(0)\n\t"
      "s_sleep 1\n\t"
      "global_load_dwordx4 v[128:131], v176, s[58:59] sc1\n\t"
      "global_load_dwordx4 v[132:135], v177, s[58:59] sc1\n\t"
      "global_load_dwordx4 v[136:139], v178, s[58:59] sc1\n\t"
      "global_load_dwordx4 v[140:143], v179, s[58:59] sc1\n\t"
      "global_load_dwordx4 v[144:147], v180, s[58:59] sc1\n\t"
      "global_load_dwordx4 v[148:151], v181, s[58:59] sc1\n\t"
      "global_load_dwordx4 v[152:155], v182, s[58:59] sc1\n\t"
      "global_load_dwordx4 v[156:159], v183, s[58:59] sc1\n\t"
      "s_waitcnt vmcnt(0)\n\t"
      "v_max3_u32 v234, v128, v129, v130\n\t"
      "v_max3_u32 v234, v234, v131, v132\n\t"
      "v_max3_u32 v234, v234, v133, v134\n\t"
      "v_max3_u32 v234, v234, v135, v136\n\t"
      "v_max3_u32 v234, v234, v137, v138\n\t"
      "v_max3_u32 v234, v234, v139, v140\n\t"
      "v_max3_u32 v234, v234, v141, v142\n\t"
      "v_max3_u32 v234, v234, v143, v144\n\t"
      "v_max3_u32 v234, v234, v145, v146\n\t"
      "v_max3_u32 v234, v234, v147, v148\n\t"
      "v_max3_u32 v234, v234, v149, v150\n\t"
      "v_max3_u32 v234, v234, v151, v152\n\t"
      "v_max3_u32 v234, v234, v153, v154\n\t"
      "v_max3_u32 v234, v234, v155, v156\n\t"
      "v_max3_u32 v234, v234, v157, v158\n\t"
      "v_max_u32_e32 v234, v234, v159\n\t"
      "v_cmp_eq_u32_e32 vcc, -1, v234\n\t"
      "s_cbranch_vccnz NQ3L_%=\n\t"
      "NQ3X_%=:\n\t"
      "v_mfma_f32_16x16x32_bf16 v[200:203], v[0:3], v[128:131], v[236:239]\n\t"
      "v_mfma_f32_16x16x32_bf16 v[204:207], v[4:7], v[128:131], 0\n\t"
      "v_mfma_f32_16x16x32_bf16 v[208:211], v[8:11], v[128:131], 0\n\t"
      "v_mfma_f32_16x16x32_bf16 v[212:215], v[12:15], v[128:131], 0\n\t"
      "v_mfma_f32_16x16x32_bf16 v[200:203], v[16:19], v[132:135], v[200:203]\n\t"
      "v_mfma_f32_16x16x32_bf16 v[204:207], v[20:23], v[132:135], v[204:207]\n\t"
      "v_mfma_f32_16x16x32_bf16 v[208:211], v[24:27], v[132:135], v[208:211]\n\t"
      "v_mfma_f32_16x16x32_bf16 v[212:215], v[28:31], v[132:135], v[212:215]\n\t"
      "v_mfma_f32_16x16x32_bf16 v[200:203], v[32:35], v[136:139], v[200:203]\n\t"
      "v_mfma_f32_16x16x32_bf16 v[204:207], v[36:39], v[136:139], v[204:207]\n\t"
      "v_mfma_f32_16x16x32_bf16 v[208:211], v[40:43], v[136:139], v[208:211]\n\t"
      "v_mfma_f32_16x16x32_bf16 v[212:215], v[44:47], v[136:139], v[212:215]\n\t"
      "v_mfma_f32_16x16x32_bf16 v[200:203], v[48:51], v[140:143], v[200:203]\n\t"
      "v_mfma_f32_16x16x32_bf16 v[204:207], v[52:55], v[140:143], v[204:207]\n\t"
      "v_mfma_f32_16x16x32_bf16 v[208:211], v[56:59], v[140:143], v[208:211]\n\t"
      "v_mfma_f32_16x16x32_bf16 v[212:215], v[60:63], v[140:143], v[212:215]\n\t"
      "v_mfma_f32_16x16x32_bf16 v[200:203], v[64:67], v[144:147], v[200:203]\n\t"
      "v_mfma_f32_16x16x32_bf16 v[204:207], v[68:71], v[144:147], v[204:207]\n\t"
      "v_mfma_f32_16x16x32_bf16 v[208:211], v[72:75], v[144:147], v[208:211]\n\t"
      "v_mfma_f32_16x16x32_bf16 v[212:215], v[76:79], v[144:147], v[212:215]\n\t"
      "v_mfma_f32_16x16x32_bf16 v[200:203], v[80:83], v[148:151], v[200:203]\n\t"
      "v_mfma_f32_16x16x32_bf16 v[204:207], v[84:87], v[148:151], v[204:207]\n\t"
      "v_mfma_f32_16x16x32_bf16 v[208:211], v[88:91], v[148:151], v[208:211]\n\t"
      "v_mfma_f32_16x16x32_bf16 v[212:215], v[92:95], v[148:151], v[212:215]\n\t"
      "v_mfma_f32_16x16x32_bf16 v[200:203], v[96:99], v[152:155], v[200:203]\n\t"
      "v_mfma_f32_16x16x32_bf16 v[204:207], v[100:103], v[152:155], v[204:207]\n\t"
      "v_mfma_f32_16x16x32_bf16 v[208:211], v[104:107], v[152:155], v[208:211]\n\t"
      "v_mfma_f32_16x16x32_bf16 v[212:215], v[108:111], v[152:155], v[212:215]\n\t"
      "v_mfma_f32_16x16x32_bf16 v[200:203], v[112:115], v[156:159], v[200:203]\n\t"
      "v_mfma_f32_16x16x32_bf16 v[204:207], v[116:119], v[156:159], v[204:207]\n\t"
      "v_mfma_f32_16x16x32_bf16 v[208:211], v[120:123], v[156:159], v[208:211]\n\t"
      "v_mfma_f32_16x16x32_bf16 v[212:215], v[124:127], v[156:159], v[212:215]\n\t"
      "global_load_dwordx4 v[128:131], v176, s[60:61] sc1\n\t"
      "global_load_dwordx4 v[132:135], v177, s[60:61] sc1\n\t"
      "global_load_dwordx4 v[136:139], v178, s[60:61] sc1\n\t"
      "global_load_dwordx4 v[140:143], v179, s[60:61] sc1\n\t"
      "global_load_dwordx4 v[144:147], v180, s[60:61] sc1\n\t"
      "global_load_dwordx4 v[148:151], v181, s[60:61] sc1\n\t"
      "global_load_dwordx4 v[152:155], v182, s[60:61] sc1\n\t"
      "global_load_dwordx4 v[156:159], v183, s[60:61] sc1\n\t"
      "s_waitcnt vmcnt(0)\n\t"
      "s_nop 7\n\t"
      "s_branch NQ3B_%=\n\t"
      "NQ4R_%=:\n\t"
      "s_mov_b32 s51, 0\n\t"
      "NQ4L_%=:\n\t"
      "s_add_u32 s51, s51, 1\n\t"
      "s_cmp_lt_u32 s51, 262144\n\t"
      "s_cbranch_scc0 NQ4X_%=\n\t"
      "s_waitcnt vmcnt(0)\n\t"
      "s_sleep 1\n\t"
      "global_load_dwordx4 v[128:131], v176, s[60:61] sc1\n\t"
      "global_load_dwordx4 v[132:135], v177, s[60:61] sc1\n\t"
      "global_load_dwordx4 v[136:139], v178, s[60:61] sc1\n\t"
      "global_load_dwordx4 v[140:143], v179, s[60:61] sc1\n\t"
      "global_load_dwordx4 v[144:147], v180, s[60:61] sc1\n\t"
      "global_load_dwordx4 v[148:151], v181, s[60:61] sc1\n\t"
      "global_load_dwordx4 v[152:155], v182, s[60:61] sc1\n\t"
      "global_load_dwordx4 v[156:159], v183, s[60:61] sc1\n\t"
      "s_waitcnt vmcnt(0)\n\t"
      "v_max3_u32 v234, v128, v129, v130\n\t"
      "v_max3_u32 v234, v234, v131, v132\n\t"
      "v_max3_u32 v234, v234, v133, v134\n\t"
      "v_max3_u32 v234, v234, v135, v136\n\t"
      "v_max3_u32 v234, v234, v137, v138\n\t"
      "v_max3_u32 v234, v234, v139, v140\n\t"
      "v_max3_u32 v234, v234, v141, v142\n\t"
      "v_max3_u32 v234, v234, v143, v144\n\t"
      "v_max3_u32 v234, v234, v145, v146\n\t"
      "v_max3_u32 v234, v234, v147, v148\n\t"
      "v_max3_u32 v234, v234, v149, v150\n\t"
      "v_max3_u32 v234, v234, v151, v152\n\t"
      "v_max3_u32 v234, v234, v153, v154\n\t"
      "v_max3_u32 v234, v234, v155, v156\n\t"
      "v_max3_u32 v234, v234, v157, v158\n\t"
      "v_max_u32_e32 v234, v234, v159\n\t"
      "v_cmp_eq_u32_e32 vcc, -1, v234\n\t"
      "s_cbranch_vccnz NQ4L_%=\n\t"
      "NQ4X_%=:\n\t"
      "v_mfma_f32_16x16x32_bf16 v[184:187], v[0:3], v[128:131], v[236:239]\n\t"
      "v_mfma_f32_16x16x32_bf16 v[188:191], v[4:7], v[128:131], 0\n\t"
      "v_mfma_f32_16x16x32_bf16 v[192:195], v[8:11], v[128:131], 0\n\t"
      "v_mfma_f32_16x16x32_bf16 v[196:199], v[12:15], v[128:131], 0\n\t"
      "v_mfma_f32_16x16x32_bf16 v[184:187], v[16:19], v[132:135], v[184:187]\n\t"
      "v_mfma_f32_16x16x32_bf16 v[188:191], v[20:23], v[132:135], v[188:191]\n\t"
      "v_mfma_f32_16x16x32_bf16 v[192:195], v[24:27], v[132:135], v[192:195]\n\t"
      "v_mfma_f32_16x16x32_bf16 v[196:199], v[28:31], v[132:135], v[196:199]\n\t"
      "v_mfma_f32_16x16x32_bf16 v[184:187], v[32:35], v[136:139], v[184:187]\n\t"
      "v_mfma_f32_16x16x32_bf16 v[188:191], v[36:39], v[136:139], v[188:191]\n\t"
      "v_mfma_f32_16x16x32_bf16 v[192:195], v[40:43], v[136:139], v[192:195]\n\t"
      "v_mfma_f32_16x16x32_bf16 v[196:199], v[44:47], v[136:139], v[196:199]\n\t"
      "v_mfma_f32_16x16x32_bf16 v[184:187], v[48:51], v[140:143], v[184:187]\n\t"
      "v_mfma_f32_16x16x32_bf16 v[188:191], v[52:55], v[140:143], v[188:191]\n\t"
      "v_mfma_f32_16x16x32_bf16 v[192:195], v[56:59], v[140:143], v[192:195]\n\t"
      "v_mfma_f32_16x16x32_bf16 v[196:199], v[60:63], v[140:143], v[196:199]\n\t"
      "v_mfma_f32_16x16x32_bf16 v[184:187], v[64:67], v[144:147], v[184:187]\n\t"
      "v_mfma_f32_16x16x32_bf16 v[188:191], v[68:71], v[144:147], v[188:191]\n\t"
      "v_mfma_f32_16x16x32_bf16 v[192:195], v[72:75], v[144:147], v[192:195]\n\t"
      "v_mfma_f32_16x16x32_bf16 v[196:199], v[76:79], v[144:147], v[196:199]\n\t"
      "v_mfma_f32_16x16x32_bf16 v[184:187], v[80:83], v[148:151], v[184:187]\n\t"
      "v_mfma_f32_16x16x32_bf16 v[188:191], v[84:87], v[148:151], v[188:191]\n\t"
      "v_mfma_f32_16x16x32_bf16 v[192:195], v[88:91], v[148:151], v[192:195]\n\t"
      "v_mfma_f32_16x16x32_bf16 v[196:199], v[92:95], v[148:151], v[196:199]\n\t"
      "v_mfma_f32_16x16x32_bf16 v[184:187], v[96:99], v[152:155], v[184:187]\n\t"
      "v_mfma_f32_16x16x32_bf16 v[188:191], v[100:103], v[152:155], v[188:191]\n\t"
      "v_mfma_f32_16x16x32_bf16 v[192:195], v[104:107], v[152:155], v[192:195]\n\t"
      "v_mfma_f32_16x16x32_bf16 v[196:199], v[108:111], v[152:155], v[196:199]\n\t"
      "v_mfma_f32_16x16x32_bf16 v[184:187], v[112:115], v[156:159], v[184:187]\n\t"
      "v_mfma_f32_16x16x32_bf16 v[188:191], v[116:119], v[156:159], v[188:191]\n\t"
      "v_mfma_f32_16x16x32_bf16 v[192:195], v[120:123], v[156:159], v[192:195]\n\t"
      "v_mfma_f32_16x16x32_bf16 v[196:199], v[124:127], v[156:159], v[196:199]\n\t"
      "global_load_dwordx4 v[128:131], v176, s[62:63] sc1\n\t"
      "global_load_dwordx4 v[132:135], v177, s[62:63] sc1\n\t"
      "global_load_dwordx4 v[136:139], v178, s[62:63] sc1\n\t"
      "global_load_dwordx4 v[140:143], v179, s[62:63] sc1\n\t"
      "global_load_dwordx4 v[144:147], v180, s[62:63] sc1\n\t"
      "global_load_dwordx4 v[148:151], v181, s[62:63] sc1\n\t"
      "global_load_dwordx4 v[152:155], v182, s[62:63] sc1\n\t"
      "global_load_dwordx4 v[156:159], v183, s[62:63] sc1\n\t"
      "s_waitcnt vmcnt(0)\n\t"
      "s_nop 7\n\t"
      "s_branch NQ4B_%=\n\t"
      "NQ5R_%=:\n\t"
      "s_mov_b32 s51, 0\n\t"
      "NQ5L_%=:\n\t"
      "s_add_u32 s51, s51, 1\n\t"
      "s_cmp_lt_u32 s51, 262144\n\t"
      "s_cbranch_scc0 NQ5X_%=\n\t"
      "s_waitcnt vmcnt(0)\n\t"
      "s_sleep 1\n\t"
      "global_load_dwordx4 v[128:131], v176, s[62:63] sc1\n\t"
      "global_load_dwordx4 v[132:135], v177, s[62:63] sc1\n\t"
      "global_load_dwordx4 v[136:139], v178, s[62:63] sc1\n\t"
      "global_load_dwordx4 v[140:143], v179, s[62:63] sc1\n\t"
      "global_load_dwordx4 v[144:147], v180, s[62:63] sc1\n\t"
      "global_load_dwordx4 v[148:151], v181, s[62:63] sc1\n\t"
      "global_load_dwordx4 v[152:155], v182, s[62:63] sc1\n\t"
      "global_load_dwordx4 v[156:159], v183, s[62:63] sc1\n\t"
      "s_waitcnt vmcnt(0)\n\t"
      "v_max3_u32 v234, v128, v129, v130\n\t"
      "v_max3_u32 v234, v234, v131, v132\n\t"
      "v_max3_u32 v234, v234, v133, v134\n\t"
      "v_max3_u32 v234, v234, v135, v136\n\t"
      "v_max3_u32 v234, v234, v137, v138\n\t"
      "v_max3_u32 v234, v234, v139, v140\n\t"
      "v_max3_u32 v234, v234, v141, v142\n\t"
      "v_max3_u32 v234, v234, v143, v144\n\t"
      "v_max3_u32 v234, v234, v145, v146\n\t"
      "v_max3_u32 v234, v234, v147, v148\n\t"
      "v_max3_u32 v234, v234, v149, v150\n\t"
      "v_max3_u32 v234, v234, v151, v152\n\t"
      "v_max3_u32 v234, v234, v153, v154\n\t"
      "v_max3_u32 v234, v234, v155, v156\n\t"
      "v_max3_u32 v234, v234, v157, v158\n\t"
      "v_max_u32_e32 v234, v234, v159\n\t"
      "v_cmp_eq_u32_e32 vcc, -1, v234\n\t"
      "s_cbranch_vccnz NQ5L_%=\n\t"
      "NQ5X_%=:\n\t"
      "v_mfma_f32_16x16x32_bf16 v[200:203], v[0:3], v[128:131], v[236:239]\n\t"
      "v_mfma_f32_16x16x32_bf16 v[204:207], v[4:7], v[128:131], 0\n\t"
      "v_mfma_f32_16x16x32_bf16 v[208:211], v[8:11], v[128:131], 0\n\t"
      "v_mfma_f32_16x16x32_bf16 v[212:215], v[12:15], v[128:131], 0\n\t"
      "v_mfma_f32_16x16x32_bf16 v[200:203], v[16:19], v[132:135], v[200:203]\n\t"
      "v_mfma_f32_16x16x32_bf16 v[204:207], v[20:23], v[132:135], v[204:207]\n\t"
      "v_mfma_f32_16x16x32_bf16 v[208:211], v[24:27], v[132:135], v[208:211]\n\t"
      "v_mfma_f32_16x16x32_bf16 v[212:215], v[28:31], v[132:135], v[212:215]\n\t"
      "v_mfma_f32_16x16x32_bf16 v[200:203], v[32:35], v[136:139], v[200:203]\n\t"
      "v_mfma_f32_16x16x32_bf16 v[204:207], v[36:39], v[136:139], v[204:207]\n\t"
      "v_mfma_f32_16x16x32_bf16 v[208:211], v[40:43], v[136:139], v[208:211]\n\t"
      "v_mfma_f32_16x16x32_bf16 v[212:215], v[44:47], v[136:139], v[212:215]\n\t"
      "v_mfma_f32_16x16x32_bf16 v[200:203], v[48:51], v[140:143], v[200:203]\n\t"
      "v_mfma_f32_16x16x32_bf16 v[204:207], v[52:55], v[140:143], v[204:207]\n\t"
      "v_mfma_f32_16x16x32_bf16 v[208:211], v[56:59], v[140:143], v[208:211]\n\t"
      "v_mfma_f32_16x16x32_bf16 v[212:215], v[60:63], v[140:143], v[212:215]\n\t"
      "v_mfma_f32_16x16x32_bf16 v[200:203], v[64:67], v[144:147], v[200:203]\n\t"
      "v_mfma_f32_16x16x32_bf16 v[204:207], v[68:71], v[144:147], v[204:207]\n\t"
      "v_mfma_f32_16x16x32_bf16 v[208:211], v[72:75], v[144:147], v[208:211]\n\t"
      "v_mfma_f32_16x16x32_bf16 v[212:215], v[76:79], v[144:147], v[212:215]\n\t"
      "v_mfma_f32_16x16x32_bf16 v[200:203], v[80:83], v[148:151], v[200:203]\n\t"
      "v_mfma_f32_16x16x32_bf16 v[204:207], v[84:87], v[148:151], v[204:207]\n\t"
      "v_mfma_f32_16x16x32_bf16 v[208:211], v[88:91], v[148:151], v[208:211]\n\t"
      "v_mfma_f32_16x16x32_bf16 v[212:215], v[92:95], v[148:151], v[212:215]\n\t"
      "v_mfma_f32_16x16x32_bf16 v[200:203], v[96:99], v[152:155], v[200:203]\n\t"
      "v_mfma_f32_16x16x32_bf16 v[204:207], v[100:103], v[152:155], v[204:207]\n\t"
      "v_mfma_f32_16x16x32_bf16 v[208:211], v[104:107], v[152:155], v[208:211]\n\t"
      "v_mfma_f32_16x16x32_bf16 v[212:215], v[108:111], v[152:155], v[212:215]\n\t"
      "v_mfma_f32_16x16x32_bf16 v[200:203], v[112:115], v[156:159], v[200:203]\n\t"
      "v_mfma_f32_16x16x32_bf16 v[204:207], v[116:119], v[156:159], v[204:207]\n\t"
      "v_mfma_f32_16x16x32_bf16 v[208:211], v[120:123], v[156:159], v[208:211]\n\t"
      "v_mfma_f32_16x16x32_bf16 v[212:215], v[124:127], v[156:159], v[212:215]\n\t"
      "global_load_dwordx4 v[128:131], v176, s[64:65] sc1\n\t"
      "global_load_dwordx4 v[132:135], v177, s[64:65] sc1\n\t"
      "global_load_dwordx4 v[136:139], v178, s[64:65] sc1\n\t"
      "global_load_dwordx4 v[140:143], v179, s[64:65] sc1\n\t"
      "global_load_dwordx4 v[144:147], v180, s[64:65] sc1\n\t"
      "global_load_dwordx4 v[148:151], v181, s[64:65] sc1\n\t"
      "global_load_dwordx4 v[152:155], v182, s[64:65] sc1\n\t"
      "global_load_dwordx4 v[156:159], v183, s[64:65] sc1\n\t"
      "s_waitcnt vmcnt(0)\n\t"
      "s_nop 7\n\t"
      "s_branch NQ5B_%=\n\t"
      "NQ6R_%=:\n\t"
      "s_mov_b32 s51, 0\n\t"
      "NQ6L_%=:\n\t"
      "s_add_u32 s51, s51, 1\n\t"
      "s_cmp_lt_u32 s51, 262144\n\t"
      "s_cbranch_scc0 NQ6X_%=\n\t"
      "s_waitcnt vmcnt(0)\n\t"
      "s_sleep 1\n\t"
      "global_load_dwordx4 v[128:131], v176, s[64:65] sc1\n\t"
      "global_load_dwordx4 v[132:135], v177, s[64:65] sc1\n\t"
      "global_load_dwordx4 v[136:139], v178, s[64:65] sc1\n\t"
      "global_load_dwordx4 v[140:143], v179, s[64:65] sc1\n\t"
      "global_load_dwordx4 v[144:147], v180, s[64:65] sc1\n\t"
      "global_load_dwordx4 v[148:151], v181, s[64:65] sc1\n\t"
      "global_load_dwordx4 v[152:155], v182, s[64:65] sc1\n\t"
      "global_load_dwordx4 v[156:159], v183, s[64:65] sc1\n\t"
      "s_waitcnt vmcnt(0)\n\t"
      "v_max3_u32 v234, v128, v129, v130\n\t"
      "v_max3_u32 v234, v234, v131, v132\n\t"
      "v_max3_u32 v234, v234, v133, v134\n\t"
      "v_max3_u32 v234, v234, v135, v136\n\t"
      "v_max3_u32 v234, v234, v137, v138\n\t"
      "v_max3_u32 v234, v234, v139, v140\n\t"
      "v_max3_u32 v234, v234, v141, v142\n\t"
      "v_max3_u32 v234, v234, v143, v144\n\t"
      "v_max3_u32 v234, v234, v145, v146\n\t"
      "v_max3_u32 v234, v234, v147, v148\n\t"
      "v_max3_u32 v234, v234, v149, v150\n\t"
      "v_max3_u32 v234, v234, v151, v152\n\t"
      "v_max3_u32 v234, v234, v153, v154\n\t"
      "v_max3_u32 v234, v234, v155, v156\n\t"
      "v_max3_u32 v234, v234, v157, v158\n\t"
      "v_max_u32_e32 v234, v234, v159\n\t"
      "v_cmp_eq_u32_e32 vcc, -1, v234\n\t"
      "s_cbranch_vccnz NQ6L_%=\n\t"
      "NQ6X_%=:\n\t"
      "v_mfma_f32_16x16x32_bf16 v[184:187], v[0:3], v[128:131], v[236:239]\n\t"
      "v_mfma_f32_16x16x32_bf16 v[188:191], v[4:7], v[128:131], 0\n\t"
      "v_mfma_f32_16x16x32_bf16 v[192:195], v[8:11], v[128:131], 0\n\t"
      "v_mfma_f32_16x16x32_bf16 v[196:199], v[12:15], v[128:131], 0\n\t"
      "v_mfma_f32_16x16x32_bf16 v[184:187], v[16:19], v[132:135], v[184:187]\n\t"
      "v_mfma_f32_16x16x32_bf16 v[188:191], v[20:23], v[132:135], v[188:191]\n\t"
      "v_mfma_f32_16x16x32_bf16 v[192:195], v[24:27], v[132:135], v[192:195]\n\t"
      "v_mfma_f32_16x16x32_bf16 v[196:199], v[28:31], v[132:135], v[196:199]\n\t"
      "v_mfma_f32_16x16x32_bf16 v[184:187], v[32:35], v[136:139], v[184:187]\n\t"
      "v_mfma_f32_16x16x32_bf16 v[188:191], v[36:39], v[136:139], v[188:191]\n\t"
      "v_mfma_f32_16x16x32_bf16 v[192:195], v[40:43], v[136:139], v[192:195]\n\t"
      "v_mfma_f32_16x16x32_bf16 v[196:199], v[44:47], v[136:139], v[196:199]\n\t"
      "v_mfma_f32_16x16x32_bf16 v[184:187], v[48:51], v[140:143], v[184:187]\n\t"
      "v_mfma_f32_16x16x32_bf16 v[188:191], v[52:55], v[140:143], v[188:191]\n\t"
      "v_mfma_f32_16x16x32_bf16 v[192:195], v[56:59], v[140:143], v[192:195]\n\t"
      "v_mfma_f32_16x16x32_bf16 v[196:199], v[60:63], v[140:143], v[196:199]\n\t"
      "v_mfma_f32_16x16x32_bf16 v[184:187], v[64:67], v[144:147], v[184:187]\n\t"
      "v_mfma_f32_16x16x32_bf16 v[188:191], v[68:71], v[144:147], v[188:191]\n\t"
      "v_mfma_f32_16x16x32_bf16 v[192:195], v[72:75], v[144:147], v[192:195]\n\t"
      "v_mfma_f32_16x16x32_bf16 v[196:199], v[76:79], v[144:147], v[196:199]\n\t"
      "v_mfma_f32_16x16x32_bf16 v[184:187], v[80:83], v[148:151], v[184:187]\n\t"
      "v_mfma_f32_16x16x32_bf16 v[188:191], v[84:87], v[148:151], v[188:191]\n\t"
      "v_mfma_f32_16x16x32_bf16 v[192:195], v[88:91], v[148:151], v[192:195]\n\t"
      "v_mfma_f32_16x16x32_bf16 v[196:199], v[92:95], v[148:151], v[196:199]\n\t"
      "v_mfma_f32_16x16x32_bf16 v[184:187], v[96:99], v[152:155], v[184:187]\n\t"
      "v_mfma_f32_16x16x32_bf16 v[188:191], v[100:103], v[152:155], v[188:191]\n\t"
      "v_mfma_f32_16x16x32_bf16 v[192:195], v[104:107], v[152:155], v[192:195]\n\t"
      "v_mfma_f32_16x16x32_bf16 v[196:199], v[108:111], v[152:155], v[196:199]\n\t"
      "v_mfma_f32_16x16x32_bf16 v[184:187], v[112:115], v[156:159], v[184:187]\n\t"
      "v_mfma_f32_16x16x32_bf16 v[188:191], v[116:119], v[156:159], v[188:191]\n\t"
      "v_mfma_f32_16x16x32_bf16 v[192:195], v[120:123], v[156:159], v[192:195]\n\t"
      "v_mfma_f32_16x16x32_bf16 v[196:199], v[124:127], v[156:159], v[196:199]\n\t"
      "s_waitcnt vmcnt(0)\n\t"
      "s_nop 7\n\t"
      "s_branch NQ6B_%=\n\t"
      "FP0R_%=:\n\t"
      "s_mov_b32 s51, 0\n\t"
      "FP0L_%=:\n\t"
      "s_add_u32 s51, s51, 1\n\t"
      "s_cmp_lt_u32 s51, 262144\n\t"
      "s_cbranch_scc0 FP0X_%=\n\t"
      "s_waitcnt vmcnt(0)\n\t"
      "s_sleep 1\n\t"
      "global_load_dwordx4 v[128:131], v176, s[52:53] sc1\n\t"
      "global_load_dwordx4 v[132:135], v177, s[52:53] sc1\n\t"
      "global_load_dwordx4 v[136:139], v178, s[52:53] sc1\n\t"
      "global_load_dwordx4 v[140:143], v179, s[52:53] sc1\n\t"
      "global_load_dwordx4 v[144:147], v180, s[52:53] sc1\n\t"
      "global_load_dwordx4 v[148:151], v181, s[52:53] sc1\n\t"
      "global_load_dwordx4 v[152:155], v182, s[52:53] sc1\n\t"
      "global_load_dwordx4 v[156:159], v183, s[52:53] sc1\n\t"
      "s_waitcnt vmcnt(7)\n\t"
      "v_max3_u32 v234, v128, v129, v130\n\t"
      "v_max_u32_e32 v234, v234, v131\n\t"
      "v_cmp_eq_u32_e32 vcc, -1, v234\n\t"
      "s_cbranch_vccnz FP0L_%=\n\t"
      "FP0X_%=:\n\t"
      "s_branch FP0B_%=\n\t"
      "FP1R_%=:\n\t"
      "s_mov_b32 s51, 0\n\t"
      "FP1L_%=:\n\t"
      "s_add_u32 s51, s51, 1\n\t"
      "s_cmp_lt_u32 s51, 262144\n\t"
      "s_cbranch_scc0 FP1X_%=\n\t"
      "s_waitcnt vmcnt(0)\n\t"
      "s_sleep 1\n\t"
      "global_load_dwordx4 v[132:135], v177, s[52:53] sc1\n\t"
      "global_load_dwordx4 v[136:139], v178, s[52:53] sc1\n\t"
      "global_load_dwordx4 v[140:143], v179, s[52:53] sc1\n\t"
      "global_load_dwordx4 v[144:147], v180, s[52:53] sc1\n\t"
      "global_load_dwordx4 v[148:151], v181, s[52:53] sc1\n\t"
      "global_load_dwordx4 v[152:155], v182, s[52:53] sc1\n\t"
      "global_load_dwordx4 v[156:159], v183, s[52:53] sc1\n\t"
      "s_waitcnt vmcnt(6)\n\t"
      "v_max3_u32 v234, v132, v133, v134\n\t"
      "v_max_u32_e32 v234, v234, v135\n\t"
      "v_cmp_eq_u32_e32 vcc, -1, v234\n\t"
      "s_cbranch_vccnz FP1L_%=\n\t"
      "FP1X_%=:\n\t"
      "s_branch FP1B_%=\n\t"
      "FP2R_%=:\n\t"
      "s_mov_b32 s51, 0\n\t"
      "FP2L_%=:\n\t"
      "s_add_u32 s51, s51, 1\n\t"
      "s_cmp_lt_u32 s51, 262144\n\t"
      "s_cbranch_scc0 FP2X_%=\n\t"
      "s_waitcnt vmcnt(0)\n\t"
      "s_sleep 1\n\t"
      "global_load_dwordx4 v[136:139], v178, s[52:53] sc1\n\t"
      "global_load_dwordx4 v[140:143], v179, s[52:53] sc1\n\t"
      "global_load_dwordx4 v[144:147], v180, s[52:53] sc1\n\t"
      "global_load_dwordx4 v[148:151], v181, s[52:53] sc1\n\t"
      "global_load_dwordx4 v[152:155], v182, s[52:53] sc1\n\t"
      "global_load_dwordx4 v[156:159], v183, s[52:53] sc1\n\t"
      "global_load_dwordx4 v[128:131], v176, s[54:55] sc1\n\t"
      "global_load_dwordx4 v[132:135], v177, s[54:55] sc1\n\t"
      "s_waitcnt vmcnt(7)\n\t"
      "v_max3_u32 v234, v136, v137, v138\n\t"
      "v_max_u32_e32 v234, v234, v139\n\t"
      "v_cmp_eq_u32_e32 vcc, -1, v234\n\t"
      "s_cbranch_vccnz FP2L_%=\n\t"
      "FP2X_%=:\n\t"
      "s_branch FP2B_%=\n\t"
      "FP3R_%=:\n\t"
      "s_mov_b32 s51, 0\n\t"
      "FP3L_%=:\n\t"
      "s_add_u32 s51, s51, 1\n\t"
      "s_cmp_lt_u32 s51, 262144\n\t"
      "s_cbranch_scc0 FP3X_%=\n\t"
      "s_waitcnt vmcnt(0)\n\t"
      "s_sleep 1\n\t"
      "global_load_dwordx4 v[140:143], v179, s[52:53] sc1\n\t"
      "global_load_dwordx4 v[144:147], v180, s[52:53] sc1\n\t"
      "global_load_dwordx4 v[148:151], v181, s[52:53] sc1\n\t"
      "global_load_dwordx4 v[152:155], v182, s[52:53] sc1\n\t"
      "global_load_dwordx4 v[156:159], v183, s[52:53] sc1\n\t"
      "global_load_dwordx4 v[128:131], v176, s[54:55] sc1\n\t"
      "global_load_dwordx4 v[132:135], v177, s[54:55] sc1\n\t"
      "s_waitcnt vmcnt(6)\n\t"
      "v_max3_u32 v234, v140, v141, v142\n\t"
      "v_max_u32_e32 v234, v234, v143\n\t"
      "v_cmp_eq_u32_e32 vcc, -1, v234\n\t"
      "s_cbranch_vccnz FP3L_%=\n\t"
      "FP3X_%=:\n\t"
      "s_branch FP3B_%=\n\t"
      "FP4R_%=:\n\t"
      "s_mov_b32 s51, 0\n\t"
      "FP4L_%=:\n\t"
      "s_add_u32 s51, s51, 1\n\t"
      "s_cmp_lt_u32 s51, 262144\n\t"
      "s_cbranch_scc0 FP4X_%=\n\t"
      "s_waitcnt vmcnt(0)\n\t"
      "s_sleep 1\n\t"
      "global_load_dwordx4 v[144:147], v180, s[52:53] sc1\n\t"
      "global_load_dwordx4 v[148:151], v181, s[52:53] sc1\n\t"
      "global_load_dwordx4 v[152:155], v182, s[52:53] sc1\n\t"
      "global_load_dwordx4 v[156:159], v183, s[52:53] sc1\n\t"
      "global_load_dwordx4 v[128:131], v176, s[54:55] sc1\n\t"
      "global_load_dwordx4 v[132:135], v177, s[54:55] sc1\n\t"
      "global_load_dwordx4 v[136:139], v178, s[54:55] sc1\n\t"
      "global_load_dwordx4 v[140:143], v179, s[54:55] sc1\n\t"
      "s_waitcnt vmcnt(7)\n\t"
      "v_max3_u32 v234, v144, v145, v146\n\t"
      "v_max_u32_e32 v234, v234, v147\n\t"
      "v_cmp_eq_u32_e32 vcc, -1, v234\n\t"
      "s_cbranch_vccnz FP4L_%=\n\t"
      "FP4X_%=:\n\t"
      "s_branch FP4B_%=\n\t"
      "FP5R_%=:\n\t"
      "s_mov_b32 s51, 0\n\t"
      "FP5L_%=:\n\t"
      "s_add_u32 s51, s51, 1\n\t"
      "s_cmp_lt_u32 s51, 262144\n\t"
      "s_cbranch_scc0 FP5X_%=\n\t"
      "s_waitcnt vmcnt(0)\n\t"
      "s_sleep 1\n\t"
      "global_load_dwordx4 v[148:151], v181, s[52:53] sc1\n\t"
      "global_load_dwordx4 v[152:155], v182, s[52:53] sc1\n\t"
      "global_load_dwordx4 v[156:159], v183, s[52:53] sc1\n\t"
      "global_load_dwordx4 v[128:131], v176, s[54:55] sc1\n\t"
      "global_load_dwordx4 v[132:135], v177, s[54:55] sc1\n\t"
      "global_load_dwordx4 v[136:139], v178, s[54:55] sc1\n\t"
      "global_load_dwordx4 v[140:143], v179, s[54:55] sc1\n\t"
      "s_waitcnt vmcnt(6)\n\t"
      "v_max3_u32 v234, v148, v149, v150\n\t"
      "v_max_u32_e32 v234, v234, v151\n\t"
      "v_cmp_eq_u32_e32 vcc, -1, v234\n\t"
      "s_cbranch_vccnz FP5L_%=\n\t"
      "FP5X_%=:\n\t"
      "s_branch FP5B_%=\n\t"
      "FP6R_%=:\n\t"
      "s_mov_b32 s51, 0\n\t"
      "FP6L_%=:\n\t"
      "s_add_u32 s51, s51, 1\n\t"
      "s_cmp_lt_u32 s51, 262144\n\t"
      "s_cbranch_scc0 FP6X_%=\n\t"
      "s_waitcnt vmcnt(0)\n\t"
      "s_sleep 1\n\t"
      "global_load_dwordx4 v[152:155], v182, s[52:53] sc1\n\t"
      "global_load_dwordx4 v[156:159], v183, s[52:53] sc1\n\t"
      "global_load_dwordx4 v[128:131], v176, s[54:55] sc1\n\t"
      "global_load_dwordx4 v[132:135], v177, s[54:55] sc1\n\t"
      "global_load_dwordx4 v[136:139], v178, s[54:55] sc1\n\t"
      "global_load_dwordx4 v[140:143], v179, s[54:55] sc1\n\t"
      "global_load_dwordx4 v[144:147], v180, s[54:55] sc1\n\t"
      "global_load_dwordx4 v[148:151], v181, s[54:55] sc1\n\t"
      "s_waitcnt vmcnt(7)\n\t"
      "v_max3_u32 v234, v152, v153, v154\n\t"
      "v_max_u32_e32 v234, v234, v155\n\t"
      "v_cmp_eq_u32_e32 vcc, -1, v234\n\t"
      "s_cbranch_vccnz FP6L_%=\n\t"
      "FP6X_%=:\n\t"
      "s_branch FP6B_%=\n\t"
      "FP7R_%=:\n\t"
      "s_mov_b32 s51, 0\n\t"
      "FP7L_%=:\n\t"
      "s_add_u32 s51, s51, 1\n\t"
      "s_cmp_lt_u32 s51, 262144\n\t"
      "s_cbranch_scc0 FP7X_%=\n\t"
      "s_waitcnt vmcnt(0)\n\t"
      "s_sleep 1\n\t"
      "global_load_dwordx4 v[156:159], v183, s[52:53] sc1\n\t"
      "global_load_dwordx4 v[128:131], v176, s[54:55] sc1\n\t"
      "global_load_dwordx4 v[132:135], v177, s[54:55] sc1\n\t"
      "global_load_dwordx4 v[136:139], v178, s[54:55] sc1\n\t"
      "global_load_dwordx4 v[140:143], v179, s[54:55] sc1\n\t"
      "global_load_dwordx4 v[144:147], v180, s[54:55] sc1\n\t"
      "global_load_dwordx4 v[148:151], v181, s[54:55] sc1\n\t"
      "s_waitcnt vmcnt(6)\n\t"
      "v_max3_u32 v234, v156, v157, v158\n\t"
      "v_max_u32_e32 v234, v234, v159\n\t"
      "v_cmp_eq_u32_e32 vcc, -1, v234\n\t"
      "s_cbranch_vccnz FP7L_%=\n\t"
      "FP7X_%=:\n\t"
      "s_branch FP7B_%=\n\t"
      "NP1R_%=:\n\t"
      "s_mov_b32 s51, 0\n\t"
      "NP1L_%=:\n\t"
      "s_add_u32 s51, s51, 1\n\t"
      "s_cmp_lt_u32 s51, 262144\n\t"
      "s_cbranch_scc0 NP1X_%=\n\t"
      "s_waitcnt vmcnt(0)\n\t"
      "s_sleep 1\n\t"
      "global_load_dwordx4 v[128:131], v176, s[54:55] sc1\n\t"
      "global_load_dwordx4 v[132:135], v177, s[54:55] sc1\n\t"
      "global_load_dwordx4 v[136:139], v178, s[54:55] sc1\n\t"
      "global_load_dwordx4 v[140:143], v179, s[54:55] sc1\n\t"
      "global_load_dwordx4 v[144:147], v180, s[54:55] sc1\n\t"
      "global_load_dwordx4 v[148:151], v181, s[54:55] sc1\n\t"
      "global_load_dwordx4 v[152:155], v182, s[54:55] sc1\n\t"
      "global_load_dwordx4 v[156:159], v183, s[54:55] sc1\n\t"
      "s_waitcnt vmcnt(0)\n\t"
      "v_max3_u32 v234, v128, v129, v130\n\t"
      "v_max3_u32 v234, v234, v131, v132\n\t"
      "v_max3_u32 v234, v234, v133, v134\n\t"
      "v_max3_u32 v234, v234, v135, v136\n\t"
      "v_max3_u32 v234, v234, v137, v138\n\t"
      "v_max3_u32 v234, v234, v139, v140\n\t"
      "v_max3_u32 v234, v234, v141, v142\n\t"
      "v_max3_u32 v234, v234, v143, v144\n\t"
      "v_max3_u32 v234, v234, v145, v146\n\t"
      "v_max3_u32 v234, v234, v147, v148\n\t"
      "v_max3_u32 v234, v234, v149, v150\n\t"
      "v_max3_u32 v234, v234, v151, v152\n\t"
      "v_max3_u32 v234, v234, v153, v154\n\t"
      "v_max3_u32 v234, v234, v155, v156\n\t"
      "v_max3_u32 v234, v234, v157, v158\n\t"
      "v_max_u32_e32 v234, v234, v159\n\t"
      "v_cmp_eq_u32_e32 vcc, -1, v234\n\t"
      "s_cbranch_vccnz NP1L_%=\n\t"
      "NP1X_%=:\n\t"
      "v_mfma_f32_16x16x32_bf16 v[200:203], v[0:3], v[128:131], 0\n\t"
      "v_mfma_f32_16x16x32_bf16 v[204:207], v[4:7], v[128:131], 0\n\t"
      "v_mfma_f32_16x16x32_bf16 v[208:211], v[8:11], v[128:131], 0\n\t"
      "v_mfma_f32_16x16x32_bf16 v[212:215], v[12:15], v[128:131], 0\n\t"
      "v_mfma_f32_16x16x32_bf16 v[200:203], v[16:19], v[132:135], v[200:203]\n\t"
      "v_mfma_f32_16x16x32_bf16 v[204:207], v[20:23], v[132:135], v[204:207]\n\t"
      "v_mfma_f32_16x16x32_bf16 v[208:211], v[24:27], v[132:135], v[208:211]\n\t"
      "v_mfma_f32_16x16x32_bf16 v[212:215], v[28:31], v[132:135], v[212:215]\n\t"
      "v_mfma_f32_16x16x32_bf16 v[200:203], v[32:35], v[136:139], v[200:203]\n\t"
      "v_mfma_f32_16x16x32_bf16 v[204:207], v[36:39], v[136:139], v[204:207]\n\t"
      "v_mfma_f32_16x16x32_bf16 v[208:211], v[40:43], v[136:139], v[208:211]\n\t"
      "v_mfma_f32_16x16x32_bf16 v[212:215], v[44:47], v[136:139], v[212:215]\n\t"
      "v_mfma_f32_16x16x32_bf16 v[200:203], v[48:51], v[140:143], v[200:203]\n\t"
      "v_mfma_f32_16x16x32_bf16 v[204:207], v[52:55], v[140:143], v[204:207]\n\t"
      "v_mfma_f32_16x16x32_bf16 v[208:211], v[56:59], v[140:143], v[208:211]\n\t"
      "v_mfma_f32_16x16x32_bf16 v[212:215], v[60:63], v[140:143], v[212:215]\n\t"
      "v_mfma_f32_16x16x32_bf16 v[200:203], v[64:67], v[144:147], v[200:203]\n\t"
      "v_mfma_f32_16x16x32_bf16 v[204:207], v[68:71], v[144:147], v[204:207]\n\t"
      "v_mfma_f32_16x16x32_bf16 v[208:211], v[72:75], v[144:147], v[208:211]\n\t"
      "v_mfma_f32_16x16x32_bf16 v[212:215], v[76:79], v[144:147], v[212:215]\n\t"
      "v_mfma_f32_16x16x32_bf16 v[200:203], v[80:83], v[148:151], v[200:203]\n\t"
      "v_mfma_f32_16x16x32_bf16 v[204:207], v[84:87], v[148:151], v[204:207]\n\t"
      "v_mfma_f32_16x16x32_bf16 v[208:211], v[88:91], v[148:151], v[208:211]\n\t"
      "v_mfma_f32_16x16x32_bf16 v[212:215], v[92:95], v[148:151], v[212:215]\n\t"
      "v_mfma_f32_16x16x32_bf16 v[200:203], v[96:99], v[152:155], v[200:203]\n\t"
      "v_mfma_f32_16x16x32_bf16 v[204:207], v[100:103], v[152:155], v[204:207]\n\t"
      "v_mfma_f32_16x16x32_bf16 v[208:211], v[104:107], v[152:155], v[208:211]\n\t"
      "v_mfma_f32_16x16x32_bf16 v[200:203], v[112:115], v[156:159], v[200:203]\n\t"
      "v_mfma_f32_16x16x32_bf16 v[204:207], v[116:119], v[156:159], v[204:207]\n\t"
      "v_mfma_f32_16x16x32_bf16 v[208:211], v[120:123], v[156:159], v[208:211]\n\t"
      "global_load_dwordx4 v[128:131], v176, s[56:57] sc1\n\t"
      "global_load_dwordx4 v[132:135], v177, s[56:57] sc1\n\t"
      "global_load_dwordx4 v[136:139], v178, s[56:57] sc1\n\t"
      "global_load_dwordx4 v[140:143], v179, s[56:57] sc1\n\t"
      "global_load_dwordx4 v[144:147], v180, s[56:57] sc1\n\t"
      "global_load_dwordx4 v[148:151], v181, s[56:57] sc1\n\t"
      "s_waitcnt vmcnt(0)\n\t"
      "s_nop 7\n\t"
      "s_branch NP1B_%=\n\t"
      "NP2R_%=:\n\t"
      "s_mov_b32 s51, 0\n\t"
      "NP2L_%=:\n\t"
      "s_add_u32 s51, s51, 1\n\t"
      "s_cmp_lt_u32 s51, 262144\n\t"
      "s_cbranch_scc0 NP2X_%=\n\t"
      "s_waitcnt vmcnt(0)\n\t"
      "s_sleep 1\n\t"
      "global_load_dwordx4 v[128:131], v176, s[56:57] sc1\n\t"
      "global_load_dwordx4 v[132:135], v177, s[56:57] sc1\n\t"
      "global_load_dwordx4 v[136:139], v178, s[56:57] sc1\n\t"
      "global_load_dwordx4 v[140:143], v179, s[56:57] sc1\n\t"
      "global_load_dwordx4 v[144:147], v180, s[56:57] sc1\n\t"
      "global_load_dwordx4 v[148:151], v181, s[56:57] sc1\n\t"
      "global_load_dwordx4 v[152:155], v182, s[56:57] sc1\n\t"
      "global_load_dwordx4 v[156:159], v183, s[56:57] sc1\n\t"
      "s_waitcnt vmcnt(0)\n\t"
      "v_max3_u32 v234, v128, v129, v130\n\t"
      "v_max3_u32 v234, v234, v131, v132\n\t"
      "v_max3_u32 v234, v234, v133, v134\n\t"
      "v_max3_u32 v234, v234, v135, v136\n\t"
      "v_max3_u32 v234, v234, v137, v138\n\t"
      "v_max3_u32 v234, v234, v139, v140\n\t"
      "v_max3_u32 v234, v234, v141, v142\n\t"
      "v_max3_u32 v234, v234, v143, v144\n\t"
      "v_max3_u32 v234, v234, v145, v146\n\t"
      "v_max3_u32 v234, v234, v147, v148\n\t"
      "v_max3_u32 v234, v234, v149, v150\n\t"
      "v_max3_u32 v234, v234, v151, v152\n\t"
      "v_max3_u32 v234, v234, v153, v154\n\t"
      "v_max3_u32 v234, v234, v155, v156\n\t"
      "v_max3_u32 v234, v234, v157, v158\n\t"
      "v_max_u32_e32 v234, v234, v159\n\t"
      "v_cmp_eq_u32_e32 vcc, -1, v234\n\t"
      "s_cbranch_vccnz NP2L_%=\n\t"
      "NP2X_%=:\n\t"
      "v_mfma_f32_16x16x32_bf16 v[184:187], v[0:3], v[128:131], 0\n\t"
      "v_mfma_f32_16x16x32_bf16 v[188:191], v[4:7], v[128:131], 0\n\t"
      "v_mfma_f32_16x16x32_bf16 v[192:195], v[8:11], v[128:131], 0\n\t"
      "v_mfma_f32_16x16x32_bf16 v[196:199], v[12:15], v[128:131], 0\n\t"
      "v_mfma_f32_16x16x32_bf16 v[184:187], v[16:19], v[132:135], v[184:187]\n\t"
      "v_mfma_f32_16x16x32_bf16 v[188:191], v[20:23], v[132:135], v[188:191]\n\t"
      "v_mfma_f32_16x16x32_bf16 v[192:195], v[24:27], v[132:135], v[192:195]\n\t"
      "v_mfma_f32_16x16x32_bf16 v[196:199], v[28:31], v[132:135], v[196:199]\n\t"
      "v_mfma_f32_16x16x32_bf16 v[184:187], v[32:35], v[136:139], v[184:187]\n\t"
      "v_mfma_f32_16x16x32_bf16 v[188:191], v[36:39], v[136:139], v[188:191]\n\t"
      "v_mfma_f32_16x16x32_bf16 v[192:195], v[40:43], v[136:139], v[192:195]\n\t"
      "v_mfma_f32_16x16x32_bf16 v[196:199], v[44:47], v[136:139], v[196:199]\n\t"
      "v_mfma_f32_16x16x32_bf16 v[184:187], v[48:51], v[140:143], v[184:187]\n\t"
      "v_mfma_f32_16x16x32_bf16 v[188:191], v[52:55], v[140:143], v[188:191]\n\t"
      "v_mfma_f32_16x16x32_bf16 v[192:195], v[56:59], v[140:143], v[192:195]\n\t"
      "v_mfma_f32_16x16x32_bf16 v[196:199], v[60:63], v[140:143], v[196:199]\n\t"
      "v_mfma_f32_16x16x32_bf16 v[184:187], v[64:67], v[144:147], v[184:187]\n\t"
      "v_mfma_f32_16x16x32_bf16 v[188:191], v[68:71], v[144:147], v[188:191]\n\t"
      "v_mfma_f32_16x16x32_bf16 v[192:195], v[72:75], v[144:147], v[192:195]\n\t"
      "v_mfma_f32_16x16x32_bf16 v[196:199], v[76:79], v[144:147], v[196:199]\n\t"
      "v_mfma_f32_16x16x32_bf16 v[184:187], v[80:83], v[148:151], v[184:187]\n\t"
      "v_mfma_f32_16x16x32_bf16 v[188:191], v[84:87], v[148:151], v[188:191]\n\t"
      "v_mfma_f32_16x16x32_bf16 v[192:195], v[88:91], v[148:151], v[192:195]\n\t"
      "v_mfma_f32_16x16x32_bf16 v[196:199], v[92:95], v[148:151], v[196:199]\n\t"
      "v_mfma_f32_16x16x32_bf16 v[184:187], v[96:99], v[152:155], v[184:187]\n\t"
      "v_mfma_f32_16x16x32_bf16 v[188:191], v[100:103], v[152:155], v[188:191]\n\t"
      "v_mfma_f32_16x16x32_bf16 v[192:195], v[104:107], v[152:155], v[192:195]\n\t"
      "v_mfma_f32_16x16x32_bf16 v[184:187], v[112:115], v[156:159], v[184:187]\n\t"
      "v_mfma_f32_16x16x32_bf16 v[188:191], v[116:119], v[156:159], v[188:191]\n\t"
      "v_mfma_f32_16x16x32_bf16 v[192:195], v[120:123], v[156:159], v[192:195]\n\t"
      "global_load_dwordx4 v[128:131], v176, s[58:59] sc1\n\t"
      "global_load_dwordx4 v[132:135], v177, s[58:59] sc1\n\t"
      "global_load_dwordx4 v[136:139], v178, s[58:59] sc1\n\t"
      "global_load_dwordx4 v[140:143], v179, s[58:59] sc1\n\t"
      "global_load_dwordx4 v[144:147], v180, s[58:59] sc1\n\t"
      "global_load_dwordx4 v[148:151], v181, s[58:59] sc1\n\t"
      "s_waitcnt vmcnt(0)\n\t"
      "s_nop 7\n\t"
      "s_branch NP2B_%=\n\t"
      "NP3R_%=:\n\t"
      "s_mov_b32 s51, 0\n\t"
      "NP3L_%=:\n\t"
      "s_add_u32 s51, s51, 1\n\t"
      "s_cmp_lt_u32 s51, 262144\n\t"
      "s_cbranch_scc0 NP3X_%=\n\t"
      "s_waitcnt vmcnt(0)\n\t"
      "s_sleep 1\n\t"
      "global_load_dwordx4 v[128:131], v176, s[58:59] sc1\n\t"
      "global_load_dwordx4 v[132:135], v177, s[58:59] sc1\n\t"
      "global_load_dwordx4 v[136:139], v178, s[58:59] sc1\n\t"
      "global_load_dwordx4 v[140:143], v179, s[58:59] sc1\n\t"
      "global_load_dwordx4 v[144:147], v180, s[58:59] sc1\n\t"
      "global_load_dwordx4 v[148:151], v181, s[58:59] sc1\n\t"
      "global_load_dwordx4 v[152:155], v182, s[58:59] sc1\n\t"
      "global_load_dwordx4 v[156:159], v183, s[58:59] sc1\n\t"
      "s_waitcnt vmcnt(0)\n\t"
      "v_max3_u32 v234, v128, v129, v130\n\t"
      "v_max3_u32 v234, v234, v131, v132\n\t"
      "v_max3_u32 v234, v234, v133, v134\n\t"
      "v_max3_u32 v234, v234, v135, v136\n\t"
      "v_max3_u32 v234, v234, v137, v138\n\t"
      "v_max3_u32 v234, v234, v139, v140\n\t"
      "v_max3_u32 v234, v234, v141, v142\n\t"
      "v_max3_u32 v234, v234, v143, v144\n\t"
      "v_max3_u32 v234, v234, v145, v146\n\t"
      "v_max3_u32 v234, v234, v147, v148\n\t"
      "v_max3_u32 v234, v234, v149, v150\n\t"
      "v_max3_u32 v234, v234, v151, v152\n\t"
      "v_max3_u32 v234, v234, v153, v154\n\t"
      "v_max3_u32 v234, v234, v155, v156\n\t"
      "v_max3_u32 v234, v234, v157, v158\n\t"
      "v_max_u32_e32 v234, v234, v159\n\t"
      "v_cmp_eq_u32_e32 vcc, -1, v234\n\t"
      "s_cbranch_vccnz NP3L_%=\n\t"
      "NP3X_%=:\n\t"
      "v_mfma_f32_16x16x32_bf16 v[200:203], v[0:3], v[128:131], 0\n\t"
      "v_mfma_f32_16x16x32_bf16 v[204:207], v[4:7], v[128:131], 0\n\t"
      "v_mfma_f32_16x16x32_bf16 v[208:211], v[8:11], v[128:131], 0\n\t"
      "v_mfma_f32_16x16x32_bf16 v[212:215], v[12:15], v[128:131], 0\n\t"
      "v_mfma_f32_16x16x32_bf16 v[200:203], v[16:19], v[132:135], v[200:203]\n\t"
      "v_mfma_f32_16x16x32_bf16 v[204:207], v[20:23], v[132:135], v[204:207]\n\t"
      "v_mfma_f32_16x16x32_bf16 v[208:211], v[24:27], v[132:135], v[208:211]\n\t"
      "v_mfma_f32_16x16x32_bf16 v[212:215], v[28:31], v[132:135], v[212:215]\n\t"
      "v_mfma_f32_16x16x32_bf16 v[200:203], v[32:35], v[136:139], v[200:203]\n\t"
      "v_mfma_f32_16x16x32_bf16 v[204:207], v[36:39], v[136:139], v[204:207]\n\t"
      "v_mfma_f32_16x16x32_bf16 v[208:211], v[40:43], v[136:139], v[208:211]\n\t"
      "v_mfma_f32_16x16x32_bf16 v[212:215], v[44:47], v[136:139], v[212:215]\n\t"
      "v_mfma_f32_16x16x32_bf16 v[200:203], v[48:51], v[140:143], v[200:203]\n\t"
      "v_mfma_f32_16x16x32_bf16 v[204:207], v[52:55], v[140:143], v[204:207]\n\t"
      "v_mfma_f32_16x16x32_bf16 v[208:211], v[56:59], v[140:143], v[208:211]\n\t"
      "v_mfma_f32_16x16x32_bf16 v[212:215], v[60:63], v[140:143], v[212:215]\n\t"
      "v_mfma_f32_16x16x32_bf16 v[200:203], v[64:67], v[144:147], v[200:203]\n\t"
      "v_mfma_f32_16x16x32_bf16 v[204:207], v[68:71], v[144:147], v[204:207]\n\t"
      "v_mfma_f32_16x16x32_bf16 v[208:211], v[72:75], v[144:147], v[208:211]\n\t"
      "v_mfma_f32_16x16x32_bf16 v[212:215], v[76:79], v[144:147], v[212:215]\n\t"
      "v_mfma_f32_16x16x32_bf16 v[200:203], v[80:83], v[148:151], v[200:203]\n\t"
      "v_mfma_f32_16x16x32_bf16 v[204:207], v[84:87], v[148:151], v[204:207]\n\t"
      "v_mfma_f32_16x16x32_bf16 v[208:211], v[88:91], v[148:151], v[208:211]\n\t"
      "v_mfma_f32_16x16x32_bf16 v[212:215], v[92:95], v[148:151], v[212:215]\n\t"
      "v_mfma_f32_16x16x32_bf16 v[200:203], v[96:99], v[152:155], v[200:203]\n\t"
      "v_mfma_f32_16x16x32_bf16 v[204:207], v[100:103], v[152:155], v[204:207]\n\t"
      "v_mfma_f32_16x16x32_bf16 v[208:211], v[104:107], v[152:155], v[208:211]\n\t"
      "v_mfma_f32_16x16x32_bf16 v[200:203], v[112:115], v[156:159], v[200:203]\n\t"
      "v_mfma_f32_16x16x32_bf16 v[204:207], v[116:119], v[156:159], v[204:207]\n\t"
      "v_mfma_f32_16x16x32_bf16 v[208:211], v[120:123], v[156:159], v[208:211]\n\t"
      "global_load_dwordx4 v[128:131], v176, s[60:61] sc1\n\t"
      "global_load_dwordx4 v[132:135], v177, s[60:61] sc1\n\t"
      "global_load_dwordx4 v[136:139], v178, s[60:61] sc1\n\t"
      "global_load_dwordx4 v[140:143], v179, s[60:61] sc1\n\t"
      "global_load_dwordx4 v[144:147], v180, s[60:61] sc1\n\t"
      "global_load_dwordx4 v[148:151], v181, s[60:61] sc1\n\t"
      "s_waitcnt vmcnt(0)\n\t"
      "s_nop 7\n\t"
      "s_branch NP3B_%=\n\t"
      "NP4R_%=:\n\t"
      "s_mov_b32 s51, 0\n\t"
      "NP4L_%=:\n\t"
      "s_add_u32 s51, s51, 1\n\t"
      "s_cmp_lt_u32 s51, 262144\n\t"
      "s_cbranch_scc0 NP4X_%=\n\t"
      "s_waitcnt vmcnt(0)\n\t"
      "s_sleep 1\n\t"
      "global_load_dwordx4 v[128:131], v176, s[60:61] sc1\n\t"
      "global_load_dwordx4 v[132:135], v177, s[60:61] sc1\n\t"
      "global_load_dwordx4 v[136:139], v178, s[60:61] sc1\n\t"
      "global_load_dwordx4 v[140:143], v179, s[60:61] sc1\n\t"
      "global_load_dwordx4 v[144:147], v180, s[60:61] sc1\n\t"
      "global_load_dwordx4 v[148:151], v181, s[60:61] sc1\n\t"
      "global_load_dwordx4 v[152:155], v182, s[60:61] sc1\n\t"
      "global_load_dwordx4 v[156:159], v183, s[60:61] sc1\n\t"
      "s_waitcnt vmcnt(0)\n\t"
      "v_max3_u32 v234, v128, v129, v130\n\t"
      "v_max3_u32 v234, v234, v131, v132\n\t"
      "v_max3_u32 v234, v234, v133, v134\n\t"
      "v_max3_u32 v234, v234, v135, v136\n\t"
      "v_max3_u32 v234, v234, v137, v138\n\t"
      "v_max3_u32 v234, v234, v139, v140\n\t"
      "v_max3_u32 v234, v234, v141, v142\n\t"
      "v_max3_u32 v234, v234, v143, v144\n\t"
      "v_max3_u32 v234, v234, v145, v146\n\t"
      "v_max3_u32 v234, v234, v147, v148\n\t"
      "v_max3_u32 v234, v234, v149, v150\n\t"
      "v_max3_u32 v234, v234, v151, v152\n\t"
      "v_max3_u32 v234, v234, v153, v154\n\t"
      "v_max3_u32 v234, v234, v155, v156\n\t"
      "v_max3_u32 v234, v234, v157, v158\n\t"
      "v_max_u32_e32 v234, v234, v159\n\t"
      "v_cmp_eq_u32_e32 vcc, -1, v234\n\t"
      "s_cbranch_vccnz NP4L_%=\n\t"
      "NP4X_%=:\n\t"
      "v_mfma_f32_16x16x32_bf16 v[184:187], v[0:3], v[128:131], 0\n\t"
      "v_mfma_f32_16x16x32_bf16 v[188:191], v[4:7], v[128:131], 0\n\t"
      "v_mfma_f32_16x16x32_bf16 v[192:195], v[8:11], v[128:131], 0\n\t"
      "v_mfma_f32_16x16x32_bf16 v[196:199], v[12:15], v[128:131], 0\n\t"
      "v_mfma_f32_16x16x32_bf16 v[184:187], v[16:19], v[132:135], v[184:187]\n\t"
      "v_mfma_f32_16x16x32_bf16 v[188:191], v[20:23], v[132:135], v[188:191]\n\t"
      "v_mfma_f32_16x16x32_bf16 v[192:195], v[24:27], v[132:135], v[192:195]\n\t"
      "v_mfma_f32_16x16x32_bf16 v[196:199], v[28:31], v[132:135], v[196:199]\n\t"
      "v_mfma_f32_16x16x32_bf16 v[184:187], v[32:35], v[136:139], v[184:187]\n\t"
      "v_mfma_f32_16x16x32_bf16 v[188:191], v[36:39], v[136:139], v[188:191]\n\t"
      "v_mfma_f32_16x16x32_bf16 v[192:195], v[40:43], v[136:139], v[192:195]\n\t"
      "v_mfma_f32_16x16x32_bf16 v[196:199], v[44:47], v[136:139], v[196:199]\n\t"
      "v_mfma_f32_16x16x32_bf16 v[184:187], v[48:51], v[140:143], v[184:187]\n\t"
      "v_mfma_f32_16x16x32_bf16 v[188:191], v[52:55], v[140:143], v[188:191]\n\t"
      "v_mfma_f32_16x16x32_bf16 v[192:195], v[56:59], v[140:143], v[192:195]\n\t"
      "v_mfma_f32_16x16x32_bf16 v[196:199], v[60:63], v[140:143], v[196:199]\n\t"
      "v_mfma_f32_16x16x32_bf16 v[184:187], v[64:67], v[144:147], v[184:187]\n\t"
      "v_mfma_f32_16x16x32_bf16 v[188:191], v[68:71], v[144:147], v[188:191]\n\t"
      "v_mfma_f32_16x16x32_bf16 v[192:195], v[72:75], v[144:147], v[192:195]\n\t"
      "v_mfma_f32_16x16x32_bf16 v[196:199], v[76:79], v[144:147], v[196:199]\n\t"
      "v_mfma_f32_16x16x32_bf16 v[184:187], v[80:83], v[148:151], v[184:187]\n\t"
      "v_mfma_f32_16x16x32_bf16 v[188:191], v[84:87], v[148:151], v[188:191]\n\t"
      "v_mfma_f32_16x16x32_bf16 v[192:195], v[88:91], v[148:151], v[192:195]\n\t"
      "v_mfma_f32_16x16x32_bf16 v[196:199], v[92:95], v[148:151], v[196:199]\n\t"
      "v_mfma_f32_16x16x32_bf16 v[184:187], v[96:99], v[152:155], v[184:187]\n\t"
      "v_mfma_f32_16x16x32_bf16 v[188:191], v[100:103], v[152:155], v[188:191]\n\t"
      "v_mfma_f32_16x16x32_bf16 v[192:195], v[104:107], v[152:155], v[192:195]\n\t"
      "v_mfma_f32_16x16x32_bf16 v[184:187], v[112:115], v[156:159], v[184:187]\n\t"
      "v_mfma_f32_16x16x32_bf16 v[188:191], v[116:119], v[156:159], v[188:191]\n\t"
      "v_mfma_f32_16x16x32_bf16 v[192:195], v[120:123], v[156:159], v[192:195]\n\t"
      "global_load_dwordx4 v[128:131], v176, s[62:63] sc1\n\t"
      "global_load_dwordx4 v[132:135], v177, s[62:63] sc1\n\t"
      "global_load_dwordx4 v[136:139], v178, s[62:63] sc1\n\t"
      "global_load_dwordx4 v[140:143], v179, s[62:63] sc1\n\t"
      "global_load_dwordx4 v[144:147], v180, s[62:63] sc1\n\t"
      "global_load_dwordx4 v[148:151], v181, s[62:63] sc1\n\t"
      "s_waitcnt vmcnt(0)\n\t"
      "s_nop 7\n\t"
      "s_branch NP4B_%=\n\t"
      "NP5R_%=:\n\t"
      "s_mov_b32 s51, 0\n\t"
      "NP5L_%=:\n\t"
      "s_add_u32 s51, s51, 1\n\t"
      "s_cmp_lt_u32 s51, 262144\n\t"
      "s_cbranch_scc0 NP5X_%=\n\t"
      "s_waitcnt vmcnt(0)\n\t"
      "s_sleep 1\n\t"
      "global_load_dwordx4 v[128:131], v176, s[62:63] sc1\n\t"
      "global_load_dwordx4 v[132:135], v177, s[62:63] sc1\n\t"
      "global_load_dwordx4 v[136:139], v178, s[62:63] sc1\n\t"
      "global_load_dwordx4 v[140:143], v179, s[62:63] sc1\n\t"
      "global_load_dwordx4 v[144:147], v180, s[62:63] sc1\n\t"
      "global_load_dwordx4 v[148:151], v181, s[62:63] sc1\n\t"
      "global_load_dwordx4 v[152:155], v182, s[62:63] sc1\n\t"
      "global_load_dwordx4 v[156:159], v183, s[62:63] sc1\n\t"
      "s_waitcnt vmcnt(0)\n\t"
      "v_max3_u32 v234, v128, v129, v130\n\t"
      "v_max3_u32 v234, v234, v131, v132\n\t"
      "v_max3_u32 v234, v234, v133, v134\n\t"
      "v_max3_u32 v234, v234, v135, v136\n\t"
      "v_max3_u32 v234, v234, v137, v138\n\t"
      "v_max3_u32 v234, v234, v139, v140\n\t"
      "v_max3_u32 v234, v234, v141, v142\n\t"
      "v_max3_u32 v234, v234, v143, v144\n\t"
      "v_max3_u32 v234, v234, v145, v146\n\t"
      "v_max3_u32 v234, v234, v147, v148\n\t"
      "v_max3_u32 v234, v234, v149, v150\n\t"
      "v_max3_u32 v234, v234, v151, v152\n\t"
      "v_max3_u32 v234, v234, v153, v154\n\t"
      "v_max3_u32 v234, v234, v155, v156\n\t"
      "v_max3_u32 v234, v234, v157, v158\n\t"
      "v_max_u32_e32 v234, v234, v159\n\t"
      "v_cmp_eq_u32_e32 vcc, -1, v234\n\t"
      "s_cbranch_vccnz NP5L_%=\n\t"
      "NP5X_%=:\n\t"
      "v_mfma_f32_16x16x32_bf16 v[200:203], v[0:3], v[128:131], 0\n\t"
      "v_mfma_f32_16x16x32_bf16 v[204:207], v[4:7], v[128:131], 0\n\t"
      "v_mfma_f32_16x16x32_bf16 v[208:211], v[8:11], v[128:131], 0\n\t"
      "v_mfma_f32_16x16x32_bf16 v[212:215], v[12:15], v[128:131], 0\n\t"
      "v_mfma_f32_16x16x32_bf16 v[200:203], v[16:19], v[132:135], v[200:203]\n\t"
      "v_mfma_f32_16x16x32_bf16 v[204:207], v[20:23], v[132:135], v[204:207]\n\t"
      "v_mfma_f32_16x16x32_bf16 v[208:211], v[24:27], v[132:135], v[208:211]\n\t"
      "v_mfma_f32_16x16x32_bf16 v[212:215], v[28:31], v[132:135], v[212:215]\n\t"
      "v_mfma_f32_16x16x32_bf16 v[200:203], v[32:35], v[136:139], v[200:203]\n\t"
      "v_mfma_f32_16x16x32_bf16 v[204:207], v[36:39], v[136:139], v[204:207]\n\t"
      "v_mfma_f32_16x16x32_bf16 v[208:211], v[40:43], v[136:139], v[208:211]\n\t"
      "v_mfma_f32_16x16x32_bf16 v[212:215], v[44:47], v[136:139], v[212:215]\n\t"
      "v_mfma_f32_16x16x32_bf16 v[200:203], v[48:51], v[140:143], v[200:203]\n\t"
      "v_mfma_f32_16x16x32_bf16 v[204:207], v[52:55], v[140:143], v[204:207]\n\t"
      "v_mfma_f32_16x16x32_bf16 v[208:211], v[56:59], v[140:143], v[208:211]\n\t"
      "v_mfma_f32_16x16x32_bf16 v[212:215], v[60:63], v[140:143], v[212:215]\n\t"
      "v_mfma_f32_16x16x32_bf16 v[200:203], v[64:67], v[144:147], v[200:203]\n\t"
      "v_mfma_f32_16x16x32_bf16 v[204:207], v[68:71], v[144:147], v[204:207]\n\t"
      "v_mfma_f32_16x16x32_bf16 v[208:211], v[72:75], v[144:147], v[208:211]\n\t"
      "v_mfma_f32_16x16x32_bf16 v[212:215], v[76:79], v[144:147], v[212:215]\n\t"
      "v_mfma_f32_16x16x32_bf16 v[200:203], v[80:83], v[148:151], v[200:203]\n\t"
      "v_mfma_f32_16x16x32_bf16 v[204:207], v[84:87], v[148:151], v[204:207]\n\t"
      "v_mfma_f32_16x16x32_bf16 v[208:211], v[88:91], v[148:151], v[208:211]\n\t"
      "v_mfma_f32_16x16x32_bf16 v[212:215], v[92:95], v[148:151], v[212:215]\n\t"
      "v_mfma_f32_16x16x32_bf16 v[200:203], v[96:99], v[152:155], v[200:203]\n\t"
      "v_mfma_f32_16x16x32_bf16 v[204:207], v[100:103], v[152:155], v[204:207]\n\t"
      "v_mfma_f32_16x16x32_bf16 v[208:211], v[104:107], v[152:155], v[208:211]\n\t"
      "v_mfma_f32_16x16x32_bf16 v[200:203], v[112:115], v[156:159], v[200:203]\n\t"
      "v_mfma_f32_16x16x32_bf16 v[204:207], v[116:119], v[156:159], v[204:207]\n\t"
      "v_mfma_f32_16x16x32_bf16 v[208:211], v[120:123], v[156:159], v[208:211]\n\t"
      "global_load_dwordx4 v[128:131], v176, s[64:65] sc1\n\t"
      "global_load_dwordx4 v[132:135], v177, s[64:65] sc1\n\t"
      "global_load_dwordx4 v[136:139], v178, s[64:65] sc1\n\t"
      "global_load_dwordx4 v[140:143], v179, s[64:65] sc1\n\t"
      "global_load_dwordx4 v[144:147], v180, s[64:65] sc1\n\t"
      "global_load_dwordx4 v[148:151], v181, s[64:65] sc1\n\t"
      "s_waitcnt vmcnt(0)\n\t"
      "s_nop 7\n\t"
      "s_branch NP5B_%=\n\t"
      "NP6R_%=:\n\t"
      "s_mov_b32 s51, 0\n\t"
      "NP6L_%=:\n\t"
      "s_add_u32 s51, s51, 1\n\t"
      "s_cmp_lt_u32 s51, 262144\n\t"
      "s_cbranch_scc0 NP6X_%=\n\t"
      "s_waitcnt vmcnt(0)\n\t"
      "s_sleep 1\n\t"
      "global_load_dwordx4 v[128:131], v176, s[64:65] sc1\n\t"
      "global_load_dwordx4 v[132:135], v177, s[64:65] sc1\n\t"
      "global_load_dwordx4 v[136:139], v178, s[64:65] sc1\n\t"
      "global_load_dwordx4 v[140:143], v179, s[64:65] sc1\n\t"
      "global_load_dwordx4 v[144:147], v180, s[64:65] sc1\n\t"
      "global_load_dwordx4 v[148:151], v181, s[64:65] sc1\n\t"
      "global_load_dwordx4 v[152:155], v182, s[64:65] sc1\n\t"
      "global_load_dwordx4 v[156:159], v183, s[64:65] sc1\n\t"
      "s_waitcnt vmcnt(0)\n\t"
      "v_max3_u32 v234, v128, v129, v130\n\t"
      "v_max3_u32 v234, v234, v131, v132\n\t"
      "v_max3_u32 v234, v234, v133, v134\n\t"
      "v_max3_u32 v234, v234, v135, v136\n\t"
      "v_max3_u32 v234, v234, v137, v138\n\t"
      "v_max3_u32 v234, v234, v139, v140\n\t"
      "v_max3_u32 v234, v234, v141, v142\n\t"
      "v_max3_u32 v234, v234, v143, v144\n\t"
      "v_max3_u32 v234, v234, v145, v146\n\t"
      "v_max3_u32 v234, v234, v147, v148\n\t"
      "v_max3_u32 v234, v234, v149, v150\n\t"
      "v_max3_u32 v234, v234, v151, v152\n\t"
      "v_max3_u32 v234, v234, v153, v154\n\t"
      "v_max3_u32 v234, v234, v155, v156\n\t"
      "v_max3_u32 v234, v234, v157, v158\n\t"
      "v_max_u32_e32 v234, v234, v159\n\t"
      "v_cmp_eq_u32_e32 vcc, -1, v234\n\t"
      "s_cbranch_vccnz NP6L_%=\n\t"
      "NP6X_%=:\n\t"
      "v_mfma_f32_16x16x32_bf16 v[184:187], v[0:3], v[128:131], 0\n\t"
      "v_mfma_f32_16x16x32_bf16 v[188:191], v[4:7], v[128:131], 0\n\t"
      "v_mfma_f32_16x16x32_bf16 v[192:195], v[8:11], v[128:131], 0\n\t"
      "v_mfma_f32_16x16x32_bf16 v[196:199], v[12:15], v[128:131], 0\n\t"
      "v_mfma_f32_16x16x32_bf16 v[184:187], v[16:19], v[132:135], v[184:187]\n\t"
      "v_mfma_f32_16x16x32_bf16 v[188:191], v[20:23], v[132:135], v[188:191]\n\t"
      "v_mfma_f32_16x16x32_bf16 v[192:195], v[24:27], v[132:135], v[192:195]\n\t"
      "v_mfma_f32_16x16x32_bf16 v[196:199], v[28:31], v[132:135], v[196:199]\n\t"
      "v_mfma_f32_16x16x32_bf16 v[184:187], v[32:35], v[136:139], v[184:187]\n\t"
      "v_mfma_f32_16x16x32_bf16 v[188:191], v[36:39], v[136:139], v[188:191]\n\t"
      "v_mfma_f32_16x16x32_bf16 v[192:195], v[40:43], v[136:139], v[192:195]\n\t"
      "v_mfma_f32_16x16x32_bf16 v[196:199], v[44:47], v[136:139], v[196:199]\n\t"
      "v_mfma_f32_16x16x32_bf16 v[184:187], v[48:51], v[140:143], v[184:187]\n\t"
      "v_mfma_f32_16x16x32_bf16 v[188:191], v[52:55], v[140:143], v[188:191]\n\t"
      "v_mfma_f32_16x16x32_bf16 v[192:195], v[56:59], v[140:143], v[192:195]\n\t"
      "v_mfma_f32_16x16x32_bf16 v[196:199], v[60:63], v[140:143], v[196:199]\n\t"
      "v_mfma_f32_16x16x32_bf16 v[184:187], v[64:67], v[144:147], v[184:187]\n\t"
      "v_mfma_f32_16x16x32_bf16 v[188:191], v[68:71], v[144:147], v[188:191]\n\t"
      "v_mfma_f32_16x16x32_bf16 v[192:195], v[72:75], v[144:147], v[192:195]\n\t"
      "v_mfma_f32_16x16x32_bf16 v[196:199], v[76:79], v[144:147], v[196:199]\n\t"
      "v_mfma_f32_16x16x32_bf16 v[184:187], v[80:83], v[148:151], v[184:187]\n\t"
      "v_mfma_f32_16x16x32_bf16 v[188:191], v[84:87], v[148:151], v[188:191]\n\t"
      "v_mfma_f32_16x16x32_bf16 v[192:195], v[88:91], v[148:151], v[192:195]\n\t"
      "v_mfma_f32_16x16x32_bf16 v[196:199], v[92:95], v[148:151], v[196:199]\n\t"
      "v_mfma_f32_16x16x32_bf16 v[184:187], v[96:99], v[152:155], v[184:187]\n\t"
      "v_mfma_f32_16x16x32_bf16 v[188:191], v[100:103], v[152:155], v[188:191]\n\t"
      "v_mfma_f32_16x16x32_bf16 v[192:195], v[104:107], v[152:155], v[192:195]\n\t"
      "v_mfma_f32_16x16x32_bf16 v[184:187], v[112:115], v[156:159], v[184:187]\n\t"
      "v_mfma_f32_16x16x32_bf16 v[188:191], v[116:119], v[156:159], v[188:191]\n\t"
      "v_mfma_f32_16x16x32_bf16 v[192:195], v[120:123], v[156:159], v[192:195]\n\t"
      "s_waitcnt vmcnt(0)\n\t"
      "s_nop 7\n\t"
      "s_branch NP6B_%=\n\t"
      : "+{s[4:19]}"(s), "+{s[20:21]}"(kr), "+{s22}"(hint), "+{v[240:247]}"(v)
      :
      : "memory", "vcc", "scc",
        "v0", "v1", "v2", "v3", "v4", "v5", "v6", "v7", "v8", "v9", "v10", "v11", "v12", "v13", "v14", "v15", "v16",
        "v17", "v18", "v19", "v20", "v21", "v22", "v23", "v24", "v25", "v26", "v27", "v28", "v29", "v30", "v31", "v32",
        "v33", "v34", "v35", "v36", "v37", "v38", "v39", "v40", "v41", "v42", "v43", "v44", "v45", "v46", "v47", "v48",
        "v49", "v50", "v51", "v52", "v53", "v54", "v55", "v56", "v57", "v58", "v59", "v60", "v61", "v62", "v63", "v64",
        "v65", "v66", "v67", "v68", "v69", "v70", "v71", "v72", "v73", "v74", "v75", "v76", "v77", "v78", "v79", "v80",
        "v81", "v82", "v83", "v84", "v85", "v86", "v87", "v88", "v89", "v90", "v91", "v92", "v93", "v94", "v95", "v96",
        "v97", "v98", "v99", "v100", "v101", "v102", "v103", "v104", "v105", "v106", "v107", "v108", "v109", "v110",
        "v111", "v112", "v113", "v114", "v115", "v116", "v117", "v118", "v119", "v120", "v121", "v122", "v123", "v124",
        "v125", "v126", "v127", "v128", "v129", "v130", "v131", "v132", "v133", "v134", "v135", "v136", "v137", "v138",
        "v139", "v140", "v141", "v142", "v143", "v144", "v145", "v146", "v147", "v148", "v149", "v150", "v151", "v152",
        "v153", "v154", "v155", "v156", "v157", "v158", "v159", "v160", "v161", "v162", "v163", "v164", "v165", "v166",
        "v167", "v168", "v169", "v170", "v171", "v172", "v173", "v174", "v175", "v176", "v177", "v178", "v179", "v180",
        "v181", "v182", "v183", "v184", "v185", "v186", "v187", "v188", "v189", "v190", "v191", "v192", "v193", "v194",
        "v195", "v196", "v197", "v198", "v199", "v200", "v201", "v202", "v203", "v204", "v205", "v206", "v207", "v208",
        "v209", "v210", "v211", "v212", "v213", "v214", "v215", "v216", "v217", "v218", "v219", "v220", "v221", "v222",
        "v223", "v224", "v225", "v226", "v227", "v228", "v229", "v230", "v231", "v232", "v233", "v234", "v235", "v236",
        "v237", "v238", "v239", "v248", "v249", "v250", "v251", "v252", "v253", "s44", "s45", "s46", "s47", "s48",
        "s49", "s50", "s51", "s52", "s53", "s54", "s55", "s56", "s57", "s58", "s59", "s60", "s61", "s62", "s63", "s64",
        "s65", "s66", "s67", "s68", "s69", "s70"
    );
    __builtin_amdgcn_endpgm();
  } else {
#define B_INDICES                                                                                            \
  const int ib = idx - 16, t = 16 * (4 * side + ((xp + XO) & 3)) + ib;                         \
  const int st = 8 * xp + (ib >> 1), rot = 4 * (ib & 1) + 2 * side - (ib & side & 1);                        \
  const int cb0 = 4 * wave - max(wave - 4, 0);               \
  const int k1 = (ib + 2 * xcd) & 15, k0 = k1 - (k1 >= 14 ? 7 : 0)
    u32x16 acc;
    u32x8 v1, s1;
    u32x4 s3;
    unsigned bits = (wave >> 2) | (wave == 0 ? 16 : 0), id = blockIdx.x, wv = wave;
    {
      LANE_BITS(threadIdx.x & 63);
      B_INDICES;
      v1[0] = (8 * (wave & 3) + lr) * 896 + (wave >> 2) * 128 + ((lc ^ swz_tr(8 * wave + lr)) << 4) + 4096;
      v1[1] = B_X + (4 * g + q4) * 896 + cb0 * 32 + p4 * 8;
      v1[2] = B_PS + (4 * g + q4) * 64 + wave * 8;
      v1[3] = B_PS + (4 * g + p4) * 64 + q4 * 16;
      v1[4] = (t * 16 + 4 * g + p4) * 16;
      v1[5] = lr * 4096 + ((lc ^ swz_tr(lr)) << 4);
      v1[6] = lane * 16;
      v1[7] = WS_HN + (7 * (wave & 1) + 2 * (wave >> 2) + (g & 1)) * 131072 + (t >> 4) * 16384 + (wave & 2) * 4096 + (t & 15) * 512 + (g >> 1) * 256 + c * 16;
      put(s1, 0, (u64)(wo + k0 * 32 * 896 - 4096));
      put(s1, 2, (u64)(ws + WS_ST));
      put(s1, 4, 0);
      put(s1, 6, (u64)(w2 + st * 128 + (wave * 224 + rot * 32) * 4096));
      s3[0] = (wave & 3) * B_UNIT + (wave >> 2) * 1024;
      s3[1] = (wave >> 2) - 1;
      s3[2] = 13 - k0;
      s3[3] = wave * 4096;
    asm volatile(
      "s_waitcnt lgkmcnt(0)\n\ts_nop 4\n\t"
      ".p2align 3\n\t"
      "s_load_dwordx2 s[40:41], s[6:7], 0x20000\n\t"
      "v_mov_b32_e32 v248, -1\n\t"
      "v_mov_b32_e32 v249, -1\n\t"
      "v_mov_b32_e32 v250, -1\n\t"
      "v_mov_b32_e32 v251, -1\n\t"
      "v_add_u32_e32 v221, 4294965504, v224\n\t"
      "v_add_u32_e32 v222, 512, v224\n\t"
      "v_add_u32_e32 v223, 4294966016, v224\n\t"
      "s_bitcmp1_b32 s16, 0\n\t"
      "s_cselect_b64 s[46:47], -1, 0\n\t"
      "s_mov_b64 vcc, s[46:47]\n\t"
      "s_waitcnt vmcnt(3)\n\t"
      "s_barrier\n\t"
      "ds_read_b64_tr_b16 v[204:205], v225\n\t"
      "ds_read_b64_tr_b16 v[206:207], v225 offset:32\n\t"
      "ds_read_b64_tr_b16 v[208:209], v225 offset:64\n\t"
      "ds_read_b64_tr_b16 v[210:211], v225 offset:96\n\t"
      "s_waitcnt lgkmcnt(0)\n\t"
      "s_add_u32 s40, s40, 1\n\t"
      "s_addc_u32 s41, s41, 0\n\t"
      "s_mul_i32 s48, s40, 0x9e3779b9\n\t"
      "s_mul_hi_u32 s9, s40, 0x7f4a7c15\n\t"
      "s_add_u32 s9, s9, s48\n\t"
      "s_mul_i32 s48, s41, 0x7f4a7c15\n\t"
      "s_add_u32 s9, s9, s48\n\t"
      "s_mul_i32 s8, s40, 0x7f4a7c15\n\t"
      "s_add_u32 s8, s8, 0xd192ed03\n\t"
      "s_addc_u32 s9, s9, 0xd1b54a32\n\t"
      "v_mov_b32_e32 v188, s8\n\t"
      "v_mov_b32_e32 v189, s9\n\t"
      "v_mov_b32_e32 v190, s40\n\t"
      "v_mov_b32_e32 v191, s41\n\t"
      "v_mov_b32_e32 v192, 163600\n\t"
      "s_mov_b64 exec, 1\n\t"
      "ds_write_b128 v192, v[188:191]\n\t"
      "s_mov_b64 exec, -1\n\t"
      "v_and_b32_e32 v210, s13, v210\n\t"
      "v_and_b32_e32 v211, s13, v211\n\t"
      "v_lshlrev_b32_e32 v160, 16, v204\n\t"
      "v_and_b32_e32 v161, 0xffff0000, v204\n\t"
      "v_lshlrev_b32_e32 v162, 16, v205\n\t"
      "v_and_b32_e32 v163, 0xffff0000, v205\n\t"
      "v_lshlrev_b32_e32 v164, 16, v206\n\t"
      "v_and_b32_e32 v165, 0xffff0000, v206\n\t"
      "v_lshlrev_b32_e32 v166, 16, v207\n\t"
      "v_and_b32_e32 v167, 0xffff0000, v207\n\t"
      "v_lshlrev_b32_e32 v168, 16, v208\n\t"
      "v_and_b32_e32 v169, 0xffff0000, v208\n\t"
      "v_lshlrev_b32_e32 v170, 16, v209\n\t"
      "v_and_b32_e32 v171, 0xffff0000, v209\n\t"
      "v_lshlrev_b32_e32 v172, 16, v210\n\t"
      "v_and_b32_e32 v173, 0xffff0000, v210\n\t"
      "v_lshlrev_b32_e32 v174, 16, v211\n\t"
      "v_and_b32_e32 v175, 0xffff0000, v211\n\t"
      "v_pk_add_f32 v[188:189], v[160:161], v[164:165]\n\t"
      "v_pk_mul_f32 v[192:193], v[160:161], v[160:161]\n\t"
      "v_pk_add_f32 v[190:191], v[162:163], v[166:167]\n\t"
      "v_pk_mul_f32 v[194:195], v[162:163], v[162:163]\n\t"
      "v_pk_fma_f32 v[192:193], v[164:165], v[164:165], v[192:193]\n\t"
      "v_pk_fma_f32 v[194:195], v[166:167], v[166:167], v[194:195]\n\t"
      "v_pk_add_f32 v[188:189], v[188:189], v[168:169]\n\t"
      "v_pk_fma_f32 v[192:193], v[168:169], v[168:169], v[192:193]\n\t"
      "v_pk_add_f32 v[190:191], v[190:191], v[170:171]\n\t"
      "v_pk_fma_f32 v[194:195], v[170:171], v[170:171], v[194:195]\n\t"
      "s_cbranch_vccnz SB0_%=\n\t"
      "s_nop 0\n\t"
      "v_pk_add_f32 v[188:189], v[188:189], v[172:173]\n\t"
      "v_pk_fma_f32 v[192:193], v[172:173], v[172:173], v[192:193]\n\t"
      "v_pk_add_f32 v[190:191], v[190:191], v[174:175]\n\t"
      "v_pk_fma_f32 v[194:195], v[174:175], v[174:175], v[194:195]\n\t"
      "SB0_%=:\n\t"
      "v_add_f32_dpp v196, v188, v188 row_ror:8 row_mask:0xf bank_mask:0x3\n\t"
      "v_add_f32_dpp v197, v189, v189 row_ror:8 row_mask:0xf bank_mask:0x3\n\t"
      "v_add_f32_dpp v198, v192, v192 row_ror:8 row_mask:0xf bank_mask:0x3\n\t"
      "v_add_f32_dpp v199, v193, v193 row_ror:8 row_mask:0xf bank_mask:0x3\n\t"
      "v_add_f32_dpp v196, v190, v190 row_ror:8 row_mask:0xf bank_mask:0xc\n\t"
      "v_add_f32_dpp v197, v191, v191 row_ror:8 row_mask:0xf bank_mask:0xc\n\t"
      "v_add_f32_dpp v198, v194, v194 row_ror:8 row_mask:0xf bank_mask:0xc\n\t"
      "v_add_f32_dpp v199, v195, v195 row_ror:8 row_mask:0xf bank_mask:0xc\n\t"
      "v_add_f32_dpp v188, v196, v196 row_shl:4 row_mask:0xf bank_mask:0x5\n\t"
      "v_add_f32_dpp v189, v198, v198 row_shl:4 row_mask:0xf bank_mask:0x5\n\t"
      "s_nop 0\n\t"
      "v_add_f32_dpp v188, v197, v197 row_shr:4 row_mask:0xf bank_mask:0xa\n\t"
      "v_add_f32_dpp v189, v199, v199 row_shr:4 row_mask:0xf bank_mask:0xa\n\t"
      "s_nop 0\n\t"
      "v_add_f32_dpp v188, v188, v188 quad_perm:[1,0,3,2] row_mask:0xf bank_mask:0xf\n\t"
      "v_add_f32_dpp v189, v189, v189 quad_perm:[1,0,3,2] row_mask:0xf bank_mask:0xf\n\t"
      "s_nop 0\n\t"
      "v_add_f32_dpp v188, v188, v188 quad_perm:[2,3,0,1] row_mask:0xf bank_mask:0xf\n\t"
      "v_add_f32_dpp v189, v189, v189 quad_perm:[2,3,0,1] row_mask:0xf bank_mask:0xf\n\t"
      "ds_write_b64 v226, v[188:189]\n\t"
      "s_waitcnt lgkmcnt(0)\n\t"
      "s_barrier\n\t"
      "ds_read_b128 v[188:191], v227\n\t"
      "s_waitcnt lgkmcnt(0)\n\t"
      "v_pk_add_f32 v[188:189], v[188:189], v[190:191]\n\t"
      "s_nop 1\n\t"
      "v_add_f32_dpp v188, v188, v188 row_ror:4 row_mask:0xf bank_mask:0xf\n\t"
      "v_add_f32_dpp v189, v189, v189 row_ror:4 row_mask:0xf bank_mask:0xf\n\t"
      "s_nop 0\n\t"
      "v_add_f32_dpp v188, v188, v188 row_ror:8 row_mask:0xf bank_mask:0xf\n\t"
      "v_add_f32_dpp v189, v189, v189 row_ror:8 row_mask:0xf bank_mask:0xf\n\t"
      "v_mul_f32_e32 v188, 0x3b124925, v188\n\t"
      "v_mul_f32_e32 v189, 0x3b124925, v189\n\t"
      "v_fma_f32 v189, -v188, v188, v189\n\t"
      "v_max_f32_e32 v189, 0, v189\n\t"
      "s_bitcmp1_b32 s16, 4\n\t"
      "s_cbranch_scc0 GW_%=\n\t"
      "v_add_f32_e32 v189, 0x3727c5ac, v189\n\t"
      "v_rsq_f32_e32 v189, v189\n\t"
      "v_xor_b32_e32 v188, 0x80000000, v188\n\t"
      "v_xor_b32_e32 v190, s8, v188\n\t"
      "v_xor_b32_e32 v191, s9, v189\n\t"
      "s_mov_b32 s48, 0xf000f\n\t"
      "s_mov_b32 s49, 0xf000f\n\t"
      "s_mov_b64 exec, s[48:49]\n\t"
      "global_store_dwordx4 v228, v[188:191], s[6:7] sc1\n\t"
      "s_mov_b64 exec, -1\n\t"
      "GW_%=:\n\t"
      "s_add_u32 m0, s12, 0\n\t"
      "s_nop 0\n\t"
      "global_load_lds_dwordx4 v224, s[4:5]\n\t"
      "global_load_lds_dwordx4 v221, s[4:5] offset:2048\n\t"
      "s_add_u32 m0, s12, 4096\n\t"
      "s_nop 0\n\t"
      "global_load_lds_dwordx4 v222, s[4:5]\n\t"
      "s_cbranch_vccnz WR0_%=\n\t"
      "global_load_lds_dwordx4 v223, s[4:5] offset:2048\n\t"
      "WR0_%=:\n\t"
      "s_sub_u32 s14, s14, 1\n\t"
      "s_cselect_b32 s48, -13, 1\n\t"
      "s_cselect_b32 s49, -1, 0\n\t"
      "s_mul_i32 s48, s48, 28672\n\t"
      "s_add_u32 s4, s4, s48\n\t"
      "s_addc_u32 s5, s5, s49\n\t"
      "s_add_u32 m0, s12, 28672\n\t"
      "s_nop 0\n\t"
      "global_load_lds_dwordx4 v224, s[4:5]\n\t"
      "global_load_lds_dwordx4 v221, s[4:5] offset:2048\n\t"
      "s_add_u32 m0, s12, 32768\n\t"
      "s_nop 0\n\t"
      "global_load_lds_dwordx4 v222, s[4:5]\n\t"
      "s_cbranch_vccnz WR1_%=\n\t"
      "global_load_lds_dwordx4 v223, s[4:5] offset:2048\n\t"
      "WR1_%=:\n\t"
      "s_sub_u32 s14, s14, 1\n\t"
      "s_cselect_b32 s48, -13, 1\n\t"
      "s_cselect_b32 s49, -1, 0\n\t"
      "s_mul_i32 s48, s48, 28672\n\t"
      "s_add_u32 s4, s4, s48\n\t"
      "s_addc_u32 s5, s5, s49\n\t"
      "s_waitcnt lgkmcnt(0)\n\t"
      "s_nop 1\n\t"
      : "+{s[4:11]}"(s1), "+{s[12:15]}"(s3), "+{s16}"(bits), "+{s17}"(id), "+{s18}"(wv), "+{v[224:231]}"(v1), "={v[160:175]}"(acc)
      :
      : "memory", "vcc", "scc",
        "v176", "v177", "v178", "v179", "v180", "v181", "v182", "v183", "v184", "v185", "v186", "v187", "v188", "v189",
        "v190", "v191", "v192", "v193", "v194", "v195", "v196", "v197", "v198", "v199", "v200", "v201", "v202", "v203",
        "v204", "v205", "v206", "v207", "v208", "v209", "v210", "v211", "v212", "v213", "v214", "v215", "v216", "v217",
        "v218", "v219", "v220", "v221", "v222", "v223", "v248", "v249", "v250", "v251", "s40", "s41", "s42", "s43",
        "s44", "s45", "s46", "s47", "s48", "s49", "s50", "s51"
    );
    }
    PIN();
    {
    const int wave = wv & 0x3ffffff, xcd = id & 7, idx = id >> 3, side = xcd >> 2, xp = xcd & 3;
    B_INDICES;
    const int dg = 2 * side + (ib & 1), blk0 = 7 * dg, rn = (rot + W2E) % 7;
    bits |= (2 - ((wave >> 1) & 2)) | (((t + wave - 1) >> 26) & 32) | ((((wave - 7) & (t - 112)) >> 25) & 64);
    u32x4 vo, v3;
    u64 par = ST_PW + xcd * 128;
    {
      LANE_BITS(v1[6] >> 4);
      u32x8 v;
      u32x4 s;
      const unsigned tv = t + (lane >> 6), wv = wave + (lane >> 6), sv = (tv >> 6) * 8192;
      const unsigned ab = g * B_UNIT + q4 * 128 + (p4 & 1) * 8, al = ((p4 >> 1) ^ swz_tr(8 * g + q4)) << 4;
      const unsigned oi = B_X + c * B_OP, b0 = 112 * wv + 8 * g - 64 * k0;
#pragma unroll
      for (int b = 0; b < 4; ++b) {
        const unsigned cb = 4 * wv - (wv >> 2) * (wv & 3) + b;
        v[b] = ab + (cb >> 2) * 1024 + (((cb & 3) << 5) ^ al);
        const unsigned by = b0 + 56 * (b >> 1) + 32 * (b & 1);
        vo[b] = oi + min(by, by + 896);
      }
      v[4] = oi + 16 * g;
      v[5] = WS_G + tv * 16384 + wv * 1024 + lane * 16;
      v[6] = v1[7] + 4 * 131072;
      v[7] = v[5] + 8192;
      v3[0] = (wv * 224 + 96 + 64 * (side ^ (lane >> 5)) + (tv & 15) * 4 + (lr & 3)) * 4096 + lc * 128 + xp * 1024;
      v3[1] = ST_H3 + (lc & 3) * 32768 + (lc >> 2) * 8192 + 2 * sv + (tv & 63) * 128;
      v3[2] = ST_H2 + sv + (tv & 32) * 512 + (tv & 24) * 256 + wv * 256 + (tv & 4) * 32 + (lane & 1) * 64;
      v3[3] = ST_CK + sv + (tv & 56) * 2048 + (tv & 4) * 1024 + wv * 512 + (g >> 1) * 256 + (tv & 3) * 64 + c * 4;
      put(s, 0, (u64)w2);
      put(s, 2, (u64)(ws + (unsigned)(WS_O + (t & 63) * 32768 + (wave >> 2) * 16384 + side * 8192 + (wave & 3) * 2048)));
    asm volatile(
      "s_waitcnt lgkmcnt(0)\n\ts_nop 4\n\t"
      ".p2align 3\n\t"
      "s_nop 0\n\t"
      "v_mov_b32_e32 v248, -1\n\t"
      "v_mov_b32_e32 v249, -1\n\t"
      "v_mov_b32_e32 v250, -1\n\t"
      "v_mov_b32_e64 v251, -1\n\t"
      "v_add_u32_e32 v221, 4294965504, v224\n\t"
      "v_add_u32_e32 v222, 512, v224\n\t"
      "v_add_u32_e32 v223, 4294966016, v224\n\t"
      "s_bitcmp1_b32 s16, 0\n\t"
      "s_cselect_b64 s[46:47], -1, 0\n\t"
      "s_mov_b64 vcc, s[46:47]\n\t"
      "s_mov_b32 s44, 0xffffffff\n\t"
      "s_mov_b32 s45, 0xffff\n\t"
      "s_or_b32 s42, s13, 0xffff\n\t"
      "s_or_b32 s43, s13, 0xffff\n\t"
      "v_xor_b32_e64 v176, 64, v229\n\t"
      "v_add_u32_e32 v177, 63488, v229\n\t"
      "v_add_u32_e32 v178, 95232, v176\n\t"
      "v_add_u32_e32 v176, 31744, v176\n\t"
      "s_add_u32 m0, s15, 130560\n\t"
      "s_nop 0\n\t"
      "global_load_lds_dwordx4 v229, s[10:11]\n\t"
      "global_load_lds_dwordx4 v176, s[10:11] offset:1024\n\t"
      "global_load_lds_dwordx4 v177, s[10:11] offset:2048\n\t"
      "global_load_lds_dwordx4 v178, s[10:11] offset:3072\n\t"
      "s_add_u32 s10, s10, 131072\n\t"
      "s_addc_u32 s11, s11, 0\n\t"
      "s_cbranch_vccnz SN3_%=\n\t"
      "global_store_dwordx4 v231, v[248:251], s[6:7] sc1\n\t"
      "SN3_%=:\n\t"
      "s_cbranch_vccnz WW0_%=\n\t"
      "s_waitcnt vmcnt(8)\n\t"
      "s_branch WX0_%=\n\t"
      "WW0_%=:\n\t"
      "s_waitcnt vmcnt(7)\n\t"
      "WX0_%=:\n\t"
      "s_waitcnt lgkmcnt(0)\n\t"
      "s_barrier\n\t"
      "ds_read_b64_tr_b16 v[0:1], v232\n\t"
      "ds_read_b64_tr_b16 v[2:3], v232 offset:512\n\t"
      "ds_read_b64_tr_b16 v[4:5], v233\n\t"
      "ds_read_b64_tr_b16 v[6:7], v233 offset:512\n\t"
      "ds_read_b64_tr_b16 v[8:9], v234\n\t"
      "ds_read_b64_tr_b16 v[10:11], v234 offset:512\n\t"
      "ds_read_b64_tr_b16 v[12:13], v235\n\t"
      "ds_read_b64_tr_b16 v[14:15], v235 offset:512\n\t"
      "s_add_u32 m0, s12, 57344\n\t"
      "s_nop 0\n\t"
      "global_load_lds_dwordx4 v224, s[4:5]\n\t"
      "global_load_lds_dwordx4 v221, s[4:5] offset:2048\n\t"
      "s_add_u32 m0, s12, 61440\n\t"
      "s_nop 0\n\t"
      "global_load_lds_dwordx4 v222, s[4:5]\n\t"
      "s_cbranch_vccnz WR2_%=\n\t"
      "global_load_lds_dwordx4 v223, s[4:5] offset:2048\n\t"
      "WR2_%=:\n\t"
      "s_sub_u32 s14, s14, 1\n\t"
      "s_cselect_b32 s48, -13, 1\n\t"
      "s_cselect_b32 s49, -1, 0\n\t"
      "s_mul_i32 s48, s48, 28672\n\t"
      "s_add_u32 s4, s4, s48\n\t"
      "s_addc_u32 s5, s5, s49\n\t"
      "s_cbranch_vccnz WW1_%=\n\t"
      "s_waitcnt vmcnt(8)\n\t"
      "s_branch WX1_%=\n\t"
      "WW1_%=:\n\t"
      "s_waitcnt vmcnt(7)\n\t"
      "WX1_%=:\n\t"
      "s_waitcnt lgkmcnt(0)\n\t"
      "s_barrier\n\t"
      "ds_read_b64_tr_b16 v[16:17], v232 offset:28672\n\t"
      "ds_read_b64_tr_b16 v[18:19], v232 offset:29184\n\t"
      "ds_read_b64_tr_b16 v[20:21], v233 offset:28672\n\t"
      "ds_read_b64_tr_b16 v[22:23], v233 offset:29184\n\t"
      "ds_read_b64_tr_b16 v[24:25], v234 offset:28672\n\t"
      "ds_read_b64_tr_b16 v[26:27], v234 offset:29184\n\t"
      "ds_read_b64_tr_b16 v[28:29], v235 offset:28672\n\t"
      "ds_read_b64_tr_b16 v[30:31], v235 offset:29184\n\t"
      "s_add_u32 m0, s12, 86016\n\t"
      "s_nop 0\n\t"
      "global_load_lds_dwordx4 v224, s[4:5]\n\t"
      "global_load_lds_dwordx4 v221, s[4:5] offset:2048\n\t"
      "s_add_u32 m0, s12, 90112\n\t"
      "s_nop 0\n\t"
      "global_load_lds_dwordx4 v222, s[4:5]\n\t"
      "s_cbranch_vccnz WR3_%=\n\t"
      "global_load_lds_dwordx4 v223, s[4:5] offset:2048\n\t"
      "WR3_%=:\n\t"
      "s_sub_u32 s14, s14, 1\n\t"
      "s_cselect_b32 s48, -13, 1\n\t"
      "s_cselect_b32 s49, -1, 0\n\t"
      "s_mul_i32 s48, s48, 28672\n\t"
      "s_add_u32 s4, s4, s48\n\t"
      "s_addc_u32 s5, s5, s49\n\t"
      "v_add_u32_e32 v232, 57344, v232\n\t"
      "v_add_u32_e32 v233, 57344, v233\n\t"
      "v_add_u32_e32 v234, 57344, v234\n\t"
      "v_add_u32_e32 v235, 57344, v235\n\t"
      "s_cbranch_vccnz WW2_%=\n\t"
      "s_waitcnt vmcnt(4)\n\t"
      "s_branch WX2_%=\n\t"
      "WW2_%=:\n\t"
      "s_waitcnt vmcnt(3)\n\t"
      "WX2_%=:\n\t"
      "s_waitcnt lgkmcnt(0)\n\t"
      "s_barrier\n\t"
      "ds_read_b64_tr_b16 v[32:33], v232\n\t"
      "ds_read_b64_tr_b16 v[34:35], v232 offset:512\n\t"
      "ds_read_b64_tr_b16 v[36:37], v233\n\t"
      "ds_read_b64_tr_b16 v[38:39], v233 offset:512\n\t"
      "ds_read_b64_tr_b16 v[40:41], v234\n\t"
      "ds_read_b64_tr_b16 v[42:43], v234 offset:512\n\t"
      "ds_read_b64_tr_b16 v[44:45], v235\n\t"
      "ds_read_b64_tr_b16 v[46:47], v235 offset:512\n\t"
      "s_add_u32 m0, s12, 0\n\t"
      "s_nop 0\n\t"
      "global_load_lds_dwordx4 v224, s[4:5]\n\t"
      "global_load_lds_dwordx4 v221, s[4:5] offset:2048\n\t"
      "s_add_u32 m0, s12, 4096\n\t"
      "s_nop 0\n\t"
      "global_load_lds_dwordx4 v222, s[4:5]\n\t"
      "s_cbranch_vccnz WR4_%=\n\t"
      "global_load_lds_dwordx4 v223, s[4:5] offset:2048\n\t"
      "WR4_%=:\n\t"
      "s_sub_u32 s14, s14, 1\n\t"
      "s_cselect_b32 s48, -13, 1\n\t"
      "s_cselect_b32 s49, -1, 0\n\t"
      "s_mul_i32 s48, s48, 28672\n\t"
      "s_add_u32 s4, s4, s48\n\t"
      "s_addc_u32 s5, s5, s49\n\t"
      "s_cbranch_vccnz WW3_%=\n\t"
      "s_waitcnt vmcnt(4)\n\t"
      "s_branch WX3_%=\n\t"
      "WW3_%=:\n\t"
      "s_waitcnt vmcnt(3)\n\t"
      "WX3_%=:\n\t"
      "s_waitcnt lgkmcnt(0)\n\t"
      "s_barrier\n\t"
      "ds_read_b64_tr_b16 v[48:49], v232 offset:28672\n\t"
      "ds_read_b64_tr_b16 v[50:51], v232 offset:29184\n\t"
      "ds_read_b64_tr_b16 v[52:53], v233 offset:28672\n\t"
      "ds_read_b64_tr_b16 v[54:55], v233 offset:29184\n\t"
      "ds_read_b64_tr_b16 v[56:57], v234 offset:28672\n\t"
      "ds_read_b64_tr_b16 v[58:59], v234 offset:29184\n\t"
      "ds_read_b64_tr_b16 v[60:61], v235 offset:28672\n\t"
      "ds_read_b64_tr_b16 v[62:63], v235 offset:29184\n\t"
      "s_add_u32 m0, s12, 28672\n\t"
      "s_nop 0\n\t"
      "global_load_lds_dwordx4 v224, s[4:5]\n\t"
      "global_load_lds_dwordx4 v221, s[4:5] offset:2048\n\t"
      "s_add_u32 m0, s12, 32768\n\t"
      "s_nop 0\n\t"
      "global_load_lds_dwordx4 v222, s[4:5]\n\t"
      "s_cbranch_vccnz WR5_%=\n\t"
      "global_load_lds_dwordx4 v223, s[4:5] offset:2048\n\t"
      "WR5_%=:\n\t"
      "s_sub_u32 s14, s14, 1\n\t"
      "s_cselect_b32 s48, -13, 1\n\t"
      "s_cselect_b32 s49, -1, 0\n\t"
      "s_mul_i32 s48, s48, 28672\n\t"
      "s_add_u32 s4, s4, s48\n\t"
      "s_addc_u32 s5, s5, s49\n\t"
      "v_add_u32_e32 v232, 4294909952, v232\n\t"
      "v_add_u32_e32 v233, 4294909952, v233\n\t"
      "v_add_u32_e32 v234, 4294909952, v234\n\t"
      "v_add_u32_e32 v235, 4294909952, v235\n\t"
      "s_cbranch_vccnz WW4_%=\n\t"
      "s_waitcnt vmcnt(4)\n\t"
      "s_branch WX4_%=\n\t"
      "WW4_%=:\n\t"
      "s_waitcnt vmcnt(3)\n\t"
      "WX4_%=:\n\t"
      "s_waitcnt lgkmcnt(0)\n\t"
      "s_barrier\n\t"
      "ds_read_b64_tr_b16 v[64:65], v232\n\t"
      "ds_read_b64_tr_b16 v[66:67], v232 offset:512\n\t"
      "ds_read_b64_tr_b16 v[68:69], v233\n\t"
      "ds_read_b64_tr_b16 v[70:71], v233 offset:512\n\t"
      "ds_read_b64_tr_b16 v[72:73], v234\n\t"
      "ds_read_b64_tr_b16 v[74:75], v234 offset:512\n\t"
      "ds_read_b64_tr_b16 v[76:77], v235\n\t"
      "ds_read_b64_tr_b16 v[78:79], v235 offset:512\n\t"
      "s_add_u32 m0, s12, 57344\n\t"
      "s_nop 0\n\t"
      "global_load_lds_dwordx4 v224, s[4:5]\n\t"
      "global_load_lds_dwordx4 v221, s[4:5] offset:2048\n\t"
      "s_add_u32 m0, s12, 61440\n\t"
      "s_nop 0\n\t"
      "global_load_lds_dwordx4 v222, s[4:5]\n\t"
      "s_cbranch_vccnz WR6_%=\n\t"
      "global_load_lds_dwordx4 v223, s[4:5] offset:2048\n\t"
      "WR6_%=:\n\t"
      "s_sub_u32 s14, s14, 1\n\t"
      "s_cselect_b32 s48, -13, 1\n\t"
      "s_cselect_b32 s49, -1, 0\n\t"
      "s_mul_i32 s48, s48, 28672\n\t"
      "s_add_u32 s4, s4, s48\n\t"
      "s_addc_u32 s5, s5, s49\n\t"
      "s_cbranch_vccnz WW5_%=\n\t"
      "s_waitcnt vmcnt(4)\n\t"
      "s_branch WX5_%=\n\t"
      "WW5_%=:\n\t"
      "s_waitcnt vmcnt(3)\n\t"
      "WX5_%=:\n\t"
      "s_waitcnt lgkmcnt(0)\n\t"
      "s_barrier\n\t"
      "ds_read_b64_tr_b16 v[80:81], v232 offset:28672\n\t"
      "ds_read_b64_tr_b16 v[82:83], v232 offset:29184\n\t"
      "ds_read_b64_tr_b16 v[84:85], v233 offset:28672\n\t"
      "ds_read_b64_tr_b16 v[86:87], v233 offset:29184\n\t"
      "ds_read_b64_tr_b16 v[88:89], v234 offset:28672\n\t"
      "ds_read_b64_tr_b16 v[90:91], v234 offset:29184\n\t"
      "ds_read_b64_tr_b16 v[92:93], v235 offset:28672\n\t"
      "ds_read_b64_tr_b16 v[94:95], v235 offset:29184\n\t"
      "s_add_u32 m0, s12, 86016\n\t"
      "s_nop 0\n\t"
      "global_load_lds_dwordx4 v224, s[4:5]\n\t"
      "global_load_lds_dwordx4 v221, s[4:5] offset:2048\n\t"
      "s_add_u32 m0, s12, 90112\n\t"
      "s_nop 0\n\t"
      "global_load_lds_dwordx4 v222, s[4:5]\n\t"
      "s_cbranch_vccnz WR7_%=\n\t"
      "global_load_lds_dwordx4 v223, s[4:5] offset:2048\n\t"
      "WR7_%=:\n\t"
      "s_sub_u32 s14, s14, 1\n\t"
      "s_cselect_b32 s48, -13, 1\n\t"
      "s_cselect_b32 s49, -1, 0\n\t"
      "s_mul_i32 s48, s48, 28672\n\t"
      "s_add_u32 s4, s4, s48\n\t"
      "s_addc_u32 s5, s5, s49\n\t"
      "v_add_u32_e32 v232, 57344, v232\n\t"
      "v_add_u32_e32 v233, 57344, v233\n\t"
      "v_add_u32_e32 v234, 57344, v234\n\t"
      "v_add_u32_e32 v235, 57344, v235\n\t"
      "s_cbranch_vccnz WW6_%=\n\t"
      "s_waitcnt vmcnt(4)\n\t"
      "s_branch WX6_%=\n\t"
      "WW6_%=:\n\t"
      "s_waitcnt vmcnt(3)\n\t"
      "WX6_%=:\n\t"
      "s_waitcnt lgkmcnt(0)\n\t"
      "s_barrier\n\t"
      "ds_read_b64_tr_b16 v[96:97], v232\n\t"
      "ds_read_b64_tr_b16 v[98:99], v232 offset:512\n\t"
      "ds_read_b64_tr_b16 v[100:101], v233\n\t"
      "ds_read_b64_tr_b16 v[102:103], v233 offset:512\n\t"
      "ds_read_b64_tr_b16 v[104:105], v234\n\t"
      "ds_read_b64_tr_b16 v[106:107], v234 offset:512\n\t"
      "ds_read_b64_tr_b16 v[108:109], v235\n\t"
      "ds_read_b64_tr_b16 v[110:111], v235 offset:512\n\t"
      "s_add_u32 m0, s12, 0\n\t"
      "s_nop 0\n\t"
      "global_load_lds_dwordx4 v224, s[4:5]\n\t"
      "global_load_lds_dwordx4 v221, s[4:5] offset:2048\n\t"
      "s_add_u32 m0, s12, 4096\n\t"
      "s_nop 0\n\t"
      "global_load_lds_dwordx4 v222, s[4:5]\n\t"
      "s_cbranch_vccnz WR8_%=\n\t"
      "global_load_lds_dwordx4 v223, s[4:5] offset:2048\n\t"
      "WR8_%=:\n\t"
      "s_sub_u32 s14, s14, 1\n\t"
      "s_cselect_b32 s48, -13, 1\n\t"
      "s_cselect_b32 s49, -1, 0\n\t"
      "s_mul_i32 s48, s48, 28672\n\t"
      "s_add_u32 s4, s4, s48\n\t"
      "s_addc_u32 s5, s5, s49\n\t"
      "s_cbranch_vccnz WW7_%=\n\t"
      "s_waitcnt vmcnt(4)\n\t"
      "s_branch WX7_%=\n\t"
      "WW7_%=:\n\t"
      "s_waitcnt vmcnt(3)\n\t"
      "WX7_%=:\n\t"
      "s_waitcnt lgkmcnt(0)\n\t"
      "s_barrier\n\t"
      "ds_read_b64_tr_b16 v[112:113], v232 offset:28672\n\t"
      "ds_read_b64_tr_b16 v[114:115], v232 offset:29184\n\t"
      "ds_read_b64_tr_b16 v[116:117], v233 offset:28672\n\t"
      "ds_read_b64_tr_b16 v[118:119], v233 offset:29184\n\t"
      "ds_read_b64_tr_b16 v[120:121], v234 offset:28672\n\t"
      "ds_read_b64_tr_b16 v[122:123], v234 offset:29184\n\t"
      "ds_read_b64_tr_b16 v[124:125], v235 offset:28672\n\t"
      "ds_read_b64_tr_b16 v[126:127], v235 offset:29184\n\t"
      "s_add_u32 m0, s12, 28672\n\t"
      "s_nop 0\n\t"
      "global_load_lds_dwordx4 v224, s[4:5]\n\t"
      "global_load_lds_dwordx4 v221, s[4:5] offset:2048\n\t"
      "s_add_u32 m0, s12, 32768\n\t"
      "s_nop 0\n\t"
      "global_load_lds_dwordx4 v222, s[4:5]\n\t"
      "s_cbranch_vccnz WR9_%=\n\t"
      "global_load_lds_dwordx4 v223, s[4:5] offset:2048\n\t"
      "WR9_%=:\n\t"
      "s_sub_u32 s14, s14, 1\n\t"
      "s_cselect_b32 s48, -13, 1\n\t"
      "s_cselect_b32 s49, -1, 0\n\t"
      "s_mul_i32 s48, s48, 28672\n\t"
      "s_add_u32 s4, s4, s48\n\t"
      "s_addc_u32 s5, s5, s49\n\t"
      "s_nop 0\n\t"
      "v_add_u32_e32 v232, 4294909952, v232\n\t"
      "v_add_u32_e32 v233, 4294909952, v233\n\t"
      "v_add_u32_e32 v234, 4294909952, v234\n\t"
      "v_add_u32_e32 v235, 4294909952, v235\n\t"
      "s_cbranch_vccnz WW8_%=\n\t"
      "s_waitcnt vmcnt(4)\n\t"
      "s_branch WX8_%=\n\t"
      "WW8_%=:\n\t"
      "s_waitcnt vmcnt(3)\n\t"
      "WX8_%=:\n\t"
      "s_waitcnt lgkmcnt(0)\n\t"
      "s_barrier\n\t"
      "ds_read_b64_tr_b16 v[128:129], v232\n\t"
      "ds_read_b64_tr_b16 v[130:131], v232 offset:512\n\t"
      "ds_read_b64_tr_b16 v[132:133], v233\n\t"
      "ds_read_b64_tr_b16 v[134:135], v233 offset:512\n\t"
      "ds_read_b64_tr_b16 v[136:137], v234\n\t"
      "ds_read_b64_tr_b16 v[138:139], v234 offset:512\n\t"
      "ds_read_b64_tr_b16 v[140:141], v235\n\t"
      "ds_read_b64_tr_b16 v[142:143], v235 offset:512\n\t"
      "s_add_u32 m0, s12, 57344\n\t"
      "s_nop 0\n\t"
      "global_load_lds_dwordx4 v224, s[4:5]\n\t"
      "global_load_lds_dwordx4 v221, s[4:5] offset:2048\n\t"
      "s_add_u32 m0, s12, 61440\n\t"
      "s_nop 0\n\t"
      "global_load_lds_dwordx4 v222, s[4:5]\n\t"
      "s_cbranch_vccnz WR10_%=\n\t"
      "global_load_lds_dwordx4 v223, s[4:5] offset:2048\n\t"
      "WR10_%=:\n\t"
      "s_sub_u32 s14, s14, 1\n\t"
      "s_cselect_b32 s48, -13, 1\n\t"
      "s_cselect_b32 s49, -1, 0\n\t"
      "s_mul_i32 s48, s48, 28672\n\t"
      "s_add_u32 s4, s4, s48\n\t"
      "s_addc_u32 s5, s5, s49\n\t"
      "s_cbranch_vccnz WW9_%=\n\t"
      "s_waitcnt vmcnt(4)\n\t"
      "s_branch WX9_%=\n\t"
      "WW9_%=:\n\t"
      "s_waitcnt vmcnt(3)\n\t"
      "WX9_%=:\n\t"
      "s_waitcnt lgkmcnt(0)\n\t"
      "s_barrier\n\t"
      "ds_read_b64_tr_b16 v[144:145], v232 offset:28672\n\t"
      "ds_read_b64_tr_b16 v[146:147], v232 offset:29184\n\t"
      "ds_read_b64_tr_b16 v[148:149], v233 offset:28672\n\t"
      "ds_read_b64_tr_b16 v[150:151], v233 offset:29184\n\t"
      "ds_read_b64_tr_b16 v[152:153], v234 offset:28672\n\t"
      "ds_read_b64_tr_b16 v[154:155], v234 offset:29184\n\t"
      "ds_read_b64_tr_b16 v[156:157], v235 offset:28672\n\t"
      "ds_read_b64_tr_b16 v[158:159], v235 offset:29184\n\t"
      "s_add_u32 m0, s12, 86016\n\t"
      "s_nop 0\n\t"
      "global_load_lds_dwordx4 v224, s[4:5]\n\t"
      "global_load_lds_dwordx4 v221, s[4:5] offset:2048\n\t"
      "s_add_u32 m0, s12, 90112\n\t"
      "s_nop 0\n\t"
      "global_load_lds_dwordx4 v222, s[4:5]\n\t"
      "s_cbranch_vccnz WR11_%=\n\t"
      "global_load_lds_dwordx4 v223, s[4:5] offset:2048\n\t"
      "WR11_%=:\n\t"
      "s_sub_u32 s14, s14, 1\n\t"
      "s_cselect_b32 s48, -13, 1\n\t"
      "s_cselect_b32 s49, -1, 0\n\t"
      "s_mul_i32 s48, s48, 28672\n\t"
      "s_add_u32 s4, s4, s48\n\t"
      "s_addc_u32 s5, s5, s49\n\t"
      "v_add_u32_e32 v232, 57344, v232\n\t"
      "v_add_u32_e32 v233, 57344, v233\n\t"
      "v_add_u32_e32 v234, 57344, v234\n\t"
      "v_add_u32_e32 v235, 57344, v235\n\t"
      "v_min_u32_e32 v184, 32, v230\n\t"
      "v_add_u32_e32 v184, s28, v184\n\t"
      "global_load_dwordx4 v[180:183], v184, s[6:7] sc1\n\t"
      "s_cbranch_vccnz WW10_%=\n\t"
      "s_waitcnt vmcnt(5)\n\t"
      "s_branch WX10_%=\n\t"
      "WW10_%=:\n\t"
      "s_waitcnt vmcnt(4)\n\t"
      "WX10_%=:\n\t"
      "s_waitcnt lgkmcnt(0)\n\t"
      "s_barrier\n\t"
      "ds_read_b64_tr_b16 v[188:189], v232\n\t"
      "ds_read_b64_tr_b16 v[190:191], v232 offset:512\n\t"
      "ds_read_b64_tr_b16 v[192:193], v233\n\t"
      "ds_read_b64_tr_b16 v[194:195], v233 offset:512\n\t"
      "ds_read_b64_tr_b16 v[196:197], v234\n\t"
      "ds_read_b64_tr_b16 v[198:199], v234 offset:512\n\t"
      "ds_read_b64_tr_b16 v[200:201], v235\n\t"
      "ds_read_b64_tr_b16 v[202:203], v235 offset:512\n\t"
      "s_add_u32 m0, s12, 0\n\t"
      "s_nop 0\n\t"
      "global_load_lds_dwordx4 v224, s[4:5]\n\t"
      "global_load_lds_dwordx4 v221, s[4:5] offset:2048\n\t"
      "s_add_u32 m0, s12, 4096\n\t"
      "s_nop 0\n\t"
      "global_load_lds_dwordx4 v222, s[4:5]\n\t"
      "s_cbranch_vccnz WR12_%=\n\t"
      "global_load_lds_dwordx4 v223, s[4:5] offset:2048\n\t"
      "WR12_%=:\n\t"
      "s_sub_u32 s14, s14, 1\n\t"
      "s_cselect_b32 s48, -13, 1\n\t"
      "s_cselect_b32 s49, -1, 0\n\t"
      "s_mul_i32 s48, s48, 28672\n\t"
      "s_add_u32 s4, s4, s48\n\t"
      "s_addc_u32 s5, s5, s49\n\t"
      "s_cbranch_vccnz WW11_%=\n\t"
      "s_waitcnt vmcnt(5)\n\t"
      "s_branch WX11_%=\n\t"
      "WW11_%=:\n\t"
      "s_waitcnt vmcnt(4)\n\t"
      "WX11_%=:\n\t"
      "s_waitcnt lgkmcnt(0)\n\t"
      "s_barrier\n\t"
      "ds_read_b64_tr_b16 v[204:205], v232 offset:28672\n\t"
      "ds_read_b64_tr_b16 v[206:207], v232 offset:29184\n\t"
      "ds_read_b64_tr_b16 v[208:209], v233 offset:28672\n\t"
      "ds_read_b64_tr_b16 v[210:211], v233 offset:29184\n\t"
      "ds_read_b64_tr_b16 v[212:213], v234 offset:28672\n\t"
      "ds_read_b64_tr_b16 v[214:215], v234 offset:29184\n\t"
      "ds_read_b64_tr_b16 v[216:217], v235 offset:28672\n\t"
      "ds_read_b64_tr_b16 v[218:219], v235 offset:29184\n\t"
      "s_add_u32 m0, s12, 28672\n\t"
      "s_nop 0\n\t"
      "global_load_lds_dwordx4 v224, s[4:5]\n\t"
      "global_load_lds_dwordx4 v221, s[4:5] offset:2048\n\t"
      "s_add_u32 m0, s12, 32768\n\t"
      "s_nop 0\n\t"
      "global_load_lds_dwordx4 v222, s[4:5]\n\t"
      "s_cbranch_vccnz WR13_%=\n\t"
      "global_load_lds_dwordx4 v223, s[4:5] offset:2048\n\t"
      "WR13_%=:\n\t"
      "v_add_u32_e32 v232, 4294909952, v232\n\t"
      "v_add_u32_e32 v233, 4294909952, v233\n\t"
      "v_add_u32_e32 v234, 4294909952, v234\n\t"
      "v_add_u32_e32 v235, 4294909952, v235\n\t"
      "s_waitcnt vmcnt(6)\n\t"
      "v_xor_b32_e32 v182, v182, v180\n\t"
      "v_xor_b32_e64 v183, v183, v181\n\t"
      "v_cmp_ne_u64_e32 vcc, s[8:9], v[182:183]\n\t"
      "s_cbranch_vccnz PWR_%=\n\t"
      "PWB_%=:\n\t"
      "v_readlane_b32 s48, v180, 0\n\t"
      "v_readlane_b32 s28, v180, 1\n\t"
      "v_readlane_b32 s29, v180, 2\n\t"
      "v_mov_b32_e32 v220, s8\n\t"
      "v_mov_b32_e32 v221, s9\n\t"
      "s_xor_b32 s22, s22, s48\n\t"
      "v_xor_b32_e32 v231, s28, v231\n\t"
      "v_xor_b32_e32 v238, s28, v238\n\t"
      "v_xor_b32_e32 v246, s28, v246\n\t"
      "v_xor_b32_e32 v247, s28, v247\n\t"
      "v_xor_b32_e32 v245, s29, v245\n\t"
      "s_waitcnt lgkmcnt(0)\n\t"
      "s_waitcnt vmcnt(0)\n\t"
      "s_mov_b32 s51, 0\n\t"
      "s_branch QE_%=\n\t"
      ".p2align 6\n\t"
      "QL_%=:\n\t"
      "s_sleep 2\n\t"
      "s_add_u32 s51, s51, 1\n\t"
      "s_cmp_lt_u32 s51, 262144\n\t"
      "s_cbranch_scc0 QX_%=\n\t"
      "QE_%=:\n\t"
      "s_mov_b64 exec, 3\n\t"
      "v_mov_b64_e32 v[176:177], 0\n\t"
      "global_atomic_or_x2 v[176:177], v246, v[176:177], s[6:7] sc0 sc1\n\t"
      "s_waitcnt vmcnt(0)\n\t"
      "v_cmp_ne_u64_e32 vcc, s[8:9], v[176:177]\n\t"
      "s_mov_b64 exec, -1\n\t"
      "s_cbranch_vccnz QL_%=\n\t"
      "QX_%=:\n\t"
      "s_mov_b32 s51, 0\n\t"
      "s_branch OE_%=\n\t"
      "OL_%=:\n\t"
      "s_sleep 2\n\t"
      "s_add_u32 s51, s51, 1\n\t"
      "s_cmp_lt_u32 s51, 262144\n\t"
      "s_cbranch_scc0 OX_%=\n\t"
      "OE_%=:\n\t"
      "global_load_dwordx4 v[176:179], v230, s[22:23] sc1\n\t"
      "global_load_dwordx4 v[180:183], v230, s[22:23] offset:1024 sc1\n\t"
      "global_load_dword v186, v247, s[6:7] sc1\n\t"
      "s_waitcnt vmcnt(2)\n\t"
      "ds_write_b64 v240, v[176:177]\n\t"
      "s_mov_b64 exec, s[44:45]\n\t"
      "ds_write_b64 v241, v[178:179]\n\t"
      "s_mov_b64 exec, -1\n\t"
      "v_add3_u32 v184, v176, v177, v178\n\t"
      "v_add_u32_e32 v184, v184, v179\n\t"
      "s_waitcnt vmcnt(1)\n\t"
      "ds_write_b64 v242, v[180:181]\n\t"
      "s_mov_b64 exec, s[44:45]\n\t"
      "ds_write_b64 v243, v[182:183]\n\t"
      "s_mov_b64 exec, -1\n\t"
      "v_add3_u32 v185, v180, v181, v182\n\t"
      "v_add_u32_e32 v185, v185, v183\n\t"
      "s_nop 1\n\t"
      "v_permlane32_swap_b32_e32 v184, v185\n\t"
      "v_add_u32_e64 v184, v184, v185\n\t"
      "v_mov_b32_e32 v185, v184\n\t"
      "s_nop 1\n\t"
      "v_permlane16_swap_b32_e32 v184, v185\n\t"
      "v_add3_u32 v184, v184, v185, s8\n\t"
      "s_waitcnt vmcnt(0) lgkmcnt(0)\n\t"
      "v_cmp_ne_u32_e32 vcc, v184, v186\n\t"
      "s_cbranch_vccnz OL_%=\n\t"
      "OX_%=:\n\t"
      "s_mov_b64 vcc, s[46:47]\n\t"
      "s_waitcnt lgkmcnt(0)\n\t"
      "s_barrier\n\t"
      "ds_read_b128 v[176:179], v236\n\t"
      "ds_read_b128 v[180:183], v236 offset:64\n\t"
      "ds_read_b128 v[184:187], v236 offset:128\n\t"
      "s_waitcnt lgkmcnt(2)\n\t"
      "v_mfma_f32_16x16x32_bf16 v[160:163], v[176:179], v[0:3], v[160:163]\n\t"
      "v_mfma_f32_16x16x32_bf16 v[164:167], v[176:179], v[4:7], v[164:167]\n\t"
      "v_mfma_f32_16x16x32_bf16 v[168:171], v[176:179], v[8:11], v[168:171]\n\t"
      "s_cbranch_vccnz M0_%=\n\t"
      "v_mfma_f32_16x16x32_bf16 v[172:175], v[176:179], v[12:15], v[172:175]\n\t"
      "M0_%=:\n\t"
      "s_cbranch_vccz SN4_%=\n\t"
      "global_store_dwordx4 v231, v[248:251], s[6:7] sc1\n\t"
      "SN4_%=:\n\t"
      "ds_read_b128 v[176:179], v236 offset:192\n\t"
      "s_waitcnt lgkmcnt(2)\n\t"
      "v_mfma_f32_16x16x32_bf16 v[160:163], v[180:183], v[16:19], v[160:163]\n\t"
      "v_mfma_f32_16x16x32_bf16 v[164:167], v[180:183], v[20:23], v[164:167]\n\t"
      "v_mfma_f32_16x16x32_bf16 v[168:171], v[180:183], v[24:27], v[168:171]\n\t"
      "s_cbranch_vccnz M1_%=\n\t"
      "v_mfma_f32_16x16x32_bf16 v[172:175], v[180:183], v[28:31], v[172:175]\n\t"
      "M1_%=:\n\t"
      "s_mov_b64 exec, s[42:43]\n\t"
      "global_store_dwordx4 v238, v[248:251], s[6:7] sc1\n\t"
      "s_mov_b64 exec, -1\n\t"
      "ds_read_b128 v[180:183], v236 offset:256\n\t"
      "s_waitcnt lgkmcnt(2)\n\t"
      "v_mfma_f32_16x16x32_bf16 v[160:163], v[184:187], v[32:35], v[160:163]\n\t"
      "v_mfma_f32_16x16x32_bf16 v[164:167], v[184:187], v[36:39], v[164:167]\n\t"
      "v_mfma_f32_16x16x32_bf16 v[168:171], v[184:187], v[40:43], v[168:171]\n\t"
      "s_cbranch_vccnz M2_%=\n\t"
      "v_mfma_f32_16x16x32_bf16 v[172:175], v[184:187], v[44:47], v[172:175]\n\t"
      "M2_%=:\n\t"
      "ds_read_b64_tr_b16 v[0:1], v232\n\t"
      "ds_read_b64_tr_b16 v[2:3], v232 offset:512\n\t"
      "s_bitcmp1_b32 s16, 6\n\t"
      "s_cbranch_scc0 SG2_%=\n\t"
      "global_store_dwordx4 v237, v[248:251], s[6:7] sc1\n\t"
      "global_store_dwordx4 v239, v[248:251], s[6:7] sc1\n\t"
      "SG2_%=:\n\t"
      "ds_read_b128 v[184:187], v236 offset:320\n\t"
      "s_waitcnt lgkmcnt(4)\n\t"
      "v_mfma_f32_16x16x32_bf16 v[160:163], v[176:179], v[48:51], v[160:163]\n\t"
      "v_mfma_f32_16x16x32_bf16 v[164:167], v[176:179], v[52:55], v[164:167]\n\t"
      "v_mfma_f32_16x16x32_bf16 v[168:171], v[176:179], v[56:59], v[168:171]\n\t"
      "s_cbranch_vccnz M3_%=\n\t"
      "v_mfma_f32_16x16x32_bf16 v[172:175], v[176:179], v[60:63], v[172:175]\n\t"
      "M3_%=:\n\t"
      "ds_read_b64_tr_b16 v[4:5], v233\n\t"
      "ds_read_b64_tr_b16 v[6:7], v233 offset:512\n\t"
      "ds_read_b128 v[176:179], v236 offset:384\n\t"
      "s_waitcnt lgkmcnt(6)\n\t"
      "v_mfma_f32_16x16x32_bf16 v[160:163], v[180:183], v[64:67], v[160:163]\n\t"
      "v_mfma_f32_16x16x32_bf16 v[164:167], v[180:183], v[68:71], v[164:167]\n\t"
      "v_mfma_f32_16x16x32_bf16 v[168:171], v[180:183], v[72:75], v[168:171]\n\t"
      "s_cbranch_vccnz M4_%=\n\t"
      "v_mfma_f32_16x16x32_bf16 v[172:175], v[180:183], v[76:79], v[172:175]\n\t"
      "M4_%=:\n\t"
      "ds_read_b64_tr_b16 v[8:9], v234\n\t"
      "ds_read_b64_tr_b16 v[10:11], v234 offset:512\n\t"
      "ds_read_b128 v[180:183], v236 offset:448\n\t"
      "s_waitcnt lgkmcnt(6)\n\t"
      "v_mfma_f32_16x16x32_bf16 v[160:163], v[184:187], v[80:83], v[160:163]\n\t"
      "v_mfma_f32_16x16x32_bf16 v[164:167], v[184:187], v[84:87], v[164:167]\n\t"
      "v_mfma_f32_16x16x32_bf16 v[168:171], v[184:187], v[88:91], v[168:171]\n\t"
      "s_cbranch_vccnz M5_%=\n\t"
      "v_mfma_f32_16x16x32_bf16 v[172:175], v[184:187], v[92:95], v[172:175]\n\t"
      "M5_%=:\n\t"
      "ds_read_b64_tr_b16 v[12:13], v235\n\t"
      "ds_read_b64_tr_b16 v[14:15], v235 offset:512\n\t"
      "s_mov_b32 exec_lo, 0xffff\n\t"
      "s_mov_b32 exec_hi, 0x0\n\t"
      "s_mov_b32 m0, 163328\n\t"
      "s_nop 0\n\t"
      "global_load_lds_dword v244, s[20:21]\n\t"
      "s_mov_b64 exec, -1\n\t"
      "ds_read_b128 v[184:187], v236 offset:512\n\t"
      "s_waitcnt lgkmcnt(6)\n\t"
      "v_mfma_f32_16x16x32_bf16 v[160:163], v[176:179], v[96:99], v[160:163]\n\t"
      "v_mfma_f32_16x16x32_bf16 v[164:167], v[176:179], v[100:103], v[164:167]\n\t"
      "v_mfma_f32_16x16x32_bf16 v[168:171], v[176:179], v[104:107], v[168:171]\n\t"
      "s_cbranch_vccnz M6_%=\n\t"
      "v_mfma_f32_16x16x32_bf16 v[172:175], v[176:179], v[108:111], v[172:175]\n\t"
      "M6_%=:\n\t"
      "ds_read_b64_tr_b16 v[16:17], v232 offset:28672\n\t"
      "ds_read_b64_tr_b16 v[18:19], v232 offset:29184\n\t"
      "ds_read_b128 v[176:179], v236 offset:576\n\t"
      "s_waitcnt lgkmcnt(6)\n\t"
      "v_mfma_f32_16x16x32_bf16 v[160:163], v[180:183], v[112:115], v[160:163]\n\t"
      "v_mfma_f32_16x16x32_bf16 v[164:167], v[180:183], v[116:119], v[164:167]\n\t"
      "v_mfma_f32_16x16x32_bf16 v[168:171], v[180:183], v[120:123], v[168:171]\n\t"
      "s_cbranch_vccnz M7_%=\n\t"
      "v_mfma_f32_16x16x32_bf16 v[172:175], v[180:183], v[124:127], v[172:175]\n\t"
      "M7_%=:\n\t"
      "ds_read_b64_tr_b16 v[20:21], v233 offset:28672\n\t"
      "ds_read_b64_tr_b16 v[22:23], v233 offset:29184\n\t"
      "ds_read_b128 v[180:183], v236 offset:640\n\t"
      "s_waitcnt lgkmcnt(6)\n\t"
      "v_mfma_f32_16x16x32_bf16 v[160:163], v[184:187], v[128:131], v[160:163]\n\t"
      "v_mfma_f32_16x16x32_bf16 v[164:167], v[184:187], v[132:135], v[164:167]\n\t"
      "v_mfma_f32_16x16x32_bf16 v[168:171], v[184:187], v[136:139], v[168:171]\n\t"
      "s_cbranch_vccnz M8_%=\n\t"
      "v_mfma_f32_16x16x32_bf16 v[172:175], v[184:187], v[140:143], v[172:175]\n\t"
      "M8_%=:\n\t"
      "ds_read_b64_tr_b16 v[24:25], v234 offset:28672\n\t"
      "ds_read_b64_tr_b16 v[26:27], v234 offset:29184\n\t"
      "ds_read_b128 v[184:187], v236 offset:704\n\t"
      "s_waitcnt lgkmcnt(6)\n\t"
      "v_mfma_f32_16x16x32_bf16 v[160:163], v[176:179], v[144:147], v[160:163]\n\t"
      "v_mfma_f32_16x16x32_bf16 v[164:167], v[176:179], v[148:151], v[164:167]\n\t"
      "v_mfma_f32_16x16x32_bf16 v[168:171], v[176:179], v[152:155], v[168:171]\n\t"
      "s_cbranch_vccnz M9_%=\n\t"
      "v_mfma_f32_16x16x32_bf16 v[172:175], v[176:179], v[156:159], v[172:175]\n\t"
      "M9_%=:\n\t"
      "ds_read_b64_tr_b16 v[28:29], v235 offset:28672\n\t"
      "ds_read_b64_tr_b16 v[30:31], v235 offset:29184\n\t"
      "ds_read_b128 v[176:179], v236 offset:768\n\t"
      "s_waitcnt lgkmcnt(6)\n\t"
      "v_mfma_f32_16x16x32_bf16 v[160:163], v[180:183], v[188:191], v[160:163]\n\t"
      "v_mfma_f32_16x16x32_bf16 v[164:167], v[180:183], v[192:195], v[164:167]\n\t"
      "v_mfma_f32_16x16x32_bf16 v[168:171], v[180:183], v[196:199], v[168:171]\n\t"
      "s_cbranch_vccnz M10_%=\n\t"
      "v_mfma_f32_16x16x32_bf16 v[172:175], v[180:183], v[200:203], v[172:175]\n\t"
      "M10_%=:\n\t"
      "ds_read_b128 v[180:183], v236 offset:832\n\t"
      "s_waitcnt lgkmcnt(4)\n\t"
      "v_mfma_f32_16x16x32_bf16 v[160:163], v[184:187], v[204:207], v[160:163]\n\t"
      "v_mfma_f32_16x16x32_bf16 v[164:167], v[184:187], v[208:211], v[164:167]\n\t"
      "v_mfma_f32_16x16x32_bf16 v[168:171], v[184:187], v[212:215], v[168:171]\n\t"
      "s_cbranch_vccnz M11_%=\n\t"
      "v_mfma_f32_16x16x32_bf16 v[172:175], v[184:187], v[216:219], v[172:175]\n\t"
      "M11_%=:\n\t"
      "s_waitcnt lgkmcnt(1)\n\t"
      "v_mfma_f32_16x16x32_bf16 v[160:163], v[176:179], v[0:3], v[160:163]\n\t"
      "v_mfma_f32_16x16x32_bf16 v[164:167], v[176:179], v[4:7], v[164:167]\n\t"
      "v_mfma_f32_16x16x32_bf16 v[168:171], v[176:179], v[8:11], v[168:171]\n\t"
      "s_cbranch_vccnz M12_%=\n\t"
      "v_mfma_f32_16x16x32_bf16 v[172:175], v[176:179], v[12:15], v[172:175]\n\t"
      "M12_%=:\n\t"
      "s_waitcnt lgkmcnt(0)\n\t"
      "v_mfma_f32_16x16x32_bf16 v[160:163], v[180:183], v[16:19], v[160:163]\n\t"
      "v_mfma_f32_16x16x32_bf16 v[164:167], v[180:183], v[20:23], v[164:167]\n\t"
      "v_mfma_f32_16x16x32_bf16 v[168:171], v[180:183], v[24:27], v[168:171]\n\t"
      "s_cbranch_vccnz M13_%=\n\t"
      "v_mfma_f32_16x16x32_bf16 v[172:175], v[180:183], v[28:31], v[172:175]\n\t"
      "M13_%=:\n\t"
      "s_nop 5\n\t"
      "v_pk_add_f32 v[188:189], v[160:161], v[164:165]\n\t"
      "v_pk_mul_f32 v[192:193], v[160:161], v[160:161]\n\t"
      "v_pk_add_f32 v[190:191], v[162:163], v[166:167]\n\t"
      "v_pk_mul_f32 v[194:195], v[162:163], v[162:163]\n\t"
      "v_pk_fma_f32 v[192:193], v[164:165], v[164:165], v[192:193]\n\t"
      "v_pk_fma_f32 v[194:195], v[166:167], v[166:167], v[194:195]\n\t"
      "v_pk_add_f32 v[188:189], v[188:189], v[168:169]\n\t"
      "v_pk_fma_f32 v[192:193], v[168:169], v[168:169], v[192:193]\n\t"
      "v_pk_add_f32 v[190:191], v[190:191], v[170:171]\n\t"
      "v_pk_fma_f32 v[194:195], v[170:171], v[170:171], v[194:195]\n\t"
      "s_cbranch_vccnz SB1_%=\n\t"
      "s_nop 0\n\t"
      "v_pk_add_f32 v[188:189], v[188:189], v[172:173]\n\t"
      "v_pk_fma_f32 v[192:193], v[172:173], v[172:173], v[192:193]\n\t"
      "v_pk_add_f32 v[190:191], v[190:191], v[174:175]\n\t"
      "v_pk_fma_f32 v[194:195], v[174:175], v[174:175], v[194:195]\n\t"
      "SB1_%=:\n\t"
      "v_add_f32_dpp v196, v188, v188 row_ror:8 row_mask:0xf bank_mask:0x3\n\t"
      "v_add_f32_dpp v197, v189, v189 row_ror:8 row_mask:0xf bank_mask:0x3\n\t"
      "v_add_f32_dpp v198, v192, v192 row_ror:8 row_mask:0xf bank_mask:0x3\n\t"
      "v_add_f32_dpp v199, v193, v193 row_ror:8 row_mask:0xf bank_mask:0x3\n\t"
      "v_add_f32_dpp v196, v190, v190 row_ror:8 row_mask:0xf bank_mask:0xc\n\t"
      "v_add_f32_dpp v197, v191, v191 row_ror:8 row_mask:0xf bank_mask:0xc\n\t"
      "v_add_f32_dpp v198, v194, v194 row_ror:8 row_mask:0xf bank_mask:0xc\n\t"
      "v_add_f32_dpp v199, v195, v195 row_ror:8 row_mask:0xf bank_mask:0xc\n\t"
      "v_add_f32_dpp v188, v196, v196 row_shl:4 row_mask:0xf bank_mask:0x5\n\t"
      "v_add_f32_dpp v189, v198, v198 row_shl:4 row_mask:0xf bank_mask:0x5\n\t"
      "s_nop 0\n\t"
      "v_add_f32_dpp v188, v197, v197 row_shr:4 row_mask:0xf bank_mask:0xa\n\t"
      "v_add_f32_dpp v189, v199, v199 row_shr:4 row_mask:0xf bank_mask:0xa\n\t"
      "s_nop 0\n\t"
      "v_add_f32_dpp v188, v188, v188 quad_perm:[1,0,3,2] row_mask:0xf bank_mask:0xf\n\t"
      "v_add_f32_dpp v189, v189, v189 quad_perm:[1,0,3,2] row_mask:0xf bank_mask:0xf\n\t"
      "s_nop 0\n\t"
      "v_add_f32_dpp v188, v188, v188 quad_perm:[2,3,0,1] row_mask:0xf bank_mask:0xf\n\t"
      "v_add_f32_dpp v189, v189, v189 quad_perm:[2,3,0,1] row_mask:0xf bank_mask:0xf\n\t"
      "ds_write_b64 v226, v[188:189]\n\t"
      "s_bitcmp1_b32 s16, 4\n\t"
      "s_cbranch_scc0 HI_%=\n\t"
      "s_mov_b64 exec, 255\n\t"
      "global_store_dwordx2 v245, v[220:221], s[6:7] sc1\n\t"
      "s_mov_b64 exec, -1\n\t"
      "HI_%=:\n\t"
      "s_waitcnt vmcnt(1)\n\t"
      "s_waitcnt lgkmcnt(0)\n\t"
      "s_barrier\n\t"
      "ds_read_b128 v[188:191], v227\n\t"
      "s_waitcnt lgkmcnt(0)\n\t"
      "v_pk_add_f32 v[188:189], v[188:189], v[190:191]\n\t"
      "s_nop 1\n\t"
      "v_add_f32_dpp v188, v188, v188 row_ror:4 row_mask:0xf bank_mask:0xf\n\t"
      "v_add_f32_dpp v189, v189, v189 row_ror:4 row_mask:0xf bank_mask:0xf\n\t"
      "s_nop 0\n\t"
      "v_add_f32_dpp v188, v188, v188 row_ror:8 row_mask:0xf bank_mask:0xf\n\t"
      "v_add_f32_dpp v189, v189, v189 row_ror:8 row_mask:0xf bank_mask:0xf\n\t"
      "v_mul_f32_e32 v188, 0x3b124925, v188\n\t"
      "v_mul_f32_e32 v189, 0x3b124925, v189\n\t"
      "v_fma_f32 v189, -v188, v188, v189\n\t"
      "v_max_f32_e64 v189, 0, v189\n\t"
      "v_add_f32_e32 v190, 0x3727c5ac, v189\n\t"
      "v_fmac_f32_e32 v189, 0x3727c5ac, v190\n\t"
      "v_rsq_f32_e64 v191, v189\n\t"
      "s_nop 0\n\t"
      "v_mul_f32_e64 v190, -v188, v191\n\t"
      "v_mov_b32_dpp v192, v191 quad_perm:[0,0,0,0] row_mask:0xf bank_mask:0xf\n\t"
      "s_nop 0\n\t"
      "v_mov_b32_dpp v196, v190 quad_perm:[0,0,0,0] row_mask:0xf bank_mask:0xf\n\t"
      "v_mov_b32_dpp v193, v191 quad_perm:[1,1,1,1] row_mask:0xf bank_mask:0xf\n\t"
      "v_mov_b32_dpp v197, v190 quad_perm:[1,1,1,1] row_mask:0xf bank_mask:0xf\n\t"
      "v_mov_b32_dpp v194, v191 quad_perm:[2,2,2,2] row_mask:0xf bank_mask:0xf\n\t"
      "v_mov_b32_dpp v198, v190 quad_perm:[2,2,2,2] row_mask:0xf bank_mask:0xf\n\t"
      "v_mov_b32_dpp v195, v191 quad_perm:[3,3,3,3] row_mask:0xf bank_mask:0xf\n\t"
      "v_mov_b32_dpp v199, v190 quad_perm:[3,3,3,3] row_mask:0xf bank_mask:0xf\n\t"
      "v_pk_fma_f32 v[160:161], v[160:161], v[192:193], v[196:197]\n\t"
      "v_pk_fma_f32 v[162:163], v[162:163], v[194:195], v[198:199]\n\t"
      "v_pk_fma_f32 v[164:165], v[164:165], v[192:193], v[196:197]\n\t"
      "v_pk_fma_f32 v[166:167], v[166:167], v[194:195], v[198:199]\n\t"
      "v_cvt_pk_bf16_f32 v212, v160, v161\n\t"
      "v_cvt_pk_bf16_f32 v213, v162, v163\n\t"
      "v_cvt_pk_bf16_f32 v214, v164, v165\n\t"
      "v_cvt_pk_bf16_f32 v215, v166, v167\n\t"
      "v_min_u32_e32 v212, -2, v212\n\t"
      "v_min_u32_e32 v213, -2, v213\n\t"
      "v_min_u32_e64 v214, -2, v214\n\t"
      "v_min_u32_e32 v215, -2, v215\n\t"
      "s_nop 0\n\t"
      "v_permlane16_swap_b32_e32 v212, v214\n\t"
      "v_permlane16_swap_b32_e32 v213, v215\n\t"
      "global_store_dwordx4 v231, v[212:215], s[6:7] sc1\n\t"
      "v_pk_fma_f32 v[168:169], v[168:169], v[192:193], v[196:197]\n\t"
      "v_pk_fma_f32 v[170:171], v[170:171], v[194:195], v[198:199]\n\t"
      "v_pk_fma_f32 v[172:173], v[172:173], v[192:193], v[196:197]\n\t"
      "v_pk_fma_f32 v[174:175], v[174:175], v[194:195], v[198:199]\n\t"
      "v_cvt_pk_bf16_f32 v216, v168, v169\n\t"
      "v_cvt_pk_bf16_f32 v217, v170, v171\n\t"
      "v_cvt_pk_bf16_f32 v218, v172, v173\n\t"
      "v_cvt_pk_bf16_f32 v219, v174, v175\n\t"
      "v_min_u32_e32 v216, -2, v216\n\t"
      "v_min_u32_e32 v217, -2, v217\n\t"
      "v_min_u32_e32 v218, -2, v218\n\t"
      "v_min_u32_e32 v219, -2, v219\n\t"
      "s_nop 0\n\t"
      "v_permlane16_swap_b32_e32 v216, v218\n\t"
      "v_permlane16_swap_b32_e32 v217, v219\n\t"
      "s_mov_b64 exec, s[42:43]\n\t"
      "global_store_dwordx4 v238, v[216:219], s[6:7] sc1\n\t"
      "s_mov_b64 exec, -1\n\t"
      "s_nop 1\n\t"
      "v_mov_b32_e32 v240, v188\n\t"
      "v_mul_f32_e32 v241, v189, v191\n\t"
      "v_add_u32_e32 v246, 65536, v228\n\t"
      "v_xor_b32_e32 v242, s8, v188\n\t"
      "v_xor_b32_e32 v243, s9, v241\n\t"
      "s_nop 1\n\t"
      "s_branch BFX_%=\n\t"
      "PWR_%=:\n\t"
      "s_mov_b32 s51, 0\n\t"
      "PWL_%=:\n\t"
      "s_add_u32 s51, s51, 1\n\t"
      "s_cmp_lt_u32 s51, 262144\n\t"
      "s_cbranch_scc0 PWX_%=\n\t"
      "s_sleep 1\n\t"
      "v_min_u32_e32 v184, 32, v230\n\t"
      "v_add_u32_e32 v184, s28, v184\n\t"
      "global_load_dwordx4 v[180:183], v184, s[6:7] sc1\n\t"
      "s_waitcnt vmcnt(0)\n\t"
      "v_xor_b32_e32 v182, v182, v180\n\t"
      "v_xor_b32_e32 v183, v183, v181\n\t"
      "v_cmp_ne_u64_e32 vcc, s[8:9], v[182:183]\n\t"
      "s_cbranch_vccnz PWL_%=\n\t"
      "PWX_%=:\n\t"
      "v_and_b32_e32 v180, 0x2000, v180\n\t"
      "s_nop 1\n\t"
      "s_branch PWB_%=\n\t"
      "BFX_%=:\n\t"
      : "+{s[4:11]}"(s1), "+{s[12:15]}"(s3), "+{s16}"(bits), "+{s[20:23]}"(s), "+{s[28:29]}"(par), "+{v[160:175]}"(acc), "+{v[224:231]}"(v1), "+{v[232:239]}"(v), "+{v[240:243]}"(vo), "+{v[244:247]}"(v3)
      :
      : "memory", "vcc", "scc",
        "v0", "v1", "v2", "v3", "v4", "v5", "v6", "v7", "v8", "v9", "v10", "v11", "v12", "v13", "v14", "v15", "v16",
        "v17", "v18", "v19", "v20", "v21", "v22", "v23", "v24", "v25", "v26", "v27", "v28", "v29", "v30", "v31", "v32",
        "v33", "v34", "v35", "v36", "v37", "v38", "v39", "v40", "v41", "v42", "v43", "v44", "v45", "v46", "v47", "v48",
        "v49", "v50", "v51", "v52", "v53", "v54", "v55", "v56", "v57", "v58", "v59", "v60", "v61", "v62", "v63", "v64",
        "v65", "v66", "v67", "v68", "v69", "v70", "v71", "v72", "v73", "v74", "v75", "v76", "v77", "v78", "v79", "v80",
        "v81", "v82", "v83", "v84", "v85", "v86", "v87", "v88", "v89", "v90", "v91", "v92", "v93", "v94", "v95", "v96",
        "v97", "v98", "v99", "v100", "v101", "v102", "v103", "v104", "v105", "v106", "v107", "v108", "v109", "v110",
        "v111", "v112", "v113", "v114", "v115", "v116", "v117", "v118", "v119", "v120", "v121", "v122", "v123", "v124",
        "v125", "v126", "v127", "v128", "v129", "v130", "v131", "v132", "v133", "v134", "v135", "v136", "v137", "v138",
        "v139", "v140", "v141", "v142", "v143", "v144", "v145", "v146", "v147", "v148", "v149", "v150", "v151", "v152",
        "v153", "v154", "v155", "v156", "v157", "v158", "v159", "v176", "v177", "v178", "v179", "v180", "v181", "v182",
        "v183", "v184", "v185", "v186", "v187", "v188", "v189", "v190", "v191", "v192", "v193", "v194", "v195", "v196",
        "v197", "v198", "v199", "v200", "v201", "v202", "v203", "v204", "v205", "v206", "v207", "v208", "v209", "v210",
        "v211", "v212", "v213", "v214", "v215", "v216", "v217", "v218", "v219", "v220", "v221", "v222", "v223", "v248",
        "v249", "v250", "v251", "s40", "s41", "s42", "s43", "s44", "s45", "s46", "s47", "s48", "s49", "s50", "s51"
    );
    }
    unsigned vq = v3[2];
    PIN();
    LANE_BITS(__builtin_amdgcn_mbcnt_hi(~0u, __builtin_amdgcn_mbcnt_lo(~0u, 0u)));
    u32x8 v, s2;
    u32x16 s;
    v[0] = lane * 16;
    u64 kr = (u64)(6 - rn) | ((u64)rot << 32);
    v[1] = lr * 4096 + ((lc ^ swz_tr(lr)) << 4);
    v[2] = wave * 4096 + g * 1024 + q4 * 128 + (((2 * (wave & 3) + (p4 >> 1)) ^ swz_tr(8 * g + q4)) << 4) + (p4 & 1) * 8;
    v[3] = wave * 4096 + (wave & 3) * 1024 + ((lane * 16) ^ (g << 6));
    v[4] = (wave & 3) * 1024 + (((p4 * 16 + (lane >> 2)) * 16) ^ (p4 << 6));
    v[5] = (lane >> 2) * 896 + p4 * 8;
    v[6] = c * 2;
    const unsigned mh = (unsigned)par, mg = (unsigned)(par >> 32);
    v[7] = ST_H4 + xp * 16384 + ((side * 8192) ^ mg) + ((ib & 1) * 28 + ((7 * wave) >> 1) + p4) * 128;
    put(s, 0, (u64)(w2 + st * 128 + (wave * 224 + rn * 32) * 4096));
    put(s, 2, (u64)(ws + WS_G + (unit(blk0, wave, 0) ^ mg)));
    put(s, 4, (u64)(out + (64 * st + 16 * (wave & 3)) * 896 + dg * 128));
    unsigned od = (16 - dg) * 32;
    put(s, 6, (u64)(b2 + (64 * st + 16 * (wave & 3)) * 2));
    put(s, 8, (u64)(ws + WS_ST));
    s[12] = bits;
    s[13] = wave * G_WV;
    put(s2, 0, (u64)(ws + WS_HN + (unit(blk0, st >> 2, 0) ^ mh) + (2 * (st & 3) + ((wave >> 1) & 1)) * 1024));
    s2[2] = ST_FL + (64 * st + 16 * (wave & 3)) * 16;
    s2[3] = UNI(wave & 1 ? ~0u : 0u);
    put(s2, 4, (u64)(ws + WS_EP));
    asm volatile(
      "s_waitcnt lgkmcnt(0)\n\ts_nop 4\n\t"
      ".p2align 3\n\t"
      "s_lshr_b32 s31, s17, 2\n\t"
      "v_mov_b32_e32 v112, 163600\n\t"
      "ds_read_b128 v[112:115], v112\n\t"
      "v_xor_b32_e32 v248, 64, v241\n\t"
      "v_add_u32_e32 v249, 63488, v241\n\t"
      "v_add_u32_e32 v250, 95232, v248\n\t"
      "v_add_u32_e32 v248, 31744, v248\n\t"
      "v_xor_b32_e64 v251, 32, v242\n\t"
      "v_xor_b32_e32 v252, 64, v242\n\t"
      "v_xor_b32_e32 v253, 96, v242\n\t"
      "s_bitcmp1_b32 s16, 4\n\t"
      "s_cbranch_scc0 GW2_%=\n\t"
      "s_mov_b32 s48, 0xf000f\n\t"
      "s_mov_b32 s49, 0xf000f\n\t"
      "s_mov_b64 exec, s[48:49]\n\t"
      "global_store_dwordx4 v140, v[136:139], s[12:13] sc1\n\t"
      "s_mov_b64 exec, -1\n\t"
      "GW2_%=:\n\t"
      "s_add_u32 m0, s31, 66048\n\t"
      "s_nop 0\n\t"
      "global_load_lds_dwordx4 v241, s[4:5]\n\t"
      "global_load_lds_dwordx4 v248, s[4:5] offset:1024\n\t"
      "global_load_lds_dwordx4 v249, s[4:5] offset:2048\n\t"
      "global_load_lds_dwordx4 v250, s[4:5] offset:3072\n\t"
      "s_sub_u32 s28, s28, 1\n\t"
      "s_cselect_b32 s48, -6, 1\n\t"
      "s_cselect_b32 s49, -1, 0\n\t"
      "s_mul_i32 s48, s48, 131072\n\t"
      "s_add_u32 s4, s4, s48\n\t"
      "s_addc_u32 s5, s5, s49\n\t"
      "global_load_ushort v236, v246, s[10:11]\n\t"
      "s_waitcnt lgkmcnt(0)\n\t"
      "v_readfirstlane_b32 s14, v112\n\t"
      "v_readfirstlane_b32 s15, v113\n\t"
      "v_readfirstlane_b32 s26, v114\n\t"
      "v_readfirstlane_b32 s27, v115\n\t"
      "s_add_u32 s52, s6, 0\n\t"
      "s_addc_u32 s53, s7, 0\n\t"
      "s_add_u32 s54, s6, 131072\n\t"
      "s_addc_u32 s55, s7, 0\n\t"
      "s_add_u32 s56, s6, 262144\n\t"
      "s_addc_u32 s57, s7, 0\n\t"
      "s_add_u32 s58, s6, 393216\n\t"
      "s_addc_u32 s59, s7, 0\n\t"
      "s_add_u32 s60, s6, 524288\n\t"
      "s_addc_u32 s61, s7, 0\n\t"
      "s_add_u32 s62, s6, 655360\n\t"
      "s_addc_u32 s63, s7, 0\n\t"
      "s_add_u32 s64, s6, 786432\n\t"
      "s_addc_u32 s65, s7, 0\n\t"
      "s_add_u32 s48, s29, 0\n\t"
      "s_cmp_ge_u32 s48, 7\n\t"
      "s_cselect_b32 s49, 7, 0\n\t"
      "s_sub_u32 s48, s48, s49\n\t"
      "v_lshl_add_u32 v224, s48, 10, v240\n\t"
      "s_add_u32 s48, s29, 1\n\t"
      "s_cmp_ge_u32 s48, 7\n\t"
      "s_cselect_b32 s49, 7, 0\n\t"
      "s_sub_u32 s48, s48, s49\n\t"
      "v_lshl_add_u32 v225, s48, 10, v240\n\t"
      "s_add_u32 s48, s29, 2\n\t"
      "s_cmp_ge_u32 s48, 7\n\t"
      "s_cselect_b32 s49, 7, 0\n\t"
      "s_sub_u32 s48, s48, s49\n\t"
      "v_lshl_add_u32 v226, s48, 10, v240\n\t"
      "s_add_u32 s48, s29, 3\n\t"
      "s_cmp_ge_u32 s48, 7\n\t"
      "s_cselect_b32 s49, 7, 0\n\t"
      "s_sub_u32 s48, s48, s49\n\t"
      "v_lshl_add_u32 v227, s48, 10, v240\n\t"
      "s_add_u32 s48, s29, 4\n\t"
      "s_cmp_ge_u32 s48, 7\n\t"
      "s_cselect_b32 s49, 7, 0\n\t"
      "s_sub_u32 s48, s48, s49\n\t"
      "v_lshl_add_u32 v228, s48, 10, v240\n\t"
      "s_add_u32 s48, s29, 5\n\t"
      "s_cmp_ge_u32 s48, 7\n\t"
      "s_cselect_b32 s49, 7, 0\n\t"
      "s_sub_u32 s48, s48, s49\n\t"
      "v_lshl_add_u32 v229, s48, 10, v240\n\t"
      "s_add_u32 s48, s29, 6\n\t"
      "s_cmp_ge_u32 s48, 7\n\t"
      "s_cselect_b32 s49, 7, 0\n\t"
      "s_sub_u32 s48, s48, s49\n\t"
      "v_lshl_add_u32 v230, s48, 10, v240\n\t"
      "v_xor_b32_e32 v220, 1024, v243\n\t"
      "v_xor_b32_e32 v221, 2048, v243\n\t"
      "v_xor_b32_e32 v222, 3072, v243\n\t"
      "s_add_u32 s66, s8, s30\n\t"
      "s_addc_u32 s67, s9, 0\n\t"
      "v_lshrrev_b32_e32 v112, 4, v240\n\t"
      "v_and_b32_e32 v113, 15, v112\n\t"
      "v_lshrrev_b32_e32 v112, 4, v112\n\t"
      "s_lshr_b32 s48, s17, 10\n\t"
      "s_and_b32 s48, s48, 16\n\t"
      "v_lshlrev_b32_e32 v112, 3, v112\n\t"
      "v_sub_u32_e32 v112, v113, v112\n\t"
      "v_add_u32_e32 v112, s48, v112\n\t"
      "v_mov_b32_e32 v114, 0xffff\n\t"
      "v_mov_b32_e32 v115, 0xffff0000\n\t"
      "v_cmp_eq_u32_e32 vcc, 0, v112\n\t"
      "s_nop 1\n\t"
      "v_cndmask_b32_e32 v216, 0, v114, vcc\n\t"
      "v_cmp_eq_u32_e32 vcc, 1, v112\n\t"
      "s_nop 1\n\t"
      "v_cndmask_b32_e32 v216, v216, v115, vcc\n\t"
      "v_cmp_eq_u32_e32 vcc, 2, v112\n\t"
      "s_nop 1\n\t"
      "v_cndmask_b32_e32 v217, 0, v114, vcc\n\t"
      "v_cmp_eq_u32_e32 vcc, 3, v112\n\t"
      "s_nop 1\n\t"
      "v_cndmask_b32_e32 v217, v217, v115, vcc\n\t"
      "v_cmp_eq_u32_e32 vcc, 4, v112\n\t"
      "s_nop 1\n\t"
      "v_cndmask_b32_e32 v218, 0, v114, vcc\n\t"
      "v_cmp_eq_u32_e32 vcc, 5, v112\n\t"
      "s_nop 1\n\t"
      "v_cndmask_b32_e32 v218, v218, v115, vcc\n\t"
      "v_cmp_eq_u32_e32 vcc, 6, v112\n\t"
      "s_nop 1\n\t"
      "v_cndmask_b32_e32 v219, 0, v114, vcc\n\t"
      "v_cmp_eq_u32_e32 vcc, 7, v112\n\t"
      "s_nop 1\n\t"
      "v_cndmask_b32_e32 v219, v219, v115, vcc\n\t"
      "v_and_b32_e32 v116, 0xf0, v240\n\t"
      "v_add_u32_e32 v116, s22, v116\n\t"
      "v_mov_b64_e32 v[184:185], 0\n\t"
      "v_mov_b64_e32 v[186:187], 0\n\t"
      "v_mov_b64_e32 v[188:189], 0\n\t"
      "v_mov_b64_e32 v[190:191], 0\n\t"
      "v_mov_b64_e32 v[192:193], 0\n\t"
      "v_mov_b64_e32 v[194:195], 0\n\t"
      "v_mov_b64_e32 v[196:197], 0\n\t"
      "v_mov_b64_e32 v[198:199], 0\n\t"
      "v_mov_b64_e32 v[200:201], 0\n\t"
      "v_mov_b64_e32 v[202:203], 0\n\t"
      "v_mov_b64_e32 v[204:205], 0\n\t"
      "v_mov_b64_e32 v[206:207], 0\n\t"
      "v_mov_b64_e32 v[208:209], 0\n\t"
      "v_mov_b64_e32 v[210:211], 0\n\t"
      "s_waitcnt vmcnt(5)\n\t"
      "v_add_u32_e32 v242, 66048, v242\n\t"
      "v_add_u32_e32 v251, 66048, v251\n\t"
      "v_add_u32_e32 v252, 66048, v252\n\t"
      "v_add_u32_e32 v253, 66048, v253\n\t"
      "ds_read_b64_tr_b16 v[0:1], v242 offset:64512\n\t"
      "ds_read_b64_tr_b16 v[2:3], v242 offset:65024\n\t"
      "ds_read_b64_tr_b16 v[4:5], v251 offset:64512\n\t"
      "ds_read_b64_tr_b16 v[6:7], v251 offset:65024\n\t"
      "ds_read_b64_tr_b16 v[8:9], v252 offset:64512\n\t"
      "ds_read_b64_tr_b16 v[10:11], v252 offset:65024\n\t"
      "ds_read_b64_tr_b16 v[12:13], v253 offset:64512\n\t"
      "ds_read_b64_tr_b16 v[14:15], v253 offset:65024\n\t"
      "s_waitcnt vmcnt(1)\n\t"
      "s_add_u32 m0, s31, 0\n\t"
      "s_nop 0\n\t"
      "global_load_lds_dwordx4 v241, s[4:5]\n\t"
      "global_load_lds_dwordx4 v248, s[4:5] offset:1024\n\t"
      "global_load_lds_dwordx4 v249, s[4:5] offset:2048\n\t"
      "global_load_lds_dwordx4 v250, s[4:5] offset:3072\n\t"
      "s_sub_u32 s28, s28, 1\n\t"
      "s_cselect_b32 s48, -6, 1\n\t"
      "s_cselect_b32 s49, -1, 0\n\t"
      "s_mul_i32 s48, s48, 131072\n\t"
      "s_add_u32 s4, s4, s48\n\t"
      "s_addc_u32 s5, s5, s49\n\t"
      "ds_read_b64_tr_b16 v[16:17], v242\n\t"
      "ds_read_b64_tr_b16 v[18:19], v242 offset:512\n\t"
      "ds_read_b64_tr_b16 v[20:21], v251\n\t"
      "ds_read_b64_tr_b16 v[22:23], v251 offset:512\n\t"
      "ds_read_b64_tr_b16 v[24:25], v252\n\t"
      "ds_read_b64_tr_b16 v[26:27], v252 offset:512\n\t"
      "ds_read_b64_tr_b16 v[28:29], v253\n\t"
      "ds_read_b64_tr_b16 v[30:31], v253 offset:512\n\t"
      "s_waitcnt vmcnt(0)\n\t"
      "s_add_u32 m0, s31, 32768\n\t"
      "s_nop 0\n\t"
      "global_load_lds_dwordx4 v241, s[4:5]\n\t"
      "global_load_lds_dwordx4 v248, s[4:5] offset:1024\n\t"
      "global_load_lds_dwordx4 v249, s[4:5] offset:2048\n\t"
      "global_load_lds_dwordx4 v250, s[4:5] offset:3072\n\t"
      "s_sub_u32 s28, s28, 1\n\t"
      "s_cselect_b32 s48, -6, 1\n\t"
      "s_cselect_b32 s49, -1, 0\n\t"
      "s_mul_i32 s48, s48, 131072\n\t"
      "s_add_u32 s4, s4, s48\n\t"
      "s_addc_u32 s5, s5, s49\n\t"
      "s_nop 0\n\t"
      "v_add_u32_e32 v242, 4294901248, v242\n\t"
      "v_add_u32_e32 v251, 4294901248, v251\n\t"
      "v_add_u32_e32 v252, 4294901248, v252\n\t"
      "v_add_u32_e32 v253, 4294901248, v253\n\t"
      "ds_read_b64_tr_b16 v[32:33], v242\n\t"
      "ds_read_b64_tr_b16 v[34:35], v242 offset:512\n\t"
      "ds_read_b64_tr_b16 v[36:37], v251\n\t"
      "ds_read_b64_tr_b16 v[38:39], v251 offset:512\n\t"
      "ds_read_b64_tr_b16 v[40:41], v252\n\t"
      "ds_read_b64_tr_b16 v[42:43], v252 offset:512\n\t"
      "ds_read_b64_tr_b16 v[44:45], v253\n\t"
      "ds_read_b64_tr_b16 v[46:47], v253 offset:512\n\t"
      "s_waitcnt vmcnt(0)\n\t"
      "s_waitcnt lgkmcnt(14)\n\t"
      "s_add_u32 m0, s31, 130560\n\t"
      "s_nop 0\n\t"
      "global_load_lds_dwordx4 v241, s[4:5]\n\t"
      "global_load_lds_dwordx4 v248, s[4:5] offset:1024\n\t"
      "global_load_lds_dwordx4 v249, s[4:5] offset:2048\n\t"
      "global_load_lds_dwordx4 v250, s[4:5] offset:3072\n\t"
      "s_sub_u32 s28, s28, 1\n\t"
      "s_cselect_b32 s48, -6, 1\n\t"
      "s_cselect_b32 s49, -1, 0\n\t"
      "s_mul_i32 s48, s48, 131072\n\t"
      "s_add_u32 s4, s4, s48\n\t"
      "s_addc_u32 s5, s5, s49\n\t"
      "ds_read_b64_tr_b16 v[48:49], v242 offset:32768\n\t"
      "ds_read_b64_tr_b16 v[50:51], v242 offset:33280\n\t"
      "ds_read_b64_tr_b16 v[52:53], v251 offset:32768\n\t"
      "ds_read_b64_tr_b16 v[54:55], v251 offset:33280\n\t"
      "ds_read_b64_tr_b16 v[56:57], v252 offset:32768\n\t"
      "ds_read_b64_tr_b16 v[58:59], v252 offset:33280\n\t"
      "ds_read_b64_tr_b16 v[60:61], v253 offset:32768\n\t"
      "ds_read_b64_tr_b16 v[62:63], v253 offset:33280\n\t"
      "s_bitcmp1_b32 s16, 1\n\t"
      "s_cbranch_scc0 HWQ_%=\n\t"
      "global_load_dwordx4 v[112:115], v116, s[12:13] sc1\n\t"
      "HWQ_%=:\n\t"
      "s_waitcnt vmcnt(0)\n\t"
      "s_waitcnt lgkmcnt(14)\n\t"
      "s_add_u32 m0, s31, 66048\n\t"
      "s_nop 0\n\t"
      "global_load_lds_dwordx4 v241, s[4:5]\n\t"
      "global_load_lds_dwordx4 v248, s[4:5] offset:1024\n\t"
      "global_load_lds_dwordx4 v249, s[4:5] offset:2048\n\t"
      "global_load_lds_dwordx4 v250, s[4:5] offset:3072\n\t"
      "s_sub_u32 s28, s28, 1\n\t"
      "s_cselect_b32 s48, -6, 1\n\t"
      "s_cselect_b32 s49, -1, 0\n\t"
      "s_mul_i32 s48, s48, 131072\n\t"
      "s_add_u32 s4, s4, s48\n\t"
      "s_addc_u32 s5, s5, s49\n\t"
      "v_add_u32_e32 v242, 66048, v242\n\t"
      "v_add_u32_e32 v251, 66048, v251\n\t"
      "v_add_u32_e32 v252, 66048, v252\n\t"
      "v_add_u32_e32 v253, 66048, v253\n\t"
      "ds_read_b64_tr_b16 v[64:65], v242 offset:64512\n\t"
      "ds_read_b64_tr_b16 v[66:67], v242 offset:65024\n\t"
      "ds_read_b64_tr_b16 v[68:69], v251 offset:64512\n\t"
      "ds_read_b64_tr_b16 v[70:71], v251 offset:65024\n\t"
      "ds_read_b64_tr_b16 v[72:73], v252 offset:64512\n\t"
      "ds_read_b64_tr_b16 v[74:75], v252 offset:65024\n\t"
      "ds_read_b64_tr_b16 v[76:77], v253 offset:64512\n\t"
      "ds_read_b64_tr_b16 v[78:79], v253 offset:65024\n\t"
      "s_waitcnt vmcnt(0)\n\t"
      "s_waitcnt lgkmcnt(14)\n\t"
      "s_add_u32 m0, s31, 0\n\t"
      "s_nop 0\n\t"
      "global_load_lds_dwordx4 v241, s[4:5]\n\t"
      "global_load_lds_dwordx4 v248, s[4:5] offset:1024\n\t"
      "global_load_lds_dwordx4 v249, s[4:5] offset:2048\n\t"
      "global_load_lds_dwordx4 v250, s[4:5] offset:3072\n\t"
      "ds_read_b64_tr_b16 v[80:81], v242\n\t"
      "ds_read_b64_tr_b16 v[82:83], v242 offset:512\n\t"
      "ds_read_b64_tr_b16 v[84:85], v251\n\t"
      "ds_read_b64_tr_b16 v[86:87], v251 offset:512\n\t"
      "ds_read_b64_tr_b16 v[88:89], v252\n\t"
      "ds_read_b64_tr_b16 v[90:91], v252 offset:512\n\t"
      "ds_read_b64_tr_b16 v[92:93], v253\n\t"
      "ds_read_b64_tr_b16 v[94:95], v253 offset:512\n\t"
      "s_bitcmp1_b32 s16, 1\n\t"
      "s_cbranch_scc0 HWH_%=\n\t"
      "v_xor_b32_e32 v114, v114, v112\n\t"
      "v_xor_b32_e32 v115, v115, v113\n\t"
      "v_cmp_ne_u64_e32 vcc, s[14:15], v[114:115]\n\t"
      "s_cbranch_vccnz HWR_%=\n\t"
      "HWB_%=:\n\t"
      "s_mov_b32 exec_hi, s23\n\t"
      "s_not_b32 exec_lo, s23\n\t"
      "s_add_u32 s68, s20, 0\n\t"
      "s_addc_u32 s69, s21, 0\n\t"
      "global_load_dwordx4 v[184:187], v240, s[68:69] sc1\n\t"
      "s_add_u32 s68, s20, 131072\n\t"
      "s_addc_u32 s69, s21, 0\n\t"
      "global_load_dwordx4 v[188:191], v240, s[68:69] sc1\n\t"
      "s_add_u32 s68, s20, 262144\n\t"
      "s_addc_u32 s69, s21, 0\n\t"
      "global_load_dwordx4 v[192:195], v240, s[68:69] sc1\n\t"
      "s_add_u32 s68, s20, 393216\n\t"
      "s_addc_u32 s69, s21, 0\n\t"
      "global_load_dwordx4 v[196:199], v240, s[68:69] sc1\n\t"
      "s_add_u32 s68, s20, 524288\n\t"
      "s_addc_u32 s69, s21, 0\n\t"
      "global_load_dwordx4 v[200:203], v240, s[68:69] sc1\n\t"
      "s_add_u32 s68, s20, 655360\n\t"
      "s_addc_u32 s69, s21, 0\n\t"
      "global_load_dwordx4 v[204:207], v240, s[68:69] sc1\n\t"
      "s_add_u32 s68, s20, 786432\n\t"
      "s_addc_u32 s69, s21, 0\n\t"
      "global_load_dwordx4 v[208:211], v240, s[68:69] sc1\n\t"
      "s_mov_b64 exec, -1\n\t"
      "v_lshlrev_b32_e32 v236, 16, v236\n\t"
      "v_add_f32_e32 v236, v236, v112\n\t"
      "v_cvt_pk_bf16_f32 v114, v113, v113\n\t"
      "v_lshlrev_b32_e32 v115, 16, v114\n\t"
      "v_sub_f32_e32 v115, v113, v115\n\t"
      "v_cvt_pk_bf16_f32 v115, v115, v115\n\t"
      "v_mov_b32_e32 v237, v236\n\t"
      "v_mov_b32_e32 v238, v236\n\t"
      "v_mov_b32_e32 v239, v236\n\t"
      "v_and_b32_e32 v120, v216, v115\n\t"
      "v_and_b32_e32 v216, v216, v114\n\t"
      "v_and_b32_e32 v121, v217, v115\n\t"
      "v_and_b32_e32 v217, v217, v114\n\t"
      "v_and_b32_e32 v122, v218, v115\n\t"
      "v_and_b32_e32 v218, v218, v114\n\t"
      "v_and_b32_e32 v123, v219, v115\n\t"
      "v_and_b32_e32 v219, v219, v114\n\t"
      "HWH_%=:\n\t"
      "s_waitcnt vmcnt(0)\n\t"
      "v_add_u32_e32 v242, 4294901248, v242\n\t"
      "v_add_u32_e32 v251, 4294901248, v251\n\t"
      "v_add_u32_e32 v252, 4294901248, v252\n\t"
      "v_add_u32_e32 v253, 4294901248, v253\n\t"
      "ds_read_b64_tr_b16 v[96:97], v242\n\t"
      "ds_read_b64_tr_b16 v[98:99], v242 offset:512\n\t"
      "ds_read_b64_tr_b16 v[100:101], v251\n\t"
      "ds_read_b64_tr_b16 v[102:103], v251 offset:512\n\t"
      "ds_read_b64_tr_b16 v[104:105], v252\n\t"
      "ds_read_b64_tr_b16 v[106:107], v252 offset:512\n\t"
      "ds_read_b64_tr_b16 v[108:109], v253\n\t"
      "ds_read_b64_tr_b16 v[110:111], v253 offset:512\n\t"
      "s_mov_b64 exec, 15\n\t"
      "v_mov_b64_e32 v[232:233], 0\n\t"
      "global_atomic_or_x2 v[232:233], v247, v[232:233], s[12:13] sc0 sc1\n\t"
      "s_mov_b64 exec, -1\n\t"
      "s_waitcnt lgkmcnt(0)\n\t"
      "s_barrier\n\t"
      "s_mov_b64 exec, 15\n\t"
      "s_waitcnt vmcnt(0)\n\t"
      "v_subrev_u32_e32 v232, s14, v232\n\t"
      "v_cmp_ne_u32_e64 s[48:49], s15, v233\n\t"
      "v_subrev_u32_e32 v232, 0, v232\n\t"
      "v_cmp_lt_u32_e32 vcc, 7, v232\n\t"
      "s_mov_b64 exec, -1\n\t"
      "s_or_b64 vcc, vcc, s[48:49]\n\t"
      "s_cbranch_vccz PRY_%=\n\t"
      "s_mov_b32 s51, 0\n\t"
      "s_branch PRE_%=\n\t"
      "PRL_%=:\n\t"
      "s_sleep 4\n\t"
      "s_add_u32 s51, s51, 1\n\t"
      "s_cmp_lt_u32 s51, 262144\n\t"
      "s_cbranch_scc0 PRX_%=\n\t"
      "PRE_%=:\n\t"
      "s_mov_b64 exec, 15\n\t"
      "v_mov_b64_e32 v[232:233], 0\n\t"
      "global_atomic_or_x2 v[232:233], v247, v[232:233], s[12:13] sc0 sc1\n\t"
      "s_waitcnt vmcnt(0)\n\t"
      "v_subrev_u32_e32 v232, s14, v232\n\t"
      "v_cmp_ne_u32_e64 s[48:49], s15, v233\n\t"
      "v_subrev_u32_e32 v232, 0, v232\n\t"
      "v_cmp_lt_u32_e32 vcc, 7, v232\n\t"
      "s_mov_b64 exec, -1\n\t"
      "s_or_b64 vcc, vcc, s[48:49]\n\t"
      "s_cbranch_vccnz PRL_%=\n\t"
      "PRX_%=:\n\t"
      "PRY_%=:\n\t"
      "global_load_dwordx4 v[128:131], v224, s[52:53] sc1\n\t"
      "global_load_dwordx4 v[132:135], v225, s[52:53] sc1\n\t"
      "global_load_dwordx4 v[136:139], v226, s[52:53] sc1\n\t"
      "global_load_dwordx4 v[140:143], v227, s[52:53] sc1\n\t"
      "global_load_dwordx4 v[144:147], v228, s[52:53] sc1\n\t"
      "global_load_dwordx4 v[148:151], v229, s[52:53] sc1\n\t"
      "global_load_dwordx4 v[152:155], v230, s[52:53] sc1\n\t"
      "s_bitcmp1_b32 s16, 1\n\t"
      "s_cbranch_scc0 HWE_%=\n\t"
      "v_max3_u32 v234, v184, v185, v186\n\t"
      "v_max3_u32 v234, v234, v187, v188\n\t"
      "v_max3_u32 v234, v234, v189, v190\n\t"
      "v_max3_u32 v234, v234, v191, v192\n\t"
      "v_max3_u32 v234, v234, v193, v194\n\t"
      "v_max3_u32 v234, v234, v195, v196\n\t"
      "v_max3_u32 v234, v234, v197, v198\n\t"
      "v_max3_u32 v234, v234, v199, v200\n\t"
      "v_max3_u32 v234, v234, v201, v202\n\t"
      "v_max3_u32 v234, v234, v203, v204\n\t"
      "v_max3_u32 v234, v234, v205, v206\n\t"
      "v_max3_u32 v234, v234, v207, v208\n\t"
      "v_max3_u32 v234, v234, v209, v210\n\t"
      "v_max_u32_e32 v234, v234, v211\n\t"
      "v_cmp_eq_u32_e32 vcc, -1, v234\n\t"
      "s_cbranch_vccnz HXR_%=\n\t"
      "HXB_%=:\n\t"
      "v_mfma_f32_16x16x32_bf16 v[156:159], v[184:187], v[120:123], v[236:239]\n\t"
      "v_mfma_f32_16x16x32_bf16 v[160:163], v[188:191], v[120:123], v[236:239]\n\t"
      "v_mfma_f32_16x16x32_bf16 v[164:167], v[192:195], v[120:123], v[236:239]\n\t"
      "v_mfma_f32_16x16x32_bf16 v[168:171], v[196:199], v[120:123], v[236:239]\n\t"
      "v_mfma_f32_16x16x32_bf16 v[172:175], v[200:203], v[120:123], v[236:239]\n\t"
      "v_mfma_f32_16x16x32_bf16 v[176:179], v[204:207], v[120:123], v[236:239]\n\t"
      "v_mfma_f32_16x16x32_bf16 v[180:183], v[208:211], v[120:123], v[236:239]\n\t"
      "v_mfma_f32_16x16x32_bf16 v[156:159], v[184:187], v[216:219], v[156:159]\n\t"
      "v_mfma_f32_16x16x32_bf16 v[160:163], v[188:191], v[216:219], v[160:163]\n\t"
      "v_mfma_f32_16x16x32_bf16 v[164:167], v[192:195], v[216:219], v[164:167]\n\t"
      "v_mfma_f32_16x16x32_bf16 v[168:171], v[196:199], v[216:219], v[168:171]\n\t"
      "v_mfma_f32_16x16x32_bf16 v[172:175], v[200:203], v[216:219], v[172:175]\n\t"
      "v_mfma_f32_16x16x32_bf16 v[176:179], v[204:207], v[216:219], v[176:179]\n\t"
      "v_mfma_f32_16x16x32_bf16 v[180:183], v[208:211], v[216:219], v[180:183]\n\t"
      "HWE_%=:\n\t"
      "s_nop 7\n\t"
      "s_cmp_ge_u32 s17, 65536\n\t"
      "s_cbranch_scc1 GQ_%=\n\t"
      ".p2align 6\n\t"
      "s_setprio 1\n\t"
      "s_waitcnt vmcnt(6)\n\t"
      "v_mfma_f32_16x16x32_bf16 v[184:187], v[128:131], v[0:3], v[156:159]\n\t"
      "v_mfma_f32_16x16x32_bf16 v[188:191], v[128:131], v[4:7], 0\n\t"
      "v_mfma_f32_16x16x32_bf16 v[192:195], v[128:131], v[8:11], 0\n\t"
      "v_mfma_f32_16x16x32_bf16 v[196:199], v[128:131], v[12:15], 0\n\t"
      "global_load_dwordx4 v[128:131], v224, s[54:55] sc1\n\t"
      "s_waitcnt vmcnt(6)\n\t"
      "v_mfma_f32_16x16x32_bf16 v[184:187], v[132:135], v[16:19], v[184:187]\n\t"
      "s_waitcnt vmcnt(5)\n\t"
      "v_mfma_f32_16x16x32_bf16 v[184:187], v[136:139], v[32:35], v[184:187]\n\t"
      "v_mfma_f32_16x16x32_bf16 v[188:191], v[132:135], v[20:23], v[188:191]\n\t"
      "v_mfma_f32_16x16x32_bf16 v[188:191], v[136:139], v[36:39], v[188:191]\n\t"
      "v_mfma_f32_16x16x32_bf16 v[192:195], v[132:135], v[24:27], v[192:195]\n\t"
      "v_mfma_f32_16x16x32_bf16 v[192:195], v[136:139], v[40:43], v[192:195]\n\t"
      "v_mfma_f32_16x16x32_bf16 v[196:199], v[132:135], v[28:31], v[196:199]\n\t"
      "global_load_dwordx4 v[132:135], v225, s[54:55] sc1\n\t"
      "v_mfma_f32_16x16x32_bf16 v[196:199], v[136:139], v[44:47], v[196:199]\n\t"
      "global_load_dwordx4 v[136:139], v226, s[54:55] sc1\n\t"
      "s_waitcnt vmcnt(6)\n\t"
      "v_mfma_f32_16x16x32_bf16 v[184:187], v[140:143], v[48:51], v[184:187]\n\t"
      "s_waitcnt vmcnt(5)\n\t"
      "v_mfma_f32_16x16x32_bf16 v[184:187], v[144:147], v[64:67], v[184:187]\n\t"
      "v_mfma_f32_16x16x32_bf16 v[188:191], v[140:143], v[52:55], v[188:191]\n\t"
      "v_mfma_f32_16x16x32_bf16 v[188:191], v[144:147], v[68:71], v[188:191]\n\t"
      "v_mfma_f32_16x16x32_bf16 v[192:195], v[140:143], v[56:59], v[192:195]\n\t"
      "v_mfma_f32_16x16x32_bf16 v[192:195], v[144:147], v[72:75], v[192:195]\n\t"
      "v_mfma_f32_16x16x32_bf16 v[196:199], v[140:143], v[60:63], v[196:199]\n\t"
      "global_load_dwordx4 v[140:143], v227, s[54:55] sc1\n\t"
      "v_mfma_f32_16x16x32_bf16 v[196:199], v[144:147], v[76:79], v[196:199]\n\t"
      "global_load_dwordx4 v[144:147], v228, s[54:55] sc1\n\t"
      "s_waitcnt vmcnt(6)\n\t"
      "v_mfma_f32_16x16x32_bf16 v[184:187], v[148:151], v[80:83], v[184:187]\n\t"
      "s_waitcnt vmcnt(5)\n\t"
      "v_mfma_f32_16x16x32_bf16 v[184:187], v[152:155], v[96:99], v[184:187]\n\t"
      "v_mfma_f32_16x16x32_bf16 v[188:191], v[148:151], v[84:87], v[188:191]\n\t"
      "v_mfma_f32_16x16x32_bf16 v[188:191], v[152:155], v[100:103], v[188:191]\n\t"
      "s_nop 5\n\t"
      "v_cmp_u_f32_e32 vcc, v184, v185\n\t"
      "v_cmp_u_f32_e64 s[48:49], v186, v187\n\t"
      "v_mfma_f32_16x16x32_bf16 v[192:195], v[148:151], v[88:91], v[192:195]\n\t"
      "v_mfma_f32_16x16x32_bf16 v[192:195], v[152:155], v[104:107], v[192:195]\n\t"
      "s_or_b64 vcc, vcc, s[48:49]\n\t"
      "s_cbranch_scc1 NP0R_%=\n\t"
      "NP0B_%=:\n\t"
      "ds_write_b128 v243, v[184:187]\n\t"
      "v_mfma_f32_16x16x32_bf16 v[196:199], v[148:151], v[92:95], v[196:199]\n\t"
      "global_load_dwordx4 v[148:151], v229, s[54:55] sc1\n\t"
      "v_mfma_f32_16x16x32_bf16 v[196:199], v[152:155], v[108:111], v[196:199]\n\t"
      "global_load_dwordx4 v[152:155], v230, s[54:55] sc1\n\t"
      "ds_write_b128 v220, v[188:191]\n\t"
      "ds_write_b128 v221, v[192:195]\n\t"
      "s_nop 4\n\t"
      "ds_write_b128 v222, v[196:199]\n\t"
      "s_waitcnt lgkmcnt(0)\n\t"
      "s_barrier\n\t"
      "ds_read_b128 v[112:115], v244\n\t"
      "ds_read_b128 v[116:119], v244 offset:4096\n\t"
      "ds_read_b128 v[120:123], v244 offset:8192\n\t"
      "ds_read_b128 v[124:127], v244 offset:12288\n\t"
      "ds_read_b128 v[184:187], v244 offset:16384\n\t"
      "ds_read_b128 v[188:191], v244 offset:20480\n\t"
      "ds_read_b128 v[192:195], v244 offset:24576\n\t"
      "ds_read_b128 v[196:199], v244 offset:28672\n\t"
      "s_waitcnt vmcnt(6)\n\t"
      "v_mfma_f32_16x16x32_bf16 v[200:203], v[128:131], v[0:3], v[160:163]\n\t"
      "v_mfma_f32_16x16x32_bf16 v[204:207], v[128:131], v[4:7], 0\n\t"
      "s_waitcnt lgkmcnt(6)\n\t"
      "v_add_f32_e32 v112, v112, v116\n\t"
      "v_add_f32_e32 v113, v113, v117\n\t"
      "v_mfma_f32_16x16x32_bf16 v[208:211], v[128:131], v[8:11], 0\n\t"
      "v_add_f32_e32 v114, v114, v118\n\t"
      "v_add_f32_e32 v115, v115, v119\n\t"
      "v_mfma_f32_16x16x32_bf16 v[212:215], v[128:131], v[12:15], 0\n\t"
      "global_load_dwordx4 v[128:131], v224, s[56:57] sc1\n\t"
      "s_waitcnt vmcnt(6)\n\t"
      "v_mfma_f32_16x16x32_bf16 v[200:203], v[132:135], v[16:19], v[200:203]\n\t"
      "s_waitcnt lgkmcnt(4)\n\t"
      "v_add_f32_e32 v120, v120, v124\n\t"
      "s_waitcnt vmcnt(5)\n\t"
      "v_mfma_f32_16x16x32_bf16 v[200:203], v[136:139], v[32:35], v[200:203]\n\t"
      "v_add_f32_e32 v121, v121, v125\n\t"
      "v_add_f32_e32 v122, v122, v126\n\t"
      "v_mfma_f32_16x16x32_bf16 v[204:207], v[132:135], v[20:23], v[204:207]\n\t"
      "v_add_f32_e32 v123, v123, v127\n\t"
      "s_waitcnt lgkmcnt(2)\n\t"
      "v_add_f32_e32 v184, v184, v188\n\t"
      "v_mfma_f32_16x16x32_bf16 v[204:207], v[136:139], v[36:39], v[204:207]\n\t"
      "v_add_f32_e32 v185, v185, v189\n\t"
      "v_add_f32_e32 v186, v186, v190\n\t"
      "v_mfma_f32_16x16x32_bf16 v[208:211], v[132:135], v[24:27], v[208:211]\n\t"
      "v_add_f32_e32 v187, v187, v191\n\t"
      "v_add_f32_e32 v112, v112, v120\n\t"
      "v_mfma_f32_16x16x32_bf16 v[208:211], v[136:139], v[40:43], v[208:211]\n\t"
      "v_add_f32_e32 v113, v113, v121\n\t"
      "v_add_f32_e32 v114, v114, v122\n\t"
      "v_mfma_f32_16x16x32_bf16 v[212:215], v[132:135], v[28:31], v[212:215]\n\t"
      "global_load_dwordx4 v[132:135], v225, s[56:57] sc1\n\t"
      "v_add_f32_e32 v115, v115, v123\n\t"
      "v_mfma_f32_16x16x32_bf16 v[212:215], v[136:139], v[44:47], v[212:215]\n\t"
      "global_load_dwordx4 v[136:139], v226, s[56:57] sc1\n\t"
      "s_waitcnt vmcnt(6)\n\t"
      "v_mfma_f32_16x16x32_bf16 v[200:203], v[140:143], v[48:51], v[200:203]\n\t"
      "s_waitcnt lgkmcnt(0)\n\t"
      "v_add_f32_e32 v192, v192, v196\n\t"
      "s_waitcnt vmcnt(5)\n\t"
      "v_mfma_f32_16x16x32_bf16 v[200:203], v[144:147], v[64:67], v[200:203]\n\t"
      "v_add_f32_e32 v193, v193, v197\n\t"
      "v_add_f32_e32 v194, v194, v198\n\t"
      "v_mfma_f32_16x16x32_bf16 v[204:207], v[140:143], v[52:55], v[204:207]\n\t"
      "v_add_f32_e32 v195, v195, v199\n\t"
      "v_add_f32_e32 v184, v184, v192\n\t"
      "v_mfma_f32_16x16x32_bf16 v[204:207], v[144:147], v[68:71], v[204:207]\n\t"
      "v_add_f32_e32 v185, v185, v193\n\t"
      "v_add_f32_e32 v186, v186, v194\n\t"
      "v_mfma_f32_16x16x32_bf16 v[208:211], v[140:143], v[56:59], v[208:211]\n\t"
      "v_add_f32_e32 v187, v187, v195\n\t"
      "v_add_f32_e32 v112, v112, v184\n\t"
      "v_mfma_f32_16x16x32_bf16 v[208:211], v[144:147], v[72:75], v[208:211]\n\t"
      "v_add_f32_e32 v113, v113, v185\n\t"
      "v_add_f32_e32 v114, v114, v186\n\t"
      "v_mfma_f32_16x16x32_bf16 v[212:215], v[140:143], v[60:63], v[212:215]\n\t"
      "global_load_dwordx4 v[140:143], v227, s[56:57] sc1\n\t"
      "v_add_f32_e32 v115, v115, v187\n\t"
      "v_mfma_f32_16x16x32_bf16 v[212:215], v[144:147], v[76:79], v[212:215]\n\t"
      "global_load_dwordx4 v[144:147], v228, s[56:57] sc1\n\t"
      "s_waitcnt vmcnt(6)\n\t"
      "v_mfma_f32_16x16x32_bf16 v[200:203], v[148:151], v[80:83], v[200:203]\n\t"
      "v_cvt_pk_bf16_f32 v116, v112, v113\n\t"
      "s_waitcnt vmcnt(5)\n\t"
      "v_mfma_f32_16x16x32_bf16 v[200:203], v[152:155], v[96:99], v[200:203]\n\t"
      "v_cvt_pk_bf16_f32 v117, v114, v115\n\t"
      "global_store_dwordx2 v245, v[116:117], s[8:9]\n\t"
      "v_mfma_f32_16x16x32_bf16 v[204:207], v[148:151], v[84:87], v[204:207]\n\t"
      "v_mfma_f32_16x16x32_bf16 v[204:207], v[152:155], v[100:103], v[204:207]\n\t"
      "s_nop 3\n\t"
      "v_cmp_u_f32_e32 vcc, v200, v201\n\t"
      "v_cmp_u_f32_e64 s[48:49], v202, v203\n\t"
      "v_mfma_f32_16x16x32_bf16 v[208:211], v[148:151], v[88:91], v[208:211]\n\t"
      "v_mfma_f32_16x16x32_bf16 v[208:211], v[152:155], v[104:107], v[208:211]\n\t"
      "s_or_b64 vcc, vcc, s[48:49]\n\t"
      "s_cbranch_scc1 NP1R_%=\n\t"
      "NP1B_%=:\n\t"
      "ds_write_b128 v243, v[200:203] offset:32768\n\t"
      "v_mfma_f32_16x16x32_bf16 v[212:215], v[148:151], v[92:95], v[212:215]\n\t"
      "global_load_dwordx4 v[148:151], v229, s[56:57] sc1\n\t"
      "v_mfma_f32_16x16x32_bf16 v[212:215], v[152:155], v[108:111], v[212:215]\n\t"
      "global_load_dwordx4 v[152:155], v230, s[56:57] sc1\n\t"
      "ds_write_b128 v220, v[204:207] offset:32768\n\t"
      "ds_write_b128 v221, v[208:211] offset:32768\n\t"
      "s_nop 4\n\t"
      "ds_write_b128 v222, v[212:215] offset:32768\n\t"
      "s_waitcnt lgkmcnt(0)\n\t"
      "s_barrier\n\t"
      "ds_read_b128 v[112:115], v244 offset:32768\n\t"
      "ds_read_b128 v[116:119], v244 offset:36864\n\t"
      "ds_read_b128 v[120:123], v244 offset:40960\n\t"
      "ds_read_b128 v[124:127], v244 offset:45056\n\t"
      "ds_read_b128 v[200:203], v244 offset:49152\n\t"
      "ds_read_b128 v[204:207], v244 offset:53248\n\t"
      "ds_read_b128 v[208:211], v244 offset:57344\n\t"
      "ds_read_b128 v[212:215], v244 offset:61440\n\t"
      "s_waitcnt vmcnt(7)\n\t"
      "v_mfma_f32_16x16x32_bf16 v[184:187], v[128:131], v[0:3], v[164:167]\n\t"
      "v_mfma_f32_16x16x32_bf16 v[188:191], v[128:131], v[4:7], 0\n\t"
      "s_waitcnt lgkmcnt(6)\n\t"
      "v_add_f32_e32 v112, v112, v116\n\t"
      "v_add_f32_e32 v113, v113, v117\n\t"
      "v_mfma_f32_16x16x32_bf16 v[192:195], v[128:131], v[8:11], 0\n\t"
      "v_add_f32_e32 v114, v114, v118\n\t"
      "v_add_f32_e32 v115, v115, v119\n\t"
      "v_mfma_f32_16x16x32_bf16 v[196:199], v[128:131], v[12:15], 0\n\t"
      "global_load_dwordx4 v[128:131], v224, s[58:59] sc1\n\t"
      "s_waitcnt vmcnt(7)\n\t"
      "v_mfma_f32_16x16x32_bf16 v[184:187], v[132:135], v[16:19], v[184:187]\n\t"
      "s_waitcnt lgkmcnt(4)\n\t"
      "v_add_f32_e32 v120, v120, v124\n\t"
      "s_waitcnt vmcnt(6)\n\t"
      "v_mfma_f32_16x16x32_bf16 v[184:187], v[136:139], v[32:35], v[184:187]\n\t"
      "v_add_f32_e32 v121, v121, v125\n\t"
      "v_add_f32_e32 v122, v122, v126\n\t"
      "v_mfma_f32_16x16x32_bf16 v[188:191], v[132:135], v[20:23], v[188:191]\n\t"
      "v_add_f32_e32 v123, v123, v127\n\t"
      "s_waitcnt lgkmcnt(2)\n\t"
      "v_add_f32_e32 v200, v200, v204\n\t"
      "v_mfma_f32_16x16x32_bf16 v[188:191], v[136:139], v[36:39], v[188:191]\n\t"
      "v_add_f32_e32 v201, v201, v205\n\t"
      "v_add_f32_e32 v202, v202, v206\n\t"
      "v_mfma_f32_16x16x32_bf16 v[192:195], v[132:135], v[24:27], v[192:195]\n\t"
      "v_add_f32_e32 v203, v203, v207\n\t"
      "v_add_f32_e32 v112, v112, v120\n\t"
      "v_mfma_f32_16x16x32_bf16 v[192:195], v[136:139], v[40:43], v[192:195]\n\t"
      "v_add_f32_e32 v113, v113, v121\n\t"
      "v_add_f32_e32 v114, v114, v122\n\t"
      "v_mfma_f32_16x16x32_bf16 v[196:199], v[132:135], v[28:31], v[196:199]\n\t"
      "global_load_dwordx4 v[132:135], v225, s[58:59] sc1\n\t"
      "v_add_f32_e32 v115, v115, v123\n\t"
      "v_mfma_f32_16x16x32_bf16 v[196:199], v[136:139], v[44:47], v[196:199]\n\t"
      "global_load_dwordx4 v[136:139], v226, s[58:59] sc1\n\t"
      "s_waitcnt vmcnt(7)\n\t"
      "v_mfma_f32_16x16x32_bf16 v[184:187], v[140:143], v[48:51], v[184:187]\n\t"
      "s_waitcnt lgkmcnt(0)\n\t"
      "v_add_f32_e32 v208, v208, v212\n\t"
      "s_waitcnt vmcnt(6)\n\t"
      "v_mfma_f32_16x16x32_bf16 v[184:187], v[144:147], v[64:67], v[184:187]\n\t"
      "v_add_f32_e32 v209, v209, v213\n\t"
      "v_add_f32_e32 v210, v210, v214\n\t"
      "v_mfma_f32_16x16x32_bf16 v[188:191], v[140:143], v[52:55], v[188:191]\n\t"
      "v_add_f32_e32 v211, v211, v215\n\t"
      "v_add_f32_e32 v200, v200, v208\n\t"
      "v_mfma_f32_16x16x32_bf16 v[188:191], v[144:147], v[68:71], v[188:191]\n\t"
      "v_add_f32_e32 v201, v201, v209\n\t"
      "v_add_f32_e32 v202, v202, v210\n\t"
      "v_mfma_f32_16x16x32_bf16 v[192:195], v[140:143], v[56:59], v[192:195]\n\t"
      "v_add_f32_e32 v203, v203, v211\n\t"
      "v_add_f32_e32 v112, v112, v200\n\t"
      "v_mfma_f32_16x16x32_bf16 v[192:195], v[144:147], v[72:75], v[192:195]\n\t"
      "v_add_f32_e32 v113, v113, v201\n\t"
      "v_add_f32_e32 v114, v114, v202\n\t"
      "v_mfma_f32_16x16x32_bf16 v[196:199], v[140:143], v[60:63], v[196:199]\n\t"
      "global_load_dwordx4 v[140:143], v227, s[58:59] sc1\n\t"
      "v_add_f32_e32 v115, v115, v203\n\t"
      "v_mfma_f32_16x16x32_bf16 v[196:199], v[144:147], v[76:79], v[196:199]\n\t"
      "global_load_dwordx4 v[144:147], v228, s[58:59] sc1\n\t"
      "s_waitcnt vmcnt(6)\n\t"
      "v_mfma_f32_16x16x32_bf16 v[184:187], v[148:151], v[80:83], v[184:187]\n\t"
      "v_cvt_pk_bf16_f32 v116, v112, v113\n\t"
      "s_waitcnt vmcnt(5)\n\t"
      "v_mfma_f32_16x16x32_bf16 v[184:187], v[152:155], v[96:99], v[184:187]\n\t"
      "v_cvt_pk_bf16_f32 v117, v114, v115\n\t"
      "global_store_dwordx2 v245, v[116:117], s[8:9] offset:32\n\t"
      "v_mfma_f32_16x16x32_bf16 v[188:191], v[148:151], v[84:87], v[188:191]\n\t"
      "v_mfma_f32_16x16x32_bf16 v[188:191], v[152:155], v[100:103], v[188:191]\n\t"
      "s_nop 3\n\t"
      "v_cmp_u_f32_e32 vcc, v184, v185\n\t"
      "v_cmp_u_f32_e64 s[48:49], v186, v187\n\t"
      "v_mfma_f32_16x16x32_bf16 v[192:195], v[148:151], v[88:91], v[192:195]\n\t"
      "v_mfma_f32_16x16x32_bf16 v[192:195], v[152:155], v[104:107], v[192:195]\n\t"
      "s_or_b64 vcc, vcc, s[48:49]\n\t"
      "s_cbranch_scc1 NP2R_%=\n\t"
      "NP2B_%=:\n\t"
      "ds_write_b128 v243, v[184:187]\n\t"
      "v_mfma_f32_16x16x32_bf16 v[196:199], v[148:151], v[92:95], v[196:199]\n\t"
      "global_load_dwordx4 v[148:151], v229, s[58:59] sc1\n\t"
      "v_mfma_f32_16x16x32_bf16 v[196:199], v[152:155], v[108:111], v[196:199]\n\t"
      "global_load_dwordx4 v[152:155], v230, s[58:59] sc1\n\t"
      "ds_write_b128 v220, v[188:191]\n\t"
      "ds_write_b128 v221, v[192:195]\n\t"
      "s_nop 4\n\t"
      "ds_write_b128 v222, v[196:199]\n\t"
      "s_waitcnt lgkmcnt(0)\n\t"
      "s_barrier\n\t"
      "ds_read_b128 v[112:115], v244\n\t"
      "ds_read_b128 v[116:119], v244 offset:4096\n\t"
      "ds_read_b128 v[120:123], v244 offset:8192\n\t"
      "ds_read_b128 v[124:127], v244 offset:12288\n\t"
      "ds_read_b128 v[184:187], v244 offset:16384\n\t"
      "ds_read_b128 v[188:191], v244 offset:20480\n\t"
      "ds_read_b128 v[192:195], v244 offset:24576\n\t"
      "ds_read_b128 v[196:199], v244 offset:28672\n\t"
      "s_waitcnt vmcnt(7)\n\t"
      "v_mfma_f32_16x16x32_bf16 v[200:203], v[128:131], v[0:3], v[168:171]\n\t"
      "v_mfma_f32_16x16x32_bf16 v[204:207], v[128:131], v[4:7], 0\n\t"
      "s_waitcnt lgkmcnt(6)\n\t"
      "v_add_f32_e32 v112, v112, v116\n\t"
      "v_add_f32_e32 v113, v113, v117\n\t"
      "v_mfma_f32_16x16x32_bf16 v[208:211], v[128:131], v[8:11], 0\n\t"
      "v_add_f32_e32 v114, v114, v118\n\t"
      "v_add_f32_e32 v115, v115, v119\n\t"
      "v_mfma_f32_16x16x32_bf16 v[212:215], v[128:131], v[12:15], 0\n\t"
      "global_load_dwordx4 v[128:131], v224, s[60:61] sc1\n\t"
      "s_waitcnt vmcnt(7)\n\t"
      "v_mfma_f32_16x16x32_bf16 v[200:203], v[132:135], v[16:19], v[200:203]\n\t"
      "s_waitcnt lgkmcnt(4)\n\t"
      "v_add_f32_e32 v120, v120, v124\n\t"
      "s_waitcnt vmcnt(6)\n\t"
      "v_mfma_f32_16x16x32_bf16 v[200:203], v[136:139], v[32:35], v[200:203]\n\t"
      "v_add_f32_e32 v121, v121, v125\n\t"
      "v_add_f32_e32 v122, v122, v126\n\t"
      "v_mfma_f32_16x16x32_bf16 v[204:207], v[132:135], v[20:23], v[204:207]\n\t"
      "v_add_f32_e32 v123, v123, v127\n\t"
      "s_waitcnt lgkmcnt(2)\n\t"
      "v_add_f32_e32 v184, v184, v188\n\t"
      "v_mfma_f32_16x16x32_bf16 v[204:207], v[136:139], v[36:39], v[204:207]\n\t"
      "v_add_f32_e32 v185, v185, v189\n\t"
      "v_add_f32_e32 v186, v186, v190\n\t"
      "v_mfma_f32_16x16x32_bf16 v[208:211], v[132:135], v[24:27], v[208:211]\n\t"
      "v_add_f32_e32 v187, v187, v191\n\t"
      "v_add_f32_e32 v112, v112, v120\n\t"
      "v_mfma_f32_16x16x32_bf16 v[208:211], v[136:139], v[40:43], v[208:211]\n\t"
      "v_add_f32_e32 v113, v113, v121\n\t"
      "v_add_f32_e32 v114, v114, v122\n\t"
      "v_mfma_f32_16x16x32_bf16 v[212:215], v[132:135], v[28:31], v[212:215]\n\t"
      "global_load_dwordx4 v[132:135], v225, s[60:61] sc1\n\t"
      "v_add_f32_e32 v115, v115, v123\n\t"
      "v_mfma_f32_16x16x32_bf16 v[212:215], v[136:139], v[44:47], v[212:215]\n\t"
      "global_load_dwordx4 v[136:139], v226, s[60:61] sc1\n\t"
      "s_waitcnt vmcnt(7)\n\t"
      "v_mfma_f32_16x16x32_bf16 v[200:203], v[140:143], v[48:51], v[200:203]\n\t"
      "s_waitcnt lgkmcnt(0)\n\t"
      "v_add_f32_e32 v192, v192, v196\n\t"
      "s_waitcnt vmcnt(6)\n\t"
      "v_mfma_f32_16x16x32_bf16 v[200:203], v[144:147], v[64:67], v[200:203]\n\t"
      "v_add_f32_e32 v193, v193, v197\n\t"
      "v_add_f32_e32 v194, v194, v198\n\t"
      "v_mfma_f32_16x16x32_bf16 v[204:207], v[140:143], v[52:55], v[204:207]\n\t"
      "v_add_f32_e32 v195, v195, v199\n\t"
      "v_add_f32_e32 v184, v184, v192\n\t"
      "v_mfma_f32_16x16x32_bf16 v[204:207], v[144:147], v[68:71], v[204:207]\n\t"
      "v_add_f32_e32 v185, v185, v193\n\t"
      "v_add_f32_e32 v186, v186, v194\n\t"
      "v_mfma_f32_16x16x32_bf16 v[208:211], v[140:143], v[56:59], v[208:211]\n\t"
      "v_add_f32_e32 v187, v187, v195\n\t"
      "v_add_f32_e32 v112, v112, v184\n\t"
      "v_mfma_f32_16x16x32_bf16 v[208:211], v[144:147], v[72:75], v[208:211]\n\t"
      "v_add_f32_e32 v113, v113, v185\n\t"
      "v_add_f32_e32 v114, v114, v186\n\t"
      "v_mfma_f32_16x16x32_bf16 v[212:215], v[140:143], v[60:63], v[212:215]\n\t"
      "global_load_dwordx4 v[140:143], v227, s[60:61] sc1\n\t"
      "v_add_f32_e32 v115, v115, v187\n\t"
      "v_mfma_f32_16x16x32_bf16 v[212:215], v[144:147], v[76:79], v[212:215]\n\t"
      "global_load_dwordx4 v[144:147], v228, s[60:61] sc1\n\t"
      "s_waitcnt vmcnt(6)\n\t"
      "v_mfma_f32_16x16x32_bf16 v[200:203], v[148:151], v[80:83], v[200:203]\n\t"
      "v_cvt_pk_bf16_f32 v116, v112, v113\n\t"
      "s_waitcnt vmcnt(5)\n\t"
      "v_mfma_f32_16x16x32_bf16 v[200:203], v[152:155], v[96:99], v[200:203]\n\t"
      "v_cvt_pk_bf16_f32 v117, v114, v115\n\t"
      "global_store_dwordx2 v245, v[116:117], s[66:67]\n\t"
      "v_mfma_f32_16x16x32_bf16 v[204:207], v[148:151], v[84:87], v[204:207]\n\t"
      "v_mfma_f32_16x16x32_bf16 v[204:207], v[152:155], v[100:103], v[204:207]\n\t"
      "s_nop 3\n\t"
      "v_cmp_u_f32_e32 vcc, v200, v201\n\t"
      "v_cmp_u_f32_e64 s[48:49], v202, v203\n\t"
      "v_mfma_f32_16x16x32_bf16 v[208:211], v[148:151], v[88:91], v[208:211]\n\t"
      "v_mfma_f32_16x16x32_bf16 v[208:211], v[152:155], v[104:107], v[208:211]\n\t"
      "s_or_b64 vcc, vcc, s[48:49]\n\t"
      "s_cbranch_scc1 NP3R_%=\n\t"
      "NP3B_%=:\n\t"
      "ds_write_b128 v243, v[200:203] offset:32768\n\t"
      "v_mfma_f32_16x16x32_bf16 v[212:215], v[148:151], v[92:95], v[212:215]\n\t"
      "global_load_dwordx4 v[148:151], v229, s[60:61] sc1\n\t"
      "v_mfma_f32_16x16x32_bf16 v[212:215], v[152:155], v[108:111], v[212:215]\n\t"
      "global_load_dwordx4 v[152:155], v230, s[60:61] sc1\n\t"
      "ds_write_b128 v220, v[204:207] offset:32768\n\t"
      "ds_write_b128 v221, v[208:211] offset:32768\n\t"
      "s_nop 4\n\t"
      "ds_write_b128 v222, v[212:215] offset:32768\n\t"
      "s_waitcnt lgkmcnt(0)\n\t"
      "s_barrier\n\t"
      "ds_read_b128 v[112:115], v244 offset:32768\n\t"
      "ds_read_b128 v[116:119], v244 offset:36864\n\t"
      "ds_read_b128 v[120:123], v244 offset:40960\n\t"
      "ds_read_b128 v[124:127], v244 offset:45056\n\t"
      "ds_read_b128 v[200:203], v244 offset:49152\n\t"
      "ds_read_b128 v[204:207], v244 offset:53248\n\t"
      "ds_read_b128 v[208:211], v244 offset:57344\n\t"
      "ds_read_b128 v[212:215], v244 offset:61440\n\t"
      "s_waitcnt vmcnt(7)\n\t"
      "v_mfma_f32_16x16x32_bf16 v[184:187], v[128:131], v[0:3], v[172:175]\n\t"
      "v_mfma_f32_16x16x32_bf16 v[188:191], v[128:131], v[4:7], 0\n\t"
      "s_waitcnt lgkmcnt(6)\n\t"
      "v_add_f32_e32 v112, v112, v116\n\t"
      "v_add_f32_e32 v113, v113, v117\n\t"
      "v_mfma_f32_16x16x32_bf16 v[192:195], v[128:131], v[8:11], 0\n\t"
      "v_add_f32_e32 v114, v114, v118\n\t"
      "v_add_f32_e32 v115, v115, v119\n\t"
      "v_mfma_f32_16x16x32_bf16 v[196:199], v[128:131], v[12:15], 0\n\t"
      "global_load_dwordx4 v[128:131], v224, s[62:63] sc1\n\t"
      "s_waitcnt vmcnt(7)\n\t"
      "v_mfma_f32_16x16x32_bf16 v[184:187], v[132:135], v[16:19], v[184:187]\n\t"
      "s_waitcnt lgkmcnt(4)\n\t"
      "v_add_f32_e32 v120, v120, v124\n\t"
      "s_waitcnt vmcnt(6)\n\t"
      "v_mfma_f32_16x16x32_bf16 v[184:187], v[136:139], v[32:35], v[184:187]\n\t"
      "v_add_f32_e32 v121, v121, v125\n\t"
      "v_add_f32_e32 v122, v122, v126\n\t"
      "v_mfma_f32_16x16x32_bf16 v[188:191], v[132:135], v[20:23], v[188:191]\n\t"
      "v_add_f32_e32 v123, v123, v127\n\t"
      "s_waitcnt lgkmcnt(2)\n\t"
      "v_add_f32_e32 v200, v200, v204\n\t"
      "v_mfma_f32_16x16x32_bf16 v[188:191], v[136:139], v[36:39], v[188:191]\n\t"
      "v_add_f32_e32 v201, v201, v205\n\t"
      "v_add_f32_e32 v202, v202, v206\n\t"
      "v_mfma_f32_16x16x32_bf16 v[192:195], v[132:135], v[24:27], v[192:195]\n\t"
      "v_add_f32_e32 v203, v203, v207\n\t"
      "v_add_f32_e32 v112, v112, v120\n\t"
      "v_mfma_f32_16x16x32_bf16 v[192:195], v[136:139], v[40:43], v[192:195]\n\t"
      "v_add_f32_e32 v113, v113, v121\n\t"
      "v_add_f32_e32 v114, v114, v122\n\t"
      "v_mfma_f32_16x16x32_bf16 v[196:199], v[132:135], v[28:31], v[196:199]\n\t"
      "global_load_dwordx4 v[132:135], v225, s[62:63] sc1\n\t"
      "v_add_f32_e32 v115, v115, v123\n\t"
      "v_mfma_f32_16x16x32_bf16 v[196:199], v[136:139], v[44:47], v[196:199]\n\t"
      "global_load_dwordx4 v[136:139], v226, s[62:63] sc1\n\t"
      "s_waitcnt vmcnt(7)\n\t"
      "v_mfma_f32_16x16x32_bf16 v[184:187], v[140:143], v[48:51], v[184:187]\n\t"
      "s_waitcnt lgkmcnt(0)\n\t"
      "v_add_f32_e32 v208, v208, v212\n\t"
      "s_waitcnt vmcnt(6)\n\t"
      "v_mfma_f32_16x16x32_bf16 v[184:187], v[144:147], v[64:67], v[184:187]\n\t"
      "v_add_f32_e32 v209, v209, v213\n\t"
      "v_add_f32_e32 v210, v210, v214\n\t"
      "v_mfma_f32_16x16x32_bf16 v[188:191], v[140:143], v[52:55], v[188:191]\n\t"
      "v_add_f32_e32 v211, v211, v215\n\t"
      "v_add_f32_e32 v200, v200, v208\n\t"
      "v_mfma_f32_16x16x32_bf16 v[188:191], v[144:147], v[68:71], v[188:191]\n\t"
      "v_add_f32_e32 v201, v201, v209\n\t"
      "v_add_f32_e32 v202, v202, v210\n\t"
      "v_mfma_f32_16x16x32_bf16 v[192:195], v[140:143], v[56:59], v[192:195]\n\t"
      "v_add_f32_e32 v203, v203, v211\n\t"
      "v_add_f32_e32 v112, v112, v200\n\t"
      "v_mfma_f32_16x16x32_bf16 v[192:195], v[144:147], v[72:75], v[192:195]\n\t"
      "v_add_f32_e32 v113, v113, v201\n\t"
      "v_add_f32_e32 v114, v114, v202\n\t"
      "v_mfma_f32_16x16x32_bf16 v[196:199], v[140:143], v[60:63], v[196:199]\n\t"
      "global_load_dwordx4 v[140:143], v227, s[62:63] sc1\n\t"
      "v_add_f32_e32 v115, v115, v203\n\t"
      "v_mfma_f32_16x16x32_bf16 v[196:199], v[144:147], v[76:79], v[196:199]\n\t"
      "global_load_dwordx4 v[144:147], v228, s[62:63] sc1\n\t"
      "s_waitcnt vmcnt(6)\n\t"
      "v_mfma_f32_16x16x32_bf16 v[184:187], v[148:151], v[80:83], v[184:187]\n\t"
      "v_cvt_pk_bf16_f32 v116, v112, v113\n\t"
      "s_waitcnt vmcnt(5)\n\t"
      "v_mfma_f32_16x16x32_bf16 v[184:187], v[152:155], v[96:99], v[184:187]\n\t"
      "v_cvt_pk_bf16_f32 v117, v114, v115\n\t"
      "global_store_dwordx2 v245, v[116:117], s[66:67] offset:32\n\t"
      "v_mfma_f32_16x16x32_bf16 v[188:191], v[148:151], v[84:87], v[188:191]\n\t"
      "v_mfma_f32_16x16x32_bf16 v[188:191], v[152:155], v[100:103], v[188:191]\n\t"
      "s_nop 3\n\t"
      "v_cmp_u_f32_e32 vcc, v184, v185\n\t"
      "v_cmp_u_f32_e64 s[48:49], v186, v187\n\t"
      "v_mfma_f32_16x16x32_bf16 v[192:195], v[148:151], v[88:91], v[192:195]\n\t"
      "v_mfma_f32_16x16x32_bf16 v[192:195], v[152:155], v[104:107], v[192:195]\n\t"
      "s_or_b64 vcc, vcc, s[48:49]\n\t"
      "s_cbranch_scc1 NP4R_%=\n\t"
      "NP4B_%=:\n\t"
      "ds_write_b128 v243, v[184:187]\n\t"
      "v_mfma_f32_16x16x32_bf16 v[196:199], v[148:151], v[92:95], v[196:199]\n\t"
      "global_load_dwordx4 v[148:151], v229, s[62:63] sc1\n\t"
      "v_mfma_f32_16x16x32_bf16 v[196:199], v[152:155], v[108:111], v[196:199]\n\t"
      "global_load_dwordx4 v[152:155], v230, s[62:63] sc1\n\t"
      "ds_write_b128 v220, v[188:191]\n\t"
      "ds_write_b128 v221, v[192:195]\n\t"
      "s_nop 4\n\t"
      "ds_write_b128 v222, v[196:199]\n\t"
      "s_waitcnt lgkmcnt(0)\n\t"
      "s_barrier\n\t"
      "ds_read_b128 v[112:115], v244\n\t"
      "ds_read_b128 v[116:119], v244 offset:4096\n\t"
      "ds_read_b128 v[120:123], v244 offset:8192\n\t"
      "ds_read_b128 v[124:127], v244 offset:12288\n\t"
      "ds_read_b128 v[184:187], v244 offset:16384\n\t"
      "ds_read_b128 v[188:191], v244 offset:20480\n\t"
      "ds_read_b128 v[192:195], v244 offset:24576\n\t"
      "ds_read_b128 v[196:199], v244 offset:28672\n\t"
      "s_waitcnt vmcnt(7)\n\t"
      "v_mfma_f32_16x16x32_bf16 v[200:203], v[128:131], v[0:3], v[176:179]\n\t"
      "v_mfma_f32_16x16x32_bf16 v[204:207], v[128:131], v[4:7], 0\n\t"
      "s_waitcnt lgkmcnt(6)\n\t"
      "v_add_f32_e32 v112, v112, v116\n\t"
      "v_add_f32_e32 v113, v113, v117\n\t"
      "v_mfma_f32_16x16x32_bf16 v[208:211], v[128:131], v[8:11], 0\n\t"
      "v_add_f32_e32 v114, v114, v118\n\t"
      "v_add_f32_e32 v115, v115, v119\n\t"
      "v_mfma_f32_16x16x32_bf16 v[212:215], v[128:131], v[12:15], 0\n\t"
      "global_load_dwordx4 v[128:131], v224, s[64:65] sc1\n\t"
      "s_waitcnt vmcnt(7)\n\t"
      "v_mfma_f32_16x16x32_bf16 v[200:203], v[132:135], v[16:19], v[200:203]\n\t"
      "s_waitcnt lgkmcnt(4)\n\t"
      "v_add_f32_e32 v120, v120, v124\n\t"
      "s_waitcnt vmcnt(6)\n\t"
      "v_mfma_f32_16x16x32_bf16 v[200:203], v[136:139], v[32:35], v[200:203]\n\t"
      "v_add_f32_e32 v121, v121, v125\n\t"
      "v_add_f32_e32 v122, v122, v126\n\t"
      "v_mfma_f32_16x16x32_bf16 v[204:207], v[132:135], v[20:23], v[204:207]\n\t"
      "v_add_f32_e32 v123, v123, v127\n\t"
      "s_waitcnt lgkmcnt(2)\n\t"
      "v_add_f32_e32 v184, v184, v188\n\t"
      "v_mfma_f32_16x16x32_bf16 v[204:207], v[136:139], v[36:39], v[204:207]\n\t"
      "v_add_f32_e32 v185, v185, v189\n\t"
      "v_add_f32_e32 v186, v186, v190\n\t"
      "v_mfma_f32_16x16x32_bf16 v[208:211], v[132:135], v[24:27], v[208:211]\n\t"
      "v_add_f32_e32 v187, v187, v191\n\t"
      "v_add_f32_e32 v112, v112, v120\n\t"
      "v_mfma_f32_16x16x32_bf16 v[208:211], v[136:139], v[40:43], v[208:211]\n\t"
      "v_add_f32_e32 v113, v113, v121\n\t"
      "v_add_f32_e32 v114, v114, v122\n\t"
      "v_mfma_f32_16x16x32_bf16 v[212:215], v[132:135], v[28:31], v[212:215]\n\t"
      "global_load_dwordx4 v[132:135], v225, s[64:65] sc1\n\t"
      "v_add_f32_e32 v115, v115, v123\n\t"
      "v_mfma_f32_16x16x32_bf16 v[212:215], v[136:139], v[44:47], v[212:215]\n\t"
      "global_load_dwordx4 v[136:139], v226, s[64:65] sc1\n\t"
      "s_waitcnt vmcnt(7)\n\t"
      "v_mfma_f32_16x16x32_bf16 v[200:203], v[140:143], v[48:51], v[200:203]\n\t"
      "s_waitcnt lgkmcnt(0)\n\t"
      "v_add_f32_e32 v192, v192, v196\n\t"
      "s_waitcnt vmcnt(6)\n\t"
      "v_mfma_f32_16x16x32_bf16 v[200:203], v[144:147], v[64:67], v[200:203]\n\t"
      "v_add_f32_e32 v193, v193, v197\n\t"
      "v_add_f32_e32 v194, v194, v198\n\t"
      "v_mfma_f32_16x16x32_bf16 v[204:207], v[140:143], v[52:55], v[204:207]\n\t"
      "v_add_f32_e32 v195, v195, v199\n\t"
      "v_add_f32_e32 v184, v184, v192\n\t"
      "v_mfma_f32_16x16x32_bf16 v[204:207], v[144:147], v[68:71], v[204:207]\n\t"
      "v_add_f32_e32 v185, v185, v193\n\t"
      "v_add_f32_e32 v186, v186, v194\n\t"
      "v_mfma_f32_16x16x32_bf16 v[208:211], v[140:143], v[56:59], v[208:211]\n\t"
      "v_add_f32_e32 v187, v187, v195\n\t"
      "v_add_f32_e32 v112, v112, v184\n\t"
      "v_mfma_f32_16x16x32_bf16 v[208:211], v[144:147], v[72:75], v[208:211]\n\t"
      "v_add_f32_e32 v113, v113, v185\n\t"
      "v_add_f32_e32 v114, v114, v186\n\t"
      "v_mfma_f32_16x16x32_bf16 v[212:215], v[140:143], v[60:63], v[212:215]\n\t"
      "global_load_dwordx4 v[140:143], v227, s[64:65] sc1\n\t"
      "v_add_f32_e32 v115, v115, v187\n\t"
      "v_mfma_f32_16x16x32_bf16 v[212:215], v[144:147], v[76:79], v[212:215]\n\t"
      "global_load_dwordx4 v[144:147], v228, s[64:65] sc1\n\t"
      "s_waitcnt vmcnt(6)\n\t"
      "v_mfma_f32_16x16x32_bf16 v[200:203], v[148:151], v[80:83], v[200:203]\n\t"
      "v_cvt_pk_bf16_f32 v116, v112, v113\n\t"
      "s_waitcnt vmcnt(5)\n\t"
      "v_mfma_f32_16x16x32_bf16 v[200:203], v[152:155], v[96:99], v[200:203]\n\t"
      "v_cvt_pk_bf16_f32 v117, v114, v115\n\t"
      "global_store_dwordx2 v245, v[116:117], s[8:9] offset:64\n\t"
      "v_mfma_f32_16x16x32_bf16 v[204:207], v[148:151], v[84:87], v[204:207]\n\t"
      "v_mfma_f32_16x16x32_bf16 v[204:207], v[152:155], v[100:103], v[204:207]\n\t"
      "s_nop 3\n\t"
      "v_cmp_u_f32_e32 vcc, v200, v201\n\t"
      "v_cmp_u_f32_e64 s[48:49], v202, v203\n\t"
      "v_mfma_f32_16x16x32_bf16 v[208:211], v[148:151], v[88:91], v[208:211]\n\t"
      "v_mfma_f32_16x16x32_bf16 v[208:211], v[152:155], v[104:107], v[208:211]\n\t"
      "s_or_b64 vcc, vcc, s[48:49]\n\t"
      "s_cbranch_scc1 NP5R_%=\n\t"
      "NP5B_%=:\n\t"
      "ds_write_b128 v243, v[200:203] offset:32768\n\t"
      "v_mfma_f32_16x16x32_bf16 v[212:215], v[148:151], v[92:95], v[212:215]\n\t"
      "global_load_dwordx4 v[148:151], v229, s[64:65] sc1\n\t"
      "v_mfma_f32_16x16x32_bf16 v[212:215], v[152:155], v[108:111], v[212:215]\n\t"
      "global_load_dwordx4 v[152:155], v230, s[64:65] sc1\n\t"
      "ds_write_b128 v220, v[204:207] offset:32768\n\t"
      "ds_write_b128 v221, v[208:211] offset:32768\n\t"
      "s_nop 4\n\t"
      "ds_write_b128 v222, v[212:215] offset:32768\n\t"
      "s_waitcnt lgkmcnt(0)\n\t"
      "s_barrier\n\t"
      "ds_read_b128 v[112:115], v244 offset:32768\n\t"
      "ds_read_b128 v[116:119], v244 offset:36864\n\t"
      "ds_read_b128 v[120:123], v244 offset:40960\n\t"
      "ds_read_b128 v[124:127], v244 offset:45056\n\t"
      "ds_read_b128 v[200:203], v244 offset:49152\n\t"
      "ds_read_b128 v[204:207], v244 offset:53248\n\t"
      "ds_read_b128 v[208:211], v244 offset:57344\n\t"
      "ds_read_b128 v[212:215], v244 offset:61440\n\t"
      "s_waitcnt vmcnt(7)\n\t"
      "v_mfma_f32_16x16x32_bf16 v[184:187], v[128:131], v[0:3], v[180:183]\n\t"
      "v_mfma_f32_16x16x32_bf16 v[188:191], v[128:131], v[4:7], 0\n\t"
      "v_mfma_f32_16x16x32_bf16 v[192:195], v[128:131], v[8:11], 0\n\t"
      "v_mfma_f32_16x16x32_bf16 v[196:199], v[128:131], v[12:15], 0\n\t"
      "s_waitcnt vmcnt(6)\n\t"
      "v_mfma_f32_16x16x32_bf16 v[184:187], v[132:135], v[16:19], v[184:187]\n\t"
      "s_waitcnt lgkmcnt(6)\n\t"
      "v_add_f32_e32 v112, v112, v116\n\t"
      "s_waitcnt vmcnt(5)\n\t"
      "v_mfma_f32_16x16x32_bf16 v[184:187], v[136:139], v[32:35], v[184:187]\n\t"
      "v_add_f32_e32 v113, v113, v117\n\t"
      "v_add_f32_e32 v114, v114, v118\n\t"
      "v_mfma_f32_16x16x32_bf16 v[188:191], v[132:135], v[20:23], v[188:191]\n\t"
      "v_add_f32_e32 v115, v115, v119\n\t"
      "s_waitcnt lgkmcnt(4)\n\t"
      "v_add_f32_e32 v120, v120, v124\n\t"
      "v_mfma_f32_16x16x32_bf16 v[188:191], v[136:139], v[36:39], v[188:191]\n\t"
      "v_add_f32_e32 v121, v121, v125\n\t"
      "v_add_f32_e32 v122, v122, v126\n\t"
      "v_mfma_f32_16x16x32_bf16 v[192:195], v[132:135], v[24:27], v[192:195]\n\t"
      "v_add_f32_e32 v123, v123, v127\n\t"
      "s_waitcnt lgkmcnt(2)\n\t"
      "v_add_f32_e32 v200, v200, v204\n\t"
      "v_mfma_f32_16x16x32_bf16 v[192:195], v[136:139], v[40:43], v[192:195]\n\t"
      "v_add_f32_e32 v201, v201, v205\n\t"
      "v_add_f32_e32 v202, v202, v206\n\t"
      "v_mfma_f32_16x16x32_bf16 v[196:199], v[132:135], v[28:31], v[196:199]\n\t"
      "v_add_f32_e32 v203, v203, v207\n\t"
      "v_add_f32_e32 v112, v112, v120\n\t"
      "v_mfma_f32_16x16x32_bf16 v[196:199], v[136:139], v[44:47], v[196:199]\n\t"
      "v_add_f32_e32 v113, v113, v121\n\t"
      "s_waitcnt vmcnt(4)\n\t"
      "v_mfma_f32_16x16x32_bf16 v[184:187], v[140:143], v[48:51], v[184:187]\n\t"
      "v_add_f32_e32 v114, v114, v122\n\t"
      "s_waitcnt vmcnt(3)\n\t"
      "v_mfma_f32_16x16x32_bf16 v[184:187], v[144:147], v[64:67], v[184:187]\n\t"
      "v_add_f32_e32 v115, v115, v123\n\t"
      "s_waitcnt lgkmcnt(0)\n\t"
      "v_add_f32_e32 v208, v208, v212\n\t"
      "v_mfma_f32_16x16x32_bf16 v[188:191], v[140:143], v[52:55], v[188:191]\n\t"
      "v_add_f32_e32 v209, v209, v213\n\t"
      "v_add_f32_e32 v210, v210, v214\n\t"
      "v_mfma_f32_16x16x32_bf16 v[188:191], v[144:147], v[68:71], v[188:191]\n\t"
      "v_add_f32_e32 v211, v211, v215\n\t"
      "v_add_f32_e32 v200, v200, v208\n\t"
      "v_mfma_f32_16x16x32_bf16 v[192:195], v[140:143], v[56:59], v[192:195]\n\t"
      "v_add_f32_e32 v201, v201, v209\n\t"
      "v_add_f32_e32 v202, v202, v210\n\t"
      "v_mfma_f32_16x16x32_bf16 v[192:195], v[144:147], v[72:75], v[192:195]\n\t"
      "v_add_f32_e32 v203, v203, v211\n\t"
      "v_add_f32_e32 v112, v112, v200\n\t"
      "v_mfma_f32_16x16x32_bf16 v[196:199], v[140:143], v[60:63], v[196:199]\n\t"
      "v_add_f32_e32 v113, v113, v201\n\t"
      "v_add_f32_e32 v114, v114, v202\n\t"
      "v_mfma_f32_16x16x32_bf16 v[196:199], v[144:147], v[76:79], v[196:199]\n\t"
      "v_add_f32_e32 v115, v115, v203\n\t"
      "s_waitcnt vmcnt(1)\n\t"
      "v_mfma_f32_16x16x32_bf16 v[184:187], v[148:151], v[80:83], v[184:187]\n\t"
      "v_cvt_pk_bf16_f32 v116, v112, v113\n\t"
      "s_waitcnt vmcnt(0)\n\t"
      "v_mfma_f32_16x16x32_bf16 v[184:187], v[152:155], v[96:99], v[184:187]\n\t"
      "v_cvt_pk_bf16_f32 v117, v114, v115\n\t"
      "global_store_dwordx2 v245, v[116:117], s[8:9] offset:96\n\t"
      "v_mfma_f32_16x16x32_bf16 v[188:191], v[148:151], v[84:87], v[188:191]\n\t"
      "v_mfma_f32_16x16x32_bf16 v[188:191], v[152:155], v[100:103], v[188:191]\n\t"
      "s_nop 3\n\t"
      "v_cmp_u_f32_e32 vcc, v184, v185\n\t"
      "v_cmp_u_f32_e64 s[48:49], v186, v187\n\t"
      "v_mfma_f32_16x16x32_bf16 v[192:195], v[148:151], v[88:91], v[192:195]\n\t"
      "v_mfma_f32_16x16x32_bf16 v[192:195], v[152:155], v[104:107], v[192:195]\n\t"
      "s_or_b64 vcc, vcc, s[48:49]\n\t"
      "s_cbranch_scc1 NP6R_%=\n\t"
      "NP6B_%=:\n\t"
      "ds_write_b128 v243, v[184:187]\n\t"
      "v_mfma_f32_16x16x32_bf16 v[196:199], v[148:151], v[92:95], v[196:199]\n\t"
      "v_mfma_f32_16x16x32_bf16 v[196:199], v[152:155], v[108:111], v[196:199]\n\t"
      "ds_write_b128 v220, v[188:191]\n\t"
      "s_nop 1\n\t"
      "ds_write_b128 v221, v[192:195]\n\t"
      "s_nop 3\n\t"
      "ds_write_b128 v222, v[196:199]\n\t"
      "s_waitcnt lgkmcnt(0)\n\t"
      "s_barrier\n\t"
      "ds_read_b128 v[112:115], v244\n\t"
      "ds_read_b128 v[116:119], v244 offset:4096\n\t"
      "ds_read_b128 v[120:123], v244 offset:8192\n\t"
      "ds_read_b128 v[124:127], v244 offset:12288\n\t"
      "ds_read_b128 v[184:187], v244 offset:16384\n\t"
      "ds_read_b128 v[188:191], v244 offset:20480\n\t"
      "ds_read_b128 v[192:195], v244 offset:24576\n\t"
      "ds_read_b128 v[196:199], v244 offset:28672\n\t"
      "s_waitcnt lgkmcnt(6)\n\t"
      "v_add_f32_e32 v112, v112, v116\n\t"
      "v_add_f32_e32 v113, v113, v117\n\t"
      "v_add_f32_e32 v114, v114, v118\n\t"
      "v_add_f32_e32 v115, v115, v119\n\t"
      "s_waitcnt lgkmcnt(4)\n\t"
      "v_add_f32_e32 v120, v120, v124\n\t"
      "v_add_f32_e32 v121, v121, v125\n\t"
      "v_add_f32_e32 v122, v122, v126\n\t"
      "v_add_f32_e32 v123, v123, v127\n\t"
      "s_waitcnt lgkmcnt(2)\n\t"
      "v_add_f32_e32 v184, v184, v188\n\t"
      "v_add_f32_e32 v185, v185, v189\n\t"
      "v_add_f32_e32 v186, v186, v190\n\t"
      "v_add_f32_e32 v187, v187, v191\n\t"
      "v_add_f32_e32 v112, v112, v120\n\t"
      "v_add_f32_e32 v113, v113, v121\n\t"
      "v_add_f32_e32 v114, v114, v122\n\t"
      "v_add_f32_e32 v115, v115, v123\n\t"
      "s_waitcnt lgkmcnt(0)\n\t"
      "v_add_f32_e32 v192, v192, v196\n\t"
      "v_add_f32_e32 v193, v193, v197\n\t"
      "v_add_f32_e32 v194, v194, v198\n\t"
      "v_add_f32_e32 v195, v195, v199\n\t"
      "v_add_f32_e32 v184, v184, v192\n\t"
      "v_add_f32_e32 v185, v185, v193\n\t"
      "v_add_f32_e32 v186, v186, v194\n\t"
      "v_add_f32_e32 v187, v187, v195\n\t"
      "v_add_f32_e32 v112, v112, v184\n\t"
      "v_add_f32_e32 v113, v113, v185\n\t"
      "v_add_f32_e32 v114, v114, v186\n\t"
      "v_add_f32_e32 v115, v115, v187\n\t"
      "v_cvt_pk_bf16_f32 v116, v112, v113\n\t"
      "v_cvt_pk_bf16_f32 v117, v114, v115\n\t"
      "global_store_dwordx2 v245, v[116:117], s[66:67] offset:64\n\t"
      "GX_%=:\n\t"
      "s_bitcmp1_b32 s16, 5\n\t"
      "s_cbranch_scc0 EP_%=\n\t"
      "v_mov_b32_e32 v0, s26\n\t"
      "v_mov_b32_e32 v1, s27\n\t"
      "v_mov_b32_e32 v2, 0\n\t"
      "s_mov_b64 exec, 1\n\t"
      "global_store_dwordx2 v2, v[0:1], s[24:25] sc1\n\t"
      "s_mov_b64 exec, -1\n\t"
      "EP_%=:\n\t"
      "s_endpgm\n\t"
      ".p2align 6\n\t"
      "GQ_%=:\n\t"
      "s_waitcnt vmcnt(6)\n\t"
      "v_mfma_f32_16x16x32_bf16 v[184:187], v[128:131], v[0:3], 0\n\t"
      "s_waitcnt vmcnt(5)\n\t"
      "v_cmp_eq_u32_e32 vcc, -1, v132\n\t"
      "v_mfma_f32_16x16x32_bf16 v[188:191], v[128:131], v[4:7], 0\n\t"
      "v_mfma_f32_16x16x32_bf16 v[192:195], v[128:131], v[8:11], 0\n\t"
      "s_cbranch_vccnz FQ1R_%=\n\t"
      "FQ1B_%=:\n\t"
      "v_mfma_f32_16x16x32_bf16 v[196:199], v[128:131], v[12:15], 0\n\t"
      "global_load_dwordx4 v[128:131], v224, s[54:55] sc1\n\t"
      "v_mfma_f32_16x16x32_bf16 v[184:187], v[132:135], v[16:19], v[184:187]\n\t"
      "s_waitcnt vmcnt(5)\n\t"
      "v_cmp_eq_u32_e32 vcc, -1, v136\n\t"
      "v_mfma_f32_16x16x32_bf16 v[188:191], v[132:135], v[20:23], v[188:191]\n\t"
      "v_mfma_f32_16x16x32_bf16 v[192:195], v[132:135], v[24:27], v[192:195]\n\t"
      "s_cbranch_vccnz FQ2R_%=\n\t"
      "FQ2B_%=:\n\t"
      "v_mfma_f32_16x16x32_bf16 v[196:199], v[132:135], v[28:31], v[196:199]\n\t"
      "global_load_dwordx4 v[132:135], v225, s[54:55] sc1\n\t"
      "v_mfma_f32_16x16x32_bf16 v[184:187], v[136:139], v[32:35], v[184:187]\n\t"
      "s_waitcnt vmcnt(5)\n\t"
      "v_cmp_eq_u32_e32 vcc, -1, v140\n\t"
      "v_mfma_f32_16x16x32_bf16 v[188:191], v[136:139], v[36:39], v[188:191]\n\t"
      "v_mfma_f32_16x16x32_bf16 v[192:195], v[136:139], v[40:43], v[192:195]\n\t"
      "s_cbranch_vccnz FQ3R_%=\n\t"
      "FQ3B_%=:\n\t"
      "v_mfma_f32_16x16x32_bf16 v[196:199], v[136:139], v[44:47], v[196:199]\n\t"
      "global_load_dwordx4 v[136:139], v226, s[54:55] sc1\n\t"
      "v_mfma_f32_16x16x32_bf16 v[184:187], v[140:143], v[48:51], v[184:187]\n\t"
      "s_waitcnt vmcnt(5)\n\t"
      "v_cmp_eq_u32_e32 vcc, -1, v144\n\t"
      "v_mfma_f32_16x16x32_bf16 v[188:191], v[140:143], v[52:55], v[188:191]\n\t"
      "v_mfma_f32_16x16x32_bf16 v[192:195], v[140:143], v[56:59], v[192:195]\n\t"
      "s_cbranch_vccnz FQ4R_%=\n\t"
      "FQ4B_%=:\n\t"
      "v_mfma_f32_16x16x32_bf16 v[196:199], v[140:143], v[60:63], v[196:199]\n\t"
      "global_load_dwordx4 v[140:143], v227, s[54:55] sc1\n\t"
      "v_mfma_f32_16x16x32_bf16 v[184:187], v[144:147], v[64:67], v[184:187]\n\t"
      "s_waitcnt vmcnt(5)\n\t"
      "v_cmp_eq_u32_e32 vcc, -1, v148\n\t"
      "v_mfma_f32_16x16x32_bf16 v[188:191], v[144:147], v[68:71], v[188:191]\n\t"
      "v_mfma_f32_16x16x32_bf16 v[192:195], v[144:147], v[72:75], v[192:195]\n\t"
      "s_cbranch_vccnz FQ5R_%=\n\t"
      "FQ5B_%=:\n\t"
      "v_mfma_f32_16x16x32_bf16 v[196:199], v[144:147], v[76:79], v[196:199]\n\t"
      "global_load_dwordx4 v[144:147], v228, s[54:55] sc1\n\t"
      "v_mfma_f32_16x16x32_bf16 v[184:187], v[148:151], v[80:83], v[184:187]\n\t"
      "s_waitcnt vmcnt(5)\n\t"
      "v_cmp_eq_u32_e32 vcc, -1, v152\n\t"
      "v_mfma_f32_16x16x32_bf16 v[188:191], v[148:151], v[84:87], v[188:191]\n\t"
      "v_mfma_f32_16x16x32_bf16 v[192:195], v[148:151], v[88:91], v[192:195]\n\t"
      "s_cbranch_vccnz FQ6R_%=\n\t"
      "FQ6B_%=:\n\t"
      "v_mfma_f32_16x16x32_bf16 v[196:199], v[148:151], v[92:95], v[196:199]\n\t"
      "global_load_dwordx4 v[148:151], v229, s[54:55] sc1\n\t"
      "v_mfma_f32_16x16x32_bf16 v[184:187], v[152:155], v[96:99], v[184:187]\n\t"
      "s_waitcnt vmcnt(5)\n\t"
      "v_cmp_eq_u32_e32 vcc, -1, v128\n\t"
      "v_mfma_f32_16x16x32_bf16 v[188:191], v[152:155], v[100:103], v[188:191]\n\t"
      "v_mfma_f32_16x16x32_bf16 v[192:195], v[152:155], v[104:107], v[192:195]\n\t"
      "s_cbranch_vccnz FQ7R_%=\n\t"
      "FQ7B_%=:\n\t"
      "v_mfma_f32_16x16x32_bf16 v[196:199], v[152:155], v[108:111], v[196:199]\n\t"
      "global_load_dwordx4 v[152:155], v230, s[54:55] sc1\n\t"
      "s_nop 0\n\t"
      "v_cmp_u_f32_e32 vcc, v184, v185\n\t"
      "v_cmp_u_f32_e64 s[48:49], v186, v187\n\t"
      "s_nop 0\n\t"
      "s_or_b64 vcc, vcc, s[48:49]\n\t"
      "s_cbranch_scc1 NQ0R_%=\n\t"
      "NQ0B_%=:\n\t"
      "ds_write_b128 v243, v[184:187]\n\t"
      "ds_write_b128 v220, v[188:191]\n\t"
      "ds_write_b128 v221, v[192:195]\n\t"
      "ds_write_b128 v222, v[196:199]\n\t"
      "v_mfma_f32_16x16x32_bf16 v[200:203], v[128:131], v[0:3], 0\n\t"
      "s_waitcnt vmcnt(5)\n\t"
      "v_cmp_eq_u32_e32 vcc, -1, v132\n\t"
      "v_mfma_f32_16x16x32_bf16 v[204:207], v[128:131], v[4:7], 0\n\t"
      "v_mfma_f32_16x16x32_bf16 v[208:211], v[128:131], v[8:11], 0\n\t"
      "s_cbranch_vccnz FQ8R_%=\n\t"
      "FQ8B_%=:\n\t"
      "v_mfma_f32_16x16x32_bf16 v[212:215], v[128:131], v[12:15], 0\n\t"
      "global_load_dwordx4 v[128:131], v224, s[56:57] sc1\n\t"
      "v_mfma_f32_16x16x32_bf16 v[200:203], v[132:135], v[16:19], v[200:203]\n\t"
      "s_waitcnt vmcnt(5)\n\t"
      "v_cmp_eq_u32_e32 vcc, -1, v136\n\t"
      "v_mfma_f32_16x16x32_bf16 v[204:207], v[132:135], v[20:23], v[204:207]\n\t"
      "v_mfma_f32_16x16x32_bf16 v[208:211], v[132:135], v[24:27], v[208:211]\n\t"
      "s_cbranch_vccnz FQ9R_%=\n\t"
      "FQ9B_%=:\n\t"
      "v_mfma_f32_16x16x32_bf16 v[212:215], v[132:135], v[28:31], v[212:215]\n\t"
      "global_load_dwordx4 v[132:135], v225, s[56:57] sc1\n\t"
      "v_mfma_f32_16x16x32_bf16 v[200:203], v[136:139], v[32:35], v[200:203]\n\t"
      "s_waitcnt vmcnt(5)\n\t"
      "v_cmp_eq_u32_e32 vcc, -1, v140\n\t"
      "v_mfma_f32_16x16x32_bf16 v[204:207], v[136:139], v[36:39], v[204:207]\n\t"
      "v_mfma_f32_16x16x32_bf16 v[208:211], v[136:139], v[40:43], v[208:211]\n\t"
      "s_cbranch_vccnz FQ10R_%=\n\t"
      "FQ10B_%=:\n\t"
      "v_mfma_f32_16x16x32_bf16 v[212:215], v[136:139], v[44:47], v[212:215]\n\t"
      "global_load_dwordx4 v[136:139], v226, s[56:57] sc1\n\t"
      "v_mfma_f32_16x16x32_bf16 v[200:203], v[140:143], v[48:51], v[200:203]\n\t"
      "s_waitcnt vmcnt(5)\n\t"
      "v_cmp_eq_u32_e32 vcc, -1, v144\n\t"
      "v_mfma_f32_16x16x32_bf16 v[204:207], v[140:143], v[52:55], v[204:207]\n\t"
      "v_mfma_f32_16x16x32_bf16 v[208:211], v[140:143], v[56:59], v[208:211]\n\t"
      "s_cbranch_vccnz FQ11R_%=\n\t"
      "FQ11B_%=:\n\t"
      "v_mfma_f32_16x16x32_bf16 v[212:215], v[140:143], v[60:63], v[212:215]\n\t"
      "global_load_dwordx4 v[140:143], v227, s[56:57] sc1\n\t"
      "v_mfma_f32_16x16x32_bf16 v[200:203], v[144:147], v[64:67], v[200:203]\n\t"
      "s_waitcnt vmcnt(5)\n\t"
      "v_cmp_eq_u32_e32 vcc, -1, v148\n\t"
      "v_mfma_f32_16x16x32_bf16 v[204:207], v[144:147], v[68:71], v[204:207]\n\t"
      "v_mfma_f32_16x16x32_bf16 v[208:211], v[144:147], v[72:75], v[208:211]\n\t"
      "s_cbranch_vccnz FQ12R_%=\n\t"
      "FQ12B_%=:\n\t"
      "v_mfma_f32_16x16x32_bf16 v[212:215], v[144:147], v[76:79], v[212:215]\n\t"
      "global_load_dwordx4 v[144:147], v228, s[56:57] sc1\n\t"
      "s_waitcnt lgkmcnt(0)\n\t"
      "s_barrier\n\t"
      "s_setprio 2\n\t"
      "v_mfma_f32_16x16x32_bf16 v[200:203], v[148:151], v[80:83], v[200:203]\n\t"
      "s_waitcnt vmcnt(5)\n\t"
      "v_cmp_eq_u32_e32 vcc, -1, v152\n\t"
      "v_mfma_f32_16x16x32_bf16 v[204:207], v[148:151], v[84:87], v[204:207]\n\t"
      "v_mfma_f32_16x16x32_bf16 v[208:211], v[148:151], v[88:91], v[208:211]\n\t"
      "s_cbranch_vccnz FQ13R_%=\n\t"
      "FQ13B_%=:\n\t"
      "v_mfma_f32_16x16x32_bf16 v[212:215], v[148:151], v[92:95], v[212:215]\n\t"
      "global_load_dwordx4 v[148:151], v229, s[56:57] sc1\n\t"
      "v_mfma_f32_16x16x32_bf16 v[200:203], v[152:155], v[96:99], v[200:203]\n\t"
      "s_waitcnt vmcnt(5)\n\t"
      "v_cmp_eq_u32_e32 vcc, -1, v128\n\t"
      "v_mfma_f32_16x16x32_bf16 v[204:207], v[152:155], v[100:103], v[204:207]\n\t"
      "v_mfma_f32_16x16x32_bf16 v[208:211], v[152:155], v[104:107], v[208:211]\n\t"
      "s_cbranch_vccnz FQ14R_%=\n\t"
      "FQ14B_%=:\n\t"
      "v_mfma_f32_16x16x32_bf16 v[212:215], v[152:155], v[108:111], v[212:215]\n\t"
      "global_load_dwordx4 v[152:155], v230, s[56:57] sc1\n\t"
      "s_setprio 0\n\t"
      "v_cmp_u_f32_e32 vcc, v200, v201\n\t"
      "v_cmp_u_f32_e64 s[48:49], v202, v203\n\t"
      "s_nop 0\n\t"
      "s_or_b64 vcc, vcc, s[48:49]\n\t"
      "s_cbranch_scc1 NQ1R_%=\n\t"
      "NQ1B_%=:\n\t"
      "ds_write_b128 v243, v[200:203] offset:32768\n\t"
      "ds_write_b128 v220, v[204:207] offset:32768\n\t"
      "ds_write_b128 v221, v[208:211] offset:32768\n\t"
      "ds_write_b128 v222, v[212:215] offset:32768\n\t"
      "v_mfma_f32_16x16x32_bf16 v[184:187], v[128:131], v[0:3], 0\n\t"
      "s_waitcnt vmcnt(5)\n\t"
      "v_cmp_eq_u32_e32 vcc, -1, v132\n\t"
      "v_mfma_f32_16x16x32_bf16 v[188:191], v[128:131], v[4:7], 0\n\t"
      "v_mfma_f32_16x16x32_bf16 v[192:195], v[128:131], v[8:11], 0\n\t"
      "s_cbranch_vccnz FQ15R_%=\n\t"
      "FQ15B_%=:\n\t"
      "v_mfma_f32_16x16x32_bf16 v[196:199], v[128:131], v[12:15], 0\n\t"
      "global_load_dwordx4 v[128:131], v224, s[58:59] sc1\n\t"
      "v_mfma_f32_16x16x32_bf16 v[184:187], v[132:135], v[16:19], v[184:187]\n\t"
      "s_waitcnt vmcnt(5)\n\t"
      "v_cmp_eq_u32_e32 vcc, -1, v136\n\t"
      "v_mfma_f32_16x16x32_bf16 v[188:191], v[132:135], v[20:23], v[188:191]\n\t"
      "v_mfma_f32_16x16x32_bf16 v[192:195], v[132:135], v[24:27], v[192:195]\n\t"
      "s_cbranch_vccnz FQ16R_%=\n\t"
      "FQ16B_%=:\n\t"
      "v_mfma_f32_16x16x32_bf16 v[196:199], v[132:135], v[28:31], v[196:199]\n\t"
      "global_load_dwordx4 v[132:135], v225, s[58:59] sc1\n\t"
      "v_mfma_f32_16x16x32_bf16 v[184:187], v[136:139], v[32:35], v[184:187]\n\t"
      "s_waitcnt vmcnt(5)\n\t"
      "v_cmp_eq_u32_e32 vcc, -1, v140\n\t"
      "v_mfma_f32_16x16x32_bf16 v[188:191], v[136:139], v[36:39], v[188:191]\n\t"
      "v_mfma_f32_16x16x32_bf16 v[192:195], v[136:139], v[40:43], v[192:195]\n\t"
      "s_cbranch_vccnz FQ17R_%=\n\t"
      "FQ17B_%=:\n\t"
      "v_mfma_f32_16x16x32_bf16 v[196:199], v[136:139], v[44:47], v[196:199]\n\t"
      "global_load_dwordx4 v[136:139], v226, s[58:59] sc1\n\t"
      "v_mfma_f32_16x16x32_bf16 v[184:187], v[140:143], v[48:51], v[184:187]\n\t"
      "s_waitcnt vmcnt(5)\n\t"
      "v_cmp_eq_u32_e32 vcc, -1, v144\n\t"
      "v_mfma_f32_16x16x32_bf16 v[188:191], v[140:143], v[52:55], v[188:191]\n\t"
      "v_mfma_f32_16x16x32_bf16 v[192:195], v[140:143], v[56:59], v[192:195]\n\t"
      "s_cbranch_vccnz FQ18R_%=\n\t"
      "FQ18B_%=:\n\t"
      "v_mfma_f32_16x16x32_bf16 v[196:199], v[140:143], v[60:63], v[196:199]\n\t"
      "global_load_dwordx4 v[140:143], v227, s[58:59] sc1\n\t"
      "v_mfma_f32_16x16x32_bf16 v[184:187], v[144:147], v[64:67], v[184:187]\n\t"
      "s_waitcnt vmcnt(5)\n\t"
      "v_cmp_eq_u32_e32 vcc, -1, v148\n\t"
      "v_mfma_f32_16x16x32_bf16 v[188:191], v[144:147], v[68:71], v[188:191]\n\t"
      "v_mfma_f32_16x16x32_bf16 v[192:195], v[144:147], v[72:75], v[192:195]\n\t"
      "s_cbranch_vccnz FQ19R_%=\n\t"
      "FQ19B_%=:\n\t"
      "v_mfma_f32_16x16x32_bf16 v[196:199], v[144:147], v[76:79], v[196:199]\n\t"
      "global_load_dwordx4 v[144:147], v228, s[58:59] sc1\n\t"
      "s_waitcnt lgkmcnt(0)\n\t"
      "s_barrier\n\t"
      "s_setprio 2\n\t"
      "v_mfma_f32_16x16x32_bf16 v[184:187], v[148:151], v[80:83], v[184:187]\n\t"
      "s_waitcnt vmcnt(5)\n\t"
      "v_cmp_eq_u32_e32 vcc, -1, v152\n\t"
      "v_mfma_f32_16x16x32_bf16 v[188:191], v[148:151], v[84:87], v[188:191]\n\t"
      "v_mfma_f32_16x16x32_bf16 v[192:195], v[148:151], v[88:91], v[192:195]\n\t"
      "s_cbranch_vccnz FQ20R_%=\n\t"
      "FQ20B_%=:\n\t"
      "v_mfma_f32_16x16x32_bf16 v[196:199], v[148:151], v[92:95], v[196:199]\n\t"
      "global_load_dwordx4 v[148:151], v229, s[58:59] sc1\n\t"
      "v_mfma_f32_16x16x32_bf16 v[184:187], v[152:155], v[96:99], v[184:187]\n\t"
      "s_waitcnt vmcnt(5)\n\t"
      "v_cmp_eq_u32_e32 vcc, -1, v128\n\t"
      "v_mfma_f32_16x16x32_bf16 v[188:191], v[152:155], v[100:103], v[188:191]\n\t"
      "v_mfma_f32_16x16x32_bf16 v[192:195], v[152:155], v[104:107], v[192:195]\n\t"
      "s_cbranch_vccnz FQ21R_%=\n\t"
      "FQ21B_%=:\n\t"
      "v_mfma_f32_16x16x32_bf16 v[196:199], v[152:155], v[108:111], v[196:199]\n\t"
      "global_load_dwordx4 v[152:155], v230, s[58:59] sc1\n\t"
      "s_setprio 0\n\t"
      "v_cmp_u_f32_e32 vcc, v184, v185\n\t"
      "v_cmp_u_f32_e64 s[48:49], v186, v187\n\t"
      "s_nop 0\n\t"
      "s_or_b64 vcc, vcc, s[48:49]\n\t"
      "s_cbranch_scc1 NQ2R_%=\n\t"
      "NQ2B_%=:\n\t"
      "ds_write_b128 v243, v[184:187]\n\t"
      "ds_write_b128 v220, v[188:191]\n\t"
      "ds_write_b128 v221, v[192:195]\n\t"
      "ds_write_b128 v222, v[196:199]\n\t"
      "v_mfma_f32_16x16x32_bf16 v[200:203], v[128:131], v[0:3], 0\n\t"
      "s_waitcnt vmcnt(5)\n\t"
      "v_cmp_eq_u32_e32 vcc, -1, v132\n\t"
      "v_mfma_f32_16x16x32_bf16 v[204:207], v[128:131], v[4:7], 0\n\t"
      "v_mfma_f32_16x16x32_bf16 v[208:211], v[128:131], v[8:11], 0\n\t"
      "s_cbranch_vccnz FQ22R_%=\n\t"
      "FQ22B_%=:\n\t"
      "v_mfma_f32_16x16x32_bf16 v[212:215], v[128:131], v[12:15], 0\n\t"
      "global_load_dwordx4 v[128:131], v224, s[60:61] sc1\n\t"
      "v_mfma_f32_16x16x32_bf16 v[200:203], v[132:135], v[16:19], v[200:203]\n\t"
      "s_waitcnt vmcnt(5)\n\t"
      "v_cmp_eq_u32_e32 vcc, -1, v136\n\t"
      "v_mfma_f32_16x16x32_bf16 v[204:207], v[132:135], v[20:23], v[204:207]\n\t"
      "v_mfma_f32_16x16x32_bf16 v[208:211], v[132:135], v[24:27], v[208:211]\n\t"
      "s_cbranch_vccnz FQ23R_%=\n\t"
      "FQ23B_%=:\n\t"
      "v_mfma_f32_16x16x32_bf16 v[212:215], v[132:135], v[28:31], v[212:215]\n\t"
      "global_load_dwordx4 v[132:135], v225, s[60:61] sc1\n\t"
      "v_mfma_f32_16x16x32_bf16 v[200:203], v[136:139], v[32:35], v[200:203]\n\t"
      "s_waitcnt vmcnt(5)\n\t"
      "v_cmp_eq_u32_e32 vcc, -1, v140\n\t"
      "v_mfma_f32_16x16x32_bf16 v[204:207], v[136:139], v[36:39], v[204:207]\n\t"
      "v_mfma_f32_16x16x32_bf16 v[208:211], v[136:139], v[40:43], v[208:211]\n\t"
      "s_cbranch_vccnz FQ24R_%=\n\t"
      "FQ24B_%=:\n\t"
      "v_mfma_f32_16x16x32_bf16 v[212:215], v[136:139], v[44:47], v[212:215]\n\t"
      "global_load_dwordx4 v[136:139], v226, s[60:61] sc1\n\t"
      "v_mfma_f32_16x16x32_bf16 v[200:203], v[140:143], v[48:51], v[200:203]\n\t"
      "s_waitcnt vmcnt(5)\n\t"
      "v_cmp_eq_u32_e32 vcc, -1, v144\n\t"
      "v_mfma_f32_16x16x32_bf16 v[204:207], v[140:143], v[52:55], v[204:207]\n\t"
      "v_mfma_f32_16x16x32_bf16 v[208:211], v[140:143], v[56:59], v[208:211]\n\t"
      "s_cbranch_vccnz FQ25R_%=\n\t"
      "FQ25B_%=:\n\t"
      "v_mfma_f32_16x16x32_bf16 v[212:215], v[140:143], v[60:63], v[212:215]\n\t"
      "global_load_dwordx4 v[140:143], v227, s[60:61] sc1\n\t"
      "v_mfma_f32_16x16x32_bf16 v[200:203], v[144:147], v[64:67], v[200:203]\n\t"
      "s_waitcnt vmcnt(5)\n\t"
      "v_cmp_eq_u32_e32 vcc, -1, v148\n\t"
      "v_mfma_f32_16x16x32_bf16 v[204:207], v[144:147], v[68:71], v[204:207]\n\t"
      "v_mfma_f32_16x16x32_bf16 v[208:211], v[144:147], v[72:75], v[208:211]\n\t"
      "s_cbranch_vccnz FQ26R_%=\n\t"
      "FQ26B_%=:\n\t"
      "v_mfma_f32_16x16x32_bf16 v[212:215], v[144:147], v[76:79], v[212:215]\n\t"
      "global_load_dwordx4 v[144:147], v228, s[60:61] sc1\n\t"
      "s_waitcnt lgkmcnt(0)\n\t"
      "s_barrier\n\t"
      "s_setprio 2\n\t"
      "v_mfma_f32_16x16x32_bf16 v[200:203], v[148:151], v[80:83], v[200:203]\n\t"
      "s_waitcnt vmcnt(5)\n\t"
      "v_cmp_eq_u32_e32 vcc, -1, v152\n\t"
      "v_mfma_f32_16x16x32_bf16 v[204:207], v[148:151], v[84:87], v[204:207]\n\t"
      "v_mfma_f32_16x16x32_bf16 v[208:211], v[148:151], v[88:91], v[208:211]\n\t"
      "s_cbranch_vccnz FQ27R_%=\n\t"
      "FQ27B_%=:\n\t"
      "v_mfma_f32_16x16x32_bf16 v[212:215], v[148:151], v[92:95], v[212:215]\n\t"
      "global_load_dwordx4 v[148:151], v229, s[60:61] sc1\n\t"
      "v_mfma_f32_16x16x32_bf16 v[200:203], v[152:155], v[96:99], v[200:203]\n\t"
      "s_waitcnt vmcnt(5)\n\t"
      "v_cmp_eq_u32_e32 vcc, -1, v128\n\t"
      "v_mfma_f32_16x16x32_bf16 v[204:207], v[152:155], v[100:103], v[204:207]\n\t"
      "v_mfma_f32_16x16x32_bf16 v[208:211], v[152:155], v[104:107], v[208:211]\n\t"
      "s_cbranch_vccnz FQ28R_%=\n\t"
      "FQ28B_%=:\n\t"
      "v_mfma_f32_16x16x32_bf16 v[212:215], v[152:155], v[108:111], v[212:215]\n\t"
      "global_load_dwordx4 v[152:155], v230, s[60:61] sc1\n\t"
      "s_setprio 0\n\t"
      "v_cmp_u_f32_e32 vcc, v200, v201\n\t"
      "v_cmp_u_f32_e64 s[48:49], v202, v203\n\t"
      "s_nop 0\n\t"
      "s_or_b64 vcc, vcc, s[48:49]\n\t"
      "s_cbranch_scc1 NQ3R_%=\n\t"
      "NQ3B_%=:\n\t"
      "ds_write_b128 v243, v[200:203] offset:32768\n\t"
      "ds_write_b128 v220, v[204:207] offset:32768\n\t"
      "ds_write_b128 v221, v[208:211] offset:32768\n\t"
      "ds_write_b128 v222, v[212:215] offset:32768\n\t"
      "v_mfma_f32_16x16x32_bf16 v[184:187], v[128:131], v[0:3], 0\n\t"
      "s_waitcnt vmcnt(5)\n\t"
      "v_cmp_eq_u32_e32 vcc, -1, v132\n\t"
      "v_mfma_f32_16x16x32_bf16 v[188:191], v[128:131], v[4:7], 0\n\t"
      "v_mfma_f32_16x16x32_bf16 v[192:195], v[128:131], v[8:11], 0\n\t"
      "s_cbranch_vccnz FQ29R_%=\n\t"
      "FQ29B_%=:\n\t"
      "v_mfma_f32_16x16x32_bf16 v[196:199], v[128:131], v[12:15], 0\n\t"
      "global_load_dwordx4 v[128:131], v224, s[62:63] sc1\n\t"
      "v_mfma_f32_16x16x32_bf16 v[184:187], v[132:135], v[16:19], v[184:187]\n\t"
      "s_waitcnt vmcnt(5)\n\t"
      "v_cmp_eq_u32_e32 vcc, -1, v136\n\t"
      "v_mfma_f32_16x16x32_bf16 v[188:191], v[132:135], v[20:23], v[188:191]\n\t"
      "v_mfma_f32_16x16x32_bf16 v[192:195], v[132:135], v[24:27], v[192:195]\n\t"
      "s_cbranch_vccnz FQ30R_%=\n\t"
      "FQ30B_%=:\n\t"
      "v_mfma_f32_16x16x32_bf16 v[196:199], v[132:135], v[28:31], v[196:199]\n\t"
      "global_load_dwordx4 v[132:135], v225, s[62:63] sc1\n\t"
      "v_mfma_f32_16x16x32_bf16 v[184:187], v[136:139], v[32:35], v[184:187]\n\t"
      "s_waitcnt vmcnt(5)\n\t"
      "v_cmp_eq_u32_e32 vcc, -1, v140\n\t"
      "v_mfma_f32_16x16x32_bf16 v[188:191], v[136:139], v[36:39], v[188:191]\n\t"
      "v_mfma_f32_16x16x32_bf16 v[192:195], v[136:139], v[40:43], v[192:195]\n\t"
      "s_cbranch_vccnz FQ31R_%=\n\t"
      "FQ31B_%=:\n\t"
      "v_mfma_f32_16x16x32_bf16 v[196:199], v[136:139], v[44:47], v[196:199]\n\t"
      "global_load_dwordx4 v[136:139], v226, s[62:63] sc1\n\t"
      "v_mfma_f32_16x16x32_bf16 v[184:187], v[140:143], v[48:51], v[184:187]\n\t"
      "s_waitcnt vmcnt(5)\n\t"
      "v_cmp_eq_u32_e32 vcc, -1, v144\n\t"
      "v_mfma_f32_16x16x32_bf16 v[188:191], v[140:143], v[52:55], v[188:191]\n\t"
      "v_mfma_f32_16x16x32_bf16 v[192:195], v[140:143], v[56:59], v[192:195]\n\t"
      "s_cbranch_vccnz FQ32R_%=\n\t"
      "FQ32B_%=:\n\t"
      "v_mfma_f32_16x16x32_bf16 v[196:199], v[140:143], v[60:63], v[196:199]\n\t"
      "global_load_dwordx4 v[140:143], v227, s[62:63] sc1\n\t"
      "v_mfma_f32_16x16x32_bf16 v[184:187], v[144:147], v[64:67], v[184:187]\n\t"
      "s_waitcnt vmcnt(5)\n\t"
      "v_cmp_eq_u32_e32 vcc, -1, v148\n\t"
      "v_mfma_f32_16x16x32_bf16 v[188:191], v[144:147], v[68:71], v[188:191]\n\t"
      "v_mfma_f32_16x16x32_bf16 v[192:195], v[144:147], v[72:75], v[192:195]\n\t"
      "s_cbranch_vccnz FQ33R_%=\n\t"
      "FQ33B_%=:\n\t"
      "v_mfma_f32_16x16x32_bf16 v[196:199], v[144:147], v[76:79], v[196:199]\n\t"
      "global_load_dwordx4 v[144:147], v228, s[62:63] sc1\n\t"
      "s_waitcnt lgkmcnt(0)\n\t"
      "s_barrier\n\t"
      "s_setprio 2\n\t"
      "v_mfma_f32_16x16x32_bf16 v[184:187], v[148:151], v[80:83], v[184:187]\n\t"
      "s_waitcnt vmcnt(5)\n\t"
      "v_cmp_eq_u32_e32 vcc, -1, v152\n\t"
      "v_mfma_f32_16x16x32_bf16 v[188:191], v[148:151], v[84:87], v[188:191]\n\t"
      "v_mfma_f32_16x16x32_bf16 v[192:195], v[148:151], v[88:91], v[192:195]\n\t"
      "s_cbranch_vccnz FQ34R_%=\n\t"
      "FQ34B_%=:\n\t"
      "v_mfma_f32_16x16x32_bf16 v[196:199], v[148:151], v[92:95], v[196:199]\n\t"
      "global_load_dwordx4 v[148:151], v229, s[62:63] sc1\n\t"
      "v_mfma_f32_16x16x32_bf16 v[184:187], v[152:155], v[96:99], v[184:187]\n\t"
      "s_waitcnt vmcnt(5)\n\t"
      "v_cmp_eq_u32_e32 vcc, -1, v128\n\t"
      "v_mfma_f32_16x16x32_bf16 v[188:191], v[152:155], v[100:103], v[188:191]\n\t"
      "v_mfma_f32_16x16x32_bf16 v[192:195], v[152:155], v[104:107], v[192:195]\n\t"
      "s_cbranch_vccnz FQ35R_%=\n\t"
      "FQ35B_%=:\n\t"
      "v_mfma_f32_16x16x32_bf16 v[196:199], v[152:155], v[108:111], v[196:199]\n\t"
      "global_load_dwordx4 v[152:155], v230, s[62:63] sc1\n\t"
      "s_setprio 0\n\t"
      "v_cmp_u_f32_e32 vcc, v184, v185\n\t"
      "v_cmp_u_f32_e64 s[48:49], v186, v187\n\t"
      "s_nop 0\n\t"
      "s_or_b64 vcc, vcc, s[48:49]\n\t"
      "s_cbranch_scc1 NQ4R_%=\n\t"
      "NQ4B_%=:\n\t"
      "ds_write_b128 v243, v[184:187]\n\t"
      "ds_write_b128 v220, v[188:191]\n\t"
      "ds_write_b128 v221, v[192:195]\n\t"
      "ds_write_b128 v222, v[196:199]\n\t"
      "v_mfma_f32_16x16x32_bf16 v[200:203], v[128:131], v[0:3], 0\n\t"
      "s_waitcnt vmcnt(5)\n\t"
      "v_cmp_eq_u32_e32 vcc, -1, v132\n\t"
      "v_mfma_f32_16x16x32_bf16 v[204:207], v[128:131], v[4:7], 0\n\t"
      "v_mfma_f32_16x16x32_bf16 v[208:211], v[128:131], v[8:11], 0\n\t"
      "s_cbranch_vccnz FQ36R_%=\n\t"
      "FQ36B_%=:\n\t"
      "v_mfma_f32_16x16x32_bf16 v[212:215], v[128:131], v[12:15], 0\n\t"
      "global_load_dwordx4 v[128:131], v224, s[64:65] sc1\n\t"
      "v_mfma_f32_16x16x32_bf16 v[200:203], v[132:135], v[16:19], v[200:203]\n\t"
      "s_waitcnt vmcnt(5)\n\t"
      "v_cmp_eq_u32_e32 vcc, -1, v136\n\t"
      "v_mfma_f32_16x16x32_bf16 v[204:207], v[132:135], v[20:23], v[204:207]\n\t"
      "v_mfma_f32_16x16x32_bf16 v[208:211], v[132:135], v[24:27], v[208:211]\n\t"
      "s_cbranch_vccnz FQ37R_%=\n\t"
      "FQ37B_%=:\n\t"
      "v_mfma_f32_16x16x32_bf16 v[212:215], v[132:135], v[28:31], v[212:215]\n\t"
      "global_load_dwordx4 v[132:135], v225, s[64:65] sc1\n\t"
      "v_mfma_f32_16x16x32_bf16 v[200:203], v[136:139], v[32:35], v[200:203]\n\t"
      "s_waitcnt vmcnt(5)\n\t"
      "v_cmp_eq_u32_e32 vcc, -1, v140\n\t"
      "v_mfma_f32_16x16x32_bf16 v[204:207], v[136:139], v[36:39], v[204:207]\n\t"
      "v_mfma_f32_16x16x32_bf16 v[208:211], v[136:139], v[40:43], v[208:211]\n\t"
      "s_cbranch_vccnz FQ38R_%=\n\t"
      "FQ38B_%=:\n\t"
      "v_mfma_f32_16x16x32_bf16 v[212:215], v[136:139], v[44:47], v[212:215]\n\t"
      "global_load_dwordx4 v[136:139], v226, s[64:65] sc1\n\t"
      "v_mfma_f32_16x16x32_bf16 v[200:203], v[140:143], v[48:51], v[200:203]\n\t"
      "s_waitcnt vmcnt(5)\n\t"
      "v_cmp_eq_u32_e32 vcc, -1, v144\n\t"
      "v_mfma_f32_16x16x32_bf16 v[204:207], v[140:143], v[52:55], v[204:207]\n\t"
      "v_mfma_f32_16x16x32_bf16 v[208:211], v[140:143], v[56:59], v[208:211]\n\t"
      "s_cbranch_vccnz FQ39R_%=\n\t"
      "FQ39B_%=:\n\t"
      "v_mfma_f32_16x16x32_bf16 v[212:215], v[140:143], v[60:63], v[212:215]\n\t"
      "global_load_dwordx4 v[140:143], v227, s[64:65] sc1\n\t"
      "v_mfma_f32_16x16x32_bf16 v[200:203], v[144:147], v[64:67], v[200:203]\n\t"
      "s_waitcnt vmcnt(5)\n\t"
      "v_cmp_eq_u32_e32 vcc, -1, v148\n\t"
      "v_mfma_f32_16x16x32_bf16 v[204:207], v[144:147], v[68:71], v[204:207]\n\t"
      "v_mfma_f32_16x16x32_bf16 v[208:211], v[144:147], v[72:75], v[208:211]\n\t"
      "s_cbranch_vccnz FQ40R_%=\n\t"
      "FQ40B_%=:\n\t"
      "v_mfma_f32_16x16x32_bf16 v[212:215], v[144:147], v[76:79], v[212:215]\n\t"
      "global_load_dwordx4 v[144:147], v228, s[64:65] sc1\n\t"
      "s_waitcnt lgkmcnt(0)\n\t"
      "s_barrier\n\t"
      "s_setprio 2\n\t"
      "v_mfma_f32_16x16x32_bf16 v[200:203], v[148:151], v[80:83], v[200:203]\n\t"
      "s_waitcnt vmcnt(5)\n\t"
      "v_cmp_eq_u32_e32 vcc, -1, v152\n\t"
      "v_mfma_f32_16x16x32_bf16 v[204:207], v[148:151], v[84:87], v[204:207]\n\t"
      "v_mfma_f32_16x16x32_bf16 v[208:211], v[148:151], v[88:91], v[208:211]\n\t"
      "s_cbranch_vccnz FQ41R_%=\n\t"
      "FQ41B_%=:\n\t"
      "v_mfma_f32_16x16x32_bf16 v[212:215], v[148:151], v[92:95], v[212:215]\n\t"
      "global_load_dwordx4 v[148:151], v229, s[64:65] sc1\n\t"
      "v_mfma_f32_16x16x32_bf16 v[200:203], v[152:155], v[96:99], v[200:203]\n\t"
      "s_waitcnt vmcnt(5)\n\t"
      "v_cmp_eq_u32_e32 vcc, -1, v128\n\t"
      "v_mfma_f32_16x16x32_bf16 v[204:207], v[152:155], v[100:103], v[204:207]\n\t"
      "v_mfma_f32_16x16x32_bf16 v[208:211], v[152:155], v[104:107], v[208:211]\n\t"
      "s_cbranch_vccnz FQ42R_%=\n\t"
      "FQ42B_%=:\n\t"
      "v_mfma_f32_16x16x32_bf16 v[212:215], v[152:155], v[108:111], v[212:215]\n\t"
      "global_load_dwordx4 v[152:155], v230, s[64:65] sc1\n\t"
      "s_setprio 0\n\t"
      "v_cmp_u_f32_e32 vcc, v200, v201\n\t"
      "v_cmp_u_f32_e64 s[48:49], v202, v203\n\t"
      "s_nop 0\n\t"
      "s_or_b64 vcc, vcc, s[48:49]\n\t"
      "s_cbranch_scc1 NQ5R_%=\n\t"
      "NQ5B_%=:\n\t"
      "ds_write_b128 v243, v[200:203] offset:32768\n\t"
      "ds_write_b128 v220, v[204:207] offset:32768\n\t"
      "ds_write_b128 v221, v[208:211] offset:32768\n\t"
      "ds_write_b128 v222, v[212:215] offset:32768\n\t"
      "v_mfma_f32_16x16x32_bf16 v[184:187], v[128:131], v[0:3], 0\n\t"
      "s_waitcnt vmcnt(5)\n\t"
      "v_cmp_eq_u32_e32 vcc, -1, v132\n\t"
      "v_mfma_f32_16x16x32_bf16 v[188:191], v[128:131], v[4:7], 0\n\t"
      "v_mfma_f32_16x16x32_bf16 v[192:195], v[128:131], v[8:11], 0\n\t"
      "s_cbranch_vccnz FQ43R_%=\n\t"
      "FQ43B_%=:\n\t"
      "v_mfma_f32_16x16x32_bf16 v[196:199], v[128:131], v[12:15], 0\n\t"
      "v_mfma_f32_16x16x32_bf16 v[184:187], v[132:135], v[16:19], v[184:187]\n\t"
      "s_waitcnt vmcnt(4)\n\t"
      "v_cmp_eq_u32_e32 vcc, -1, v136\n\t"
      "v_mfma_f32_16x16x32_bf16 v[188:191], v[132:135], v[20:23], v[188:191]\n\t"
      "v_mfma_f32_16x16x32_bf16 v[192:195], v[132:135], v[24:27], v[192:195]\n\t"
      "s_cbranch_vccnz FQ44R_%=\n\t"
      "FQ44B_%=:\n\t"
      "v_mfma_f32_16x16x32_bf16 v[196:199], v[132:135], v[28:31], v[196:199]\n\t"
      "v_mfma_f32_16x16x32_bf16 v[184:187], v[136:139], v[32:35], v[184:187]\n\t"
      "s_waitcnt vmcnt(3)\n\t"
      "v_cmp_eq_u32_e32 vcc, -1, v140\n\t"
      "v_mfma_f32_16x16x32_bf16 v[188:191], v[136:139], v[36:39], v[188:191]\n\t"
      "v_mfma_f32_16x16x32_bf16 v[192:195], v[136:139], v[40:43], v[192:195]\n\t"
      "s_cbranch_vccnz FQ45R_%=\n\t"
      "FQ45B_%=:\n\t"
      "v_mfma_f32_16x16x32_bf16 v[196:199], v[136:139], v[44:47], v[196:199]\n\t"
      "v_mfma_f32_16x16x32_bf16 v[184:187], v[140:143], v[48:51], v[184:187]\n\t"
      "s_waitcnt vmcnt(2)\n\t"
      "v_cmp_eq_u32_e32 vcc, -1, v144\n\t"
      "v_mfma_f32_16x16x32_bf16 v[188:191], v[140:143], v[52:55], v[188:191]\n\t"
      "v_mfma_f32_16x16x32_bf16 v[192:195], v[140:143], v[56:59], v[192:195]\n\t"
      "s_cbranch_vccnz FQ46R_%=\n\t"
      "FQ46B_%=:\n\t"
      "v_mfma_f32_16x16x32_bf16 v[196:199], v[140:143], v[60:63], v[196:199]\n\t"
      "v_mfma_f32_16x16x32_bf16 v[184:187], v[144:147], v[64:67], v[184:187]\n\t"
      "s_waitcnt vmcnt(1)\n\t"
      "v_cmp_eq_u32_e32 vcc, -1, v148\n\t"
      "v_mfma_f32_16x16x32_bf16 v[188:191], v[144:147], v[68:71], v[188:191]\n\t"
      "v_mfma_f32_16x16x32_bf16 v[192:195], v[144:147], v[72:75], v[192:195]\n\t"
      "s_cbranch_vccnz FQ47R_%=\n\t"
      "FQ47B_%=:\n\t"
      "v_mfma_f32_16x16x32_bf16 v[196:199], v[144:147], v[76:79], v[196:199]\n\t"
      "s_waitcnt lgkmcnt(0)\n\t"
      "s_barrier\n\t"
      "s_setprio 2\n\t"
      "v_mfma_f32_16x16x32_bf16 v[184:187], v[148:151], v[80:83], v[184:187]\n\t"
      "s_waitcnt vmcnt(0)\n\t"
      "v_cmp_eq_u32_e32 vcc, -1, v152\n\t"
      "v_mfma_f32_16x16x32_bf16 v[188:191], v[148:151], v[84:87], v[188:191]\n\t"
      "v_mfma_f32_16x16x32_bf16 v[192:195], v[148:151], v[88:91], v[192:195]\n\t"
      "s_cbranch_vccnz FQ48R_%=\n\t"
      "FQ48B_%=:\n\t"
      "v_mfma_f32_16x16x32_bf16 v[196:199], v[148:151], v[92:95], v[196:199]\n\t"
      "v_mfma_f32_16x16x32_bf16 v[184:187], v[152:155], v[96:99], v[184:187]\n\t"
      "v_mfma_f32_16x16x32_bf16 v[188:191], v[152:155], v[100:103], v[188:191]\n\t"
      "v_mfma_f32_16x16x32_bf16 v[192:195], v[152:155], v[104:107], v[192:195]\n\t"
      "v_mfma_f32_16x16x32_bf16 v[196:199], v[152:155], v[108:111], v[196:199]\n\t"
      "s_nop 4\n\t"
      "v_cmp_u_f32_e32 vcc, v184, v185\n\t"
      "v_cmp_u_f32_e64 s[48:49], v186, v187\n\t"
      "s_nop 0\n\t"
      "s_or_b64 vcc, vcc, s[48:49]\n\t"
      "s_cbranch_scc1 NQ6R_%=\n\t"
      "NQ6B_%=:\n\t"
      "ds_write_b128 v243, v[184:187]\n\t"
      "ds_write_b128 v220, v[188:191]\n\t"
      "ds_write_b128 v221, v[192:195]\n\t"
      "ds_write_b128 v222, v[196:199]\n\t"
      "s_waitcnt lgkmcnt(0)\n\t"
      "s_barrier\n\t"
      "s_branch GX_%=\n\t"
      "HWR_%=:\n\t"
      "s_mov_b32 s51, 0\n\t"
      "HWL_%=:\n\t"
      "s_add_u32 s51, s51, 1\n\t"
      "s_cmp_lt_u32 s51, 262144\n\t"
      "s_cbranch_scc0 HWX_%=\n\t"
      "s_sleep 2\n\t"
      "global_load_dwordx4 v[112:115], v116, s[12:13] sc1\n\t"
      "s_waitcnt vmcnt(0)\n\t"
      "v_xor_b32_e64 v114, v114, v112\n\t"
      "v_xor_b32_e32 v115, v115, v113\n\t"
      "v_cmp_ne_u64_e32 vcc, s[14:15], v[114:115]\n\t"
      "s_cbranch_vccnz HWL_%=\n\t"
      "HWX_%=:\n\t"
      "s_branch HWB_%=\n\t"
      "HXR_%=:\n\t"
      "s_mov_b32 s51, 0\n\t"
      "HXL_%=:\n\t"
      "s_add_u32 s51, s51, 1\n\t"
      "s_cmp_lt_u32 s51, 262144\n\t"
      "s_cbranch_scc0 HXX_%=\n\t"
      "s_sleep 2\n\t"
      "s_mov_b32 exec_hi, s23\n\t"
      "s_not_b32 exec_lo, s23\n\t"
      "s_add_u32 s68, s20, 0\n\t"
      "s_addc_u32 s69, s21, 0\n\t"
      "global_load_dwordx4 v[184:187], v240, s[68:69] sc1\n\t"
      "s_add_u32 s68, s20, 131072\n\t"
      "s_addc_u32 s69, s21, 0\n\t"
      "global_load_dwordx4 v[188:191], v240, s[68:69] sc1\n\t"
      "s_add_u32 s68, s20, 262144\n\t"
      "s_addc_u32 s69, s21, 0\n\t"
      "global_load_dwordx4 v[192:195], v240, s[68:69] sc1\n\t"
      "s_add_u32 s68, s20, 393216\n\t"
      "s_addc_u32 s69, s21, 0\n\t"
      "global_load_dwordx4 v[196:199], v240, s[68:69] sc1\n\t"
      "s_add_u32 s68, s20, 524288\n\t"
      "s_addc_u32 s69, s21, 0\n\t"
      "global_load_dwordx4 v[200:203], v240, s[68:69] sc1\n\t"
      "s_add_u32 s68, s20, 655360\n\t"
      "s_addc_u32 s69, s21, 0\n\t"
      "global_load_dwordx4 v[204:207], v240, s[68:69] sc1\n\t"
      "s_add_u32 s68, s20, 786432\n\t"
      "s_addc_u32 s69, s21, 0\n\t"
      "global_load_dwordx4 v[208:211], v240, s[68:69] sc1\n\t"
      "s_mov_b64 exec, -1\n\t"
      "s_waitcnt vmcnt(0)\n\t"
      "v_max3_u32 v234, v184, v185, v186\n\t"
      "v_max3_u32 v234, v234, v187, v188\n\t"
      "v_max3_u32 v234, v234, v189, v190\n\t"
      "v_max3_u32 v234, v234, v191, v192\n\t"
      "v_max3_u32 v234, v234, v193, v194\n\t"
      "v_max3_u32 v234, v234, v195, v196\n\t"
      "v_max3_u32 v234, v234, v197, v198\n\t"
      "v_max3_u32 v234, v234, v199, v200\n\t"
      "v_max3_u32 v234, v234, v201, v202\n\t"
      "v_max3_u32 v234, v234, v203, v204\n\t"
      "v_max3_u32 v234, v234, v205, v206\n\t"
      "v_max3_u32 v234, v234, v207, v208\n\t"
      "v_max3_u32 v234, v234, v209, v210\n\t"
      "v_max_u32_e32 v234, v234, v211\n\t"
      "v_cmp_eq_u32_e32 vcc, -1, v234\n\t"
      "s_cbranch_vccnz HXL_%=\n\t"
      "HXX_%=:\n\t"
      "s_nop 1\n\t"
      "s_branch HXB_%=\n\t"
      "FQ1R_%=:\n\t"
      "s_mov_b32 s51, 0\n\t"
      "FQ1L_%=:\n\t"
      "s_add_u32 s51, s51, 1\n\t"
      "s_cmp_lt_u32 s51, 262144\n\t"
      "s_cbranch_scc0 FQ1X_%=\n\t"
      "s_waitcnt vmcnt(0)\n\t"
      "s_sleep 1\n\t"
      "global_load_dwordx4 v[132:135], v225, s[52:53] sc1\n\t"
      "global_load_dwordx4 v[136:139], v226, s[52:53] sc1\n\t"
      "global_load_dwordx4 v[140:143], v227, s[52:53] sc1\n\t"
      "global_load_dwordx4 v[144:147], v228, s[52:53] sc1\n\t"
      "global_load_dwordx4 v[148:151], v229, s[52:53] sc1\n\t"
      "global_load_dwordx4 v[152:155], v230, s[52:53] sc1\n\t"
      "s_waitcnt vmcnt(5)\n\t"
      "v_max3_u32 v234, v132, v133, v134\n\t"
      "v_max_u32_e32 v234, v234, v135\n\t"
      "v_cmp_eq_u32_e32 vcc, -1, v234\n\t"
      "s_cbranch_vccnz FQ1L_%=\n\t"
      "FQ1X_%=:\n\t"
      "s_branch FQ1B_%=\n\t"
      "FQ2R_%=:\n\t"
      "s_mov_b32 s51, 0\n\t"
      "FQ2L_%=:\n\t"
      "s_add_u32 s51, s51, 1\n\t"
      "s_cmp_lt_u32 s51, 262144\n\t"
      "s_cbranch_scc0 FQ2X_%=\n\t"
      "s_waitcnt vmcnt(0)\n\t"
      "s_sleep 1\n\t"
      "global_load_dwordx4 v[136:139], v226, s[52:53] sc1\n\t"
      "global_load_dwordx4 v[140:143], v227, s[52:53] sc1\n\t"
      "global_load_dwordx4 v[144:147], v228, s[52:53] sc1\n\t"
      "global_load_dwordx4 v[148:151], v229, s[52:53] sc1\n\t"
      "global_load_dwordx4 v[152:155], v230, s[52:53] sc1\n\t"
      "global_load_dwordx4 v[128:131], v224, s[54:55] sc1\n\t"
      "s_waitcnt vmcnt(5)\n\t"
      "v_max3_u32 v234, v136, v137, v138\n\t"
      "v_max_u32_e32 v234, v234, v139\n\t"
      "v_cmp_eq_u32_e32 vcc, -1, v234\n\t"
      "s_cbranch_vccnz FQ2L_%=\n\t"
      "FQ2X_%=:\n\t"
      "s_branch FQ2B_%=\n\t"
      "FQ3R_%=:\n\t"
      "s_mov_b32 s51, 0\n\t"
      "FQ3L_%=:\n\t"
      "s_add_u32 s51, s51, 1\n\t"
      "s_cmp_lt_u32 s51, 262144\n\t"
      "s_cbranch_scc0 FQ3X_%=\n\t"
      "s_waitcnt vmcnt(0)\n\t"
      "s_sleep 1\n\t"
      "global_load_dwordx4 v[140:143], v227, s[52:53] sc1\n\t"
      "global_load_dwordx4 v[144:147], v228, s[52:53] sc1\n\t"
      "global_load_dwordx4 v[148:151], v229, s[52:53] sc1\n\t"
      "global_load_dwordx4 v[152:155], v230, s[52:53] sc1\n\t"
      "global_load_dwordx4 v[128:131], v224, s[54:55] sc1\n\t"
      "global_load_dwordx4 v[132:135], v225, s[54:55] sc1\n\t"
      "s_waitcnt vmcnt(5)\n\t"
      "v_max3_u32 v234, v140, v141, v142\n\t"
      "v_max_u32_e32 v234, v234, v143\n\t"
      "v_cmp_eq_u32_e32 vcc, -1, v234\n\t"
      "s_cbranch_vccnz FQ3L_%=\n\t"
      "FQ3X_%=:\n\t"
      "s_branch FQ3B_%=\n\t"
      "FQ4R_%=:\n\t"
      "s_mov_b32 s51, 0\n\t"
      "FQ4L_%=:\n\t"
      "s_add_u32 s51, s51, 1\n\t"
      "s_cmp_lt_u32 s51, 262144\n\t"
      "s_cbranch_scc0 FQ4X_%=\n\t"
      "s_waitcnt vmcnt(0)\n\t"
      "s_sleep 1\n\t"
      "global_load_dwordx4 v[144:147], v228, s[52:53] sc1\n\t"
      "global_load_dwordx4 v[148:151], v229, s[52:53] sc1\n\t"
      "global_load_dwordx4 v[152:155], v230, s[52:53] sc1\n\t"
      "global_load_dwordx4 v[128:131], v224, s[54:55] sc1\n\t"
      "global_load_dwordx4 v[132:135], v225, s[54:55] sc1\n\t"
      "global_load_dwordx4 v[136:139], v226, s[54:55] sc1\n\t"
      "s_waitcnt vmcnt(5)\n\t"
      "v_max3_u32 v234, v144, v145, v146\n\t"
      "v_max_u32_e32 v234, v234, v147\n\t"
      "v_cmp_eq_u32_e32 vcc, -1, v234\n\t"
      "s_cbranch_vccnz FQ4L_%=\n\t"
      "FQ4X_%=:\n\t"
      "s_branch FQ4B_%=\n\t"
      "FQ5R_%=:\n\t"
      "s_mov_b32 s51, 0\n\t"
      "FQ5L_%=:\n\t"
      "s_add_u32 s51, s51, 1\n\t"
      "s_cmp_lt_u32 s51, 262144\n\t"
      "s_cbranch_scc0 FQ5X_%=\n\t"
      "s_waitcnt vmcnt(0)\n\t"
      "s_sleep 1\n\t"
      "global_load_dwordx4 v[148:151], v229, s[52:53] sc1\n\t"
      "global_load_dwordx4 v[152:155], v230, s[52:53] sc1\n\t"
      "global_load_dwordx4 v[128:131], v224, s[54:55] sc1\n\t"
      "global_load_dwordx4 v[132:135], v225, s[54:55] sc1\n\t"
      "global_load_dwordx4 v[136:139], v226, s[54:55] sc1\n\t"
      "global_load_dwordx4 v[140:143], v227, s[54:55] sc1\n\t"
      "s_waitcnt vmcnt(5)\n\t"
      "v_max3_u32 v234, v148, v149, v150\n\t"
      "v_max_u32_e32 v234, v234, v151\n\t"
      "v_cmp_eq_u32_e32 vcc, -1, v234\n\t"
      "s_cbranch_vccnz FQ5L_%=\n\t"
      "FQ5X_%=:\n\t"
      "s_branch FQ5B_%=\n\t"
      "FQ6R_%=:\n\t"
      "s_mov_b32 s51, 0\n\t"
      "FQ6L_%=:\n\t"
      "s_add_u32 s51, s51, 1\n\t"
      "s_cmp_lt_u32 s51, 262144\n\t"
      "s_cbranch_scc0 FQ6X_%=\n\t"
      "s_waitcnt vmcnt(0)\n\t"
      "s_sleep 1\n\t"
      "global_load_dwordx4 v[152:155], v230, s[52:53] sc1\n\t"
      "global_load_dwordx4 v[128:131], v224, s[54:55] sc1\n\t"
      "global_load_dwordx4 v[132:135], v225, s[54:55] sc1\n\t"
      "global_load_dwordx4 v[136:139], v226, s[54:55] sc1\n\t"
      "global_load_dwordx4 v[140:143], v227, s[54:55] sc1\n\t"
      "global_load_dwordx4 v[144:147], v228, s[54:55] sc1\n\t"
      "s_waitcnt vmcnt(5)\n\t"
      "v_max3_u32 v234, v152, v153, v154\n\t"
      "v_max_u32_e32 v234, v234, v155\n\t"
      "v_cmp_eq_u32_e32 vcc, -1, v234\n\t"
      "s_cbranch_vccnz FQ6L_%=\n\t"
      "FQ6X_%=:\n\t"
      "s_branch FQ6B_%=\n\t"
      "FQ7R_%=:\n\t"
      "s_mov_b32 s51, 0\n\t"
      "FQ7L_%=:\n\t"
      "s_add_u32 s51, s51, 1\n\t"
      "s_cmp_lt_u32 s51, 262144\n\t"
      "s_cbranch_scc0 FQ7X_%=\n\t"
      "s_waitcnt vmcnt(0)\n\t"
      "s_sleep 4\n\t"
      "global_load_dwordx4 v[128:131], v224, s[54:55] sc1\n\t"
      "global_load_dwordx4 v[132:135], v225, s[54:55] sc1\n\t"
      "global_load_dwordx4 v[136:139], v226, s[54:55] sc1\n\t"
      "global_load_dwordx4 v[140:143], v227, s[54:55] sc1\n\t"
      "global_load_dwordx4 v[144:147], v228, s[54:55] sc1\n\t"
      "global_load_dwordx4 v[148:151], v229, s[54:55] sc1\n\t"
      "s_waitcnt vmcnt(5)\n\t"
      "v_max3_u32 v234, v128, v129, v130\n\t"
      "v_max_u32_e32 v234, v234, v131\n\t"
      "v_cmp_eq_u32_e32 vcc, -1, v234\n\t"
      "s_cbranch_vccnz FQ7L_%=\n\t"
      "FQ7X_%=:\n\t"
      "s_branch FQ7B_%=\n\t"
      "NQ0R_%=:\n\t"
      "s_mov_b32 s51, 0\n\t"
      "NQ0L_%=:\n\t"
      "s_add_u32 s51, s51, 1\n\t"
      "s_cmp_lt_u32 s51, 262144\n\t"
      "s_cbranch_scc0 NQ0X_%=\n\t"
      "s_waitcnt vmcnt(0)\n\t"
      "s_sleep 1\n\t"
      "global_load_dwordx4 v[128:131], v224, s[52:53] sc1\n\t"
      "global_load_dwordx4 v[132:135], v225, s[52:53] sc1\n\t"
      "global_load_dwordx4 v[136:139], v226, s[52:53] sc1\n\t"
      "global_load_dwordx4 v[140:143], v227, s[52:53] sc1\n\t"
      "global_load_dwordx4 v[144:147], v228, s[52:53] sc1\n\t"
      "global_load_dwordx4 v[148:151], v229, s[52:53] sc1\n\t"
      "global_load_dwordx4 v[152:155], v230, s[52:53] sc1\n\t"
      "s_waitcnt vmcnt(0)\n\t"
      "s_nop 0\n\t"
      "v_max3_u32 v234, v128, v129, v130\n\t"
      "v_max3_u32 v234, v234, v131, v132\n\t"
      "v_max3_u32 v234, v234, v133, v134\n\t"
      "v_max3_u32 v234, v234, v135, v136\n\t"
      "v_max3_u32 v234, v234, v137, v138\n\t"
      "v_max3_u32 v234, v234, v139, v140\n\t"
      "v_max3_u32 v234, v234, v141, v142\n\t"
      "v_max3_u32 v234, v234, v143, v144\n\t"
      "v_max3_u32 v234, v234, v145, v146\n\t"
      "v_max3_u32 v234, v234, v147, v148\n\t"
      "v_max3_u32 v234, v234, v149, v150\n\t"
      "v_max3_u32 v234, v234, v151, v152\n\t"
      "v_max3_u32 v234, v234, v153, v154\n\t"
      "v_max_u32_e32 v234, v234, v155\n\t"
      "v_cmp_eq_u32_e32 vcc, -1, v234\n\t"
      "s_cbranch_vccnz NQ0L_%=\n\t"
      "NQ0X_%=:\n\t"
      "v_mfma_f32_16x16x32_bf16 v[184:187], v[128:131], v[0:3], 0\n\t"
      "v_mfma_f32_16x16x32_bf16 v[188:191], v[128:131], v[4:7], 0\n\t"
      "v_mfma_f32_16x16x32_bf16 v[192:195], v[128:131], v[8:11], 0\n\t"
      "v_mfma_f32_16x16x32_bf16 v[196:199], v[128:131], v[12:15], 0\n\t"
      "v_mfma_f32_16x16x32_bf16 v[184:187], v[132:135], v[16:19], v[184:187]\n\t"
      "v_mfma_f32_16x16x32_bf16 v[188:191], v[132:135], v[20:23], v[188:191]\n\t"
      "v_mfma_f32_16x16x32_bf16 v[192:195], v[132:135], v[24:27], v[192:195]\n\t"
      "v_mfma_f32_16x16x32_bf16 v[196:199], v[132:135], v[28:31], v[196:199]\n\t"
      "v_mfma_f32_16x16x32_bf16 v[184:187], v[136:139], v[32:35], v[184:187]\n\t"
      "v_mfma_f32_16x16x32_bf16 v[188:191], v[136:139], v[36:39], v[188:191]\n\t"
      "v_mfma_f32_16x16x32_bf16 v[192:195], v[136:139], v[40:43], v[192:195]\n\t"
      "v_mfma_f32_16x16x32_bf16 v[196:199], v[136:139], v[44:47], v[196:199]\n\t"
      "v_mfma_f32_16x16x32_bf16 v[184:187], v[140:143], v[48:51], v[184:187]\n\t"
      "v_mfma_f32_16x16x32_bf16 v[188:191], v[140:143], v[52:55], v[188:191]\n\t"
      "v_mfma_f32_16x16x32_bf16 v[192:195], v[140:143], v[56:59], v[192:195]\n\t"
      "v_mfma_f32_16x16x32_bf16 v[196:199], v[140:143], v[60:63], v[196:199]\n\t"
      "v_mfma_f32_16x16x32_bf16 v[184:187], v[144:147], v[64:67], v[184:187]\n\t"
      "v_mfma_f32_16x16x32_bf16 v[188:191], v[144:147], v[68:71], v[188:191]\n\t"
      "v_mfma_f32_16x16x32_bf16 v[192:195], v[144:147], v[72:75], v[192:195]\n\t"
      "v_mfma_f32_16x16x32_bf16 v[196:199], v[144:147], v[76:79], v[196:199]\n\t"
      "v_mfma_f32_16x16x32_bf16 v[184:187], v[148:151], v[80:83], v[184:187]\n\t"
      "v_mfma_f32_16x16x32_bf16 v[188:191], v[148:151], v[84:87], v[188:191]\n\t"
      "v_mfma_f32_16x16x32_bf16 v[192:195], v[148:151], v[88:91], v[192:195]\n\t"
      "v_mfma_f32_16x16x32_bf16 v[196:199], v[148:151], v[92:95], v[196:199]\n\t"
      "v_mfma_f32_16x16x32_bf16 v[184:187], v[152:155], v[96:99], v[184:187]\n\t"
      "v_mfma_f32_16x16x32_bf16 v[188:191], v[152:155], v[100:103], v[188:191]\n\t"
      "v_mfma_f32_16x16x32_bf16 v[192:195], v[152:155], v[104:107], v[192:195]\n\t"
      "v_mfma_f32_16x16x32_bf16 v[196:199], v[152:155], v[108:111], v[196:199]\n\t"
      "global_load_dwordx4 v[128:131], v224, s[54:55] sc1\n\t"
      "global_load_dwordx4 v[132:135], v225, s[54:55] sc1\n\t"
      "global_load_dwordx4 v[136:139], v226, s[54:55] sc1\n\t"
      "global_load_dwordx4 v[140:143], v227, s[54:55] sc1\n\t"
      "global_load_dwordx4 v[144:147], v228, s[54:55] sc1\n\t"
      "global_load_dwordx4 v[148:151], v229, s[54:55] sc1\n\t"
      "global_load_dwordx4 v[152:155], v230, s[54:55] sc1\n\t"
      "s_waitcnt vmcnt(0)\n\t"
      "s_nop 7\n\t"
      "s_branch NQ0B_%=\n\t"
      "FQ8R_%=:\n\t"
      "s_mov_b32 s51, 0\n\t"
      "FQ8L_%=:\n\t"
      "s_add_u32 s51, s51, 1\n\t"
      "s_cmp_lt_u32 s51, 262144\n\t"
      "s_cbranch_scc0 FQ8X_%=\n\t"
      "s_waitcnt vmcnt(0)\n\t"
      "s_sleep 4\n\t"
      "global_load_dwordx4 v[132:135], v225, s[54:55] sc1\n\t"
      "global_load_dwordx4 v[136:139], v226, s[54:55] sc1\n\t"
      "global_load_dwordx4 v[140:143], v227, s[54:55] sc1\n\t"
      "global_load_dwordx4 v[144:147], v228, s[54:55] sc1\n\t"
      "global_load_dwordx4 v[148:151], v229, s[54:55] sc1\n\t"
      "global_load_dwordx4 v[152:155], v230, s[54:55] sc1\n\t"
      "s_waitcnt vmcnt(5)\n\t"
      "v_max3_u32 v234, v132, v133, v134\n\t"
      "v_max_u32_e32 v234, v234, v135\n\t"
      "v_cmp_eq_u32_e32 vcc, -1, v234\n\t"
      "s_cbranch_vccnz FQ8L_%=\n\t"
      "FQ8X_%=:\n\t"
      "s_branch FQ8B_%=\n\t"
      "FQ9R_%=:\n\t"
      "s_mov_b32 s51, 0\n\t"
      "FQ9L_%=:\n\t"
      "s_add_u32 s51, s51, 1\n\t"
      "s_cmp_lt_u32 s51, 262144\n\t"
      "s_cbranch_scc0 FQ9X_%=\n\t"
      "s_waitcnt vmcnt(0)\n\t"
      "s_sleep 4\n\t"
      "global_load_dwordx4 v[136:139], v226, s[54:55] sc1\n\t"
      "global_load_dwordx4 v[140:143], v227, s[54:55] sc1\n\t"
      "global_load_dwordx4 v[144:147], v228, s[54:55] sc1\n\t"
      "global_load_dwordx4 v[148:151], v229, s[54:55] sc1\n\t"
      "global_load_dwordx4 v[152:155], v230, s[54:55] sc1\n\t"
      "global_load_dwordx4 v[128:131], v224, s[56:57] sc1\n\t"
      "s_waitcnt vmcnt(5)\n\t"
      "v_max3_u32 v234, v136, v137, v138\n\t"
      "v_max_u32_e32 v234, v234, v139\n\t"
      "v_cmp_eq_u32_e32 vcc, -1, v234\n\t"
      "s_cbranch_vccnz FQ9L_%=\n\t"
      "FQ9X_%=:\n\t"
      "s_branch FQ9B_%=\n\t"
      "FQ10R_%=:\n\t"
      "s_mov_b32 s51, 0\n\t"
      "FQ10L_%=:\n\t"
      "s_add_u32 s51, s51, 1\n\t"
      "s_cmp_lt_u32 s51, 262144\n\t"
      "s_cbranch_scc0 FQ10X_%=\n\t"
      "s_waitcnt vmcnt(0)\n\t"
      "s_sleep 4\n\t"
      "global_load_dwordx4 v[140:143], v227, s[54:55] sc1\n\t"
      "global_load_dwordx4 v[144:147], v228, s[54:55] sc1\n\t"
      "global_load_dwordx4 v[148:151], v229, s[54:55] sc1\n\t"
      "global_load_dwordx4 v[152:155], v230, s[54:55] sc1\n\t"
      "global_load_dwordx4 v[128:131], v224, s[56:57] sc1\n\t"
      "global_load_dwordx4 v[132:135], v225, s[56:57] sc1\n\t"
      "s_waitcnt vmcnt(5)\n\t"
      "v_max3_u32 v234, v140, v141, v142\n\t"
      "v_max_u32_e32 v234, v234, v143\n\t"
      "v_cmp_eq_u32_e32 vcc, -1, v234\n\t"
      "s_cbranch_vccnz FQ10L_%=\n\t"
      "FQ10X_%=:\n\t"
      "s_branch FQ10B_%=\n\t"
      "FQ11R_%=:\n\t"
      "s_mov_b32 s51, 0\n\t"
      "FQ11L_%=:\n\t"
      "s_add_u32 s51, s51, 1\n\t"
      "s_cmp_lt_u32 s51, 262144\n\t"
      "s_cbranch_scc0 FQ11X_%=\n\t"
      "s_waitcnt vmcnt(0)\n\t"
      "s_sleep 4\n\t"
      "global_load_dwordx4 v[144:147], v228, s[54:55] sc1\n\t"
      "global_load_dwordx4 v[148:151], v229, s[54:55] sc1\n\t"
      "global_load_dwordx4 v[152:155], v230, s[54:55] sc1\n\t"
      "global_load_dwordx4 v[128:131], v224, s[56:57] sc1\n\t"
      "global_load_dwordx4 v[132:135], v225, s[56:57] sc1\n\t"
      "global_load_dwordx4 v[136:139], v226, s[56:57] sc1\n\t"
      "s_waitcnt vmcnt(5)\n\t"
      "v_max3_u32 v234, v144, v145, v146\n\t"
      "v_max_u32_e32 v234, v234, v147\n\t"
      "v_cmp_eq_u32_e32 vcc, -1, v234\n\t"
      "s_cbranch_vccnz FQ11L_%=\n\t"
      "FQ11X_%=:\n\t"
      "s_branch FQ11B_%=\n\t"
      "FQ12R_%=:\n\t"
      "s_mov_b32 s51, 0\n\t"
      "FQ12L_%=:\n\t"
      "s_add_u32 s51, s51, 1\n\t"
      "s_cmp_lt_u32 s51, 262144\n\t"
      "s_cbranch_scc0 FQ12X_%=\n\t"
      "s_waitcnt vmcnt(0)\n\t"
      "s_sleep 4\n\t"
      "global_load_dwordx4 v[148:151], v229, s[54:55] sc1\n\t"
      "global_load_dwordx4 v[152:155], v230, s[54:55] sc1\n\t"
      "global_load_dwordx4 v[128:131], v224, s[56:57] sc1\n\t"
      "global_load_dwordx4 v[132:135], v225, s[56:57] sc1\n\t"
      "global_load_dwordx4 v[136:139], v226, s[56:57] sc1\n\t"
      "global_load_dwordx4 v[140:143], v227, s[56:57] sc1\n\t"
      "s_waitcnt vmcnt(5)\n\t"
      "v_max3_u32 v234, v148, v149, v150\n\t"
      "v_max_u32_e32 v234, v234, v151\n\t"
      "v_cmp_eq_u32_e32 vcc, -1, v234\n\t"
      "s_cbranch_vccnz FQ12L_%=\n\t"
      "FQ12X_%=:\n\t"
      "s_branch FQ12B_%=\n\t"
      "FQ13R_%=:\n\t"
      "s_mov_b32 s51, 0\n\t"
      "FQ13L_%=:\n\t"
      "s_add_u32 s51, s51, 1\n\t"
      "s_cmp_lt_u32 s51, 262144\n\t"
      "s_cbranch_scc0 FQ13X_%=\n\t"
      "s_waitcnt vmcnt(0)\n\t"
      "s_sleep 4\n\t"
      "global_load_dwordx4 v[152:155], v230, s[54:55] sc1\n\t"
      "global_load_dwordx4 v[128:131], v224, s[56:57] sc1\n\t"
      "global_load_dwordx4 v[132:135], v225, s[56:57] sc1\n\t"
      "global_load_dwordx4 v[136:139], v226, s[56:57] sc1\n\t"
      "global_load_dwordx4 v[140:143], v227, s[56:57] sc1\n\t"
      "global_load_dwordx4 v[144:147], v228, s[56:57] sc1\n\t"
      "s_waitcnt vmcnt(5)\n\t"
      "v_max3_u32 v234, v152, v153, v154\n\t"
      "v_max_u32_e32 v234, v234, v155\n\t"
      "v_cmp_eq_u32_e32 vcc, -1, v234\n\t"
      "s_cbranch_vccnz FQ13L_%=\n\t"
      "FQ13X_%=:\n\t"
      "s_branch FQ13B_%=\n\t"
      "FQ14R_%=:\n\t"
      "s_mov_b32 s51, 0\n\t"
      "FQ14L_%=:\n\t"
      "s_add_u32 s51, s51, 1\n\t"
      "s_cmp_lt_u32 s51, 262144\n\t"
      "s_cbranch_scc0 FQ14X_%=\n\t"
      "s_waitcnt vmcnt(0)\n\t"
      "s_sleep 4\n\t"
      "global_load_dwordx4 v[128:131], v224, s[56:57] sc1\n\t"
      "global_load_dwordx4 v[132:135], v225, s[56:57] sc1\n\t"
      "global_load_dwordx4 v[136:139], v226, s[56:57] sc1\n\t"
      "global_load_dwordx4 v[140:143], v227, s[56:57] sc1\n\t"
      "global_load_dwordx4 v[144:147], v228, s[56:57] sc1\n\t"
      "global_load_dwordx4 v[148:151], v229, s[56:57] sc1\n\t"
      "s_waitcnt vmcnt(5)\n\t"
      "v_max3_u32 v234, v128, v129, v130\n\t"
      "v_max_u32_e32 v234, v234, v131\n\t"
      "v_cmp_eq_u32_e32 vcc, -1, v234\n\t"
      "s_cbranch_vccnz FQ14L_%=\n\t"
      "FQ14X_%=:\n\t"
      "s_branch FQ14B_%=\n\t"
      "NQ1R_%=:\n\t"
      "s_mov_b32 s51, 0\n\t"
      "NQ1L_%=:\n\t"
      "s_add_u32 s51, s51, 1\n\t"
      "s_cmp_lt_u32 s51, 262144\n\t"
      "s_cbranch_scc0 NQ1X_%=\n\t"
      "s_waitcnt vmcnt(0)\n\t"
      "s_sleep 1\n\t"
      "global_load_dwordx4 v[128:131], v224, s[54:55] sc1\n\t"
      "global_load_dwordx4 v[132:135], v225, s[54:55] sc1\n\t"
      "global_load_dwordx4 v[136:139], v226, s[54:55] sc1\n\t"
      "global_load_dwordx4 v[140:143], v227, s[54:55] sc1\n\t"
      "global_load_dwordx4 v[144:147], v228, s[54:55] sc1\n\t"
      "global_load_dwordx4 v[148:151], v229, s[54:55] sc1\n\t"
      "global_load_dwordx4 v[152:155], v230, s[54:55] sc1\n\t"
      "s_waitcnt vmcnt(0)\n\t"
      "v_max3_u32 v234, v128, v129, v130\n\t"
      "v_max3_u32 v234, v234, v131, v132\n\t"
      "v_max3_u32 v234, v234, v133, v134\n\t"
      "v_max3_u32 v234, v234, v135, v136\n\t"
      "v_max3_u32 v234, v234, v137, v138\n\t"
      "v_max3_u32 v234, v234, v139, v140\n\t"
      "v_max3_u32 v234, v234, v141, v142\n\t"
      "v_max3_u32 v234, v234, v143, v144\n\t"
      "v_max3_u32 v234, v234, v145, v146\n\t"
      "v_max3_u32 v234, v234, v147, v148\n\t"
      "v_max3_u32 v234, v234, v149, v150\n\t"
      "v_max3_u32 v234, v234, v151, v152\n\t"
      "v_max3_u32 v234, v234, v153, v154\n\t"
      "v_max_u32_e32 v234, v234, v155\n\t"
      "v_cmp_eq_u32_e32 vcc, -1, v234\n\t"
      "s_cbranch_vccnz NQ1L_%=\n\t"
      "NQ1X_%=:\n\t"
      "v_mfma_f32_16x16x32_bf16 v[200:203], v[128:131], v[0:3], 0\n\t"
      "v_mfma_f32_16x16x32_bf16 v[204:207], v[128:131], v[4:7], 0\n\t"
      "v_mfma_f32_16x16x32_bf16 v[208:211], v[128:131], v[8:11], 0\n\t"
      "v_mfma_f32_16x16x32_bf16 v[212:215], v[128:131], v[12:15], 0\n\t"
      "v_mfma_f32_16x16x32_bf16 v[200:203], v[132:135], v[16:19], v[200:203]\n\t"
      "v_mfma_f32_16x16x32_bf16 v[204:207], v[132:135], v[20:23], v[204:207]\n\t"
      "v_mfma_f32_16x16x32_bf16 v[208:211], v[132:135], v[24:27], v[208:211]\n\t"
      "v_mfma_f32_16x16x32_bf16 v[212:215], v[132:135], v[28:31], v[212:215]\n\t"
      "v_mfma_f32_16x16x32_bf16 v[200:203], v[136:139], v[32:35], v[200:203]\n\t"
      "v_mfma_f32_16x16x32_bf16 v[204:207], v[136:139], v[36:39], v[204:207]\n\t"
      "v_mfma_f32_16x16x32_bf16 v[208:211], v[136:139], v[40:43], v[208:211]\n\t"
      "v_mfma_f32_16x16x32_bf16 v[212:215], v[136:139], v[44:47], v[212:215]\n\t"
      "v_mfma_f32_16x16x32_bf16 v[200:203], v[140:143], v[48:51], v[200:203]\n\t"
      "v_mfma_f32_16x16x32_bf16 v[204:207], v[140:143], v[52:55], v[204:207]\n\t"
      "v_mfma_f32_16x16x32_bf16 v[208:211], v[140:143], v[56:59], v[208:211]\n\t"
      "v_mfma_f32_16x16x32_bf16 v[212:215], v[140:143], v[60:63], v[212:215]\n\t"
      "v_mfma_f32_16x16x32_bf16 v[200:203], v[144:147], v[64:67], v[200:203]\n\t"
      "v_mfma_f32_16x16x32_bf16 v[204:207], v[144:147], v[68:71], v[204:207]\n\t"
      "v_mfma_f32_16x16x32_bf16 v[208:211], v[144:147], v[72:75], v[208:211]\n\t"
      "v_mfma_f32_16x16x32_bf16 v[212:215], v[144:147], v[76:79], v[212:215]\n\t"
      "v_mfma_f32_16x16x32_bf16 v[200:203], v[148:151], v[80:83], v[200:203]\n\t"
      "v_mfma_f32_16x16x32_bf16 v[204:207], v[148:151], v[84:87], v[204:207]\n\t"
      "v_mfma_f32_16x16x32_bf16 v[208:211], v[148:151], v[88:91], v[208:211]\n\t"
      "v_mfma_f32_16x16x32_bf16 v[212:215], v[148:151], v[92:95], v[212:215]\n\t"
      "v_mfma_f32_16x16x32_bf16 v[200:203], v[152:155], v[96:99], v[200:203]\n\t"
      "v_mfma_f32_16x16x32_bf16 v[204:207], v[152:155], v[100:103], v[204:207]\n\t"
      "v_mfma_f32_16x16x32_bf16 v[208:211], v[152:155], v[104:107], v[208:211]\n\t"
      "v_mfma_f32_16x16x32_bf16 v[212:215], v[152:155], v[108:111], v[212:215]\n\t"
      "global_load_dwordx4 v[128:131], v224, s[56:57] sc1\n\t"
      "global_load_dwordx4 v[132:135], v225, s[56:57] sc1\n\t"
      "global_load_dwordx4 v[136:139], v226, s[56:57] sc1\n\t"
      "global_load_dwordx4 v[140:143], v227, s[56:57] sc1\n\t"
      "global_load_dwordx4 v[144:147], v228, s[56:57] sc1\n\t"
      "global_load_dwordx4 v[148:151], v229, s[56:57] sc1\n\t"
      "global_load_dwordx4 v[152:155], v230, s[56:57] sc1\n\t"
      "s_waitcnt vmcnt(0)\n\t"
      "s_nop 7\n\t"
      "s_branch NQ1B_%=\n\t"
      "FQ15R_%=:\n\t"
      "s_mov_b32 s51, 0\n\t"
      "FQ15L_%=:\n\t"
      "s_add_u32 s51, s51, 1\n\t"
      "s_cmp_lt_u32 s51, 262144\n\t"
      "s_cbranch_scc0 FQ15X_%=\n\t"
      "s_waitcnt vmcnt(0)\n\t"
      "s_sleep 4\n\t"
      "global_load_dwordx4 v[132:135], v225, s[56:57] sc1\n\t"
      "global_load_dwordx4 v[136:139], v226, s[56:57] sc1\n\t"
      "global_load_dwordx4 v[140:143], v227, s[56:57] sc1\n\t"
      "global_load_dwordx4 v[144:147], v228, s[56:57] sc1\n\t"
      "global_load_dwordx4 v[148:151], v229, s[56:57] sc1\n\t"
      "global_load_dwordx4 v[152:155], v230, s[56:57] sc1\n\t"
      "s_waitcnt vmcnt(5)\n\t"
      "v_max3_u32 v234, v132, v133, v134\n\t"
      "v_max_u32_e32 v234, v234, v135\n\t"
      "v_cmp_eq_u32_e32 vcc, -1, v234\n\t"
      "s_cbranch_vccnz FQ15L_%=\n\t"
      "FQ15X_%=:\n\t"
      "s_branch FQ15B_%=\n\t"
      "FQ16R_%=:\n\t"
      "s_mov_b32 s51, 0\n\t"
      "FQ16L_%=:\n\t"
      "s_add_u32 s51, s51, 1\n\t"
      "s_cmp_lt_u32 s51, 262144\n\t"
      "s_cbranch_scc0 FQ16X_%=\n\t"
      "s_waitcnt vmcnt(0)\n\t"
      "s_sleep 4\n\t"
      "global_load_dwordx4 v[136:139], v226, s[56:57] sc1\n\t"
      "global_load_dwordx4 v[140:143], v227, s[56:57] sc1\n\t"
      "global_load_dwordx4 v[144:147], v228, s[56:57] sc1\n\t"
      "global_load_dwordx4 v[148:151], v229, s[56:57] sc1\n\t"
      "global_load_dwordx4 v[152:155], v230, s[56:57] sc1\n\t"
      "global_load_dwordx4 v[128:131], v224, s[58:59] sc1\n\t"
      "s_waitcnt vmcnt(5)\n\t"
      "v_max3_u32 v234, v136, v137, v138\n\t"
      "v_max_u32_e32 v234, v234, v139\n\t"
      "v_cmp_eq_u32_e32 vcc, -1, v234\n\t"
      "s_cbranch_vccnz FQ16L_%=\n\t"
      "FQ16X_%=:\n\t"
      "s_branch FQ16B_%=\n\t"
      "FQ17R_%=:\n\t"
      "s_mov_b32 s51, 0\n\t"
      "FQ17L_%=:\n\t"
      "s_add_u32 s51, s51, 1\n\t"
      "s_cmp_lt_u32 s51, 262144\n\t"
      "s_cbranch_scc0 FQ17X_%=\n\t"
      "s_waitcnt vmcnt(0)\n\t"
      "s_sleep 4\n\t"
      "global_load_dwordx4 v[140:143], v227, s[56:57] sc1\n\t"
      "global_load_dwordx4 v[144:147], v228, s[56:57] sc1\n\t"
      "global_load_dwordx4 v[148:151], v229, s[56:57] sc1\n\t"
      "global_load_dwordx4 v[152:155], v230, s[56:57] sc1\n\t"
      "global_load_dwordx4 v[128:131], v224, s[58:59] sc1\n\t"
      "global_load_dwordx4 v[132:135], v225, s[58:59] sc1\n\t"
      "s_waitcnt vmcnt(5)\n\t"
      "v_max3_u32 v234, v140, v141, v142\n\t"
      "v_max_u32_e32 v234, v234, v143\n\t"
      "v_cmp_eq_u32_e32 vcc, -1, v234\n\t"
      "s_cbranch_vccnz FQ17L_%=\n\t"
      "FQ17X_%=:\n\t"
      "s_branch FQ17B_%=\n\t"
      "FQ18R_%=:\n\t"
      "s_mov_b32 s51, 0\n\t"
      "FQ18L_%=:\n\t"
      "s_add_u32 s51, s51, 1\n\t"
      "s_cmp_lt_u32 s51, 262144\n\t"
      "s_cbranch_scc0 FQ18X_%=\n\t"
      "s_waitcnt vmcnt(0)\n\t"
      "s_sleep 4\n\t"
      "global_load_dwordx4 v[144:147], v228, s[56:57] sc1\n\t"
      "global_load_dwordx4 v[148:151], v229, s[56:57] sc1\n\t"
      "global_load_dwordx4 v[152:155], v230, s[56:57] sc1\n\t"
      "global_load_dwordx4 v[128:131], v224, s[58:59] sc1\n\t"
      "global_load_dwordx4 v[132:135], v225, s[58:59] sc1\n\t"
      "global_load_dwordx4 v[136:139], v226, s[58:59] sc1\n\t"
      "s_waitcnt vmcnt(5)\n\t"
      "v_max3_u32 v234, v144, v145, v146\n\t"
      "v_max_u32_e32 v234, v234, v147\n\t"
      "v_cmp_eq_u32_e32 vcc, -1, v234\n\t"
      "s_cbranch_vccnz FQ18L_%=\n\t"
      "FQ18X_%=:\n\t"
      "s_branch FQ18B_%=\n\t"
      "FQ19R_%=:\n\t"
      "s_mov_b32 s51, 0\n\t"
      "FQ19L_%=:\n\t"
      "s_add_u32 s51, s51, 1\n\t"
      "s_cmp_lt_u32 s51, 262144\n\t"
      "s_cbranch_scc0 FQ19X_%=\n\t"
      "s_waitcnt vmcnt(0)\n\t"
      "s_sleep 4\n\t"
      "global_load_dwordx4 v[148:151], v229, s[56:57] sc1\n\t"
      "global_load_dwordx4 v[152:155], v230, s[56:57] sc1\n\t"
      "global_load_dwordx4 v[128:131], v224, s[58:59] sc1\n\t"
      "global_load_dwordx4 v[132:135], v225, s[58:59] sc1\n\t"
      "global_load_dwordx4 v[136:139], v226, s[58:59] sc1\n\t"
      "global_load_dwordx4 v[140:143], v227, s[58:59] sc1\n\t"
      "s_waitcnt vmcnt(5)\n\t"
      "v_max3_u32 v234, v148, v149, v150\n\t"
      "v_max_u32_e32 v234, v234, v151\n\t"
      "v_cmp_eq_u32_e32 vcc, -1, v234\n\t"
      "s_cbranch_vccnz FQ19L_%=\n\t"
      "FQ19X_%=:\n\t"
      "s_branch FQ19B_%=\n\t"
      "FQ20R_%=:\n\t"
      "s_mov_b32 s51, 0\n\t"
      "FQ20L_%=:\n\t"
      "s_add_u32 s51, s51, 1\n\t"
      "s_cmp_lt_u32 s51, 262144\n\t"
      "s_cbranch_scc0 FQ20X_%=\n\t"
      "s_waitcnt vmcnt(0)\n\t"
      "s_sleep 4\n\t"
      "global_load_dwordx4 v[152:155], v230, s[56:57] sc1\n\t"
      "global_load_dwordx4 v[128:131], v224, s[58:59] sc1\n\t"
      "global_load_dwordx4 v[132:135], v225, s[58:59] sc1\n\t"
      "global_load_dwordx4 v[136:139], v226, s[58:59] sc1\n\t"
      "global_load_dwordx4 v[140:143], v227, s[58:59] sc1\n\t"
      "global_load_dwordx4 v[144:147], v228, s[58:59] sc1\n\t"
      "s_waitcnt vmcnt(5)\n\t"
      "v_max3_u32 v234, v152, v153, v154\n\t"
      "v_max_u32_e32 v234, v234, v155\n\t"
      "v_cmp_eq_u32_e32 vcc, -1, v234\n\t"
      "s_cbranch_vccnz FQ20L_%=\n\t"
      "FQ20X_%=:\n\t"
      "s_branch FQ20B_%=\n\t"
      "FQ21R_%=:\n\t"
      "s_mov_b32 s51, 0\n\t"
      "FQ21L_%=:\n\t"
      "s_add_u32 s51, s51, 1\n\t"
      "s_cmp_lt_u32 s51, 262144\n\t"
      "s_cbranch_scc0 FQ21X_%=\n\t"
      "s_waitcnt vmcnt(0)\n\t"
      "s_sleep 4\n\t"
      "global_load_dwordx4 v[128:131], v224, s[58:59] sc1\n\t"
      "global_load_dwordx4 v[132:135], v225, s[58:59] sc1\n\t"
      "global_load_dwordx4 v[136:139], v226, s[58:59] sc1\n\t"
      "global_load_dwordx4 v[140:143], v227, s[58:59] sc1\n\t"
      "global_load_dwordx4 v[144:147], v228, s[58:59] sc1\n\t"
      "global_load_dwordx4 v[148:151], v229, s[58:59] sc1\n\t"
      "s_waitcnt vmcnt(5)\n\t"
      "v_max3_u32 v234, v128, v129, v130\n\t"
      "v_max_u32_e32 v234, v234, v131\n\t"
      "v_cmp_eq_u32_e32 vcc, -1, v234\n\t"
      "s_cbranch_vccnz FQ21L_%=\n\t"
      "FQ21X_%=:\n\t"
      "s_branch FQ21B_%=\n\t"
      "NQ2R_%=:\n\t"
      "s_mov_b32 s51, 0\n\t"
      "NQ2L_%=:\n\t"
      "s_add_u32 s51, s51, 1\n\t"
      "s_cmp_lt_u32 s51, 262144\n\t"
      "s_cbranch_scc0 NQ2X_%=\n\t"
      "s_waitcnt vmcnt(0)\n\t"
      "s_sleep 1\n\t"
      "global_load_dwordx4 v[128:131], v224, s[56:57] sc1\n\t"
      "global_load_dwordx4 v[132:135], v225, s[56:57] sc1\n\t"
      "global_load_dwordx4 v[136:139], v226, s[56:57] sc1\n\t"
      "global_load_dwordx4 v[140:143], v227, s[56:57] sc1\n\t"
      "global_load_dwordx4 v[144:147], v228, s[56:57] sc1\n\t"
      "global_load_dwordx4 v[148:151], v229, s[56:57] sc1\n\t"
      "global_load_dwordx4 v[152:155], v230, s[56:57] sc1\n\t"
      "s_waitcnt vmcnt(0)\n\t"
      "v_max3_u32 v234, v128, v129, v130\n\t"
      "v_max3_u32 v234, v234, v131, v132\n\t"
      "v_max3_u32 v234, v234, v133, v134\n\t"
      "v_max3_u32 v234, v234, v135, v136\n\t"
      "v_max3_u32 v234, v234, v137, v138\n\t"
      "v_max3_u32 v234, v234, v139, v140\n\t"
      "v_max3_u32 v234, v234, v141, v142\n\t"
      "v_max3_u32 v234, v234, v143, v144\n\t"
      "v_max3_u32 v234, v234, v145, v146\n\t"
      "v_max3_u32 v234, v234, v147, v148\n\t"
      "v_max3_u32 v234, v234, v149, v150\n\t"
      "v_max3_u32 v234, v234, v151, v152\n\t"
      "v_max3_u32 v234, v234, v153, v154\n\t"
      "v_max_u32_e32 v234, v234, v155\n\t"
      "v_cmp_eq_u32_e32 vcc, -1, v234\n\t"
      "s_cbranch_vccnz NQ2L_%=\n\t"
      "NQ2X_%=:\n\t"
      "v_mfma_f32_16x16x32_bf16 v[184:187], v[128:131], v[0:3], 0\n\t"
      "v_mfma_f32_16x16x32_bf16 v[188:191], v[128:131], v[4:7], 0\n\t"
      "v_mfma_f32_16x16x32_bf16 v[192:195], v[128:131], v[8:11], 0\n\t"
      "v_mfma_f32_16x16x32_bf16 v[196:199], v[128:131], v[12:15], 0\n\t"
      "v_mfma_f32_16x16x32_bf16 v[184:187], v[132:135], v[16:19], v[184:187]\n\t"
      "v_mfma_f32_16x16x32_bf16 v[188:191], v[132:135], v[20:23], v[188:191]\n\t"
      "v_mfma_f32_16x16x32_bf16 v[192:195], v[132:135], v[24:27], v[192:195]\n\t"
      "v_mfma_f32_16x16x32_bf16 v[196:199], v[132:135], v[28:31], v[196:199]\n\t"
      "v_mfma_f32_16x16x32_bf16 v[184:187], v[136:139], v[32:35], v[184:187]\n\t"
      "v_mfma_f32_16x16x32_bf16 v[188:191], v[136:139], v[36:39], v[188:191]\n\t"
      "v_mfma_f32_16x16x32_bf16 v[192:195], v[136:139], v[40:43], v[192:195]\n\t"
      "v_mfma_f32_16x16x32_bf16 v[196:199], v[136:139], v[44:47], v[196:199]\n\t"
      "v_mfma_f32_16x16x32_bf16 v[184:187], v[140:143], v[48:51], v[184:187]\n\t"
      "v_mfma_f32_16x16x32_bf16 v[188:191], v[140:143], v[52:55], v[188:191]\n\t"
      "v_mfma_f32_16x16x32_bf16 v[192:195], v[140:143], v[56:59], v[192:195]\n\t"
      "v_mfma_f32_16x16x32_bf16 v[196:199], v[140:143], v[60:63], v[196:199]\n\t"
      "v_mfma_f32_16x16x32_bf16 v[184:187], v[144:147], v[64:67], v[184:187]\n\t"
      "v_mfma_f32_16x16x32_bf16 v[188:191], v[144:147], v[68:71], v[188:191]\n\t"
      "v_mfma_f32_16x16x32_bf16 v[192:195], v[144:147], v[72:75], v[192:195]\n\t"
      "v_mfma_f32_16x16x32_bf16 v[196:199], v[144:147], v[76:79], v[196:199]\n\t"
      "v_mfma_f32_16x16x32_bf16 v[184:187], v[148:151], v[80:83], v[184:187]\n\t"
      "v_mfma_f32_16x16x32_bf16 v[188:191], v[148:151], v[84:87], v[188:191]\n\t"
      "v_mfma_f32_16x16x32_bf16 v[192:195], v[148:151], v[88:91], v[192:195]\n\t"
      "v_mfma_f32_16x16x32_bf16 v[196:199], v[148:151], v[92:95], v[196:199]\n\t"
      "v_mfma_f32_16x16x32_bf16 v[184:187], v[152:155], v[96:99], v[184:187]\n\t"
      "v_mfma_f32_16x16x32_bf16 v[188:191], v[152:155], v[100:103], v[188:191]\n\t"
      "v_mfma_f32_16x16x32_bf16 v[192:195], v[152:155], v[104:107], v[192:195]\n\t"
      "v_mfma_f32_16x16x32_bf16 v[196:199], v[152:155], v[108:111], v[196:199]\n\t"
      "global_load_dwordx4 v[128:131], v224, s[58:59] sc1\n\t"
      "global_load_dwordx4 v[132:135], v225, s[58:59] sc1\n\t"
      "global_load_dwordx4 v[136:139], v226, s[58:59] sc1\n\t"
      "global_load_dwordx4 v[140:143], v227, s[58:59] sc1\n\t"
      "global_load_dwordx4 v[144:147], v228, s[58:59] sc1\n\t"
      "global_load_dwordx4 v[148:151], v229, s[58:59] sc1\n\t"
      "global_load_dwordx4 v[152:155], v230, s[58:59] sc1\n\t"
      "s_waitcnt vmcnt(0)\n\t"
      "s_nop 7\n\t"
      "s_branch NQ2B_%=\n\t"
      "FQ22R_%=:\n\t"
      "s_mov_b32 s51, 0\n\t"
      "FQ22L_%=:\n\t"
      "s_add_u32 s51, s51, 1\n\t"
      "s_cmp_lt_u32 s51, 262144\n\t"
      "s_cbranch_scc0 FQ22X_%=\n\t"
      "s_waitcnt vmcnt(0)\n\t"
      "s_sleep 4\n\t"
      "global_load_dwordx4 v[132:135], v225, s[58:59] sc1\n\t"
      "global_load_dwordx4 v[136:139], v226, s[58:59] sc1\n\t"
      "global_load_dwordx4 v[140:143], v227, s[58:59] sc1\n\t"
      "global_load_dwordx4 v[144:147], v228, s[58:59] sc1\n\t"
      "global_load_dwordx4 v[148:151], v229, s[58:59] sc1\n\t"
      "global_load_dwordx4 v[152:155], v230, s[58:59] sc1\n\t"
      "s_waitcnt vmcnt(5)\n\t"
      "v_max3_u32 v234, v132, v133, v134\n\t"
      "v_max_u32_e32 v234, v234, v135\n\t"
      "v_cmp_eq_u32_e32 vcc, -1, v234\n\t"
      "s_cbranch_vccnz FQ22L_%=\n\t"
      "FQ22X_%=:\n\t"
      "s_branch FQ22B_%=\n\t"
      "FQ23R_%=:\n\t"
      "s_mov_b32 s51, 0\n\t"
      "FQ23L_%=:\n\t"
      "s_add_u32 s51, s51, 1\n\t"
      "s_cmp_lt_u32 s51, 262144\n\t"
      "s_cbranch_scc0 FQ23X_%=\n\t"
      "s_waitcnt vmcnt(0)\n\t"
      "s_sleep 4\n\t"
      "global_load_dwordx4 v[136:139], v226, s[58:59] sc1\n\t"
      "global_load_dwordx4 v[140:143], v227, s[58:59] sc1\n\t"
      "global_load_dwordx4 v[144:147], v228, s[58:59] sc1\n\t"
      "global_load_dwordx4 v[148:151], v229, s[58:59] sc1\n\t"
      "global_load_dwordx4 v[152:155], v230, s[58:59] sc1\n\t"
      "global_load_dwordx4 v[128:131], v224, s[60:61] sc1\n\t"
      "s_waitcnt vmcnt(5)\n\t"
      "v_max3_u32 v234, v136, v137, v138\n\t"
      "v_max_u32_e32 v234, v234, v139\n\t"
      "v_cmp_eq_u32_e32 vcc, -1, v234\n\t"
      "s_cbranch_vccnz FQ23L_%=\n\t"
      "FQ23X_%=:\n\t"
      "s_branch FQ23B_%=\n\t"
      "FQ24R_%=:\n\t"
      "s_mov_b32 s51, 0\n\t"
      "FQ24L_%=:\n\t"
      "s_add_u32 s51, s51, 1\n\t"
      "s_cmp_lt_u32 s51, 262144\n\t"
      "s_cbranch_scc0 FQ24X_%=\n\t"
      "s_waitcnt vmcnt(0)\n\t"
      "s_sleep 4\n\t"
      "global_load_dwordx4 v[140:143], v227, s[58:59] sc1\n\t"
      "global_load_dwordx4 v[144:147], v228, s[58:59] sc1\n\t"
      "global_load_dwordx4 v[148:151], v229, s[58:59] sc1\n\t"
      "global_load_dwordx4 v[152:155], v230, s[58:59] sc1\n\t"
      "global_load_dwordx4 v[128:131], v224, s[60:61] sc1\n\t"
      "global_load_dwordx4 v[132:135], v225, s[60:61] sc1\n\t"
      "s_waitcnt vmcnt(5)\n\t"
      "v_max3_u32 v234, v140, v141, v142\n\t"
      "v_max_u32_e32 v234, v234, v143\n\t"
      "v_cmp_eq_u32_e32 vcc, -1, v234\n\t"
      "s_cbranch_vccnz FQ24L_%=\n\t"
      "FQ24X_%=:\n\t"
      "s_branch FQ24B_%=\n\t"
      "FQ25R_%=:\n\t"
      "s_mov_b32 s51, 0\n\t"
      "FQ25L_%=:\n\t"
      "s_add_u32 s51, s51, 1\n\t"
      "s_cmp_lt_u32 s51, 262144\n\t"
      "s_cbranch_scc0 FQ25X_%=\n\t"
      "s_waitcnt vmcnt(0)\n\t"
      "s_sleep 4\n\t"
      "global_load_dwordx4 v[144:147], v228, s[58:59] sc1\n\t"
      "global_load_dwordx4 v[148:151], v229, s[58:59] sc1\n\t"
      "global_load_dwordx4 v[152:155], v230, s[58:59] sc1\n\t"
      "global_load_dwordx4 v[128:131], v224, s[60:61] sc1\n\t"
      "global_load_dwordx4 v[132:135], v225, s[60:61] sc1\n\t"
      "global_load_dwordx4 v[136:139], v226, s[60:61] sc1\n\t"
      "s_waitcnt vmcnt(5)\n\t"
      "v_max3_u32 v234, v144, v145, v146\n\t"
      "v_max_u32_e32 v234, v234, v147\n\t"
      "v_cmp_eq_u32_e32 vcc, -1, v234\n\t"
      "s_cbranch_vccnz FQ25L_%=\n\t"
      "FQ25X_%=:\n\t"
      "s_branch FQ25B_%=\n\t"
      "FQ26R_%=:\n\t"
      "s_mov_b32 s51, 0\n\t"
      "FQ26L_%=:\n\t"
      "s_add_u32 s51, s51, 1\n\t"
      "s_cmp_lt_u32 s51, 262144\n\t"
      "s_cbranch_scc0 FQ26X_%=\n\t"
      "s_waitcnt vmcnt(0)\n\t"
      "s_sleep 4\n\t"
      "global_load_dwordx4 v[148:151], v229, s[58:59] sc1\n\t"
      "global_load_dwordx4 v[152:155], v230, s[58:59] sc1\n\t"
      "global_load_dwordx4 v[128:131], v224, s[60:61] sc1\n\t"
      "global_load_dwordx4 v[132:135], v225, s[60:61] sc1\n\t"
      "global_load_dwordx4 v[136:139], v226, s[60:61] sc1\n\t"
      "global_load_dwordx4 v[140:143], v227, s[60:61] sc1\n\t"
      "s_waitcnt vmcnt(5)\n\t"
      "v_max3_u32 v234, v148, v149, v150\n\t"
      "v_max_u32_e32 v234, v234, v151\n\t"
      "v_cmp_eq_u32_e32 vcc, -1, v234\n\t"
      "s_cbranch_vccnz FQ26L_%=\n\t"
      "FQ26X_%=:\n\t"
      "s_branch FQ26B_%=\n\t"
      "FQ27R_%=:\n\t"
      "s_mov_b32 s51, 0\n\t"
      "FQ27L_%=:\n\t"
      "s_add_u32 s51, s51, 1\n\t"
      "s_cmp_lt_u32 s51, 262144\n\t"
      "s_cbranch_scc0 FQ27X_%=\n\t"
      "s_waitcnt vmcnt(0)\n\t"
      "s_sleep 4\n\t"
      "global_load_dwordx4 v[152:155], v230, s[58:59] sc1\n\t"
      "global_load_dwordx4 v[128:131], v224, s[60:61] sc1\n\t"
      "global_load_dwordx4 v[132:135], v225, s[60:61] sc1\n\t"
      "global_load_dwordx4 v[136:139], v226, s[60:61] sc1\n\t"
      "global_load_dwordx4 v[140:143], v227, s[60:61] sc1\n\t"
      "global_load_dwordx4 v[144:147], v228, s[60:61] sc1\n\t"
      "s_waitcnt vmcnt(5)\n\t"
      "v_max3_u32 v234, v152, v153, v154\n\t"
      "v_max_u32_e32 v234, v234, v155\n\t"
      "v_cmp_eq_u32_e32 vcc, -1, v234\n\t"
      "s_cbranch_vccnz FQ27L_%=\n\t"
      "FQ27X_%=:\n\t"
      "s_branch FQ27B_%=\n\t"
      "FQ28R_%=:\n\t"
      "s_mov_b32 s51, 0\n\t"
      "FQ28L_%=:\n\t"
      "s_add_u32 s51, s51, 1\n\t"
      "s_cmp_lt_u32 s51, 262144\n\t"
      "s_cbranch_scc0 FQ28X_%=\n\t"
      "s_waitcnt vmcnt(0)\n\t"
      "s_sleep 4\n\t"
      "global_load_dwordx4 v[128:131], v224, s[60:61] sc1\n\t"
      "global_load_dwordx4 v[132:135], v225, s[60:61] sc1\n\t"
      "global_load_dwordx4 v[136:139], v226, s[60:61] sc1\n\t"
      "global_load_dwordx4 v[140:143], v227, s[60:61] sc1\n\t"
      "global_load_dwordx4 v[144:147], v228, s[60:61] sc1\n\t"
      "global_load_dwordx4 v[148:151], v229, s[60:61] sc1\n\t"
      "s_waitcnt vmcnt(5)\n\t"
      "v_max3_u32 v234, v128, v129, v130\n\t"
      "v_max_u32_e32 v234, v234, v131\n\t"
      "v_cmp_eq_u32_e32 vcc, -1, v234\n\t"
      "s_cbranch_vccnz FQ28L_%=\n\t"
      "FQ28X_%=:\n\t"
      "s_branch FQ28B_%=\n\t"
      "NQ3R_%=:\n\t"
      "s_mov_b32 s51, 0\n\t"
      "NQ3L_%=:\n\t"
      "s_add_u32 s51, s51, 1\n\t"
      "s_cmp_lt_u32 s51, 262144\n\t"
      "s_cbranch_scc0 NQ3X_%=\n\t"
      "s_waitcnt vmcnt(0)\n\t"
      "s_sleep 1\n\t"
      "global_load_dwordx4 v[128:131], v224, s[58:59] sc1\n\t"
      "global_load_dwordx4 v[132:135], v225, s[58:59] sc1\n\t"
      "global_load_dwordx4 v[136:139], v226, s[58:59] sc1\n\t"
      "global_load_dwordx4 v[140:143], v227, s[58:59] sc1\n\t"
      "global_load_dwordx4 v[144:147], v228, s[58:59] sc1\n\t"
      "global_load_dwordx4 v[148:151], v229, s[58:59] sc1\n\t"
      "global_load_dwordx4 v[152:155], v230, s[58:59] sc1\n\t"
      "s_waitcnt vmcnt(0)\n\t"
      "v_max3_u32 v234, v128, v129, v130\n\t"
      "v_max3_u32 v234, v234, v131, v132\n\t"
      "v_max3_u32 v234, v234, v133, v134\n\t"
      "v_max3_u32 v234, v234, v135, v136\n\t"
      "v_max3_u32 v234, v234, v137, v138\n\t"
      "v_max3_u32 v234, v234, v139, v140\n\t"
      "v_max3_u32 v234, v234, v141, v142\n\t"
      "v_max3_u32 v234, v234, v143, v144\n\t"
      "v_max3_u32 v234, v234, v145, v146\n\t"
      "v_max3_u32 v234, v234, v147, v148\n\t"
      "v_max3_u32 v234, v234, v149, v150\n\t"
      "v_max3_u32 v234, v234, v151, v152\n\t"
      "v_max3_u32 v234, v234, v153, v154\n\t"
      "v_max_u32_e32 v234, v234, v155\n\t"
      "v_cmp_eq_u32_e32 vcc, -1, v234\n\t"
      "s_cbranch_vccnz NQ3L_%=\n\t"
      "NQ3X_%=:\n\t"
      "v_mfma_f32_16x16x32_bf16 v[200:203], v[128:131], v[0:3], 0\n\t"
      "v_mfma_f32_16x16x32_bf16 v[204:207], v[128:131], v[4:7], 0\n\t"
      "v_mfma_f32_16x16x32_bf16 v[208:211], v[128:131], v[8:11], 0\n\t"
      "v_mfma_f32_16x16x32_bf16 v[212:215], v[128:131], v[12:15], 0\n\t"
      "v_mfma_f32_16x16x32_bf16 v[200:203], v[132:135], v[16:19], v[200:203]\n\t"
      "v_mfma_f32_16x16x32_bf16 v[204:207], v[132:135], v[20:23], v[204:207]\n\t"
      "v_mfma_f32_16x16x32_bf16 v[208:211], v[132:135], v[24:27], v[208:211]\n\t"
      "v_mfma_f32_16x16x32_bf16 v[212:215], v[132:135], v[28:31], v[212:215]\n\t"
      "v_mfma_f32_16x16x32_bf16 v[200:203], v[136:139], v[32:35], v[200:203]\n\t"
      "v_mfma_f32_16x16x32_bf16 v[204:207], v[136:139], v[36:39], v[204:207]\n\t"
      "v_mfma_f32_16x16x32_bf16 v[208:211], v[136:139], v[40:43], v[208:211]\n\t"
      "v_mfma_f32_16x16x32_bf16 v[212:215], v[136:139], v[44:47], v[212:215]\n\t"
      "v_mfma_f32_16x16x32_bf16 v[200:203], v[140:143], v[48:51], v[200:203]\n\t"
      "v_mfma_f32_16x16x32_bf16 v[204:207], v[140:143], v[52:55], v[204:207]\n\t"
      "v_mfma_f32_16x16x32_bf16 v[208:211], v[140:143], v[56:59], v[208:211]\n\t"
      "v_mfma_f32_16x16x32_bf16 v[212:215], v[140:143], v[60:63], v[212:215]\n\t"
      "v_mfma_f32_16x16x32_bf16 v[200:203], v[144:147], v[64:67], v[200:203]\n\t"
      "v_mfma_f32_16x16x32_bf16 v[204:207], v[144:147], v[68:71], v[204:207]\n\t"
      "v_mfma_f32_16x16x32_bf16 v[208:211], v[144:147], v[72:75], v[208:211]\n\t"
      "v_mfma_f32_16x16x32_bf16 v[212:215], v[144:147], v[76:79], v[212:215]\n\t"
      "v_mfma_f32_16x16x32_bf16 v[200:203], v[148:151], v[80:83], v[200:203]\n\t"
      "v_mfma_f32_16x16x32_bf16 v[204:207], v[148:151], v[84:87], v[204:207]\n\t"
      "v_mfma_f32_16x16x32_bf16 v[208:211], v[148:151], v[88:91], v[208:211]\n\t"
      "v_mfma_f32_16x16x32_bf16 v[212:215], v[148:151], v[92:95], v[212:215]\n\t"
      "v_mfma_f32_16x16x32_bf16 v[200:203], v[152:155], v[96:99], v[200:203]\n\t"
      "v_mfma_f32_16x16x32_bf16 v[204:207], v[152:155], v[100:103], v[204:207]\n\t"
      "v_mfma_f32_16x16x32_bf16 v[208:211], v[152:155], v[104:107], v[208:211]\n\t"
      "v_mfma_f32_16x16x32_bf16 v[212:215], v[152:155], v[108:111], v[212:215]\n\t"
      "global_load_dwordx4 v[128:131], v224, s[60:61] sc1\n\t"
      "global_load_dwordx4 v[132:135], v225, s[60:61] sc1\n\t"
      "global_load_dwordx4 v[136:139], v226, s[60:61] sc1\n\t"
      "global_load_dwordx4 v[140:143], v227, s[60:61] sc1\n\t"
      "global_load_dwordx4 v[144:147], v228, s[60:61] sc1\n\t"
      "global_load_dwordx4 v[148:151], v229, s[60:61] sc1\n\t"
      "global_load_dwordx4 v[152:155], v230, s[60:61] sc1\n\t"
      "s_waitcnt vmcnt(0)\n\t"
      "s_nop 7\n\t"
      "s_branch NQ3B_%=\n\t"
      "FQ29R_%=:\n\t"
      "s_mov_b32 s51, 0\n\t"
      "FQ29L_%=:\n\t"
      "s_add_u32 s51, s51, 1\n\t"
      "s_cmp_lt_u32 s51, 262144\n\t"
      "s_cbranch_scc0 FQ29X_%=\n\t"
      "s_waitcnt vmcnt(0)\n\t"
      "s_sleep 4\n\t"
      "global_load_dwordx4 v[132:135], v225, s[60:61] sc1\n\t"
      "global_load_dwordx4 v[136:139], v226, s[60:61] sc1\n\t"
      "global_load_dwordx4 v[140:143], v227, s[60:61] sc1\n\t"
      "global_load_dwordx4 v[144:147], v228, s[60:61] sc1\n\t"
      "global_load_dwordx4 v[148:151], v229, s[60:61] sc1\n\t"
      "global_load_dwordx4 v[152:155], v230, s[60:61] sc1\n\t"
      "s_waitcnt vmcnt(5)\n\t"
      "v_max3_u32 v234, v132, v133, v134\n\t"
      "v_max_u32_e32 v234, v234, v135\n\t"
      "v_cmp_eq_u32_e32 vcc, -1, v234\n\t"
      "s_cbranch_vccnz FQ29L_%=\n\t"
      "FQ29X_%=:\n\t"
      "s_branch FQ29B_%=\n\t"
      "FQ30R_%=:\n\t"
      "s_mov_b32 s51, 0\n\t"
      "FQ30L_%=:\n\t"
      "s_add_u32 s51, s51, 1\n\t"
      "s_cmp_lt_u32 s51, 262144\n\t"
      "s_cbranch_scc0 FQ30X_%=\n\t"
      "s_waitcnt vmcnt(0)\n\t"
      "s_sleep 4\n\t"
      "global_load_dwordx4 v[136:139], v226, s[60:61] sc1\n\t"
      "global_load_dwordx4 v[140:143], v227, s[60:61] sc1\n\t"
      "global_load_dwordx4 v[144:147], v228, s[60:61] sc1\n\t"
      "global_load_dwordx4 v[148:151], v229, s[60:61] sc1\n\t"
      "global_load_dwordx4 v[152:155], v230, s[60:61] sc1\n\t"
      "global_load_dwordx4 v[128:131], v224, s[62:63] sc1\n\t"
      "s_waitcnt vmcnt(5)\n\t"
      "v_max3_u32 v234, v136, v137, v138\n\t"
      "v_max_u32_e32 v234, v234, v139\n\t"
      "v_cmp_eq_u32_e32 vcc, -1, v234\n\t"
      "s_cbranch_vccnz FQ30L_%=\n\t"
      "FQ30X_%=:\n\t"
      "s_branch FQ30B_%=\n\t"
      "FQ31R_%=:\n\t"
      "s_mov_b32 s51, 0\n\t"
      "FQ31L_%=:\n\t"
      "s_add_u32 s51, s51, 1\n\t"
      "s_cmp_lt_u32 s51, 262144\n\t"
      "s_cbranch_scc0 FQ31X_%=\n\t"
      "s_waitcnt vmcnt(0)\n\t"
      "s_sleep 4\n\t"
      "global_load_dwordx4 v[140:143], v227, s[60:61] sc1\n\t"
      "global_load_dwordx4 v[144:147], v228, s[60:61] sc1\n\t"
      "global_load_dwordx4 v[148:151], v229, s[60:61] sc1\n\t"
      "global_load_dwordx4 v[152:155], v230, s[60:61] sc1\n\t"
      "global_load_dwordx4 v[128:131], v224, s[62:63] sc1\n\t"
      "global_load_dwordx4 v[132:135], v225, s[62:63] sc1\n\t"
      "s_waitcnt vmcnt(5)\n\t"
      "v_max3_u32 v234, v140, v141, v142\n\t"
      "v_max_u32_e32 v234, v234, v143\n\t"
      "v_cmp_eq_u32_e32 vcc, -1, v234\n\t"
      "s_cbranch_vccnz FQ31L_%=\n\t"
      "FQ31X_%=:\n\t"
      "s_branch FQ31B_%=\n\t"
      "FQ32R_%=:\n\t"
      "s_mov_b32 s51, 0\n\t"
      "FQ32L_%=:\n\t"
      "s_add_u32 s51, s51, 1\n\t"
      "s_cmp_lt_u32 s51, 262144\n\t"
      "s_cbranch_scc0 FQ32X_%=\n\t"
      "s_waitcnt vmcnt(0)\n\t"
      "s_sleep 4\n\t"
      "global_load_dwordx4 v[144:147], v228, s[60:61] sc1\n\t"
      "global_load_dwordx4 v[148:151], v229, s[60:61] sc1\n\t"
      "global_load_dwordx4 v[152:155], v230, s[60:61] sc1\n\t"
      "global_load_dwordx4 v[128:131], v224, s[62:63] sc1\n\t"
      "global_load_dwordx4 v[132:135], v225, s[62:63] sc1\n\t"
      "global_load_dwordx4 v[136:139], v226, s[62:63] sc1\n\t"
      "s_waitcnt vmcnt(5)\n\t"
      "v_max3_u32 v234, v144, v145, v146\n\t"
      "v_max_u32_e32 v234, v234, v147\n\t"
      "v_cmp_eq_u32_e32 vcc, -1, v234\n\t"
      "s_cbranch_vccnz FQ32L_%=\n\t"
      "FQ32X_%=:\n\t"
      "s_branch FQ32B_%=\n\t"
      "FQ33R_%=:\n\t"
      "s_mov_b32 s51, 0\n\t"
      "FQ33L_%=:\n\t"
      "s_add_u32 s51, s51, 1\n\t"
      "s_cmp_lt_u32 s51, 262144\n\t"
      "s_cbranch_scc0 FQ33X_%=\n\t"
      "s_waitcnt vmcnt(0)\n\t"
      "s_sleep 4\n\t"
      "global_load_dwordx4 v[148:151], v229, s[60:61] sc1\n\t"
      "global_load_dwordx4 v[152:155], v230, s[60:61] sc1\n\t"
      "global_load_dwordx4 v[128:131], v224, s[62:63] sc1\n\t"
      "global_load_dwordx4 v[132:135], v225, s[62:63] sc1\n\t"
      "global_load_dwordx4 v[136:139], v226, s[62:63] sc1\n\t"
      "global_load_dwordx4 v[140:143], v227, s[62:63] sc1\n\t"
      "s_waitcnt vmcnt(5)\n\t"
      "v_max3_u32 v234, v148, v149, v150\n\t"
      "v_max_u32_e32 v234, v234, v151\n\t"
      "v_cmp_eq_u32_e32 vcc, -1, v234\n\t"
      "s_cbranch_vccnz FQ33L_%=\n\t"
      "FQ33X_%=:\n\t"
      "s_branch FQ33B_%=\n\t"
      "FQ34R_%=:\n\t"
      "s_mov_b32 s51, 0\n\t"
      "FQ34L_%=:\n\t"
      "s_add_u32 s51, s51, 1\n\t"
      "s_cmp_lt_u32 s51, 262144\n\t"
      "s_cbranch_scc0 FQ34X_%=\n\t"
      "s_waitcnt vmcnt(0)\n\t"
      "s_sleep 4\n\t"
      "global_load_dwordx4 v[152:155], v230, s[60:61] sc1\n\t"
      "global_load_dwordx4 v[128:131], v224, s[62:63] sc1\n\t"
      "global_load_dwordx4 v[132:135], v225, s[62:63] sc1\n\t"
      "global_load_dwordx4 v[136:139], v226, s[62:63] sc1\n\t"
      "global_load_dwordx4 v[140:143], v227, s[62:63] sc1\n\t"
      "global_load_dwordx4 v[144:147], v228, s[62:63] sc1\n\t"
      "s_waitcnt vmcnt(5)\n\t"
      "v_max3_u32 v234, v152, v153, v154\n\t"
      "v_max_u32_e32 v234, v234, v155\n\t"
      "v_cmp_eq_u32_e32 vcc, -1, v234\n\t"
      "s_cbranch_vccnz FQ34L_%=\n\t"
      "FQ34X_%=:\n\t"
      "s_branch FQ34B_%=\n\t"
      "FQ35R_%=:\n\t"
      "s_mov_b32 s51, 0\n\t"
      "FQ35L_%=:\n\t"
      "s_add_u32 s51, s51, 1\n\t"
      "s_cmp_lt_u32 s51, 262144\n\t"
      "s_cbranch_scc0 FQ35X_%=\n\t"
      "s_waitcnt vmcnt(0)\n\t"
      "s_sleep 4\n\t"
      "global_load_dwordx4 v[128:131], v224, s[62:63] sc1\n\t"
      "global_load_dwordx4 v[132:135], v225, s[62:63] sc1\n\t"
      "global_load_dwordx4 v[136:139], v226, s[62:63] sc1\n\t"
      "global_load_dwordx4 v[140:143], v227, s[62:63] sc1\n\t"
      "global_load_dwordx4 v[144:147], v228, s[62:63] sc1\n\t"
      "global_load_dwordx4 v[148:151], v229, s[62:63] sc1\n\t"
      "s_waitcnt vmcnt(5)\n\t"
      "v_max3_u32 v234, v128, v129, v130\n\t"
      "v_max_u32_e32 v234, v234, v131\n\t"
      "v_cmp_eq_u32_e32 vcc, -1, v234\n\t"
      "s_cbranch_vccnz FQ35L_%=\n\t"
      "FQ35X_%=:\n\t"
      "s_branch FQ35B_%=\n\t"
      "NQ4R_%=:\n\t"
      "s_mov_b32 s51, 0\n\t"
      "NQ4L_%=:\n\t"
      "s_add_u32 s51, s51, 1\n\t"
      "s_cmp_lt_u32 s51, 262144\n\t"
      "s_cbranch_scc0 NQ4X_%=\n\t"
      "s_waitcnt vmcnt(0)\n\t"
      "s_sleep 1\n\t"
      "global_load_dwordx4 v[128:131], v224, s[60:61] sc1\n\t"
      "global_load_dwordx4 v[132:135], v225, s[60:61] sc1\n\t"
      "global_load_dwordx4 v[136:139], v226, s[60:61] sc1\n\t"
      "global_load_dwordx4 v[140:143], v227, s[60:61] sc1\n\t"
      "global_load_dwordx4 v[144:147], v228, s[60:61] sc1\n\t"
      "global_load_dwordx4 v[148:151], v229, s[60:61] sc1\n\t"
      "global_load_dwordx4 v[152:155], v230, s[60:61] sc1\n\t"
      "s_waitcnt vmcnt(0)\n\t"
      "v_max3_u32 v234, v128, v129, v130\n\t"
      "v_max3_u32 v234, v234, v131, v132\n\t"
      "v_max3_u32 v234, v234, v133, v134\n\t"
      "v_max3_u32 v234, v234, v135, v136\n\t"
      "v_max3_u32 v234, v234, v137, v138\n\t"
      "v_max3_u32 v234, v234, v139, v140\n\t"
      "v_max3_u32 v234, v234, v141, v142\n\t"
      "v_max3_u32 v234, v234, v143, v144\n\t"
      "v_max3_u32 v234, v234, v145, v146\n\t"
      "v_max3_u32 v234, v234, v147, v148\n\t"
      "v_max3_u32 v234, v234, v149, v150\n\t"
      "v_max3_u32 v234, v234, v151, v152\n\t"
      "v_max3_u32 v234, v234, v153, v154\n\t"
      "v_max_u32_e32 v234, v234, v155\n\t"
      "v_cmp_eq_u32_e32 vcc, -1, v234\n\t"
      "s_cbranch_vccnz NQ4L_%=\n\t"
      "NQ4X_%=:\n\t"
      "v_mfma_f32_16x16x32_bf16 v[184:187], v[128:131], v[0:3], 0\n\t"
      "v_mfma_f32_16x16x32_bf16 v[188:191], v[128:131], v[4:7], 0\n\t"
      "v_mfma_f32_16x16x32_bf16 v[192:195], v[128:131], v[8:11], 0\n\t"
      "v_mfma_f32_16x16x32_bf16 v[196:199], v[128:131], v[12:15], 0\n\t"
      "v_mfma_f32_16x16x32_bf16 v[184:187], v[132:135], v[16:19], v[184:187]\n\t"
      "v_mfma_f32_16x16x32_bf16 v[188:191], v[132:135], v[20:23], v[188:191]\n\t"
      "v_mfma_f32_16x16x32_bf16 v[192:195], v[132:135], v[24:27], v[192:195]\n\t"
      "v_mfma_f32_16x16x32_bf16 v[196:199], v[132:135], v[28:31], v[196:199]\n\t"
      "v_mfma_f32_16x16x32_bf16 v[184:187], v[136:139], v[32:35], v[184:187]\n\t"
      "v_mfma_f32_16x16x32_bf16 v[188:191], v[136:139], v[36:39], v[188:191]\n\t"
      "v_mfma_f32_16x16x32_bf16 v[192:195], v[136:139], v[40:43], v[192:195]\n\t"
      "v_mfma_f32_16x16x32_bf16 v[196:199], v[136:139], v[44:47], v[196:199]\n\t"
      "v_mfma_f32_16x16x32_bf16 v[184:187], v[140:143], v[48:51], v[184:187]\n\t"
      "v_mfma_f32_16x16x32_bf16 v[188:191], v[140:143], v[52:55], v[188:191]\n\t"
      "v_mfma_f32_16x16x32_bf16 v[192:195], v[140:143], v[56:59], v[192:195]\n\t"
      "v_mfma_f32_16x16x32_bf16 v[196:199], v[140:143], v[60:63], v[196:199]\n\t"
      "v_mfma_f32_16x16x32_bf16 v[184:187], v[144:147], v[64:67], v[184:187]\n\t"
      "v_mfma_f32_16x16x32_bf16 v[188:191], v[144:147], v[68:71], v[188:191]\n\t"
      "v_mfma_f32_16x16x32_bf16 v[192:195], v[144:147], v[72:75], v[192:195]\n\t"
      "v_mfma_f32_16x16x32_bf16 v[196:199], v[144:147], v[76:79], v[196:199]\n\t"
      "v_mfma_f32_16x16x32_bf16 v[184:187], v[148:151], v[80:83], v[184:187]\n\t"
      "v_mfma_f32_16x16x32_bf16 v[188:191], v[148:151], v[84:87], v[188:191]\n\t"
      "v_mfma_f32_16x16x32_bf16 v[192:195], v[148:151], v[88:91], v[192:195]\n\t"
      "v_mfma_f32_16x16x32_bf16 v[196:199], v[148:151], v[92:95], v[196:199]\n\t"
      "v_mfma_f32_16x16x32_bf16 v[184:187], v[152:155], v[96:99], v[184:187]\n\t"
      "v_mfma_f32_16x16x32_bf16 v[188:191], v[152:155], v[100:103], v[188:191]\n\t"
      "v_mfma_f32_16x16x32_bf16 v[192:195], v[152:155], v[104:107], v[192:195]\n\t"
      "v_mfma_f32_16x16x32_bf16 v[196:199], v[152:155], v[108:111], v[196:199]\n\t"
      "global_load_dwordx4 v[128:131], v224, s[62:63] sc1\n\t"
      "global_load_dwordx4 v[132:135], v225, s[62:63] sc1\n\t"
      "global_load_dwordx4 v[136:139], v226, s[62:63] sc1\n\t"
      "global_load_dwordx4 v[140:143], v227, s[62:63] sc1\n\t"
      "global_load_dwordx4 v[144:147], v228, s[62:63] sc1\n\t"
      "global_load_dwordx4 v[148:151], v229, s[62:63] sc1\n\t"
      "global_load_dwordx4 v[152:155], v230, s[62:63] sc1\n\t"
      "s_waitcnt vmcnt(0)\n\t"
      "s_nop 7\n\t"
      "s_branch NQ4B_%=\n\t"
      "FQ36R_%=:\n\t"
      "s_mov_b32 s51, 0\n\t"
      "FQ36L_%=:\n\t"
      "s_add_u32 s51, s51, 1\n\t"
      "s_cmp_lt_u32 s51, 262144\n\t"
      "s_cbranch_scc0 FQ36X_%=\n\t"
      "s_waitcnt vmcnt(0)\n\t"
      "s_sleep 4\n\t"
      "global_load_dwordx4 v[132:135], v225, s[62:63] sc1\n\t"
      "global_load_dwordx4 v[136:139], v226, s[62:63] sc1\n\t"
      "global_load_dwordx4 v[140:143], v227, s[62:63] sc1\n\t"
      "global_load_dwordx4 v[144:147], v228, s[62:63] sc1\n\t"
      "global_load_dwordx4 v[148:151], v229, s[62:63] sc1\n\t"
      "global_load_dwordx4 v[152:155], v230, s[62:63] sc1\n\t"
      "s_waitcnt vmcnt(5)\n\t"
      "v_max3_u32 v234, v132, v133, v134\n\t"
      "v_max_u32_e32 v234, v234, v135\n\t"
      "v_cmp_eq_u32_e32 vcc, -1, v234\n\t"
      "s_cbranch_vccnz FQ36L_%=\n\t"
      "FQ36X_%=:\n\t"
      "s_branch FQ36B_%=\n\t"
      "FQ37R_%=:\n\t"
      "s_mov_b32 s51, 0\n\t"
      "FQ37L_%=:\n\t"
      "s_add_u32 s51, s51, 1\n\t"
      "s_cmp_lt_u32 s51, 262144\n\t"
      "s_cbranch_scc0 FQ37X_%=\n\t"
      "s_waitcnt vmcnt(0)\n\t"
      "s_sleep 4\n\t"
      "global_load_dwordx4 v[136:139], v226, s[62:63] sc1\n\t"
      "global_load_dwordx4 v[140:143], v227, s[62:63] sc1\n\t"
      "global_load_dwordx4 v[144:147], v228, s[62:63] sc1\n\t"
      "global_load_dwordx4 v[148:151], v229, s[62:63] sc1\n\t"
      "global_load_dwordx4 v[152:155], v230, s[62:63] sc1\n\t"
      "global_load_dwordx4 v[128:131], v224, s[64:65] sc1\n\t"
      "s_waitcnt vmcnt(5)\n\t"
      "v_max3_u32 v234, v136, v137, v138\n\t"
      "v_max_u32_e32 v234, v234, v139\n\t"
      "v_cmp_eq_u32_e32 vcc, -1, v234\n\t"
      "s_cbranch_vccnz FQ37L_%=\n\t"
      "FQ37X_%=:\n\t"
      "s_branch FQ37B_%=\n\t"
      "FQ38R_%=:\n\t"
      "s_mov_b32 s51, 0\n\t"
      "FQ38L_%=:\n\t"
      "s_add_u32 s51, s51, 1\n\t"
      "s_cmp_lt_u32 s51, 262144\n\t"
      "s_cbranch_scc0 FQ38X_%=\n\t"
      "s_waitcnt vmcnt(0)\n\t"
      "s_sleep 4\n\t"
      "global_load_dwordx4 v[140:143], v227, s[62:63] sc1\n\t"
      "global_load_dwordx4 v[144:147], v228, s[62:63] sc1\n\t"
      "global_load_dwordx4 v[148:151], v229, s[62:63] sc1\n\t"
      "global_load_dwordx4 v[152:155], v230, s[62:63] sc1\n\t"
      "global_load_dwordx4 v[128:131], v224, s[64:65] sc1\n\t"
      "global_load_dwordx4 v[132:135], v225, s[64:65] sc1\n\t"
      "s_waitcnt vmcnt(5)\n\t"
      "v_max3_u32 v234, v140, v141, v142\n\t"
      "v_max_u32_e32 v234, v234, v143\n\t"
      "v_cmp_eq_u32_e32 vcc, -1, v234\n\t"
      "s_cbranch_vccnz FQ38L_%=\n\t"
      "FQ38X_%=:\n\t"
      "s_branch FQ38B_%=\n\t"
      "FQ39R_%=:\n\t"
      "s_mov_b32 s51, 0\n\t"
      "FQ39L_%=:\n\t"
      "s_add_u32 s51, s51, 1\n\t"
      "s_cmp_lt_u32 s51, 262144\n\t"
      "s_cbranch_scc0 FQ39X_%=\n\t"
      "s_waitcnt vmcnt(0)\n\t"
      "s_sleep 4\n\t"
      "global_load_dwordx4 v[144:147], v228, s[62:63] sc1\n\t"
      "global_load_dwordx4 v[148:151], v229, s[62:63] sc1\n\t"
      "global_load_dwordx4 v[152:155], v230, s[62:63] sc1\n\t"
      "global_load_dwordx4 v[128:131], v224, s[64:65] sc1\n\t"
      "global_load_dwordx4 v[132:135], v225, s[64:65] sc1\n\t"
      "global_load_dwordx4 v[136:139], v226, s[64:65] sc1\n\t"
      "s_waitcnt vmcnt(5)\n\t"
      "v_max3_u32 v234, v144, v145, v146\n\t"
      "v_max_u32_e32 v234, v234, v147\n\t"
      "v_cmp_eq_u32_e32 vcc, -1, v234\n\t"
      "s_cbranch_vccnz FQ39L_%=\n\t"
      "FQ39X_%=:\n\t"
      "s_branch FQ39B_%=\n\t"
      "FQ40R_%=:\n\t"
      "s_mov_b32 s51, 0\n\t"
      "FQ40L_%=:\n\t"
      "s_add_u32 s51, s51, 1\n\t"
      "s_cmp_lt_u32 s51, 262144\n\t"
      "s_cbranch_scc0 FQ40X_%=\n\t"
      "s_waitcnt vmcnt(0)\n\t"
      "s_sleep 4\n\t"
      "global_load_dwordx4 v[148:151], v229, s[62:63] sc1\n\t"
      "global_load_dwordx4 v[152:155], v230, s[62:63] sc1\n\t"
      "global_load_dwordx4 v[128:131], v224, s[64:65] sc1\n\t"
      "global_load_dwordx4 v[132:135], v225, s[64:65] sc1\n\t"
      "global_load_dwordx4 v[136:139], v226, s[64:65] sc1\n\t"
      "global_load_dwordx4 v[140:143], v227, s[64:65] sc1\n\t"
      "s_waitcnt vmcnt(5)\n\t"
      "v_max3_u32 v234, v148, v149, v150\n\t"
      "v_max_u32_e32 v234, v234, v151\n\t"
      "v_cmp_eq_u32_e32 vcc, -1, v234\n\t"
      "s_cbranch_vccnz FQ40L_%=\n\t"
      "FQ40X_%=:\n\t"
      "s_branch FQ40B_%=\n\t"
      "FQ41R_%=:\n\t"
      "s_mov_b32 s51, 0\n\t"
      "FQ41L_%=:\n\t"
      "s_add_u32 s51, s51, 1\n\t"
      "s_cmp_lt_u32 s51, 262144\n\t"
      "s_cbranch_scc0 FQ41X_%=\n\t"
      "s_waitcnt vmcnt(0)\n\t"
      "s_sleep 4\n\t"
      "global_load_dwordx4 v[152:155], v230, s[62:63] sc1\n\t"
      "global_load_dwordx4 v[128:131], v224, s[64:65] sc1\n\t"
      "global_load_dwordx4 v[132:135], v225, s[64:65] sc1\n\t"
      "global_load_dwordx4 v[136:139], v226, s[64:65] sc1\n\t"
      "global_load_dwordx4 v[140:143], v227, s[64:65] sc1\n\t"
      "global_load_dwordx4 v[144:147], v228, s[64:65] sc1\n\t"
      "s_waitcnt vmcnt(5)\n\t"
      "v_max3_u32 v234, v152, v153, v154\n\t"
      "v_max_u32_e32 v234, v234, v155\n\t"
      "v_cmp_eq_u32_e32 vcc, -1, v234\n\t"
      "s_cbranch_vccnz FQ41L_%=\n\t"
      "FQ41X_%=:\n\t"
      "s_branch FQ41B_%=\n\t"
      "FQ42R_%=:\n\t"
      "s_mov_b32 s51, 0\n\t"
      "FQ42L_%=:\n\t"
      "s_add_u32 s51, s51, 1\n\t"
      "s_cmp_lt_u32 s51, 262144\n\t"
      "s_cbranch_scc0 FQ42X_%=\n\t"
      "s_sleep 1\n\t"
      "s_mov_b64 exec, 15\n\t"
      "v_mov_b64_e32 v[232:233], 0\n\t"
      "global_atomic_or_x2 v[232:233], v247, v[232:233], s[12:13] sc0 sc1\n\t"
      "s_waitcnt vmcnt(0)\n\t"
      "v_subrev_u32_e32 v232, s14, v232\n\t"
      "v_cmp_ne_u32_e64 s[48:49], s15, v233\n\t"
      "v_subrev_u32_e32 v232, 6, v232\n\t"
      "v_cmp_lt_u32_e32 vcc, 7, v232\n\t"
      "s_mov_b64 exec, -1\n\t"
      "s_or_b64 vcc, vcc, s[48:49]\n\t"
      "s_cbranch_vccnz FQ42L_%=\n\t"
      "FQ42X_%=:\n\t"
      "FQ42DR_%=:\n\t"
      "s_mov_b32 s51, 0\n\t"
      "FQ42DL_%=:\n\t"
      "s_add_u32 s51, s51, 1\n\t"
      "s_cmp_lt_u32 s51, 262144\n\t"
      "s_cbranch_scc0 FQ42DX_%=\n\t"
      "s_waitcnt vmcnt(0)\n\t"
      "s_sleep 1\n\t"
      "global_load_dwordx4 v[128:131], v224, s[64:65] sc1\n\t"
      "global_load_dwordx4 v[132:135], v225, s[64:65] sc1\n\t"
      "global_load_dwordx4 v[136:139], v226, s[64:65] sc1\n\t"
      "global_load_dwordx4 v[140:143], v227, s[64:65] sc1\n\t"
      "global_load_dwordx4 v[144:147], v228, s[64:65] sc1\n\t"
      "global_load_dwordx4 v[148:151], v229, s[64:65] sc1\n\t"
      "s_waitcnt vmcnt(5)\n\t"
      "v_max3_u32 v234, v128, v129, v130\n\t"
      "v_max_u32_e32 v234, v234, v131\n\t"
      "v_cmp_eq_u32_e32 vcc, -1, v234\n\t"
      "s_cbranch_vccnz FQ42DL_%=\n\t"
      "FQ42DX_%=:\n\t"
      "s_branch FQ42B_%=\n\t"
      "NQ5R_%=:\n\t"
      "s_mov_b32 s51, 0\n\t"
      "NQ5L_%=:\n\t"
      "s_add_u32 s51, s51, 1\n\t"
      "s_cmp_lt_u32 s51, 262144\n\t"
      "s_cbranch_scc0 NQ5X_%=\n\t"
      "s_waitcnt vmcnt(0)\n\t"
      "s_sleep 1\n\t"
      "global_load_dwordx4 v[128:131], v224, s[62:63] sc1\n\t"
      "global_load_dwordx4 v[132:135], v225, s[62:63] sc1\n\t"
      "global_load_dwordx4 v[136:139], v226, s[62:63] sc1\n\t"
      "global_load_dwordx4 v[140:143], v227, s[62:63] sc1\n\t"
      "global_load_dwordx4 v[144:147], v228, s[62:63] sc1\n\t"
      "global_load_dwordx4 v[148:151], v229, s[62:63] sc1\n\t"
      "global_load_dwordx4 v[152:155], v230, s[62:63] sc1\n\t"
      "s_waitcnt vmcnt(0)\n\t"
      "s_nop 0\n\t"
      "v_max3_u32 v234, v128, v129, v130\n\t"
      "v_max3_u32 v234, v234, v131, v132\n\t"
      "v_max3_u32 v234, v234, v133, v134\n\t"
      "v_max3_u32 v234, v234, v135, v136\n\t"
      "v_max3_u32 v234, v234, v137, v138\n\t"
      "v_max3_u32 v234, v234, v139, v140\n\t"
      "v_max3_u32 v234, v234, v141, v142\n\t"
      "v_max3_u32 v234, v234, v143, v144\n\t"
      "v_max3_u32 v234, v234, v145, v146\n\t"
      "v_max3_u32 v234, v234, v147, v148\n\t"
      "v_max3_u32 v234, v234, v149, v150\n\t"
      "v_max3_u32 v234, v234, v151, v152\n\t"
      "v_max3_u32 v234, v234, v153, v154\n\t"
      "v_max_u32_e32 v234, v234, v155\n\t"
      "v_cmp_eq_u32_e32 vcc, -1, v234\n\t"
      "s_cbranch_vccnz NQ5L_%=\n\t"
      "NQ5X_%=:\n\t"
      "v_mfma_f32_16x16x32_bf16 v[200:203], v[128:131], v[0:3], 0\n\t"
      "v_mfma_f32_16x16x32_bf16 v[204:207], v[128:131], v[4:7], 0\n\t"
      "v_mfma_f32_16x16x32_bf16 v[208:211], v[128:131], v[8:11], 0\n\t"
      "v_mfma_f32_16x16x32_bf16 v[212:215], v[128:131], v[12:15], 0\n\t"
      "v_mfma_f32_16x16x32_bf16 v[200:203], v[132:135], v[16:19], v[200:203]\n\t"
      "v_mfma_f32_16x16x32_bf16 v[204:207], v[132:135], v[20:23], v[204:207]\n\t"
      "v_mfma_f32_16x16x32_bf16 v[208:211], v[132:135], v[24:27], v[208:211]\n\t"
      "v_mfma_f32_16x16x32_bf16 v[212:215], v[132:135], v[28:31], v[212:215]\n\t"
      "v_mfma_f32_16x16x32_bf16 v[200:203], v[136:139], v[32:35], v[200:203]\n\t"
      "v_mfma_f32_16x16x32_bf16 v[204:207], v[136:139], v[36:39], v[204:207]\n\t"
      "v_mfma_f32_16x16x32_bf16 v[208:211], v[136:139], v[40:43], v[208:211]\n\t"
      "v_mfma_f32_16x16x32_bf16 v[212:215], v[136:139], v[44:47], v[212:215]\n\t"
      "v_mfma_f32_16x16x32_bf16 v[200:203], v[140:143], v[48:51], v[200:203]\n\t"
      "v_mfma_f32_16x16x32_bf16 v[204:207], v[140:143], v[52:55], v[204:207]\n\t"
      "v_mfma_f32_16x16x32_bf16 v[208:211], v[140:143], v[56:59], v[208:211]\n\t"
      "v_mfma_f32_16x16x32_bf16 v[212:215], v[140:143], v[60:63], v[212:215]\n\t"
      "v_mfma_f32_16x16x32_bf16 v[200:203], v[144:147], v[64:67], v[200:203]\n\t"
      "v_mfma_f32_16x16x32_bf16 v[204:207], v[144:147], v[68:71], v[204:207]\n\t"
      "v_mfma_f32_16x16x32_bf16 v[208:211], v[144:147], v[72:75], v[208:211]\n\t"
      "v_mfma_f32_16x16x32_bf16 v[212:215], v[144:147], v[76:79], v[212:215]\n\t"
      "v_mfma_f32_16x16x32_bf16 v[200:203], v[148:151], v[80:83], v[200:203]\n\t"
      "v_mfma_f32_16x16x32_bf16 v[204:207], v[148:151], v[84:87], v[204:207]\n\t"
      "v_mfma_f32_16x16x32_bf16 v[208:211], v[148:151], v[88:91], v[208:211]\n\t"
      "v_mfma_f32_16x16x32_bf16 v[212:215], v[148:151], v[92:95], v[212:215]\n\t"
      "v_mfma_f32_16x16x32_bf16 v[200:203], v[152:155], v[96:99], v[200:203]\n\t"
      "v_mfma_f32_16x16x32_bf16 v[204:207], v[152:155], v[100:103], v[204:207]\n\t"
      "v_mfma_f32_16x16x32_bf16 v[208:211], v[152:155], v[104:107], v[208:211]\n\t"
      "v_mfma_f32_16x16x32_bf16 v[212:215], v[152:155], v[108:111], v[212:215]\n\t"
      "global_load_dwordx4 v[128:131], v224, s[64:65] sc1\n\t"
      "global_load_dwordx4 v[132:135], v225, s[64:65] sc1\n\t"
      "global_load_dwordx4 v[136:139], v226, s[64:65] sc1\n\t"
      "global_load_dwordx4 v[140:143], v227, s[64:65] sc1\n\t"
      "global_load_dwordx4 v[144:147], v228, s[64:65] sc1\n\t"
      "global_load_dwordx4 v[148:151], v229, s[64:65] sc1\n\t"
      "global_load_dwordx4 v[152:155], v230, s[64:65] sc1\n\t"
      "s_waitcnt vmcnt(0)\n\t"
      "s_nop 7\n\t"
      "s_branch NQ5B_%=\n\t"
      "FQ43R_%=:\n\t"
      "s_mov_b32 s51, 0\n\t"
      "FQ43L_%=:\n\t"
      "s_add_u32 s51, s51, 1\n\t"
      "s_cmp_lt_u32 s51, 262144\n\t"
      "s_cbranch_scc0 FQ43X_%=\n\t"
      "s_sleep 1\n\t"
      "s_mov_b64 exec, 15\n\t"
      "v_mov_b64_e32 v[232:233], 0\n\t"
      "global_atomic_or_x2 v[232:233], v247, v[232:233], s[12:13] sc0 sc1\n\t"
      "s_waitcnt vmcnt(0)\n\t"
      "v_subrev_u32_e32 v232, s14, v232\n\t"
      "v_cmp_ne_u32_e64 s[48:49], s15, v233\n\t"
      "v_subrev_u32_e32 v232, 6, v232\n\t"
      "v_cmp_lt_u32_e32 vcc, 7, v232\n\t"
      "s_mov_b64 exec, -1\n\t"
      "s_or_b64 vcc, vcc, s[48:49]\n\t"
      "s_cbranch_vccnz FQ43L_%=\n\t"
      "FQ43X_%=:\n\t"
      "FQ43DR_%=:\n\t"
      "s_mov_b32 s51, 0\n\t"
      "FQ43DL_%=:\n\t"
      "s_add_u32 s51, s51, 1\n\t"
      "s_cmp_lt_u32 s51, 262144\n\t"
      "s_cbranch_scc0 FQ43DX_%=\n\t"
      "s_waitcnt vmcnt(0)\n\t"
      "s_sleep 1\n\t"
      "global_load_dwordx4 v[132:135], v225, s[64:65] sc1\n\t"
      "global_load_dwordx4 v[136:139], v226, s[64:65] sc1\n\t"
      "global_load_dwordx4 v[140:143], v227, s[64:65] sc1\n\t"
      "global_load_dwordx4 v[144:147], v228, s[64:65] sc1\n\t"
      "global_load_dwordx4 v[148:151], v229, s[64:65] sc1\n\t"
      "global_load_dwordx4 v[152:155], v230, s[64:65] sc1\n\t"
      "s_waitcnt vmcnt(5)\n\t"
      "v_max3_u32 v234, v132, v133, v134\n\t"
      "v_max_u32_e32 v234, v234, v135\n\t"
      "v_cmp_eq_u32_e32 vcc, -1, v234\n\t"
      "s_cbranch_vccnz FQ43DL_%=\n\t"
      "FQ43DX_%=:\n\t"
      "s_branch FQ43B_%=\n\t"
      "FQ44R_%=:\n\t"
      "s_mov_b32 s51, 0\n\t"
      "FQ44L_%=:\n\t"
      "s_add_u32 s51, s51, 1\n\t"
      "s_cmp_lt_u32 s51, 262144\n\t"
      "s_cbranch_scc0 FQ44X_%=\n\t"
      "s_sleep 1\n\t"
      "s_mov_b64 exec, 15\n\t"
      "v_mov_b64_e32 v[232:233], 0\n\t"
      "global_atomic_or_x2 v[232:233], v247, v[232:233], s[12:13] sc0 sc1\n\t"
      "s_waitcnt vmcnt(0)\n\t"
      "v_subrev_u32_e32 v232, s14, v232\n\t"
      "v_cmp_ne_u32_e64 s[48:49], s15, v233\n\t"
      "v_subrev_u32_e32 v232, 6, v232\n\t"
      "v_cmp_lt_u32_e32 vcc, 7, v232\n\t"
      "s_mov_b64 exec, -1\n\t"
      "s_or_b64 vcc, vcc, s[48:49]\n\t"
      "s_cbranch_vccnz FQ44L_%=\n\t"
      "FQ44X_%=:\n\t"
      "FQ44DR_%=:\n\t"
      "s_mov_b32 s51, 0\n\t"
      "FQ44DL_%=:\n\t"
      "s_add_u32 s51, s51, 1\n\t"
      "s_cmp_lt_u32 s51, 262144\n\t"
      "s_cbranch_scc0 FQ44DX_%=\n\t"
      "s_waitcnt vmcnt(0)\n\t"
      "s_sleep 1\n\t"
      "global_load_dwordx4 v[136:139], v226, s[64:65] sc1\n\t"
      "global_load_dwordx4 v[140:143], v227, s[64:65] sc1\n\t"
      "global_load_dwordx4 v[144:147], v228, s[64:65] sc1\n\t"
      "global_load_dwordx4 v[148:151], v229, s[64:65] sc1\n\t"
      "global_load_dwordx4 v[152:155], v230, s[64:65] sc1\n\t"
      "s_waitcnt vmcnt(4)\n\t"
      "v_max3_u32 v234, v136, v137, v138\n\t"
      "v_max_u32_e32 v234, v234, v139\n\t"
      "v_cmp_eq_u32_e32 vcc, -1, v234\n\t"
      "s_cbranch_vccnz FQ44DL_%=\n\t"
      "FQ44DX_%=:\n\t"
      "s_branch FQ44B_%=\n\t"
      "FQ45R_%=:\n\t"
      "s_mov_b32 s51, 0\n\t"
      "FQ45L_%=:\n\t"
      "s_add_u32 s51, s51, 1\n\t"
      "s_cmp_lt_u32 s51, 262144\n\t"
      "s_cbranch_scc0 FQ45X_%=\n\t"
      "s_sleep 1\n\t"
      "s_mov_b64 exec, 15\n\t"
      "v_mov_b64_e32 v[232:233], 0\n\t"
      "global_atomic_or_x2 v[232:233], v247, v[232:233], s[12:13] sc0 sc1\n\t"
      "s_waitcnt vmcnt(0)\n\t"
      "v_subrev_u32_e32 v232, s14, v232\n\t"
      "v_cmp_ne_u32_e64 s[48:49], s15, v233\n\t"
      "v_subrev_u32_e32 v232, 6, v232\n\t"
      "v_cmp_lt_u32_e32 vcc, 7, v232\n\t"
      "s_mov_b64 exec, -1\n\t"
      "s_or_b64 vcc, vcc, s[48:49]\n\t"
      "s_cbranch_vccnz FQ45L_%=\n\t"
      "FQ45X_%=:\n\t"
      "FQ45DR_%=:\n\t"
      "s_mov_b32 s51, 0\n\t"
      "FQ45DL_%=:\n\t"
      "s_add_u32 s51, s51, 1\n\t"
      "s_cmp_lt_u32 s51, 262144\n\t"
      "s_cbranch_scc0 FQ45DX_%=\n\t"
      "s_waitcnt vmcnt(0)\n\t"
      "s_sleep 1\n\t"
      "global_load_dwordx4 v[140:143], v227, s[64:65] sc1\n\t"
      "global_load_dwordx4 v[144:147], v228, s[64:65] sc1\n\t"
      "global_load_dwordx4 v[148:151], v229, s[64:65] sc1\n\t"
      "global_load_dwordx4 v[152:155], v230, s[64:65] sc1\n\t"
      "s_waitcnt vmcnt(3)\n\t"
      "v_max3_u32 v234, v140, v141, v142\n\t"
      "v_max_u32_e32 v234, v234, v143\n\t"
      "v_cmp_eq_u32_e32 vcc, -1, v234\n\t"
      "s_cbranch_vccnz FQ45DL_%=\n\t"
      "FQ45DX_%=:\n\t"
      "s_branch FQ45B_%=\n\t"
      "FQ46R_%=:\n\t"
      "s_mov_b32 s51, 0\n\t"
      "FQ46L_%=:\n\t"
      "s_add_u32 s51, s51, 1\n\t"
      "s_cmp_lt_u32 s51, 262144\n\t"
      "s_cbranch_scc0 FQ46X_%=\n\t"
      "s_sleep 1\n\t"
      "s_mov_b64 exec, 15\n\t"
      "v_mov_b64_e32 v[232:233], 0\n\t"
      "global_atomic_or_x2 v[232:233], v247, v[232:233], s[12:13] sc0 sc1\n\t"
      "s_waitcnt vmcnt(0)\n\t"
      "v_subrev_u32_e32 v232, s14, v232\n\t"
      "v_cmp_ne_u32_e64 s[48:49], s15, v233\n\t"
      "v_subrev_u32_e32 v232, 6, v232\n\t"
      "v_cmp_lt_u32_e32 vcc, 7, v232\n\t"
      "s_mov_b64 exec, -1\n\t"
      "s_or_b64 vcc, vcc, s[48:49]\n\t"
      "s_cbranch_vccnz FQ46L_%=\n\t"
      "FQ46X_%=:\n\t"
      "FQ46DR_%=:\n\t"
      "s_mov_b32 s51, 0\n\t"
      "FQ46DL_%=:\n\t"
      "s_add_u32 s51, s51, 1\n\t"
      "s_cmp_lt_u32 s51, 262144\n\t"
      "s_cbranch_scc0 FQ46DX_%=\n\t"
      "s_waitcnt vmcnt(0)\n\t"
      "s_sleep 1\n\t"
      "global_load_dwordx4 v[144:147], v228, s[64:65] sc1\n\t"
      "global_load_dwordx4 v[148:151], v229, s[64:65] sc1\n\t"
      "global_load_dwordx4 v[152:155], v230, s[64:65] sc1\n\t"
      "s_waitcnt vmcnt(2)\n\t"
      "v_max3_u32 v234, v144, v145, v146\n\t"
      "v_max_u32_e32 v234, v234, v147\n\t"
      "v_cmp_eq_u32_e32 vcc, -1, v234\n\t"
      "s_cbranch_vccnz FQ46DL_%=\n\t"
      "FQ46DX_%=:\n\t"
      "s_branch FQ46B_%=\n\t"
      "FQ47R_%=:\n\t"
      "s_mov_b32 s51, 0\n\t"
      "FQ47L_%=:\n\t"
      "s_add_u32 s51, s51, 1\n\t"
      "s_cmp_lt_u32 s51, 262144\n\t"
      "s_cbranch_scc0 FQ47X_%=\n\t"
      "s_sleep 1\n\t"
      "s_mov_b64 exec, 15\n\t"
      "v_mov_b64_e32 v[232:233], 0\n\t"
      "global_atomic_or_x2 v[232:233], v247, v[232:233], s[12:13] sc0 sc1\n\t"
      "s_waitcnt vmcnt(0)\n\t"
      "v_subrev_u32_e32 v232, s14, v232\n\t"
      "v_cmp_ne_u32_e64 s[48:49], s15, v233\n\t"
      "v_subrev_u32_e32 v232, 6, v232\n\t"
      "v_cmp_lt_u32_e32 vcc, 7, v232\n\t"
      "s_mov_b64 exec, -1\n\t"
      "s_or_b64 vcc, vcc, s[48:49]\n\t"
      "s_cbranch_vccnz FQ47L_%=\n\t"
      "FQ47X_%=:\n\t"
      "FQ47DR_%=:\n\t"
      "s_mov_b32 s51, 0\n\t"
      "FQ47DL_%=:\n\t"
      "s_add_u32 s51, s51, 1\n\t"
      "s_cmp_lt_u32 s51, 262144\n\t"
      "s_cbranch_scc0 FQ47DX_%=\n\t"
      "s_waitcnt vmcnt(0)\n\t"
      "s_sleep 1\n\t"
      "global_load_dwordx4 v[148:151], v229, s[64:65] sc1\n\t"
      "global_load_dwordx4 v[152:155], v230, s[64:65] sc1\n\t"
      "s_waitcnt vmcnt(1)\n\t"
      "v_max3_u32 v234, v148, v149, v150\n\t"
      "v_max_u32_e32 v234, v234, v151\n\t"
      "v_cmp_eq_u32_e32 vcc, -1, v234\n\t"
      "s_cbranch_vccnz FQ47DL_%=\n\t"
      "FQ47DX_%=:\n\t"
      "s_branch FQ47B_%=\n\t"
      "FQ48R_%=:\n\t"
      "s_mov_b32 s51, 0\n\t"
      "FQ48L_%=:\n\t"
      "s_add_u32 s51, s51, 1\n\t"
      "s_cmp_lt_u32 s51, 262144\n\t"
      "s_cbranch_scc0 FQ48X_%=\n\t"
      "s_sleep 1\n\t"
      "s_mov_b64 exec, 15\n\t"
      "v_mov_b64_e32 v[232:233], 0\n\t"
      "global_atomic_or_x2 v[232:233], v247, v[232:233], s[12:13] sc0 sc1\n\t"
      "s_waitcnt vmcnt(0)\n\t"
      "v_subrev_u32_e32 v232, s14, v232\n\t"
      "v_cmp_ne_u32_e64 s[48:49], s15, v233\n\t"
      "v_subrev_u32_e32 v232, 6, v232\n\t"
      "v_cmp_lt_u32_e32 vcc, 7, v232\n\t"
      "s_mov_b64 exec, -1\n\t"
      "s_or_b64 vcc, vcc, s[48:49]\n\t"
      "s_cbranch_vccnz FQ48L_%=\n\t"
      "FQ48X_%=:\n\t"
      "FQ48DR_%=:\n\t"
      "s_mov_b32 s51, 0\n\t"
      "FQ48DL_%=:\n\t"
      "s_add_u32 s51, s51, 1\n\t"
      "s_cmp_lt_u32 s51, 262144\n\t"
      "s_cbranch_scc0 FQ48DX_%=\n\t"
      "s_waitcnt vmcnt(0)\n\t"
      "s_sleep 1\n\t"
      "global_load_dwordx4 v[152:155], v230, s[64:65] sc1\n\t"
      "s_waitcnt vmcnt(0)\n\t"
      "v_max3_u32 v234, v152, v153, v154\n\t"
      "v_max_u32_e32 v234, v234, v155\n\t"
      "v_cmp_eq_u32_e32 vcc, -1, v234\n\t"
      "s_cbranch_vccnz FQ48DL_%=\n\t"
      "FQ48DX_%=:\n\t"
      "s_branch FQ48B_%=\n\t"
      "NQ6R_%=:\n\t"
      "s_mov_b32 s51, 0\n\t"
      "NQ6L_%=:\n\t"
      "s_add_u32 s51, s51, 1\n\t"
      "s_cmp_lt_u32 s51, 262144\n\t"
      "s_cbranch_scc0 NQ6X_%=\n\t"
      "s_waitcnt vmcnt(0)\n\t"
      "s_sleep 1\n\t"
      "global_load_dwordx4 v[128:131], v224, s[64:65] sc1\n\t"
      "global_load_dwordx4 v[132:135], v225, s[64:65] sc1\n\t"
      "global_load_dwordx4 v[136:139], v226, s[64:65] sc1\n\t"
      "global_load_dwordx4 v[140:143], v227, s[64:65] sc1\n\t"
      "global_load_dwordx4 v[144:147], v228, s[64:65] sc1\n\t"
      "global_load_dwordx4 v[148:151], v229, s[64:65] sc1\n\t"
      "global_load_dwordx4 v[152:155], v230, s[64:65] sc1\n\t"
      "s_waitcnt vmcnt(0)\n\t"
      "v_max3_u32 v234, v128, v129, v130\n\t"
      "v_max3_u32 v234, v234, v131, v132\n\t"
      "v_max3_u32 v234, v234, v133, v134\n\t"
      "v_max3_u32 v234, v234, v135, v136\n\t"
      "v_max3_u32 v234, v234, v137, v138\n\t"
      "v_max3_u32 v234, v234, v139, v140\n\t"
      "v_max3_u32 v234, v234, v141, v142\n\t"
      "v_max3_u32 v234, v234, v143, v144\n\t"
      "v_max3_u32 v234, v234, v145, v146\n\t"
      "v_max3_u32 v234, v234, v147, v148\n\t"
      "v_max3_u32 v234, v234, v149, v150\n\t"
      "v_max3_u32 v234, v234, v151, v152\n\t"
      "v_max3_u32 v234, v234, v153, v154\n\t"
      "v_max_u32_e32 v234, v234, v155\n\t"
      "v_cmp_eq_u32_e32 vcc, -1, v234\n\t"
      "s_cbranch_vccnz NQ6L_%=\n\t"
      "NQ6X_%=:\n\t"
      "v_mfma_f32_16x16x32_bf16 v[184:187], v[128:131], v[0:3], 0\n\t"
      "v_mfma_f32_16x16x32_bf16 v[188:191], v[128:131], v[4:7], 0\n\t"
      "v_mfma_f32_16x16x32_bf16 v[192:195], v[128:131], v[8:11], 0\n\t"
      "v_mfma_f32_16x16x32_bf16 v[196:199], v[128:131], v[12:15], 0\n\t"
      "v_mfma_f32_16x16x32_bf16 v[184:187], v[132:135], v[16:19], v[184:187]\n\t"
      "v_mfma_f32_16x16x32_bf16 v[188:191], v[132:135], v[20:23], v[188:191]\n\t"
      "v_mfma_f32_16x16x32_bf16 v[192:195], v[132:135], v[24:27], v[192:195]\n\t"
      "v_mfma_f32_16x16x32_bf16 v[196:199], v[132:135], v[28:31], v[196:199]\n\t"
      "v_mfma_f32_16x16x32_bf16 v[184:187], v[136:139], v[32:35], v[184:187]\n\t"
      "v_mfma_f32_16x16x32_bf16 v[188:191], v[136:139], v[36:39], v[188:191]\n\t"
      "v_mfma_f32_16x16x32_bf16 v[192:195], v[136:139], v[40:43], v[192:195]\n\t"
      "v_mfma_f32_16x16x32_bf16 v[196:199], v[136:139], v[44:47], v[196:199]\n\t"
      "v_mfma_f32_16x16x32_bf16 v[184:187], v[140:143], v[48:51], v[184:187]\n\t"
      "v_mfma_f32_16x16x32_bf16 v[188:191], v[140:143], v[52:55], v[188:191]\n\t"
      "v_mfma_f32_16x16x32_bf16 v[192:195], v[140:143], v[56:59], v[192:195]\n\t"
      "v_mfma_f32_16x16x32_bf16 v[196:199], v[140:143], v[60:63], v[196:199]\n\t"
      "v_mfma_f32_16x16x32_bf16 v[184:187], v[144:147], v[64:67], v[184:187]\n\t"
      "v_mfma_f32_16x16x32_bf16 v[188:191], v[144:147], v[68:71], v[188:191]\n\t"
      "v_mfma_f32_16x16x32_bf16 v[192:195], v[144:147], v[72:75], v[192:195]\n\t"
      "v_mfma_f32_16x16x32_bf16 v[196:199], v[144:147], v[76:79], v[196:199]\n\t"
      "v_mfma_f32_16x16x32_bf16 v[184:187], v[148:151], v[80:83], v[184:187]\n\t"
      "v_mfma_f32_16x16x32_bf16 v[188:191], v[148:151], v[84:87], v[188:191]\n\t"
      "v_mfma_f32_16x16x32_bf16 v[192:195], v[148:151], v[88:91], v[192:195]\n\t"
      "v_mfma_f32_16x16x32_bf16 v[196:199], v[148:151], v[92:95], v[196:199]\n\t"
      "v_mfma_f32_16x16x32_bf16 v[184:187], v[152:155], v[96:99], v[184:187]\n\t"
      "v_mfma_f32_16x16x32_bf16 v[188:191], v[152:155], v[100:103], v[188:191]\n\t"
      "v_mfma_f32_16x16x32_bf16 v[192:195], v[152:155], v[104:107], v[192:195]\n\t"
      "v_mfma_f32_16x16x32_bf16 v[196:199], v[152:155], v[108:111], v[196:199]\n\t"
      "s_waitcnt vmcnt(0)\n\t"
      "s_nop 7\n\t"
      "s_branch NQ6B_%=\n\t"
      "NP0R_%=:\n\t"
      "s_mov_b32 s51, 0\n\t"
      "NP0L_%=:\n\t"
      "s_add_u32 s51, s51, 1\n\t"
      "s_cmp_lt_u32 s51, 262144\n\t"
      "s_cbranch_scc0 NP0X_%=\n\t"
      "s_waitcnt vmcnt(0)\n\t"
      "s_sleep 1\n\t"
      "global_load_dwordx4 v[128:131], v224, s[52:53] sc1\n\t"
      "global_load_dwordx4 v[132:135], v225, s[52:53] sc1\n\t"
      "global_load_dwordx4 v[136:139], v226, s[52:53] sc1\n\t"
      "global_load_dwordx4 v[140:143], v227, s[52:53] sc1\n\t"
      "global_load_dwordx4 v[144:147], v228, s[52:53] sc1\n\t"
      "global_load_dwordx4 v[148:151], v229, s[52:53] sc1\n\t"
      "global_load_dwordx4 v[152:155], v230, s[52:53] sc1\n\t"
      "s_waitcnt vmcnt(0)\n\t"
      "v_max3_u32 v234, v128, v129, v130\n\t"
      "v_max3_u32 v234, v234, v131, v132\n\t"
      "v_max3_u32 v234, v234, v133, v134\n\t"
      "v_max3_u32 v234, v234, v135, v136\n\t"
      "v_max3_u32 v234, v234, v137, v138\n\t"
      "v_max3_u32 v234, v234, v139, v140\n\t"
      "v_max3_u32 v234, v234, v141, v142\n\t"
      "v_max3_u32 v234, v234, v143, v144\n\t"
      "v_max3_u32 v234, v234, v145, v146\n\t"
      "v_max3_u32 v234, v234, v147, v148\n\t"
      "v_max3_u32 v234, v234, v149, v150\n\t"
      "v_max3_u32 v234, v234, v151, v152\n\t"
      "v_max3_u32 v234, v234, v153, v154\n\t"
      "v_max_u32_e32 v234, v234, v155\n\t"
      "v_cmp_eq_u32_e32 vcc, -1, v234\n\t"
      "s_cbranch_vccnz NP0L_%=\n\t"
      "NP0X_%=:\n\t"
      "v_mfma_f32_16x16x32_bf16 v[184:187], v[128:131], v[0:3], v[156:159]\n\t"
      "v_mfma_f32_16x16x32_bf16 v[188:191], v[128:131], v[4:7], 0\n\t"
      "v_mfma_f32_16x16x32_bf16 v[192:195], v[128:131], v[8:11], 0\n\t"
      "v_mfma_f32_16x16x32_bf16 v[196:199], v[128:131], v[12:15], 0\n\t"
      "v_mfma_f32_16x16x32_bf16 v[184:187], v[132:135], v[16:19], v[184:187]\n\t"
      "v_mfma_f32_16x16x32_bf16 v[188:191], v[132:135], v[20:23], v[188:191]\n\t"
      "v_mfma_f32_16x16x32_bf16 v[192:195], v[132:135], v[24:27], v[192:195]\n\t"
      "v_mfma_f32_16x16x32_bf16 v[196:199], v[132:135], v[28:31], v[196:199]\n\t"
      "v_mfma_f32_16x16x32_bf16 v[184:187], v[136:139], v[32:35], v[184:187]\n\t"
      "v_mfma_f32_16x16x32_bf16 v[188:191], v[136:139], v[36:39], v[188:191]\n\t"
      "v_mfma_f32_16x16x32_bf16 v[192:195], v[136:139], v[40:43], v[192:195]\n\t"
      "v_mfma_f32_16x16x32_bf16 v[196:199], v[136:139], v[44:47], v[196:199]\n\t"
      "v_mfma_f32_16x16x32_bf16 v[184:187], v[140:143], v[48:51], v[184:187]\n\t"
      "v_mfma_f32_16x16x32_bf16 v[188:191], v[140:143], v[52:55], v[188:191]\n\t"
      "v_mfma_f32_16x16x32_bf16 v[192:195], v[140:143], v[56:59], v[192:195]\n\t"
      "v_mfma_f32_16x16x32_bf16 v[196:199], v[140:143], v[60:63], v[196:199]\n\t"
      "v_mfma_f32_16x16x32_bf16 v[184:187], v[144:147], v[64:67], v[184:187]\n\t"
      "v_mfma_f32_16x16x32_bf16 v[188:191], v[144:147], v[68:71], v[188:191]\n\t"
      "v_mfma_f32_16x16x32_bf16 v[192:195], v[144:147], v[72:75], v[192:195]\n\t"
      "v_mfma_f32_16x16x32_bf16 v[196:199], v[144:147], v[76:79], v[196:199]\n\t"
      "v_mfma_f32_16x16x32_bf16 v[184:187], v[148:151], v[80:83], v[184:187]\n\t"
      "v_mfma_f32_16x16x32_bf16 v[188:191], v[148:151], v[84:87], v[188:191]\n\t"
      "v_mfma_f32_16x16x32_bf16 v[192:195], v[148:151], v[88:91], v[192:195]\n\t"
      "v_mfma_f32_16x16x32_bf16 v[184:187], v[152:155], v[96:99], v[184:187]\n\t"
      "v_mfma_f32_16x16x32_bf16 v[188:191], v[152:155], v[100:103], v[188:191]\n\t"
      "v_mfma_f32_16x16x32_bf16 v[192:195], v[152:155], v[104:107], v[192:195]\n\t"
      "global_load_dwordx4 v[128:131], v224, s[54:55] sc1\n\t"
      "global_load_dwordx4 v[132:135], v225, s[54:55] sc1\n\t"
      "global_load_dwordx4 v[136:139], v226, s[54:55] sc1\n\t"
      "global_load_dwordx4 v[140:143], v227, s[54:55] sc1\n\t"
      "global_load_dwordx4 v[144:147], v228, s[54:55] sc1\n\t"
      "s_waitcnt vmcnt(0)\n\t"
      "s_nop 7\n\t"
      "s_branch NP0B_%=\n\t"
      "NP1R_%=:\n\t"
      "s_mov_b32 s51, 0\n\t"
      "NP1L_%=:\n\t"
      "s_add_u32 s51, s51, 1\n\t"
      "s_cmp_lt_u32 s51, 262144\n\t"
      "s_cbranch_scc0 NP1X_%=\n\t"
      "s_waitcnt vmcnt(0)\n\t"
      "s_sleep 1\n\t"
      "global_load_dwordx4 v[128:131], v224, s[54:55] sc1\n\t"
      "global_load_dwordx4 v[132:135], v225, s[54:55] sc1\n\t"
      "global_load_dwordx4 v[136:139], v226, s[54:55] sc1\n\t"
      "global_load_dwordx4 v[140:143], v227, s[54:55] sc1\n\t"
      "global_load_dwordx4 v[144:147], v228, s[54:55] sc1\n\t"
      "global_load_dwordx4 v[148:151], v229, s[54:55] sc1\n\t"
      "global_load_dwordx4 v[152:155], v230, s[54:55] sc1\n\t"
      "s_waitcnt vmcnt(0)\n\t"
      "v_max3_u32 v234, v128, v129, v130\n\t"
      "v_max3_u32 v234, v234, v131, v132\n\t"
      "v_max3_u32 v234, v234, v133, v134\n\t"
      "v_max3_u32 v234, v234, v135, v136\n\t"
      "v_max3_u32 v234, v234, v137, v138\n\t"
      "v_max3_u32 v234, v234, v139, v140\n\t"
      "v_max3_u32 v234, v234, v141, v142\n\t"
      "v_max3_u32 v234, v234, v143, v144\n\t"
      "v_max3_u32 v234, v234, v145, v146\n\t"
      "v_max3_u32 v234, v234, v147, v148\n\t"
      "v_max3_u32 v234, v234, v149, v150\n\t"
      "v_max3_u32 v234, v234, v151, v152\n\t"
      "v_max3_u32 v234, v234, v153, v154\n\t"
      "v_max_u32_e32 v234, v234, v155\n\t"
      "v_cmp_eq_u32_e32 vcc, -1, v234\n\t"
      "s_cbranch_vccnz NP1L_%=\n\t"
      "NP1X_%=:\n\t"
      "v_mfma_f32_16x16x32_bf16 v[200:203], v[128:131], v[0:3], v[160:163]\n\t"
      "v_mfma_f32_16x16x32_bf16 v[204:207], v[128:131], v[4:7], 0\n\t"
      "v_mfma_f32_16x16x32_bf16 v[208:211], v[128:131], v[8:11], 0\n\t"
      "v_mfma_f32_16x16x32_bf16 v[212:215], v[128:131], v[12:15], 0\n\t"
      "v_mfma_f32_16x16x32_bf16 v[200:203], v[132:135], v[16:19], v[200:203]\n\t"
      "v_mfma_f32_16x16x32_bf16 v[204:207], v[132:135], v[20:23], v[204:207]\n\t"
      "v_mfma_f32_16x16x32_bf16 v[208:211], v[132:135], v[24:27], v[208:211]\n\t"
      "v_mfma_f32_16x16x32_bf16 v[212:215], v[132:135], v[28:31], v[212:215]\n\t"
      "v_mfma_f32_16x16x32_bf16 v[200:203], v[136:139], v[32:35], v[200:203]\n\t"
      "v_mfma_f32_16x16x32_bf16 v[204:207], v[136:139], v[36:39], v[204:207]\n\t"
      "v_mfma_f32_16x16x32_bf16 v[208:211], v[136:139], v[40:43], v[208:211]\n\t"
      "v_mfma_f32_16x16x32_bf16 v[212:215], v[136:139], v[44:47], v[212:215]\n\t"
      "v_mfma_f32_16x16x32_bf16 v[200:203], v[140:143], v[48:51], v[200:203]\n\t"
      "v_mfma_f32_16x16x32_bf16 v[204:207], v[140:143], v[52:55], v[204:207]\n\t"
      "v_mfma_f32_16x16x32_bf16 v[208:211], v[140:143], v[56:59], v[208:211]\n\t"
      "v_mfma_f32_16x16x32_bf16 v[212:215], v[140:143], v[60:63], v[212:215]\n\t"
      "v_mfma_f32_16x16x32_bf16 v[200:203], v[144:147], v[64:67], v[200:203]\n\t"
      "v_mfma_f32_16x16x32_bf16 v[204:207], v[144:147], v[68:71], v[204:207]\n\t"
      "v_mfma_f32_16x16x32_bf16 v[208:211], v[144:147], v[72:75], v[208:211]\n\t"
      "v_mfma_f32_16x16x32_bf16 v[212:215], v[144:147], v[76:79], v[212:215]\n\t"
      "v_mfma_f32_16x16x32_bf16 v[200:203], v[148:151], v[80:83], v[200:203]\n\t"
      "v_mfma_f32_16x16x32_bf16 v[204:207], v[148:151], v[84:87], v[204:207]\n\t"
      "v_mfma_f32_16x16x32_bf16 v[208:211], v[148:151], v[88:91], v[208:211]\n\t"
      "v_mfma_f32_16x16x32_bf16 v[200:203], v[152:155], v[96:99], v[200:203]\n\t"
      "v_mfma_f32_16x16x32_bf16 v[204:207], v[152:155], v[100:103], v[204:207]\n\t"
      "v_mfma_f32_16x16x32_bf16 v[208:211], v[152:155], v[104:107], v[208:211]\n\t"
      "global_load_dwordx4 v[128:131], v224, s[56:57] sc1\n\t"
      "global_load_dwordx4 v[132:135], v225, s[56:57] sc1\n\t"
      "global_load_dwordx4 v[136:139], v226, s[56:57] sc1\n\t"
      "global_load_dwordx4 v[140:143], v227, s[56:57] sc1\n\t"
      "global_load_dwordx4 v[144:147], v228, s[56:57] sc1\n\t"
      "s_waitcnt vmcnt(0)\n\t"
      "s_nop 7\n\t"
      "s_branch NP1B_%=\n\t"
      "NP2R_%=:\n\t"
      "s_mov_b32 s51, 0\n\t"
      "NP2L_%=:\n\t"
      "s_add_u32 s51, s51, 1\n\t"
      "s_cmp_lt_u32 s51, 262144\n\t"
      "s_cbranch_scc0 NP2X_%=\n\t"
      "s_waitcnt vmcnt(0)\n\t"
      "s_sleep 1\n\t"
      "global_load_dwordx4 v[128:131], v224, s[56:57] sc1\n\t"
      "global_load_dwordx4 v[132:135], v225, s[56:57] sc1\n\t"
      "global_load_dwordx4 v[136:139], v226, s[56:57] sc1\n\t"
      "global_load_dwordx4 v[140:143], v227, s[56:57] sc1\n\t"
      "global_load_dwordx4 v[144:147], v228, s[56:57] sc1\n\t"
      "global_load_dwordx4 v[148:151], v229, s[56:57] sc1\n\t"
      "global_load_dwordx4 v[152:155], v230, s[56:57] sc1\n\t"
      "s_waitcnt vmcnt(0)\n\t"
      "v_max3_u32 v234, v128, v129, v130\n\t"
      "v_max3_u32 v234, v234, v131, v132\n\t"
      "v_max3_u32 v234, v234, v133, v134\n\t"
      "v_max3_u32 v234, v234, v135, v136\n\t"
      "v_max3_u32 v234, v234, v137, v138\n\t"
      "v_max3_u32 v234, v234, v139, v140\n\t"
      "v_max3_u32 v234, v234, v141, v142\n\t"
      "v_max3_u32 v234, v234, v143, v144\n\t"
      "v_max3_u32 v234, v234, v145, v146\n\t"
      "v_max3_u32 v234, v234, v147, v148\n\t"
      "v_max3_u32 v234, v234, v149, v150\n\t"
      "v_max3_u32 v234, v234, v151, v152\n\t"
      "v_max3_u32 v234, v234, v153, v154\n\t"
      "v_max_u32_e32 v234, v234, v155\n\t"
      "v_cmp_eq_u32_e32 vcc, -1, v234\n\t"
      "s_cbranch_vccnz NP2L_%=\n\t"
      "NP2X_%=:\n\t"
      "v_mfma_f32_16x16x32_bf16 v[184:187], v[128:131], v[0:3], v[164:167]\n\t"
      "v_mfma_f32_16x16x32_bf16 v[188:191], v[128:131], v[4:7], 0\n\t"
      "v_mfma_f32_16x16x32_bf16 v[192:195], v[128:131], v[8:11], 0\n\t"
      "v_mfma_f32_16x16x32_bf16 v[196:199], v[128:131], v[12:15], 0\n\t"
      "v_mfma_f32_16x16x32_bf16 v[184:187], v[132:135], v[16:19], v[184:187]\n\t"
      "v_mfma_f32_16x16x32_bf16 v[188:191], v[132:135], v[20:23], v[188:191]\n\t"
      "v_mfma_f32_16x16x32_bf16 v[192:195], v[132:135], v[24:27], v[192:195]\n\t"
      "v_mfma_f32_16x16x32_bf16 v[196:199], v[132:135], v[28:31], v[196:199]\n\t"
      "v_mfma_f32_16x16x32_bf16 v[184:187], v[136:139], v[32:35], v[184:187]\n\t"
      "v_mfma_f32_16x16x32_bf16 v[188:191], v[136:139], v[36:39], v[188:191]\n\t"
      "v_mfma_f32_16x16x32_bf16 v[192:195], v[136:139], v[40:43], v[192:195]\n\t"
      "v_mfma_f32_16x16x32_bf16 v[196:199], v[136:139], v[44:47], v[196:199]\n\t"
      "v_mfma_f32_16x16x32_bf16 v[184:187], v[140:143], v[48:51], v[184:187]\n\t"
      "v_mfma_f32_16x16x32_bf16 v[188:191], v[140:143], v[52:55], v[188:191]\n\t"
      "v_mfma_f32_16x16x32_bf16 v[192:195], v[140:143], v[56:59], v[192:195]\n\t"
      "v_mfma_f32_16x16x32_bf16 v[196:199], v[140:143], v[60:63], v[196:199]\n\t"
      "v_mfma_f32_16x16x32_bf16 v[184:187], v[144:147], v[64:67], v[184:187]\n\t"
      "v_mfma_f32_16x16x32_bf16 v[188:191], v[144:147], v[68:71], v[188:191]\n\t"
      "v_mfma_f32_16x16x32_bf16 v[192:195], v[144:147], v[72:75], v[192:195]\n\t"
      "v_mfma_f32_16x16x32_bf16 v[196:199], v[144:147], v[76:79], v[196:199]\n\t"
      "v_mfma_f32_16x16x32_bf16 v[184:187], v[148:151], v[80:83], v[184:187]\n\t"
      "v_mfma_f32_16x16x32_bf16 v[188:191], v[148:151], v[84:87], v[188:191]\n\t"
      "v_mfma_f32_16x16x32_bf16 v[192:195], v[148:151], v[88:91], v[192:195]\n\t"
      "v_mfma_f32_16x16x32_bf16 v[184:187], v[152:155], v[96:99], v[184:187]\n\t"
      "v_mfma_f32_16x16x32_bf16 v[188:191], v[152:155], v[100:103], v[188:191]\n\t"
      "v_mfma_f32_16x16x32_bf16 v[192:195], v[152:155], v[104:107], v[192:195]\n\t"
      "global_load_dwordx4 v[128:131], v224, s[58:59] sc1\n\t"
      "global_load_dwordx4 v[132:135], v225, s[58:59] sc1\n\t"
      "global_load_dwordx4 v[136:139], v226, s[58:59] sc1\n\t"
      "global_load_dwordx4 v[140:143], v227, s[58:59] sc1\n\t"
      "global_load_dwordx4 v[144:147], v228, s[58:59] sc1\n\t"
      "s_waitcnt vmcnt(0)\n\t"
      "s_nop 7\n\t"
      "s_branch NP2B_%=\n\t"
      "NP3R_%=:\n\t"
      "s_mov_b32 s51, 0\n\t"
      "NP3L_%=:\n\t"
      "s_add_u32 s51, s51, 1\n\t"
      "s_cmp_lt_u32 s51, 262144\n\t"
      "s_cbranch_scc0 NP3X_%=\n\t"
      "s_waitcnt vmcnt(0)\n\t"
      "s_sleep 1\n\t"
      "global_load_dwordx4 v[128:131], v224, s[58:59] sc1\n\t"
      "global_load_dwordx4 v[132:135], v225, s[58:59] sc1\n\t"
      "global_load_dwordx4 v[136:139], v226, s[58:59] sc1\n\t"
      "global_load_dwordx4 v[140:143], v227, s[58:59] sc1\n\t"
      "global_load_dwordx4 v[144:147], v228, s[58:59] sc1\n\t"
      "global_load_dwordx4 v[148:151], v229, s[58:59] sc1\n\t"
      "global_load_dwordx4 v[152:155], v230, s[58:59] sc1\n\t"
      "s_waitcnt vmcnt(0)\n\t"
      "v_max3_u32 v234, v128, v129, v130\n\t"
      "v_max3_u32 v234, v234, v131, v132\n\t"
      "v_max3_u32 v234, v234, v133, v134\n\t"
      "v_max3_u32 v234, v234, v135, v136\n\t"
      "v_max3_u32 v234, v234, v137, v138\n\t"
      "v_max3_u32 v234, v234, v139, v140\n\t"
      "v_max3_u32 v234, v234, v141, v142\n\t"
      "v_max3_u32 v234, v234, v143, v144\n\t"
      "v_max3_u32 v234, v234, v145, v146\n\t"
      "v_max3_u32 v234, v234, v147, v148\n\t"
      "v_max3_u32 v234, v234, v149, v150\n\t"
      "v_max3_u32 v234, v234, v151, v152\n\t"
      "v_max3_u32 v234, v234, v153, v154\n\t"
      "v_max_u32_e32 v234, v234, v155\n\t"
      "v_cmp_eq_u32_e32 vcc, -1, v234\n\t"
      "s_cbranch_vccnz NP3L_%=\n\t"
      "NP3X_%=:\n\t"
      "v_mfma_f32_16x16x32_bf16 v[200:203], v[128:131], v[0:3], v[168:171]\n\t"
      "v_mfma_f32_16x16x32_bf16 v[204:207], v[128:131], v[4:7], 0\n\t"
      "v_mfma_f32_16x16x32_bf16 v[208:211], v[128:131], v[8:11], 0\n\t"
      "v_mfma_f32_16x16x32_bf16 v[212:215], v[128:131], v[12:15], 0\n\t"
      "v_mfma_f32_16x16x32_bf16 v[200:203], v[132:135], v[16:19], v[200:203]\n\t"
      "v_mfma_f32_16x16x32_bf16 v[204:207], v[132:135], v[20:23], v[204:207]\n\t"
      "v_mfma_f32_16x16x32_bf16 v[208:211], v[132:135], v[24:27], v[208:211]\n\t"
      "v_mfma_f32_16x16x32_bf16 v[212:215], v[132:135], v[28:31], v[212:215]\n\t"
      "v_mfma_f32_16x16x32_bf16 v[200:203], v[136:139], v[32:35], v[200:203]\n\t"
      "v_mfma_f32_16x16x32_bf16 v[204:207], v[136:139], v[36:39], v[204:207]\n\t"
      "v_mfma_f32_16x16x32_bf16 v[208:211], v[136:139], v[40:43], v[208:211]\n\t"
      "v_mfma_f32_16x16x32_bf16 v[212:215], v[136:139], v[44:47], v[212:215]\n\t"
      "v_mfma_f32_16x16x32_bf16 v[200:203], v[140:143], v[48:51], v[200:203]\n\t"
      "v_mfma_f32_16x16x32_bf16 v[204:207], v[140:143], v[52:55], v[204:207]\n\t"
      "v_mfma_f32_16x16x32_bf16 v[208:211], v[140:143], v[56:59], v[208:211]\n\t"
      "v_mfma_f32_16x16x32_bf16 v[212:215], v[140:143], v[60:63], v[212:215]\n\t"
      "v_mfma_f32_16x16x32_bf16 v[200:203], v[144:147], v[64:67], v[200:203]\n\t"
      "v_mfma_f32_16x16x32_bf16 v[204:207], v[144:147], v[68:71], v[204:207]\n\t"
      "v_mfma_f32_16x16x32_bf16 v[208:211], v[144:147], v[72:75], v[208:211]\n\t"
      "v_mfma_f32_16x16x32_bf16 v[212:215], v[144:147], v[76:79], v[212:215]\n\t"
      "v_mfma_f32_16x16x32_bf16 v[200:203], v[148:151], v[80:83], v[200:203]\n\t"
      "v_mfma_f32_16x16x32_bf16 v[204:207], v[148:151], v[84:87], v[204:207]\n\t"
      "v_mfma_f32_16x16x32_bf16 v[208:211], v[148:151], v[88:91], v[208:211]\n\t"
      "v_mfma_f32_16x16x32_bf16 v[200:203], v[152:155], v[96:99], v[200:203]\n\t"
      "v_mfma_f32_16x16x32_bf16 v[204:207], v[152:155], v[100:103], v[204:207]\n\t"
      "v_mfma_f32_16x16x32_bf16 v[208:211], v[152:155], v[104:107], v[208:211]\n\t"
      "global_load_dwordx4 v[128:131], v224, s[60:61] sc1\n\t"
      "global_load_dwordx4 v[132:135], v225, s[60:61] sc1\n\t"
      "global_load_dwordx4 v[136:139], v226, s[60:61] sc1\n\t"
      "global_load_dwordx4 v[140:143], v227, s[60:61] sc1\n\t"
      "global_load_dwordx4 v[144:147], v228, s[60:61] sc1\n\t"
      "s_waitcnt vmcnt(0)\n\t"
      "s_nop 7\n\t"
      "s_branch NP3B_%=\n\t"
      "NP4R_%=:\n\t"
      "s_mov_b32 s51, 0\n\t"
      "NP4L_%=:\n\t"
      "s_add_u32 s51, s51, 1\n\t"
      "s_cmp_lt_u32 s51, 262144\n\t"
      "s_cbranch_scc0 NP4X_%=\n\t"
      "s_waitcnt vmcnt(0)\n\t"
      "s_sleep 1\n\t"
      "global_load_dwordx4 v[128:131], v224, s[60:61] sc1\n\t"
      "global_load_dwordx4 v[132:135], v225, s[60:61] sc1\n\t"
      "global_load_dwordx4 v[136:139], v226, s[60:61] sc1\n\t"
      "global_load_dwordx4 v[140:143], v227, s[60:61] sc1\n\t"
      "global_load_dwordx4 v[144:147], v228, s[60:61] sc1\n\t"
      "global_load_dwordx4 v[148:151], v229, s[60:61] sc1\n\t"
      "global_load_dwordx4 v[152:155], v230, s[60:61] sc1\n\t"
      "s_waitcnt vmcnt(0)\n\t"
      "v_max3_u32 v234, v128, v129, v130\n\t"
      "v_max3_u32 v234, v234, v131, v132\n\t"
      "v_max3_u32 v234, v234, v133, v134\n\t"
      "v_max3_u32 v234, v234, v135, v136\n\t"
      "v_max3_u32 v234, v234, v137, v138\n\t"
      "v_max3_u32 v234, v234, v139, v140\n\t"
      "v_max3_u32 v234, v234, v141, v142\n\t"
      "v_max3_u32 v234, v234, v143, v144\n\t"
      "v_max3_u32 v234, v234, v145, v146\n\t"
      "v_max3_u32 v234, v234, v147, v148\n\t"
      "v_max3_u32 v234, v234, v149, v150\n\t"
      "v_max3_u32 v234, v234, v151, v152\n\t"
      "v_max3_u32 v234, v234, v153, v154\n\t"
      "v_max_u32_e32 v234, v234, v155\n\t"
      "v_cmp_eq_u32_e32 vcc, -1, v234\n\t"
      "s_cbranch_vccnz NP4L_%=\n\t"
      "NP4X_%=:\n\t"
      "v_mfma_f32_16x16x32_bf16 v[184:187], v[128:131], v[0:3], v[172:175]\n\t"
      "v_mfma_f32_16x16x32_bf16 v[188:191], v[128:131], v[4:7], 0\n\t"
      "v_mfma_f32_16x16x32_bf16 v[192:195], v[128:131], v[8:11], 0\n\t"
      "v_mfma_f32_16x16x32_bf16 v[196:199], v[128:131], v[12:15], 0\n\t"
      "v_mfma_f32_16x16x32_bf16 v[184:187], v[132:135], v[16:19], v[184:187]\n\t"
      "v_mfma_f32_16x16x32_bf16 v[188:191], v[132:135], v[20:23], v[188:191]\n\t"
      "v_mfma_f32_16x16x32_bf16 v[192:195], v[132:135], v[24:27], v[192:195]\n\t"
      "v_mfma_f32_16x16x32_bf16 v[196:199], v[132:135], v[28:31], v[196:199]\n\t"
      "v_mfma_f32_16x16x32_bf16 v[184:187], v[136:139], v[32:35], v[184:187]\n\t"
      "v_mfma_f32_16x16x32_bf16 v[188:191], v[136:139], v[36:39], v[188:191]\n\t"
      "v_mfma_f32_16x16x32_bf16 v[192:195], v[136:139], v[40:43], v[192:195]\n\t"
      "v_mfma_f32_16x16x32_bf16 v[196:199], v[136:139], v[44:47], v[196:199]\n\t"
      "v_mfma_f32_16x16x32_bf16 v[184:187], v[140:143], v[48:51], v[184:187]\n\t"
      "v_mfma_f32_16x16x32_bf16 v[188:191], v[140:143], v[52:55], v[188:191]\n\t"
      "v_mfma_f32_16x16x32_bf16 v[192:195], v[140:143], v[56:59], v[192:195]\n\t"
      "v_mfma_f32_16x16x32_bf16 v[196:199], v[140:143], v[60:63], v[196:199]\n\t"
      "v_mfma_f32_16x16x32_bf16 v[184:187], v[144:147], v[64:67], v[184:187]\n\t"
      "v_mfma_f32_16x16x32_bf16 v[188:191], v[144:147], v[68:71], v[188:191]\n\t"
      "v_mfma_f32_16x16x32_bf16 v[192:195], v[144:147], v[72:75], v[192:195]\n\t"
      "v_mfma_f32_16x16x32_bf16 v[196:199], v[144:147], v[76:79], v[196:199]\n\t"
      "v_mfma_f32_16x16x32_bf16 v[184:187], v[148:151], v[80:83], v[184:187]\n\t"
      "v_mfma_f32_16x16x32_bf16 v[188:191], v[148:151], v[84:87], v[188:191]\n\t"
      "v_mfma_f32_16x16x32_bf16 v[192:195], v[148:151], v[88:91], v[192:195]\n\t"
      "v_mfma_f32_16x16x32_bf16 v[184:187], v[152:155], v[96:99], v[184:187]\n\t"
      "v_mfma_f32_16x16x32_bf16 v[188:191], v[152:155], v[100:103], v[188:191]\n\t"
      "v_mfma_f32_16x16x32_bf16 v[192:195], v[152:155], v[104:107], v[192:195]\n\t"
      "global_load_dwordx4 v[128:131], v224, s[62:63] sc1\n\t"
      "global_load_dwordx4 v[132:135], v225, s[62:63] sc1\n\t"
      "global_load_dwordx4 v[136:139], v226, s[62:63] sc1\n\t"
      "global_load_dwordx4 v[140:143], v227, s[62:63] sc1\n\t"
      "global_load_dwordx4 v[144:147], v228, s[62:63] sc1\n\t"
      "s_waitcnt vmcnt(0)\n\t"
      "s_nop 7\n\t"
      "s_branch NP4B_%=\n\t"
      "NP5R_%=:\n\t"
      "s_mov_b32 s51, 0\n\t"
      "NP5L_%=:\n\t"
      "s_add_u32 s51, s51, 1\n\t"
      "s_cmp_lt_u32 s51, 262144\n\t"
      "s_cbranch_scc0 NP5X_%=\n\t"
      "s_waitcnt vmcnt(0)\n\t"
      "s_sleep 1\n\t"
      "global_load_dwordx4 v[128:131], v224, s[62:63] sc1\n\t"
      "global_load_dwordx4 v[132:135], v225, s[62:63] sc1\n\t"
      "global_load_dwordx4 v[136:139], v226, s[62:63] sc1\n\t"
      "global_load_dwordx4 v[140:143], v227, s[62:63] sc1\n\t"
      "global_load_dwordx4 v[144:147], v228, s[62:63] sc1\n\t"
      "global_load_dwordx4 v[148:151], v229, s[62:63] sc1\n\t"
      "global_load_dwordx4 v[152:155], v230, s[62:63] sc1\n\t"
      "s_waitcnt vmcnt(0)\n\t"
      "v_max3_u32 v234, v128, v129, v130\n\t"
      "v_max3_u32 v234, v234, v131, v132\n\t"
      "v_max3_u32 v234, v234, v133, v134\n\t"
      "v_max3_u32 v234, v234, v135, v136\n\t"
      "v_max3_u32 v234, v234, v137, v138\n\t"
      "v_max3_u32 v234, v234, v139, v140\n\t"
      "v_max3_u32 v234, v234, v141, v142\n\t"
      "v_max3_u32 v234, v234, v143, v144\n\t"
      "v_max3_u32 v234, v234, v145, v146\n\t"
      "v_max3_u32 v234, v234, v147, v148\n\t"
      "v_max3_u32 v234, v234, v149, v150\n\t"
      "v_max3_u32 v234, v234, v151, v152\n\t"
      "v_max3_u32 v234, v234, v153, v154\n\t"
      "v_max_u32_e32 v234, v234, v155\n\t"
      "v_cmp_eq_u32_e32 vcc, -1, v234\n\t"
      "s_cbranch_vccnz NP5L_%=\n\t"
      "NP5X_%=:\n\t"
      "v_mfma_f32_16x16x32_bf16 v[200:203], v[128:131], v[0:3], v[176:179]\n\t"
      "v_mfma_f32_16x16x32_bf16 v[204:207], v[128:131], v[4:7], 0\n\t"
      "v_mfma_f32_16x16x32_bf16 v[208:211], v[128:131], v[8:11], 0\n\t"
      "v_mfma_f32_16x16x32_bf16 v[212:215], v[128:131], v[12:15], 0\n\t"
      "v_mfma_f32_16x16x32_bf16 v[200:203], v[132:135], v[16:19], v[200:203]\n\t"
      "v_mfma_f32_16x16x32_bf16 v[204:207], v[132:135], v[20:23], v[204:207]\n\t"
      "v_mfma_f32_16x16x32_bf16 v[208:211], v[132:135], v[24:27], v[208:211]\n\t"
      "v_mfma_f32_16x16x32_bf16 v[212:215], v[132:135], v[28:31], v[212:215]\n\t"
      "v_mfma_f32_16x16x32_bf16 v[200:203], v[136:139], v[32:35], v[200:203]\n\t"
      "v_mfma_f32_16x16x32_bf16 v[204:207], v[136:139], v[36:39], v[204:207]\n\t"
      "v_mfma_f32_16x16x32_bf16 v[208:211], v[136:139], v[40:43], v[208:211]\n\t"
      "v_mfma_f32_16x16x32_bf16 v[212:215], v[136:139], v[44:47], v[212:215]\n\t"
      "v_mfma_f32_16x16x32_bf16 v[200:203], v[140:143], v[48:51], v[200:203]\n\t"
      "v_mfma_f32_16x16x32_bf16 v[204:207], v[140:143], v[52:55], v[204:207]\n\t"
      "v_mfma_f32_16x16x32_bf16 v[208:211], v[140:143], v[56:59], v[208:211]\n\t"
      "v_mfma_f32_16x16x32_bf16 v[212:215], v[140:143], v[60:63], v[212:215]\n\t"
      "v_mfma_f32_16x16x32_bf16 v[200:203], v[144:147], v[64:67], v[200:203]\n\t"
      "v_mfma_f32_16x16x32_bf16 v[204:207], v[144:147], v[68:71], v[204:207]\n\t"
      "v_mfma_f32_16x16x32_bf16 v[208:211], v[144:147], v[72:75], v[208:211]\n\t"
      "v_mfma_f32_16x16x32_bf16 v[212:215], v[144:147], v[76:79], v[212:215]\n\t"
      "v_mfma_f32_16x16x32_bf16 v[200:203], v[148:151], v[80:83], v[200:203]\n\t"
      "v_mfma_f32_16x16x32_bf16 v[204:207], v[148:151], v[84:87], v[204:207]\n\t"
      "v_mfma_f32_16x16x32_bf16 v[208:211], v[148:151], v[88:91], v[208:211]\n\t"
      "v_mfma_f32_16x16x32_bf16 v[200:203], v[152:155], v[96:99], v[200:203]\n\t"
      "v_mfma_f32_16x16x32_bf16 v[204:207], v[152:155], v[100:103], v[204:207]\n\t"
      "v_mfma_f32_16x16x32_bf16 v[208:211], v[152:155], v[104:107], v[208:211]\n\t"
      "global_load_dwordx4 v[128:131], v224, s[64:65] sc1\n\t"
      "global_load_dwordx4 v[132:135], v225, s[64:65] sc1\n\t"
      "global_load_dwordx4 v[136:139], v226, s[64:65] sc1\n\t"
      "global_load_dwordx4 v[140:143], v227, s[64:65] sc1\n\t"
      "global_load_dwordx4 v[144:147], v228, s[64:65] sc1\n\t"
      "s_waitcnt vmcnt(0)\n\t"
      "s_nop 7\n\t"
      "s_branch NP5B_%=\n\t"
      "NP6R_%=:\n\t"
      "s_mov_b32 s51, 0\n\t"
      "NP6L_%=:\n\t"
      "s_add_u32 s51, s51, 1\n\t"
      "s_cmp_lt_u32 s51, 262144\n\t"
      "s_cbranch_scc0 NP6X_%=\n\t"
      "s_waitcnt vmcnt(0)\n\t"
      "s_sleep 1\n\t"
      "global_load_dwordx4 v[128:131], v224, s[64:65] sc1\n\t"
      "global_load_dwordx4 v[132:135], v225, s[64:65] sc1\n\t"
      "global_load_dwordx4 v[136:139], v226, s[64:65] sc1\n\t"
      "global_load_dwordx4 v[140:143], v227, s[64:65] sc1\n\t"
      "global_load_dwordx4 v[144:147], v228, s[64:65] sc1\n\t"
      "global_load_dwordx4 v[148:151], v229, s[64:65] sc1\n\t"
      "global_load_dwordx4 v[152:155], v230, s[64:65] sc1\n\t"
      "s_waitcnt vmcnt(0)\n\t"
      "v_max3_u32 v234, v128, v129, v130\n\t"
      "v_max3_u32 v234, v234, v131, v132\n\t"
      "v_max3_u32 v234, v234, v133, v134\n\t"
      "v_max3_u32 v234, v234, v135, v136\n\t"
      "v_max3_u32 v234, v234, v137, v138\n\t"
      "v_max3_u32 v234, v234, v139, v140\n\t"
      "v_max3_u32 v234, v234, v141, v142\n\t"
      "v_max3_u32 v234, v234, v143, v144\n\t"
      "v_max3_u32 v234, v234, v145, v146\n\t"
      "v_max3_u32 v234, v234, v147, v148\n\t"
      "v_max3_u32 v234, v234, v149, v150\n\t"
      "v_max3_u32 v234, v234, v151, v152\n\t"
      "v_max3_u32 v234, v234, v153, v154\n\t"
      "v_max_u32_e32 v234, v234, v155\n\t"
      "v_cmp_eq_u32_e32 vcc, -1, v234\n\t"
      "s_cbranch_vccnz NP6L_%=\n\t"
      "NP6X_%=:\n\t"
      "v_mfma_f32_16x16x32_bf16 v[184:187], v[128:131], v[0:3], v[180:183]\n\t"
      "v_mfma_f32_16x16x32_bf16 v[188:191], v[128:131], v[4:7], 0\n\t"
      "v_mfma_f32_16x16x32_bf16 v[192:195], v[128:131], v[8:11], 0\n\t"
      "v_mfma_f32_16x16x32_bf16 v[196:199], v[128:131], v[12:15], 0\n\t"
      "v_mfma_f32_16x16x32_bf16 v[184:187], v[132:135], v[16:19], v[184:187]\n\t"
      "v_mfma_f32_16x16x32_bf16 v[188:191], v[132:135], v[20:23], v[188:191]\n\t"
      "v_mfma_f32_16x16x32_bf16 v[192:195], v[132:135], v[24:27], v[192:195]\n\t"
      "v_mfma_f32_16x16x32_bf16 v[196:199], v[132:135], v[28:31], v[196:199]\n\t"
      "v_mfma_f32_16x16x32_bf16 v[184:187], v[136:139], v[32:35], v[184:187]\n\t"
      "v_mfma_f32_16x16x32_bf16 v[188:191], v[136:139], v[36:39], v[188:191]\n\t"
      "v_mfma_f32_16x16x32_bf16 v[192:195], v[136:139], v[40:43], v[192:195]\n\t"
      "v_mfma_f32_16x16x32_bf16 v[196:199], v[136:139], v[44:47], v[196:199]\n\t"
      "v_mfma_f32_16x16x32_bf16 v[184:187], v[140:143], v[48:51], v[184:187]\n\t"
      "v_mfma_f32_16x16x32_bf16 v[188:191], v[140:143], v[52:55], v[188:191]\n\t"
      "v_mfma_f32_16x16x32_bf16 v[192:195], v[140:143], v[56:59], v[192:195]\n\t"
      "v_mfma_f32_16x16x32_bf16 v[196:199], v[140:143], v[60:63], v[196:199]\n\t"
      "v_mfma_f32_16x16x32_bf16 v[184:187], v[144:147], v[64:67], v[184:187]\n\t"
      "v_mfma_f32_16x16x32_bf16 v[188:191], v[144:147], v[68:71], v[188:191]\n\t"
      "v_mfma_f32_16x16x32_bf16 v[192:195], v[144:147], v[72:75], v[192:195]\n\t"
      "v_mfma_f32_16x16x32_bf16 v[196:199], v[144:147], v[76:79], v[196:199]\n\t"
      "v_mfma_f32_16x16x32_bf16 v[184:187], v[148:151], v[80:83], v[184:187]\n\t"
      "v_mfma_f32_16x16x32_bf16 v[188:191], v[148:151], v[84:87], v[188:191]\n\t"
      "v_mfma_f32_16x16x32_bf16 v[192:195], v[148:151], v[88:91], v[192:195]\n\t"
      "v_mfma_f32_16x16x32_bf16 v[184:187], v[152:155], v[96:99], v[184:187]\n\t"
      "v_mfma_f32_16x16x32_bf16 v[188:191], v[152:155], v[100:103], v[188:191]\n\t"
      "v_mfma_f32_16x16x32_bf16 v[192:195], v[152:155], v[104:107], v[192:195]\n\t"
      "s_waitcnt vmcnt(0)\n\t"
      "s_nop 7\n\t"
      "s_branch NP6B_%=\n\t"
      : "+{s[4:19]}"(s), "+{s[20:27]}"(s2), "+{s[28:29]}"(kr), "+{s30}"(od), "+{v[240:247]}"(v), "+{v[136:139]}"(vo), "+{v140}"(vq)
      :
      : "memory", "vcc", "scc",
        "v0", "v1", "v2", "v3", "v4", "v5", "v6", "v7", "v8", "v9", "v10", "v11", "v12", "v13", "v14", "v15", "v16",
        "v17", "v18", "v19", "v20", "v21", "v22", "v23", "v24", "v25", "v26", "v27", "v28", "v29", "v30", "v31", "v32",
        "v33", "v34", "v35", "v36", "v37", "v38", "v39", "v40", "v41", "v42", "v43", "v44", "v45", "v46", "v47", "v48",
        "v49", "v50", "v51", "v52", "v53", "v54", "v55", "v56", "v57", "v58", "v59", "v60", "v61", "v62", "v63", "v64",
        "v65", "v66", "v67", "v68", "v69", "v70", "v71", "v72", "v73", "v74", "v75", "v76", "v77", "v78", "v79", "v80",
        "v81", "v82", "v83", "v84", "v85", "v86", "v87", "v88", "v89", "v90", "v91", "v92", "v93", "v94", "v95", "v96",
        "v97", "v98", "v99", "v100", "v101", "v102", "v103", "v104", "v105", "v106", "v107", "v108", "v109", "v110",
        "v111", "v112", "v113", "v114", "v115", "v116", "v117", "v118", "v119", "v120", "v121", "v122", "v123", "v124",
        "v125", "v126", "v127", "v128", "v129", "v130", "v131", "v132", "v133", "v134", "v135", "v141", "v142", "v143",
        "v144", "v145", "v146", "v147", "v148", "v149", "v150", "v151", "v152", "v153", "v154", "v155", "v156", "v157",
        "v158", "v159", "v160", "v161", "v162", "v163", "v164", "v165", "v166", "v167", "v168", "v169", "v170", "v171",
        "v172", "v173", "v174", "v175", "v176", "v177", "v178", "v179", "v180", "v181", "v182", "v183", "v184", "v185",
        "v186", "v187", "v188", "v189", "v190", "v191", "v192", "v193", "v194", "v195", "v196", "v197", "v198", "v199",
        "v200", "v201", "v202", "v203", "v204", "v205", "v206", "v207", "v208", "v209", "v210", "v211", "v212", "v213",
        "v214", "v215", "v216", "v217", "v218", "v219", "v220", "v221", "v222", "v223", "v224", "v225", "v226", "v227",
        "v228", "v229", "v230", "v231", "v232", "v233", "v234", "v235", "v236", "v237", "v238", "v239", "v248", "v249",
        "v250", "v251", "v252", "v253", "s31", "s44", "s45", "s46", "s47", "s48", "s49", "s50", "s51", "s52", "s53",
        "s54", "s55", "s56", "s57", "s58", "s59", "s60", "s61", "s62", "s63", "s64", "s65", "s66", "s67", "s68", "s69",
        "s70"
    );
    __builtin_amdgcn_endpgm();
    }
  }
}

extern "C" void kernel_launch(void* const* d_in, const int* in_sizes, int n_in, void* d_out, int out_size,
                              void* d_ws, size_t ws_size, hipStream_t stream) {
  (void)hipFuncSetAttribute((const void*)fused_block, hipFuncAttributeMaxDynamicSharedMemorySize, LDS_BYTES);
  fused_block<<<256, 512, LDS_BYTES, stream>>>((const char*)d_in[0], (const char*)d_in[1], (const char*)d_in[2],
                                               (const char*)d_in[4], (const char*)d_in[5], (const char*)d_in[6],
                                               (const char*)d_in[7], (char*)d_out, (char*)d_ws);
}
```

```cpp
#include <hip/hip_runtime.h>
#include <stdint.h>

#define WS_ST 0
#define WS_HN 2097152
#define WS_O 4194304
#define WS_G 6291456
#define ST_FL 65536
#define ST_PW 114688
#define WS_EP 131072
#define PROBE 1835008
#define ST_CK 3981312
#define ST_H2 4112384
#define ST_H3 8175616
#define ST_H4 8306688
#define A_UNIT 5120
#define A_HALO 40960
#define A_SLOT 43008
#define A_CS 131072
#define A_ST 132096
#define A_EX 142336
#define A_FL 154624
#define A_MK 154752
#define B_UNIT 7168
#define B_SLOT 57344
#define B_X 114688
#define B_OP 928
#define B_PS 129536
#define B_WX 130560
#define B_WY 66048
#define G_WV 16384
#define A_W0 49152
#define A_WD 40960
#define G_RED 0
#define G_RED2 32768
#define LDS_KEY 163600
#define LDS_SINK 163328
#define LDS_BYTES 163840
#define XO 0
#define M44 1
#define PRE 2
#define GKM 1
#define KROT 14
#define KMUL 1
#define HP4 1
#define W2E 1
#define R4 0
#define SP3 1

typedef unsigned short u16;
typedef unsigned long long u64;
typedef __attribute__((ext_vector_type(4))) unsigned int u32x4;
typedef __attribute__((ext_vector_type(8))) unsigned int u32x8;
typedef __attribute__((ext_vector_type(16))) unsigned int u32x16;
#define UNI(x) (unsigned)__builtin_amdgcn_readfirstlane((int)(x))
#define PIN() __builtin_amdgcn_sched_barrier(0)

__device__ __forceinline__ int swz_tr(int row) { return (((row >> 1) & 1) | (((row >> 3) & 1) << 1)) << 1; }
__device__ __forceinline__ int swz_row(int row) { return (row >> 1) & 7; }
__device__ __forceinline__ int unit(int blk, int slice, int par) { return (((blk < 14 ? blk : blk - 14) * 8 + slice) * 2 + ((blk < 14 ? 0 : 1) ^ par)) * 8192; }
__device__ __forceinline__ int part(int n, int side) { return (n >> 13) * 16384 + side * 8192 + (n & 8191); }
template <class T>
__device__ __forceinline__ void put(T& s, int i, u64 p) { s[i] = (unsigned)p; s[i + 1] = (unsigned)(p >> 32); }
template <class T>
__device__ __forceinline__ void put2(T& s, int i, unsigned lo, unsigned hi) { s[i] = UNI(lo); s[i + 1] = UNI(hi); }

__global__ __launch_bounds__(512, 2) void fused_block(const char* __restrict__ x, const char* __restrict__ wqkv,
                                                   const char* __restrict__ wo, const char* __restrict__ w1,
                                                   const char* __restrict__ b1, const char* __restrict__ w2,
                                                   const char* __restrict__ b2, char* __restrict__ out,
                                                   char* __restrict__ ws0) {
  u32x8 f;
  asm volatile(
      "s_load_dwordx4 s[4:7], s[0:1], 0x0\n\t"
      "v_readfirstlane_b32 s3, v0\n\t"
      "s_getpc_b64 s[40:41]\n\t"
      "s_lshr_b32 s12, s2, 3\n\t"
      "s_or_b32 s13, s3, s12\n\t"
      "s_and_b32 s13, s13, 0x1d0\n\t"
      "s_cmp_lg_u32 s13, 16\n\t"
      "s_cbranch_scc1 ET2_%=\n\t"
      "s_and_b32 s12, s12, 7\n\t"
      "s_mov_b32 m0, 163328\n\t"
      "v_lshl_add_u32 v3, s12, 6, v0\n\t"
      "v_min_u32_e32 v3, 469, v3\n\t"
      "v_lshlrev_b32_e32 v3, 7, v3\n\t"
      "global_load_lds_dword v3, s[40:41]\n\t"
      "ET2_%=:\n\t"
      "v_lshrrev_b32_e32 v1, 3, v0\n\t"
      "v_and_b32_e32 v2, 7, v0\n\t"
      "s_and_b32 s17, s2, 7\n\t"
      "s_lshr_b32 s18, s2, 3\n\t"
      "s_lshr_b32 s3, s3, 6\n\t"
      "s_cmpk_gt_u32 s2, 0x7f\n\t"
      "s_cbranch_scc1 EB_%=\n\t"
      "v_and_b32_e32 v3, 2, v1\n\t"
      "v_lshrrev_b32_e32 v4, 1, v1\n\t"
      "s_lshr_b32 s19, s18, 2\n\t"
      "s_and_b32 s12, s17, 6\n\t"
      "s_lshl1_add_u32 s19, s12, s19\n\t"
      "v_and_or_b32 v3, v4, 4, v3\n\t"
      "v_xor_b32_e32 v3, v3, v2\n\t"
      "v_min_u32_e32 v3, 6, v3\n\t"
      "v_mul_u32_u24_e32 v5, 2688, v1\n\t"
      "v_lshl_add_u32 v3, v3, 4, v5\n\t"
      "v_add_u32_e32 v195, 2048, v3\n\t"
      "v_add_u32_e32 v196, 1920, v3\n\t"
      "v_add_u32_e32 v197, 5888, v3\n\t"
      "s_mul_i32 s12, s19, 114688\n\t"
      "v_and_b32_e32 v5, 0x38, v1\n\t"
      "v_add_u32_e32 v5, v5, v1\n\t"
      "v_mul_u32_u24_e32 v5, 896, v5\n\t"
      "s_and_b32 s14, s17, 1\n\t"
      "s_add_u32 s14, s14, 3\n\t"
      "s_and_b32 s13, s18, 3\n\t"
      "s_xor_b32 s13, s13, s14\n\t"
      "s_mul_i32 s13, s13, 112\n\t"
      "v_and_b32_e32 v6, 3, v4\n\t"
      "v_xor_b32_e32 v6, v6, v2\n\t"
      "v_lshl_add_u32 v5, v6, 4, v5\n\t"
      "s_sub_u32 s12, s12, 15360\n\t"
      "s_ashr_i32 s14, s12, 31\n\t"
      "s_sub_u32 s13, s13, 4096\n\t"
      "v_add_u32_e32 v192, 15360, v5\n\t"
      "v_xor_b32_e32 v5, 64, v5\n\t"
      "v_add_u32_e32 v193, 21504, v5\n\t"
      "s_mul_i32 s9, s3, 5120\n\t"
      "s_sub_u32 s3, s3, 4\n\t"
      "s_cbranch_scc0 EH_%=\n\t"
      "s_waitcnt lgkmcnt(0)\n\t"
      "s_add_u32 s4, s4, s12\n\t"
      "s_addc_u32 s5, s5, s14\n\t"
      "s_add_u32 s6, s6, s13\n\t"
      "s_addc_u32 s7, s7, -1\n\t"
      "s_add_u32 m0, s9, 0\n\t"
      "s_nop 0\n\t"
      "global_load_lds_dwordx4 v195, s[6:7] offset:2048\n\t"
      "global_load_lds_dwordx4 v192, s[4:5]\n\t"
      "global_load_lds_dwordx4 v193, s[4:5] offset:1024\n\t"
      "global_load_lds_dwordx4 v196, s[6:7] offset:3072\n\t"
      "s_add_u32 m0, s9, 4096\n\t"
      "s_nop 0\n\t"
      "global_load_lds_dwordx4 v197, s[6:7]\n\t"
      "s_branch EX_%=\n\t"
      "EH_%=:\n\t"
      "s_cmp_eq_u32 s19, 0\n\t"
      "s_cselect_b32 s13, 16, 0\n\t"
      "s_lshl2_add_u32 s13, s3, s13\n\t"
      "v_and_or_b32 v3, v1, 3, s13\n\t"
      "v_bfe_u32 v4, v3, 1, 3\n\t"
      "v_mul_u32_u24_e32 v3, 896, v3\n\t"
      "v_xor_b32_e32 v4, v4, v2\n\t"
      "v_lshl_add_u32 v3, v4, 4, v3\n\t"
      "v_add_u32_e32 v194, 1024, v3\n\t"
      "s_waitcnt lgkmcnt(0)\n\t"
      "s_branch EX_%=\n\t"
      "EB_%=:\n\t"
      "v_and_b32_e32 v3, 63, v0\n\t"
      "v_lshlrev_b32_e32 v3, 4, v3\n\t"
      "s_sub_u32 s18, s18, 16\n\t"
      "s_min_u32 s16, s3, 6\n\t"
      "s_xor_b32 s15, s17, 1\n\t"
      "s_lshl4_add_u32 s15, s15, s18\n\t"
      "s_mul_i32 s15, s15, 14336\n\t"
      "s_lshl_b32 s12, s16, 7\n\t"
      "s_add_u32 s15, s15, s12\n\t"
      "v_mul_u32_u24_e32 v6, 56, v3\n\t"
      "v_add_u32_e32 v6, s15, v6\n\t"
      "s_add_u32 s19, s17, 0\n\t"
      "s_and_b32 s19, s19, 3\n\t"
      "s_and_b32 s12, s17, 4\n\t"
      "s_or_b32 s19, s19, s12\n\t"
      "s_lshl4_add_u32 s19, s19, s18\n\t"
      "s_lshl3_add_u32 s12, s19, s3\n\t"
      "s_lshl_b32 s12, s12, 11\n\t"
      "s_mul_i32 s19, s19, 14336\n\t"
      "s_lshl_b32 s16, s16, 11\n\t"
      "s_add_u32 s19, s19, s16\n\t"
      "s_add_u32 s8, s16, 114688\n\t"
      "v_add_u32_e32 v1, s19, v3\n\t"
      "s_lshl4_add_u32 s16, s3, s18\n\t"
      "v_bfe_u32 v4, v0, 2, 2\n\t"
      "v_mul_u32_u24_e32 v4, 896, v4\n\t"
      "v_and_b32_e32 v5, 48, v3\n\t"
      "v_lshl_add_u32 v4, v5, 3, v4\n\t"
      "s_and_b32 s13, s17, 1\n\t"
      "s_mul_i32 s13, s13, 384\n\t"
      "v_add_u32_e32 v4, s13, v4\n\t"
      "v_lshrrev_b32_e32 v2, 8, v3\n\t"
      "s_mul_i32 s13, s16, 1\n\t"
      "s_mul_i32 s14, s16, 3\n\t"
      "s_add_u32 s14, s14, 128\n\t"
      "v_add_u32_e32 v5, s13, v2\n\t"
      "v_add_u32_e32 v2, s14, v2\n\t"
      "v_min_u32_e32 v2, 447, v2\n\t"
      "s_movk_i32 s13, 2688\n\t"
      "v_mad_u32_u24 v5, v5, s13, v4\n\t"
      "v_mad_u32_u24 v4, v2, s13, v4\n\t"
      "s_mov_b32 m0, 163328\n\t"
      "s_waitcnt lgkmcnt(0)\n\t"
      "s_mov_b32 m0, s8\n\t"
      "s_nop 0\n\t"
      "global_load_lds_dwordx4 v1, s[4:5]\n\t"
      "global_load_lds_dwordx4 v1, s[4:5] offset:1024\n\t"
      "s_mov_b32 m0, 163328\n\t"
      "s_mov_b32 exec_lo, 0xfff\n\t"
      "s_mov_b32 exec_hi, 0x0\n\t"
      "global_load_lds_dword v5, s[6:7]\n\t"
      "s_mov_b64 exec, -1\n\t"
      "s_barrier\n\t"
      "s_mov_b32 exec_lo, 0xfff0fff\n\t"
      "s_mov_b32 exec_hi, 0xfff\n\t"
      "global_load_lds_dword v4, s[6:7]\n\t"
      "s_mov_b64 exec, -1\n\t"
      "s_mov_b64 exec, 0xffff\n\t"
      "global_load_lds_dword v6, s[4:5]\n\t"
      "s_mov_b64 exec, -1\n\t"
      "EX_%=:\n\t"
      : "={v[192:199]}"(f)
      : "{s[0:1]}"(__builtin_amdgcn_kernarg_segment_ptr()), "{s2}"(blockIdx.x), "{v0}"(threadIdx.x)
      : "memory", "scc", "s3", "s4", "s5", "s6", "s7", "s8", "s9", "s10", "s11", "s12", "s13", "s14", "s15", "s16", "s17", "s18", "s19", "v1", "v2",
        "v3", "v4", "v5", "v6", "s40", "s41", "s42", "s43", "s48", "s49", "s50"
      );
  PIN();
  char* const ws = ws0 + (-(unsigned)(uintptr_t)ws0 & ((2u << 20) - 1));
  const int wave = __builtin_amdgcn_readfirstlane(threadIdx.x >> 6);
  const int xcd = blockIdx.x & 7, idx = blockIdx.x >> 3, side = xcd >> 2, xp = xcd & 3;
#define LANE_BITS(l) \
  const int lane = (l), g = lane >> 4, c = lane & 15, q4 = c >> 2, p4 = lane & 3, lr = lane >> 3, lc = lane & 7

  if (__builtin_expect(idx < 16, 1)) {
    const int rt = M44 ? 2 * (xcd & 6) + (idx >> 2) : 2 * xcd + (idx >> 3), hp = M44 ? (idx & 3) ^ (3 + (xcd & 1)) : idx & 7, hh = (wave >> 1) & 1, half = wave & 1;
    const unsigned sta = A_ST + half * 1024;
    if (wave < 4) {
      LANE_BITS(threadIdx.x & 63);
      u32x16 v;
      u32x8 s;
      v[0] = 4 * half * A_UNIT + c * 128 + ((g ^ swz_row(c)) << 4);
      v[1] = A_HALO + c * 128 + ((g ^ swz_row(c)) << 4);
#pragma unroll
      for (int j = 0; j < 2; ++j) {
        const int by = 56 * hh + 32 * j + 8 * p4;
        v[2 + j] = g * A_UNIT + 2048 + q4 * 128 + (((by >> 4) ^ swz_tr(8 * g + q4)) << 4) + (by & 8);
      }
      const unsigned csa = A_CS + hh * 384, exa = A_EX + hh * 3 * 2048;
      v[4] = csa + g * 16;
      v[5] = csa + c * 4;
      v[6] = v[4] + 64 * half;
      v[7] = v[5] + 64 * half;
      v[8] = A_FL + (wave ^ 1) * 8;
      v[9] = sta + g * 16;
      v[10] = sta + c * 4;
      const int key = 4 * g + (c & 1);
      v[11] = sta + 384 + (key <= c ? 128 : 64) + key * 4;
      unsigned vk1 = sta + 384 + (key + 2 <= c ? 128 : 64) + key * 4 + 8;
      v[12] = exa + half * 2048 + lane * 16;
      v[13] = exa + half * 2048 + lane * 8;
      v[14] = exa + lane * 16 + 8 * half;
      v[15] = lane * 16;
      put(s, 0, (u64)(ws + WS_O + part(((8 * (rt & 7) + 4 * half) * 16 + 2 * hp + hh) * 1024, 0)));
      s[2] = half;
      s[3] = UNI(rt == 0 && half == 0 ? 0xff800000u : 0u);
      s[4] = exa + 1024 + 512 * half;
      s[5] = A_FL + 32 + 4 * half;
      put(s, 6, (u64)(ws + WS_ST));
      unsigned hint = ST_H2 + part(((((rt & 7) * 8 + hp) * 2 + half) * 2 + hh) * 64, side);
      unsigned chk = ST_CK + part((((rt & 7) * 2 + half) * 16 + 2 * hp + hh) * 256, side);
      unsigned vn = (((wave ^ (4 * side)) * 256 + (idx & 1) * 128 + lr) * 28 + 7 * xp + min(idx >> 1, 6)) * 128 + ((lc ^ swz_tr(lr)) << 4);
      u64 tw = (u64)w1;
      unsigned lw = wave * 4096;
    asm volatile(
      "s_waitcnt lgkmcnt(0)\n\ts_nop 4\n\t"
      ".p2align 3\n\t"
      "v_xor_b32_e64 v248, 64, v224\n\t"
      "v_xor_b32_e32 v249, 64, v225\n\t"
      "s_mov_b32 s36, 0xaaaaaaaa\n\t"
      "s_mov_b32 s37, 0xaaaaaaaa\n\t"
      "s_mov_b32 s38, 0xfff0ffff\n\t"
      "s_mov_b32 s39, 0xf000ff00\n\t"
      "s_mov_b32 s40, 0xffc0fffc\n\t"
      "s_mov_b32 s41, 0xc000fc00\n\t"
      "s_mov_b32 s42, 0x0\n\t"
      "s_mov_b32 s43, 0xffff0000\n\t"
      "s_mov_b32 s44, 0x10001\n\t"
      "s_mov_b32 s45, 0x10001\n\t"
      "v_mov_b32_e32 v218, 0x0c0c0c0c\n\t"
      "v_mov_b32_e32 v216, 0xc0c0100\n\t"
      "v_mov_b32_e32 v217, 0x1000c0c\n\t"
      "v_cndmask_b32_e64 v216, v216, v217, s[36:37]\n\t"
      "v_cndmask_b32_e64 v240, v216, v218, s[38:39]\n\t"
      "v_cndmask_b32_e64 v242, v218, v216, s[38:39]\n\t"
      "v_mov_b32_e32 v216, 0xc0c0302\n\t"
      "v_mov_b32_e32 v217, 0x3020c0c\n\t"
      "v_cndmask_b32_e64 v216, v216, v217, s[36:37]\n\t"
      "v_cndmask_b32_e64 v241, v216, v218, s[40:41]\n\t"
      "v_cndmask_b32_e64 v243, v218, v216, s[40:41]\n\t"
      "v_xor_b32_e32 v213, 8, v232\n\t"
      "v_lshrrev_b32_e32 v214, 1, v239\n\t"
      "v_add_u32_e32 v214, s8, v214\n\t"
      "v_mov_b32_e32 v215, s9\n\t"
      "v_mov_b32_e32 v220, 154752\n\t"
      "v_lshrrev_b32_e32 v221, 2, v239\n\t"
      "s_add_u32 s56, s4, 32768\n\t"
      "s_addc_u32 s57, s5, 0\n\t"
      "s_add_u32 s58, s4, 65536\n\t"
      "s_addc_u32 s59, s5, 0\n\t"
      "s_add_u32 s60, s4, 98304\n\t"
      "s_addc_u32 s61, s5, 0\n\t"
      "s_and_b32 s52, s12, 0x2000\n\t"
      "v_mov_b32_e32 v244, s7\n\t"
      "v_mov_b32_e32 v245, s7\n\t"
      "v_mov_b32_e32 v246, s7\n\t"
      "v_mov_b32_e32 v247, s7\n\t"
      "v_mov_b64_e32 v[0:1], 0\n\t"
      "v_mov_b64_e32 v[2:3], 0\n\t"
      "v_mov_b64_e32 v[4:5], 0\n\t"
      "v_mov_b64_e32 v[6:7], 0\n\t"
      "v_mov_b64_e32 v[8:9], 0\n\t"
      "v_mov_b64_e32 v[10:11], 0\n\t"
      "v_mov_b64_e32 v[12:13], 0\n\t"
      "v_mov_b64_e32 v[14:15], 0\n\t"
      "v_mov_b64_e32 v[16:17], 0\n\t"
      "v_mov_b64_e32 v[18:19], 0\n\t"
      "v_mov_b64_e32 v[20:21], 0\n\t"
      "v_mov_b64_e32 v[22:23], 0\n\t"
      "v_mov_b64_e32 v[24:25], 0\n\t"
      "v_mov_b64_e32 v[26:27], 0\n\t"
      "v_mov_b64_e32 v[28:29], 0\n\t"
      "v_mov_b64_e32 v[30:31], 0\n\t"
      "v_mov_b64_e32 v[32:33], 0\n\t"
      "v_mov_b64_e32 v[34:35], 0\n\t"
      "v_mov_b64_e32 v[36:37], 0\n\t"
      "v_mov_b64_e32 v[38:39], 0\n\t"
      "v_mov_b64_e32 v[40:41], 0\n\t"
      "v_mov_b64_e32 v[42:43], 0\n\t"
      "v_mov_b64_e32 v[44:45], 0\n\t"
      "v_mov_b64_e32 v[46:47], 0\n\t"
      "v_mov_b64_e32 v[48:49], 0\n\t"
      "v_mov_b64_e32 v[50:51], 0\n\t"
      "v_mov_b64_e32 v[52:53], 0\n\t"
      "v_mov_b64_e32 v[54:55], 0\n\t"
      "v_mov_b64_e32 v[56:57], 0\n\t"
      "v_mov_b64_e32 v[58:59], 0\n\t"
      "v_mov_b64_e32 v[60:61], 0\n\t"
      "v_mov_b64_e32 v[62:63], 0\n\t"
      "v_mov_b64_e32 v[64:65], 0\n\t"
      "v_mov_b64_e32 v[66:67], 0\n\t"
      "v_mov_b64_e32 v[68:69], 0\n\t"
      "v_mov_b64_e32 v[70:71], 0\n\t"
      "v_mov_b64_e32 v[72:73], 0\n\t"
      "v_mov_b64_e32 v[74:75], 0\n\t"
      "v_mov_b64_e32 v[76:77], 0\n\t"
      "v_mov_b64_e32 v[78:79], 0\n\t"
      "v_mov_b64_e32 v[80:81], 0\n\t"
      "v_mov_b64_e32 v[82:83], 0\n\t"
      "v_mov_b64_e32 v[84:85], 0\n\t"
      "v_mov_b64_e32 v[86:87], 0\n\t"
      "v_mov_b64_e32 v[88:89], 0\n\t"
      "v_mov_b64_e32 v[90:91], 0\n\t"
      "v_mov_b64_e32 v[92:93], 0\n\t"
      "v_mov_b64_e32 v[94:95], 0\n\t"
      "v_mov_b64_e32 v[96:97], 0\n\t"
      "v_mov_b64_e32 v[98:99], 0\n\t"
      "v_mov_b64_e32 v[100:101], 0\n\t"
      "v_mov_b64_e32 v[102:103], 0\n\t"
      "v_mov_b64_e32 v[104:105], 0\n\t"
      "v_mov_b64_e32 v[106:107], 0\n\t"
      "s_mov_b32 s47, 6\n\t"
      "s_cmp_eq_u32 s6, 0\n\t"
      "s_cbranch_scc0 LOOP1_%=\n\t"
      "s_waitcnt vmcnt(0)\n\t"
      "s_barrier\n\t"
      "ds_read_b64_tr_b16 v[108:109], v226\n\t"
      "ds_read_b64_tr_b16 v[110:111], v226 offset:512\n\t"
      "ds_read_b128 v[132:135], v224\n\t"
      "ds_read_b128 v[136:139], v224 offset:5120\n\t"
      "ds_read_b128 v[140:143], v224 offset:10240\n\t"
      "ds_read_b128 v[144:147], v224 offset:15360\n\t"
      "ds_read_b64_tr_b16 v[112:113], v227\n\t"
      "ds_read_b64_tr_b16 v[114:115], v227 offset:512\n\t"
      "ds_read_b128 v[148:151], v225\n\t"
      "ds_read_b64_tr_b16 v[116:117], v226 offset:1024\n\t"
      "ds_read_b64_tr_b16 v[118:119], v226 offset:1536\n\t"
      "ds_read_b64_tr_b16 v[120:121], v227 offset:1024\n\t"
      "ds_read_b64_tr_b16 v[122:123], v227 offset:1536\n\t"
      "ds_read_b64_tr_b16 v[124:125], v226 offset:2048\n\t"
      "ds_read_b64_tr_b16 v[126:127], v226 offset:2560\n\t"
      "ds_read_b64_tr_b16 v[128:129], v227 offset:2048\n\t"
      "ds_read_b64_tr_b16 v[130:131], v227 offset:2560\n\t"
      "s_branch EVEN0_%=\n\t"
      "s_waitcnt lgkmcnt(0)\n\t"
      ".p2align 6\n\t"
      "ODD0_%=:\n\t"
      "s_waitcnt lgkmcnt(0)\n\t"
      "v_mfma_f32_16x16x32_bf16 v[0:3], v[152:155], v[176:179], v[0:3]\n\t"
      "v_add_u32_e32 v224, s46, v224\n\t"
      "v_add_u32_e32 v226, s46, v226\n\t"
      "v_mfma_f32_16x16x32_bf16 v[24:27], v[152:155], v[180:183], v[24:27]\n\t"
      "v_add_u32_e32 v227, s46, v227\n\t"
      "v_add_u32_e32 v225, s46, v225\n\t"
      "v_mfma_f32_16x16x32_bf16 v[48:51], v[152:155], v[184:187], v[48:51]\n\t"
      "s_barrier\n\t"
      "ds_read_b64_tr_b16 v[108:109], v226\n\t"
      "v_mfma_f32_16x16x32_bf16 v[72:75], v[152:155], v[188:191], v[72:75]\n\t"
      "ds_read_b64_tr_b16 v[110:111], v226 offset:512\n\t"
      "v_mfma_f32_16x16x32_bf16 v[4:7], v[156:159], v[176:179], v[4:7]\n\t"
      "ds_read_b128 v[132:135], v224\n\t"
      "v_mfma_f32_16x16x32_bf16 v[28:31], v[156:159], v[180:183], v[28:31]\n\t"
      "ds_read_b128 v[136:139], v224 offset:5120\n\t"
      "v_mfma_f32_16x16x32_bf16 v[52:55], v[156:159], v[184:187], v[52:55]\n\t"
      "ds_read_b128 v[140:143], v224 offset:10240\n\t"
      "v_mfma_f32_16x16x32_bf16 v[76:79], v[156:159], v[188:191], v[76:79]\n\t"
      "ds_read_b128 v[144:147], v224 offset:15360\n\t"
      "v_mfma_f32_16x16x32_bf16 v[96:99], v[152:155], v[192:195], v[96:99]\n\t"
      "v_mfma_f32_16x16x32_bf16 v[8:11], v[160:163], v[176:179], v[8:11]\n\t"
      "ds_read_b64_tr_b16 v[112:113], v227\n\t"
      "v_mfma_f32_16x16x32_bf16 v[32:35], v[160:163], v[180:183], v[32:35]\n\t"
      "ds_read_b64_tr_b16 v[114:115], v227 offset:512\n\t"
      "v_mfma_f32_16x16x32_bf16 v[56:59], v[160:163], v[184:187], v[56:59]\n\t"
      "ds_read_b128 v[148:151], v225\n\t"
      "v_mfma_f32_16x16x32_bf16 v[80:83], v[160:163], v[188:191], v[80:83]\n\t"
      "ds_read_b64_tr_b16 v[116:117], v226 offset:1024\n\t"
      "v_mfma_f32_16x16x32_bf16 v[12:15], v[164:167], v[176:179], v[12:15]\n\t"
      "ds_read_b64_tr_b16 v[118:119], v226 offset:1536\n\t"
      "v_mfma_f32_16x16x32_bf16 v[36:39], v[164:167], v[180:183], v[36:39]\n\t"
      "ds_read_b64_tr_b16 v[120:121], v227 offset:1024\n\t"
      "v_mfma_f32_16x16x32_bf16 v[60:63], v[164:167], v[184:187], v[60:63]\n\t"
      "v_mfma_f32_16x16x32_bf16 v[84:87], v[164:167], v[188:191], v[84:87]\n\t"
      "ds_read_b64_tr_b16 v[122:123], v227 offset:1536\n\t"
      "v_mfma_f32_16x16x32_bf16 v[100:103], v[160:163], v[192:195], v[100:103]\n\t"
      "ds_read_b64_tr_b16 v[124:125], v226 offset:2048\n\t"
      "v_mfma_f32_16x16x32_bf16 v[16:19], v[176:179], v[168:171], v[16:19]\n\t"
      "ds_read_b64_tr_b16 v[126:127], v226 offset:2560\n\t"
      "v_mfma_f32_16x16x32_bf16 v[40:43], v[180:183], v[168:171], v[40:43]\n\t"
      "ds_read_b64_tr_b16 v[128:129], v227 offset:2048\n\t"
      "v_mfma_f32_16x16x32_bf16 v[64:67], v[184:187], v[168:171], v[64:67]\n\t"
      "ds_read_b64_tr_b16 v[130:131], v227 offset:2560\n\t"
      "v_mfma_f32_16x16x32_bf16 v[88:91], v[188:191], v[168:171], v[88:91]\n\t"
      "v_mfma_f32_16x16x32_bf16 v[20:23], v[176:179], v[172:175], v[20:23]\n\t"
      "v_mfma_f32_16x16x32_bf16 v[44:47], v[180:183], v[172:175], v[44:47]\n\t"
      "v_add_u32_e32 v248, s46, v248\n\t"
      "v_add_u32_e32 v249, s46, v249\n\t"
      "v_mfma_f32_16x16x32_bf16 v[68:71], v[184:187], v[172:175], v[68:71]\n\t"
      "v_mfma_f32_16x16x32_bf16 v[92:95], v[188:191], v[172:175], v[92:95]\n\t"
      "v_mfma_f32_16x16x32_bf16 v[104:107], v[192:195], v[168:171], v[104:107]\n\t"
      "EVEN0_%=:\n\t"
      "s_waitcnt lgkmcnt(0)\n\t"
      "v_mfma_f32_16x16x32_bf16 v[0:3], v[108:111], v[132:135], v[0:3]\n\t"
      "ds_read_b64_tr_b16 v[152:153], v226 offset:20480\n\t"
      "v_mfma_f32_16x16x32_bf16 v[24:27], v[108:111], v[136:139], v[24:27]\n\t"
      "ds_read_b64_tr_b16 v[154:155], v226 offset:20992\n\t"
      "v_mfma_f32_16x16x32_bf16 v[48:51], v[108:111], v[140:143], v[48:51]\n\t"
      "ds_read_b128 v[176:179], v248\n\t"
      "v_mfma_f32_16x16x32_bf16 v[72:75], v[108:111], v[144:147], v[72:75]\n\t"
      "ds_read_b128 v[180:183], v248 offset:5120\n\t"
      "v_mfma_f32_16x16x32_bf16 v[4:7], v[112:115], v[132:135], v[4:7]\n\t"
      "v_mfma_f32_16x16x32_bf16 v[28:31], v[112:115], v[136:139], v[28:31]\n\t"
      "ds_read_b128 v[184:187], v248 offset:10240\n\t"
      "v_mfma_f32_16x16x32_bf16 v[52:55], v[112:115], v[140:143], v[52:55]\n\t"
      "ds_read_b128 v[188:191], v248 offset:15360\n\t"
      "v_mfma_f32_16x16x32_bf16 v[76:79], v[112:115], v[144:147], v[76:79]\n\t"
      "ds_read_b64_tr_b16 v[156:157], v227 offset:20480\n\t"
      "v_mfma_f32_16x16x32_bf16 v[96:99], v[108:111], v[148:151], v[96:99]\n\t"
      "v_mfma_f32_16x16x32_bf16 v[8:11], v[116:119], v[132:135], v[8:11]\n\t"
      "ds_read_b64_tr_b16 v[158:159], v227 offset:20992\n\t"
      "v_mfma_f32_16x16x32_bf16 v[32:35], v[116:119], v[136:139], v[32:35]\n\t"
      "ds_read_b128 v[192:195], v249\n\t"
      "v_mfma_f32_16x16x32_bf16 v[56:59], v[116:119], v[140:143], v[56:59]\n\t"
      "ds_read_b64_tr_b16 v[160:161], v226 offset:21504\n\t"
      "v_mfma_f32_16x16x32_bf16 v[80:83], v[116:119], v[144:147], v[80:83]\n\t"
      "ds_read_b64_tr_b16 v[162:163], v226 offset:22016\n\t"
      "v_mfma_f32_16x16x32_bf16 v[12:15], v[120:123], v[132:135], v[12:15]\n\t"
      "v_mfma_f32_16x16x32_bf16 v[36:39], v[120:123], v[136:139], v[36:39]\n\t"
      "ds_read_b64_tr_b16 v[164:165], v227 offset:21504\n\t"
      "v_mfma_f32_16x16x32_bf16 v[60:63], v[120:123], v[140:143], v[60:63]\n\t"
      "ds_read_b64_tr_b16 v[166:167], v227 offset:22016\n\t"
      "v_mfma_f32_16x16x32_bf16 v[84:87], v[120:123], v[144:147], v[84:87]\n\t"
      "ds_read_b64_tr_b16 v[168:169], v226 offset:22528\n\t"
      "v_mfma_f32_16x16x32_bf16 v[100:103], v[116:119], v[148:151], v[100:103]\n\t"
      "v_mfma_f32_16x16x32_bf16 v[16:19], v[132:135], v[124:127], v[16:19]\n\t"
      "ds_read_b64_tr_b16 v[170:171], v226 offset:23040\n\t"
      "v_mfma_f32_16x16x32_bf16 v[40:43], v[136:139], v[124:127], v[40:43]\n\t"
      "ds_read_b64_tr_b16 v[172:173], v227 offset:22528\n\t"
      "v_mfma_f32_16x16x32_bf16 v[64:67], v[140:143], v[124:127], v[64:67]\n\t"
      "ds_read_b64_tr_b16 v[174:175], v227 offset:23040\n\t"
      "s_cmp_eq_u32 s47, 4\n\t"
      "s_cselect_b32 s46, -2, 1\n\t"
      "v_mfma_f32_16x16x32_bf16 v[88:91], v[144:147], v[124:127], v[88:91]\n\t"
      "s_cmp_eq_u32 s47, 1\n\t"
      "s_cselect_b32 s46, -2, s46\n\t"
      "v_mfma_f32_16x16x32_bf16 v[20:23], v[132:135], v[128:131], v[20:23]\n\t"
      "s_mul_i32 s46, s46, 43008\n\t"
      "v_mfma_f32_16x16x32_bf16 v[44:47], v[136:139], v[128:131], v[44:47]\n\t"
      "v_mfma_f32_16x16x32_bf16 v[68:71], v[140:143], v[128:131], v[68:71]\n\t"
      "v_mfma_f32_16x16x32_bf16 v[92:95], v[144:147], v[128:131], v[92:95]\n\t"
      "s_sub_u32 s47, s47, 1\n\t"
      "v_mfma_f32_16x16x32_bf16 v[104:107], v[148:151], v[124:127], v[104:107]\n\t"
      "s_cbranch_scc0 ODD0_%=\n\t"
      "s_waitcnt lgkmcnt(0)\n\t"
      "v_mfma_f32_16x16x32_bf16 v[96:99], v[152:155], v[192:195], v[96:99]\n\t"
      "ds_read_b32 v212, v215\n\t"
      "v_mfma_f32_16x16x32_bf16 v[100:103], v[160:163], v[192:195], v[100:103]\n\t"
      "ds_read2_b32 v[134:135], v234 offset0:80 offset1:80\n\t"
      "v_mfma_f32_16x16x32_bf16 v[104:107], v[192:195], v[168:171], v[104:107]\n\t"
      "ds_read_b128 v[148:151], v233 offset:320\n\t"
      "v_mfma_f32_16x16x32_bf16 v[80:83], v[160:163], v[188:191], v[80:83]\n\t"
      "ds_read2_b32 v[248:249], v234 offset0:0 offset1:0\n\t"
      "v_mfma_f32_16x16x32_bf16 v[84:87], v[164:167], v[188:191], v[84:87]\n\t"
      "ds_read_b128 v[196:199], v233\n\t"
      "v_mfma_f32_16x16x32_bf16 v[88:91], v[188:191], v[168:171], v[88:91]\n\t"
      "ds_read2_b32 v[132:133], v234 offset0:64 offset1:64\n\t"
      "v_mfma_f32_16x16x32_bf16 v[92:95], v[188:191], v[172:175], v[92:95]\n\t"
      "s_mov_b64 exec, s[44:45]\n\t"
      "ds_write_b128 v230, v[96:99]\n\t"
      "ds_write_b128 v230, v[100:103] offset:128\n\t"
      "s_mov_b64 exec, 0xffff\n\t"
      "ds_write_b32 v231, v104 offset:256\n\t"
      "ds_write_b32 v213, v213\n\t"
      "s_mov_b64 exec, -1\n\t"
      "v_mfma_f32_16x16x32_bf16 v[56:59], v[160:163], v[184:187], v[56:59]\n\t"
      "ds_read2_b32 v[130:131], v234 offset0:48 offset1:48\n\t"
      "v_mfma_f32_16x16x32_bf16 v[60:63], v[164:167], v[184:187], v[60:63]\n\t"
      "ds_read2_b32 v[128:129], v234 offset0:32 offset1:32\n\t"
      "v_mfma_f32_16x16x32_bf16 v[64:67], v[184:187], v[168:171], v[64:67]\n\t"
      "s_waitcnt lgkmcnt(0)\n\t"
      "v_mfma_f32_16x16x32_bf16 v[68:71], v[184:187], v[172:175], v[68:71]\n\t"
      "ds_read_b32 v226, v232\n\t"
      "ds_read_b128 v[116:119], v228 offset:128\n\t"
      "v_mfma_f32_16x16x32_bf16 v[32:35], v[160:163], v[180:183], v[32:35]\n\t"
      "ds_read_b128 v[120:123], v228 offset:192\n\t"
      "v_mfma_f32_16x16x32_bf16 v[36:39], v[164:167], v[180:183], v[36:39]\n\t"
      "ds_read2_b32 v[250:251], v229 offset0:64 offset1:64\n\t"
      "v_mfma_f32_16x16x32_bf16 v[40:43], v[180:183], v[168:171], v[40:43]\n\t"
      "ds_read2_b32 v[252:253], v229 offset0:80 offset1:80\n\t"
      "v_mfma_f32_16x16x32_bf16 v[44:47], v[180:183], v[172:175], v[44:47]\n\t"
      "ds_read_b128 v[124:127], v230 offset:128\n\t"
      "v_mfma_f32_16x16x32_bf16 v[8:11], v[160:163], v[176:179], v[8:11]\n\t"
      "ds_read2_b32 v[224:225], v231 offset0:64 offset1:64\n\t"
      "v_mfma_f32_16x16x32_bf16 v[12:15], v[164:167], v[176:179], v[12:15]\n\t"
      "v_mfma_f32_16x16x32_bf16 v[16:19], v[176:179], v[168:171], v[16:19]\n\t"
      "s_waitcnt lgkmcnt(6)\n\t"
      "v_min_u32_e32 v226, v226, v212\n\t"
      "v_cmp_eq_u32_e32 vcc, 0, v226\n\t"
      "v_mfma_f32_16x16x32_bf16 v[20:23], v[176:179], v[172:175], v[20:23]\n\t"
      "LOOPX_%=:\n\t"
      "s_cbranch_vccnz XAR_%=\n\t"
      "XAB_%=:\n\t"
      "v_mfma_f32_16x16x32_bf16 v[72:75], v[152:155], v[188:191], v[72:75]\n\t"
      "s_waitcnt lgkmcnt(4)\n\t"
      "v_fmac_f32_e32 v80, v134, v116\n\t"
      "v_fmac_f32_e32 v81, v134, v117\n\t"
      "v_mfma_f32_16x16x32_bf16 v[76:79], v[156:159], v[188:191], v[76:79]\n\t"
      "v_fmac_f32_e32 v82, v134, v118\n\t"
      "v_fmac_f32_e32 v83, v134, v119\n\t"
      "v_mfma_f32_16x16x32_bf16 v[48:51], v[152:155], v[184:187], v[48:51]\n\t"
      "v_fmac_f32_e32 v84, v134, v120\n\t"
      "v_fmac_f32_e32 v85, v134, v121\n\t"
      "v_mfma_f32_16x16x32_bf16 v[52:55], v[156:159], v[184:187], v[52:55]\n\t"
      "v_fmac_f32_e32 v86, v134, v122\n\t"
      "v_fmac_f32_e32 v87, v134, v123\n\t"
      "v_mfma_f32_16x16x32_bf16 v[24:27], v[152:155], v[180:183], v[24:27]\n\t"
      "v_cvt_pk_bf16_f32 v80, v80, v81\n\t"
      "v_cvt_pk_bf16_f32 v81, v82, v83\n\t"
      "v_mfma_f32_16x16x32_bf16 v[28:31], v[156:159], v[180:183], v[28:31]\n\t"
      "v_cvt_pk_bf16_f32 v82, v84, v85\n\t"
      "v_cvt_pk_bf16_f32 v83, v86, v87\n\t"
      "v_mfma_f32_16x16x32_bf16 v[0:3], v[152:155], v[176:179], v[0:3]\n\t"
      "s_waitcnt lgkmcnt(2)\n\t"
      "v_fmac_f32_e32 v88, v148, v250\n\t"
      "v_fmac_f32_e32 v89, v149, v250\n\t"
      "v_mfma_f32_16x16x32_bf16 v[4:7], v[156:159], v[176:179], v[4:7]\n\t"
      "v_fmac_f32_e32 v90, v150, v250\n\t"
      "v_fmac_f32_e32 v91, v151, v250\n\t"
      "ds_write_b128 v236, v[80:83] offset:2048\n\t"
      "v_fmac_f32_e32 v92, v148, v252\n\t"
      "v_fmac_f32_e32 v93, v149, v252\n\t"
      "v_fmac_f32_e32 v94, v150, v252\n\t"
      "v_fmac_f32_e32 v95, v151, v252\n\t"
      "v_cvt_pk_bf16_f32 v164, v88, v89\n\t"
      "v_cvt_pk_bf16_f32 v165, v90, v91\n\t"
      "v_cvt_pk_bf16_f32 v174, v92, v93\n\t"
      "v_cvt_pk_bf16_f32 v175, v94, v95\n\t"
      "ds_read_b128 v[108:111], v228\n\t"
      "ds_read_b128 v[112:115], v228 offset:64\n\t"
      "s_waitcnt lgkmcnt(3)\n\t"
      "v_pk_fma_f32 v[100:101], v[248:249], v[124:125], v[100:101]\n\t"
      "v_pk_fma_f32 v[104:105], v[196:197], v[224:225], v[104:105]\n\t"
      "v_pk_fma_f32 v[102:103], v[248:249], v[126:127], v[102:103]\n\t"
      "v_pk_fma_f32 v[106:107], v[198:199], v[224:225], v[106:107]\n\t"
      "ds_write_b64 v237, v[164:165] offset:3072\n\t"
      "ds_write_b64 v237, v[174:175] offset:3584\n\t"
      "v_cvt_pk_bf16_f32 v100, v100, v101\n\t"
      "v_cvt_pk_bf16_f32 v101, v102, v103\n\t"
      "v_cvt_pk_bf16_f32 v102, v104, v105\n\t"
      "v_cvt_pk_bf16_f32 v103, v106, v107\n\t"
      "v_pk_fma_f32 v[56:57], v[132:133], v[116:117], v[56:57]\n\t"
      "v_pk_fma_f32 v[58:59], v[132:133], v[118:119], v[58:59]\n\t"
      "v_pk_fma_f32 v[60:61], v[132:133], v[120:121], v[60:61]\n\t"
      "v_pk_fma_f32 v[62:63], v[132:133], v[122:123], v[62:63]\n\t"
      "ds_write_b64 v238, v[100:101]\n\t"
      "ds_write_b64 v214, v[102:103]\n\t"
      "ds_write_b32 v213, v213 offset:4\n\t"
      "v_cvt_pk_bf16_f32 v56, v56, v57\n\t"
      "v_cvt_pk_bf16_f32 v57, v58, v59\n\t"
      "v_cvt_pk_bf16_f32 v58, v60, v61\n\t"
      "v_cvt_pk_bf16_f32 v59, v62, v63\n\t"
      "ds_read_b128 v[144:147], v233 offset:256\n\t"
      "ds_read_b128 v[140:143], v233 offset:192\n\t"
      "ds_read_b128 v[136:139], v233 offset:128\n\t"
      "v_pk_fma_f32 v[32:33], v[130:131], v[116:117], v[32:33]\n\t"
      "v_pk_fma_f32 v[34:35], v[130:131], v[118:119], v[34:35]\n\t"
      "v_pk_fma_f32 v[36:37], v[130:131], v[120:121], v[36:37]\n\t"
      "v_pk_fma_f32 v[38:39], v[130:131], v[122:123], v[38:39]\n\t"
      "v_pk_fma_f32 v[8:9], v[128:129], v[116:117], v[8:9]\n\t"
      "v_pk_fma_f32 v[10:11], v[128:129], v[118:119], v[10:11]\n\t"
      "v_pk_fma_f32 v[12:13], v[128:129], v[120:121], v[12:13]\n\t"
      "v_pk_fma_f32 v[14:15], v[128:129], v[122:123], v[14:15]\n\t"
      "s_waitcnt lgkmcnt(8)\n\t"
      "s_nop 0\n\t"
      "v_pk_fma_f32 v[72:73], v[134:135], v[108:109], v[72:73]\n\t"
      "v_pk_fma_f32 v[74:75], v[134:135], v[110:111], v[74:75]\n\t"
      "v_pk_fma_f32 v[76:77], v[134:135], v[112:113], v[76:77]\n\t"
      "v_pk_fma_f32 v[78:79], v[134:135], v[114:115], v[78:79]\n\t"
      "v_pk_fma_f32 v[48:49], v[132:133], v[108:109], v[48:49]\n\t"
      "v_pk_fma_f32 v[50:51], v[132:133], v[110:111], v[50:51]\n\t"
      "v_pk_fma_f32 v[52:53], v[132:133], v[112:113], v[52:53]\n\t"
      "v_pk_fma_f32 v[54:55], v[132:133], v[114:115], v[54:55]\n\t"
      "s_waitcnt lgkmcnt(3)\n\t"
      "ds_read2_b32 v[204:205], v235 offset0:0 offset1:16\n\t"
      "ds_read2_b32 v[206:207], v235 offset0:32 offset1:48\n\t"
      "v_pk_fma_f32 v[24:25], v[130:131], v[108:109], v[24:25]\n\t"
      "v_pk_fma_f32 v[26:27], v[130:131], v[110:111], v[26:27]\n\t"
      "v_pk_fma_f32 v[28:29], v[130:131], v[112:113], v[28:29]\n\t"
      "v_pk_fma_f32 v[30:31], v[130:131], v[114:115], v[30:31]\n\t"
      "ds_read2_b32 v[208:209], v254 offset0:0 offset1:16\n\t"
      "ds_read2_b32 v[210:211], v254 offset0:32 offset1:48\n\t"
      "v_pk_fma_f32 v[0:1], v[128:129], v[108:109], v[0:1]\n\t"
      "v_pk_fma_f32 v[2:3], v[128:129], v[110:111], v[2:3]\n\t"
      "v_pk_fma_f32 v[4:5], v[128:129], v[112:113], v[4:5]\n\t"
      "v_pk_fma_f32 v[6:7], v[128:129], v[114:115], v[6:7]\n\t"
      "ds_read2_b32 v[200:201], v234 offset0:192 offset1:208\n\t"
      "ds_read2_b32 v[202:203], v234 offset0:224 offset1:240\n\t"
      "ds_read_b32 v226, v232 offset:4\n\t"
      "ds_read_b128 v[152:155], v236\n\t"
      "ds_read_b64 v[156:157], v237 offset:1024\n\t"
      "ds_read_b64 v[166:167], v237 offset:1536\n\t"
      "s_waitcnt lgkmcnt(12)\n\t"
      "v_pk_fma_f32 v[64:65], v[144:145], v[250:251], v[64:65]\n\t"
      "v_pk_fma_f32 v[66:67], v[146:147], v[250:251], v[66:67]\n\t"
      "v_pk_fma_f32 v[68:69], v[144:145], v[252:253], v[68:69]\n\t"
      "v_pk_fma_f32 v[70:71], v[146:147], v[252:253], v[70:71]\n\t"
      "s_waitcnt lgkmcnt(11)\n\t"
      "v_pk_fma_f32 v[40:41], v[140:141], v[250:251], v[40:41]\n\t"
      "v_pk_fma_f32 v[42:43], v[142:143], v[250:251], v[42:43]\n\t"
      "v_pk_fma_f32 v[44:45], v[140:141], v[252:253], v[44:45]\n\t"
      "v_pk_fma_f32 v[46:47], v[142:143], v[252:253], v[46:47]\n\t"
      "s_waitcnt lgkmcnt(10)\n\t"
      "v_pk_fma_f32 v[16:17], v[136:137], v[250:251], v[16:17]\n\t"
      "v_pk_fma_f32 v[18:19], v[138:139], v[250:251], v[18:19]\n\t"
      "v_pk_fma_f32 v[20:21], v[136:137], v[252:253], v[20:21]\n\t"
      "v_pk_fma_f32 v[22:23], v[138:139], v[252:253], v[22:23]\n\t"
      "ds_read_b64 v[250:251], v220 offset:8848\n\t"
      "ds_read_b64 v[196:197], v220\n\t"
      "v_cvt_pk_bf16_f32 v72, v72, v73\n\t"
      "v_cvt_pk_bf16_f32 v73, v74, v75\n\t"
      "v_cvt_pk_bf16_f32 v74, v76, v77\n\t"
      "v_cvt_pk_bf16_f32 v75, v78, v79\n\t"
      "v_cndmask_b32_e64 v74, v74, 0, s[42:43]\n\t"
      "v_cndmask_b32_e64 v75, v75, 0, s[42:43]\n\t"
      "v_cvt_pk_bf16_f32 v48, v48, v49\n\t"
      "v_cvt_pk_bf16_f32 v49, v50, v51\n\t"
      "v_cvt_pk_bf16_f32 v50, v52, v53\n\t"
      "v_cvt_pk_bf16_f32 v51, v54, v55\n\t"
      "v_cndmask_b32_e64 v50, v50, 0, s[42:43]\n\t"
      "v_cndmask_b32_e64 v51, v51, 0, s[42:43]\n\t"
      "v_cvt_pk_bf16_f32 v32, v32, v33\n\t"
      "v_cvt_pk_bf16_f32 v33, v34, v35\n\t"
      "v_cvt_pk_bf16_f32 v34, v36, v37\n\t"
      "v_cvt_pk_bf16_f32 v35, v38, v39\n\t"
      "v_cvt_pk_bf16_f32 v8, v8, v9\n\t"
      "v_cvt_pk_bf16_f32 v9, v10, v11\n\t"
      "v_cvt_pk_bf16_f32 v10, v12, v13\n\t"
      "v_cvt_pk_bf16_f32 v11, v14, v15\n\t"
      "v_mfma_f32_16x16x32_bf16 v[76:79], v[56:59], v[72:75], 0\n\t"
      "v_cvt_pk_bf16_f32 v24, v24, v25\n\t"
      "v_cvt_pk_bf16_f32 v25, v26, v27\n\t"
      "v_mfma_f32_16x16x32_bf16 v[84:87], v[80:83], v[72:75], 0\n\t"
      "v_cvt_pk_bf16_f32 v26, v28, v29\n\t"
      "v_cvt_pk_bf16_f32 v27, v30, v31\n\t"
      "v_mfma_f32_16x16x32_bf16 v[52:55], v[32:35], v[48:51], 0\n\t"
      "v_cndmask_b32_e64 v26, v26, 0, s[42:43]\n\t"
      "v_cndmask_b32_e64 v27, v27, 0, s[42:43]\n\t"
      "v_mfma_f32_16x16x32_bf16 v[60:63], v[56:59], v[48:51], 0\n\t"
      "s_waitcnt lgkmcnt(5)\n\t"
      "v_cmp_eq_u32_e32 vcc, 0, v226\n\t"
      "v_cvt_pk_bf16_f32 v0, v0, v1\n\t"
      "v_mfma_f32_16x16x32_bf16 v[28:31], v[8:11], v[24:27], 0\n\t"
      "v_cvt_pk_bf16_f32 v1, v2, v3\n\t"
      "v_cvt_pk_bf16_f32 v2, v4, v5\n\t"
      "v_mfma_f32_16x16x32_bf16 v[36:39], v[32:35], v[24:27], 0\n\t"
      "v_cvt_pk_bf16_f32 v3, v6, v7\n\t"
      "v_cndmask_b32_e64 v2, v2, 0, s[42:43]\n\t"
      "v_cndmask_b32_e64 v3, v3, 0, s[42:43]\n\t"
      "s_cbranch_vccnz XBR_%=\n\t"
      "XBB_%=:\n\t"
      "v_cvt_pk_bf16_f32 v162, v64, v65\n\t"
      "s_waitcnt lgkmcnt(4)\n\t"
      "v_mfma_f32_16x16x32_bf16 v[4:7], v[152:155], v[0:3], v[244:247]\n\t"
      "v_cvt_pk_bf16_f32 v163, v66, v67\n\t"
      "v_cvt_pk_bf16_f32 v172, v68, v69\n\t"
      "v_mfma_f32_16x16x32_bf16 v[12:15], v[8:11], v[0:3], 0\n\t"
      "v_cvt_pk_bf16_f32 v173, v70, v71\n\t"
      "v_cvt_pk_bf16_f32 v160, v40, v41\n\t"
      "v_cndmask_b32_e64 v76, v76, v77, s[36:37]\n\t"
      "v_cndmask_b32_e64 v84, v84, v85, s[36:37]\n\t"
      "v_cndmask_b32_e64 v78, v78, v79, s[36:37]\n\t"
      "v_cndmask_b32_e64 v86, v86, v87, s[36:37]\n\t"
      "v_cndmask_b32_e64 v179, v76, v84, s[38:39]\n\t"
      "v_cndmask_b32_e64 v183, v78, v86, s[40:41]\n\t"
      "v_cndmask_b32_e64 v52, v52, v53, s[36:37]\n\t"
      "v_cndmask_b32_e64 v60, v60, v61, s[36:37]\n\t"
      "v_cndmask_b32_e64 v54, v54, v55, s[36:37]\n\t"
      "v_cndmask_b32_e64 v62, v62, v63, s[36:37]\n\t"
      "v_cndmask_b32_e64 v178, v52, v60, s[38:39]\n\t"
      "v_cndmask_b32_e64 v182, v54, v62, s[40:41]\n\t"
      "v_cndmask_b32_e64 v28, v28, v29, s[36:37]\n\t"
      "v_cndmask_b32_e64 v36, v36, v37, s[36:37]\n\t"
      "v_cndmask_b32_e64 v30, v30, v31, s[36:37]\n\t"
      "v_cndmask_b32_e64 v38, v38, v39, s[36:37]\n\t"
      "v_cndmask_b32_e64 v177, v28, v36, s[38:39]\n\t"
      "v_cndmask_b32_e64 v181, v30, v38, s[40:41]\n\t"
      "v_cndmask_b32_e64 v4, v4, v5, s[36:37]\n\t"
      "v_cndmask_b32_e64 v12, v12, v13, s[36:37]\n\t"
      "v_cndmask_b32_e64 v6, v6, v7, s[36:37]\n\t"
      "v_cndmask_b32_e64 v14, v14, v15, s[36:37]\n\t"
      "v_cndmask_b32_e64 v176, v4, v12, s[38:39]\n\t"
      "v_cndmask_b32_e64 v180, v6, v14, s[40:41]\n\t"
      "v_pk_mul_f32 v[178:179], v[178:179], v[206:207]\n\t"
      "v_pk_mul_f32 v[182:183], v[182:183], v[210:211]\n\t"
      "v_pk_mul_f32 v[176:177], v[176:177], v[204:205]\n\t"
      "v_pk_mul_f32 v[180:181], v[180:181], v[208:209]\n\t"
      "v_max_f32_e32 v219, v179, v183\n\t"
      "v_max_f32_e32 v217, v177, v181\n\t"
      "v_max_f32_e32 v218, v178, v182\n\t"
      "v_max_f32_e32 v216, v176, v180\n\t"
      "v_cvt_pk_bf16_f32 v161, v42, v43\n\t"
      "v_permlane32_swap_b32_e32 v217, v219\n\t"
      "v_permlane32_swap_b32_e32 v216, v218\n\t"
      "v_max_f32_e32 v217, v217, v219\n\t"
      "v_max_f32_e32 v216, v216, v218\n\t"
      "v_cvt_pk_bf16_f32 v170, v44, v45\n\t"
      "v_cvt_pk_bf16_f32 v171, v46, v47\n\t"
      "v_permlane16_swap_b32_e32 v216, v217\n\t"
      "v_max_f32_e32 v216, v216, v217\n\t"
      "v_mov_b32_e32 v217, v216\n\t"
      "v_cvt_pk_bf16_f32 v158, v16, v17\n\t"
      "s_waitcnt lgkmcnt(0)\n\t"
      "v_xad_u32 v252, s13, v197, v221\n\t"
      "v_permlane16_swap_b32_e32 v216, v217\n\t"
      "v_mov_b32_e32 v218, v216\n\t"
      "v_mov_b32_e64 v219, v217\n\t"
      "v_xad_u32 v196, s52, v196, v239\n\t"
      "v_permlane32_swap_b32_e32 v216, v218\n\t"
      "v_permlane32_swap_b32_e32 v217, v219\n\t"
      "v_pk_add_f32 v[178:179], v[178:179], v[218:219] neg_lo:[0,1] neg_hi:[0,1]\n\t"
      "v_pk_add_f32 v[182:183], v[182:183], v[218:219] neg_lo:[0,1] neg_hi:[0,1]\n\t"
      "v_pk_add_f32 v[176:177], v[176:177], v[216:217] neg_lo:[0,1] neg_hi:[0,1]\n\t"
      "v_pk_add_f32 v[180:181], v[180:181], v[216:217] neg_lo:[0,1] neg_hi:[0,1]\n\t"
      "v_pk_mul_f32 v[178:179], v[178:179], v[202:203]\n\t"
      "v_pk_mul_f32 v[182:183], v[182:183], v[202:203]\n\t"
      "v_pk_mul_f32 v[176:177], v[176:177], v[200:201]\n\t"
      "v_pk_mul_f32 v[180:181], v[180:181], v[200:201]\n\t"
      "v_exp_f32_e32 v179, v179\n\t"
      "v_exp_f32_e32 v183, v183\n\t"
      "v_exp_f32_e32 v178, v178\n\t"
      "v_exp_f32_e32 v182, v182\n\t"
      "v_exp_f32_e32 v177, v177\n\t"
      "v_exp_f32_e32 v181, v181\n\t"
      "v_exp_f32_e32 v176, v176\n\t"
      "v_exp_f32_e32 v180, v180\n\t"
      "v_xor_b32_e64 v253, s12, v197\n\t"
      "s_mov_b64 exec, 1\n\t"
      "global_store_dwordx2 v253, v[250:251], s[10:11] sc1\n\t"
      "s_mov_b64 exec, -1\n\t"
      "v_pk_add_f32 v[218:219], v[178:179], v[182:183]\n\t"
      "v_pk_add_f32 v[216:217], v[176:177], v[180:181]\n\t"
      "v_cvt_pk_bf16_f32 v159, v18, v19\n\t"
      "v_cvt_pk_bf16_f32 v168, v20, v21\n\t"
      "v_permlane32_swap_b32_e32 v217, v219\n\t"
      "v_permlane32_swap_b32_e32 v216, v218\n\t"
      "v_add_f32_e32 v217, v217, v219\n\t"
      "v_add_f32_e32 v216, v216, v218\n\t"
      "v_pk_mul_f32 v[178:179], v[178:179], v[206:207]\n\t"
      "v_pk_mul_f32 v[182:183], v[182:183], v[210:211]\n\t"
      "v_permlane16_swap_b32_e32 v216, v217\n\t"
      "v_add_f32_e32 v216, v216, v217\n\t"
      "v_rcp_f32_e32 v216, v216\n\t"
      "v_pk_mul_f32 v[176:177], v[176:177], v[204:205]\n\t"
      "v_mov_b32_e32 v217, v216\n\t"
      "v_pk_mul_f32 v[180:181], v[180:181], v[208:209]\n\t"
      "v_cvt_pk_bf16_f32 v169, v22, v23\n\t"
      "v_permlane16_swap_b32_e32 v216, v217\n\t"
      "v_mov_b32_e32 v218, v216\n\t"
      "v_mov_b32_e32 v219, v217\n\t"
      "s_nop 0\n\t"
      "v_permlane32_swap_b32_e32 v216, v218\n\t"
      "v_permlane32_swap_b32_e32 v217, v219\n\t"
      "v_pk_mul_f32 v[178:179], v[178:179], v[218:219]\n\t"
      "v_pk_mul_f32 v[182:183], v[182:183], v[218:219]\n\t"
      "v_pk_mul_f32 v[176:177], v[176:177], v[216:217]\n\t"
      "v_pk_mul_f32 v[180:181], v[180:181], v[216:217]\n\t"
      "v_cvt_pk_bf16_f32 v179, v179, v183\n\t"
      "v_cvt_pk_bf16_f32 v178, v178, v182\n\t"
      "v_cvt_pk_bf16_f32 v177, v177, v181\n\t"
      "v_cvt_pk_bf16_f32 v176, v176, v180\n\t"
      "v_perm_b32 v72, v179, v179, v240\n\t"
      "v_perm_b32 v73, v179, v179, v241\n\t"
      "v_perm_b32 v74, v179, v179, v242\n\t"
      "v_perm_b32 v75, v179, v179, v243\n\t"
      "v_perm_b32 v48, v178, v178, v240\n\t"
      "v_perm_b32 v49, v178, v178, v241\n\t"
      "v_mfma_f32_16x16x32_bf16 v[88:91], v[162:165], v[72:75], 0\n\t"
      "v_perm_b32 v50, v178, v178, v242\n\t"
      "v_perm_b32 v51, v178, v178, v243\n\t"
      "v_mfma_f32_16x16x32_bf16 v[92:95], v[172:175], v[72:75], 0\n\t"
      "v_perm_b32 v24, v177, v177, v240\n\t"
      "v_perm_b32 v25, v177, v177, v241\n\t"
      "v_mfma_f32_16x16x32_bf16 v[64:67], v[160:163], v[48:51], 0\n\t"
      "v_perm_b32 v26, v177, v177, v242\n\t"
      "v_perm_b32 v27, v177, v177, v243\n\t"
      "v_mfma_f32_16x16x32_bf16 v[68:71], v[170:173], v[48:51], 0\n\t"
      "v_perm_b32 v0, v176, v176, v240\n\t"
      "v_perm_b32 v1, v176, v176, v241\n\t"
      "v_mfma_f32_16x16x32_bf16 v[40:43], v[158:161], v[24:27], 0\n\t"
      "v_perm_b32 v2, v176, v176, v242\n\t"
      "v_perm_b32 v3, v176, v176, v243\n\t"
      "v_mfma_f32_16x16x32_bf16 v[44:47], v[168:171], v[24:27], 0\n\t"
      "s_nop 0\n\t"
      "v_mfma_f32_16x16x32_bf16 v[16:19], v[156:159], v[0:3], 0\n\t"
      "v_cvt_pk_bf16_f32 v80, v88, v89\n\t"
      "v_cvt_pk_bf16_f32 v81, v90, v91\n\t"
      "v_mfma_f32_16x16x32_bf16 v[20:23], v[166:169], v[0:3], 0\n\t"
      "v_cvt_pk_bf16_f32 v82, v92, v93\n\t"
      "v_cvt_pk_bf16_f32 v83, v94, v95\n\t"
      "global_store_dwordx4 v196, v[80:83], s[60:61] sc1\n\t"
      "v_add3_u32 v219, v80, v81, v82\n\t"
      "v_add_u32_e32 v219, v219, v83\n\t"
      "v_cvt_pk_bf16_f32 v56, v64, v65\n\t"
      "v_cvt_pk_bf16_f32 v57, v66, v67\n\t"
      "v_cvt_pk_bf16_f32 v58, v68, v69\n\t"
      "v_cvt_pk_bf16_f32 v59, v70, v71\n\t"
      "global_store_dwordx4 v196, v[56:59], s[58:59] sc1\n\t"
      "v_add3_u32 v218, v56, v57, v58\n\t"
      "v_add_u32_e64 v218, v218, v59\n\t"
      "v_cvt_pk_bf16_f32 v32, v40, v41\n\t"
      "v_cvt_pk_bf16_f32 v33, v42, v43\n\t"
      "v_cvt_pk_bf16_f32 v34, v44, v45\n\t"
      "v_cvt_pk_bf16_f32 v35, v46, v47\n\t"
      "global_store_dwordx4 v196, v[32:35], s[56:57] sc1\n\t"
      "v_add3_u32 v217, v32, v33, v34\n\t"
      "v_add_u32_e64 v217, v217, v35\n\t"
      "v_cvt_pk_bf16_f32 v8, v16, v17\n\t"
      "v_cvt_pk_bf16_f32 v9, v18, v19\n\t"
      "v_cvt_pk_bf16_f32 v10, v20, v21\n\t"
      "v_cvt_pk_bf16_f32 v11, v22, v23\n\t"
      "global_store_dwordx4 v196, v[8:11], s[4:5] sc1\n\t"
      "v_add3_u32 v216, v8, v9, v10\n\t"
      "v_add_u32_e32 v216, v216, v11\n\t"
      "v_permlane32_swap_b32_e32 v217, v219\n\t"
      "v_add_u32_e32 v217, v217, v219\n\t"
      "v_permlane32_swap_b32_e32 v216, v218\n\t"
      "v_add_u32_e64 v216, v216, v218\n\t"
      "s_nop 1\n\t"
      "v_permlane16_swap_b32_e32 v216, v217\n\t"
      "v_add3_u32 v216, v216, v217, v250\n\t"
      "global_store_dword v252, v216, s[10:11] sc1\n\t"
      "v_xor_b32_e64 v120, 64, v222\n\t"
      "v_add_u32_e32 v121, 55296, v222\n\t"
      "v_add_u32_e32 v122, 82944, v120\n\t"
      "v_add_u32_e32 v120, 27648, v120\n\t"
      "s_barrier\n\t"
      "s_add_u32 m0, s16, 65536\n\t"
      "s_add_u32 s48, s14, 0\n\t"
      "s_addc_u32 s49, s15, 0\n\t"
      "global_load_lds_dwordx4 v222, s[48:49]\n\t"
      "global_load_lds_dwordx4 v120, s[48:49] offset:1024\n\t"
      "global_load_lds_dwordx4 v121, s[48:49] offset:2048\n\t"
      "global_load_lds_dwordx4 v122, s[48:49] offset:3072\n\t"
      "s_add_u32 m0, s16, 98304\n\t"
      "s_add_u32 s48, s14, 114688\n\t"
      "s_addc_u32 s49, s15, 0\n\t"
      "global_load_lds_dwordx4 v222, s[48:49]\n\t"
      "global_load_lds_dwordx4 v120, s[48:49] offset:1024\n\t"
      "global_load_lds_dwordx4 v121, s[48:49] offset:2048\n\t"
      "global_load_lds_dwordx4 v122, s[48:49] offset:3072\n\t"
      "s_nop 1\n\t"
      "s_branch CEND_%=\n\t"
      "LOOP1_%=:\n\t"
      "s_waitcnt vmcnt(0)\n\t"
      "s_barrier\n\t"
      "ds_read_b64_tr_b16 v[108:109], v226\n\t"
      "ds_read_b64_tr_b16 v[110:111], v226 offset:512\n\t"
      "ds_read_b128 v[132:135], v224\n\t"
      "ds_read_b128 v[136:139], v224 offset:5120\n\t"
      "ds_read_b128 v[140:143], v224 offset:10240\n\t"
      "ds_read_b128 v[144:147], v224 offset:15360\n\t"
      "ds_read_b64_tr_b16 v[112:113], v227\n\t"
      "ds_read_b64_tr_b16 v[114:115], v227 offset:512\n\t"
      "ds_read_b128 v[148:151], v225\n\t"
      "ds_read_b64_tr_b16 v[116:117], v226 offset:1024\n\t"
      "ds_read_b64_tr_b16 v[118:119], v226 offset:1536\n\t"
      "ds_read_b64_tr_b16 v[120:121], v227 offset:1024\n\t"
      "ds_read_b64_tr_b16 v[122:123], v227 offset:1536\n\t"
      "ds_read_b64_tr_b16 v[124:125], v226 offset:2048\n\t"
      "ds_read_b64_tr_b16 v[126:127], v226 offset:2560\n\t"
      "ds_read_b64_tr_b16 v[128:129], v227 offset:2048\n\t"
      "ds_read_b64_tr_b16 v[130:131], v227 offset:2560\n\t"
      "s_branch EVEN1_%=\n\t"
      "s_waitcnt lgkmcnt(0)\n\t"
      ".p2align 6\n\t"
      "ODD1_%=:\n\t"
      "s_waitcnt lgkmcnt(0)\n\t"
      "v_mfma_f32_16x16x32_bf16 v[0:3], v[152:155], v[176:179], v[0:3]\n\t"
      "v_add_u32_e32 v224, s46, v224\n\t"
      "v_add_u32_e32 v226, s46, v226\n\t"
      "v_mfma_f32_16x16x32_bf16 v[24:27], v[152:155], v[180:183], v[24:27]\n\t"
      "v_add_u32_e32 v227, s46, v227\n\t"
      "v_add_u32_e32 v225, s46, v225\n\t"
      "v_mfma_f32_16x16x32_bf16 v[48:51], v[152:155], v[184:187], v[48:51]\n\t"
      "s_barrier\n\t"
      "ds_read_b64_tr_b16 v[108:109], v226\n\t"
      "v_mfma_f32_16x16x32_bf16 v[72:75], v[152:155], v[188:191], v[72:75]\n\t"
      "ds_read_b64_tr_b16 v[110:111], v226 offset:512\n\t"
      "v_mfma_f32_16x16x32_bf16 v[4:7], v[156:159], v[176:179], v[4:7]\n\t"
      "ds_read_b128 v[132:135], v224\n\t"
      "v_mfma_f32_16x16x32_bf16 v[28:31], v[156:159], v[180:183], v[28:31]\n\t"
      "ds_read_b128 v[136:139], v224 offset:5120\n\t"
      "v_mfma_f32_16x16x32_bf16 v[52:55], v[156:159], v[184:187], v[52:55]\n\t"
      "ds_read_b128 v[140:143], v224 offset:10240\n\t"
      "v_mfma_f32_16x16x32_bf16 v[76:79], v[156:159], v[188:191], v[76:79]\n\t"
      "ds_read_b128 v[144:147], v224 offset:15360\n\t"
      "v_mfma_f32_16x16x32_bf16 v[96:99], v[156:159], v[192:195], v[96:99]\n\t"
      "v_mfma_f32_16x16x32_bf16 v[8:11], v[160:163], v[176:179], v[8:11]\n\t"
      "ds_read_b64_tr_b16 v[112:113], v227\n\t"
      "v_mfma_f32_16x16x32_bf16 v[32:35], v[160:163], v[180:183], v[32:35]\n\t"
      "ds_read_b64_tr_b16 v[114:115], v227 offset:512\n\t"
      "v_mfma_f32_16x16x32_bf16 v[56:59], v[160:163], v[184:187], v[56:59]\n\t"
      "ds_read_b128 v[148:151], v225\n\t"
      "v_mfma_f32_16x16x32_bf16 v[80:83], v[160:163], v[188:191], v[80:83]\n\t"
      "ds_read_b64_tr_b16 v[116:117], v226 offset:1024\n\t"
      "v_mfma_f32_16x16x32_bf16 v[12:15], v[164:167], v[176:179], v[12:15]\n\t"
      "ds_read_b64_tr_b16 v[118:119], v226 offset:1536\n\t"
      "v_mfma_f32_16x16x32_bf16 v[36:39], v[164:167], v[180:183], v[36:39]\n\t"
      "ds_read_b64_tr_b16 v[120:121], v227 offset:1024\n\t"
      "v_mfma_f32_16x16x32_bf16 v[60:63], v[164:167], v[184:187], v[60:63]\n\t"
      "v_mfma_f32_16x16x32_bf16 v[84:87], v[164:167], v[188:191], v[84:87]\n\t"
      "ds_read_b64_tr_b16 v[122:123], v227 offset:1536\n\t"
      "v_mfma_f32_16x16x32_bf16 v[100:103], v[164:167], v[192:195], v[100:103]\n\t"
      "ds_read_b64_tr_b16 v[124:125], v226 offset:2048\n\t"
      "v_mfma_f32_16x16x32_bf16 v[16:19], v[176:179], v[168:171], v[16:19]\n\t"
      "ds_read_b64_tr_b16 v[126:127], v226 offset:2560\n\t"
      "v_mfma_f32_16x16x32_bf16 v[40:43], v[180:183], v[168:171], v[40:43]\n\t"
      "ds_read_b64_tr_b16 v[128:129], v227 offset:2048\n\t"
      "v_mfma_f32_16x16x32_bf16 v[64:67], v[184:187], v[168:171], v[64:67]\n\t"
      "ds_read_b64_tr_b16 v[130:131], v227 offset:2560\n\t"
      "v_mfma_f32_16x16x32_bf16 v[88:91], v[188:191], v[168:171], v[88:91]\n\t"
      "v_mfma_f32_16x16x32_bf16 v[20:23], v[176:179], v[172:175], v[20:23]\n\t"
      "v_mfma_f32_16x16x32_bf16 v[44:47], v[180:183], v[172:175], v[44:47]\n\t"
      "v_add_u32_e32 v248, s46, v248\n\t"
      "v_add_u32_e32 v249, s46, v249\n\t"
      "v_mfma_f32_16x16x32_bf16 v[68:71], v[184:187], v[172:175], v[68:71]\n\t"
      "v_mfma_f32_16x16x32_bf16 v[92:95], v[188:191], v[172:175], v[92:95]\n\t"
      "v_mfma_f32_16x16x32_bf16 v[104:107], v[192:195], v[172:175], v[104:107]\n\t"
      "EVEN1_%=:\n\t"
      "s_waitcnt lgkmcnt(0)\n\t"
      "v_mfma_f32_16x16x32_bf16 v[0:3], v[108:111], v[132:135], v[0:3]\n\t"
      "ds_read_b64_tr_b16 v[152:153], v226 offset:20480\n\t"
      "v_mfma_f32_16x16x32_bf16 v[24:27], v[108:111], v[136:139], v[24:27]\n\t"
      "ds_read_b64_tr_b16 v[154:155], v226 offset:20992\n\t"
      "v_mfma_f32_16x16x32_bf16 v[48:51], v[108:111], v[140:143], v[48:51]\n\t"
      "ds_read_b128 v[176:179], v248\n\t"
      "v_mfma_f32_16x16x32_bf16 v[72:75], v[108:111], v[144:147], v[72:75]\n\t"
      "ds_read_b128 v[180:183], v248 offset:5120\n\t"
      "v_mfma_f32_16x16x32_bf16 v[4:7], v[112:115], v[132:135], v[4:7]\n\t"
      "v_mfma_f32_16x16x32_bf16 v[28:31], v[112:115], v[136:139], v[28:31]\n\t"
      "ds_read_b128 v[184:187], v248 offset:10240\n\t"
      "v_mfma_f32_16x16x32_bf16 v[52:55], v[112:115], v[140:143], v[52:55]\n\t"
      "ds_read_b128 v[188:191], v248 offset:15360\n\t"
      "v_mfma_f32_16x16x32_bf16 v[76:79], v[112:115], v[144:147], v[76:79]\n\t"
      "ds_read_b64_tr_b16 v[156:157], v227 offset:20480\n\t"
      "v_mfma_f32_16x16x32_bf16 v[96:99], v[112:115], v[148:151], v[96:99]\n\t"
      "v_mfma_f32_16x16x32_bf16 v[8:11], v[116:119], v[132:135], v[8:11]\n\t"
      "ds_read_b64_tr_b16 v[158:159], v227 offset:20992\n\t"
      "v_mfma_f32_16x16x32_bf16 v[32:35], v[116:119], v[136:139], v[32:35]\n\t"
      "ds_read_b128 v[192:195], v249\n\t"
      "v_mfma_f32_16x16x32_bf16 v[56:59], v[116:119], v[140:143], v[56:59]\n\t"
      "ds_read_b64_tr_b16 v[160:161], v226 offset:21504\n\t"
      "v_mfma_f32_16x16x32_bf16 v[80:83], v[116:119], v[144:147], v[80:83]\n\t"
      "ds_read_b64_tr_b16 v[162:163], v226 offset:22016\n\t"
      "v_mfma_f32_16x16x32_bf16 v[12:15], v[120:123], v[132:135], v[12:15]\n\t"
      "v_mfma_f32_16x16x32_bf16 v[36:39], v[120:123], v[136:139], v[36:39]\n\t"
      "ds_read_b64_tr_b16 v[164:165], v227 offset:21504\n\t"
      "v_mfma_f32_16x16x32_bf16 v[60:63], v[120:123], v[140:143], v[60:63]\n\t"
      "ds_read_b64_tr_b16 v[166:167], v227 offset:22016\n\t"
      "v_mfma_f32_16x16x32_bf16 v[84:87], v[120:123], v[144:147], v[84:87]\n\t"
      "ds_read_b64_tr_b16 v[168:169], v226 offset:22528\n\t"
      "v_mfma_f32_16x16x32_bf16 v[100:103], v[120:123], v[148:151], v[100:103]\n\t"
      "v_mfma_f32_16x16x32_bf16 v[16:19], v[132:135], v[124:127], v[16:19]\n\t"
      "ds_read_b64_tr_b16 v[170:171], v226 offset:23040\n\t"
      "v_mfma_f32_16x16x32_bf16 v[40:43], v[136:139], v[124:127], v[40:43]\n\t"
      "ds_read_b64_tr_b16 v[172:173], v227 offset:22528\n\t"
      "v_mfma_f32_16x16x32_bf16 v[64:67], v[140:143], v[124:127], v[64:67]\n\t"
      "ds_read_b64_tr_b16 v[174:175], v227 offset:23040\n\t"
      "s_cmp_eq_u32 s47, 4\n\t"
      "s_cselect_b32 s46, -2, 1\n\t"
      "v_mfma_f32_16x16x32_bf16 v[88:91], v[144:147], v[124:127], v[88:91]\n\t"
      "s_cmp_eq_u32 s47, 1\n\t"
      "s_cselect_b32 s46, -2, s46\n\t"
      "v_mfma_f32_16x16x32_bf16 v[20:23], v[132:135], v[128:131], v[20:23]\n\t"
      "s_mul_i32 s46, s46, 43008\n\t"
      "v_mfma_f32_16x16x32_bf16 v[44:47], v[136:139], v[128:131], v[44:47]\n\t"
      "v_mfma_f32_16x16x32_bf16 v[68:71], v[140:143], v[128:131], v[68:71]\n\t"
      "v_mfma_f32_16x16x32_bf16 v[92:95], v[144:147], v[128:131], v[92:95]\n\t"
      "s_sub_u32 s47, s47, 1\n\t"
      "v_mfma_f32_16x16x32_bf16 v[104:107], v[148:151], v[128:131], v[104:107]\n\t"
      "s_cbranch_scc0 ODD1_%=\n\t"
      "s_waitcnt lgkmcnt(0)\n\t"
      "v_mfma_f32_16x16x32_bf16 v[96:99], v[156:159], v[192:195], v[96:99]\n\t"
      "ds_read_b32 v212, v215\n\t"
      "v_mfma_f32_16x16x32_bf16 v[100:103], v[164:167], v[192:195], v[100:103]\n\t"
      "ds_read2_b32 v[134:135], v234 offset0:80 offset1:80\n\t"
      "v_mfma_f32_16x16x32_bf16 v[104:107], v[192:195], v[172:175], v[104:107]\n\t"
      "ds_read_b128 v[148:151], v233 offset:320\n\t"
      "v_mfma_f32_16x16x32_bf16 v[80:83], v[160:163], v[188:191], v[80:83]\n\t"
      "ds_read2_b32 v[248:249], v234 offset0:0 offset1:0\n\t"
      "v_mfma_f32_16x16x32_bf16 v[84:87], v[164:167], v[188:191], v[84:87]\n\t"
      "ds_read_b128 v[196:199], v233\n\t"
      "v_mfma_f32_16x16x32_bf16 v[88:91], v[188:191], v[168:171], v[88:91]\n\t"
      "ds_read2_b32 v[132:133], v234 offset0:64 offset1:64\n\t"
      "v_mfma_f32_16x16x32_bf16 v[92:95], v[188:191], v[172:175], v[92:95]\n\t"
      "s_mov_b64 exec, s[44:45]\n\t"
      "ds_write_b128 v230, v[96:99]\n\t"
      "ds_write_b128 v230, v[100:103] offset:128\n\t"
      "s_mov_b64 exec, 0xffff\n\t"
      "ds_write_b32 v231, v104 offset:256\n\t"
      "ds_write_b32 v213, v213\n\t"
      "s_mov_b64 exec, -1\n\t"
      "v_mfma_f32_16x16x32_bf16 v[56:59], v[160:163], v[184:187], v[56:59]\n\t"
      "ds_read2_b32 v[130:131], v234 offset0:48 offset1:48\n\t"
      "v_mfma_f32_16x16x32_bf16 v[60:63], v[164:167], v[184:187], v[60:63]\n\t"
      "ds_read2_b32 v[128:129], v234 offset0:32 offset1:32\n\t"
      "v_mfma_f32_16x16x32_bf16 v[64:67], v[184:187], v[168:171], v[64:67]\n\t"
      "s_waitcnt lgkmcnt(0)\n\t"
      "v_mfma_f32_16x16x32_bf16 v[68:71], v[184:187], v[172:175], v[68:71]\n\t"
      "ds_read_b32 v226, v232\n\t"
      "ds_read_b128 v[116:119], v228 offset:128\n\t"
      "v_mfma_f32_16x16x32_bf16 v[32:35], v[160:163], v[180:183], v[32:35]\n\t"
      "ds_read_b128 v[120:123], v228 offset:192\n\t"
      "v_mfma_f32_16x16x32_bf16 v[36:39], v[164:167], v[180:183], v[36:39]\n\t"
      "ds_read2_b32 v[250:251], v229 offset0:64 offset1:64\n\t"
      "v_mfma_f32_16x16x32_bf16 v[40:43], v[180:183], v[168:171], v[40:43]\n\t"
      "ds_read2_b32 v[252:253], v229 offset0:80 offset1:80\n\t"
      "v_mfma_f32_16x16x32_bf16 v[44:47], v[180:183], v[172:175], v[44:47]\n\t"
      "ds_read_b128 v[124:127], v230 offset:128\n\t"
      "v_mfma_f32_16x16x32_bf16 v[8:11], v[160:163], v[176:179], v[8:11]\n\t"
      "ds_read2_b32 v[224:225], v231 offset0:64 offset1:64\n\t"
      "v_mfma_f32_16x16x32_bf16 v[12:15], v[164:167], v[176:179], v[12:15]\n\t"
      "v_mfma_f32_16x16x32_bf16 v[16:19], v[176:179], v[168:171], v[16:19]\n\t"
      "s_waitcnt lgkmcnt(6)\n\t"
      "v_min_u32_e32 v226, v226, v212\n\t"
      "v_cmp_eq_u32_e32 vcc, 0, v226\n\t"
      "v_mfma_f32_16x16x32_bf16 v[20:23], v[176:179], v[172:175], v[20:23]\n\t"
      "s_branch LOOPX_%=\n\t"
      "XAR_%=:\n\t"
      "s_mov_b32 s51, 0\n\t"
      "XAL_%=:\n\t"
      "s_add_u32 s51, s51, 1\n\t"
      "s_cmp_lt_u32 s51, 262144\n\t"
      "s_cbranch_scc0 XAX_%=\n\t"
      "s_waitcnt lgkmcnt(0)\n\t"
      "ds_read_b32 v212, v215\n\t"
      "ds_read2_b32 v[134:135], v234 offset0:80 offset1:80\n\t"
      "ds_read_b128 v[148:151], v233 offset:320\n\t"
      "ds_read2_b32 v[248:249], v234 offset0:0 offset1:0\n\t"
      "ds_read_b128 v[196:199], v233\n\t"
      "ds_read2_b32 v[132:133], v234 offset0:64 offset1:64\n\t"
      "ds_read2_b32 v[130:131], v234 offset0:48 offset1:48\n\t"
      "ds_read2_b32 v[128:129], v234 offset0:32 offset1:32\n\t"
      "s_waitcnt lgkmcnt(0)\n\t"
      "ds_read_b32 v226, v232\n\t"
      "ds_read_b128 v[116:119], v228 offset:128\n\t"
      "ds_read_b128 v[120:123], v228 offset:192\n\t"
      "ds_read2_b32 v[250:251], v229 offset0:64 offset1:64\n\t"
      "ds_read2_b32 v[252:253], v229 offset0:80 offset1:80\n\t"
      "ds_read_b128 v[124:127], v230 offset:128\n\t"
      "ds_read2_b32 v[224:225], v231 offset0:64 offset1:64\n\t"
      "s_waitcnt lgkmcnt(0)\n\t"
      "v_min_u32_e32 v226, v226, v212\n\t"
      "v_cmp_eq_u32_e32 vcc, 0, v226\n\t"
      "s_cbranch_vccnz XAL_%=\n\t"
      "XAX_%=:\n\t"
      "s_branch XAB_%=\n\t"
      "XBR_%=:\n\t"
      "s_mov_b32 s51, 0\n\t"
      "XBL_%=:\n\t"
      "s_add_u32 s51, s51, 1\n\t"
      "s_cmp_lt_u32 s51, 262144\n\t"
      "s_cbranch_scc0 XBX_%=\n\t"
      "s_waitcnt lgkmcnt(0)\n\t"
      "ds_read_b32 v226, v232 offset:4\n\t"
      "ds_read_b128 v[152:155], v236\n\t"
      "ds_read_b64 v[156:157], v237 offset:1024\n\t"
      "ds_read_b64 v[166:167], v237 offset:1536\n\t"
      "s_waitcnt lgkmcnt(0)\n\t"
      "v_cmp_eq_u32_e32 vcc, 0, v226\n\t"
      "s_cbranch_vccnz XBL_%=\n\t"
      "XBX_%=:\n\t"
      "s_branch XBB_%=\n\t"
      "CEND_%=:\n\t"
      : "+{s[4:11]}"(s), "+{s12}"(hint), "+{s13}"(chk), "+{s[14:15]}"(tw), "+{s16}"(lw), "+{v222}"(vn), "+{v[224:239]}"(v), "+{v254}"(vk1)
      :
      : "memory", "vcc", "scc",
        "v0", "v1", "v2", "v3", "v4", "v5", "v6", "v7", "v8", "v9", "v10", "v11", "v12", "v13", "v14", "v15", "v16",
        "v17", "v18", "v19", "v20", "v21", "v22", "v23", "v24", "v25", "v26", "v27", "v28", "v29", "v30", "v31", "v32",
        "v33", "v34", "v35", "v36", "v37", "v38", "v39", "v40", "v41", "v42", "v43", "v44", "v45", "v46", "v47", "v48",
        "v49", "v50", "v51", "v52", "v53", "v54", "v55", "v56", "v57", "v58", "v59", "v60", "v61", "v62", "v63", "v64",
        "v65", "v66", "v67", "v68", "v69", "v70", "v71", "v72", "v73", "v74", "v75", "v76", "v77", "v78", "v79", "v80",
        "v81", "v82", "v83", "v84", "v85", "v86", "v87", "v88", "v89", "v90", "v91", "v92", "v93", "v94", "v95", "v96",
        "v97", "v98", "v99", "v100", "v101", "v102", "v103", "v104", "v105", "v106", "v107", "v108", "v109", "v110",
        "v111", "v112", "v113", "v114", "v115", "v116", "v117", "v118", "v119", "v120", "v121", "v122", "v123", "v124",
        "v125", "v126", "v127", "v128", "v129", "v130", "v131", "v132", "v133", "v134", "v135", "v136", "v137", "v138",
        "v139", "v140", "v141", "v142", "v143", "v144", "v145", "v146", "v147", "v148", "v149", "v150", "v151", "v152",
        "v153", "v154", "v155", "v156", "v157", "v158", "v159", "v160", "v161", "v162", "v163", "v164", "v165", "v166",
        "v167", "v168", "v169", "v170", "v171", "v172", "v173", "v174", "v175", "v176", "v177", "v178", "v179", "v180",
        "v181", "v182", "v183", "v184", "v185", "v186", "v187", "v188", "v189", "v190", "v191", "v192", "v193", "v194",
        "v195", "v196", "v197", "v198", "v199", "v200", "v201", "v202", "v203", "v204", "v205", "v206", "v207", "v208",
        "v209", "v210", "v211", "v212", "v213", "v214", "v215", "v216", "v217", "v218", "v219", "v220", "v221", "v223",
        "v240", "v241", "v242", "v243", "v244", "v245", "v246", "v247", "v248", "v249", "v250", "v251", "v252", "v253",
        "s36", "s37", "s38", "s39", "s40", "s41", "s42", "s43", "s44", "s45", "s46", "s47", "s48", "s49", "s50", "s51",
        "s52", "s53", "s54", "s55", "s56", "s57", "s58", "s59", "s60", "s61"
    );
    } else {
      LANE_BITS(threadIdx.x & 63);
      u32x8 v;
      u32x16 s;
      v[0] = ((8 * rt + 4 * half + g) * 16 + c) * 16;
      const int hi = -(lane >> 5), m3 = min(c, 2);
      v[1] = (((max(8 * rt + ((g & 1) ? 0 : 4 * half) - 1, 0) * 16 + c) * 16) & ~hi) | ((ST_PW + xcd * 128 + m3 * 16) & hi);
      v[2] = sta + 128 + lane * 4;
      v[3] = ((sta + ((g & 1) ? 0 : 64) + c * 4) & ~hi) | ((A_MK + m3 * 4) & hi);
      v[4] = lr * A_SLOT + A_HALO + lc * 16;
      v[6] = (side * 1024 + (idx * 4 + wave - 4) * 8 + lr) * 3584 + (7 * xp + min(lc, 6)) * 128;
      v[7] = v[6] + (1 - 2 * side) * 1024 * 3584;
      u64 tw = (u64)w1;
      unsigned vn = (((wave ^ (4 * side)) * 256 + (idx & 1) * 128 + lr) * 28 + 7 * xp + min(idx >> 1, 6)) * 128 + ((lc ^ swz_tr(lr)) << 4);
      s[4] = UNI(idx == 15 && xcd < 3 && wave == 4 ? (xcd == 0 ? WS_O : xcd == 1 ? WS_HN + 16 : WS_G + 32) + PROBE : 0);
      v[5] = lane * (8192 + 640);
      put(s, 0, (u64)(x + (rt * 128 * 896 + 128) - (16 * 896 + 1024)));
      put(s, 2, (u64)(wqkv + (hp * 112 + 64 * 2688) - 4096));
      s[5] = wave * A_UNIT;
      put2(s, 6, wave > 4 ? 0xffffffffu : 0xffffff00u, 0u);
      s[8] = A_HALO + (wave - 4) * 512;
      s[9] = A_FL + 32 + 4 * half;
      put(s, 10, (u64)(ws + WS_ST));
    asm volatile(
      "s_waitcnt lgkmcnt(0)\n\ts_nop 4\n\t"
      ".p2align 3\n\t"
      "s_load_dwordx2 s[18:19], s[14:15], 0x20000\n\t"
      "s_sub_u32 s40, s4, 57344\n\t"
      "s_subb_u32 s41, s5, 0\n\t"
      "s_sub_u32 s42, s6, 86016\n\t"
      "s_subb_u32 s43, s7, 0\n\t"
      "v_mov_b32_e32 v0, 0x3f803f80\n\t"
      "v_mov_b32_e32 v1, 0x3f803f80\n\t"
      "v_mov_b32_e32 v2, 0x3f803f80\n\t"
      "v_mov_b32_e32 v3, 0x3f803f80\n\t"
      "s_mov_b64 exec, 0xffffff\n\t"
      "ds_write_b128 v204, v[0:3]\n\t"
      "s_mov_b64 exec, -1\n\t"
      "v_mov_b32_e32 v32, 154624\n\t"
      "v_mov_b32_e32 v33, s13\n\t"
      "v_mov_b32_e32 v36, 0\n\t"
      "v_mov_b32_e32 v37, 0\n\t"
      "v_mov_b32_e32 v38, 0\n\t"
      "v_mov_b32_e32 v39, 0\n\t"
      "ds_write_b128 v32, v[36:39]\n\t"
      "ds_write_b128 v32, v[36:39] offset:16\n\t"
      "ds_write_b128 v32, v[36:39] offset:32\n\t"
      "v_xor_b32_e64 v21, 64, v20\n\t"
      "v_add_u32_e32 v22, 55296, v20\n\t"
      "v_add_u32_e32 v23, 82944, v21\n\t"
      "v_add_u32_e32 v21, 27648, v21\n\t"
      "s_sub_u32 s4, s4, 128\n\t"
      "s_subb_u32 s5, s5, 0\n\t"
      "s_sub_u32 s6, s6, 172032\n\t"
      "s_subb_u32 s7, s7, 0\n\t"
      "s_add_u32 m0, s9, 0\n\t"
      "s_nop 0\n\t"
      "global_load_lds_dwordx4 v195, s[6:7] offset:2048\n\t"
      "global_load_lds_dwordx4 v192, s[4:5]\n\t"
      "global_load_lds_dwordx4 v193, s[4:5] offset:1024\n\t"
      "global_load_lds_dwordx4 v196, s[6:7] offset:3072\n\t"
      "s_add_u32 m0, s9, 4096\n\t"
      "s_nop 0\n\t"
      "global_load_lds_dwordx4 v197, s[6:7]\n\t"
      "s_add_u32 m0, s12, 0\n\t"
      "s_mov_b64 exec, s[10:11]\n\t"
      "global_load_lds_dwordx4 v194, s[4:5]\n\t"
      "s_mov_b64 exec, -1\n\t"
      "s_add_u32 s4, s4, 128\n\t"
      "s_addc_u32 s5, s5, 0\n\t"
      "s_add_u32 s6, s6, 172032\n\t"
      "s_addc_u32 s7, s7, 0\n\t"
      "s_waitcnt lgkmcnt(0)\n\t"
      "s_add_u32 s18, s18, 1\n\t"
      "s_addc_u32 s19, s19, 0\n\t"
      "s_mul_i32 s48, s18, 0x9e3779b9\n\t"
      "s_mul_hi_u32 s17, s18, 0x7f4a7c15\n\t"
      "s_add_u32 s17, s17, s48\n\t"
      "s_mul_i32 s48, s19, 0x7f4a7c15\n\t"
      "s_add_u32 s17, s17, s48\n\t"
      "s_mul_i32 s16, s18, 0x7f4a7c15\n\t"
      "s_add_u32 s16, s16, 0xd192ed03\n\t"
      "s_addc_u32 s17, s17, 0xd1b54a32\n\t"
      "v_mov_b32_e32 v12, s16\n\t"
      "v_mov_b32_e32 v13, s17\n\t"
      "v_mov_b32_e32 v14, s18\n\t"
      "v_mov_b32_e32 v15, s19\n\t"
      "v_mov_b32_e32 v16, 163600\n\t"
      "s_mov_b64 exec, 1\n\t"
      "ds_write_b128 v16, v[12:15]\n\t"
      "s_mov_b64 exec, -1\n\t"
      "s_and_b32 s47, s8, 48\n\t"
      "s_add_u32 s47, s47, 114688\n\t"
      "v_mbcnt_lo_u32_b32 v28, -1, 0\n\t"
      "v_mov_b32_e32 v25, 0\n\t"
      "v_mov_b32_e32 v26, s16\n\t"
      "v_mov_b32_e32 v27, s17\n\t"
      "v_lshl_add_u32 v28, v28, 7, s47\n\t"
      "s_cmp_eq_u32 s8, 0\n\t"
      "s_cbranch_scc1 PRt_%=\n\t"
      "s_add_u32 s52, s14, s8\n\t"
      "s_addc_u32 s53, s15, 0\n\t"
      "s_mov_b32 s54, 0\n\t"
      "s_mov_b64 s[56:57], -1\n\t"
      "s_load_dword s50, s[52:53], 0x1f80\n\t"
      "PRt_%=:\n\t"
      "s_cmp_eq_u32 s8, 0\n\t"
      "s_cbranch_scc1 PRu_%=\n\t"
      "s_mov_b64 exec, 3\n\t"
      "global_store_dword v205, v0, s[52:53] sc1\n\t"
      "s_mov_b64 exec, -1\n\t"
      "PRu_%=:\n\t"
      "s_add_u32 m0, s9, 22528\n\t"
      "s_nop 0\n\t"
      "global_load_lds_dwordx4 v195, s[42:43] offset:2048\n\t"
      "global_load_lds_dwordx4 v192, s[40:41]\n\t"
      "global_load_lds_dwordx4 v193, s[40:41] offset:1024\n\t"
      "global_load_lds_dwordx4 v196, s[42:43] offset:3072\n\t"
      "s_add_u32 m0, s9, 26624\n\t"
      "s_nop 0\n\t"
      "global_load_lds_dwordx4 v197, s[42:43]\n\t"
      "s_add_u32 m0, s9, 43008\n\t"
      "s_nop 0\n\t"
      "global_load_lds_dwordx4 v195, s[6:7] offset:2048\n\t"
      "global_load_lds_dwordx4 v192, s[4:5]\n\t"
      "global_load_lds_dwordx4 v193, s[4:5] offset:1024\n\t"
      "global_load_lds_dwordx4 v196, s[6:7] offset:3072\n\t"
      "s_add_u32 m0, s9, 47104\n\t"
      "s_nop 0\n\t"
      "global_load_lds_dwordx4 v197, s[6:7]\n\t"
      "s_add_u32 m0, s12, 43008\n\t"
      "s_mov_b64 exec, s[10:11]\n\t"
      "global_load_lds_dwordx4 v194, s[4:5]\n\t"
      "s_mov_b64 exec, -1\n\t"
      "s_waitcnt vmcnt(11) lgkmcnt(0)\n\t"
      "s_barrier\n\t"
      "s_cmp_eq_u32 s8, 0\n\t"
      "s_cbranch_scc1 PRC0_%=\n\t"
      "s_mov_b32 s55, 8832\n\t"
      "s_call_b64 s[44:45], PRB_%=\n\t"
      "PRC0_%=:\n\t"
      "s_add_u32 s4, s4, 128\n\t"
      "s_addc_u32 s5, s5, 0\n\t"
      "s_add_u32 s6, s6, 172032\n\t"
      "s_addc_u32 s7, s7, 0\n\t"
      "s_add_u32 s40, s40, 128\n\t"
      "s_addc_u32 s41, s41, 0\n\t"
      "s_add_u32 s42, s42, 172032\n\t"
      "s_addc_u32 s43, s43, 0\n\t"
      "s_add_u32 m0, s9, 65536\n\t"
      "s_nop 0\n\t"
      "global_load_lds_dwordx4 v195, s[42:43] offset:2048\n\t"
      "global_load_lds_dwordx4 v192, s[40:41]\n\t"
      "global_load_lds_dwordx4 v193, s[40:41] offset:1024\n\t"
      "global_load_lds_dwordx4 v196, s[42:43] offset:3072\n\t"
      "s_add_u32 m0, s9, 69632\n\t"
      "s_nop 0\n\t"
      "global_load_lds_dwordx4 v197, s[42:43]\n\t"
      "s_add_u32 m0, s9, 86016\n\t"
      "s_nop 0\n\t"
      "global_load_lds_dwordx4 v195, s[6:7] offset:2048\n\t"
      "global_load_lds_dwordx4 v192, s[4:5]\n\t"
      "global_load_lds_dwordx4 v193, s[4:5] offset:1024\n\t"
      "global_load_lds_dwordx4 v196, s[6:7] offset:3072\n\t"
      "s_add_u32 m0, s9, 90112\n\t"
      "s_nop 0\n\t"
      "global_load_lds_dwordx4 v197, s[6:7]\n\t"
      "s_add_u32 m0, s12, 86016\n\t"
      "s_mov_b64 exec, s[10:11]\n\t"
      "global_load_lds_dwordx4 v194, s[4:5]\n\t"
      "s_mov_b64 exec, -1\n\t"
      "s_waitcnt vmcnt(11)\n\t"
      "s_barrier\n\t"
      "s_cmp_eq_u32 s8, 0\n\t"
      "s_cbranch_scc1 PRC1_%=\n\t"
      "s_mov_b32 s55, 17664\n\t"
      "s_call_b64 s[44:45], PRB_%=\n\t"
      "PRC1_%=:\n\t"
      "s_add_u32 s4, s4, 128\n\t"
      "s_addc_u32 s5, s5, 0\n\t"
      "s_add_u32 s6, s6, 172032\n\t"
      "s_addc_u32 s7, s7, 0\n\t"
      "s_add_u32 s40, s40, 128\n\t"
      "s_addc_u32 s41, s41, 0\n\t"
      "s_add_u32 s42, s42, 172032\n\t"
      "s_addc_u32 s43, s43, 0\n\t"
      "s_add_u32 m0, s9, 4294946816\n\t"
      "s_nop 0\n\t"
      "global_load_lds_dwordx4 v195, s[42:43] offset:2048\n\t"
      "global_load_lds_dwordx4 v192, s[40:41]\n\t"
      "global_load_lds_dwordx4 v193, s[40:41] offset:1024\n\t"
      "global_load_lds_dwordx4 v196, s[42:43] offset:3072\n\t"
      "s_add_u32 m0, s9, 4294950912\n\t"
      "s_nop 0\n\t"
      "global_load_lds_dwordx4 v197, s[42:43]\n\t"
      "s_add_u32 m0, s9, 0\n\t"
      "s_nop 0\n\t"
      "global_load_lds_dwordx4 v195, s[6:7] offset:2048\n\t"
      "global_load_lds_dwordx4 v192, s[4:5]\n\t"
      "global_load_lds_dwordx4 v193, s[4:5] offset:1024\n\t"
      "global_load_lds_dwordx4 v196, s[6:7] offset:3072\n\t"
      "s_add_u32 m0, s9, 4096\n\t"
      "s_nop 0\n\t"
      "global_load_lds_dwordx4 v197, s[6:7]\n\t"
      "s_add_u32 m0, s12, 0\n\t"
      "s_mov_b64 exec, s[10:11]\n\t"
      "global_load_lds_dwordx4 v194, s[4:5]\n\t"
      "s_mov_b64 exec, -1\n\t"
      "s_cmp_eq_u32 s8, 0\n\t"
      "s_cbranch_scc1 PRS_%=\n\t"
      "s_cmp_lt_u32 s57, s56\n\t"
      "s_cselect_b32 s46, 0x2000, 0\n\t"
      "v_mov_b32_e32 v24, s46\n\t"
      "v_xor_b32_e32 v26, s46, v26\n\t"
      "s_mov_b64 exec, 0xff\n\t"
      "global_store_dwordx4 v28, v[24:27], s[14:15] sc1\n\t"
      "s_mov_b64 exec, -1\n\t"
      "PRS_%=:\n\t"
      "s_waitcnt vmcnt(11)\n\t"
      "s_barrier\n\t"
      "s_add_u32 s4, s4, 128\n\t"
      "s_addc_u32 s5, s5, 0\n\t"
      "s_add_u32 s6, s6, 172032\n\t"
      "s_addc_u32 s7, s7, 0\n\t"
      "s_add_u32 s40, s40, 128\n\t"
      "s_addc_u32 s41, s41, 0\n\t"
      "s_add_u32 s42, s42, 172032\n\t"
      "s_addc_u32 s43, s43, 0\n\t"
      "s_add_u32 m0, s9, 22528\n\t"
      "s_nop 0\n\t"
      "global_load_lds_dwordx4 v195, s[42:43] offset:2048\n\t"
      "global_load_lds_dwordx4 v192, s[40:41]\n\t"
      "global_load_lds_dwordx4 v193, s[40:41] offset:1024\n\t"
      "global_load_lds_dwordx4 v196, s[42:43] offset:3072\n\t"
      "s_add_u32 m0, s9, 26624\n\t"
      "s_nop 0\n\t"
      "global_load_lds_dwordx4 v197, s[42:43]\n\t"
      "s_add_u32 m0, s9, 43008\n\t"
      "s_nop 0\n\t"
      "global_load_lds_dwordx4 v195, s[6:7] offset:2048\n\t"
      "global_load_lds_dwordx4 v192, s[4:5]\n\t"
      "global_load_lds_dwordx4 v193, s[4:5] offset:1024\n\t"
      "global_load_lds_dwordx4 v196, s[6:7] offset:3072\n\t"
      "s_add_u32 m0, s9, 47104\n\t"
      "s_nop 0\n\t"
      "global_load_lds_dwordx4 v197, s[6:7]\n\t"
      "s_add_u32 m0, s12, 43008\n\t"
      "s_mov_b64 exec, s[10:11]\n\t"
      "global_load_lds_dwordx4 v194, s[4:5]\n\t"
      "s_mov_b64 exec, -1\n\t"
      "s_waitcnt vmcnt(11)\n\t"
      "s_barrier\n\t"
      "s_add_u32 s4, s4, 128\n\t"
      "s_addc_u32 s5, s5, 0\n\t"
      "s_add_u32 s6, s6, 172032\n\t"
      "s_addc_u32 s7, s7, 0\n\t"
      "s_add_u32 s40, s40, 128\n\t"
      "s_addc_u32 s41, s41, 0\n\t"
      "s_add_u32 s42, s42, 172032\n\t"
      "s_addc_u32 s43, s43, 0\n\t"
      "s_add_u32 m0, s9, 65536\n\t"
      "s_nop 0\n\t"
      "global_load_lds_dwordx4 v195, s[42:43] offset:2048\n\t"
      "global_load_lds_dwordx4 v192, s[40:41]\n\t"
      "global_load_lds_dwordx4 v193, s[40:41] offset:1024\n\t"
      "global_load_lds_dwordx4 v196, s[42:43] offset:3072\n\t"
      "s_add_u32 m0, s9, 69632\n\t"
      "s_nop 0\n\t"
      "global_load_lds_dwordx4 v197, s[42:43]\n\t"
      "s_add_u32 m0, s9, 86016\n\t"
      "s_nop 0\n\t"
      "global_load_lds_dwordx4 v195, s[6:7] offset:2048\n\t"
      "global_load_lds_dwordx4 v192, s[4:5]\n\t"
      "global_load_lds_dwordx4 v193, s[4:5] offset:1024\n\t"
      "global_load_lds_dwordx4 v196, s[6:7] offset:3072\n\t"
      "s_add_u32 m0, s9, 90112\n\t"
      "s_nop 0\n\t"
      "global_load_lds_dwordx4 v197, s[6:7]\n\t"
      "s_add_u32 m0, s12, 86016\n\t"
      "s_mov_b64 exec, s[10:11]\n\t"
      "global_load_lds_dwordx4 v194, s[4:5]\n\t"
      "s_mov_b64 exec, -1\n\t"
      "global_load_dwordx4 v[4:7], v200, s[14:15] sc1\n\t"
      "global_load_dwordx4 v[8:11], v201, s[14:15] sc1\n\t"
      "s_waitcnt vmcnt(13)\n\t"
      "s_barrier\n\t"
      "s_add_u32 s4, s4, 128\n\t"
      "s_addc_u32 s5, s5, 0\n\t"
      "s_add_u32 s6, s6, 172032\n\t"
      "s_addc_u32 s7, s7, 0\n\t"
      "s_add_u32 s40, s40, 128\n\t"
      "s_addc_u32 s41, s41, 0\n\t"
      "s_add_u32 s42, s42, 172032\n\t"
      "s_addc_u32 s43, s43, 0\n\t"
      "s_add_u32 m0, s9, 4294946816\n\t"
      "s_nop 0\n\t"
      "global_load_lds_dwordx4 v195, s[42:43] offset:2048\n\t"
      "global_load_lds_dwordx4 v192, s[40:41]\n\t"
      "global_load_lds_dwordx4 v193, s[40:41] offset:1024\n\t"
      "global_load_lds_dwordx4 v196, s[42:43] offset:3072\n\t"
      "s_add_u32 m0, s9, 4294950912\n\t"
      "s_nop 0\n\t"
      "global_load_lds_dwordx4 v197, s[42:43]\n\t"
      "s_add_u32 m0, s9, 0\n\t"
      "s_nop 0\n\t"
      "global_load_lds_dwordx4 v195, s[6:7] offset:2048\n\t"
      "global_load_lds_dwordx4 v192, s[4:5]\n\t"
      "global_load_lds_dwordx4 v193, s[4:5] offset:1024\n\t"
      "global_load_lds_dwordx4 v196, s[6:7] offset:3072\n\t"
      "s_add_u32 m0, s9, 4096\n\t"
      "s_nop 0\n\t"
      "global_load_lds_dwordx4 v197, s[6:7]\n\t"
      "s_add_u32 m0, s12, 0\n\t"
      "s_mov_b64 exec, s[10:11]\n\t"
      "global_load_lds_dwordx4 v194, s[4:5]\n\t"
      "s_mov_b64 exec, -1\n\t"
      "s_waitcnt vmcnt(13)\n\t"
      "s_barrier\n\t"
      "s_mov_b32 m0, 163328\n\t"
      "s_add_u32 s44, s20, 0\n\t"
      "s_addc_u32 s45, s21, 0\n\t"
      "global_load_lds_dword v206, s[44:45]\n\t"
      "s_mov_b32 m0, 163328\n\t"
      "s_add_u32 s44, s20, 1835008\n\t"
      "s_addc_u32 s45, s21, 0\n\t"
      "global_load_lds_dword v206, s[44:45]\n\t"
      "s_waitcnt vmcnt(2)\n\t"
      "s_barrier\n\t"
      "v_xor_b32_e32 v6, v6, v4\n\t"
      "v_xor_b32_e32 v7, v7, v5\n\t"
      "v_xor_b32_e32 v10, v10, v8\n\t"
      "v_xor_b32_e32 v11, v11, v9\n\t"
      "v_cmp_ne_u64_e64 s[48:49], s[16:17], v[6:7]\n\t"
      "v_cmp_ne_u64_e32 vcc, s[16:17], v[10:11]\n\t"
      "s_nop 0\n\t"
      "s_or_b64 vcc, vcc, s[48:49]\n\t"
      "s_cbranch_scc1 GRR_%=\n\t"
      "GRB_%=:\n\t"
      "v_mul_f32_e32 v6, 0x3e8b97f6, v5\n\t"
      "ds_write2_b32 v202, v4, v5 offset0:0 offset1:96\n\t"
      "ds_write2_b32 v203, v8, v9 offset0:0 offset1:96\n\t"
      "ds_write_b32 v202, v6 offset:640\n\t"
      "s_mov_b64 exec, 1\n\t"
      "ds_write_b32 v33, v33\n\t"
      "s_mov_b64 exec, -1\n\t"
      "s_sub_u32 s46, s12, 38912\n\t"
      "s_lshl_b32 s46, s46, 3\n\t"
      "s_add_u32 m0, s46, 65536\n\t"
      "s_add_u32 s44, s20, 0\n\t"
      "s_addc_u32 s45, s21, 0\n\t"
      "global_load_lds_dwordx4 v20, s[44:45]\n\t"
      "global_load_lds_dwordx4 v21, s[44:45] offset:1024\n\t"
      "global_load_lds_dwordx4 v22, s[44:45] offset:2048\n\t"
      "global_load_lds_dwordx4 v23, s[44:45] offset:3072\n\t"
      "s_add_u32 m0, s46, 98304\n\t"
      "s_add_u32 s44, s20, 114688\n\t"
      "s_addc_u32 s45, s21, 0\n\t"
      "global_load_lds_dwordx4 v20, s[44:45]\n\t"
      "global_load_lds_dwordx4 v21, s[44:45] offset:1024\n\t"
      "global_load_lds_dwordx4 v22, s[44:45] offset:2048\n\t"
      "global_load_lds_dwordx4 v23, s[44:45] offset:3072\n\t"
      "s_waitcnt lgkmcnt(0)\n\t"
      "s_branch HEND_%=\n\t"
      ".p2align 6\n\t"
      "PRB_%=:\n\t"
      "s_waitcnt lgkmcnt(0)\n\t"
      "s_memtime s[58:59]\n\t"
      "s_waitcnt lgkmcnt(0)\n\t"
      "s_load_dword s50, s[52:53], s54\n\t"
      "s_waitcnt lgkmcnt(0)\n\t"
      "s_memtime s[46:47]\n\t"
      "s_add_u32 s54, s54, 8832\n\t"
      "s_waitcnt lgkmcnt(0)\n\t"
      "s_sub_u32 s46, s46, s58\n\t"
      "s_min_u32 s46, s56, s46\n\t"
      "s_mov_b32 s56, s57\n\t"
      "s_mov_b32 s57, s46\n\t"
      "s_cmp_lt_u32 s54, s55\n\t"
      "s_cbranch_scc1 PRB_%=\n\t"
      "s_setpc_b64 s[44:45]\n\t"
      "GRR_%=:\n\t"
      "s_mov_b32 s51, 0\n\t"
      "GRL_%=:\n\t"
      "s_add_u32 s51, s51, 1\n\t"
      "s_cmp_lt_u32 s51, 262144\n\t"
      "s_cbranch_scc0 GRX_%=\n\t"
      "s_sleep 2\n\t"
      "global_load_dwordx4 v[4:7], v200, s[14:15] sc1\n\t"
      "global_load_dwordx4 v[8:11], v201, s[14:15] sc1\n\t"
      "s_waitcnt vmcnt(0)\n\t"
      "v_xor_b32_e32 v6, v6, v4\n\t"
      "v_xor_b32_e32 v7, v7, v5\n\t"
      "v_xor_b32_e32 v10, v10, v8\n\t"
      "v_xor_b32_e32 v11, v11, v9\n\t"
      "v_cmp_ne_u64_e64 s[48:49], s[16:17], v[6:7]\n\t"
      "v_cmp_ne_u64_e32 vcc, s[16:17], v[10:11]\n\t"
      "s_nop 0\n\t"
      "s_or_b64 vcc, vcc, s[48:49]\n\t"
      "s_cbranch_scc1 GRL_%=\n\t"
      "GRX_%=:\n\t"
      "s_mov_b32 exec_lo, 0\n\t"
      "v_and_b32_e32 v8, 0x2000, v8\n\t"
      "s_mov_b32 exec_lo, -1\n\t"
      "s_branch GRB_%=\n\t"
      "HEND_%=:\n\t"
      : "+{s[4:19]}"(s), "+{s[20:21]}"(tw), "+{v[192:199]}"(f), "+{v[200:207]}"(v), "+{v20}"(vn)
      :
      : "memory", "vcc", "scc",
        "v0", "v1", "v2", "v3", "v4", "v5", "v6", "v7", "v8", "v9", "v10", "v11", "v12", "v13", "v14", "v15", "v16",
        "v17", "v18", "v19", "v21", "v22", "v23", "v24", "v25", "v26", "v27", "v28", "v29", "v30", "v31", "v32", "v33",
        "v34", "v35", "v36", "v37", "v38", "v39", "s40", "s41", "s42", "s43", "s44", "s45", "s46", "s47", "s48", "s49",
        "s50", "s51", "s52", "s53", "s54", "s55", "s56", "s57", "s58", "s59"
    );
    }
    PIN();
    LANE_BITS(__builtin_amdgcn_mbcnt_hi(~0u, __builtin_amdgcn_mbcnt_lo(~0u, 0u)));
    const bool has3 = idx < 14;
    const int ft = has3 ? 7 * xp + (idx >> 1) : 0, blk0 = 7 * (2 * side + (idx & 1));
    u32x8 v;
    u32x16 s;
    v[0] = lane * 16;
    const int rot = 4 * (idx & 1), kg = 2 * ft + ((wave >> 1) & 1), ks = wave ^ (4 * side), rn = rot + 2;
    u64 kr = (u64)(7 - rn) | ((u64)rot << 32);
    v[1] = has3 ? lr * 3584 + ((lc ^ swz_tr(lr)) << 4) : SP3 ? (min(lane, 55) + 96 * (idx & 1)) * 4096 + 384 + xp * 1024 : (lr + 8 * side) * 4096 + lc * 128 + xp * 1024;
    v[2] = wave * 4096 + g * 1024 + q4 * 128 + (((2 * (wave & 3) + (p4 >> 1)) ^ swz_tr(8 * g + q4)) << 4) + (p4 & 1) * 8;
    v[3] = wave * 4096 + (wave & 3) * 1024 + ((lane * 16) ^ ((g & 1) << 7));
    const int gg = 2 * (lane >> 5) + (lane & 1);
    v[4] = (wave & 3) * 1024 + (((gg * 16 + ((lane >> 1) & 15)) * 16) ^ ((gg & 1) << 7));
    v[5] = (wave & 1) * 512 + lane * 8;
    v[6] = has3 ? g * 8 : (160 + 32 * side + lr) * 4096 + lc * 128 + xp * 1024;
    v[7] = ST_H3 + part(xp * 16384 + (ks * 16 + c) * 128, side);
    put(s, 0, has3 ? (u64)(w1 + ft * 128 + (ks * 256 + rn * 32) * 3584) : (u64)(w2 + (wave * 224 + (idx & 1) * 16) * 4096));
    put(s, 2, (u64)(ws + WS_HN + unit(blk0, ks, 0)));
    put(s, 4, (u64)(ws + WS_G + unit(blk0, kg / 7, 0) + kg % 7 * 1024));
    put(s, 6, (u64)(b1 + (64 * ft + 16 * (wave & 3)) * 2));
    put(s, 8, (u64)(ws + WS_ST));
    unsigned hint = ST_H4 + side * 8192 + ((idx & 1) * 28 + ft) * 128;
    s[12] = UNI((has3 ? 4 : 0) | (blockIdx.x == 0 && wave == 0 ? 32 : 0) |  (ks == 4 * side + ((xp + XO) & 3) ? 16 : 0));
    s[13] = wave * G_WV;
    asm volatile(
      "s_waitcnt lgkmcnt(0)\n\ts_nop 4\n\t"
      ".p2align 3\n\t"
      "s_bitcmp0_b32 s16, 2\n\t"
      "s_cbranch_scc1 ASP_%=\n\t"
      "s_lshr_b32 s23, s17, 2\n\t"
      "v_mov_b32_e32 v216, 163600\n\t"
      "ds_read_b128 v[216:219], v216\n\t"
      "v_mov_b32_e32 v220, 154756\n\t"
      "ds_read2_b32 v[220:221], v220 offset0:0 offset1:1\n\t"
      "v_xor_b32_e32 v248, 64, v241\n\t"
      "v_add_u32_e32 v249, 55296, v241\n\t"
      "v_add_u32_e32 v250, 82944, v248\n\t"
      "v_add_u32_e32 v248, 27648, v248\n\t"
      "v_xor_b32_e32 v251, 32, v242\n\t"
      "v_xor_b32_e32 v252, 64, v242\n\t"
      "v_xor_b32_e32 v253, 96, v242\n\t"
      "global_load_dwordx2 v[238:239], v246, s[10:11]\n\t"
      "s_waitcnt lgkmcnt(0)\n\t"
      "v_readfirstlane_b32 s14, v216\n\t"
      "v_readfirstlane_b32 s15, v217\n\t"
      "v_readfirstlane_b32 s46, v218\n\t"
      "v_readfirstlane_b32 s47, v219\n\t"
      "v_readfirstlane_b32 s48, v220\n\t"
      "v_readfirstlane_b32 s49, v221\n\t"
      "v_xor_b32_e32 v247, v247, v221\n\t"
      "s_nop 0\n\t"
      "s_xor_b32 s6, s6, s48\n\t"
      "s_xor_b32 s8, s8, s49\n\t"
      "s_xor_b32 s22, s22, s49\n\t"
      "s_add_u32 s52, s6, 0\n\t"
      "s_addc_u32 s53, s7, 0\n\t"
      "s_add_u32 s54, s6, 131072\n\t"
      "s_addc_u32 s55, s7, 0\n\t"
      "s_add_u32 s56, s6, 262144\n\t"
      "s_addc_u32 s57, s7, 0\n\t"
      "s_add_u32 s58, s6, 393216\n\t"
      "s_addc_u32 s59, s7, 0\n\t"
      "s_add_u32 s60, s6, 524288\n\t"
      "s_addc_u32 s61, s7, 0\n\t"
      "s_add_u32 s62, s6, 655360\n\t"
      "s_addc_u32 s63, s7, 0\n\t"
      "s_add_u32 s64, s6, 786432\n\t"
      "s_addc_u32 s65, s7, 0\n\t"
      "s_add_u32 s48, s21, 0\n\t"
      "s_cmp_ge_u32 s48, 8\n\t"
      "s_cselect_b32 s49, 8, 0\n\t"
      "s_sub_u32 s48, s48, s49\n\t"
      "v_lshl_add_u32 v176, s48, 10, v240\n\t"
      "s_add_u32 s48, s21, 1\n\t"
      "s_cmp_ge_u32 s48, 8\n\t"
      "s_cselect_b32 s49, 8, 0\n\t"
      "s_sub_u32 s48, s48, s49\n\t"
      "v_lshl_add_u32 v177, s48, 10, v240\n\t"
      "s_add_u32 s48, s21, 2\n\t"
      "s_cmp_ge_u32 s48, 8\n\t"
      "s_cselect_b32 s49, 8, 0\n\t"
      "s_sub_u32 s48, s48, s49\n\t"
      "v_lshl_add_u32 v178, s48, 10, v240\n\t"
      "s_add_u32 s48, s21, 3\n\t"
      "s_cmp_ge_u32 s48, 8\n\t"
      "s_cselect_b32 s49, 8, 0\n\t"
      "s_sub_u32 s48, s48, s49\n\t"
      "v_lshl_add_u32 v179, s48, 10, v240\n\t"
      "s_add_u32 s48, s21, 4\n\t"
      "s_cmp_ge_u32 s48, 8\n\t"
      "s_cselect_b32 s49, 8, 0\n\t"
      "s_sub_u32 s48, s48, s49\n\t"
      "v_lshl_add_u32 v180, s48, 10, v240\n\t"
      "s_add_u32 s48, s21, 5\n\t"
      "s_cmp_ge_u32 s48, 8\n\t"
      "s_cselect_b32 s49, 8, 0\n\t"
      "s_sub_u32 s48, s48, s49\n\t"
      "v_lshl_add_u32 v181, s48, 10, v240\n\t"
      "s_add_u32 s48, s21, 6\n\t"
      "s_cmp_ge_u32 s48, 8\n\t"
      "s_cselect_b32 s49, 8, 0\n\t"
      "s_sub_u32 s48, s48, s49\n\t"
      "v_lshl_add_u32 v182, s48, 10, v240\n\t"
      "s_add_u32 s48, s21, 7\n\t"
      "s_cmp_ge_u32 s48, 8\n\t"
      "s_cselect_b32 s49, 8, 0\n\t"
      "s_sub_u32 s48, s48, s49\n\t"
      "v_lshl_add_u32 v183, s48, 10, v240\n\t"
      "s_cmp_ge_u32 s17, 65536\n\t"
      "s_cbranch_scc0 BOH_%=\n\t"
      "s_barrier\n\t"
      "BOH_%=:\n\t"
      "s_waitcnt vmcnt(5)\n\t"
      "s_add_u32 m0, s23, 0\n\t"
      "s_nop 0\n\t"
      "global_load_lds_dwordx4 v241, s[4:5]\n\t"
      "global_load_lds_dwordx4 v248, s[4:5] offset:1024\n\t"
      "global_load_lds_dwordx4 v249, s[4:5] offset:2048\n\t"
      "global_load_lds_dwordx4 v250, s[4:5] offset:3072\n\t"
      "s_sub_u32 s20, s20, 1\n\t"
      "s_cselect_b32 s48, -7, 1\n\t"
      "s_cselect_b32 s49, -1, 0\n\t"
      "s_mul_i32 s48, s48, 114688\n\t"
      "s_add_u32 s4, s4, s48\n\t"
      "s_addc_u32 s5, s5, s49\n\t"
      "v_add_u32_e32 v242, 65536, v242\n\t"
      "v_add_u32_e32 v251, 65536, v251\n\t"
      "v_add_u32_e32 v252, 65536, v252\n\t"
      "v_add_u32_e32 v253, 65536, v253\n\t"
      "ds_read_b64_tr_b16 v[0:1], v242\n\t"
      "ds_read_b64_tr_b16 v[2:3], v242 offset:512\n\t"
      "ds_read_b64_tr_b16 v[4:5], v251\n\t"
      "ds_read_b64_tr_b16 v[6:7], v251 offset:512\n\t"
      "ds_read_b64_tr_b16 v[8:9], v252\n\t"
      "ds_read_b64_tr_b16 v[10:11], v252 offset:512\n\t"
      "ds_read_b64_tr_b16 v[12:13], v253\n\t"
      "ds_read_b64_tr_b16 v[14:15], v253 offset:512\n\t"
      "s_waitcnt vmcnt(5)\n\t"
      "s_add_u32 m0, s23, 32768\n\t"
      "s_nop 0\n\t"
      "global_load_lds_dwordx4 v241, s[4:5]\n\t"
      "global_load_lds_dwordx4 v248, s[4:5] offset:1024\n\t"
      "global_load_lds_dwordx4 v249, s[4:5] offset:2048\n\t"
      "global_load_lds_dwordx4 v250, s[4:5] offset:3072\n\t"
      "s_sub_u32 s20, s20, 1\n\t"
      "s_cselect_b32 s48, -7, 1\n\t"
      "s_cselect_b32 s49, -1, 0\n\t"
      "s_mul_i32 s48, s48, 114688\n\t"
      "s_add_u32 s4, s4, s48\n\t"
      "s_addc_u32 s5, s5, s49\n\t"
      "ds_read_b64_tr_b16 v[16:17], v242 offset:32768\n\t"
      "ds_read_b64_tr_b16 v[18:19], v242 offset:33280\n\t"
      "ds_read_b64_tr_b16 v[20:21], v251 offset:32768\n\t"
      "ds_read_b64_tr_b16 v[22:23], v251 offset:33280\n\t"
      "ds_read_b64_tr_b16 v[24:25], v252 offset:32768\n\t"
      "ds_read_b64_tr_b16 v[26:27], v252 offset:33280\n\t"
      "ds_read_b64_tr_b16 v[28:29], v253 offset:32768\n\t"
      "ds_read_b64_tr_b16 v[30:31], v253 offset:33280\n\t"
      "s_waitcnt vmcnt(8)\n\t"
      "v_xor_b32_e32 v164, 1024, v243\n\t"
      "v_xor_b32_e32 v165, 2048, v243\n\t"
      "v_xor_b32_e32 v166, 3072, v243\n\t"
      "v_lshlrev_b32_e32 v236, 16, v238\n\t"
      "v_and_b32_e32 v237, 0xffff0000, v238\n\t"
      "v_lshlrev_b32_e32 v238, 16, v239\n\t"
      "v_and_b32_e32 v239, 0xffff0000, v239\n\t"
      "v_mov_b32_e32 v235, 0xbdd2d3e8\n\t"
      "s_waitcnt vmcnt(4)\n\t"
      "s_waitcnt lgkmcnt(8)\n\t"
      "s_add_u32 m0, s23, 65536\n\t"
      "s_nop 0\n\t"
      "global_load_lds_dwordx4 v241, s[4:5]\n\t"
      "global_load_lds_dwordx4 v248, s[4:5] offset:1024\n\t"
      "global_load_lds_dwordx4 v249, s[4:5] offset:2048\n\t"
      "global_load_lds_dwordx4 v250, s[4:5] offset:3072\n\t"
      "s_sub_u32 s20, s20, 1\n\t"
      "s_cselect_b32 s48, -7, 1\n\t"
      "s_cselect_b32 s49, -1, 0\n\t"
      "s_mul_i32 s48, s48, 114688\n\t"
      "s_add_u32 s4, s4, s48\n\t"
      "s_addc_u32 s5, s5, s49\n\t"
      "v_add_u32_e32 v242, 4294901760, v242\n\t"
      "v_add_u32_e32 v251, 4294901760, v251\n\t"
      "v_add_u32_e32 v252, 4294901760, v252\n\t"
      "v_add_u32_e32 v253, 4294901760, v253\n\t"
      "ds_read_b64_tr_b16 v[32:33], v242\n\t"
      "ds_read_b64_tr_b16 v[34:35], v242 offset:512\n\t"
      "ds_read_b64_tr_b16 v[36:37], v251\n\t"
      "ds_read_b64_tr_b16 v[38:39], v251 offset:512\n\t"
      "ds_read_b64_tr_b16 v[40:41], v252\n\t"
      "ds_read_b64_tr_b16 v[42:43], v252 offset:512\n\t"
      "ds_read_b64_tr_b16 v[44:45], v253\n\t"
      "ds_read_b64_tr_b16 v[46:47], v253 offset:512\n\t"
      "s_waitcnt vmcnt(4)\n\t"
      "s_waitcnt lgkmcnt(8)\n\t"
      "s_add_u32 m0, s23, 98304\n\t"
      "s_nop 0\n\t"
      "global_load_lds_dwordx4 v241, s[4:5]\n\t"
      "global_load_lds_dwordx4 v248, s[4:5] offset:1024\n\t"
      "global_load_lds_dwordx4 v249, s[4:5] offset:2048\n\t"
      "global_load_lds_dwordx4 v250, s[4:5] offset:3072\n\t"
      "s_sub_u32 s20, s20, 1\n\t"
      "s_cselect_b32 s48, -7, 1\n\t"
      "s_cselect_b32 s49, -1, 0\n\t"
      "s_mul_i32 s48, s48, 114688\n\t"
      "s_add_u32 s4, s4, s48\n\t"
      "s_addc_u32 s5, s5, s49\n\t"
      "ds_read_b64_tr_b16 v[48:49], v242 offset:32768\n\t"
      "ds_read_b64_tr_b16 v[50:51], v242 offset:33280\n\t"
      "ds_read_b64_tr_b16 v[52:53], v251 offset:32768\n\t"
      "ds_read_b64_tr_b16 v[54:55], v251 offset:33280\n\t"
      "ds_read_b64_tr_b16 v[56:57], v252 offset:32768\n\t"
      "ds_read_b64_tr_b16 v[58:59], v252 offset:33280\n\t"
      "ds_read_b64_tr_b16 v[60:61], v253 offset:32768\n\t"
      "ds_read_b64_tr_b16 v[62:63], v253 offset:33280\n\t"
      "s_waitcnt vmcnt(4)\n\t"
      "s_waitcnt lgkmcnt(8)\n\t"
      "s_add_u32 m0, s23, 0\n\t"
      "s_nop 0\n\t"
      "global_load_lds_dwordx4 v241, s[4:5]\n\t"
      "global_load_lds_dwordx4 v248, s[4:5] offset:1024\n\t"
      "global_load_lds_dwordx4 v249, s[4:5] offset:2048\n\t"
      "global_load_lds_dwordx4 v250, s[4:5] offset:3072\n\t"
      "s_sub_u32 s20, s20, 1\n\t"
      "s_cselect_b32 s48, -7, 1\n\t"
      "s_cselect_b32 s49, -1, 0\n\t"
      "s_mul_i32 s48, s48, 114688\n\t"
      "s_add_u32 s4, s4, s48\n\t"
      "s_addc_u32 s5, s5, s49\n\t"
      "s_nop 0\n\t"
      "v_add_u32_e32 v242, 65536, v242\n\t"
      "v_add_u32_e32 v251, 65536, v251\n\t"
      "v_add_u32_e32 v252, 65536, v252\n\t"
      "v_add_u32_e32 v253, 65536, v253\n\t"
      "ds_read_b64_tr_b16 v[64:65], v242\n\t"
      "ds_read_b64_tr_b16 v[66:67], v242 offset:512\n\t"
      "ds_read_b64_tr_b16 v[68:69], v251\n\t"
      "ds_read_b64_tr_b16 v[70:71], v251 offset:512\n\t"
      "ds_read_b64_tr_b16 v[72:73], v252\n\t"
      "ds_read_b64_tr_b16 v[74:75], v252 offset:512\n\t"
      "ds_read_b64_tr_b16 v[76:77], v253\n\t"
      "ds_read_b64_tr_b16 v[78:79], v253 offset:512\n\t"
      "s_waitcnt vmcnt(4)\n\t"
      "s_waitcnt lgkmcnt(8)\n\t"
      "s_add_u32 m0, s23, 32768\n\t"
      "s_nop 0\n\t"
      "global_load_lds_dwordx4 v241, s[4:5]\n\t"
      "global_load_lds_dwordx4 v248, s[4:5] offset:1024\n\t"
      "global_load_lds_dwordx4 v249, s[4:5] offset:2048\n\t"
      "global_load_lds_dwordx4 v250, s[4:5] offset:3072\n\t"
      "ds_read_b64_tr_b16 v[80:81], v242 offset:32768\n\t"
      "ds_read_b64_tr_b16 v[82:83], v242 offset:33280\n\t"
      "ds_read_b64_tr_b16 v[84:85], v251 offset:32768\n\t"
      "ds_read_b64_tr_b16 v[86:87], v251 offset:33280\n\t"
      "ds_read_b64_tr_b16 v[88:89], v252 offset:32768\n\t"
      "ds_read_b64_tr_b16 v[90:91], v252 offset:33280\n\t"
      "ds_read_b64_tr_b16 v[92:93], v253 offset:32768\n\t"
      "ds_read_b64_tr_b16 v[94:95], v253 offset:33280\n\t"
      "s_waitcnt vmcnt(4)\n\t"
      "v_add_u32_e32 v242, 4294901760, v242\n\t"
      "v_add_u32_e32 v251, 4294901760, v251\n\t"
      "v_add_u32_e32 v252, 4294901760, v252\n\t"
      "v_add_u32_e32 v253, 4294901760, v253\n\t"
      "ds_read_b64_tr_b16 v[96:97], v242\n\t"
      "ds_read_b64_tr_b16 v[98:99], v242 offset:512\n\t"
      "ds_read_b64_tr_b16 v[100:101], v251\n\t"
      "ds_read_b64_tr_b16 v[102:103], v251 offset:512\n\t"
      "ds_read_b64_tr_b16 v[104:105], v252\n\t"
      "ds_read_b64_tr_b16 v[106:107], v252 offset:512\n\t"
      "ds_read_b64_tr_b16 v[108:109], v253\n\t"
      "ds_read_b64_tr_b16 v[110:111], v253 offset:512\n\t"
      "s_waitcnt vmcnt(0)\n\t"
      "ds_read_b64_tr_b16 v[112:113], v242 offset:32768\n\t"
      "ds_read_b64_tr_b16 v[114:115], v242 offset:33280\n\t"
      "ds_read_b64_tr_b16 v[116:117], v251 offset:32768\n\t"
      "ds_read_b64_tr_b16 v[118:119], v251 offset:33280\n\t"
      "ds_read_b64_tr_b16 v[120:121], v252 offset:32768\n\t"
      "ds_read_b64_tr_b16 v[122:123], v252 offset:33280\n\t"
      "ds_read_b64_tr_b16 v[124:125], v253 offset:32768\n\t"
      "ds_read_b64_tr_b16 v[126:127], v253 offset:33280\n\t"
      "s_waitcnt lgkmcnt(0)\n\t"
      "s_barrier\n\t"
      "s_mov_b32 s51, 0\n\t"
      "s_branch PRE_%=\n\t"
      "PRL_%=:\n\t"
      "s_sleep 4\n\t"
      "s_add_u32 s51, s51, 1\n\t"
      "s_cmp_lt_u32 s51, 262144\n\t"
      "s_cbranch_scc0 PRX_%=\n\t"
      "PRE_%=:\n\t"
      "s_mov_b64 exec, 65535\n\t"
      "s_bitcmp1_b32 s16, 4\n\t"
      "s_cbranch_scc1 PM_%=\n\t"
      "global_load_dwordx2 v[232:233], v247, s[12:13] sc1\n\t"
      "s_waitcnt vmcnt(0)\n\t"
      "PM_%=:\n\t"
      "s_cbranch_scc0 PN_%=\n\t"
      "v_mov_b64_e32 v[232:233], 0\n\t"
      "global_atomic_or_x2 v[232:233], v247, v[232:233], s[12:13] sc0 sc1\n\t"
      "s_waitcnt vmcnt(0)\n\t"
      "PN_%=:\n\t"
      "v_subrev_u32_e32 v232, s14, v232\n\t"
      "v_cmp_ne_u32_e64 s[48:49], s15, v233\n\t"
      "v_subrev_u32_e32 v232, 0, v232\n\t"
      "v_cmp_lt_u32_e32 vcc, 7, v232\n\t"
      "s_mov_b64 exec, -1\n\t"
      "s_or_b64 vcc, vcc, s[48:49]\n\t"
      "s_cbranch_vccnz PRL_%=\n\t"
      "PRX_%=:\n\t"
      "PRY_%=:\n\t"
      "global_load_dwordx4 v[128:131], v176, s[52:53] sc1\n\t"
      "global_load_dwordx4 v[132:135], v177, s[52:53] sc1\n\t"
      "global_load_dwordx4 v[136:139], v178, s[52:53] sc1\n\t"
      "global_load_dwordx4 v[140:143], v179, s[52:53] sc1\n\t"
      "global_load_dwordx4 v[144:147], v180, s[52:53] sc1\n\t"
      "global_load_dwordx4 v[148:151], v181, s[52:53] sc1\n\t"
      "global_load_dwordx4 v[152:155], v182, s[52:53] sc1\n\t"
      "global_load_dwordx4 v[156:159], v183, s[52:53] sc1\n\t"
      "s_nop 7\n\t"
      "s_cmp_ge_u32 s17, 65536\n\t"
      "s_cbranch_scc1 GQ_%=\n\t"
      ".p2align 6\n\t"
      "s_setprio 1\n\t"
      "v_mov_b32_e32 v232, s14\n\t"
      "v_mov_b32_e32 v233, s15\n\t"
      "v_lshl_add_u32 v247, v240, 10, s22\n\t"
      "s_waitcnt vmcnt(7)\n\t"
      "v_max3_u32 v234, v128, v129, v130\n\t"
      "v_max_u32_e32 v234, v234, v131\n\t"
      "v_cmp_eq_u32_e32 vcc, -1, v234\n\t"
      "s_cbranch_vccnz FP0R_%=\n\t"
      "FP0B_%=:\n\t"
      "v_mfma_f32_16x16x32_bf16 v[184:187], v[0:3], v[128:131], 0\n\t"
      "s_waitcnt vmcnt(6)\n\t"
      "v_max3_u32 v234, v132, v133, v134\n\t"
      "v_max_u32_e32 v234, v234, v135\n\t"
      "v_cmp_eq_u32_e32 vcc, -1, v234\n\t"
      "s_cbranch_vccnz FP1R_%=\n\t"
      "FP1B_%=:\n\t"
      "v_mfma_f32_16x16x32_bf16 v[184:187], v[16:19], v[132:135], v[184:187]\n\t"
      "v_mfma_f32_16x16x32_bf16 v[188:191], v[4:7], v[128:131], 0\n\t"
      "v_mfma_f32_16x16x32_bf16 v[188:191], v[20:23], v[132:135], v[188:191]\n\t"
      "v_mfma_f32_16x16x32_bf16 v[192:195], v[8:11], v[128:131], 0\n\t"
      "v_mfma_f32_16x16x32_bf16 v[192:195], v[24:27], v[132:135], v[192:195]\n\t"
      "v_mfma_f32_16x16x32_bf16 v[196:199], v[12:15], v[128:131], 0\n\t"
      "global_load_dwordx4 v[128:131], v176, s[54:55] sc1\n\t"
      "v_mfma_f32_16x16x32_bf16 v[196:199], v[28:31], v[132:135], v[196:199]\n\t"
      "global_load_dwordx4 v[132:135], v177, s[54:55] sc1\n\t"
      "s_waitcnt vmcnt(7)\n\t"
      "v_max3_u32 v234, v136, v137, v138\n\t"
      "v_max_u32_e32 v234, v234, v139\n\t"
      "v_cmp_eq_u32_e32 vcc, -1, v234\n\t"
      "s_cbranch_vccnz FP2R_%=\n\t"
      "FP2B_%=:\n\t"
      "v_mfma_f32_16x16x32_bf16 v[184:187], v[32:35], v[136:139], v[184:187]\n\t"
      "s_waitcnt vmcnt(6)\n\t"
      "v_max3_u32 v234, v140, v141, v142\n\t"
      "v_max_u32_e32 v234, v234, v143\n\t"
      "v_cmp_eq_u32_e32 vcc, -1, v234\n\t"
      "s_cbranch_vccnz FP3R_%=\n\t"
      "FP3B_%=:\n\t"
      "v_mfma_f32_16x16x32_bf16 v[184:187], v[48:51], v[140:143], v[184:187]\n\t"
      "v_mfma_f32_16x16x32_bf16 v[188:191], v[36:39], v[136:139], v[188:191]\n\t"
      "v_mfma_f32_16x16x32_bf16 v[188:191], v[52:55], v[140:143], v[188:191]\n\t"
      "v_mfma_f32_16x16x32_bf16 v[192:195], v[40:43], v[136:139], v[192:195]\n\t"
      "v_mfma_f32_16x16x32_bf16 v[192:195], v[56:59], v[140:143], v[192:195]\n\t"
      "v_mfma_f32_16x16x32_bf16 v[196:199], v[44:47], v[136:139], v[196:199]\n\t"
      "global_load_dwordx4 v[136:139], v178, s[54:55] sc1\n\t"
      "v_mfma_f32_16x16x32_bf16 v[196:199], v[60:63], v[140:143], v[196:199]\n\t"
      "global_load_dwordx4 v[140:143], v179, s[54:55] sc1\n\t"
      "s_waitcnt vmcnt(7)\n\t"
      "v_max3_u32 v234, v144, v145, v146\n\t"
      "v_max_u32_e32 v234, v234, v147\n\t"
      "v_cmp_eq_u32_e32 vcc, -1, v234\n\t"
      "s_cbranch_vccnz FP4R_%=\n\t"
      "FP4B_%=:\n\t"
      "v_mfma_f32_16x16x32_bf16 v[184:187], v[64:67], v[144:147], v[184:187]\n\t"
      "s_waitcnt vmcnt(6)\n\t"
      "v_max3_u32 v234, v148, v149, v150\n\t"
      "v_max_u32_e32 v234, v234, v151\n\t"
      "v_cmp_eq_u32_e32 vcc, -1, v234\n\t"
      "s_cbranch_vccnz FP5R_%=\n\t"
      "FP5B_%=:\n\t"
      "v_mfma_f32_16x16x32_bf16 v[184:187], v[80:83], v[148:151], v[184:187]\n\t"
      "v_mfma_f32_16x16x32_bf16 v[188:191], v[68:71], v[144:147], v[188:191]\n\t"
      "v_mfma_f32_16x16x32_bf16 v[188:191], v[84:87], v[148:151], v[188:191]\n\t"
      "v_mfma_f32_16x16x32_bf16 v[192:195], v[72:75], v[144:147], v[192:195]\n\t"
      "v_mfma_f32_16x16x32_bf16 v[192:195], v[88:91], v[148:151], v[192:195]\n\t"
      "v_mfma_f32_16x16x32_bf16 v[196:199], v[76:79], v[144:147], v[196:199]\n\t"
      "global_load_dwordx4 v[144:147], v180, s[54:55] sc1\n\t"
      "v_mfma_f32_16x16x32_bf16 v[196:199], v[92:95], v[148:151], v[196:199]\n\t"
      "global_load_dwordx4 v[148:151], v181, s[54:55] sc1\n\t"
      "s_waitcnt vmcnt(7)\n\t"
      "v_max3_u32 v234, v152, v153, v154\n\t"
      "v_max_u32_e32 v234, v234, v155\n\t"
      "v_cmp_eq_u32_e32 vcc, -1, v234\n\t"
      "s_cbranch_vccnz FP6R_%=\n\t"
      "FP6B_%=:\n\t"
      "v_mfma_f32_16x16x32_bf16 v[184:187], v[96:99], v[152:155], v[184:187]\n\t"
      "s_waitcnt vmcnt(6)\n\t"
      "v_max3_u32 v234, v156, v157, v158\n\t"
      "v_max_u32_e32 v234, v234, v159\n\t"
      "v_cmp_eq_u32_e32 vcc, -1, v234\n\t"
      "s_cbranch_vccnz FP7R_%=\n\t"
      "FP7B_%=:\n\t"
      "v_mfma_f32_16x16x32_bf16 v[184:187], v[112:115], v[156:159], v[184:187]\n\t"
      "v_mfma_f32_16x16x32_bf16 v[188:191], v[100:103], v[152:155], v[188:191]\n\t"
      "v_mfma_f32_16x16x32_bf16 v[188:191], v[116:119], v[156:159], v[188:191]\n\t"
      "v_mfma_f32_16x16x32_bf16 v[192:195], v[104:107], v[152:155], v[192:195]\n\t"
      "v_mfma_f32_16x16x32_bf16 v[192:195], v[120:123], v[156:159], v[192:195]\n\t"
      "s_nop 3\n\t"
      "ds_write_b128 v243, v[184:187]\n\t"
      "v_mfma_f32_16x16x32_bf16 v[196:199], v[108:111], v[152:155], v[196:199]\n\t"
      "global_load_dwordx4 v[152:155], v182, s[54:55] sc1\n\t"
      "v_mfma_f32_16x16x32_bf16 v[196:199], v[124:127], v[156:159], v[196:199]\n\t"
      "global_load_dwordx4 v[156:159], v183, s[54:55] sc1\n\t"
      "ds_write_b128 v164, v[188:191]\n\t"
      "ds_write_b128 v165, v[192:195]\n\t"
      "s_nop 4\n\t"
      "ds_write_b128 v166, v[196:199]\n\t"
      "s_waitcnt lgkmcnt(0)\n\t"
      "s_barrier\n\t"
      "ds_read_b128 v[216:219], v244\n\t"
      "ds_read_b128 v[220:223], v244 offset:4096\n\t"
      "ds_read_b128 v[224:227], v244 offset:8192\n\t"
      "ds_read_b128 v[228:231], v244 offset:12288\n\t"
      "ds_read_b128 v[184:187], v244 offset:16384\n\t"
      "ds_read_b128 v[188:191], v244 offset:20480\n\t"
      "ds_read_b128 v[192:195], v244 offset:24576\n\t"
      "ds_read_b128 v[196:199], v244 offset:28672\n\t"
      "s_waitcnt lgkmcnt(6)\n\t"
      "v_add_f32_e32 v216, v216, v220\n\t"
      "v_add_f32_e32 v217, v217, v221\n\t"
      "v_add_f32_e32 v218, v218, v222\n\t"
      "v_add_f32_e32 v219, v219, v223\n\t"
      "s_waitcnt lgkmcnt(4)\n\t"
      "v_add_f32_e32 v224, v224, v228\n\t"
      "v_add_f32_e32 v225, v225, v229\n\t"
      "v_add_f32_e32 v226, v226, v230\n\t"
      "v_add_f32_e32 v227, v227, v231\n\t"
      "s_waitcnt lgkmcnt(2)\n\t"
      "v_add_f32_e32 v184, v184, v188\n\t"
      "v_add_f32_e32 v185, v185, v189\n\t"
      "v_add_f32_e32 v186, v186, v190\n\t"
      "v_add_f32_e32 v187, v187, v191\n\t"
      "v_add_f32_e32 v216, v216, v224\n\t"
      "v_add_f32_e32 v217, v217, v225\n\t"
      "v_add_f32_e32 v218, v218, v226\n\t"
      "v_add_f32_e32 v219, v219, v227\n\t"
      "s_waitcnt lgkmcnt(0)\n\t"
      "v_add_f32_e32 v192, v192, v196\n\t"
      "v_add_f32_e32 v193, v193, v197\n\t"
      "v_add_f32_e32 v194, v194, v198\n\t"
      "v_add_f32_e32 v195, v195, v199\n\t"
      "v_add_f32_e32 v184, v184, v192\n\t"
      "v_add_f32_e32 v185, v185, v193\n\t"
      "v_add_f32_e32 v186, v186, v194\n\t"
      "v_add_f32_e32 v187, v187, v195\n\t"
      "v_add_f32_e32 v216, v216, v184\n\t"
      "s_waitcnt vmcnt(7)\n\t"
      "v_mfma_f32_16x16x32_bf16 v[200:203], v[0:3], v[128:131], 0\n\t"
      "v_add_f32_e32 v217, v217, v185\n\t"
      "s_waitcnt vmcnt(6)\n\t"
      "v_mfma_f32_16x16x32_bf16 v[200:203], v[16:19], v[132:135], v[200:203]\n\t"
      "v_add_f32_e32 v218, v218, v186\n\t"
      "v_add_f32_e32 v219, v219, v187\n\t"
      "v_mfma_f32_16x16x32_bf16 v[204:207], v[4:7], v[128:131], 0\n\t"
      "v_mul_f32_e32 v220, v216, v216\n\t"
      "v_mul_f32_e32 v221, v217, v217\n\t"
      "v_mfma_f32_16x16x32_bf16 v[204:207], v[20:23], v[132:135], v[204:207]\n\t"
      "v_mul_f32_e32 v222, v218, v218\n\t"
      "v_mul_f32_e32 v223, v219, v219\n\t"
      "v_mfma_f32_16x16x32_bf16 v[208:211], v[8:11], v[128:131], 0\n\t"
      "v_fmaak_f32 v220, v235, v220, 0xc0135761\n\t"
      "v_fmaak_f32 v221, v235, v221, 0xc0135761\n\t"
      "v_mfma_f32_16x16x32_bf16 v[208:211], v[24:27], v[132:135], v[208:211]\n\t"
      "v_fmaak_f32 v222, v235, v222, 0xc0135761\n\t"
      "v_fmaak_f32 v223, v235, v223, 0xc0135761\n\t"
      "v_mfma_f32_16x16x32_bf16 v[212:215], v[12:15], v[128:131], 0\n\t"
      "global_load_dwordx4 v[128:131], v176, s[56:57] sc1\n\t"
      "v_mul_f32_e32 v220, v216, v220\n\t"
      "v_mfma_f32_16x16x32_bf16 v[212:215], v[28:31], v[132:135], v[212:215]\n\t"
      "global_load_dwordx4 v[132:135], v177, s[56:57] sc1\n\t"
      "s_waitcnt vmcnt(7)\n\t"
      "v_mfma_f32_16x16x32_bf16 v[200:203], v[32:35], v[136:139], v[200:203]\n\t"
      "v_mul_f32_e32 v221, v217, v221\n\t"
      "s_waitcnt vmcnt(6)\n\t"
      "v_mfma_f32_16x16x32_bf16 v[200:203], v[48:51], v[140:143], v[200:203]\n\t"
      "v_mul_f32_e32 v222, v218, v222\n\t"
      "v_mul_f32_e32 v223, v219, v223\n\t"
      "v_mfma_f32_16x16x32_bf16 v[204:207], v[36:39], v[136:139], v[204:207]\n\t"
      "v_exp_f32_e32 v220, v220\n\t"
      "v_mfma_f32_16x16x32_bf16 v[204:207], v[52:55], v[140:143], v[204:207]\n\t"
      "v_exp_f32_e32 v221, v221\n\t"
      "v_mfma_f32_16x16x32_bf16 v[208:211], v[40:43], v[136:139], v[208:211]\n\t"
      "v_exp_f32_e32 v222, v222\n\t"
      "v_mfma_f32_16x16x32_bf16 v[208:211], v[56:59], v[140:143], v[208:211]\n\t"
      "v_exp_f32_e32 v223, v223\n\t"
      "v_mfma_f32_16x16x32_bf16 v[212:215], v[44:47], v[136:139], v[212:215]\n\t"
      "global_load_dwordx4 v[136:139], v178, s[56:57] sc1\n\t"
      "v_add_f32_e32 v220, 1.0, v220\n\t"
      "v_mfma_f32_16x16x32_bf16 v[212:215], v[60:63], v[140:143], v[212:215]\n\t"
      "global_load_dwordx4 v[140:143], v179, s[56:57] sc1\n\t"
      "s_waitcnt vmcnt(7)\n\t"
      "v_mfma_f32_16x16x32_bf16 v[200:203], v[64:67], v[144:147], v[200:203]\n\t"
      "v_add_f32_e32 v221, 1.0, v221\n\t"
      "s_waitcnt vmcnt(6)\n\t"
      "v_mfma_f32_16x16x32_bf16 v[200:203], v[80:83], v[148:151], v[200:203]\n\t"
      "v_add_f32_e32 v222, 1.0, v222\n\t"
      "v_add_f32_e32 v223, 1.0, v223\n\t"
      "v_mfma_f32_16x16x32_bf16 v[204:207], v[68:71], v[144:147], v[204:207]\n\t"
      "v_rcp_f32_e32 v220, v220\n\t"
      "v_mfma_f32_16x16x32_bf16 v[204:207], v[84:87], v[148:151], v[204:207]\n\t"
      "v_rcp_f32_e32 v221, v221\n\t"
      "v_mfma_f32_16x16x32_bf16 v[208:211], v[72:75], v[144:147], v[208:211]\n\t"
      "v_rcp_f32_e32 v222, v222\n\t"
      "v_mfma_f32_16x16x32_bf16 v[208:211], v[88:91], v[148:151], v[208:211]\n\t"
      "v_rcp_f32_e32 v223, v223\n\t"
      "v_mfma_f32_16x16x32_bf16 v[212:215], v[76:79], v[144:147], v[212:215]\n\t"
      "global_load_dwordx4 v[144:147], v180, s[56:57] sc1\n\t"
      "v_mul_f32_e32 v216, v216, v220\n\t"
      "v_mfma_f32_16x16x32_bf16 v[212:215], v[92:95], v[148:151], v[212:215]\n\t"
      "global_load_dwordx4 v[148:151], v181, s[56:57] sc1\n\t"
      "s_waitcnt vmcnt(7)\n\t"
      "v_mfma_f32_16x16x32_bf16 v[200:203], v[96:99], v[152:155], v[200:203]\n\t"
      "v_mul_f32_e32 v217, v217, v221\n\t"
      "s_waitcnt vmcnt(6)\n\t"
      "v_mfma_f32_16x16x32_bf16 v[200:203], v[112:115], v[156:159], v[200:203]\n\t"
      "v_mul_f32_e32 v218, v218, v222\n\t"
      "v_mul_f32_e32 v219, v219, v223\n\t"
      "v_mfma_f32_16x16x32_bf16 v[204:207], v[100:103], v[152:155], v[204:207]\n\t"
      "v_cvt_pk_bf16_f32 v220, v216, v217\n\t"
      "v_cvt_pk_bf16_f32 v221, v218, v219\n\t"
      "v_mfma_f32_16x16x32_bf16 v[204:207], v[116:119], v[156:159], v[204:207]\n\t"
      "s_nop 1\n\t"
      "v_cmp_u_f32_e32 vcc, v200, v200\n\t"
      "v_min_u32_e32 v220, -2, v220\n\t"
      "v_mfma_f32_16x16x32_bf16 v[208:211], v[104:107], v[152:155], v[208:211]\n\t"
      "v_min_u32_e32 v221, -2, v221\n\t"
      "global_store_dwordx2 v245, v[220:221], s[8:9] sc1\n\t"
      "s_add_u32 s8, s8, 131072\n\t"
      "s_addc_u32 s9, s9, 0\n\t"
      "v_mfma_f32_16x16x32_bf16 v[208:211], v[120:123], v[156:159], v[208:211]\n\t"
      "s_cbranch_vccnz NP1R_%=\n\t"
      "NP1B_%=:\n\t"
      "ds_write_b128 v243, v[200:203] offset:32768\n\t"
      "v_mfma_f32_16x16x32_bf16 v[212:215], v[108:111], v[152:155], v[212:215]\n\t"
      "global_load_dwordx4 v[152:155], v182, s[56:57] sc1\n\t"
      "v_mfma_f32_16x16x32_bf16 v[212:215], v[124:127], v[156:159], v[212:215]\n\t"
      "global_load_dwordx4 v[156:159], v183, s[56:57] sc1\n\t"
      "ds_write_b128 v164, v[204:207] offset:32768\n\t"
      "s_nop 0\n\t"
      "ds_write_b128 v165, v[208:211] offset:32768\n\t"
      "s_nop 3\n\t"
      "ds_write_b128 v166, v[212:215] offset:32768\n\t"
      "s_waitcnt lgkmcnt(0)\n\t"
      "s_barrier\n\t"
      "ds_read_b128 v[216:219], v244 offset:32768\n\t"
      "ds_read_b128 v[220:223], v244 offset:36864\n\t"
      "ds_read_b128 v[224:227], v244 offset:40960\n\t"
      "ds_read_b128 v[228:231], v244 offset:45056\n\t"
      "ds_read_b128 v[200:203], v244 offset:49152\n\t"
      "ds_read_b128 v[204:207], v244 offset:53248\n\t"
      "ds_read_b128 v[208:211], v244 offset:57344\n\t"
      "ds_read_b128 v[212:215], v244 offset:61440\n\t"
      "s_waitcnt lgkmcnt(6)\n\t"
      "v_add_f32_e32 v216, v216, v220\n\t"
      "v_add_f32_e32 v217, v217, v221\n\t"
      "v_add_f32_e32 v218, v218, v222\n\t"
      "v_add_f32_e32 v219, v219, v223\n\t"
      "s_waitcnt lgkmcnt(4)\n\t"
      "v_add_f32_e32 v224, v224, v228\n\t"
      "v_add_f32_e32 v225, v225, v229\n\t"
      "v_add_f32_e32 v226, v226, v230\n\t"
      "v_add_f32_e32 v227, v227, v231\n\t"
      "s_waitcnt lgkmcnt(2)\n\t"
      "v_add_f32_e32 v200, v200, v204\n\t"
      "v_add_f32_e32 v201, v201, v205\n\t"
      "v_add_f32_e32 v202, v202, v206\n\t"
      "v_add_f32_e32 v203, v203, v207\n\t"
      "v_add_f32_e32 v216, v216, v224\n\t"
      "v_add_f32_e32 v217, v217, v225\n\t"
      "v_add_f32_e32 v218, v218, v226\n\t"
      "v_add_f32_e32 v219, v219, v227\n\t"
      "s_waitcnt lgkmcnt(0)\n\t"
      "v_add_f32_e32 v208, v208, v212\n\t"
      "v_add_f32_e32 v209, v209, v213\n\t"
      "v_add_f32_e32 v210, v210, v214\n\t"
      "v_add_f32_e32 v211, v211, v215\n\t"
      "v_add_f32_e32 v200, v200, v208\n\t"
      "v_add_f32_e32 v201, v201, v209\n\t"
      "v_add_f32_e32 v202, v202, v210\n\t"
      "v_add_f32_e32 v203, v203, v211\n\t"
      "v_add_f32_e32 v216, v216, v200\n\t"
      "s_waitcnt vmcnt(8)\n\t"
      "v_mfma_f32_16x16x32_bf16 v[184:187], v[0:3], v[128:131], 0\n\t"
      "v_add_f32_e32 v217, v217, v201\n\t"
      "s_cmp_eq_u32 s17, 0\n\t"
      "s_cbranch_scc0 HI0_%=\n\t"
      "s_mov_b64 exec, 0xf\n\t"
      "global_store_dwordx2 v247, v[232:233], s[12:13] sc1\n\t"
      "s_mov_b64 exec, -1\n\t"
      "s_nop 0\n\t"
      "v_add_u32_e32 v232, 6, v232\n\t"
      "HI0_%=:\n\t"
      "s_waitcnt vmcnt(7)\n\t"
      "v_mfma_f32_16x16x32_bf16 v[184:187], v[16:19], v[132:135], v[184:187]\n\t"
      "v_add_f32_e32 v218, v218, v202\n\t"
      "v_add_f32_e32 v219, v219, v203\n\t"
      "v_mfma_f32_16x16x32_bf16 v[188:191], v[4:7], v[128:131], 0\n\t"
      "v_mul_f32_e32 v220, v216, v216\n\t"
      "v_mul_f32_e32 v221, v217, v217\n\t"
      "v_mfma_f32_16x16x32_bf16 v[188:191], v[20:23], v[132:135], v[188:191]\n\t"
      "v_mul_f32_e32 v222, v218, v218\n\t"
      "v_mul_f32_e32 v223, v219, v219\n\t"
      "v_mfma_f32_16x16x32_bf16 v[192:195], v[8:11], v[128:131], 0\n\t"
      "v_fmaak_f32 v220, v235, v220, 0xc0135761\n\t"
      "v_fmaak_f32 v221, v235, v221, 0xc0135761\n\t"
      "v_mfma_f32_16x16x32_bf16 v[192:195], v[24:27], v[132:135], v[192:195]\n\t"
      "v_fmaak_f32 v222, v235, v222, 0xc0135761\n\t"
      "v_fmaak_f32 v223, v235, v223, 0xc0135761\n\t"
      "v_mfma_f32_16x16x32_bf16 v[196:199], v[12:15], v[128:131], 0\n\t"
      "global_load_dwordx4 v[128:131], v176, s[58:59] sc1\n\t"
      "v_mul_f32_e32 v220, v216, v220\n\t"
      "v_mfma_f32_16x16x32_bf16 v[196:199], v[28:31], v[132:135], v[196:199]\n\t"
      "global_load_dwordx4 v[132:135], v177, s[58:59] sc1\n\t"
      "s_waitcnt vmcnt(8)\n\t"
      "v_mfma_f32_16x16x32_bf16 v[184:187], v[32:35], v[136:139], v[184:187]\n\t"
      "v_mul_f32_e32 v221, v217, v221\n\t"
      "s_waitcnt vmcnt(7)\n\t"
      "v_mfma_f32_16x16x32_bf16 v[184:187], v[48:51], v[140:143], v[184:187]\n\t"
      "v_mul_f32_e32 v222, v218, v222\n\t"
      "v_mul_f32_e32 v223, v219, v223\n\t"
      "v_mfma_f32_16x16x32_bf16 v[188:191], v[36:39], v[136:139], v[188:191]\n\t"
      "v_exp_f32_e32 v220, v220\n\t"
      "v_mfma_f32_16x16x32_bf16 v[188:191], v[52:55], v[140:143], v[188:191]\n\t"
      "v_exp_f32_e32 v221, v221\n\t"
      "v_mfma_f32_16x16x32_bf16 v[192:195], v[40:43], v[136:139], v[192:195]\n\t"
      "v_exp_f32_e32 v222, v222\n\t"
      "v_mfma_f32_16x16x32_bf16 v[192:195], v[56:59], v[140:143], v[192:195]\n\t"
      "v_exp_f32_e32 v223, v223\n\t"
      "v_mfma_f32_16x16x32_bf16 v[196:199], v[44:47], v[136:139], v[196:199]\n\t"
      "global_load_dwordx4 v[136:139], v178, s[58:59] sc1\n\t"
      "v_add_f32_e32 v220, 1.0, v220\n\t"
      "v_mfma_f32_16x16x32_bf16 v[196:199], v[60:63], v[140:143], v[196:199]\n\t"
      "global_load_dwordx4 v[140:143], v179, s[58:59] sc1\n\t"
      "s_waitcnt vmcnt(8)\n\t"
      "v_mfma_f32_16x16x32_bf16 v[184:187], v[64:67], v[144:147], v[184:187]\n\t"
      "v_add_f32_e32 v221, 1.0, v221\n\t"
      "s_waitcnt vmcnt(7)\n\t"
      "v_mfma_f32_16x16x32_bf16 v[184:187], v[80:83], v[148:151], v[184:187]\n\t"
      "v_add_f32_e32 v222, 1.0, v222\n\t"
      "v_add_f32_e32 v223, 1.0, v223\n\t"
      "v_mfma_f32_16x16x32_bf16 v[188:191], v[68:71], v[144:147], v[188:191]\n\t"
      "v_rcp_f32_e32 v220, v220\n\t"
      "v_mfma_f32_16x16x32_bf16 v[188:191], v[84:87], v[148:151], v[188:191]\n\t"
      "v_rcp_f32_e32 v221, v221\n\t"
      "v_mfma_f32_16x16x32_bf16 v[192:195], v[72:75], v[144:147], v[192:195]\n\t"
      "v_rcp_f32_e32 v222, v222\n\t"
      "v_mfma_f32_16x16x32_bf16 v[192:195], v[88:91], v[148:151], v[192:195]\n\t"
      "v_rcp_f32_e32 v223, v223\n\t"
      "v_mfma_f32_16x16x32_bf16 v[196:199], v[76:79], v[144:147], v[196:199]\n\t"
      "global_load_dwordx4 v[144:147], v180, s[58:59] sc1\n\t"
      "v_mul_f32_e32 v216, v216, v220\n\t"
      "v_mfma_f32_16x16x32_bf16 v[196:199], v[92:95], v[148:151], v[196:199]\n\t"
      "global_load_dwordx4 v[148:151], v181, s[58:59] sc1\n\t"
      "s_waitcnt vmcnt(7)\n\t"
      "v_mfma_f32_16x16x32_bf16 v[184:187], v[96:99], v[152:155], v[184:187]\n\t"
      "v_mul_f32_e32 v217, v217, v221\n\t"
      "s_waitcnt vmcnt(6)\n\t"
      "v_mfma_f32_16x16x32_bf16 v[184:187], v[112:115], v[156:159], v[184:187]\n\t"
      "v_mul_f32_e32 v218, v218, v222\n\t"
      "v_mul_f32_e32 v219, v219, v223\n\t"
      "v_mfma_f32_16x16x32_bf16 v[188:191], v[100:103], v[152:155], v[188:191]\n\t"
      "v_cvt_pk_bf16_f32 v220, v216, v217\n\t"
      "v_cvt_pk_bf16_f32 v221, v218, v219\n\t"
      "v_mfma_f32_16x16x32_bf16 v[188:191], v[116:119], v[156:159], v[188:191]\n\t"
      "s_nop 1\n\t"
      "v_cmp_u_f32_e32 vcc, v184, v184\n\t"
      "v_min_u32_e32 v220, -2, v220\n\t"
      "v_mfma_f32_16x16x32_bf16 v[192:195], v[104:107], v[152:155], v[192:195]\n\t"
      "v_min_u32_e32 v221, -2, v221\n\t"
      "global_store_dwordx2 v245, v[220:221], s[8:9] sc1\n\t"
      "s_add_u32 s8, s8, 131072\n\t"
      "s_addc_u32 s9, s9, 0\n\t"
      "v_mfma_f32_16x16x32_bf16 v[192:195], v[120:123], v[156:159], v[192:195]\n\t"
      "s_cbranch_vccnz NP2R_%=\n\t"
      "NP2B_%=:\n\t"
      "ds_write_b128 v243, v[184:187]\n\t"
      "v_mfma_f32_16x16x32_bf16 v[196:199], v[108:111], v[152:155], v[196:199]\n\t"
      "global_load_dwordx4 v[152:155], v182, s[58:59] sc1\n\t"
      "v_mfma_f32_16x16x32_bf16 v[196:199], v[124:127], v[156:159], v[196:199]\n\t"
      "global_load_dwordx4 v[156:159], v183, s[58:59] sc1\n\t"
      "ds_write_b128 v164, v[188:191]\n\t"
      "s_nop 0\n\t"
      "ds_write_b128 v165, v[192:195]\n\t"
      "s_nop 3\n\t"
      "ds_write_b128 v166, v[196:199]\n\t"
      "s_waitcnt lgkmcnt(0)\n\t"
      "s_barrier\n\t"
      "ds_read_b128 v[216:219], v244\n\t"
      "ds_read_b128 v[220:223], v244 offset:4096\n\t"
      "ds_read_b128 v[224:227], v244 offset:8192\n\t"
      "ds_read_b128 v[228:231], v244 offset:12288\n\t"
      "ds_read_b128 v[184:187], v244 offset:16384\n\t"
      "ds_read_b128 v[188:191], v244 offset:20480\n\t"
      "ds_read_b128 v[192:195], v244 offset:24576\n\t"
      "ds_read_b128 v[196:199], v244 offset:28672\n\t"
      "s_waitcnt lgkmcnt(6)\n\t"
      "v_add_f32_e32 v216, v216, v220\n\t"
      "v_add_f32_e32 v217, v217, v221\n\t"
      "v_add_f32_e32 v218, v218, v222\n\t"
      "v_add_f32_e32 v219, v219, v223\n\t"
      "s_waitcnt lgkmcnt(4)\n\t"
      "v_add_f32_e32 v224, v224, v228\n\t"
      "v_add_f32_e32 v225, v225, v229\n\t"
      "v_add_f32_e32 v226, v226, v230\n\t"
      "v_add_f32_e32 v227, v227, v231\n\t"
      "s_waitcnt lgkmcnt(2)\n\t"
      "v_add_f32_e32 v184, v184, v188\n\t"
      "v_add_f32_e32 v185, v185, v189\n\t"
      "v_add_f32_e32 v186, v186, v190\n\t"
      "v_add_f32_e32 v187, v187, v191\n\t"
      "v_add_f32_e32 v216, v216, v224\n\t"
      "v_add_f32_e32 v217, v217, v225\n\t"
      "v_add_f32_e32 v218, v218, v226\n\t"
      "v_add_f32_e32 v219, v219, v227\n\t"
      "s_waitcnt lgkmcnt(0)\n\t"
      "v_add_f32_e32 v192, v192, v196\n\t"
      "v_add_f32_e32 v193, v193, v197\n\t"
      "v_add_f32_e32 v194, v194, v198\n\t"
      "v_add_f32_e32 v195, v195, v199\n\t"
      "v_add_f32_e32 v184, v184, v192\n\t"
      "v_add_f32_e32 v185, v185, v193\n\t"
      "v_add_f32_e32 v186, v186, v194\n\t"
      "v_add_f32_e32 v187, v187, v195\n\t"
      "v_add_f32_e32 v216, v216, v184\n\t"
      "s_waitcnt vmcnt(8)\n\t"
      "v_mfma_f32_16x16x32_bf16 v[200:203], v[0:3], v[128:131], 0\n\t"
      "v_add_f32_e32 v217, v217, v185\n\t"
      "s_waitcnt vmcnt(7)\n\t"
      "v_mfma_f32_16x16x32_bf16 v[200:203], v[16:19], v[132:135], v[200:203]\n\t"
      "v_add_f32_e32 v218, v218, v186\n\t"
      "v_add_f32_e32 v219, v219, v187\n\t"
      "v_mfma_f32_16x16x32_bf16 v[204:207], v[4:7], v[128:131], 0\n\t"
      "v_mul_f32_e32 v220, v216, v216\n\t"
      "v_mul_f32_e32 v221, v217, v217\n\t"
      "v_mfma_f32_16x16x32_bf16 v[204:207], v[20:23], v[132:135], v[204:207]\n\t"
      "v_mul_f32_e32 v222, v218, v218\n\t"
      "v_mul_f32_e32 v223, v219, v219\n\t"
      "v_mfma_f32_16x16x32_bf16 v[208:211], v[8:11], v[128:131], 0\n\t"
      "v_fmaak_f32 v220, v235, v220, 0xc0135761\n\t"
      "v_fmaak_f32 v221, v235, v221, 0xc0135761\n\t"
      "v_mfma_f32_16x16x32_bf16 v[208:211], v[24:27], v[132:135], v[208:211]\n\t"
      "v_fmaak_f32 v222, v235, v222, 0xc0135761\n\t"
      "v_fmaak_f32 v223, v235, v223, 0xc0135761\n\t"
      "v_mfma_f32_16x16x32_bf16 v[212:215], v[12:15], v[128:131], 0\n\t"
      "global_load_dwordx4 v[128:131], v176, s[60:61] sc1\n\t"
      "v_mul_f32_e32 v220, v216, v220\n\t"
      "v_mfma_f32_16x16x32_bf16 v[212:215], v[28:31], v[132:135], v[212:215]\n\t"
      "global_load_dwordx4 v[132:135], v177, s[60:61] sc1\n\t"
      "s_waitcnt vmcnt(8)\n\t"
      "v_mfma_f32_16x16x32_bf16 v[200:203], v[32:35], v[136:139], v[200:203]\n\t"
      "v_mul_f32_e32 v221, v217, v221\n\t"
      "s_waitcnt vmcnt(7)\n\t"
      "v_mfma_f32_16x16x32_bf16 v[200:203], v[48:51], v[140:143], v[200:203]\n\t"
      "v_mul_f32_e32 v222, v218, v222\n\t"
      "v_mul_f32_e32 v223, v219, v223\n\t"
      "v_mfma_f32_16x16x32_bf16 v[204:207], v[36:39], v[136:139], v[204:207]\n\t"
      "v_exp_f32_e32 v220, v220\n\t"
      "v_mfma_f32_16x16x32_bf16 v[204:207], v[52:55], v[140:143], v[204:207]\n\t"
      "v_exp_f32_e32 v221, v221\n\t"
      "v_mfma_f32_16x16x32_bf16 v[208:211], v[40:43], v[136:139], v[208:211]\n\t"
      "v_exp_f32_e32 v222, v222\n\t"
      "v_mfma_f32_16x16x32_bf16 v[208:211], v[56:59], v[140:143], v[208:211]\n\t"
      "v_exp_f32_e32 v223, v223\n\t"
      "v_mfma_f32_16x16x32_bf16 v[212:215], v[44:47], v[136:139], v[212:215]\n\t"
      "global_load_dwordx4 v[136:139], v178, s[60:61] sc1\n\t"
      "v_add_f32_e32 v220, 1.0, v220\n\t"
      "v_mfma_f32_16x16x32_bf16 v[212:215], v[60:63], v[140:143], v[212:215]\n\t"
      "global_load_dwordx4 v[140:143], v179, s[60:61] sc1\n\t"
      "s_waitcnt vmcnt(8)\n\t"
      "v_mfma_f32_16x16x32_bf16 v[200:203], v[64:67], v[144:147], v[200:203]\n\t"
      "v_add_f32_e32 v221, 1.0, v221\n\t"
      "s_waitcnt vmcnt(7)\n\t"
      "v_mfma_f32_16x16x32_bf16 v[200:203], v[80:83], v[148:151], v[200:203]\n\t"
      "v_add_f32_e32 v222, 1.0, v222\n\t"
      "v_add_f32_e32 v223, 1.0, v223\n\t"
      "v_mfma_f32_16x16x32_bf16 v[204:207], v[68:71], v[144:147], v[204:207]\n\t"
      "v_rcp_f32_e32 v220, v220\n\t"
      "v_mfma_f32_16x16x32_bf16 v[204:207], v[84:87], v[148:151], v[204:207]\n\t"
      "v_rcp_f32_e32 v221, v221\n\t"
      "v_mfma_f32_16x16x32_bf16 v[208:211], v[72:75], v[144:147], v[208:211]\n\t"
      "v_rcp_f32_e32 v222, v222\n\t"
      "v_mfma_f32_16x16x32_bf16 v[208:211], v[88:91], v[148:151], v[208:211]\n\t"
      "v_rcp_f32_e32 v223, v223\n\t"
      "v_mfma_f32_16x16x32_bf16 v[212:215], v[76:79], v[144:147], v[212:215]\n\t"
      "global_load_dwordx4 v[144:147], v180, s[60:61] sc1\n\t"
      "v_mul_f32_e32 v216, v216, v220\n\t"
      "v_mfma_f32_16x16x32_bf16 v[212:215], v[92:95], v[148:151], v[212:215]\n\t"
      "global_load_dwordx4 v[148:151], v181, s[60:61] sc1\n\t"
      "s_waitcnt vmcnt(7)\n\t"
      "v_mfma_f32_16x16x32_bf16 v[200:203], v[96:99], v[152:155], v[200:203]\n\t"
      "v_mul_f32_e32 v217, v217, v221\n\t"
      "s_waitcnt vmcnt(6)\n\t"
      "v_mfma_f32_16x16x32_bf16 v[200:203], v[112:115], v[156:159], v[200:203]\n\t"
      "v_mul_f32_e32 v218, v218, v222\n\t"
      "v_mul_f32_e32 v219, v219, v223\n\t"
      "v_mfma_f32_16x16x32_bf16 v[204:207], v[100:103], v[152:155], v[204:207]\n\t"
      "v_cvt_pk_bf16_f32 v220, v216, v217\n\t"
      "v_cvt_pk_bf16_f32 v221, v218, v219\n\t"
      "v_mfma_f32_16x16x32_bf16 v[204:207], v[116:119], v[156:159], v[204:207]\n\t"
      "s_nop 1\n\t"
      "v_cmp_u_f32_e32 vcc, v200, v200\n\t"
      "v_min_u32_e32 v220, -2, v220\n\t"
      "v_mfma_f32_16x16x32_bf16 v[208:211], v[104:107], v[152:155], v[208:211]\n\t"
      "v_min_u32_e32 v221, -2, v221\n\t"
      "global_store_dwordx2 v245, v[220:221], s[8:9] sc1\n\t"
      "s_add_u32 s8, s8, 131072\n\t"
      "s_addc_u32 s9, s9, 0\n\t"
      "v_mfma_f32_16x16x32_bf16 v[208:211], v[120:123], v[156:159], v[208:211]\n\t"
      "s_cbranch_vccnz NP3R_%=\n\t"
      "NP3B_%=:\n\t"
      "ds_write_b128 v243, v[200:203] offset:32768\n\t"
      "v_mfma_f32_16x16x32_bf16 v[212:215], v[108:111], v[152:155], v[212:215]\n\t"
      "global_load_dwordx4 v[152:155], v182, s[60:61] sc1\n\t"
      "v_mfma_f32_16x16x32_bf16 v[212:215], v[124:127], v[156:159], v[212:215]\n\t"
      "global_load_dwordx4 v[156:159], v183, s[60:61] sc1\n\t"
      "ds_write_b128 v164, v[204:207] offset:32768\n\t"
      "s_nop 0\n\t"
      "ds_write_b128 v165, v[208:211] offset:32768\n\t"
      "s_nop 3\n\t"
      "ds_write_b128 v166, v[212:215] offset:32768\n\t"
      "s_waitcnt lgkmcnt(0)\n\t"
      "s_barrier\n\t"
      "ds_read_b128 v[216:219], v244 offset:32768\n\t"
      "ds_read_b128 v[220:223], v244 offset:36864\n\t"
      "ds_read_b128 v[224:227], v244 offset:40960\n\t"
      "ds_read_b128 v[228:231], v244 offset:45056\n\t"
      "ds_read_b128 v[200:203], v244 offset:49152\n\t"
      "ds_read_b128 v[204:207], v244 offset:53248\n\t"
      "ds_read_b128 v[208:211], v244 offset:57344\n\t"
      "ds_read_b128 v[212:215], v244 offset:61440\n\t"
      "s_waitcnt lgkmcnt(6)\n\t"
      "v_add_f32_e32 v216, v216, v220\n\t"
      "v_add_f32_e32 v217, v217, v221\n\t"
      "v_add_f32_e32 v218, v218, v222\n\t"
      "v_add_f32_e32 v219, v219, v223\n\t"
      "s_waitcnt lgkmcnt(4)\n\t"
      "v_add_f32_e32 v224, v224, v228\n\t"
      "v_add_f32_e32 v225, v225, v229\n\t"
      "v_add_f32_e32 v226, v226, v230\n\t"
      "v_add_f32_e32 v227, v227, v231\n\t"
      "s_waitcnt lgkmcnt(2)\n\t"
      "v_add_f32_e32 v200, v200, v204\n\t"
      "v_add_f32_e32 v201, v201, v205\n\t"
      "v_add_f32_e32 v202, v202, v206\n\t"
      "v_add_f32_e32 v203, v203, v207\n\t"
      "v_add_f32_e32 v216, v216, v224\n\t"
      "v_add_f32_e32 v217, v217, v225\n\t"
      "v_add_f32_e32 v218, v218, v226\n\t"
      "v_add_f32_e32 v219, v219, v227\n\t"
      "s_waitcnt lgkmcnt(0)\n\t"
      "v_add_f32_e32 v208, v208, v212\n\t"
      "v_add_f32_e32 v209, v209, v213\n\t"
      "v_add_f32_e32 v210, v210, v214\n\t"
      "v_add_f32_e32 v211, v211, v215\n\t"
      "v_add_f32_e32 v200, v200, v208\n\t"
      "v_add_f32_e32 v201, v201, v209\n\t"
      "v_add_f32_e32 v202, v202, v210\n\t"
      "v_add_f32_e32 v203, v203, v211\n\t"
      "v_add_f32_e32 v216, v216, v200\n\t"
      "s_waitcnt vmcnt(8)\n\t"
      "v_mfma_f32_16x16x32_bf16 v[184:187], v[0:3], v[128:131], 0\n\t"
      "v_add_f32_e32 v217, v217, v201\n\t"
      "s_waitcnt vmcnt(7)\n\t"
      "v_mfma_f32_16x16x32_bf16 v[184:187], v[16:19], v[132:135], v[184:187]\n\t"
      "v_add_f32_e32 v218, v218, v202\n\t"
      "v_add_f32_e32 v219, v219, v203\n\t"
      "v_mfma_f32_16x16x32_bf16 v[188:191], v[4:7], v[128:131], 0\n\t"
      "v_mul_f32_e32 v220, v216, v216\n\t"
      "v_mul_f32_e32 v221, v217, v217\n\t"
      "v_mfma_f32_16x16x32_bf16 v[188:191], v[20:23], v[132:135], v[188:191]\n\t"
      "v_mul_f32_e32 v222, v218, v218\n\t"
      "v_mul_f32_e32 v223, v219, v219\n\t"
      "v_mfma_f32_16x16x32_bf16 v[192:195], v[8:11], v[128:131], 0\n\t"
      "v_fmaak_f32 v220, v235, v220, 0xc0135761\n\t"
      "v_fmaak_f32 v221, v235, v221, 0xc0135761\n\t"
      "v_mfma_f32_16x16x32_bf16 v[192:195], v[24:27], v[132:135], v[192:195]\n\t"
      "v_fmaak_f32 v222, v235, v222, 0xc0135761\n\t"
      "v_fmaak_f32 v223, v235, v223, 0xc0135761\n\t"
      "v_mfma_f32_16x16x32_bf16 v[196:199], v[12:15], v[128:131], 0\n\t"
      "global_load_dwordx4 v[128:131], v176, s[62:63] sc1\n\t"
      "v_mul_f32_e32 v220, v216, v220\n\t"
      "v_mfma_f32_16x16x32_bf16 v[196:199], v[28:31], v[132:135], v[196:199]\n\t"
      "global_load_dwordx4 v[132:135], v177, s[62:63] sc1\n\t"
      "s_waitcnt vmcnt(8)\n\t"
      "v_mfma_f32_16x16x32_bf16 v[184:187], v[32:35], v[136:139], v[184:187]\n\t"
      "v_mul_f32_e32 v221, v217, v221\n\t"
      "s_waitcnt vmcnt(7)\n\t"
      "v_mfma_f32_16x16x32_bf16 v[184:187], v[48:51], v[140:143], v[184:187]\n\t"
      "v_mul_f32_e32 v222, v218, v222\n\t"
      "v_mul_f32_e32 v223, v219, v223\n\t"
      "v_mfma_f32_16x16x32_bf16 v[188:191], v[36:39], v[136:139], v[188:191]\n\t"
      "v_exp_f32_e32 v220, v220\n\t"
      "v_mfma_f32_16x16x32_bf16 v[188:191], v[52:55], v[140:143], v[188:191]\n\t"
      "v_exp_f32_e32 v221, v221\n\t"
      "v_mfma_f32_16x16x32_bf16 v[192:195], v[40:43], v[136:139], v[192:195]\n\t"
      "v_exp_f32_e32 v222, v222\n\t"
      "v_mfma_f32_16x16x32_bf16 v[192:195], v[56:59], v[140:143], v[192:195]\n\t"
      "v_exp_f32_e32 v223, v223\n\t"
      "v_mfma_f32_16x16x32_bf16 v[196:199], v[44:47], v[136:139], v[196:199]\n\t"
      "global_load_dwordx4 v[136:139], v178, s[62:63] sc1\n\t"
      "v_add_f32_e32 v220, 1.0, v220\n\t"
      "v_mfma_f32_16x16x32_bf16 v[196:199], v[60:63], v[140:143], v[196:199]\n\t"
      "global_load_dwordx4 v[140:143], v179, s[62:63] sc1\n\t"
      "s_waitcnt vmcnt(8)\n\t"
      "v_mfma_f32_16x16x32_bf16 v[184:187], v[64:67], v[144:147], v[184:187]\n\t"
      "v_add_f32_e32 v221, 1.0, v221\n\t"
      "s_waitcnt vmcnt(7)\n\t"
      "v_mfma_f32_16x16x32_bf16 v[184:187], v[80:83], v[148:151], v[184:187]\n\t"
      "v_add_f32_e32 v222, 1.0, v222\n\t"
      "v_add_f32_e32 v223, 1.0, v223\n\t"
      "v_mfma_f32_16x16x32_bf16 v[188:191], v[68:71], v[144:147], v[188:191]\n\t"
      "v_rcp_f32_e32 v220, v220\n\t"
      "v_mfma_f32_16x16x32_bf16 v[188:191], v[84:87], v[148:151], v[188:191]\n\t"
      "v_rcp_f32_e32 v221, v221\n\t"
      "v_mfma_f32_16x16x32_bf16 v[192:195], v[72:75], v[144:147], v[192:195]\n\t"
      "v_rcp_f32_e32 v222, v222\n\t"
      "v_mfma_f32_16x16x32_bf16 v[192:195], v[88:91], v[148:151], v[192:195]\n\t"
      "v_rcp_f32_e32 v223, v223\n\t"
      "v_mfma_f32_16x16x32_bf16 v[196:199], v[76:79], v[144:147], v[196:199]\n\t"
      "global_load_dwordx4 v[144:147], v180, s[62:63] sc1\n\t"
      "v_mul_f32_e32 v216, v216, v220\n\t"
      "v_mfma_f32_16x16x32_bf16 v[196:199], v[92:95], v[148:151], v[196:199]\n\t"
      "global_load_dwordx4 v[148:151], v181, s[62:63] sc1\n\t"
      "s_waitcnt vmcnt(7)\n\t"
      "v_mfma_f32_16x16x32_bf16 v[184:187], v[96:99], v[152:155], v[184:187]\n\t"
      "v_mul_f32_e32 v217, v217, v221\n\t"
      "s_waitcnt vmcnt(6)\n\t"
      "v_mfma_f32_16x16x32_bf16 v[184:187], v[112:115], v[156:159], v[184:187]\n\t"
      "v_mul_f32_e32 v218, v218, v222\n\t"
      "v_mul_f32_e32 v219, v219, v223\n\t"
      "v_mfma_f32_16x16x32_bf16 v[188:191], v[100:103], v[152:155], v[188:191]\n\t"
      "v_cvt_pk_bf16_f32 v220, v216, v217\n\t"
      "v_cvt_pk_bf16_f32 v221, v218, v219\n\t"
      "v_mfma_f32_16x16x32_bf16 v[188:191], v[116:119], v[156:159], v[188:191]\n\t"
      "s_nop 1\n\t"
      "v_cmp_u_f32_e32 vcc, v184, v184\n\t"
      "v_min_u32_e32 v220, -2, v220\n\t"
      "v_mfma_f32_16x16x32_bf16 v[192:195], v[104:107], v[152:155], v[192:195]\n\t"
      "v_min_u32_e32 v221, -2, v221\n\t"
      "global_store_dwordx2 v245, v[220:221], s[8:9] sc1\n\t"
      "s_add_u32 s8, s8, 131072\n\t"
      "s_addc_u32 s9, s9, 0\n\t"
      "v_mfma_f32_16x16x32_bf16 v[192:195], v[120:123], v[156:159], v[192:195]\n\t"
      "s_cbranch_vccnz NP4R_%=\n\t"
      "NP4B_%=:\n\t"
      "ds_write_b128 v243, v[184:187]\n\t"
      "v_mfma_f32_16x16x32_bf16 v[196:199], v[108:111], v[152:155], v[196:199]\n\t"
      "global_load_dwordx4 v[152:155], v182, s[62:63] sc1\n\t"
      "v_mfma_f32_16x16x32_bf16 v[196:199], v[124:127], v[156:159], v[196:199]\n\t"
      "global_load_dwordx4 v[156:159], v183, s[62:63] sc1\n\t"
      "ds_write_b128 v164, v[188:191]\n\t"
      "s_nop 0\n\t"
      "ds_write_b128 v165, v[192:195]\n\t"
      "s_nop 3\n\t"
      "ds_write_b128 v166, v[196:199]\n\t"
      "s_waitcnt lgkmcnt(0)\n\t"
      "s_barrier\n\t"
      "ds_read_b128 v[216:219], v244\n\t"
      "ds_read_b128 v[220:223], v244 offset:4096\n\t"
      "ds_read_b128 v[224:227], v244 offset:8192\n\t"
      "ds_read_b128 v[228:231], v244 offset:12288\n\t"
      "ds_read_b128 v[184:187], v244 offset:16384\n\t"
      "ds_read_b128 v[188:191], v244 offset:20480\n\t"
      "ds_read_b128 v[192:195], v244 offset:24576\n\t"
      "ds_read_b128 v[196:199], v244 offset:28672\n\t"
      "s_waitcnt lgkmcnt(6)\n\t"
      "v_add_f32_e32 v216, v216, v220\n\t"
      "v_add_f32_e32 v217, v217, v221\n\t"
      "v_add_f32_e32 v218, v218, v222\n\t"
      "v_add_f32_e32 v219, v219, v223\n\t"
      "s_waitcnt lgkmcnt(4)\n\t"
      "v_add_f32_e32 v224, v224, v228\n\t"
      "v_add_f32_e32 v225, v225, v229\n\t"
      "v_add_f32_e32 v226, v226, v230\n\t"
      "v_add_f32_e32 v227, v227, v231\n\t"
      "s_waitcnt lgkmcnt(2)\n\t"
      "v_add_f32_e32 v184, v184, v188\n\t"
      "v_add_f32_e32 v185, v185, v189\n\t"
      "v_add_f32_e32 v186, v186, v190\n\t"
      "v_add_f32_e32 v187, v187, v191\n\t"
      "v_add_f32_e32 v216, v216, v224\n\t"
      "v_add_f32_e32 v217, v217, v225\n\t"
      "v_add_f32_e32 v218, v218, v226\n\t"
      "v_add_f32_e32 v219, v219, v227\n\t"
      "s_waitcnt lgkmcnt(0)\n\t"
      "v_add_f32_e32 v192, v192, v196\n\t"
      "v_add_f32_e32 v193, v193, v197\n\t"
      "v_add_f32_e32 v194, v194, v198\n\t"
      "v_add_f32_e32 v195, v195, v199\n\t"
      "v_add_f32_e32 v184, v184, v192\n\t"
      "v_add_f32_e32 v185, v185, v193\n\t"
      "v_add_f32_e32 v186, v186, v194\n\t"
      "v_add_f32_e32 v187, v187, v195\n\t"
      "v_add_f32_e32 v216, v216, v184\n\t"
      "s_waitcnt vmcnt(8)\n\t"
      "v_mfma_f32_16x16x32_bf16 v[200:203], v[0:3], v[128:131], 0\n\t"
      "v_add_f32_e32 v217, v217, v185\n\t"
      "s_waitcnt vmcnt(7)\n\t"
      "v_mfma_f32_16x16x32_bf16 v[200:203], v[16:19], v[132:135], v[200:203]\n\t"
      "v_add_f32_e32 v218, v218, v186\n\t"
      "v_add_f32_e32 v219, v219, v187\n\t"
      "v_mfma_f32_16x16x32_bf16 v[204:207], v[4:7], v[128:131], 0\n\t"
      "v_mul_f32_e32 v220, v216, v216\n\t"
      "v_mul_f32_e32 v221, v217, v217\n\t"
      "v_mfma_f32_16x16x32_bf16 v[204:207], v[20:23], v[132:135], v[204:207]\n\t"
      "v_mul_f32_e32 v222, v218, v218\n\t"
      "v_mul_f32_e32 v223, v219, v219\n\t"
      "v_mfma_f32_16x16x32_bf16 v[208:211], v[8:11], v[128:131], 0\n\t"
      "v_fmaak_f32 v220, v235, v220, 0xc0135761\n\t"
      "v_fmaak_f32 v221, v235, v221, 0xc0135761\n\t"
      "v_mfma_f32_16x16x32_bf16 v[208:211], v[24:27], v[132:135], v[208:211]\n\t"
      "v_fmaak_f32 v222, v235, v222, 0xc0135761\n\t"
      "v_fmaak_f32 v223, v235, v223, 0xc0135761\n\t"
      "v_mfma_f32_16x16x32_bf16 v[212:215], v[12:15], v[128:131], 0\n\t"
      "global_load_dwordx4 v[128:131], v176, s[64:65] sc1\n\t"
      "v_mul_f32_e32 v220, v216, v220\n\t"
      "v_mfma_f32_16x16x32_bf16 v[212:215], v[28:31], v[132:135], v[212:215]\n\t"
      "global_load_dwordx4 v[132:135], v177, s[64:65] sc1\n\t"
      "s_waitcnt vmcnt(8)\n\t"
      "v_mfma_f32_16x16x32_bf16 v[200:203], v[32:35], v[136:139], v[200:203]\n\t"
      "v_mul_f32_e32 v221, v217, v221\n\t"
      "s_waitcnt vmcnt(7)\n\t"
      "v_mfma_f32_16x16x32_bf16 v[200:203], v[48:51], v[140:143], v[200:203]\n\t"
      "v_mul_f32_e32 v222, v218, v222\n\t"
      "v_mul_f32_e32 v223, v219, v223\n\t"
      "v_mfma_f32_16x16x32_bf16 v[204:207], v[36:39], v[136:139], v[204:207]\n\t"
      "v_exp_f32_e32 v220, v220\n\t"
      "v_mfma_f32_16x16x32_bf16 v[204:207], v[52:55], v[140:143], v[204:207]\n\t"
      "v_exp_f32_e32 v221, v221\n\t"
      "v_mfma_f32_16x16x32_bf16 v[208:211], v[40:43], v[136:139], v[208:211]\n\t"
      "v_exp_f32_e32 v222, v222\n\t"
      "v_mfma_f32_16x16x32_bf16 v[208:211], v[56:59], v[140:143], v[208:211]\n\t"
      "v_exp_f32_e32 v223, v223\n\t"
      "v_mfma_f32_16x16x32_bf16 v[212:215], v[44:47], v[136:139], v[212:215]\n\t"
      "global_load_dwordx4 v[136:139], v178, s[64:65] sc1\n\t"
      "v_add_f32_e32 v220, 1.0, v220\n\t"
      "v_mfma_f32_16x16x32_bf16 v[212:215], v[60:63], v[140:143], v[212:215]\n\t"
      "global_load_dwordx4 v[140:143], v179, s[64:65] sc1\n\t"
      "s_waitcnt vmcnt(8)\n\t"
      "v_mfma_f32_16x16x32_bf16 v[200:203], v[64:67], v[144:147], v[200:203]\n\t"
      "v_add_f32_e32 v221, 1.0, v221\n\t"
      "s_waitcnt vmcnt(7)\n\t"
      "v_mfma_f32_16x16x32_bf16 v[200:203], v[80:83], v[148:151], v[200:203]\n\t"
      "v_add_f32_e32 v222, 1.0, v222\n\t"
      "v_add_f32_e32 v223, 1.0, v223\n\t"
      "v_mfma_f32_16x16x32_bf16 v[204:207], v[68:71], v[144:147], v[204:207]\n\t"
      "v_rcp_f32_e32 v220, v220\n\t"
      "v_mfma_f32_16x16x32_bf16 v[204:207], v[84:87], v[148:151], v[204:207]\n\t"
      "v_rcp_f32_e32 v221, v221\n\t"
      "v_mfma_f32_16x16x32_bf16 v[208:211], v[72:75], v[144:147], v[208:211]\n\t"
      "v_rcp_f32_e32 v222, v222\n\t"
      "v_mfma_f32_16x16x32_bf16 v[208:211], v[88:91], v[148:151], v[208:211]\n\t"
      "v_rcp_f32_e32 v223, v223\n\t"
      "v_mfma_f32_16x16x32_bf16 v[212:215], v[76:79], v[144:147], v[212:215]\n\t"
      "global_load_dwordx4 v[144:147], v180, s[64:65] sc1\n\t"
      "v_mul_f32_e32 v216, v216, v220\n\t"
      "v_mfma_f32_16x16x32_bf16 v[212:215], v[92:95], v[148:151], v[212:215]\n\t"
      "global_load_dwordx4 v[148:151], v181, s[64:65] sc1\n\t"
      "s_waitcnt vmcnt(7)\n\t"
      "v_mfma_f32_16x16x32_bf16 v[200:203], v[96:99], v[152:155], v[200:203]\n\t"
      "v_mul_f32_e32 v217, v217, v221\n\t"
      "s_waitcnt vmcnt(6)\n\t"
      "v_mfma_f32_16x16x32_bf16 v[200:203], v[112:115], v[156:159], v[200:203]\n\t"
      "v_mul_f32_e32 v218, v218, v222\n\t"
      "v_mul_f32_e32 v219, v219, v223\n\t"
      "v_mfma_f32_16x16x32_bf16 v[204:207], v[100:103], v[152:155], v[204:207]\n\t"
      "v_cvt_pk_bf16_f32 v220, v216, v217\n\t"
      "v_cvt_pk_bf16_f32 v221, v218, v219\n\t"
      "v_mfma_f32_16x16x32_bf16 v[204:207], v[116:119], v[156:159], v[204:207]\n\t"
      "s_nop 1\n\t"
      "v_cmp_u_f32_e32 vcc, v200, v200\n\t"
      "v_min_u32_e32 v220, -2, v220\n\t"
      "v_mfma_f32_16x16x32_bf16 v[208:211], v[104:107], v[152:155], v[208:211]\n\t"
      "v_min_u32_e32 v221, -2, v221\n\t"
      "global_store_dwordx2 v245, v[220:221], s[8:9] sc1\n\t"
      "s_add_u32 s8, s8, 131072\n\t"
      "s_addc_u32 s9, s9, 0\n\t"
      "v_mfma_f32_16x16x32_bf16 v[208:211], v[120:123], v[156:159], v[208:211]\n\t"
      "s_cbranch_vccnz NP5R_%=\n\t"
      "NP5B_%=:\n\t"
      "ds_write_b128 v243, v[200:203] offset:32768\n\t"
      "v_mfma_f32_16x16x32_bf16 v[212:215], v[108:111], v[152:155], v[212:215]\n\t"
      "global_load_dwordx4 v[152:155], v182, s[64:65] sc1\n\t"
      "v_mfma_f32_16x16x32_bf16 v[212:215], v[124:127], v[156:159], v[212:215]\n\t"
      "global_load_dwordx4 v[156:159], v183, s[64:65] sc1\n\t"
      "ds_write_b128 v164, v[204:207] offset:32768\n\t"
      "s_nop 0\n\t"
      "ds_write_b128 v165, v[208:211] offset:32768\n\t"
      "s_nop 3\n\t"
      "ds_write_b128 v166, v[212:215] offset:32768\n\t"
      "s_waitcnt lgkmcnt(0)\n\t"
      "s_barrier\n\t"
      "ds_read_b128 v[216:219], v244 offset:32768\n\t"
      "ds_read_b128 v[220:223], v244 offset:36864\n\t"
      "ds_read_b128 v[224:227], v244 offset:40960\n\t"
      "ds_read_b128 v[228:231], v244 offset:45056\n\t"
      "ds_read_b128 v[200:203], v244 offset:49152\n\t"
      "ds_read_b128 v[204:207], v244 offset:53248\n\t"
      "ds_read_b128 v[208:211], v244 offset:57344\n\t"
      "ds_read_b128 v[212:215], v244 offset:61440\n\t"
      "s_waitcnt lgkmcnt(6)\n\t"
      "v_add_f32_e32 v216, v216, v220\n\t"
      "v_add_f32_e32 v217, v217, v221\n\t"
      "v_add_f32_e32 v218, v218, v222\n\t"
      "v_add_f32_e32 v219, v219, v223\n\t"
      "s_waitcnt lgkmcnt(4)\n\t"
      "v_add_f32_e32 v224, v224, v228\n\t"
      "v_add_f32_e32 v225, v225, v229\n\t"
      "v_add_f32_e32 v226, v226, v230\n\t"
      "v_add_f32_e32 v227, v227, v231\n\t"
      "s_waitcnt lgkmcnt(2)\n\t"
      "v_add_f32_e32 v200, v200, v204\n\t"
      "v_add_f32_e32 v201, v201, v205\n\t"
      "v_add_f32_e32 v202, v202, v206\n\t"
      "v_add_f32_e32 v203, v203, v207\n\t"
      "v_add_f32_e32 v216, v216, v224\n\t"
      "v_add_f32_e32 v217, v217, v225\n\t"
      "v_add_f32_e32 v218, v218, v226\n\t"
      "v_add_f32_e32 v219, v219, v227\n\t"
      "s_waitcnt lgkmcnt(0)\n\t"
      "v_add_f32_e32 v208, v208, v212\n\t"
      "v_add_f32_e32 v209, v209, v213\n\t"
      "v_add_f32_e32 v210, v210, v214\n\t"
      "v_add_f32_e32 v211, v211, v215\n\t"
      "v_add_f32_e32 v200, v200, v208\n\t"
      "s_waitcnt vmcnt(8)\n\t"
      "v_mfma_f32_16x16x32_bf16 v[184:187], v[0:3], v[128:131], 0\n\t"
      "v_add_f32_e32 v201, v201, v209\n\t"
      "s_waitcnt vmcnt(7)\n\t"
      "v_mfma_f32_16x16x32_bf16 v[184:187], v[16:19], v[132:135], v[184:187]\n\t"
      "v_add_f32_e32 v202, v202, v210\n\t"
      "v_add_f32_e32 v203, v203, v211\n\t"
      "v_mfma_f32_16x16x32_bf16 v[188:191], v[4:7], v[128:131], 0\n\t"
      "v_add_f32_e32 v216, v216, v200\n\t"
      "v_add_f32_e32 v217, v217, v201\n\t"
      "v_mfma_f32_16x16x32_bf16 v[188:191], v[20:23], v[132:135], v[188:191]\n\t"
      "v_add_f32_e32 v218, v218, v202\n\t"
      "v_add_f32_e32 v219, v219, v203\n\t"
      "v_mfma_f32_16x16x32_bf16 v[192:195], v[8:11], v[128:131], 0\n\t"
      "v_mul_f32_e32 v220, v216, v216\n\t"
      "v_mul_f32_e32 v221, v217, v217\n\t"
      "v_mfma_f32_16x16x32_bf16 v[192:195], v[24:27], v[132:135], v[192:195]\n\t"
      "v_mul_f32_e32 v222, v218, v218\n\t"
      "v_mul_f32_e32 v223, v219, v219\n\t"
      "v_mfma_f32_16x16x32_bf16 v[196:199], v[12:15], v[128:131], 0\n\t"
      "v_fmaak_f32 v220, v235, v220, 0xc0135761\n\t"
      "v_fmaak_f32 v221, v235, v221, 0xc0135761\n\t"
      "v_mfma_f32_16x16x32_bf16 v[196:199], v[28:31], v[132:135], v[196:199]\n\t"
      "v_fmaak_f32 v222, v235, v222, 0xc0135761\n\t"
      "s_waitcnt vmcnt(6)\n\t"
      "v_mfma_f32_16x16x32_bf16 v[184:187], v[32:35], v[136:139], v[184:187]\n\t"
      "v_fmaak_f32 v223, v235, v223, 0xc0135761\n\t"
      "s_waitcnt vmcnt(5)\n\t"
      "v_mfma_f32_16x16x32_bf16 v[184:187], v[48:51], v[140:143], v[184:187]\n\t"
      "v_mul_f32_e32 v220, v216, v220\n\t"
      "v_mul_f32_e32 v221, v217, v221\n\t"
      "v_mfma_f32_16x16x32_bf16 v[188:191], v[36:39], v[136:139], v[188:191]\n\t"
      "v_mul_f32_e32 v222, v218, v222\n\t"
      "v_mul_f32_e32 v223, v219, v223\n\t"
      "v_mfma_f32_16x16x32_bf16 v[188:191], v[52:55], v[140:143], v[188:191]\n\t"
      "v_exp_f32_e32 v220, v220\n\t"
      "v_mfma_f32_16x16x32_bf16 v[192:195], v[40:43], v[136:139], v[192:195]\n\t"
      "v_exp_f32_e32 v221, v221\n\t"
      "v_mfma_f32_16x16x32_bf16 v[192:195], v[56:59], v[140:143], v[192:195]\n\t"
      "v_exp_f32_e32 v222, v222\n\t"
      "v_mfma_f32_16x16x32_bf16 v[196:199], v[44:47], v[136:139], v[196:199]\n\t"
      "v_exp_f32_e32 v223, v223\n\t"
      "v_mfma_f32_16x16x32_bf16 v[196:199], v[60:63], v[140:143], v[196:199]\n\t"
      "s_waitcnt vmcnt(4)\n\t"
      "v_mfma_f32_16x16x32_bf16 v[184:187], v[64:67], v[144:147], v[184:187]\n\t"
      "s_waitcnt vmcnt(3)\n\t"
      "v_mfma_f32_16x16x32_bf16 v[184:187], v[80:83], v[148:151], v[184:187]\n\t"
      "v_add_f32_e32 v220, 1.0, v220\n\t"
      "v_add_f32_e32 v221, 1.0, v221\n\t"
      "v_mfma_f32_16x16x32_bf16 v[188:191], v[68:71], v[144:147], v[188:191]\n\t"
      "v_add_f32_e32 v222, 1.0, v222\n\t"
      "v_add_f32_e32 v223, 1.0, v223\n\t"
      "v_mfma_f32_16x16x32_bf16 v[188:191], v[84:87], v[148:151], v[188:191]\n\t"
      "v_rcp_f32_e32 v220, v220\n\t"
      "v_mfma_f32_16x16x32_bf16 v[192:195], v[72:75], v[144:147], v[192:195]\n\t"
      "v_rcp_f32_e32 v221, v221\n\t"
      "v_mfma_f32_16x16x32_bf16 v[192:195], v[88:91], v[148:151], v[192:195]\n\t"
      "v_rcp_f32_e32 v222, v222\n\t"
      "v_mfma_f32_16x16x32_bf16 v[196:199], v[76:79], v[144:147], v[196:199]\n\t"
      "v_rcp_f32_e32 v223, v223\n\t"
      "v_mfma_f32_16x16x32_bf16 v[196:199], v[92:95], v[148:151], v[196:199]\n\t"
      "v_mul_f32_e32 v216, v216, v220\n\t"
      "s_waitcnt vmcnt(1)\n\t"
      "v_mfma_f32_16x16x32_bf16 v[184:187], v[96:99], v[152:155], v[184:187]\n\t"
      "v_mul_f32_e32 v217, v217, v221\n\t"
      "s_waitcnt vmcnt(0)\n\t"
      "v_mfma_f32_16x16x32_bf16 v[184:187], v[112:115], v[156:159], v[184:187]\n\t"
      "v_mul_f32_e32 v218, v218, v222\n\t"
      "v_mul_f32_e32 v219, v219, v223\n\t"
      "v_mfma_f32_16x16x32_bf16 v[188:191], v[100:103], v[152:155], v[188:191]\n\t"
      "v_cvt_pk_bf16_f32 v220, v216, v217\n\t"
      "v_cvt_pk_bf16_f32 v221, v218, v219\n\t"
      "v_mfma_f32_16x16x32_bf16 v[188:191], v[116:119], v[156:159], v[188:191]\n\t"
      "s_nop 1\n\t"
      "v_cmp_u_f32_e32 vcc, v184, v184\n\t"
      "v_min_u32_e32 v220, -2, v220\n\t"
      "v_mfma_f32_16x16x32_bf16 v[192:195], v[104:107], v[152:155], v[192:195]\n\t"
      "v_min_u32_e32 v221, -2, v221\n\t"
      "global_store_dwordx2 v245, v[220:221], s[8:9] sc1\n\t"
      "s_add_u32 s8, s8, 131072\n\t"
      "s_addc_u32 s9, s9, 0\n\t"
      "v_mfma_f32_16x16x32_bf16 v[192:195], v[120:123], v[156:159], v[192:195]\n\t"
      "s_cbranch_vccnz NP6R_%=\n\t"
      "NP6B_%=:\n\t"
      "ds_write_b128 v243, v[184:187]\n\t"
      "v_mfma_f32_16x16x32_bf16 v[196:199], v[108:111], v[152:155], v[196:199]\n\t"
      "v_mfma_f32_16x16x32_bf16 v[196:199], v[124:127], v[156:159], v[196:199]\n\t"
      "ds_write_b128 v164, v[188:191]\n\t"
      "s_nop 2\n\t"
      "ds_write_b128 v165, v[192:195]\n\t"
      "s_nop 2\n\t"
      "ds_write_b128 v166, v[196:199]\n\t"
      "s_waitcnt lgkmcnt(0)\n\t"
      "s_barrier\n\t"
      "ds_read_b128 v[216:219], v244\n\t"
      "ds_read_b128 v[220:223], v244 offset:4096\n\t"
      "ds_read_b128 v[224:227], v244 offset:8192\n\t"
      "ds_read_b128 v[228:231], v244 offset:12288\n\t"
      "ds_read_b128 v[184:187], v244 offset:16384\n\t"
      "ds_read_b128 v[188:191], v244 offset:20480\n\t"
      "ds_read_b128 v[192:195], v244 offset:24576\n\t"
      "ds_read_b128 v[196:199], v244 offset:28672\n\t"
      "s_waitcnt lgkmcnt(6)\n\t"
      "v_add_f32_e32 v216, v216, v220\n\t"
      "v_add_f32_e32 v217, v217, v221\n\t"
      "v_add_f32_e32 v218, v218, v222\n\t"
      "v_add_f32_e32 v219, v219, v223\n\t"
      "s_waitcnt lgkmcnt(4)\n\t"
      "v_add_f32_e32 v224, v224, v228\n\t"
      "v_add_f32_e32 v225, v225, v229\n\t"
      "v_add_f32_e32 v226, v226, v230\n\t"
      "v_add_f32_e32 v227, v227, v231\n\t"
      "s_waitcnt lgkmcnt(2)\n\t"
      "v_add_f32_e32 v184, v184, v188\n\t"
      "v_add_f32_e32 v185, v185, v189\n\t"
      "v_add_f32_e32 v186, v186, v190\n\t"
      "v_add_f32_e32 v187, v187, v191\n\t"
      "v_add_f32_e32 v216, v216, v224\n\t"
      "v_add_f32_e32 v217, v217, v225\n\t"
      "v_add_f32_e32 v218, v218, v226\n\t"
      "v_add_f32_e32 v219, v219, v227\n\t"
      "s_waitcnt lgkmcnt(0)\n\t"
      "v_add_f32_e32 v192, v192, v196\n\t"
      "v_add_f32_e32 v193, v193, v197\n\t"
      "v_add_f32_e32 v194, v194, v198\n\t"
      "v_add_f32_e32 v195, v195, v199\n\t"
      "v_add_f32_e32 v184, v184, v192\n\t"
      "v_add_f32_e32 v185, v185, v193\n\t"
      "v_add_f32_e32 v186, v186, v194\n\t"
      "v_add_f32_e32 v187, v187, v195\n\t"
      "v_add_f32_e32 v216, v216, v184\n\t"
      "v_add_f32_e32 v217, v217, v185\n\t"
      "v_add_f32_e32 v218, v218, v186\n\t"
      "v_add_f32_e32 v219, v219, v187\n\t"
      "v_mul_f32_e32 v220, v216, v216\n\t"
      "v_mul_f32_e32 v221, v217, v217\n\t"
      "v_mul_f32_e32 v222, v218, v218\n\t"
      "v_mul_f32_e64 v223, v219, v219\n\t"
      "v_fmaak_f32 v220, v235, v220, 0xc0135761\n\t"
      "v_fmaak_f32 v221, v235, v221, 0xc0135761\n\t"
      "v_fmaak_f32 v222, v235, v222, 0xc0135761\n\t"
      "v_fmaak_f32 v223, v235, v223, 0xc0135761\n\t"
      "v_mul_f32_e32 v220, v216, v220\n\t"
      "v_mul_f32_e32 v221, v217, v221\n\t"
      "v_mul_f32_e32 v222, v218, v222\n\t"
      "v_mul_f32_e32 v223, v219, v223\n\t"
      "v_exp_f32_e32 v220, v220\n\t"
      "v_exp_f32_e32 v221, v221\n\t"
      "v_exp_f32_e32 v222, v222\n\t"
      "v_exp_f32_e32 v223, v223\n\t"
      "v_add_f32_e32 v220, 1.0, v220\n\t"
      "v_add_f32_e32 v221, 1.0, v221\n\t"
      "v_add_f32_e32 v222, 1.0, v222\n\t"
      "v_add_f32_e32 v223, 1.0, v223\n\t"
      "v_rcp_f32_e32 v220, v220\n\t"
      "v_rcp_f32_e32 v221, v221\n\t"
      "v_rcp_f32_e32 v222, v222\n\t"
      "v_rcp_f32_e32 v223, v223\n\t"
      "v_mul_f32_e32 v216, v216, v220\n\t"
      "v_mul_f32_e32 v217, v217, v221\n\t"
      "v_mul_f32_e32 v218, v218, v222\n\t"
      "v_mul_f32_e32 v219, v219, v223\n\t"
      "v_cvt_pk_bf16_f32 v220, v216, v217\n\t"
      "v_cvt_pk_bf16_f32 v221, v218, v219\n\t"
      "v_min_u32_e32 v220, -2, v220\n\t"
      "v_min_u32_e32 v221, -2, v221\n\t"
      "s_cmp_eq_u32 s17, 0\n\t"
      "s_cbranch_scc0 HI6_%=\n\t"
      "s_mov_b64 exec, 0xf\n\t"
      "global_store_dwordx2 v247, v[232:233], s[12:13] sc1\n\t"
      "s_mov_b64 exec, -1\n\t"
      "HI6_%=:\n\t"
      "global_store_dwordx2 v245, v[220:221], s[8:9] sc1\n\t"
      "GX_%=:\n\t"
      "s_bitcmp1_b32 s16, 5\n\t"
      "s_cbranch_scc0 EP_%=\n\t"
      "v_mov_b32_e32 v0, s46\n\t"
      "v_mov_b32_e32 v1, s47\n\t"
      "v_mov_b32_e32 v2, 131072\n\t"
      "s_mov_b64 exec, 1\n\t"
      "global_store_dwordx2 v2, v[0:1], s[12:13] sc1\n\t"
      "s_mov_b64 exec, -1\n\t"
      "EP_%=:\n\t"
      "AEND_%=:\n\t"
      "s_endpgm\n\t"
      ".p2align 6\n\t"
      "GQ_%=:\n\t"
      "s_waitcnt vmcnt(7)\n\t"
      "v_max3_u32 v234, v128, v129, v130\n\t"
      "v_max_u32_e32 v234, v234, v131\n\t"
      "v_cmp_eq_u32_e32 vcc, -1, v234\n\t"
      "s_cbranch_vccnz FQ0R_%=\n\t"
      "FQ0B_%=:\n\t"
      "v_mfma_f32_16x16x32_bf16 v[184:187], v[0:3], v[128:131], v[236:239]\n\t"
      "s_waitcnt vmcnt(6)\n\t"
      "v_max3_u32 v234, v132, v133, v134\n\t"
      "v_max_u32_e32 v234, v234, v135\n\t"
      "v_cmp_eq_u32_e32 vcc, -1, v234\n\t"
      "s_cbranch_vccnz FQ1R_%=\n\t"
      "FQ1B_%=:\n\t"
      "v_mfma_f32_16x16x32_bf16 v[184:187], v[16:19], v[132:135], v[184:187]\n\t"
      "v_mfma_f32_16x16x32_bf16 v[188:191], v[4:7], v[128:131], 0\n\t"
      "v_mfma_f32_16x16x32_bf16 v[188:191], v[20:23], v[132:135], v[188:191]\n\t"
      "v_mfma_f32_16x16x32_bf16 v[192:195], v[8:11], v[128:131], 0\n\t"
      "v_mfma_f32_16x16x32_bf16 v[192:195], v[24:27], v[132:135], v[192:195]\n\t"
      "v_mfma_f32_16x16x32_bf16 v[196:199], v[12:15], v[128:131], 0\n\t"
      "global_load_dwordx4 v[128:131], v176, s[54:55] sc1\n\t"
      "v_mfma_f32_16x16x32_bf16 v[196:199], v[28:31], v[132:135], v[196:199]\n\t"
      "global_load_dwordx4 v[132:135], v177, s[54:55] sc1\n\t"
      "s_waitcnt vmcnt(7)\n\t"
      "v_max3_u32 v234, v136, v137, v138\n\t"
      "v_max_u32_e32 v234, v234, v139\n\t"
      "v_cmp_eq_u32_e32 vcc, -1, v234\n\t"
      "s_cbranch_vccnz FQ2R_%=\n\t"
      "FQ2B_%=:\n\t"
      "v_mfma_f32_16x16x32_bf16 v[184:187], v[32:35], v[136:139], v[184:187]\n\t"
      "s_waitcnt vmcnt(6)\n\t"
      "v_max3_u32 v234, v140, v141, v142\n\t"
      "v_max_u32_e32 v234, v234, v143\n\t"
      "v_cmp_eq_u32_e32 vcc, -1, v234\n\t"
      "s_cbranch_vccnz FQ3R_%=\n\t"
      "FQ3B_%=:\n\t"
      "v_mfma_f32_16x16x32_bf16 v[184:187], v[48:51], v[140:143], v[184:187]\n\t"
      "v_mfma_f32_16x16x32_bf16 v[188:191], v[36:39], v[136:139], v[188:191]\n\t"
      "v_mfma_f32_16x16x32_bf16 v[188:191], v[52:55], v[140:143], v[188:191]\n\t"
      "v_mfma_f32_16x16x32_bf16 v[192:195], v[40:43], v[136:139], v[192:195]\n\t"
      "v_mfma_f32_16x16x32_bf16 v[192:195], v[56:59], v[140:143], v[192:195]\n\t"
      "v_mfma_f32_16x16x32_bf16 v[196:199], v[44:47], v[136:139], v[196:199]\n\t"
      "global_load_dwordx4 v[136:139], v178, s[54:55] sc1\n\t"
      "v_mfma_f32_16x16x32_bf16 v[196:199], v[60:63], v[140:143], v[196:199]\n\t"
      "global_load_dwordx4 v[140:143], v179, s[54:55] sc1\n\t"
      "s_waitcnt vmcnt(7)\n\t"
      "v_max3_u32 v234, v144, v145, v146\n\t"
      "v_max_u32_e32 v234, v234, v147\n\t"
      "v_cmp_eq_u32_e32 vcc, -1, v234\n\t"
      "s_cbranch_vccnz FQ4R_%=\n\t"
      "FQ4B_%=:\n\t"
      "v_mfma_f32_16x16x32_bf16 v[184:187], v[64:67], v[144:147], v[184:187]\n\t"
      "s_waitcnt vmcnt(6)\n\t"
      "v_max3_u32 v234, v148, v149, v150\n\t"
      "v_max_u32_e32 v234, v234, v151\n\t"
      "v_cmp_eq_u32_e32 vcc, -1, v234\n\t"
      "s_cbranch_vccnz FQ5R_%=\n\t"
      "FQ5B_%=:\n\t"
      "v_mfma_f32_16x16x32_bf16 v[184:187], v[80:83], v[148:151], v[184:187]\n\t"
      "v_mfma_f32_16x16x32_bf16 v[188:191], v[68:71], v[144:147], v[188:191]\n\t"
      "v_mfma_f32_16x16x32_bf16 v[188:191], v[84:87], v[148:151], v[188:191]\n\t"
      "v_mfma_f32_16x16x32_bf16 v[192:195], v[72:75], v[144:147], v[192:195]\n\t"
      "v_mfma_f32_16x16x32_bf16 v[192:195], v[88:91], v[148:151], v[192:195]\n\t"
      "v_mfma_f32_16x16x32_bf16 v[196:199], v[76:79], v[144:147], v[196:199]\n\t"
      "global_load_dwordx4 v[144:147], v180, s[54:55] sc1\n\t"
      "v_mfma_f32_16x16x32_bf16 v[196:199], v[92:95], v[148:151], v[196:199]\n\t"
      "global_load_dwordx4 v[148:151], v181, s[54:55] sc1\n\t"
      "s_waitcnt vmcnt(7)\n\t"
      "v_max3_u32 v234, v152, v153, v154\n\t"
      "v_max_u32_e32 v234, v234, v155\n\t"
      "v_cmp_eq_u32_e32 vcc, -1, v234\n\t"
      "s_cbranch_vccnz FQ6R_%=\n\t"
      "FQ6B_%=:\n\t"
      "v_mfma_f32_16x16x32_bf16 v[184:187], v[96:99], v[152:155], v[184:187]\n\t"
      "s_waitcnt vmcnt(6)\n\t"
      "v_max3_u32 v234, v156, v157, v158\n\t"
      "v_max_u32_e32 v234, v234, v159\n\t"
      "v_cmp_eq_u32_e32 vcc, -1, v234\n\t"
      "s_cbranch_vccnz FQ7R_%=\n\t"
      "FQ7B_%=:\n\t"
      "v_mfma_f32_16x16x32_bf16 v[184:187], v[112:115], v[156:159], v[184:187]\n\t"
      "v_mfma_f32_16x16x32_bf16 v[188:191], v[100:103], v[152:155], v[188:191]\n\t"
      "v_mfma_f32_16x16x32_bf16 v[188:191], v[116:119], v[156:159], v[188:191]\n\t"
      "v_mfma_f32_16x16x32_bf16 v[192:195], v[104:107], v[152:155], v[192:195]\n\t"
      "v_mfma_f32_16x16x32_bf16 v[192:195], v[120:123], v[156:159], v[192:195]\n\t"
      "v_mfma_f32_16x16x32_bf16 v[196:199], v[108:111], v[152:155], v[196:199]\n\t"
      "global_load_dwordx4 v[152:155], v182, s[54:55] sc1\n\t"
      "v_mfma_f32_16x16x32_bf16 v[196:199], v[124:127], v[156:159], v[196:199]\n\t"
      "global_load_dwordx4 v[156:159], v183, s[54:55] sc1\n\t"
      "ds_write_b128 v243, v[184:187]\n\t"
      "s_nop 0\n\t"
      "ds_write_b128 v164, v[188:191]\n\t"
      "s_nop 0\n\t"
      "ds_write_b128 v165, v[192:195]\n\t"
      "s_nop 1\n\t"
      "ds_write_b128 v166, v[196:199]\n\t"
      "s_waitcnt vmcnt(7)\n\t"
      "v_mfma_f32_16x16x32_bf16 v[200:203], v[0:3], v[128:131], v[236:239]\n\t"
      "s_waitcnt vmcnt(6)\n\t"
      "v_mfma_f32_16x16x32_bf16 v[200:203], v[16:19], v[132:135], v[200:203]\n\t"
      "v_mfma_f32_16x16x32_bf16 v[204:207], v[4:7], v[128:131], 0\n\t"
      "v_mfma_f32_16x16x32_bf16 v[204:207], v[20:23], v[132:135], v[204:207]\n\t"
      "v_mfma_f32_16x16x32_bf16 v[208:211], v[8:11], v[128:131], 0\n\t"
      "v_mfma_f32_16x16x32_bf16 v[208:211], v[24:27], v[132:135], v[208:211]\n\t"
      "v_mfma_f32_16x16x32_bf16 v[212:215], v[12:15], v[128:131], 0\n\t"
      "global_load_dwordx4 v[128:131], v176, s[56:57] sc1\n\t"
      "v_mfma_f32_16x16x32_bf16 v[212:215], v[28:31], v[132:135], v[212:215]\n\t"
      "global_load_dwordx4 v[132:135], v177, s[56:57] sc1\n\t"
      "s_waitcnt vmcnt(7)\n\t"
      "v_mfma_f32_16x16x32_bf16 v[200:203], v[32:35], v[136:139], v[200:203]\n\t"
      "s_waitcnt vmcnt(6)\n\t"
      "v_mfma_f32_16x16x32_bf16 v[200:203], v[48:51], v[140:143], v[200:203]\n\t"
      "v_mfma_f32_16x16x32_bf16 v[204:207], v[36:39], v[136:139], v[204:207]\n\t"
      "v_mfma_f32_16x16x32_bf16 v[204:207], v[52:55], v[140:143], v[204:207]\n\t"
      "v_mfma_f32_16x16x32_bf16 v[208:211], v[40:43], v[136:139], v[208:211]\n\t"
      "v_mfma_f32_16x16x32_bf16 v[208:211], v[56:59], v[140:143], v[208:211]\n\t"
      "v_mfma_f32_16x16x32_bf16 v[212:215], v[44:47], v[136:139], v[212:215]\n\t"
      "global_load_dwordx4 v[136:139], v178, s[56:57] sc1\n\t"
      "v_mfma_f32_16x16x32_bf16 v[212:215], v[60:63], v[140:143], v[212:215]\n\t"
      "global_load_dwordx4 v[140:143], v179, s[56:57] sc1\n\t"
      "s_waitcnt vmcnt(7)\n\t"
      "v_mfma_f32_16x16x32_bf16 v[200:203], v[64:67], v[144:147], v[200:203]\n\t"
      "s_waitcnt vmcnt(6)\n\t"
      "v_mfma_f32_16x16x32_bf16 v[200:203], v[80:83], v[148:151], v[200:203]\n\t"
      "v_mfma_f32_16x16x32_bf16 v[204:207], v[68:71], v[144:147], v[204:207]\n\t"
      "v_mfma_f32_16x16x32_bf16 v[204:207], v[84:87], v[148:151], v[204:207]\n\t"
      "v_mfma_f32_16x16x32_bf16 v[208:211], v[72:75], v[144:147], v[208:211]\n\t"
      "v_mfma_f32_16x16x32_bf16 v[208:211], v[88:91], v[148:151], v[208:211]\n\t"
      "s_waitcnt lgkmcnt(0)\n\t"
      "s_barrier\n\t"
      "s_setprio 2\n\t"
      "v_mfma_f32_16x16x32_bf16 v[212:215], v[76:79], v[144:147], v[212:215]\n\t"
      "global_load_dwordx4 v[144:147], v180, s[56:57] sc1\n\t"
      "v_mfma_f32_16x16x32_bf16 v[212:215], v[92:95], v[148:151], v[212:215]\n\t"
      "global_load_dwordx4 v[148:151], v181, s[56:57] sc1\n\t"
      "s_waitcnt vmcnt(7)\n\t"
      "v_mfma_f32_16x16x32_bf16 v[200:203], v[96:99], v[152:155], v[200:203]\n\t"
      "s_waitcnt vmcnt(6)\n\t"
      "v_mfma_f32_16x16x32_bf16 v[200:203], v[112:115], v[156:159], v[200:203]\n\t"
      "v_mfma_f32_16x16x32_bf16 v[204:207], v[100:103], v[152:155], v[204:207]\n\t"
      "v_mfma_f32_16x16x32_bf16 v[204:207], v[116:119], v[156:159], v[204:207]\n\t"
      "v_mfma_f32_16x16x32_bf16 v[208:211], v[104:107], v[152:155], v[208:211]\n\t"
      "v_mfma_f32_16x16x32_bf16 v[208:211], v[120:123], v[156:159], v[208:211]\n\t"
      "v_mfma_f32_16x16x32_bf16 v[212:215], v[108:111], v[152:155], v[212:215]\n\t"
      "global_load_dwordx4 v[152:155], v182, s[56:57] sc1\n\t"
      "v_mfma_f32_16x16x32_bf16 v[212:215], v[124:127], v[156:159], v[212:215]\n\t"
      "global_load_dwordx4 v[156:159], v183, s[56:57] sc1\n\t"
      "s_setprio 0\n\t"
      "v_cmp_u_f32_e32 vcc, v200, v200\n\t"
      "s_cbranch_vccnz NQ1R_%=\n\t"
      "NQ1B_%=:\n\t"
      "ds_write_b128 v243, v[200:203] offset:32768\n\t"
      "ds_write_b128 v164, v[204:207] offset:32768\n\t"
      "ds_write_b128 v165, v[208:211] offset:32768\n\t"
      "s_nop 0\n\t"
      "ds_write_b128 v166, v[212:215] offset:32768\n\t"
      "s_waitcnt vmcnt(7)\n\t"
      "v_mfma_f32_16x16x32_bf16 v[184:187], v[0:3], v[128:131], v[236:239]\n\t"
      "s_waitcnt vmcnt(6)\n\t"
      "v_mfma_f32_16x16x32_bf16 v[184:187], v[16:19], v[132:135], v[184:187]\n\t"
      "v_mfma_f32_16x16x32_bf16 v[188:191], v[4:7], v[128:131], 0\n\t"
      "v_mfma_f32_16x16x32_bf16 v[188:191], v[20:23], v[132:135], v[188:191]\n\t"
      "v_mfma_f32_16x16x32_bf16 v[192:195], v[8:11], v[128:131], 0\n\t"
      "v_mfma_f32_16x16x32_bf16 v[192:195], v[24:27], v[132:135], v[192:195]\n\t"
      "v_mfma_f32_16x16x32_bf16 v[196:199], v[12:15], v[128:131], 0\n\t"
      "global_load_dwordx4 v[128:131], v176, s[58:59] sc1\n\t"
      "v_mfma_f32_16x16x32_bf16 v[196:199], v[28:31], v[132:135], v[196:199]\n\t"
      "global_load_dwordx4 v[132:135], v177, s[58:59] sc1\n\t"
      "s_waitcnt vmcnt(7)\n\t"
      "v_mfma_f32_16x16x32_bf16 v[184:187], v[32:35], v[136:139], v[184:187]\n\t"
      "s_waitcnt vmcnt(6)\n\t"
      "v_mfma_f32_16x16x32_bf16 v[184:187], v[48:51], v[140:143], v[184:187]\n\t"
      "v_mfma_f32_16x16x32_bf16 v[188:191], v[36:39], v[136:139], v[188:191]\n\t"
      "v_mfma_f32_16x16x32_bf16 v[188:191], v[52:55], v[140:143], v[188:191]\n\t"
      "v_mfma_f32_16x16x32_bf16 v[192:195], v[40:43], v[136:139], v[192:195]\n\t"
      "v_mfma_f32_16x16x32_bf16 v[192:195], v[56:59], v[140:143], v[192:195]\n\t"
      "v_mfma_f32_16x16x32_bf16 v[196:199], v[44:47], v[136:139], v[196:199]\n\t"
      "global_load_dwordx4 v[136:139], v178, s[58:59] sc1\n\t"
      "v_mfma_f32_16x16x32_bf16 v[196:199], v[60:63], v[140:143], v[196:199]\n\t"
      "global_load_dwordx4 v[140:143], v179, s[58:59] sc1\n\t"
      "s_waitcnt vmcnt(7)\n\t"
      "v_mfma_f32_16x16x32_bf16 v[184:187], v[64:67], v[144:147], v[184:187]\n\t"
      "s_waitcnt vmcnt(6)\n\t"
      "v_mfma_f32_16x16x32_bf16 v[184:187], v[80:83], v[148:151], v[184:187]\n\t"
      "v_mfma_f32_16x16x32_bf16 v[188:191], v[68:71], v[144:147], v[188:191]\n\t"
      "v_mfma_f32_16x16x32_bf16 v[188:191], v[84:87], v[148:151], v[188:191]\n\t"
      "v_mfma_f32_16x16x32_bf16 v[192:195], v[72:75], v[144:147], v[192:195]\n\t"
      "v_mfma_f32_16x16x32_bf16 v[192:195], v[88:91], v[148:151], v[192:195]\n\t"
      "s_waitcnt lgkmcnt(0)\n\t"
      "s_barrier\n\t"
      "s_setprio 2\n\t"
      "v_mfma_f32_16x16x32_bf16 v[196:199], v[76:79], v[144:147], v[196:199]\n\t"
      "global_load_dwordx4 v[144:147], v180, s[58:59] sc1\n\t"
      "v_mfma_f32_16x16x32_bf16 v[196:199], v[92:95], v[148:151], v[196:199]\n\t"
      "global_load_dwordx4 v[148:151], v181, s[58:59] sc1\n\t"
      "s_waitcnt vmcnt(7)\n\t"
      "v_mfma_f32_16x16x32_bf16 v[184:187], v[96:99], v[152:155], v[184:187]\n\t"
      "s_waitcnt vmcnt(6)\n\t"
      "v_mfma_f32_16x16x32_bf16 v[184:187], v[112:115], v[156:159], v[184:187]\n\t"
      "v_mfma_f32_16x16x32_bf16 v[188:191], v[100:103], v[152:155], v[188:191]\n\t"
      "v_mfma_f32_16x16x32_bf16 v[188:191], v[116:119], v[156:159], v[188:191]\n\t"
      "v_mfma_f32_16x16x32_bf16 v[192:195], v[104:107], v[152:155], v[192:195]\n\t"
      "v_mfma_f32_16x16x32_bf16 v[192:195], v[120:123], v[156:159], v[192:195]\n\t"
      "v_mfma_f32_16x16x32_bf16 v[196:199], v[108:111], v[152:155], v[196:199]\n\t"
      "global_load_dwordx4 v[152:155], v182, s[58:59] sc1\n\t"
      "v_mfma_f32_16x16x32_bf16 v[196:199], v[124:127], v[156:159], v[196:199]\n\t"
      "global_load_dwordx4 v[156:159], v183, s[58:59] sc1\n\t"
      "s_setprio 0\n\t"
      "v_cmp_u_f32_e32 vcc, v184, v184\n\t"
      "s_cbranch_vccnz NQ2R_%=\n\t"
      "NQ2B_%=:\n\t"
      "ds_write_b128 v243, v[184:187]\n\t"
      "ds_write_b128 v164, v[188:191]\n\t"
      "ds_write_b128 v165, v[192:195]\n\t"
      "s_nop 0\n\t"
      "ds_write_b128 v166, v[196:199]\n\t"
      "s_waitcnt vmcnt(7)\n\t"
      "v_mfma_f32_16x16x32_bf16 v[200:203], v[0:3], v[128:131], v[236:239]\n\t"
      "s_waitcnt vmcnt(6)\n\t"
      "v_mfma_f32_16x16x32_bf16 v[200:203], v[16:19], v[132:135], v[200:203]\n\t"
      "v_mfma_f32_16x16x32_bf16 v[204:207], v[4:7], v[128:131], 0\n\t"
      "v_mfma_f32_16x16x32_bf16 v[204:207], v[20:23], v[132:135], v[204:207]\n\t"
      "v_mfma_f32_16x16x32_bf16 v[208:211], v[8:11], v[128:131], 0\n\t"
      "v_mfma_f32_16x16x32_bf16 v[208:211], v[24:27], v[132:135], v[208:211]\n\t"
      "v_mfma_f32_16x16x32_bf16 v[212:215], v[12:15], v[128:131], 0\n\t"
      "global_load_dwordx4 v[128:131], v176, s[60:61] sc1\n\t"
      "v_mfma_f32_16x16x32_bf16 v[212:215], v[28:31], v[132:135], v[212:215]\n\t"
      "global_load_dwordx4 v[132:135], v177, s[60:61] sc1\n\t"
      "s_waitcnt vmcnt(7)\n\t"
      "v_mfma_f32_16x16x32_bf16 v[200:203], v[32:35], v[136:139], v[200:203]\n\t"
      "s_waitcnt vmcnt(6)\n\t"
      "v_mfma_f32_16x16x32_bf16 v[200:203], v[48:51], v[140:143], v[200:203]\n\t"
      "v_mfma_f32_16x16x32_bf16 v[204:207], v[36:39], v[136:139], v[204:207]\n\t"
      "v_mfma_f32_16x16x32_bf16 v[204:207], v[52:55], v[140:143], v[204:207]\n\t"
      "v_mfma_f32_16x16x32_bf16 v[208:211], v[40:43], v[136:139], v[208:211]\n\t"
      "v_mfma_f32_16x16x32_bf16 v[208:211], v[56:59], v[140:143], v[208:211]\n\t"
      "v_mfma_f32_16x16x32_bf16 v[212:215], v[44:47], v[136:139], v[212:215]\n\t"
      "global_load_dwordx4 v[136:139], v178, s[60:61] sc1\n\t"
      "v_mfma_f32_16x16x32_bf16 v[212:215], v[60:63], v[140:143], v[212:215]\n\t"
      "global_load_dwordx4 v[140:143], v179, s[60:61] sc1\n\t"
      "s_waitcnt vmcnt(7)\n\t"
      "v_mfma_f32_16x16x32_bf16 v[200:203], v[64:67], v[144:147], v[200:203]\n\t"
      "s_waitcnt vmcnt(6)\n\t"
      "v_mfma_f32_16x16x32_bf16 v[200:203], v[80:83], v[148:151], v[200:203]\n\t"
      "v_mfma_f32_16x16x32_bf16 v[204:207], v[68:71], v[144:147], v[204:207]\n\t"
      "v_mfma_f32_16x16x32_bf16 v[204:207], v[84:87], v[148:151], v[204:207]\n\t"
      "v_mfma_f32_16x16x32_bf16 v[208:211], v[72:75], v[144:147], v[208:211]\n\t"
      "v_mfma_f32_16x16x32_bf16 v[208:211], v[88:91], v[148:151], v[208:211]\n\t"
      "s_waitcnt lgkmcnt(0)\n\t"
      "s_barrier\n\t"
      "s_setprio 2\n\t"
      "v_mfma_f32_16x16x32_bf16 v[212:215], v[76:79], v[144:147], v[212:215]\n\t"
      "global_load_dwordx4 v[144:147], v180, s[60:61] sc1\n\t"
      "v_mfma_f32_16x16x32_bf16 v[212:215], v[92:95], v[148:151], v[212:215]\n\t"
      "global_load_dwordx4 v[148:151], v181, s[60:61] sc1\n\t"
      "s_waitcnt vmcnt(7)\n\t"
      "v_mfma_f32_16x16x32_bf16 v[200:203], v[96:99], v[152:155], v[200:203]\n\t"
      "s_waitcnt vmcnt(6)\n\t"
      "v_mfma_f32_16x16x32_bf16 v[200:203], v[112:115], v[156:159], v[200:203]\n\t"
      "v_mfma_f32_16x16x32_bf16 v[204:207], v[100:103], v[152:155], v[204:207]\n\t"
      "v_mfma_f32_16x16x32_bf16 v[204:207], v[116:119], v[156:159], v[204:207]\n\t"
      "v_mfma_f32_16x16x32_bf16 v[208:211], v[104:107], v[152:155], v[208:211]\n\t"
      "v_mfma_f32_16x16x32_bf16 v[208:211], v[120:123], v[156:159], v[208:211]\n\t"
      "v_mfma_f32_16x16x32_bf16 v[212:215], v[108:111], v[152:155], v[212:215]\n\t"
      "global_load_dwordx4 v[152:155], v182, s[60:61] sc1\n\t"
      "v_mfma_f32_16x16x32_bf16 v[212:215], v[124:127], v[156:159], v[212:215]\n\t"
      "global_load_dwordx4 v[156:159], v183, s[60:61] sc1\n\t"
      "s_setprio 0\n\t"
      "v_cmp_u_f32_e32 vcc, v200, v200\n\t"
      "s_cbranch_vccnz NQ3R_%=\n\t"
      "NQ3B_%=:\n\t"
      "ds_write_b128 v243, v[200:203] offset:32768\n\t"
      "ds_write_b128 v164, v[204:207] offset:32768\n\t"
      "ds_write_b128 v165, v[208:211] offset:32768\n\t"
      "s_nop 0\n\t"
      "ds_write_b128 v166, v[212:215] offset:32768\n\t"
      "s_waitcnt vmcnt(7)\n\t"
      "v_mfma_f32_16x16x32_bf16 v[184:187], v[0:3], v[128:131], v[236:239]\n\t"
      "s_waitcnt vmcnt(6)\n\t"
      "v_mfma_f32_16x16x32_bf16 v[184:187], v[16:19], v[132:135], v[184:187]\n\t"
      "v_mfma_f32_16x16x32_bf16 v[188:191], v[4:7], v[128:131], 0\n\t"
      "v_mfma_f32_16x16x32_bf16 v[188:191], v[20:23], v[132:135], v[188:191]\n\t"
      "v_mfma_f32_16x16x32_bf16 v[192:195], v[8:11], v[128:131], 0\n\t"
      "v_mfma_f32_16x16x32_bf16 v[192:195], v[24:27], v[132:135], v[192:195]\n\t"
      "v_mfma_f32_16x16x32_bf16 v[196:199], v[12:15], v[128:131], 0\n\t"
      "global_load_dwordx4 v[128:131], v176, s[62:63] sc1\n\t"
      "v_mfma_f32_16x16x32_bf16 v[196:199], v[28:31], v[132:135], v[196:199]\n\t"
      "global_load_dwordx4 v[132:135], v177, s[62:63] sc1\n\t"
      "s_waitcnt vmcnt(7)\n\t"
      "v_mfma_f32_16x16x32_bf16 v[184:187], v[32:35], v[136:139], v[184:187]\n\t"
      "s_waitcnt vmcnt(6)\n\t"
      "v_mfma_f32_16x16x32_bf16 v[184:187], v[48:51], v[140:143], v[184:187]\n\t"
      "v_mfma_f32_16x16x32_bf16 v[188:191], v[36:39], v[136:139], v[188:191]\n\t"
      "v_mfma_f32_16x16x32_bf16 v[188:191], v[52:55], v[140:143], v[188:191]\n\t"
      "v_mfma_f32_16x16x32_bf16 v[192:195], v[40:43], v[136:139], v[192:195]\n\t"
      "v_mfma_f32_16x16x32_bf16 v[192:195], v[56:59], v[140:143], v[192:195]\n\t"
      "v_mfma_f32_16x16x32_bf16 v[196:199], v[44:47], v[136:139], v[196:199]\n\t"
      "global_load_dwordx4 v[136:139], v178, s[62:63] sc1\n\t"
      "v_mfma_f32_16x16x32_bf16 v[196:199], v[60:63], v[140:143], v[196:199]\n\t"
      "global_load_dwordx4 v[140:143], v179, s[62:63] sc1\n\t"
      "s_waitcnt vmcnt(7)\n\t"
      "v_mfma_f32_16x16x32_bf16 v[184:187], v[64:67], v[144:147], v[184:187]\n\t"
      "s_waitcnt vmcnt(6)\n\t"
      "v_mfma_f32_16x16x32_bf16 v[184:187], v[80:83], v[148:151], v[184:187]\n\t"
      "v_mfma_f32_16x16x32_bf16 v[188:191], v[68:71], v[144:147], v[188:191]\n\t"
      "v_mfma_f32_16x16x32_bf16 v[188:191], v[84:87], v[148:151], v[188:191]\n\t"
      "v_mfma_f32_16x16x32_bf16 v[192:195], v[72:75], v[144:147], v[192:195]\n\t"
      "v_mfma_f32_16x16x32_bf16 v[192:195], v[88:91], v[148:151], v[192:195]\n\t"
      "s_waitcnt lgkmcnt(0)\n\t"
      "s_barrier\n\t"
      "s_setprio 2\n\t"
      "v_mfma_f32_16x16x32_bf16 v[196:199], v[76:79], v[144:147], v[196:199]\n\t"
      "global_load_dwordx4 v[144:147], v180, s[62:63] sc1\n\t"
      "v_mfma_f32_16x16x32_bf16 v[196:199], v[92:95], v[148:151], v[196:199]\n\t"
      "global_load_dwordx4 v[148:151], v181, s[62:63] sc1\n\t"
      "s_waitcnt vmcnt(7)\n\t"
      "v_mfma_f32_16x16x32_bf16 v[184:187], v[96:99], v[152:155], v[184:187]\n\t"
      "s_waitcnt vmcnt(6)\n\t"
      "v_mfma_f32_16x16x32_bf16 v[184:187], v[112:115], v[156:159], v[184:187]\n\t"
      "v_mfma_f32_16x16x32_bf16 v[188:191], v[100:103], v[152:155], v[188:191]\n\t"
      "v_mfma_f32_16x16x32_bf16 v[188:191], v[116:119], v[156:159], v[188:191]\n\t"
      "v_mfma_f32_16x16x32_bf16 v[192:195], v[104:107], v[152:155], v[192:195]\n\t"
      "v_mfma_f32_16x16x32_bf16 v[192:195], v[120:123], v[156:159], v[192:195]\n\t"
      "v_mfma_f32_16x16x32_bf16 v[196:199], v[108:111], v[152:155], v[196:199]\n\t"
      "global_load_dwordx4 v[152:155], v182, s[62:63] sc1\n\t"
      "v_mfma_f32_16x16x32_bf16 v[196:199], v[124:127], v[156:159], v[196:199]\n\t"
      "global_load_dwordx4 v[156:159], v183, s[62:63] sc1\n\t"
      "s_setprio 0\n\t"
      "v_cmp_u_f32_e32 vcc, v184, v184\n\t"
      "s_cbranch_vccnz NQ4R_%=\n\t"
      "NQ4B_%=:\n\t"
      "ds_write_b128 v243, v[184:187]\n\t"
      "ds_write_b128 v164, v[188:191]\n\t"
      "ds_write_b128 v165, v[192:195]\n\t"
      "s_nop 0\n\t"
      "ds_write_b128 v166, v[196:199]\n\t"
      "s_waitcnt vmcnt(7)\n\t"
      "v_mfma_f32_16x16x32_bf16 v[200:203], v[0:3], v[128:131], v[236:239]\n\t"
      "s_waitcnt vmcnt(6)\n\t"
      "v_mfma_f32_16x16x32_bf16 v[200:203], v[16:19], v[132:135], v[200:203]\n\t"
      "v_mfma_f32_16x16x32_bf16 v[204:207], v[4:7], v[128:131], 0\n\t"
      "v_mfma_f32_16x16x32_bf16 v[204:207], v[20:23], v[132:135], v[204:207]\n\t"
      "v_mfma_f32_16x16x32_bf16 v[208:211], v[8:11], v[128:131], 0\n\t"
      "v_mfma_f32_16x16x32_bf16 v[208:211], v[24:27], v[132:135], v[208:211]\n\t"
      "v_mfma_f32_16x16x32_bf16 v[212:215], v[12:15], v[128:131], 0\n\t"
      "global_load_dwordx4 v[128:131], v176, s[64:65] sc1\n\t"
      "v_mfma_f32_16x16x32_bf16 v[212:215], v[28:31], v[132:135], v[212:215]\n\t"
      "global_load_dwordx4 v[132:135], v177, s[64:65] sc1\n\t"
      "s_waitcnt vmcnt(7)\n\t"
      "v_mfma_f32_16x16x32_bf16 v[200:203], v[32:35], v[136:139], v[200:203]\n\t"
      "s_waitcnt vmcnt(6)\n\t"
      "v_mfma_f32_16x16x32_bf16 v[200:203], v[48:51], v[140:143], v[200:203]\n\t"
      "v_mfma_f32_16x16x32_bf16 v[204:207], v[36:39], v[136:139], v[204:207]\n\t"
      "v_mfma_f32_16x16x32_bf16 v[204:207], v[52:55], v[140:143], v[204:207]\n\t"
      "v_mfma_f32_16x16x32_bf16 v[208:211], v[40:43], v[136:139], v[208:211]\n\t"
      "v_mfma_f32_16x16x32_bf16 v[208:211], v[56:59], v[140:143], v[208:211]\n\t"
      "v_mfma_f32_16x16x32_bf16 v[212:215], v[44:47], v[136:139], v[212:215]\n\t"
      "global_load_dwordx4 v[136:139], v178, s[64:65] sc1\n\t"
      "v_mfma_f32_16x16x32_bf16 v[212:215], v[60:63], v[140:143], v[212:215]\n\t"
      "global_load_dwordx4 v[140:143], v179, s[64:65] sc1\n\t"
      "s_waitcnt vmcnt(7)\n\t"
      "v_mfma_f32_16x16x32_bf16 v[200:203], v[64:67], v[144:147], v[200:203]\n\t"
      "s_waitcnt vmcnt(6)\n\t"
      "v_mfma_f32_16x16x32_bf16 v[200:203], v[80:83], v[148:151], v[200:203]\n\t"
      "v_mfma_f32_16x16x32_bf16 v[204:207], v[68:71], v[144:147], v[204:207]\n\t"
      "v_mfma_f32_16x16x32_bf16 v[204:207], v[84:87], v[148:151], v[204:207]\n\t"
      "v_mfma_f32_16x16x32_bf16 v[208:211], v[72:75], v[144:147], v[208:211]\n\t"
      "v_mfma_f32_16x16x32_bf16 v[208:211], v[88:91], v[148:151], v[208:211]\n\t"
      "s_waitcnt lgkmcnt(0)\n\t"
      "s_barrier\n\t"
      "s_setprio 2\n\t"
      "v_mfma_f32_16x16x32_bf16 v[212:215], v[76:79], v[144:147], v[212:215]\n\t"
      "global_load_dwordx4 v[144:147], v180, s[64:65] sc1\n\t"
      "v_mfma_f32_16x16x32_bf16 v[212:215], v[92:95], v[148:151], v[212:215]\n\t"
      "global_load_dwordx4 v[148:151], v181, s[64:65] sc1\n\t"
      "s_waitcnt vmcnt(7)\n\t"
      "v_mfma_f32_16x16x32_bf16 v[200:203], v[96:99], v[152:155], v[200:203]\n\t"
      "s_waitcnt vmcnt(6)\n\t"
      "v_mfma_f32_16x16x32_bf16 v[200:203], v[112:115], v[156:159], v[200:203]\n\t"
      "v_mfma_f32_16x16x32_bf16 v[204:207], v[100:103], v[152:155], v[204:207]\n\t"
      "v_mfma_f32_16x16x32_bf16 v[204:207], v[116:119], v[156:159], v[204:207]\n\t"
      "v_mfma_f32_16x16x32_bf16 v[208:211], v[104:107], v[152:155], v[208:211]\n\t"
      "v_mfma_f32_16x16x32_bf16 v[208:211], v[120:123], v[156:159], v[208:211]\n\t"
      "v_mfma_f32_16x16x32_bf16 v[212:215], v[108:111], v[152:155], v[212:215]\n\t"
      "global_load_dwordx4 v[152:155], v182, s[64:65] sc1\n\t"
      "v_mfma_f32_16x16x32_bf16 v[212:215], v[124:127], v[156:159], v[212:215]\n\t"
      "global_load_dwordx4 v[156:159], v183, s[64:65] sc1\n\t"
      "s_setprio 0\n\t"
      "v_cmp_u_f32_e32 vcc, v200, v200\n\t"
      "s_cbranch_vccnz NQ5R_%=\n\t"
      "NQ5B_%=:\n\t"
      "ds_write_b128 v243, v[200:203] offset:32768\n\t"
      "ds_write_b128 v164, v[204:207] offset:32768\n\t"
      "ds_write_b128 v165, v[208:211] offset:32768\n\t"
      "s_nop 0\n\t"
      "ds_write_b128 v166, v[212:215] offset:32768\n\t"
      "s_waitcnt vmcnt(7)\n\t"
      "v_mfma_f32_16x16x32_bf16 v[184:187], v[0:3], v[128:131], v[236:239]\n\t"
      "s_waitcnt vmcnt(6)\n\t"
      "v_mfma_f32_16x16x32_bf16 v[184:187], v[16:19], v[132:135], v[184:187]\n\t"
      "v_mfma_f32_16x16x32_bf16 v[188:191], v[4:7], v[128:131], 0\n\t"
      "v_mfma_f32_16x16x32_bf16 v[188:191], v[20:23], v[132:135], v[188:191]\n\t"
      "v_mfma_f32_16x16x32_bf16 v[192:195], v[8:11], v[128:131], 0\n\t"
      "v_mfma_f32_16x16x32_bf16 v[192:195], v[24:27], v[132:135], v[192:195]\n\t"
      "v_mfma_f32_16x16x32_bf16 v[196:199], v[12:15], v[128:131], 0\n\t"
      "v_mfma_f32_16x16x32_bf16 v[196:199], v[28:31], v[132:135], v[196:199]\n\t"
      "s_waitcnt vmcnt(5)\n\t"
      "v_mfma_f32_16x16x32_bf16 v[184:187], v[32:35], v[136:139], v[184:187]\n\t"
      "s_waitcnt vmcnt(4)\n\t"
      "v_mfma_f32_16x16x32_bf16 v[184:187], v[48:51], v[140:143], v[184:187]\n\t"
      "v_mfma_f32_16x16x32_bf16 v[188:191], v[36:39], v[136:139], v[188:191]\n\t"
      "v_mfma_f32_16x16x32_bf16 v[188:191], v[52:55], v[140:143], v[188:191]\n\t"
      "v_mfma_f32_16x16x32_bf16 v[192:195], v[40:43], v[136:139], v[192:195]\n\t"
      "v_mfma_f32_16x16x32_bf16 v[192:195], v[56:59], v[140:143], v[192:195]\n\t"
      "v_mfma_f32_16x16x32_bf16 v[196:199], v[44:47], v[136:139], v[196:199]\n\t"
      "v_mfma_f32_16x16x32_bf16 v[196:199], v[60:63], v[140:143], v[196:199]\n\t"
      "s_waitcnt vmcnt(3)\n\t"
      "v_mfma_f32_16x16x32_bf16 v[184:187], v[64:67], v[144:147], v[184:187]\n\t"
      "s_waitcnt vmcnt(2)\n\t"
      "v_mfma_f32_16x16x32_bf16 v[184:187], v[80:83], v[148:151], v[184:187]\n\t"
      "v_mfma_f32_16x16x32_bf16 v[188:191], v[68:71], v[144:147], v[188:191]\n\t"
      "v_mfma_f32_16x16x32_bf16 v[188:191], v[84:87], v[148:151], v[188:191]\n\t"
      "v_mfma_f32_16x16x32_bf16 v[192:195], v[72:75], v[144:147], v[192:195]\n\t"
      "v_mfma_f32_16x16x32_bf16 v[192:195], v[88:91], v[148:151], v[192:195]\n\t"
      "s_waitcnt lgkmcnt(0)\n\t"
      "s_barrier\n\t"
      "s_setprio 2\n\t"
      "v_mfma_f32_16x16x32_bf16 v[196:199], v[76:79], v[144:147], v[196:199]\n\t"
      "v_mfma_f32_16x16x32_bf16 v[196:199], v[92:95], v[148:151], v[196:199]\n\t"
      "s_waitcnt vmcnt(1)\n\t"
      "v_mfma_f32_16x16x32_bf16 v[184:187], v[96:99], v[152:155], v[184:187]\n\t"
      "s_waitcnt vmcnt(0)\n\t"
      "v_mfma_f32_16x16x32_bf16 v[184:187], v[112:115], v[156:159], v[184:187]\n\t"
      "v_mfma_f32_16x16x32_bf16 v[188:191], v[100:103], v[152:155], v[188:191]\n\t"
      "v_mfma_f32_16x16x32_bf16 v[188:191], v[116:119], v[156:159], v[188:191]\n\t"
      "v_mfma_f32_16x16x32_bf16 v[192:195], v[104:107], v[152:155], v[192:195]\n\t"
      "v_mfma_f32_16x16x32_bf16 v[192:195], v[120:123], v[156:159], v[192:195]\n\t"
      "v_mfma_f32_16x16x32_bf16 v[196:199], v[108:111], v[152:155], v[196:199]\n\t"
      "v_mfma_f32_16x16x32_bf16 v[196:199], v[124:127], v[156:159], v[196:199]\n\t"
      "s_nop 1\n\t"
      "v_cmp_u_f32_e32 vcc, v184, v184\n\t"
      "s_cbranch_vccnz NQ6R_%=\n\t"
      "NQ6B_%=:\n\t"
      "ds_write_b128 v243, v[184:187]\n\t"
      "ds_write_b128 v164, v[188:191]\n\t"
      "ds_write_b128 v165, v[192:195]\n\t"
      "s_nop 0\n\t"
      "ds_write_b128 v166, v[196:199]\n\t"
      "s_waitcnt lgkmcnt(0)\n\t"
      "s_barrier\n\t"
      "s_branch GX_%=\n\t"
      "ASP_%=:\n\t"
      "s_mov_b32 m0, 163328\n\t"
      "s_cmp_ge_u32 s17, 65536\n\t"
      "s_cbranch_scc0 BOT_%=\n\t"
      "s_barrier\n\t"
      "BOT_%=:\n\t"
      "v_add_u32_e32 v1, 229376, v241\n\t"
      "v_add_u32_e32 v2, 32768, v246\n\t"
      "global_load_lds_dword v241, s[4:5]\n\t"
      "global_load_lds_dword v1, s[4:5]\n\t"
      "s_waitcnt vmcnt(0)\n\t"
      "s_sleep 1\n\t"
      "s_getpc_b64 s[44:45]\n\t"
      "s_add_u32 s44, s44, _Z11fused_blockPKcS0_S0_S0_S0_S0_S0_PcS1_@rel32@lo+4\n\t"
      "s_addc_u32 s45, s45, _Z11fused_blockPKcS0_S0_S0_S0_S0_S0_PcS1_@rel32@hi+12\n\t"
      "s_lshr_b32 s46, s17, 8\n\t"
      "v_lshrrev_b32_e32 v0, 4, v240\n\t"
      "v_add_u32_e32 v0, s46, v0\n\t"
      "v_min_u32_e32 v0, 469, v0\n\t"
      "v_lshlrev_b32_e32 v0, 7, v0\n\t"
      "global_load_lds_dword v0, s[44:45] sc1\n\t"
      "s_branch AEND_%=\n\t"
      "FQ0R_%=:\n\t"
      "s_mov_b32 s51, 0\n\t"
      "FQ0L_%=:\n\t"
      "s_add_u32 s51, s51, 1\n\t"
      "s_cmp_lt_u32 s51, 262144\n\t"
      "s_cbranch_scc0 FQ0X_%=\n\t"
      "s_waitcnt vmcnt(0)\n\t"
      "s_sleep 1\n\t"
      "global_load_dwordx4 v[128:131], v176, s[52:53] sc1\n\t"
      "global_load_dwordx4 v[132:135], v177, s[52:53] sc1\n\t"
      "global_load_dwordx4 v[136:139], v178, s[52:53] sc1\n\t"
      "global_load_dwordx4 v[140:143], v179, s[52:53] sc1\n\t"
      "global_load_dwordx4 v[144:147], v180, s[52:53] sc1\n\t"
      "global_load_dwordx4 v[148:151], v181, s[52:53] sc1\n\t"
      "global_load_dwordx4 v[152:155], v182, s[52:53] sc1\n\t"
      "global_load_dwordx4 v[156:159], v183, s[52:53] sc1\n\t"
      "s_waitcnt vmcnt(7)\n\t"
      "v_max3_u32 v234, v128, v129, v130\n\t"
      "v_max_u32_e32 v234, v234, v131\n\t"
      "v_cmp_eq_u32_e32 vcc, -1, v234\n\t"
      "s_cbranch_vccnz FQ0L_%=\n\t"
      "FQ0X_%=:\n\t"
      "s_branch FQ0B_%=\n\t"
      "FQ1R_%=:\n\t"
      "s_mov_b32 s51, 0\n\t"
      "FQ1L_%=:\n\t"
      "s_add_u32 s51, s51, 1\n\t"
      "s_cmp_lt_u32 s51, 262144\n\t"
      "s_cbranch_scc0 FQ1X_%=\n\t"
      "s_waitcnt vmcnt(0)\n\t"
      "s_sleep 1\n\t"
      "global_load_dwordx4 v[132:135], v177, s[52:53] sc1\n\t"
      "global_load_dwordx4 v[136:139], v178, s[52:53] sc1\n\t"
      "global_load_dwordx4 v[140:143], v179, s[52:53] sc1\n\t"
      "global_load_dwordx4 v[144:147], v180, s[52:53] sc1\n\t"
      "global_load_dwordx4 v[148:151], v181, s[52:53] sc1\n\t"
      "global_load_dwordx4 v[152:155], v182, s[52:53] sc1\n\t"
      "global_load_dwordx4 v[156:159], v183, s[52:53] sc1\n\t"
      "s_waitcnt vmcnt(6)\n\t"
      "v_max3_u32 v234, v132, v133, v134\n\t"
      "v_max_u32_e32 v234, v234, v135\n\t"
      "v_cmp_eq_u32_e32 vcc, -1, v234\n\t"
      "s_cbranch_vccnz FQ1L_%=\n\t"
      "FQ1X_%=:\n\t"
      "s_branch FQ1B_%=\n\t"
      "FQ2R_%=:\n\t"
      "s_mov_b32 s51, 0\n\t"
      "FQ2L_%=:\n\t"
      "s_add_u32 s51, s51, 1\n\t"
      "s_cmp_lt_u32 s51, 262144\n\t"
      "s_cbranch_scc0 FQ2X_%=\n\t"
      "s_waitcnt vmcnt(0)\n\t"
      "s_sleep 1\n\t"
      "global_load_dwordx4 v[136:139], v178, s[52:53] sc1\n\t"
      "global_load_dwordx4 v[140:143], v179, s[52:53] sc1\n\t"
      "global_load_dwordx4 v[144:147], v180, s[52:53] sc1\n\t"
      "global_load_dwordx4 v[148:151], v181, s[52:53] sc1\n\t"
      "global_load_dwordx4 v[152:155], v182, s[52:53] sc1\n\t"
      "global_load_dwordx4 v[156:159], v183, s[52:53] sc1\n\t"
      "global_load_dwordx4 v[128:131], v176, s[54:55] sc1\n\t"
      "global_load_dwordx4 v[132:135], v177, s[54:55] sc1\n\t"
      "s_waitcnt vmcnt(7)\n\t"
      "v_max3_u32 v234, v136, v137, v138\n\t"
      "v_max_u32_e32 v234, v234, v139\n\t"
      "v_cmp_eq_u32_e32 vcc, -1, v234\n\t"
      "s_cbranch_vccnz FQ2L_%=\n\t"
      "FQ2X_%=:\n\t"
      "s_branch FQ2B_%=\n\t"
      "FQ3R_%=:\n\t"
      "s_mov_b32 s51, 0\n\t"
      "FQ3L_%=:\n\t"
      "s_add_u32 s51, s51, 1\n\t"
      "s_cmp_lt_u32 s51, 262144\n\t"
      "s_cbranch_scc0 FQ3X_%=\n\t"
      "s_waitcnt vmcnt(0)\n\t"
      "s_sleep 1\n\t"
      "global_load_dwordx4 v[140:143], v179, s[52:53] sc1\n\t"
      "global_load_dwordx4 v[144:147], v180, s[52:53] sc1\n\t"
      "global_load_dwordx4 v[148:151], v181, s[52:53] sc1\n\t"
      "global_load_dwordx4 v[152:155], v182, s[52:53] sc1\n\t"
      "global_load_dwordx4 v[156:159], v183, s[52:53] sc1\n\t"
      "global_load_dwordx4 v[128:131], v176, s[54:55] sc1\n\t"
      "global_load_dwordx4 v[132:135], v177, s[54:55] sc1\n\t"
      "s_waitcnt vmcnt(6)\n\t"
      "v_max3_u32 v234, v140, v141, v142\n\t"
      "v_max_u32_e32 v234, v234, v143\n\t"
      "v_cmp_eq_u32_e32 vcc, -1, v234\n\t"
      "s_cbranch_vccnz FQ3L_%=\n\t"
      "FQ3X_%=:\n\t"
      "s_branch FQ3B_%=\n\t"
      "FQ4R_%=:\n\t"
      "s_mov_b32 s51, 0\n\t"
      "FQ4L_%=:\n\t"
      "s_add_u32 s51, s51, 1\n\t"
      "s_cmp_lt_u32 s51, 262144\n\t"
      "s_cbranch_scc0 FQ4X_%=\n\t"
      "s_waitcnt vmcnt(0)\n\t"
      "s_sleep 1\n\t"
      "global_load_dwordx4 v[144:147], v180, s[52:53] sc1\n\t"
      "global_load_dwordx4 v[148:151], v181, s[52:53] sc1\n\t"
      "global_load_dwordx4 v[152:155], v182, s[52:53] sc1\n\t"
      "global_load_dwordx4 v[156:159], v183, s[52:53] sc1\n\t"
      "global_load_dwordx4 v[128:131], v176, s[54:55] sc1\n\t"
      "global_load_dwordx4 v[132:135], v177, s[54:55] sc1\n\t"
      "global_load_dwordx4 v[136:139], v178, s[54:55] sc1\n\t"
      "global_load_dwordx4 v[140:143], v179, s[54:55] sc1\n\t"
      "s_waitcnt vmcnt(7)\n\t"
      "v_max3_u32 v234, v144, v145, v146\n\t"
      "v_max_u32_e32 v234, v234, v147\n\t"
      "v_cmp_eq_u32_e32 vcc, -1, v234\n\t"
      "s_cbranch_vccnz FQ4L_%=\n\t"
      "FQ4X_%=:\n\t"
      "s_branch FQ4B_%=\n\t"
      "FQ5R_%=:\n\t"
      "s_mov_b32 s51, 0\n\t"
      "FQ5L_%=:\n\t"
      "s_add_u32 s51, s51, 1\n\t"
      "s_cmp_lt_u32 s51, 262144\n\t"
      "s_cbranch_scc0 FQ5X_%=\n\t"
      "s_waitcnt vmcnt(0)\n\t"
      "s_sleep 1\n\t"
      "global_load_dwordx4 v[148:151], v181, s[52:53] sc1\n\t"
      "global_load_dwordx4 v[152:155], v182, s[52:53] sc1\n\t"
      "global_load_dwordx4 v[156:159], v183, s[52:53] sc1\n\t"
      "global_load_dwordx4 v[128:131], v176, s[54:55] sc1\n\t"
      "global_load_dwordx4 v[132:135], v177, s[54:55] sc1\n\t"
      "global_load_dwordx4 v[136:139], v178, s[54:55] sc1\n\t"
      "global_load_dwordx4 v[140:143], v179, s[54:55] sc1\n\t"
      "s_waitcnt vmcnt(6)\n\t"
      "v_max3_u32 v234, v148, v149, v150\n\t"
      "v_max_u32_e32 v234, v234, v151\n\t"
      "v_cmp_eq_u32_e32 vcc, -1, v234\n\t"
      "s_cbranch_vccnz FQ5L_%=\n\t"
      "FQ5X_%=:\n\t"
      "s_branch FQ5B_%=\n\t"
      "FQ6R_%=:\n\t"
      "s_mov_b32 s51, 0\n\t"
      "FQ6L_%=:\n\t"
      "s_add_u32 s51, s51, 1\n\t"
      "s_cmp_lt_u32 s51, 262144\n\t"
      "s_cbranch_scc0 FQ6X_%=\n\t"
      "s_waitcnt vmcnt(0)\n\t"
      "s_sleep 1\n\t"
      "global_load_dwordx4 v[152:155], v182, s[52:53] sc1\n\t"
      "global_load_dwordx4 v[156:159], v183, s[52:53] sc1\n\t"
      "global_load_dwordx4 v[128:131], v176, s[54:55] sc1\n\t"
      "global_load_dwordx4 v[132:135], v177, s[54:55] sc1\n\t"
      "global_load_dwordx4 v[136:139], v178, s[54:55] sc1\n\t"
      "global_load_dwordx4 v[140:143], v179, s[54:55] sc1\n\t"
      "global_load_dwordx4 v[144:147], v180, s[54:55] sc1\n\t"
      "global_load_dwordx4 v[148:151], v181, s[54:55] sc1\n\t"
      "s_waitcnt vmcnt(7)\n\t"
      "v_max3_u32 v234, v152, v153, v154\n\t"
      "v_max_u32_e32 v234, v234, v155\n\t"
      "v_cmp_eq_u32_e32 vcc, -1, v234\n\t"
      "s_cbranch_vccnz FQ6L_%=\n\t"
      "FQ6X_%=:\n\t"
      "s_branch FQ6B_%=\n\t"
      "FQ7R_%=:\n\t"
      "s_mov_b32 s51, 0\n\t"
      "FQ7L_%=:\n\t"
      "s_add_u32 s51, s51, 1\n\t"
      "s_cmp_lt_u32 s51, 262144\n\t"
      "s_cbranch_scc0 FQ7X_%=\n\t"
      "s_waitcnt vmcnt(0)\n\t"
      "s_sleep 1\n\t"
      "global_load_dwordx4 v[156:159], v183, s[52:53] sc1\n\t"
      "global_load_dwordx4 v[128:131], v176, s[54:55] sc1\n\t"
      "global_load_dwordx4 v[132:135], v177, s[54:55] sc1\n\t"
      "global_load_dwordx4 v[136:139], v178, s[54:55] sc1\n\t"
      "global_load_dwordx4 v[140:143], v179, s[54:55] sc1\n\t"
      "global_load_dwordx4 v[144:147], v180, s[54:55] sc1\n\t"
      "global_load_dwordx4 v[148:151], v181, s[54:55] sc1\n\t"
      "s_waitcnt vmcnt(6)\n\t"
      "v_max3_u32 v234, v156, v157, v158\n\t"
      "v_max_u32_e32 v234, v234, v159\n\t"
      "v_cmp_eq_u32_e32 vcc, -1, v234\n\t"
      "s_cbranch_vccnz FQ7L_%=\n\t"
      "FQ7X_%=:\n\t"
      "s_branch FQ7B_%=\n\t"
      "NQ1R_%=:\n\t"
      "s_mov_b32 s51, 0\n\t"
      "NQ1L_%=:\n\t"
      "s_add_u32 s51, s51, 1\n\t"
      "s_cmp_lt_u32 s51, 262144\n\t"
      "s_cbranch_scc0 NQ1X_%=\n\t"
      "s_waitcnt vmcnt(0)\n\t"
      "s_sleep 1\n\t"
      "global_load_dwordx4 v[128:131], v176, s[54:55] sc1\n\t"
      "global_load_dwordx4 v[132:135], v177, s[54:55] sc1\n\t"
      "global_load_dwordx4 v[136:139], v178, s[54:55] sc1\n\t"
      "global_load_dwordx4 v[140:143], v179, s[54:55] sc1\n\t"
      "global_load_dwordx4 v[144:147], v180, s[54:55] sc1\n\t"
      "global_load_dwordx4 v[148:151], v181, s[54:55] sc1\n\t"
      "global_load_dwordx4 v[152:155], v182, s[54:55] sc1\n\t"
      "global_load_dwordx4 v[156:159], v183, s[54:55] sc1\n\t"
      "s_waitcnt vmcnt(0)\n\t"
      "v_max3_u32 v234, v128, v129, v130\n\t"
      "v_max3_u32 v234, v234, v131, v132\n\t"
      "v_max3_u32 v234, v234, v133, v134\n\t"
      "v_max3_u32 v234, v234, v135, v136\n\t"
      "v_max3_u32 v234, v234, v137, v138\n\t"
      "v_max3_u32 v234, v234, v139, v140\n\t"
      "v_max3_u32 v234, v234, v141, v142\n\t"
      "v_max3_u32 v234, v234, v143, v144\n\t"
      "v_max3_u32 v234, v234, v145, v146\n\t"
      "v_max3_u32 v234, v234, v147, v148\n\t"
      "v_max3_u32 v234, v234, v149, v150\n\t"
      "v_max3_u32 v234, v234, v151, v152\n\t"
      "v_max3_u32 v234, v234, v153, v154\n\t"
      "v_max3_u32 v234, v234, v155, v156\n\t"
      "v_max3_u32 v234, v234, v157, v158\n\t"
      "v_max_u32_e32 v234, v234, v159\n\t"
      "v_cmp_eq_u32_e32 vcc, -1, v234\n\t"
      "s_cbranch_vccnz NQ1L_%=\n\t"
      "NQ1X_%=:\n\t"
      "v_mfma_f32_16x16x32_bf16 v[200:203], v[0:3], v[128:131], v[236:239]\n\t"
      "v_mfma_f32_16x16x32_bf16 v[204:207], v[4:7], v[128:131], 0\n\t"
      "v_mfma_f32_16x16x32_bf16 v[208:211], v[8:11], v[128:131], 0\n\t"
      "v_mfma_f32_16x16x32_bf16 v[212:215], v[12:15], v[128:131], 0\n\t"
      "v_mfma_f32_16x16x32_bf16 v[200:203], v[16:19], v[132:135], v[200:203]\n\t"
      "v_mfma_f32_16x16x32_bf16 v[204:207], v[20:23], v[132:135], v[204:207]\n\t"
      "v_mfma_f32_16x16x32_bf16 v[208:211], v[24:27], v[132:135], v[208:211]\n\t"
      "v_mfma_f32_16x16x32_bf16 v[212:215], v[28:31], v[132:135], v[212:215]\n\t"
      "v_mfma_f32_16x16x32_bf16 v[200:203], v[32:35], v[136:139], v[200:203]\n\t"
      "v_mfma_f32_16x16x32_bf16 v[204:207], v[36:39], v[136:139], v[204:207]\n\t"
      "v_mfma_f32_16x16x32_bf16 v[208:211], v[40:43], v[136:139], v[208:211]\n\t"
      "v_mfma_f32_16x16x32_bf16 v[212:215], v[44:47], v[136:139], v[212:215]\n\t"
      "v_mfma_f32_16x16x32_bf16 v[200:203], v[48:51], v[140:143], v[200:203]\n\t"
      "v_mfma_f32_16x16x32_bf16 v[204:207], v[52:55], v[140:143], v[204:207]\n\t"
      "v_mfma_f32_16x16x32_bf16 v[208:211], v[56:59], v[140:143], v[208:211]\n\t"
      "v_mfma_f32_16x16x32_bf16 v[212:215], v[60:63], v[140:143], v[212:215]\n\t"
      "v_mfma_f32_16x16x32_bf16 v[200:203], v[64:67], v[144:147], v[200:203]\n\t"
      "v_mfma_f32_16x16x32_bf16 v[204:207], v[68:71], v[144:147], v[204:207]\n\t"
      "v_mfma_f32_16x16x32_bf16 v[208:211], v[72:75], v[144:147], v[208:211]\n\t"
      "v_mfma_f32_16x16x32_bf16 v[212:215], v[76:79], v[144:147], v[212:215]\n\t"
      "v_mfma_f32_16x16x32_bf16 v[200:203], v[80:83], v[148:151], v[200:203]\n\t"
      "v_mfma_f32_16x16x32_bf16 v[204:207], v[84:87], v[148:151], v[204:207]\n\t"
      "v_mfma_f32_16x16x32_bf16 v[208:211], v[88:91], v[148:151], v[208:211]\n\t"
      "v_mfma_f32_16x16x32_bf16 v[212:215], v[92:95], v[148:151], v[212:215]\n\t"
      "v_mfma_f32_16x16x32_bf16 v[200:203], v[96:99], v[152:155], v[200:203]\n\t"
      "v_mfma_f32_16x16x32_bf16 v[204:207], v[100:103], v[152:155], v[204:207]\n\t"
      "v_mfma_f32_16x16x32_bf16 v[208:211], v[104:107], v[152:155], v[208:211]\n\t"
      "v_mfma_f32_16x16x32_bf16 v[212:215], v[108:111], v[152:155], v[212:215]\n\t"
      "v_mfma_f32_16x16x32_bf16 v[200:203], v[112:115], v[156:159], v[200:203]\n\t"
      "v_mfma_f32_16x16x32_bf16 v[204:207], v[116:119], v[156:159], v[204:207]\n\t"
      "v_mfma_f32_16x16x32_bf16 v[208:211], v[120:123], v[156:159], v[208:211]\n\t"
      "v_mfma_f32_16x16x32_bf16 v[212:215], v[124:127], v[156:159], v[212:215]\n\t"
      "global_load_dwordx4 v[128:131], v176, s[56:57] sc1\n\t"
      "global_load_dwordx4 v[132:135], v177, s[56:57] sc1\n\t"
      "global_load_dwordx4 v[136:139], v178, s[56:57] sc1\n\t"
      "global_load_dwordx4 v[140:143], v179, s[56:57] sc1\n\t"
      "global_load_dwordx4 v[144:147], v180, s[56:57] sc1\n\t"
      "global_load_dwordx4 v[148:151], v181, s[56:57] sc1\n\t"
      "global_load_dwordx4 v[152:155], v182, s[56:57] sc1\n\t"
      "global_load_dwordx4 v[156:159], v183, s[56:57] sc1\n\t"
      "s_waitcnt vmcnt(0)\n\t"
      "s_nop 7\n\t"
      "s_branch NQ1B_%=\n\t"
      "NQ2R_%=:\n\t"
      "s_mov_b32 s51, 0\n\t"
      "NQ2L_%=:\n\t"
      "s_add_u32 s51, s51, 1\n\t"
      "s_cmp_lt_u32 s51, 262144\n\t"
      "s_cbranch_scc0 NQ2X_%=\n\t"
      "s_waitcnt vmcnt(0)\n\t"
      "s_sleep 1\n\t"
      "global_load_dwordx4 v[128:131], v176, s[56:57] sc1\n\t"
      "global_load_dwordx4 v[132:135], v177, s[56:57] sc1\n\t"
      "global_load_dwordx4 v[136:139], v178, s[56:57] sc1\n\t"
      "global_load_dwordx4 v[140:143], v179, s[56:57] sc1\n\t"
      "global_load_dwordx4 v[144:147], v180, s[56:57] sc1\n\t"
      "global_load_dwordx4 v[148:151], v181, s[56:57] sc1\n\t"
      "global_load_dwordx4 v[152:155], v182, s[56:57] sc1\n\t"
      "global_load_dwordx4 v[156:159], v183, s[56:57] sc1\n\t"
      "s_waitcnt vmcnt(0)\n\t"
      "v_max3_u32 v234, v128, v129, v130\n\t"
      "v_max3_u32 v234, v234, v131, v132\n\t"
      "v_max3_u32 v234, v234, v133, v134\n\t"
      "v_max3_u32 v234, v234, v135, v136\n\t"
      "v_max3_u32 v234, v234, v137, v138\n\t"
      "v_max3_u32 v234, v234, v139, v140\n\t"
      "v_max3_u32 v234, v234, v141, v142\n\t"
      "v_max3_u32 v234, v234, v143, v144\n\t"
      "v_max3_u32 v234, v234, v145, v146\n\t"
      "v_max3_u32 v234, v234, v147, v148\n\t"
      "v_max3_u32 v234, v234, v149, v150\n\t"
      "v_max3_u32 v234, v234, v151, v152\n\t"
      "v_max3_u32 v234, v234, v153, v154\n\t"
      "v_max3_u32 v234, v234, v155, v156\n\t"
      "v_max3_u32 v234, v234, v157, v158\n\t"
      "v_max_u32_e32 v234, v234, v159\n\t"
      "v_cmp_eq_u32_e32 vcc, -1, v234\n\t"
      "s_cbranch_vccnz NQ2L_%=\n\t"
      "NQ2X_%=:\n\t"
      "v_mfma_f32_16x16x32_bf16 v[184:187], v[0:3], v[128:131], v[236:239]\n\t"
      "v_mfma_f32_16x16x32_bf16 v[188:191], v[4:7], v[128:131], 0\n\t"
      "v_mfma_f32_16x16x32_bf16 v[192:195], v[8:11], v[128:131], 0\n\t"
      "v_mfma_f32_16x16x32_bf16 v[196:199], v[12:15], v[128:131], 0\n\t"
      "v_mfma_f32_16x16x32_bf16 v[184:187], v[16:19], v[132:135], v[184:187]\n\t"
      "v_mfma_f32_16x16x32_bf16 v[188:191], v[20:23], v[132:135], v[188:191]\n\t"
      "v_mfma_f32_16x16x32_bf16 v[192:195], v[24:27], v[132:135], v[192:195]\n\t"
      "v_mfma_f32_16x16x32_bf16 v[196:199], v[28:31], v[132:135], v[196:199]\n\t"
      "v_mfma_f32_16x16x32_bf16 v[184:187], v[32:35], v[136:139], v[184:187]\n\t"
      "v_mfma_f32_16x16x32_bf16 v[188:191], v[36:39], v[136:139], v[188:191]\n\t"
      "v_mfma_f32_16x16x32_bf16 v[192:195], v[40:43], v[136:139], v[192:195]\n\t"
      "v_mfma_f32_16x16x32_bf16 v[196:199], v[44:47], v[136:139], v[196:199]\n\t"
      "v_mfma_f32_16x16x32_bf16 v[184:187], v[48:51], v[140:143], v[184:187]\n\t"
      "v_mfma_f32_16x16x32_bf16 v[188:191], v[52:55], v[140:143], v[188:191]\n\t"
      "v_mfma_f32_16x16x32_bf16 v[192:195], v[56:59], v[140:143], v[192:195]\n\t"
      "v_mfma_f32_16x16x32_bf16 v[196:199], v[60:63], v[140:143], v[196:199]\n\t"
      "v_mfma_f32_16x16x32_bf16 v[184:187], v[64:67], v[144:147], v[184:187]\n\t"
      "v_mfma_f32_16x16x32_bf16 v[188:191], v[68:71], v[144:147], v[188:191]\n\t"
      "v_mfma_f32_16x16x32_bf16 v[192:195], v[72:75], v[144:147], v[192:195]\n\t"
      "v_mfma_f32_16x16x32_bf16 v[196:199], v[76:79], v[144:147], v[196:199]\n\t"
      "v_mfma_f32_16x16x32_bf16 v[184:187], v[80:83], v[148:151], v[184:187]\n\t"
      "v_mfma_f32_16x16x32_bf16 v[188:191], v[84:87], v[148:151], v[188:191]\n\t"
      "v_mfma_f32_16x16x32_bf16 v[192:195], v[88:91], v[148:151], v[192:195]\n\t"
      "v_mfma_f32_16x16x32_bf16 v[196:199], v[92:95], v[148:151], v[196:199]\n\t"
      "v_mfma_f32_16x16x32_bf16 v[184:187], v[96:99], v[152:155], v[184:187]\n\t"
      "v_mfma_f32_16x16x32_bf16 v[188:191], v[100:103], v[152:155], v[188:191]\n\t"
      "v_mfma_f32_16x16x32_bf16 v[192:195], v[104:107], v[152:155], v[192:195]\n\t"
      "v_mfma_f32_16x16x32_bf16 v[196:199], v[108:111], v[152:155], v[196:199]\n\t"
      "v_mfma_f32_16x16x32_bf16 v[184:187], v[112:115], v[156:159], v[184:187]\n\t"
      "v_mfma_f32_16x16x32_bf16 v[188:191], v[116:119], v[156:159], v[188:191]\n\t"
      "v_mfma_f32_16x16x32_bf16 v[192:195], v[120:123], v[156:159], v[192:195]\n\t"
      "v_mfma_f32_16x16x32_bf16 v[196:199], v[124:127], v[156:159], v[196:199]\n\t"
      "global_load_dwordx4 v[128:131], v176, s[58:59] sc1\n\t"
      "global_load_dwordx4 v[132:135], v177, s[58:59] sc1\n\t"
      "global_load_dwordx4 v[136:139], v178, s[58:59] sc1\n\t"
      "global_load_dwordx4 v[140:143], v179, s[58:59] sc1\n\t"
      "global_load_dwordx4 v[144:147], v180, s[58:59] sc1\n\t"
      "global_load_dwordx4 v[148:151], v181, s[58:59] sc1\n\t"
      "global_load_dwordx4 v[152:155], v182, s[58:59] sc1\n\t"
      "global_load_dwordx4 v[156:159], v183, s[58:59] sc1\n\t"
      "s_waitcnt vmcnt(0)\n\t"
      "s_nop 7\n\t"
      "s_branch NQ2B_%=\n\t"
      "NQ3R_%=:\n\t"
      "s_mov_b32 s51, 0\n\t"
      "NQ3L_%=:\n\t"
      "s_add_u32 s51, s51, 1\n\t"
      "s_cmp_lt_u32 s51, 262144\n\t"
      "s_cbranch_scc0 NQ3X_%=\n\t"
      "s_waitcnt vmcnt(0)\n\t"
      "s_sleep 1\n\t"
      "global_load_dwordx4 v[128:131], v176, s[58:59] sc1\n\t"
      "global_load_dwordx4 v[132:135], v177, s[58:59] sc1\n\t"
      "global_load_dwordx4 v[136:139], v178, s[58:59] sc1\n\t"
      "global_load_dwordx4 v[140:143], v179, s[58:59] sc1\n\t"
      "global_load_dwordx4 v[144:147], v180, s[58:59] sc1\n\t"
      "global_load_dwordx4 v[148:151], v181, s[58:59] sc1\n\t"
      "global_load_dwordx4 v[152:155], v182, s[58:59] sc1\n\t"
      "global_load_dwordx4 v[156:159], v183, s[58:59] sc1\n\t"
      "s_waitcnt vmcnt(0)\n\t"
      "v_max3_u32 v234, v128, v129, v130\n\t"
      "v_max3_u32 v234, v234, v131, v132\n\t"
      "v_max3_u32 v234, v234, v133, v134\n\t"
      "v_max3_u32 v234, v234, v135, v136\n\t"
      "v_max3_u32 v234, v234, v137, v138\n\t"
      "v_max3_u32 v234, v234, v139, v140\n\t"
      "v_max3_u32 v234, v234, v141, v142\n\t"
      "v_max3_u32 v234, v234, v143, v144\n\t"
      "v_max3_u32 v234, v234, v145, v146\n\t"
      "v_max3_u32 v234, v234, v147, v148\n\t"
      "v_max3_u32 v234, v234, v149, v150\n\t"
      "v_max3_u32 v234, v234, v151, v152\n\t"
      "v_max3_u32 v234, v234, v153, v154\n\t"
      "v_max3_u32 v234, v234, v155, v156\n\t"
      "v_max3_u32 v234, v234, v157, v158\n\t"
      "v_max_u32_e32 v234, v234, v159\n\t"
      "v_cmp_eq_u32_e32 vcc, -1, v234\n\t"
      "s_cbranch_vccnz NQ3L_%=\n\t"
      "NQ3X_%=:\n\t"
      "v_mfma_f32_16x16x32_bf16 v[200:203], v[0:3], v[128:131], v[236:239]\n\t"
      "v_mfma_f32_16x16x32_bf16 v[204:207], v[4:7], v[128:131], 0\n\t"
      "v_mfma_f32_16x16x32_bf16 v[208:211], v[8:11], v[128:131], 0\n\t"
      "v_mfma_f32_16x16x32_bf16 v[212:215], v[12:15], v[128:131], 0\n\t"
      "v_mfma_f32_16x16x32_bf16 v[200:203], v[16:19], v[132:135], v[200:203]\n\t"
      "v_mfma_f32_16x16x32_bf16 v[204:207], v[20:23], v[132:135], v[204:207]\n\t"
      "v_mfma_f32_16x16x32_bf16 v[208:211], v[24:27], v[132:135], v[208:211]\n\t"
      "v_mfma_f32_16x16x32_bf16 v[212:215], v[28:31], v[132:135], v[212:215]\n\t"
      "v_mfma_f32_16x16x32_bf16 v[200:203], v[32:35], v[136:139], v[200:203]\n\t"
      "v_mfma_f32_16x16x32_bf16 v[204:207], v[36:39], v[136:139], v[204:207]\n\t"
      "v_mfma_f32_16x16x32_bf16 v[208:211], v[40:43], v[136:139], v[208:211]\n\t"
      "v_mfma_f32_16x16x32_bf16 v[212:215], v[44:47], v[136:139], v[212:215]\n\t"
      "v_mfma_f32_16x16x32_bf16 v[200:203], v[48:51], v[140:143], v[200:203]\n\t"
      "v_mfma_f32_16x16x32_bf16 v[204:207], v[52:55], v[140:143], v[204:207]\n\t"
      "v_mfma_f32_16x16x32_bf16 v[208:211], v[56:59], v[140:143], v[208:211]\n\t"
      "v_mfma_f32_16x16x32_bf16 v[212:215], v[60:63], v[140:143], v[212:215]\n\t"
      "v_mfma_f32_16x16x32_bf16 v[200:203], v[64:67], v[144:147], v[200:203]\n\t"
      "v_mfma_f32_16x16x32_bf16 v[204:207], v[68:71], v[144:147], v[204:207]\n\t"
      "v_mfma_f32_16x16x32_bf16 v[208:211], v[72:75], v[144:147], v[208:211]\n\t"
      "v_mfma_f32_16x16x32_bf16 v[212:215], v[76:79], v[144:147], v[212:215]\n\t"
      "v_mfma_f32_16x16x32_bf16 v[200:203], v[80:83], v[148:151], v[200:203]\n\t"
      "v_mfma_f32_16x16x32_bf16 v[204:207], v[84:87], v[148:151], v[204:207]\n\t"
      "v_mfma_f32_16x16x32_bf16 v[208:211], v[88:91], v[148:151], v[208:211]\n\t"
      "v_mfma_f32_16x16x32_bf16 v[212:215], v[92:95], v[148:151], v[212:215]\n\t"
      "v_mfma_f32_16x16x32_bf16 v[200:203], v[96:99], v[152:155], v[200:203]\n\t"
      "v_mfma_f32_16x16x32_bf16 v[204:207], v[100:103], v[152:155], v[204:207]\n\t"
      "v_mfma_f32_16x16x32_bf16 v[208:211], v[104:107], v[152:155], v[208:211]\n\t"
      "v_mfma_f32_16x16x32_bf16 v[212:215], v[108:111], v[152:155], v[212:215]\n\t"
      "v_mfma_f32_16x16x32_bf16 v[200:203], v[112:115], v[156:159], v[200:203]\n\t"
      "v_mfma_f32_16x16x32_bf16 v[204:207], v[116:119], v[156:159], v[204:207]\n\t"
      "v_mfma_f32_16x16x32_bf16 v[208:211], v[120:123], v[156:159], v[208:211]\n\t"
      "v_mfma_f32_16x16x32_bf16 v[212:215], v[124:127], v[156:159], v[212:215]\n\t"
      "global_load_dwordx4 v[128:131], v176, s[60:61] sc1\n\t"
      "global_load_dwordx4 v[132:135], v177, s[60:61] sc1\n\t"
      "global_load_dwordx4 v[136:139], v178, s[60:61] sc1\n\t"
      "global_load_dwordx4 v[140:143], v179, s[60:61] sc1\n\t"
      "global_load_dwordx4 v[144:147], v180, s[60:61] sc1\n\t"
      "global_load_dwordx4 v[148:151], v181, s[60:61] sc1\n\t"
      "global_load_dwordx4 v[152:155], v182, s[60:61] sc1\n\t"
      "global_load_dwordx4 v[156:159], v183, s[60:61] sc1\n\t"
      "s_waitcnt vmcnt(0)\n\t"
      "s_nop 7\n\t"
      "s_branch NQ3B_%=\n\t"
      "NQ4R_%=:\n\t"
      "s_mov_b32 s51, 0\n\t"
      "NQ4L_%=:\n\t"
      "s_add_u32 s51, s51, 1\n\t"
      "s_cmp_lt_u32 s51, 262144\n\t"
      "s_cbranch_scc0 NQ4X_%=\n\t"
      "s_waitcnt vmcnt(0)\n\t"
      "s_sleep 1\n\t"
      "global_load_dwordx4 v[128:131], v176, s[60:61] sc1\n\t"
      "global_load_dwordx4 v[132:135], v177, s[60:61] sc1\n\t"
      "global_load_dwordx4 v[136:139], v178, s[60:61] sc1\n\t"
      "global_load_dwordx4 v[140:143], v179, s[60:61] sc1\n\t"
      "global_load_dwordx4 v[144:147], v180, s[60:61] sc1\n\t"
      "global_load_dwordx4 v[148:151], v181, s[60:61] sc1\n\t"
      "global_load_dwordx4 v[152:155], v182, s[60:61] sc1\n\t"
      "global_load_dwordx4 v[156:159], v183, s[60:61] sc1\n\t"
      "s_waitcnt vmcnt(0)\n\t"
      "v_max3_u32 v234, v128, v129, v130\n\t"
      "v_max3_u32 v234, v234, v131, v132\n\t"
      "v_max3_u32 v234, v234, v133, v134\n\t"
      "v_max3_u32 v234, v234, v135, v136\n\t"
      "v_max3_u32 v234, v234, v137, v138\n\t"
      "v_max3_u32 v234, v234, v139, v140\n\t"
      "v_max3_u32 v234, v234, v141, v142\n\t"
      "v_max3_u32 v234, v234, v143, v144\n\t"
      "v_max3_u32 v234, v234, v145, v146\n\t"
      "v_max3_u32 v234, v234, v147, v148\n\t"
      "v_max3_u32 v234, v234, v149, v150\n\t"
      "v_max3_u32 v234, v234, v151, v152\n\t"
      "v_max3_u32 v234, v234, v153, v154\n\t"
      "v_max3_u32 v234, v234, v155, v156\n\t"
      "v_max3_u32 v234, v234, v157, v158\n\t"
      "v_max_u32_e32 v234, v234, v159\n\t"
      "v_cmp_eq_u32_e32 vcc, -1, v234\n\t"
      "s_cbranch_vccnz NQ4L_%=\n\t"
      "NQ4X_%=:\n\t"
      "v_mfma_f32_16x16x32_bf16 v[184:187], v[0:3], v[128:131], v[236:239]\n\t"
      "v_mfma_f32_16x16x32_bf16 v[188:191], v[4:7], v[128:131], 0\n\t"
      "v_mfma_f32_16x16x32_bf16 v[192:195], v[8:11], v[128:131], 0\n\t"
      "v_mfma_f32_16x16x32_bf16 v[196:199], v[12:15], v[128:131], 0\n\t"
      "v_mfma_f32_16x16x32_bf16 v[184:187], v[16:19], v[132:135], v[184:187]\n\t"
      "v_mfma_f32_16x16x32_bf16 v[188:191], v[20:23], v[132:135], v[188:191]\n\t"
      "v_mfma_f32_16x16x32_bf16 v[192:195], v[24:27], v[132:135], v[192:195]\n\t"
      "v_mfma_f32_16x16x32_bf16 v[196:199], v[28:31], v[132:135], v[196:199]\n\t"
      "v_mfma_f32_16x16x32_bf16 v[184:187], v[32:35], v[136:139], v[184:187]\n\t"
      "v_mfma_f32_16x16x32_bf16 v[188:191], v[36:39], v[136:139], v[188:191]\n\t"
      "v_mfma_f32_16x16x32_bf16 v[192:195], v[40:43], v[136:139], v[192:195]\n\t"
      "v_mfma_f32_16x16x32_bf16 v[196:199], v[44:47], v[136:139], v[196:199]\n\t"
      "v_mfma_f32_16x16x32_bf16 v[184:187], v[48:51], v[140:143], v[184:187]\n\t"
      "v_mfma_f32_16x16x32_bf16 v[188:191], v[52:55], v[140:143], v[188:191]\n\t"
      "v_mfma_f32_16x16x32_bf16 v[192:195], v[56:59], v[140:143], v[192:195]\n\t"
      "v_mfma_f32_16x16x32_bf16 v[196:199], v[60:63], v[140:143], v[196:199]\n\t"
      "v_mfma_f32_16x16x32_bf16 v[184:187], v[64:67], v[144:147], v[184:187]\n\t"
      "v_mfma_f32_16x16x32_bf16 v[188:191], v[68:71], v[144:147], v[188:191]\n\t"
      "v_mfma_f32_16x16x32_bf16 v[192:195], v[72:75], v[144:147], v[192:195]\n\t"
      "v_mfma_f32_16x16x32_bf16 v[196:199], v[76:79], v[144:147], v[196:199]\n\t"
      "v_mfma_f32_16x16x32_bf16 v[184:187], v[80:83], v[148:151], v[184:187]\n\t"
      "v_mfma_f32_16x16x32_bf16 v[188:191], v[84:87], v[148:151], v[188:191]\n\t"
      "v_mfma_f32_16x16x32_bf16 v[192:195], v[88:91], v[148:151], v[192:195]\n\t"
      "v_mfma_f32_16x16x32_bf16 v[196:199], v[92:95], v[148:151], v[196:199]\n\t"
      "v_mfma_f32_16x16x32_bf16 v[184:187], v[96:99], v[152:155], v[184:187]\n\t"
      "v_mfma_f32_16x16x32_bf16 v[188:191], v[100:103], v[152:155], v[188:191]\n\t"
      "v_mfma_f32_16x16x32_bf16 v[192:195], v[104:107], v[152:155], v[192:195]\n\t"
      "v_mfma_f32_16x16x32_bf16 v[196:199], v[108:111], v[152:155], v[196:199]\n\t"
      "v_mfma_f32_16x16x32_bf16 v[184:187], v[112:115], v[156:159], v[184:187]\n\t"
      "v_mfma_f32_16x16x32_bf16 v[188:191], v[116:119], v[156:159], v[188:191]\n\t"
      "v_mfma_f32_16x16x32_bf16 v[192:195], v[120:123], v[156:159], v[192:195]\n\t"
      "v_mfma_f32_16x16x32_bf16 v[196:199], v[124:127], v[156:159], v[196:199]\n\t"
      "global_load_dwordx4 v[128:131], v176, s[62:63] sc1\n\t"
      "global_load_dwordx4 v[132:135], v177, s[62:63] sc1\n\t"
      "global_load_dwordx4 v[136:139], v178, s[62:63] sc1\n\t"
      "global_load_dwordx4 v[140:143], v179, s[62:63] sc1\n\t"
      "global_load_dwordx4 v[144:147], v180, s[62:63] sc1\n\t"
      "global_load_dwordx4 v[148:151], v181, s[62:63] sc1\n\t"
      "global_load_dwordx4 v[152:155], v182, s[62:63] sc1\n\t"
      "global_load_dwordx4 v[156:159], v183, s[62:63] sc1\n\t"
      "s_waitcnt vmcnt(0)\n\t"
      "s_nop 7\n\t"
      "s_branch NQ4B_%=\n\t"
      "NQ5R_%=:\n\t"
      "s_mov_b32 s51, 0\n\t"
      "NQ5L_%=:\n\t"
      "s_add_u32 s51, s51, 1\n\t"
      "s_cmp_lt_u32 s51, 262144\n\t"
      "s_cbranch_scc0 NQ5X_%=\n\t"
      "s_waitcnt vmcnt(0)\n\t"
      "s_sleep 1\n\t"
      "global_load_dwordx4 v[128:131], v176, s[62:63] sc1\n\t"
      "global_load_dwordx4 v[132:135], v177, s[62:63] sc1\n\t"
      "global_load_dwordx4 v[136:139], v178, s[62:63] sc1\n\t"
      "global_load_dwordx4 v[140:143], v179, s[62:63] sc1\n\t"
      "global_load_dwordx4 v[144:147], v180, s[62:63] sc1\n\t"
      "global_load_dwordx4 v[148:151], v181, s[62:63] sc1\n\t"
      "global_load_dwordx4 v[152:155], v182, s[62:63] sc1\n\t"
      "global_load_dwordx4 v[156:159], v183, s[62:63] sc1\n\t"
      "s_waitcnt vmcnt(0)\n\t"
      "v_max3_u32 v234, v128, v129, v130\n\t"
      "v_max3_u32 v234, v234, v131, v132\n\t"
      "v_max3_u32 v234, v234, v133, v134\n\t"
      "v_max3_u32 v234, v234, v135, v136\n\t"
      "v_max3_u32 v234, v234, v137, v138\n\t"
      "v_max3_u32 v234, v234, v139, v140\n\t"
      "v_max3_u32 v234, v234, v141, v142\n\t"
      "v_max3_u32 v234, v234, v143, v144\n\t"
      "v_max3_u32 v234, v234, v145, v146\n\t"
      "v_max3_u32 v234, v234, v147, v148\n\t"
      "v_max3_u32 v234, v234, v149, v150\n\t"
      "v_max3_u32 v234, v234, v151, v152\n\t"
      "v_max3_u32 v234, v234, v153, v154\n\t"
      "v_max3_u32 v234, v234, v155, v156\n\t"
      "v_max3_u32 v234, v234, v157, v158\n\t"
      "v_max_u32_e32 v234, v234, v159\n\t"
      "v_cmp_eq_u32_e32 vcc, -1, v234\n\t"
      "s_cbranch_vccnz NQ5L_%=\n\t"
      "NQ5X_%=:\n\t"
      "v_mfma_f32_16x16x32_bf16 v[200:203], v[0:3], v[128:131], v[236:239]\n\t"
      "v_mfma_f32_16x16x32_bf16 v[204:207], v[4:7], v[128:131], 0\n\t"
      "v_mfma_f32_16x16x32_bf16 v[208:211], v[8:11], v[128:131], 0\n\t"
      "v_mfma_f32_16x16x32_bf16 v[212:215], v[12:15], v[128:131], 0\n\t"
      "v_mfma_f32_16x16x32_bf16 v[200:203], v[16:19], v[132:135], v[200:203]\n\t"
      "v_mfma_f32_16x16x32_bf16 v[204:207], v[20:23], v[132:135], v[204:207]\n\t"
      "v_mfma_f32_16x16x32_bf16 v[208:211], v[24:27], v[132:135], v[208:211]\n\t"
      "v_mfma_f32_16x16x32_bf16 v[212:215], v[28:31], v[132:135], v[212:215]\n\t"
      "v_mfma_f32_16x16x32_bf16 v[200:203], v[32:35], v[136:139], v[200:203]\n\t"
      "v_mfma_f32_16x16x32_bf16 v[204:207], v[36:39], v[136:139], v[204:207]\n\t"
      "v_mfma_f32_16x16x32_bf16 v[208:211], v[40:43], v[136:139], v[208:211]\n\t"
      "v_mfma_f32_16x16x32_bf16 v[212:215], v[44:47], v[136:139], v[212:215]\n\t"
      "v_mfma_f32_16x16x32_bf16 v[200:203], v[48:51], v[140:143], v[200:203]\n\t"
      "v_mfma_f32_16x16x32_bf16 v[204:207], v[52:55], v[140:143], v[204:207]\n\t"
      "v_mfma_f32_16x16x32_bf16 v[208:211], v[56:59], v[140:143], v[208:211]\n\t"
      "v_mfma_f32_16x16x32_bf16 v[212:215], v[60:63], v[140:143], v[212:215]\n\t"
      "v_mfma_f32_16x16x32_bf16 v[200:203], v[64:67], v[144:147], v[200:203]\n\t"
      "v_mfma_f32_16x16x32_bf16 v[204:207], v[68:71], v[144:147], v[204:207]\n\t"
      "v_mfma_f32_16x16x32_bf16 v[208:211], v[72:75], v[144:147], v[208:211]\n\t"
      "v_mfma_f32_16x16x32_bf16 v[212:215], v[76:79], v[144:147], v[212:215]\n\t"
      "v_mfma_f32_16x16x32_bf16 v[200:203], v[80:83], v[148:151], v[200:203]\n\t"
      "v_mfma_f32_16x16x32_bf16 v[204:207], v[84:87], v[148:151], v[204:207]\n\t"
      "v_mfma_f32_16x16x32_bf16 v[208:211], v[88:91], v[148:151], v[208:211]\n\t"
      "v_mfma_f32_16x16x32_bf16 v[212:215], v[92:95], v[148:151], v[212:215]\n\t"
      "v_mfma_f32_16x16x32_bf16 v[200:203], v[96:99], v[152:155], v[200:203]\n\t"
      "v_mfma_f32_16x16x32_bf16 v[204:207], v[100:103], v[152:155], v[204:207]\n\t"
      "v_mfma_f32_16x16x32_bf16 v[208:211], v[104:107], v[152:155], v[208:211]\n\t"
      "v_mfma_f32_16x16x32_bf16 v[212:215], v[108:111], v[152:155], v[212:215]\n\t"
      "v_mfma_f32_16x16x32_bf16 v[200:203], v[112:115], v[156:159], v[200:203]\n\t"
      "v_mfma_f32_16x16x32_bf16 v[204:207], v[116:119], v[156:159], v[204:207]\n\t"
      "v_mfma_f32_16x16x32_bf16 v[208:211], v[120:123], v[156:159], v[208:211]\n\t"
      "v_mfma_f32_16x16x32_bf16 v[212:215], v[124:127], v[156:159], v[212:215]\n\t"
      "global_load_dwordx4 v[128:131], v176, s[64:65] sc1\n\t"
      "global_load_dwordx4 v[132:135], v177, s[64:65] sc1\n\t"
      "global_load_dwordx4 v[136:139], v178, s[64:65] sc1\n\t"
      "global_load_dwordx4 v[140:143], v179, s[64:65] sc1\n\t"
      "global_load_dwordx4 v[144:147], v180, s[64:65] sc1\n\t"
      "global_load_dwordx4 v[148:151], v181, s[64:65] sc1\n\t"
      "global_load_dwordx4 v[152:155], v182, s[64:65] sc1\n\t"
      "global_load_dwordx4 v[156:159], v183, s[64:65] sc1\n\t"
      "s_waitcnt vmcnt(0)\n\t"
      "s_nop 7\n\t"
      "s_branch NQ5B_%=\n\t"
      "NQ6R_%=:\n\t"
      "s_mov_b32 s51, 0\n\t"
      "NQ6L_%=:\n\t"
      "s_add_u32 s51, s51, 1\n\t"
      "s_cmp_lt_u32 s51, 262144\n\t"
      "s_cbranch_scc0 NQ6X_%=\n\t"
      "s_waitcnt vmcnt(0)\n\t"
      "s_sleep 1\n\t"
      "global_load_dwordx4 v[128:131], v176, s[64:65] sc1\n\t"
      "global_load_dwordx4 v[132:135], v177, s[64:65] sc1\n\t"
      "global_load_dwordx4 v[136:139], v178, s[64:65] sc1\n\t"
      "global_load_dwordx4 v[140:143], v179, s[64:65] sc1\n\t"
      "global_load_dwordx4 v[144:147], v180, s[64:65] sc1\n\t"
      "global_load_dwordx4 v[148:151], v181, s[64:65] sc1\n\t"
      "global_load_dwordx4 v[152:155], v182, s[64:65] sc1\n\t"
      "global_load_dwordx4 v[156:159], v183, s[64:65] sc1\n\t"
      "s_waitcnt vmcnt(0)\n\t"
      "v_max3_u32 v234, v128, v129, v130\n\t"
      "v_max3_u32 v234, v234, v131, v132\n\t"
      "v_max3_u32 v234, v234, v133, v134\n\t"
      "v_max3_u32 v234, v234, v135, v136\n\t"
      "v_max3_u32 v234, v234, v137, v138\n\t"
      "v_max3_u32 v234, v234, v139, v140\n\t"
      "v_max3_u32 v234, v234, v141, v142\n\t"
      "v_max3_u32 v234, v234, v143, v144\n\t"
      "v_max3_u32 v234, v234, v145, v146\n\t"
      "v_max3_u32 v234, v234, v147, v148\n\t"
      "v_max3_u32 v234, v234, v149, v150\n\t"
      "v_max3_u32 v234, v234, v151, v152\n\t"
      "v_max3_u32 v234, v234, v153, v154\n\t"
      "v_max3_u32 v234, v234, v155, v156\n\t"
      "v_max3_u32 v234, v234, v157, v158\n\t"
      "v_max_u32_e32 v234, v234, v159\n\t"
      "v_cmp_eq_u32_e32 vcc, -1, v234\n\t"
      "s_cbranch_vccnz NQ6L_%=\n\t"
      "NQ6X_%=:\n\t"
      "v_mfma_f32_16x16x32_bf16 v[184:187], v[0:3], v[128:131], v[236:239]\n\t"
      "v_mfma_f32_16x16x32_bf16 v[188:191], v[4:7], v[128:131], 0\n\t"
      "v_mfma_f32_16x16x32_bf16 v[192:195], v[8:11], v[128:131], 0\n\t"
      "v_mfma_f32_16x16x32_bf16 v[196:199], v[12:15], v[128:131], 0\n\t"
      "v_mfma_f32_16x16x32_bf16 v[184:187], v[16:19], v[132:135], v[184:187]\n\t"
      "v_mfma_f32_16x16x32_bf16 v[188:191], v[20:23], v[132:135], v[188:191]\n\t"
      "v_mfma_f32_16x16x32_bf16 v[192:195], v[24:27], v[132:135], v[192:195]\n\t"
      "v_mfma_f32_16x16x32_bf16 v[196:199], v[28:31], v[132:135], v[196:199]\n\t"
      "v_mfma_f32_16x16x32_bf16 v[184:187], v[32:35], v[136:139], v[184:187]\n\t"
      "v_mfma_f32_16x16x32_bf16 v[188:191], v[36:39], v[136:139], v[188:191]\n\t"
      "v_mfma_f32_16x16x32_bf16 v[192:195], v[40:43], v[136:139], v[192:195]\n\t"
      "v_mfma_f32_16x16x32_bf16 v[196:199], v[44:47], v[136:139], v[196:199]\n\t"
      "v_mfma_f32_16x16x32_bf16 v[184:187], v[48:51], v[140:143], v[184:187]\n\t"
      "v_mfma_f32_16x16x32_bf16 v[188:191], v[52:55], v[140:143], v[188:191]\n\t"
      "v_mfma_f32_16x16x32_bf16 v[192:195], v[56:59], v[140:143], v[192:195]\n\t"
      "v_mfma_f32_16x16x32_bf16 v[196:199], v[60:63], v[140:143], v[196:199]\n\t"
      "v_mfma_f32_16x16x32_bf16 v[184:187], v[64:67], v[144:147], v[184:187]\n\t"
      "v_mfma_f32_16x16x32_bf16 v[188:191], v[68:71], v[144:147], v[188:191]\n\t"
      "v_mfma_f32_16x16x32_bf16 v[192:195], v[72:75], v[144:147], v[192:195]\n\t"
      "v_mfma_f32_16x16x32_bf16 v[196:199], v[76:79], v[144:147], v[196:199]\n\t"
      "v_mfma_f32_16x16x32_bf16 v[184:187], v[80:83], v[148:151], v[184:187]\n\t"
      "v_mfma_f32_16x16x32_bf16 v[188:191], v[84:87], v[148:151], v[188:191]\n\t"
      "v_mfma_f32_16x16x32_bf16 v[192:195], v[88:91], v[148:151], v[192:195]\n\t"
      "v_mfma_f32_16x16x32_bf16 v[196:199], v[92:95], v[148:151], v[196:199]\n\t"
      "v_mfma_f32_16x16x32_bf16 v[184:187], v[96:99], v[152:155], v[184:187]\n\t"
      "v_mfma_f32_16x16x32_bf16 v[188:191], v[100:103], v[152:155], v[188:191]\n\t"
      "v_mfma_f32_16x16x32_bf16 v[192:195], v[104:107], v[152:155], v[192:195]\n\t"
      "v_mfma_f32_16x16x32_bf16 v[196:199], v[108:111], v[152:155], v[196:199]\n\t"
      "v_mfma_f32_16x16x32_bf16 v[184:187], v[112:115], v[156:159], v[184:187]\n\t"
      "v_mfma_f32_16x16x32_bf16 v[188:191], v[116:119], v[156:159], v[188:191]\n\t"
      "v_mfma_f32_16x16x32_bf16 v[192:195], v[120:123], v[156:159], v[192:195]\n\t"
      "v_mfma_f32_16x16x32_bf16 v[196:199], v[124:127], v[156:159], v[196:199]\n\t"
      "s_waitcnt vmcnt(0)\n\t"
      "s_nop 7\n\t"
      "s_branch NQ6B_%=\n\t"
      "FP0R_%=:\n\t"
      "s_mov_b32 s51, 0\n\t"
      "FP0L_%=:\n\t"
      "s_add_u32 s51, s51, 1\n\t"
      "s_cmp_lt_u32 s51, 262144\n\t"
      "s_cbranch_scc0 FP0X_%=\n\t"
      "s_waitcnt vmcnt(0)\n\t"
      "s_sleep 1\n\t"
      "global_load_dwordx4 v[128:131], v176, s[52:53] sc1\n\t"
      "global_load_dwordx4 v[132:135], v177, s[52:53] sc1\n\t"
      "global_load_dwordx4 v[136:139], v178, s[52:53] sc1\n\t"
      "global_load_dwordx4 v[140:143], v179, s[52:53] sc1\n\t"
      "global_load_dwordx4 v[144:147], v180, s[52:53] sc1\n\t"
      "global_load_dwordx4 v[148:151], v181, s[52:53] sc1\n\t"
      "global_load_dwordx4 v[152:155], v182, s[52:53] sc1\n\t"
      "global_load_dwordx4 v[156:159], v183, s[52:53] sc1\n\t"
      "s_waitcnt vmcnt(7)\n\t"
      "v_max3_u32 v234, v128, v129, v130\n\t"
      "v_max_u32_e32 v234, v234, v131\n\t"
      "v_cmp_eq_u32_e32 vcc, -1, v234\n\t"
      "s_cbranch_vccnz FP0L_%=\n\t"
      "FP0X_%=:\n\t"
      "s_branch FP0B_%=\n\t"
      "FP1R_%=:\n\t"
      "s_mov_b32 s51, 0\n\t"
      "FP1L_%=:\n\t"
      "s_add_u32 s51, s51, 1\n\t"
      "s_cmp_lt_u32 s51, 262144\n\t"
      "s_cbranch_scc0 FP1X_%=\n\t"
      "s_waitcnt vmcnt(0)\n\t"
      "s_sleep 1\n\t"
      "global_load_dwordx4 v[132:135], v177, s[52:53] sc1\n\t"
      "global_load_dwordx4 v[136:139], v178, s[52:53] sc1\n\t"
      "global_load_dwordx4 v[140:143], v179, s[52:53] sc1\n\t"
      "global_load_dwordx4 v[144:147], v180, s[52:53] sc1\n\t"
      "global_load_dwordx4 v[148:151], v181, s[52:53] sc1\n\t"
      "global_load_dwordx4 v[152:155], v182, s[52:53] sc1\n\t"
      "global_load_dwordx4 v[156:159], v183, s[52:53] sc1\n\t"
      "s_waitcnt vmcnt(6)\n\t"
      "v_max3_u32 v234, v132, v133, v134\n\t"
      "v_max_u32_e32 v234, v234, v135\n\t"
      "v_cmp_eq_u32_e32 vcc, -1, v234\n\t"
      "s_cbranch_vccnz FP1L_%=\n\t"
      "FP1X_%=:\n\t"
      "s_branch FP1B_%=\n\t"
      "FP2R_%=:\n\t"
      "s_mov_b32 s51, 0\n\t"
      "FP2L_%=:\n\t"
      "s_add_u32 s51, s51, 1\n\t"
      "s_cmp_lt_u32 s51, 262144\n\t"
      "s_cbranch_scc0 FP2X_%=\n\t"
      "s_waitcnt vmcnt(0)\n\t"
      "s_sleep 1\n\t"
      "global_load_dwordx4 v[136:139], v178, s[52:53] sc1\n\t"
      "global_load_dwordx4 v[140:143], v179, s[52:53] sc1\n\t"
      "global_load_dwordx4 v[144:147], v180, s[52:53] sc1\n\t"
      "global_load_dwordx4 v[148:151], v181, s[52:53] sc1\n\t"
      "global_load_dwordx4 v[152:155], v182, s[52:53] sc1\n\t"
      "global_load_dwordx4 v[156:159], v183, s[52:53] sc1\n\t"
      "global_load_dwordx4 v[128:131], v176, s[54:55] sc1\n\t"
      "global_load_dwordx4 v[132:135], v177, s[54:55] sc1\n\t"
      "s_waitcnt vmcnt(7)\n\t"
      "v_max3_u32 v234, v136, v137, v138\n\t"
      "v_max_u32_e32 v234, v234, v139\n\t"
      "v_cmp_eq_u32_e32 vcc, -1, v234\n\t"
      "s_cbranch_vccnz FP2L_%=\n\t"
      "FP2X_%=:\n\t"
      "s_branch FP2B_%=\n\t"
      "FP3R_%=:\n\t"
      "s_mov_b32 s51, 0\n\t"
      "FP3L_%=:\n\t"
      "s_add_u32 s51, s51, 1\n\t"
      "s_cmp_lt_u32 s51, 262144\n\t"
      "s_cbranch_scc0 FP3X_%=\n\t"
      "s_waitcnt vmcnt(0)\n\t"
      "s_sleep 1\n\t"
      "global_load_dwordx4 v[140:143], v179, s[52:53] sc1\n\t"
      "global_load_dwordx4 v[144:147], v180, s[52:53] sc1\n\t"
      "global_load_dwordx4 v[148:151], v181, s[52:53] sc1\n\t"
      "global_load_dwordx4 v[152:155], v182, s[52:53] sc1\n\t"
      "global_load_dwordx4 v[156:159], v183, s[52:53] sc1\n\t"
      "global_load_dwordx4 v[128:131], v176, s[54:55] sc1\n\t"
      "global_load_dwordx4 v[132:135], v177, s[54:55] sc1\n\t"
      "s_waitcnt vmcnt(6)\n\t"
      "v_max3_u32 v234, v140, v141, v142\n\t"
      "v_max_u32_e32 v234, v234, v143\n\t"
      "v_cmp_eq_u32_e32 vcc, -1, v234\n\t"
      "s_cbranch_vccnz FP3L_%=\n\t"
      "FP3X_%=:\n\t"
      "s_branch FP3B_%=\n\t"
      "FP4R_%=:\n\t"
      "s_mov_b32 s51, 0\n\t"
      "FP4L_%=:\n\t"
      "s_add_u32 s51, s51, 1\n\t"
      "s_cmp_lt_u32 s51, 262144\n\t"
      "s_cbranch_scc0 FP4X_%=\n\t"
      "s_waitcnt vmcnt(0)\n\t"
      "s_sleep 1\n\t"
      "global_load_dwordx4 v[144:147], v180, s[52:53] sc1\n\t"
      "global_load_dwordx4 v[148:151], v181, s[52:53] sc1\n\t"
      "global_load_dwordx4 v[152:155], v182, s[52:53] sc1\n\t"
      "global_load_dwordx4 v[156:159], v183, s[52:53] sc1\n\t"
      "global_load_dwordx4 v[128:131], v176, s[54:55] sc1\n\t"
      "global_load_dwordx4 v[132:135], v177, s[54:55] sc1\n\t"
      "global_load_dwordx4 v[136:139], v178, s[54:55] sc1\n\t"
      "global_load_dwordx4 v[140:143], v179, s[54:55] sc1\n\t"
      "s_waitcnt vmcnt(7)\n\t"
      "v_max3_u32 v234, v144, v145, v146\n\t"
      "v_max_u32_e32 v234, v234, v147\n\t"
      "v_cmp_eq_u32_e32 vcc, -1, v234\n\t"
      "s_cbranch_vccnz FP4L_%=\n\t"
      "FP4X_%=:\n\t"
      "s_branch FP4B_%=\n\t"
      "FP5R_%=:\n\t"
      "s_mov_b32 s51, 0\n\t"
      "FP5L_%=:\n\t"
      "s_add_u32 s51, s51, 1\n\t"
      "s_cmp_lt_u32 s51, 262144\n\t"
      "s_cbranch_scc0 FP5X_%=\n\t"
      "s_waitcnt vmcnt(0)\n\t"
      "s_sleep 1\n\t"
      "global_load_dwordx4 v[148:151], v181, s[52:53] sc1\n\t"
      "global_load_dwordx4 v[152:155], v182, s[52:53] sc1\n\t"
      "global_load_dwordx4 v[156:159], v183, s[52:53] sc1\n\t"
      "global_load_dwordx4 v[128:131], v176, s[54:55] sc1\n\t"
      "global_load_dwordx4 v[132:135], v177, s[54:55] sc1\n\t"
      "global_load_dwordx4 v[136:139], v178, s[54:55] sc1\n\t"
      "global_load_dwordx4 v[140:143], v179, s[54:55] sc1\n\t"
      "s_waitcnt vmcnt(6)\n\t"
      "v_max3_u32 v234, v148, v149, v150\n\t"
      "v_max_u32_e32 v234, v234, v151\n\t"
      "v_cmp_eq_u32_e32 vcc, -1, v234\n\t"
      "s_cbranch_vccnz FP5L_%=\n\t"
      "FP5X_%=:\n\t"
      "s_branch FP5B_%=\n\t"
      "FP6R_%=:\n\t"
      "s_mov_b32 s51, 0\n\t"
      "FP6L_%=:\n\t"
      "s_add_u32 s51, s51, 1\n\t"
      "s_cmp_lt_u32 s51, 262144\n\t"
      "s_cbranch_scc0 FP6X_%=\n\t"
      "s_waitcnt vmcnt(0)\n\t"
      "s_sleep 1\n\t"
      "global_load_dwordx4 v[152:155], v182, s[52:53] sc1\n\t"
      "global_load_dwordx4 v[156:159], v183, s[52:53] sc1\n\t"
      "global_load_dwordx4 v[128:131], v176, s[54:55] sc1\n\t"
      "global_load_dwordx4 v[132:135], v177, s[54:55] sc1\n\t"
      "global_load_dwordx4 v[136:139], v178, s[54:55] sc1\n\t"
      "global_load_dwordx4 v[140:143], v179, s[54:55] sc1\n\t"
      "global_load_dwordx4 v[144:147], v180, s[54:55] sc1\n\t"
      "global_load_dwordx4 v[148:151], v181, s[54:55] sc1\n\t"
      "s_waitcnt vmcnt(7)\n\t"
      "v_max3_u32 v234, v152, v153, v154\n\t"
      "v_max_u32_e32 v234, v234, v155\n\t"
      "v_cmp_eq_u32_e32 vcc, -1, v234\n\t"
      "s_cbranch_vccnz FP6L_%=\n\t"
      "FP6X_%=:\n\t"
      "s_branch FP6B_%=\n\t"
      "FP7R_%=:\n\t"
      "s_mov_b32 s51, 0\n\t"
      "FP7L_%=:\n\t"
      "s_add_u32 s51, s51, 1\n\t"
      "s_cmp_lt_u32 s51, 262144\n\t"
      "s_cbranch_scc0 FP7X_%=\n\t"
      "s_waitcnt vmcnt(0)\n\t"
      "s_sleep 1\n\t"
      "global_load_dwordx4 v[156:159], v183, s[52:53] sc1\n\t"
      "global_load_dwordx4 v[128:131], v176, s[54:55] sc1\n\t"
      "global_load_dwordx4 v[132:135], v177, s[54:55] sc1\n\t"
      "global_load_dwordx4 v[136:139], v178, s[54:55] sc1\n\t"
      "global_load_dwordx4 v[140:143], v179, s[54:55] sc1\n\t"
      "global_load_dwordx4 v[144:147], v180, s[54:55] sc1\n\t"
      "global_load_dwordx4 v[148:151], v181, s[54:55] sc1\n\t"
      "s_waitcnt vmcnt(6)\n\t"
      "v_max3_u32 v234, v156, v157, v158\n\t"
      "v_max_u32_e32 v234, v234, v159\n\t"
      "v_cmp_eq_u32_e32 vcc, -1, v234\n\t"
      "s_cbranch_vccnz FP7L_%=\n\t"
      "FP7X_%=:\n\t"
      "s_branch FP7B_%=\n\t"
      "NP1R_%=:\n\t"
      "s_mov_b32 s51, 0\n\t"
      "NP1L_%=:\n\t"
      "s_add_u32 s51, s51, 1\n\t"
      "s_cmp_lt_u32 s51, 262144\n\t"
      "s_cbranch_scc0 NP1X_%=\n\t"
      "s_waitcnt vmcnt(0)\n\t"
      "s_sleep 1\n\t"
      "global_load_dwordx4 v[128:131], v176, s[54:55] sc1\n\t"
      "global_load_dwordx4 v[132:135], v177, s[54:55] sc1\n\t"
      "global_load_dwordx4 v[136:139], v178, s[54:55] sc1\n\t"
      "global_load_dwordx4 v[140:143], v179, s[54:55] sc1\n\t"
      "global_load_dwordx4 v[144:147], v180, s[54:55] sc1\n\t"
      "global_load_dwordx4 v[148:151], v181, s[54:55] sc1\n\t"
      "global_load_dwordx4 v[152:155], v182, s[54:55] sc1\n\t"
      "global_load_dwordx4 v[156:159], v183, s[54:55] sc1\n\t"
      "s_waitcnt vmcnt(0)\n\t"
      "v_max3_u32 v234, v128, v129, v130\n\t"
      "v_max3_u32 v234, v234, v131, v132\n\t"
      "v_max3_u32 v234, v234, v133, v134\n\t"
      "v_max3_u32 v234, v234, v135, v136\n\t"
      "v_max3_u32 v234, v234, v137, v138\n\t"
      "v_max3_u32 v234, v234, v139, v140\n\t"
      "v_max3_u32 v234, v234, v141, v142\n\t"
      "v_max3_u32 v234, v234, v143, v144\n\t"
      "v_max3_u32 v234, v234, v145, v146\n\t"
      "v_max3_u32 v234, v234, v147, v148\n\t"
      "v_max3_u32 v234, v234, v149, v150\n\t"
      "v_max3_u32 v234, v234, v151, v152\n\t"
      "v_max3_u32 v234, v234, v153, v154\n\t"
      "v_max3_u32 v234, v234, v155, v156\n\t"
      "v_max3_u32 v234, v234, v157, v158\n\t"
      "v_max_u32_e32 v234, v234, v159\n\t"
      "v_cmp_eq_u32_e32 vcc, -1, v234\n\t"
      "s_cbranch_vccnz NP1L_%=\n\t"
      "NP1X_%=:\n\t"
      "v_mfma_f32_16x16x32_bf16 v[200:203], v[0:3], v[128:131], 0\n\t"
      "v_mfma_f32_16x16x32_bf16 v[204:207], v[4:7], v[128:131], 0\n\t"
      "v_mfma_f32_16x16x32_bf16 v[208:211], v[8:11], v[128:131], 0\n\t"
      "v_mfma_f32_16x16x32_bf16 v[212:215], v[12:15], v[128:131], 0\n\t"
      "v_mfma_f32_16x16x32_bf16 v[200:203], v[16:19], v[132:135], v[200:203]\n\t"
      "v_mfma_f32_16x16x32_bf16 v[204:207], v[20:23], v[132:135], v[204:207]\n\t"
      "v_mfma_f32_16x16x32_bf16 v[208:211], v[24:27], v[132:135], v[208:211]\n\t"
      "v_mfma_f32_16x16x32_bf16 v[212:215], v[28:31], v[132:135], v[212:215]\n\t"
      "v_mfma_f32_16x16x32_bf16 v[200:203], v[32:35], v[136:139], v[200:203]\n\t"
      "v_mfma_f32_16x16x32_bf16 v[204:207], v[36:39], v[136:139], v[204:207]\n\t"
      "v_mfma_f32_16x16x32_bf16 v[208:211], v[40:43], v[136:139], v[208:211]\n\t"
      "v_mfma_f32_16x16x32_bf16 v[212:215], v[44:47], v[136:139], v[212:215]\n\t"
      "v_mfma_f32_16x16x32_bf16 v[200:203], v[48:51], v[140:143], v[200:203]\n\t"
      "v_mfma_f32_16x16x32_bf16 v[204:207], v[52:55], v[140:143], v[204:207]\n\t"
      "v_mfma_f32_16x16x32_bf16 v[208:211], v[56:59], v[140:143], v[208:211]\n\t"
      "v_mfma_f32_16x16x32_bf16 v[212:215], v[60:63], v[140:143], v[212:215]\n\t"
      "v_mfma_f32_16x16x32_bf16 v[200:203], v[64:67], v[144:147], v[200:203]\n\t"
      "v_mfma_f32_16x16x32_bf16 v[204:207], v[68:71], v[144:147], v[204:207]\n\t"
      "v_mfma_f32_16x16x32_bf16 v[208:211], v[72:75], v[144:147], v[208:211]\n\t"
      "v_mfma_f32_16x16x32_bf16 v[212:215], v[76:79], v[144:147], v[212:215]\n\t"
      "v_mfma_f32_16x16x32_bf16 v[200:203], v[80:83], v[148:151], v[200:203]\n\t"
      "v_mfma_f32_16x16x32_bf16 v[204:207], v[84:87], v[148:151], v[204:207]\n\t"
      "v_mfma_f32_16x16x32_bf16 v[208:211], v[88:91], v[148:151], v[208:211]\n\t"
      "v_mfma_f32_16x16x32_bf16 v[212:215], v[92:95], v[148:151], v[212:215]\n\t"
      "v_mfma_f32_16x16x32_bf16 v[200:203], v[96:99], v[152:155], v[200:203]\n\t"
      "v_mfma_f32_16x16x32_bf16 v[204:207], v[100:103], v[152:155], v[204:207]\n\t"
      "v_mfma_f32_16x16x32_bf16 v[208:211], v[104:107], v[152:155], v[208:211]\n\t"
      "v_mfma_f32_16x16x32_bf16 v[200:203], v[112:115], v[156:159], v[200:203]\n\t"
      "v_mfma_f32_16x16x32_bf16 v[204:207], v[116:119], v[156:159], v[204:207]\n\t"
      "v_mfma_f32_16x16x32_bf16 v[208:211], v[120:123], v[156:159], v[208:211]\n\t"
      "global_load_dwordx4 v[128:131], v176, s[56:57] sc1\n\t"
      "global_load_dwordx4 v[132:135], v177, s[56:57] sc1\n\t"
      "global_load_dwordx4 v[136:139], v178, s[56:57] sc1\n\t"
      "global_load_dwordx4 v[140:143], v179, s[56:57] sc1\n\t"
      "global_load_dwordx4 v[144:147], v180, s[56:57] sc1\n\t"
      "global_load_dwordx4 v[148:151], v181, s[56:57] sc1\n\t"
      "s_waitcnt vmcnt(0)\n\t"
      "s_nop 7\n\t"
      "s_branch NP1B_%=\n\t"
      "NP2R_%=:\n\t"
      "s_mov_b32 s51, 0\n\t"
      "NP2L_%=:\n\t"
      "s_add_u32 s51, s51, 1\n\t"
      "s_cmp_lt_u32 s51, 262144\n\t"
      "s_cbranch_scc0 NP2X_%=\n\t"
      "s_waitcnt vmcnt(0)\n\t"
      "s_sleep 1\n\t"
      "global_load_dwordx4 v[128:131], v176, s[56:57] sc1\n\t"
      "global_load_dwordx4 v[132:135], v177, s[56:57] sc1\n\t"
      "global_load_dwordx4 v[136:139], v178, s[56:57] sc1\n\t"
      "global_load_dwordx4 v[140:143], v179, s[56:57] sc1\n\t"
      "global_load_dwordx4 v[144:147], v180, s[56:57] sc1\n\t"
      "global_load_dwordx4 v[148:151], v181, s[56:57] sc1\n\t"
      "global_load_dwordx4 v[152:155], v182, s[56:57] sc1\n\t"
      "global_load_dwordx4 v[156:159], v183, s[56:57] sc1\n\t"
      "s_waitcnt vmcnt(0)\n\t"
      "v_max3_u32 v234, v128, v129, v130\n\t"
      "v_max3_u32 v234, v234, v131, v132\n\t"
      "v_max3_u32 v234, v234, v133, v134\n\t"
      "v_max3_u32 v234, v234, v135, v136\n\t"
      "v_max3_u32 v234, v234, v137, v138\n\t"
      "v_max3_u32 v234, v234, v139, v140\n\t"
      "v_max3_u32 v234, v234, v141, v142\n\t"
      "v_max3_u32 v234, v234, v143, v144\n\t"
      "v_max3_u32 v234, v234, v145, v146\n\t"
      "v_max3_u32 v234, v234, v147, v148\n\t"
      "v_max3_u32 v234, v234, v149, v150\n\t"
      "v_max3_u32 v234, v234, v151, v152\n\t"
      "v_max3_u32 v234, v234, v153, v154\n\t"
      "v_max3_u32 v234, v234, v155, v156\n\t"
      "v_max3_u32 v234, v234, v157, v158\n\t"
      "v_max_u32_e32 v234, v234, v159\n\t"
      "v_cmp_eq_u32_e32 vcc, -1, v234\n\t"
      "s_cbranch_vccnz NP2L_%=\n\t"
      "NP2X_%=:\n\t"
      "v_mfma_f32_16x16x32_bf16 v[184:187], v[0:3], v[128:131], 0\n\t"
      "v_mfma_f32_16x16x32_bf16 v[188:191], v[4:7], v[128:131], 0\n\t"
      "v_mfma_f32_16x16x32_bf16 v[192:195], v[8:11], v[128:131], 0\n\t"
      "v_mfma_f32_16x16x32_bf16 v[196:199], v[12:15], v[128:131], 0\n\t"
      "v_mfma_f32_16x16x32_bf16 v[184:187], v[16:19], v[132:135], v[184:187]\n\t"
      "v_mfma_f32_16x16x32_bf16 v[188:191], v[20:23], v[132:135], v[188:191]\n\t"
      "v_mfma_f32_16x16x32_bf16 v[192:195], v[24:27], v[132:135], v[192:195]\n\t"
      "v_mfma_f32_16x16x32_bf16 v[196:199], v[28:31], v[132:135], v[196:199]\n\t"
      "v_mfma_f32_16x16x32_bf16 v[184:187], v[32:35], v[136:139], v[184:187]\n\t"
      "v_mfma_f32_16x16x32_bf16 v[188:191], v[36:39], v[136:139], v[188:191]\n\t"
      "v_mfma_f32_16x16x32_bf16 v[192:195], v[40:43], v[136:139], v[192:195]\n\t"
      "v_mfma_f32_16x16x32_bf16 v[196:199], v[44:47], v[136:139], v[196:199]\n\t"
      "v_mfma_f32_16x16x32_bf16 v[184:187], v[48:51], v[140:143], v[184:187]\n\t"
      "v_mfma_f32_16x16x32_bf16 v[188:191], v[52:55], v[140:143], v[188:191]\n\t"
      "v_mfma_f32_16x16x32_bf16 v[192:195], v[56:59], v[140:143], v[192:195]\n\t"
      "v_mfma_f32_16x16x32_bf16 v[196:199], v[60:63], v[140:143], v[196:199]\n\t"
      "v_mfma_f32_16x16x32_bf16 v[184:187], v[64:67], v[144:147], v[184:187]\n\t"
      "v_mfma_f32_16x16x32_bf16 v[188:191], v[68:71], v[144:147], v[188:191]\n\t"
      "v_mfma_f32_16x16x32_bf16 v[192:195], v[72:75], v[144:147], v[192:195]\n\t"
      "v_mfma_f32_16x16x32_bf16 v[196:199], v[76:79], v[144:147], v[196:199]\n\t"
      "v_mfma_f32_16x16x32_bf16 v[184:187], v[80:83], v[148:151], v[184:187]\n\t"
      "v_mfma_f32_16x16x32_bf16 v[188:191], v[84:87], v[148:151], v[188:191]\n\t"
      "v_mfma_f32_16x16x32_bf16 v[192:195], v[88:91], v[148:151], v[192:195]\n\t"
      "v_mfma_f32_16x16x32_bf16 v[196:199], v[92:95], v[148:151], v[196:199]\n\t"
      "v_mfma_f32_16x16x32_bf16 v[184:187], v[96:99], v[152:155], v[184:187]\n\t"
      "v_mfma_f32_16x16x32_bf16 v[188:191], v[100:103], v[152:155], v[188:191]\n\t"
      "v_mfma_f32_16x16x32_bf16 v[192:195], v[104:107], v[152:155], v[192:195]\n\t"
      "v_mfma_f32_16x16x32_bf16 v[184:187], v[112:115], v[156:159], v[184:187]\n\t"
      "v_mfma_f32_16x16x32_bf16 v[188:191], v[116:119], v[156:159], v[188:191]\n\t"
      "v_mfma_f32_16x16x32_bf16 v[192:195], v[120:123], v[156:159], v[192:195]\n\t"
      "global_load_dwordx4 v[128:131], v176, s[58:59] sc1\n\t"
      "global_load_dwordx4 v[132:135], v177, s[58:59] sc1\n\t"
      "global_load_dwordx4 v[136:139], v178, s[58:59] sc1\n\t"
      "global_load_dwordx4 v[140:143], v179, s[58:59] sc1\n\t"
      "global_load_dwordx4 v[144:147], v180, s[58:59] sc1\n\t"
      "global_load_dwordx4 v[148:151], v181, s[58:59] sc1\n\t"
      "s_waitcnt vmcnt(0)\n\t"
      "s_nop 7\n\t"
      "s_branch NP2B_%=\n\t"
      "NP3R_%=:\n\t"
      "s_mov_b32 s51, 0\n\t"
      "NP3L_%=:\n\t"
      "s_add_u32 s51, s51, 1\n\t"
      "s_cmp_lt_u32 s51, 262144\n\t"
      "s_cbranch_scc0 NP3X_%=\n\t"
      "s_waitcnt vmcnt(0)\n\t"
      "s_sleep 1\n\t"
      "global_load_dwordx4 v[128:131], v176, s[58:59] sc1\n\t"
      "global_load_dwordx4 v[132:135], v177, s[58:59] sc1\n\t"
      "global_load_dwordx4 v[136:139], v178, s[58:59] sc1\n\t"
      "global_load_dwordx4 v[140:143], v179, s[58:59] sc1\n\t"
      "global_load_dwordx4 v[144:147], v180, s[58:59] sc1\n\t"
      "global_load_dwordx4 v[148:151], v181, s[58:59] sc1\n\t"
      "global_load_dwordx4 v[152:155], v182, s[58:59] sc1\n\t"
      "global_load_dwordx4 v[156:159], v183, s[58:59] sc1\n\t"
      "s_waitcnt vmcnt(0)\n\t"
      "v_max3_u32 v234, v128, v129, v130\n\t"
      "v_max3_u32 v234, v234, v131, v132\n\t"
      "v_max3_u32 v234, v234, v133, v134\n\t"
      "v_max3_u32 v234, v234, v135, v136\n\t"
      "v_max3_u32 v234, v234, v137, v138\n\t"
      "v_max3_u32 v234, v234, v139, v140\n\t"
      "v_max3_u32 v234, v234, v141, v142\n\t"
      "v_max3_u32 v234, v234, v143, v144\n\t"
      "v_max3_u32 v234, v234, v145, v146\n\t"
      "v_max3_u32 v234, v234, v147, v148\n\t"
      "v_max3_u32 v234, v234, v149, v150\n\t"
      "v_max3_u32 v234, v234, v151, v152\n\t"
      "v_max3_u32 v234, v234, v153, v154\n\t"
      "v_max3_u32 v234, v234, v155, v156\n\t"
      "v_max3_u32 v234, v234, v157, v158\n\t"
      "v_max_u32_e32 v234, v234, v159\n\t"
      "v_cmp_eq_u32_e32 vcc, -1, v234\n\t"
      "s_cbranch_vccnz NP3L_%=\n\t"
      "NP3X_%=:\n\t"
      "v_mfma_f32_16x16x32_bf16 v[200:203], v[0:3], v[128:131], 0\n\t"
      "v_mfma_f32_16x16x32_bf16 v[204:207], v[4:7], v[128:131], 0\n\t"
      "v_mfma_f32_16x16x32_bf16 v[208:211], v[8:11], v[128:131], 0\n\t"
      "v_mfma_f32_16x16x32_bf16 v[212:215], v[12:15], v[128:131], 0\n\t"
      "v_mfma_f32_16x16x32_bf16 v[200:203], v[16:19], v[132:135], v[200:203]\n\t"
      "v_mfma_f32_16x16x32_bf16 v[204:207], v[20:23], v[132:135], v[204:207]\n\t"
      "v_mfma_f32_16x16x32_bf16 v[208:211], v[24:27], v[132:135], v[208:211]\n\t"
      "v_mfma_f32_16x16x32_bf16 v[212:215], v[28:31], v[132:135], v[212:215]\n\t"
      "v_mfma_f32_16x16x32_bf16 v[200:203], v[32:35], v[136:139], v[200:203]\n\t"
      "v_mfma_f32_16x16x32_bf16 v[204:207], v[36:39], v[136:139], v[204:207]\n\t"
      "v_mfma_f32_16x16x32_bf16 v[208:211], v[40:43], v[136:139], v[208:211]\n\t"
      "v_mfma_f32_16x16x32_bf16 v[212:215], v[44:47], v[136:139], v[212:215]\n\t"
      "v_mfma_f32_16x16x32_bf16 v[200:203], v[48:51], v[140:143], v[200:203]\n\t"
      "v_mfma_f32_16x16x32_bf16 v[204:207], v[52:55], v[140:143], v[204:207]\n\t"
      "v_mfma_f32_16x16x32_bf16 v[208:211], v[56:59], v[140:143], v[208:211]\n\t"
      "v_mfma_f32_16x16x32_bf16 v[212:215], v[60:63], v[140:143], v[212:215]\n\t"
      "v_mfma_f32_16x16x32_bf16 v[200:203], v[64:67], v[144:147], v[200:203]\n\t"
      "v_mfma_f32_16x16x32_bf16 v[204:207], v[68:71], v[144:147], v[204:207]\n\t"
      "v_mfma_f32_16x16x32_bf16 v[208:211], v[72:75], v[144:147], v[208:211]\n\t"
      "v_mfma_f32_16x16x32_bf16 v[212:215], v[76:79], v[144:147], v[212:215]\n\t"
      "v_mfma_f32_16x16x32_bf16 v[200:203], v[80:83], v[148:151], v[200:203]\n\t"
      "v_mfma_f32_16x16x32_bf16 v[204:207], v[84:87], v[148:151], v[204:207]\n\t"
      "v_mfma_f32_16x16x32_bf16 v[208:211], v[88:91], v[148:151], v[208:211]\n\t"
      "v_mfma_f32_16x16x32_bf16 v[212:215], v[92:95], v[148:151], v[212:215]\n\t"
      "v_mfma_f32_16x16x32_bf16 v[200:203], v[96:99], v[152:155], v[200:203]\n\t"
      "v_mfma_f32_16x16x32_bf16 v[204:207], v[100:103], v[152:155], v[204:207]\n\t"
      "v_mfma_f32_16x16x32_bf16 v[208:211], v[104:107], v[152:155], v[208:211]\n\t"
      "v_mfma_f32_16x16x32_bf16 v[200:203], v[112:115], v[156:159], v[200:203]\n\t"
      "v_mfma_f32_16x16x32_bf16 v[204:207], v[116:119], v[156:159], v[204:207]\n\t"
      "v_mfma_f32_16x16x32_bf16 v[208:211], v[120:123], v[156:159], v[208:211]\n\t"
      "global_load_dwordx4 v[128:131], v176, s[60:61] sc1\n\t"
      "global_load_dwordx4 v[132:135], v177, s[60:61] sc1\n\t"
      "global_load_dwordx4 v[136:139], v178, s[60:61] sc1\n\t"
      "global_load_dwordx4 v[140:143], v179, s[60:61] sc1\n\t"
      "global_load_dwordx4 v[144:147], v180, s[60:61] sc1\n\t"
      "global_load_dwordx4 v[148:151], v181, s[60:61] sc1\n\t"
      "s_waitcnt vmcnt(0)\n\t"
      "s_nop 7\n\t"
      "s_branch NP3B_%=\n\t"
      "NP4R_%=:\n\t"
      "s_mov_b32 s51, 0\n\t"
      "NP4L_%=:\n\t"
      "s_add_u32 s51, s51, 1\n\t"
      "s_cmp_lt_u32 s51, 262144\n\t"
      "s_cbranch_scc0 NP4X_%=\n\t"
      "s_waitcnt vmcnt(0)\n\t"
      "s_sleep 1\n\t"
      "global_load_dwordx4 v[128:131], v176, s[60:61] sc1\n\t"
      "global_load_dwordx4 v[132:135], v177, s[60:61] sc1\n\t"
      "global_load_dwordx4 v[136:139], v178, s[60:61] sc1\n\t"
      "global_load_dwordx4 v[140:143], v179, s[60:61] sc1\n\t"
      "global_load_dwordx4 v[144:147], v180, s[60:61] sc1\n\t"
      "global_load_dwordx4 v[148:151], v181, s[60:61] sc1\n\t"
      "global_load_dwordx4 v[152:155], v182, s[60:61] sc1\n\t"
      "global_load_dwordx4 v[156:159], v183, s[60:61] sc1\n\t"
      "s_waitcnt vmcnt(0)\n\t"
      "v_max3_u32 v234, v128, v129, v130\n\t"
      "v_max3_u32 v234, v234, v131, v132\n\t"
      "v_max3_u32 v234, v234, v133, v134\n\t"
      "v_max3_u32 v234, v234, v135, v136\n\t"
      "v_max3_u32 v234, v234, v137, v138\n\t"
      "v_max3_u32 v234, v234, v139, v140\n\t"
      "v_max3_u32 v234, v234, v141, v142\n\t"
      "v_max3_u32 v234, v234, v143, v144\n\t"
      "v_max3_u32 v234, v234, v145, v146\n\t"
      "v_max3_u32 v234, v234, v147, v148\n\t"
      "v_max3_u32 v234, v234, v149, v150\n\t"
      "v_max3_u32 v234, v234, v151, v152\n\t"
      "v_max3_u32 v234, v234, v153, v154\n\t"
      "v_max3_u32 v234, v234, v155, v156\n\t"
      "v_max3_u32 v234, v234, v157, v158\n\t"
      "v_max_u32_e32 v234, v234, v159\n\t"
      "v_cmp_eq_u32_e32 vcc, -1, v234\n\t"
      "s_cbranch_vccnz NP4L_%=\n\t"
      "NP4X_%=:\n\t"
      "v_mfma_f32_16x16x32_bf16 v[184:187], v[0:3], v[128:131], 0\n\t"
      "v_mfma_f32_16x16x32_bf16 v[188:191], v[4:7], v[128:131], 0\n\t"
      "v_mfma_f32_16x16x32_bf16 v[192:195], v[8:11], v[128:131], 0\n\t"
      "v_mfma_f32_16x16x32_bf16 v[196:199], v[12:15], v[128:131], 0\n\t"
      "v_mfma_f32_16x16x32_bf16 v[184:187], v[16:19], v[132:135], v[184:187]\n\t"
      "v_mfma_f32_16x16x32_bf16 v[188:191], v[20:23], v[132:135], v[188:191]\n\t"
      "v_mfma_f32_16x16x32_bf16 v[192:195], v[24:27], v[132:135], v[192:195]\n\t"
      "v_mfma_f32_16x16x32_bf16 v[196:199], v[28:31], v[132:135], v[196:199]\n\t"
      "v_mfma_f32_16x16x32_bf16 v[184:187], v[32:35], v[136:139], v[184:187]\n\t"
      "v_mfma_f32_16x16x32_bf16 v[188:191], v[36:39], v[136:139], v[188:191]\n\t"
      "v_mfma_f32_16x16x32_bf16 v[192:195], v[40:43], v[136:139], v[192:195]\n\t"
      "v_mfma_f32_16x16x32_bf16 v[196:199], v[44:47], v[136:139], v[196:199]\n\t"
      "v_mfma_f32_16x16x32_bf16 v[184:187], v[48:51], v[140:143], v[184:187]\n\t"
      "v_mfma_f32_16x16x32_bf16 v[188:191], v[52:55], v[140:143], v[188:191]\n\t"
      "v_mfma_f32_16x16x32_bf16 v[192:195], v[56:59], v[140:143], v[192:195]\n\t"
      "v_mfma_f32_16x16x32_bf16 v[196:199], v[60:63], v[140:143], v[196:199]\n\t"
      "v_mfma_f32_16x16x32_bf16 v[184:187], v[64:67], v[144:147], v[184:187]\n\t"
      "v_mfma_f32_16x16x32_bf16 v[188:191], v[68:71], v[144:147], v[188:191]\n\t"
      "v_mfma_f32_16x16x32_bf16 v[192:195], v[72:75], v[144:147], v[192:195]\n\t"
      "v_mfma_f32_16x16x32_bf16 v[196:199], v[76:79], v[144:147], v[196:199]\n\t"
      "v_mfma_f32_16x16x32_bf16 v[184:187], v[80:83], v[148:151], v[184:187]\n\t"
      "v_mfma_f32_16x16x32_bf16 v[188:191], v[84:87], v[148:151], v[188:191]\n\t"
      "v_mfma_f32_16x16x32_bf16 v[192:195], v[88:91], v[148:151], v[192:195]\n\t"
      "v_mfma_f32_16x16x32_bf16 v[196:199], v[92:95], v[148:151], v[196:199]\n\t"
      "v_mfma_f32_16x16x32_bf16 v[184:187], v[96:99], v[152:155], v[184:187]\n\t"
      "v_mfma_f32_16x16x32_bf16 v[188:191], v[100:103], v[152:155], v[188:191]\n\t"
      "v_mfma_f32_16x16x32_bf16 v[192:195], v[104:107], v[152:155], v[192:195]\n\t"
      "v_mfma_f32_16x16x32_bf16 v[184:187], v[112:115], v[156:159], v[184:187]\n\t"
      "v_mfma_f32_16x16x32_bf16 v[188:191], v[116:119], v[156:159], v[188:191]\n\t"
      "v_mfma_f32_16x16x32_bf16 v[192:195], v[120:123], v[156:159], v[192:195]\n\t"
      "global_load_dwordx4 v[128:131], v176, s[62:63] sc1\n\t"
      "global_load_dwordx4 v[132:135], v177, s[62:63] sc1\n\t"
      "global_load_dwordx4 v[136:139], v178, s[62:63] sc1\n\t"
      "global_load_dwordx4 v[140:143], v179, s[62:63] sc1\n\t"
      "global_load_dwordx4 v[144:147], v180, s[62:63] sc1\n\t"
      "global_load_dwordx4 v[148:151], v181, s[62:63] sc1\n\t"
      "s_waitcnt vmcnt(0)\n\t"
      "s_nop 7\n\t"
      "s_branch NP4B_%=\n\t"
      "NP5R_%=:\n\t"
      "s_mov_b32 s51, 0\n\t"
      "NP5L_%=:\n\t"
      "s_add_u32 s51, s51, 1\n\t"
      "s_cmp_lt_u32 s51, 262144\n\t"
      "s_cbranch_scc0 NP5X_%=\n\t"
      "s_waitcnt vmcnt(0)\n\t"
      "s_sleep 1\n\t"
      "global_load_dwordx4 v[128:131], v176, s[62:63] sc1\n\t"
      "global_load_dwordx4 v[132:135], v177, s[62:63] sc1\n\t"
      "global_load_dwordx4 v[136:139], v178, s[62:63] sc1\n\t"
      "global_load_dwordx4 v[140:143], v179, s[62:63] sc1\n\t"
      "global_load_dwordx4 v[144:147], v180, s[62:63] sc1\n\t"
      "global_load_dwordx4 v[148:151], v181, s[62:63] sc1\n\t"
      "global_load_dwordx4 v[152:155], v182, s[62:63] sc1\n\t"
      "global_load_dwordx4 v[156:159], v183, s[62:63] sc1\n\t"
      "s_waitcnt vmcnt(0)\n\t"
      "v_max3_u32 v234, v128, v129, v130\n\t"
      "v_max3_u32 v234, v234, v131, v132\n\t"
      "v_max3_u32 v234, v234, v133, v134\n\t"
      "v_max3_u32 v234, v234, v135, v136\n\t"
      "v_max3_u32 v234, v234, v137, v138\n\t"
      "v_max3_u32 v234, v234, v139, v140\n\t"
      "v_max3_u32 v234, v234, v141, v142\n\t"
      "v_max3_u32 v234, v234, v143, v144\n\t"
      "v_max3_u32 v234, v234, v145, v146\n\t"
      "v_max3_u32 v234, v234, v147, v148\n\t"
      "v_max3_u32 v234, v234, v149, v150\n\t"
      "v_max3_u32 v234, v234, v151, v152\n\t"
      "v_max3_u32 v234, v234, v153, v154\n\t"
      "v_max3_u32 v234, v234, v155, v156\n\t"
      "v_max3_u32 v234, v234, v157, v158\n\t"
      "v_max_u32_e32 v234, v234, v159\n\t"
      "v_cmp_eq_u32_e32 vcc, -1, v234\n\t"
      "s_cbranch_vccnz NP5L_%=\n\t"
      "NP5X_%=:\n\t"
      "v_mfma_f32_16x16x32_bf16 v[200:203], v[0:3], v[128:131], 0\n\t"
      "v_mfma_f32_16x16x32_bf16 v[204:207], v[4:7], v[128:131], 0\n\t"
      "v_mfma_f32_16x16x32_bf16 v[208:211], v[8:11], v[128:131], 0\n\t"
      "v_mfma_f32_16x16x32_bf16 v[212:215], v[12:15], v[128:131], 0\n\t"
      "v_mfma_f32_16x16x32_bf16 v[200:203], v[16:19], v[132:135], v[200:203]\n\t"
      "v_mfma_f32_16x16x32_bf16 v[204:207], v[20:23], v[132:135], v[204:207]\n\t"
      "v_mfma_f32_16x16x32_bf16 v[208:211], v[24:27], v[132:135], v[208:211]\n\t"
      "v_mfma_f32_16x16x32_bf16 v[212:215], v[28:31], v[132:135], v[212:215]\n\t"
      "v_mfma_f32_16x16x32_bf16 v[200:203], v[32:35], v[136:139], v[200:203]\n\t"
      "v_mfma_f32_16x16x32_bf16 v[204:207], v[36:39], v[136:139], v[204:207]\n\t"
      "v_mfma_f32_16x16x32_bf16 v[208:211], v[40:43], v[136:139], v[208:211]\n\t"
      "v_mfma_f32_16x16x32_bf16 v[212:215], v[44:47], v[136:139], v[212:215]\n\t"
      "v_mfma_f32_16x16x32_bf16 v[200:203], v[48:51], v[140:143], v[200:203]\n\t"
      "v_mfma_f32_16x16x32_bf16 v[204:207], v[52:55], v[140:143], v[204:207]\n\t"
      "v_mfma_f32_16x16x32_bf16 v[208:211], v[56:59], v[140:143], v[208:211]\n\t"
      "v_mfma_f32_16x16x32_bf16 v[212:215], v[60:63], v[140:143], v[212:215]\n\t"
      "v_mfma_f32_16x16x32_bf16 v[200:203], v[64:67], v[144:147], v[200:203]\n\t"
      "v_mfma_f32_16x16x32_bf16 v[204:207], v[68:71], v[144:147], v[204:207]\n\t"
      "v_mfma_f32_16x16x32_bf16 v[208:211], v[72:75], v[144:147], v[208:211]\n\t"
      "v_mfma_f32_16x16x32_bf16 v[212:215], v[76:79], v[144:147], v[212:215]\n\t"
      "v_mfma_f32_16x16x32_bf16 v[200:203], v[80:83], v[148:151], v[200:203]\n\t"
      "v_mfma_f32_16x16x32_bf16 v[204:207], v[84:87], v[148:151], v[204:207]\n\t"
      "v_mfma_f32_16x16x32_bf16 v[208:211], v[88:91], v[148:151], v[208:211]\n\t"
      "v_mfma_f32_16x16x32_bf16 v[212:215], v[92:95], v[148:151], v[212:215]\n\t"
      "v_mfma_f32_16x16x32_bf16 v[200:203], v[96:99], v[152:155], v[200:203]\n\t"
      "v_mfma_f32_16x16x32_bf16 v[204:207], v[100:103], v[152:155], v[204:207]\n\t"
      "v_mfma_f32_16x16x32_bf16 v[208:211], v[104:107], v[152:155], v[208:211]\n\t"
      "v_mfma_f32_16x16x32_bf16 v[200:203], v[112:115], v[156:159], v[200:203]\n\t"
      "v_mfma_f32_16x16x32_bf16 v[204:207], v[116:119], v[156:159], v[204:207]\n\t"
      "v_mfma_f32_16x16x32_bf16 v[208:211], v[120:123], v[156:159], v[208:211]\n\t"
      "global_load_dwordx4 v[128:131], v176, s[64:65] sc1\n\t"
      "global_load_dwordx4 v[132:135], v177, s[64:65] sc1\n\t"
      "global_load_dwordx4 v[136:139], v178, s[64:65] sc1\n\t"
      "global_load_dwordx4 v[140:143], v179, s[64:65] sc1\n\t"
      "global_load_dwordx4 v[144:147], v180, s[64:65] sc1\n\t"
      "global_load_dwordx4 v[148:151], v181, s[64:65] sc1\n\t"
      "s_waitcnt vmcnt(0)\n\t"
      "s_nop 7\n\t"
      "s_branch NP5B_%=\n\t"
      "NP6R_%=:\n\t"
      "s_mov_b32 s51, 0\n\t"
      "NP6L_%=:\n\t"
      "s_add_u32 s51, s51, 1\n\t"
      "s_cmp_lt_u32 s51, 262144\n\t"
      "s_cbranch_scc0 NP6X_%=\n\t"
      "s_waitcnt vmcnt(0)\n\t"
      "s_sleep 1\n\t"
      "global_load_dwordx4 v[128:131], v176, s[64:65] sc1\n\t"
      "global_load_dwordx4 v[132:135], v177, s[64:65] sc1\n\t"
      "global_load_dwordx4 v[136:139], v178, s[64:65] sc1\n\t"
      "global_load_dwordx4 v[140:143], v179, s[64:65] sc1\n\t"
      "global_load_dwordx4 v[144:147], v180, s[64:65] sc1\n\t"
      "global_load_dwordx4 v[148:151], v181, s[64:65] sc1\n\t"
      "global_load_dwordx4 v[152:155], v182, s[64:65] sc1\n\t"
      "global_load_dwordx4 v[156:159], v183, s[64:65] sc1\n\t"
      "s_waitcnt vmcnt(0)\n\t"
      "v_max3_u32 v234, v128, v129, v130\n\t"
      "v_max3_u32 v234, v234, v131, v132\n\t"
      "v_max3_u32 v234, v234, v133, v134\n\t"
      "v_max3_u32 v234, v234, v135, v136\n\t"
      "v_max3_u32 v234, v234, v137, v138\n\t"
      "v_max3_u32 v234, v234, v139, v140\n\t"
      "v_max3_u32 v234, v234, v141, v142\n\t"
      "v_max3_u32 v234, v234, v143, v144\n\t"
      "v_max3_u32 v234, v234, v145, v146\n\t"
      "v_max3_u32 v234, v234, v147, v148\n\t"
      "v_max3_u32 v234, v234, v149, v150\n\t"
      "v_max3_u32 v234, v234, v151, v152\n\t"
      "v_max3_u32 v234, v234, v153, v154\n\t"
      "v_max3_u32 v234, v234, v155, v156\n\t"
      "v_max3_u32 v234, v234, v157, v158\n\t"
      "v_max_u32_e32 v234, v234, v159\n\t"
      "v_cmp_eq_u32_e32 vcc, -1, v234\n\t"
      "s_cbranch_vccnz NP6L_%=\n\t"
      "NP6X_%=:\n\t"
      "v_mfma_f32_16x16x32_bf16 v[184:187], v[0:3], v[128:131], 0\n\t"
      "v_mfma_f32_16x16x32_bf16 v[188:191], v[4:7], v[128:131], 0\n\t"
      "v_mfma_f32_16x16x32_bf16 v[192:195], v[8:11], v[128:131], 0\n\t"
      "v_mfma_f32_16x16x32_bf16 v[196:199], v[12:15], v[128:131], 0\n\t"
      "v_mfma_f32_16x16x32_bf16 v[184:187], v[16:19], v[132:135], v[184:187]\n\t"
      "v_mfma_f32_16x16x32_bf16 v[188:191], v[20:23], v[132:135], v[188:191]\n\t"
      "v_mfma_f32_16x16x32_bf16 v[192:195], v[24:27], v[132:135], v[192:195]\n\t"
      "v_mfma_f32_16x16x32_bf16 v[196:199], v[28:31], v[132:135], v[196:199]\n\t"
      "v_mfma_f32_16x16x32_bf16 v[184:187], v[32:35], v[136:139], v[184:187]\n\t"
      "v_mfma_f32_16x16x32_bf16 v[188:191], v[36:39], v[136:139], v[188:191]\n\t"
      "v_mfma_f32_16x16x32_bf16 v[192:195], v[40:43], v[136:139], v[192:195]\n\t"
      "v_mfma_f32_16x16x32_bf16 v[196:199], v[44:47], v[136:139], v[196:199]\n\t"
      "v_mfma_f32_16x16x32_bf16 v[184:187], v[48:51], v[140:143], v[184:187]\n\t"
      "v_mfma_f32_16x16x32_bf16 v[188:191], v[52:55], v[140:143], v[188:191]\n\t"
      "v_mfma_f32_16x16x32_bf16 v[192:195], v[56:59], v[140:143], v[192:195]\n\t"
      "v_mfma_f32_16x16x32_bf16 v[196:199], v[60:63], v[140:143], v[196:199]\n\t"
      "v_mfma_f32_16x16x32_bf16 v[184:187], v[64:67], v[144:147], v[184:187]\n\t"
      "v_mfma_f32_16x16x32_bf16 v[188:191], v[68:71], v[144:147], v[188:191]\n\t"
      "v_mfma_f32_16x16x32_bf16 v[192:195], v[72:75], v[144:147], v[192:195]\n\t"
      "v_mfma_f32_16x16x32_bf16 v[196:199], v[76:79], v[144:147], v[196:199]\n\t"
      "v_mfma_f32_16x16x32_bf16 v[184:187], v[80:83], v[148:151], v[184:187]\n\t"
      "v_mfma_f32_16x16x32_bf16 v[188:191], v[84:87], v[148:151], v[188:191]\n\t"
      "v_mfma_f32_16x16x32_bf16 v[192:195], v[88:91], v[148:151], v[192:195]\n\t"
      "v_mfma_f32_16x16x32_bf16 v[196:199], v[92:95], v[148:151], v[196:199]\n\t"
      "v_mfma_f32_16x16x32_bf16 v[184:187], v[96:99], v[152:155], v[184:187]\n\t"
      "v_mfma_f32_16x16x32_bf16 v[188:191], v[100:103], v[152:155], v[188:191]\n\t"
      "v_mfma_f32_16x16x32_bf16 v[192:195], v[104:107], v[152:155], v[192:195]\n\t"
      "v_mfma_f32_16x16x32_bf16 v[184:187], v[112:115], v[156:159], v[184:187]\n\t"
      "v_mfma_f32_16x16x32_bf16 v[188:191], v[116:119], v[156:159], v[188:191]\n\t"
      "v_mfma_f32_16x16x32_bf16 v[192:195], v[120:123], v[156:159], v[192:195]\n\t"
      "s_waitcnt vmcnt(0)\n\t"
      "s_nop 7\n\t"
      "s_branch NP6B_%=\n\t"
      : "+{s[4:19]}"(s), "+{s[20:21]}"(kr), "+{s22}"(hint), "+{v[240:247]}"(v)
      :
      : "memory", "vcc", "scc",
        "v0", "v1", "v2", "v3", "v4", "v5", "v6", "v7", "v8", "v9", "v10", "v11", "v12", "v13", "v14", "v15", "v16",
        "v17", "v18", "v19", "v20", "v21", "v22", "v23", "v24", "v25", "v26", "v27", "v28", "v29", "v30", "v31", "v32",
        "v33", "v34", "v35", "v36", "v37", "v38", "v39", "v40", "v41", "v42", "v43", "v44", "v45", "v46", "v47", "v48",
        "v49", "v50", "v51", "v52", "v53", "v54", "v55", "v56", "v57", "v58", "v59", "v60", "v61", "v62", "v63", "v64",
        "v65", "v66", "v67", "v68", "v69", "v70", "v71", "v72", "v73", "v74", "v75", "v76", "v77", "v78", "v79", "v80",
        "v81", "v82", "v83", "v84", "v85", "v86", "v87", "v88", "v89", "v90", "v91", "v92", "v93", "v94", "v95", "v96",
        "v97", "v98", "v99", "v100", "v101", "v102", "v103", "v104", "v105", "v106", "v107", "v108", "v109", "v110",
        "v111", "v112", "v113", "v114", "v115", "v116", "v117", "v118", "v119", "v120", "v121", "v122", "v123", "v124",
        "v125", "v126", "v127", "v128", "v129", "v130", "v131", "v132", "v133", "v134", "v135", "v136", "v137", "v138",
        "v139", "v140", "v141", "v142", "v143", "v144", "v145", "v146", "v147", "v148", "v149", "v150", "v151", "v152",
        "v153", "v154", "v155", "v156", "v157", "v158", "v159", "v160", "v161", "v162", "v163", "v164", "v165", "v166",
        "v167", "v168", "v169", "v170", "v171", "v172", "v173", "v174", "v175", "v176", "v177", "v178", "v179", "v180",
        "v181", "v182", "v183", "v184", "v185", "v186", "v187", "v188", "v189", "v190", "v191", "v192", "v193", "v194",
        "v195", "v196", "v197", "v198", "v199", "v200", "v201", "v202", "v203", "v204", "v205", "v206", "v207", "v208",
        "v209", "v210", "v211", "v212", "v213", "v214", "v215", "v216", "v217", "v218", "v219", "v220", "v221", "v222",
        "v223", "v224", "v225", "v226", "v227", "v228", "v229", "v230", "v231", "v232", "v233", "v234", "v235", "v236",
        "v237", "v238", "v239", "v248", "v249", "v250", "v251", "v252", "v253", "s23", "s44", "s45", "s46", "s47",
        "s48", "s49", "s50", "s51", "s52", "s53", "s54", "s55", "s56", "s57", "s58", "s59", "s60", "s61", "s62", "s63",
        "s64", "s65", "s66", "s67", "s68", "s69", "s70"
    );
    __builtin_amdgcn_endpgm();
  } else {
#define B_INDICES                                                                                            \
  const int ib = idx - 16, t = 16 * (4 * side + ((xp + XO) & 3)) + ib;                         \
  const int st = 8 * xp + (ib >> 1), rot = 4 * (ib & 1) + 2 * side - (ib & side & 1);                        \
  const int cb0 = 4 * wave - max(wave - 4, 0);               \
  const int k1 = (ib + 2 * xcd) & 15, k0 = k1 - (k1 >= 14 ? 7 : 0)
    u32x16 acc;
    u32x8 v1, s1;
    u32x4 s3;
    unsigned bits = (wave >> 2) | (wave == 0 ? 16 : 0), id = blockIdx.x, wv = wave;
    {
      LANE_BITS(threadIdx.x & 63);
      B_INDICES;
      v1[0] = (8 * (wave & 3) + lr) * 896 + (wave >> 2) * 128 + ((lc ^ swz_tr(8 * wave + lr)) << 4) + 4096;
      v1[1] = B_X + (4 * g + q4) * 896 + cb0 * 32 + p4 * 8;
      v1[2] = B_PS + (4 * g + q4) * 64 + wave * 8;
      v1[3] = B_PS + (4 * g + p4) * 64 + q4 * 16;
      v1[4] = (t * 16 + 4 * g + p4) * 16;
      v1[5] = lr * 4096 + ((lc ^ swz_tr(lr)) << 4);
      v1[6] = lane * 16;
      v1[7] = WS_HN + (7 * (wave & 1) + 2 * (wave >> 2) + (g & 1)) * 131072 + (t >> 4) * 16384 + (wave & 2) * 4096 + (t & 15) * 512 + (g >> 1) * 256 + c * 16;
      put(s1, 0, (u64)(wo + k0 * 32 * 896 - 4096));
      put(s1, 2, (u64)(ws + WS_ST));
      put(s1, 4, 0);
      put(s1, 6, (u64)(w2 + st * 128 + (wave * 224 + rot * 32) * 4096));
      s3[0] = (wave & 3) * B_UNIT + (wave >> 2) * 1024;
      s3[1] = (wave >> 2) - 1;
      s3[2] = 13 - k0;
      s3[3] = wave * 4096;
    asm volatile(
      "s_waitcnt lgkmcnt(0)\n\ts_nop 4\n\t"
      ".p2align 3\n\t"
      "s_load_dwordx2 s[40:41], s[6:7], 0x20000\n\t"
      "v_mov_b32_e32 v248, -1\n\t"
      "v_mov_b32_e32 v249, -1\n\t"
      "v_mov_b32_e32 v250, -1\n\t"
      "v_mov_b32_e32 v251, -1\n\t"
      "v_add_u32_e32 v221, 4294965504, v224\n\t"
      "v_add_u32_e32 v222, 512, v224\n\t"
      "v_add_u32_e32 v223, 4294966016, v224\n\t"
      "s_bitcmp1_b32 s16, 0\n\t"
      "s_cselect_b64 s[46:47], -1, 0\n\t"
      "s_mov_b64 vcc, s[46:47]\n\t"
      "s_waitcnt vmcnt(3)\n\t"
      "s_barrier\n\t"
      "ds_read_b64_tr_b16 v[204:205], v225\n\t"
      "ds_read_b64_tr_b16 v[206:207], v225 offset:32\n\t"
      "ds_read_b64_tr_b16 v[208:209], v225 offset:64\n\t"
      "ds_read_b64_tr_b16 v[210:211], v225 offset:96\n\t"
      "s_waitcnt lgkmcnt(0)\n\t"
      "s_add_u32 s40, s40, 1\n\t"
      "s_addc_u32 s41, s41, 0\n\t"
      "s_mul_i32 s48, s40, 0x9e3779b9\n\t"
      "s_mul_hi_u32 s9, s40, 0x7f4a7c15\n\t"
      "s_add_u32 s9, s9, s48\n\t"
      "s_mul_i32 s48, s41, 0x7f4a7c15\n\t"
      "s_add_u32 s9, s9, s48\n\t"
      "s_mul_i32 s8, s40, 0x7f4a7c15\n\t"
      "s_add_u32 s8, s8, 0xd192ed03\n\t"
      "s_addc_u32 s9, s9, 0xd1b54a32\n\t"
      "v_mov_b32_e32 v188, s8\n\t"
      "v_mov_b32_e32 v189, s9\n\t"
      "v_mov_b32_e32 v190, s40\n\t"
      "v_mov_b32_e32 v191, s41\n\t"
      "v_mov_b32_e32 v192, 163600\n\t"
      "s_mov_b64 exec, 1\n\t"
      "ds_write_b128 v192, v[188:191]\n\t"
      "s_mov_b64 exec, -1\n\t"
      "v_and_b32_e32 v210, s13, v210\n\t"
      "v_and_b32_e32 v211, s13, v211\n\t"
      "v_lshlrev_b32_e32 v160, 16, v204\n\t"
      "v_and_b32_e32 v161, 0xffff0000, v204\n\t"
      "v_lshlrev_b32_e32 v162, 16, v205\n\t"
      "v_and_b32_e32 v163, 0xffff0000, v205\n\t"
      "v_lshlrev_b32_e32 v164, 16, v206\n\t"
      "v_and_b32_e32 v165, 0xffff0000, v206\n\t"
      "v_lshlrev_b32_e32 v166, 16, v207\n\t"
      "v_and_b32_e32 v167, 0xffff0000, v207\n\t"
      "v_lshlrev_b32_e32 v168, 16, v208\n\t"
      "v_and_b32_e32 v169, 0xffff0000, v208\n\t"
      "v_lshlrev_b32_e32 v170, 16, v209\n\t"
      "v_and_b32_e32 v171, 0xffff0000, v209\n\t"
      "v_lshlrev_b32_e32 v172, 16, v210\n\t"
      "v_and_b32_e32 v173, 0xffff0000, v210\n\t"
      "v_lshlrev_b32_e32 v174, 16, v211\n\t"
      "v_and_b32_e32 v175, 0xffff0000, v211\n\t"
      "v_pk_add_f32 v[188:189], v[160:161], v[164:165]\n\t"
      "v_pk_mul_f32 v[192:193], v[160:161], v[160:161]\n\t"
      "v_pk_add_f32 v[190:191], v[162:163], v[166:167]\n\t"
      "v_pk_mul_f32 v[194:195], v[162:163], v[162:163]\n\t"
      "v_pk_fma_f32 v[192:193], v[164:165], v[164:165], v[192:193]\n\t"
      "v_pk_fma_f32 v[194:195], v[166:167], v[166:167], v[194:195]\n\t"
      "v_pk_add_f32 v[188:189], v[188:189], v[168:169]\n\t"
      "v_pk_fma_f32 v[192:193], v[168:169], v[168:169], v[192:193]\n\t"
      "v_pk_add_f32 v[190:191], v[190:191], v[170:171]\n\t"
      "v_pk_fma_f32 v[194:195], v[170:171], v[170:171], v[194:195]\n\t"
      "s_cbranch_vccnz SB0_%=\n\t"
      "s_nop 0\n\t"
      "v_pk_add_f32 v[188:189], v[188:189], v[172:173]\n\t"
      "v_pk_fma_f32 v[192:193], v[172:173], v[172:173], v[192:193]\n\t"
      "v_pk_add_f32 v[190:191], v[190:191], v[174:175]\n\t"
      "v_pk_fma_f32 v[194:195], v[174:175], v[174:175], v[194:195]\n\t"
      "SB0_%=:\n\t"
      "v_add_f32_dpp v196, v188, v188 row_ror:8 row_mask:0xf bank_mask:0x3\n\t"
      "v_add_f32_dpp v197, v189, v189 row_ror:8 row_mask:0xf bank_mask:0x3\n\t"
      "v_add_f32_dpp v198, v192, v192 row_ror:8 row_mask:0xf bank_mask:0x3\n\t"
      "v_add_f32_dpp v199, v193, v193 row_ror:8 row_mask:0xf bank_mask:0x3\n\t"
      "v_add_f32_dpp v196, v190, v190 row_ror:8 row_mask:0xf bank_mask:0xc\n\t"
      "v_add_f32_dpp v197, v191, v191 row_ror:8 row_mask:0xf bank_mask:0xc\n\t"
      "v_add_f32_dpp v198, v194, v194 row_ror:8 row_mask:0xf bank_mask:0xc\n\t"
      "v_add_f32_dpp v199, v195, v195 row_ror:8 row_mask:0xf bank_mask:0xc\n\t"
      "v_add_f32_dpp v188, v196, v196 row_shl:4 row_mask:0xf bank_mask:0x5\n\t"
      "v_add_f32_dpp v189, v198, v198 row_shl:4 row_mask:0xf bank_mask:0x5\n\t"
      "s_nop 0\n\t"
      "v_add_f32_dpp v188, v197, v197 row_shr:4 row_mask:0xf bank_mask:0xa\n\t"
      "v_add_f32_dpp v189, v199, v199 row_shr:4 row_mask:0xf bank_mask:0xa\n\t"
      "s_nop 0\n\t"
      "v_add_f32_dpp v188, v188, v188 quad_perm:[1,0,3,2] row_mask:0xf bank_mask:0xf\n\t"
      "v_add_f32_dpp v189, v189, v189 quad_perm:[1,0,3,2] row_mask:0xf bank_mask:0xf\n\t"
      "s_nop 0\n\t"
      "v_add_f32_dpp v188, v188, v188 quad_perm:[2,3,0,1] row_mask:0xf bank_mask:0xf\n\t"
      "v_add_f32_dpp v189, v189, v189 quad_perm:[2,3,0,1] row_mask:0xf bank_mask:0xf\n\t"
      "ds_write_b64 v226, v[188:189]\n\t"
      "s_waitcnt lgkmcnt(0)\n\t"
      "s_barrier\n\t"
      "ds_read_b128 v[188:191], v227\n\t"
      "s_waitcnt lgkmcnt(0)\n\t"
      "v_pk_add_f32 v[188:189], v[188:189], v[190:191]\n\t"
      "s_nop 1\n\t"
      "v_add_f32_dpp v188, v188, v188 row_ror:4 row_mask:0xf bank_mask:0xf\n\t"
      "v_add_f32_dpp v189, v189, v189 row_ror:4 row_mask:0xf bank_mask:0xf\n\t"
      "s_nop 0\n\t"
      "v_add_f32_dpp v188, v188, v188 row_ror:8 row_mask:0xf bank_mask:0xf\n\t"
      "v_add_f32_dpp v189, v189, v189 row_ror:8 row_mask:0xf bank_mask:0xf\n\t"
      "v_mul_f32_e32 v188, 0x3b124925, v188\n\t"
      "v_mul_f32_e32 v189, 0x3b124925, v189\n\t"
      "v_fma_f32 v189, -v188, v188, v189\n\t"
      "v_max_f32_e32 v189, 0, v189\n\t"
      "s_bitcmp1_b32 s16, 4\n\t"
      "s_cbranch_scc0 GW_%=\n\t"
      "v_add_f32_e32 v189, 0x3727c5ac, v189\n\t"
      "v_rsq_f32_e32 v189, v189\n\t"
      "v_xor_b32_e32 v188, 0x80000000, v188\n\t"
      "v_xor_b32_e32 v190, s8, v188\n\t"
      "v_xor_b32_e32 v191, s9, v189\n\t"
      "s_mov_b32 s48, 0xf000f\n\t"
      "s_mov_b32 s49, 0xf000f\n\t"
      "s_mov_b64 exec, s[48:49]\n\t"
      "global_store_dwordx4 v228, v[188:191], s[6:7] sc1\n\t"
      "s_mov_b64 exec, -1\n\t"
      "GW_%=:\n\t"
      "s_add_u32 m0, s12, 0\n\t"
      "s_nop 0\n\t"
      "global_load_lds_dwordx4 v224, s[4:5]\n\t"
      "global_load_lds_dwordx4 v221, s[4:5] offset:2048\n\t"
      "s_add_u32 m0, s12, 4096\n\t"
      "s_nop 0\n\t"
      "global_load_lds_dwordx4 v222, s[4:5]\n\t"
      "s_cbranch_vccnz WR0_%=\n\t"
      "global_load_lds_dwordx4 v223, s[4:5] offset:2048\n\t"
      "WR0_%=:\n\t"
      "s_sub_u32 s14, s14, 1\n\t"
      "s_cselect_b32 s48, -13, 1\n\t"
      "s_cselect_b32 s49, -1, 0\n\t"
      "s_mul_i32 s48, s48, 28672\n\t"
      "s_add_u32 s4, s4, s48\n\t"
      "s_addc_u32 s5, s5, s49\n\t"
      "s_add_u32 m0, s12, 28672\n\t"
      "s_nop 0\n\t"
      "global_load_lds_dwordx4 v224, s[4:5]\n\t"
      "global_load_lds_dwordx4 v221, s[4:5] offset:2048\n\t"
      "s_add_u32 m0, s12, 32768\n\t"
      "s_nop 0\n\t"
      "global_load_lds_dwordx4 v222, s[4:5]\n\t"
      "s_cbranch_vccnz WR1_%=\n\t"
      "global_load_lds_dwordx4 v223, s[4:5] offset:2048\n\t"
      "WR1_%=:\n\t"
      "s_sub_u32 s14, s14, 1\n\t"
      "s_cselect_b32 s48, -13, 1\n\t"
      "s_cselect_b32 s49, -1, 0\n\t"
      "s_mul_i32 s48, s48, 28672\n\t"
      "s_add_u32 s4, s4, s48\n\t"
      "s_addc_u32 s5, s5, s49\n\t"
      "s_waitcnt lgkmcnt(0)\n\t"
      "s_nop 1\n\t"
      : "+{s[4:11]}"(s1), "+{s[12:15]}"(s3), "+{s16}"(bits), "+{s17}"(id), "+{s18}"(wv), "+{v[224:231]}"(v1), "={v[160:175]}"(acc)
      :
      : "memory", "vcc", "scc",
        "v176", "v177", "v178", "v179", "v180", "v181", "v182", "v183", "v184", "v185", "v186", "v187", "v188", "v189",
        "v190", "v191", "v192", "v193", "v194", "v195", "v196", "v197", "v198", "v199", "v200", "v201", "v202", "v203",
        "v204", "v205", "v206", "v207", "v208", "v209", "v210", "v211", "v212", "v213", "v214", "v215", "v216", "v217",
        "v218", "v219", "v220", "v221", "v222", "v223", "v248", "v249", "v250", "v251", "s40", "s41", "s42", "s43",
        "s44", "s45", "s46", "s47", "s48", "s49", "s50", "s51"
    );
    }
    PIN();
    {
    const int wave = wv & 0x3ffffff, xcd = id & 7, idx = id >> 3, side = xcd >> 2, xp = xcd & 3;
    B_INDICES;
    const int dg = 2 * side + (ib & 1), blk0 = 7 * dg, rn = (rot + W2E) % 7;
    bits |= (2 - ((wave >> 1) & 2)) | (((t + wave - 1) >> 26) & 32) | ((((wave - 7) & (t - 112)) >> 25) & 64) | (((t - 112) >> 24) & 128);
    u32x4 vo, v3;
    u64 par = ST_PW + xcd * 128;
    {
      LANE_BITS(v1[6] >> 4);
      u32x8 v;
      u32x4 s;
      const unsigned tv = t + (lane >> 6), wv = wave + (lane >> 6), sv = (tv >> 6) * 8192;
      const unsigned ab = g * B_UNIT + q4 * 128 + (p4 & 1) * 8, al = ((p4 >> 1) ^ swz_tr(8 * g + q4)) << 4;
      const unsigned oi = B_X + c * B_OP, b0 = 112 * wv + 8 * g - 64 * k0;
#pragma unroll
      for (int b = 0; b < 4; ++b) {
        const unsigned cb = 4 * wv - (wv >> 2) * (wv & 3) + b;
        v[b] = ab + (cb >> 2) * 1024 + (((cb & 3) << 5) ^ al);
        const unsigned by = b0 + 56 * (b >> 1) + 32 * (b & 1);
        vo[b] = oi + min(by, by + 896);
      }
      v[4] = oi + 16 * g;
      v[5] = WS_G + tv * 16384 + wv * 1024 + lane * 16;
      v[6] = v1[7] + 4 * 131072;
      v[7] = v[5] + 8192;
      v3[0] = (((wv & 3) + (lr & 2) * 2) * 224 + 32 + 64 * side + (tv & 15) * 2 + (lr & 1) + (lr >> 2) * 32) * 4096 + lc * 128 + xp * 1024;
      v3[1] = ST_H3 + (lc & 3) * 32768 + (lc >> 2) * 8192 + 2 * sv + (tv & 63) * 128;
      v3[2] = ST_H2 + sv + (tv & 32) * 512 + (tv & 24) * 256 + wv * 256 + (tv & 4) * 32 + (lane & 1) * 64;
      v3[3] = ST_CK + sv + (tv & 56) * 2048 + (tv & 4) * 1024 + wv * 512 + (g >> 1) * 256 + (tv & 3) * 64 + c * 4;
      put(s, 0, (u64)w2);
      put(s, 2, (u64)(ws + (unsigned)(WS_O + (t & 63) * 32768 + (wave >> 2) * 16384 + side * 8192 + (wave & 3) * 2048)));
    asm volatile(
      "s_waitcnt lgkmcnt(0)\n\ts_nop 4\n\t"
      ".p2align 3\n\t"
      "s_nop 0\n\t"
      "v_mov_b32_e32 v248, -1\n\t"
      "v_mov_b32_e32 v249, -1\n\t"
      "v_mov_b32_e32 v250, -1\n\t"
      "v_mov_b32_e64 v251, -1\n\t"
      "v_add_u32_e32 v221, 4294965504, v224\n\t"
      "v_add_u32_e32 v222, 512, v224\n\t"
      "v_add_u32_e32 v223, 4294966016, v224\n\t"
      "s_bitcmp1_b32 s16, 0\n\t"
      "s_cselect_b64 s[46:47], -1, 0\n\t"
      "s_mov_b64 vcc, s[46:47]\n\t"
      "s_mov_b32 s44, 0xffffffff\n\t"
      "s_mov_b32 s45, 0xffff\n\t"
      "s_or_b32 s42, s13, 0xffff\n\t"
      "s_or_b32 s43, s13, 0xffff\n\t"
      "v_xor_b32_e64 v176, 64, v229\n\t"
      "v_add_u32_e32 v177, 63488, v229\n\t"
      "v_add_u32_e32 v178, 95232, v176\n\t"
      "v_add_u32_e32 v176, 31744, v176\n\t"
      "s_add_u32 m0, s15, 130560\n\t"
      "s_nop 0\n\t"
      "global_load_lds_dwordx4 v229, s[10:11]\n\t"
      "global_load_lds_dwordx4 v176, s[10:11] offset:1024\n\t"
      "global_load_lds_dwordx4 v177, s[10:11] offset:2048\n\t"
      "global_load_lds_dwordx4 v178, s[10:11] offset:3072\n\t"
      "s_add_u32 s10, s10, 131072\n\t"
      "s_addc_u32 s11, s11, 0\n\t"
      "s_cbranch_vccnz SN3_%=\n\t"
      "global_store_dwordx4 v231, v[248:251], s[6:7] sc1\n\t"
      "SN3_%=:\n\t"
      "s_cbranch_vccnz WW0_%=\n\t"
      "s_waitcnt vmcnt(8)\n\t"
      "s_branch WX0_%=\n\t"
      "WW0_%=:\n\t"
      "s_waitcnt vmcnt(7)\n\t"
      "WX0_%=:\n\t"
      "s_waitcnt lgkmcnt(0)\n\t"
      "s_barrier\n\t"
      "ds_read_b64_tr_b16 v[0:1], v232\n\t"
      "ds_read_b64_tr_b16 v[2:3], v232 offset:512\n\t"
      "ds_read_b64_tr_b16 v[4:5], v233\n\t"
      "ds_read_b64_tr_b16 v[6:7], v233 offset:512\n\t"
      "ds_read_b64_tr_b16 v[8:9], v234\n\t"
      "ds_read_b64_tr_b16 v[10:11], v234 offset:512\n\t"
      "ds_read_b64_tr_b16 v[12:13], v235\n\t"
      "ds_read_b64_tr_b16 v[14:15], v235 offset:512\n\t"
      "s_add_u32 m0, s12, 57344\n\t"
      "s_nop 0\n\t"
      "global_load_lds_dwordx4 v224, s[4:5]\n\t"
      "global_load_lds_dwordx4 v221, s[4:5] offset:2048\n\t"
      "s_add_u32 m0, s12, 61440\n\t"
      "s_nop 0\n\t"
      "global_load_lds_dwordx4 v222, s[4:5]\n\t"
      "s_cbranch_vccnz WR2_%=\n\t"
      "global_load_lds_dwordx4 v223, s[4:5] offset:2048\n\t"
      "WR2_%=:\n\t"
      "s_sub_u32 s14, s14, 1\n\t"
      "s_cselect_b32 s48, -13, 1\n\t"
      "s_cselect_b32 s49, -1, 0\n\t"
      "s_mul_i32 s48, s48, 28672\n\t"
      "s_add_u32 s4, s4, s48\n\t"
      "s_addc_u32 s5, s5, s49\n\t"
      "s_cbranch_vccnz WW1_%=\n\t"
      "s_waitcnt vmcnt(8)\n\t"
      "s_branch WX1_%=\n\t"
      "WW1_%=:\n\t"
      "s_waitcnt vmcnt(7)\n\t"
      "WX1_%=:\n\t"
      "s_waitcnt lgkmcnt(0)\n\t"
      "s_barrier\n\t"
      "ds_read_b64_tr_b16 v[16:17], v232 offset:28672\n\t"
      "ds_read_b64_tr_b16 v[18:19], v232 offset:29184\n\t"
      "ds_read_b64_tr_b16 v[20:21], v233 offset:28672\n\t"
      "ds_read_b64_tr_b16 v[22:23], v233 offset:29184\n\t"
      "ds_read_b64_tr_b16 v[24:25], v234 offset:28672\n\t"
      "ds_read_b64_tr_b16 v[26:27], v234 offset:29184\n\t"
      "ds_read_b64_tr_b16 v[28:29], v235 offset:28672\n\t"
      "ds_read_b64_tr_b16 v[30:31], v235 offset:29184\n\t"
      "s_add_u32 m0, s12, 86016\n\t"
      "s_nop 0\n\t"
      "global_load_lds_dwordx4 v224, s[4:5]\n\t"
      "global_load_lds_dwordx4 v221, s[4:5] offset:2048\n\t"
      "s_add_u32 m0, s12, 90112\n\t"
      "s_nop 0\n\t"
      "global_load_lds_dwordx4 v222, s[4:5]\n\t"
      "s_cbranch_vccnz WR3_%=\n\t"
      "global_load_lds_dwordx4 v223, s[4:5] offset:2048\n\t"
      "WR3_%=:\n\t"
      "s_sub_u32 s14, s14, 1\n\t"
      "s_cselect_b32 s48, -13, 1\n\t"
      "s_cselect_b32 s49, -1, 0\n\t"
      "s_mul_i32 s48, s48, 28672\n\t"
      "s_add_u32 s4, s4, s48\n\t"
      "s_addc_u32 s5, s5, s49\n\t"
      "v_add_u32_e32 v232, 57344, v232\n\t"
      "v_add_u32_e32 v233, 57344, v233\n\t"
      "v_add_u32_e32 v234, 57344, v234\n\t"
      "v_add_u32_e32 v235, 57344, v235\n\t"
      "s_cbranch_vccnz WW2_%=\n\t"
      "s_waitcnt vmcnt(4)\n\t"
      "s_branch WX2_%=\n\t"
      "WW2_%=:\n\t"
      "s_waitcnt vmcnt(3)\n\t"
      "WX2_%=:\n\t"
      "s_waitcnt lgkmcnt(0)\n\t"
      "s_barrier\n\t"
      "ds_read_b64_tr_b16 v[32:33], v232\n\t"
      "ds_read_b64_tr_b16 v[34:35], v232 offset:512\n\t"
      "ds_read_b64_tr_b16 v[36:37], v233\n\t"
      "ds_read_b64_tr_b16 v[38:39], v233 offset:512\n\t"
      "ds_read_b64_tr_b16 v[40:41], v234\n\t"
      "ds_read_b64_tr_b16 v[42:43], v234 offset:512\n\t"
      "ds_read_b64_tr_b16 v[44:45], v235\n\t"
      "ds_read_b64_tr_b16 v[46:47], v235 offset:512\n\t"
      "s_add_u32 m0, s12, 0\n\t"
      "s_nop 0\n\t"
      "global_load_lds_dwordx4 v224, s[4:5]\n\t"
      "global_load_lds_dwordx4 v221, s[4:5] offset:2048\n\t"
      "s_add_u32 m0, s12, 4096\n\t"
      "s_nop 0\n\t"
      "global_load_lds_dwordx4 v222, s[4:5]\n\t"
      "s_cbranch_vccnz WR4_%=\n\t"
      "global_load_lds_dwordx4 v223, s[4:5] offset:2048\n\t"
      "WR4_%=:\n\t"
      "s_sub_u32 s14, s14, 1\n\t"
      "s_cselect_b32 s48, -13, 1\n\t"
      "s_cselect_b32 s49, -1, 0\n\t"
      "s_mul_i32 s48, s48, 28672\n\t"
      "s_add_u32 s4, s4, s48\n\t"
      "s_addc_u32 s5, s5, s49\n\t"
      "s_cbranch_vccnz WW3_%=\n\t"
      "s_waitcnt vmcnt(4)\n\t"
      "s_branch WX3_%=\n\t"
      "WW3_%=:\n\t"
      "s_waitcnt vmcnt(3)\n\t"
      "WX3_%=:\n\t"
      "s_waitcnt lgkmcnt(0)\n\t"
      "s_barrier\n\t"
      "ds_read_b64_tr_b16 v[48:49], v232 offset:28672\n\t"
      "ds_read_b64_tr_b16 v[50:51], v232 offset:29184\n\t"
      "ds_read_b64_tr_b16 v[52:53], v233 offset:28672\n\t"
      "ds_read_b64_tr_b16 v[54:55], v233 offset:29184\n\t"
      "ds_read_b64_tr_b16 v[56:57], v234 offset:28672\n\t"
      "ds_read_b64_tr_b16 v[58:59], v234 offset:29184\n\t"
      "ds_read_b64_tr_b16 v[60:61], v235 offset:28672\n\t"
      "ds_read_b64_tr_b16 v[62:63], v235 offset:29184\n\t"
      "s_add_u32 m0, s12, 28672\n\t"
      "s_nop 0\n\t"
      "global_load_lds_dwordx4 v224, s[4:5]\n\t"
      "global_load_lds_dwordx4 v221, s[4:5] offset:2048\n\t"
      "s_add_u32 m0, s12, 32768\n\t"
      "s_nop 0\n\t"
      "global_load_lds_dwordx4 v222, s[4:5]\n\t"
      "s_cbranch_vccnz WR5_%=\n\t"
      "global_load_lds_dwordx4 v223, s[4:5] offset:2048\n\t"
      "WR5_%=:\n\t"
      "s_sub_u32 s14, s14, 1\n\t"
      "s_cselect_b32 s48, -13, 1\n\t"
      "s_cselect_b32 s49, -1, 0\n\t"
      "s_mul_i32 s48, s48, 28672\n\t"
      "s_add_u32 s4, s4, s48\n\t"
      "s_addc_u32 s5, s5, s49\n\t"
      "v_add_u32_e32 v232, 4294909952, v232\n\t"
      "v_add_u32_e32 v233, 4294909952, v233\n\t"
      "v_add_u32_e32 v234, 4294909952, v234\n\t"
      "v_add_u32_e32 v235, 4294909952, v235\n\t"
      "s_cbranch_vccnz WW4_%=\n\t"
      "s_waitcnt vmcnt(4)\n\t"
      "s_branch WX4_%=\n\t"
      "WW4_%=:\n\t"
      "s_waitcnt vmcnt(3)\n\t"
      "WX4_%=:\n\t"
      "s_waitcnt lgkmcnt(0)\n\t"
      "s_barrier\n\t"
      "ds_read_b64_tr_b16 v[64:65], v232\n\t"
      "ds_read_b64_tr_b16 v[66:67], v232 offset:512\n\t"
      "ds_read_b64_tr_b16 v[68:69], v233\n\t"
      "ds_read_b64_tr_b16 v[70:71], v233 offset:512\n\t"
      "ds_read_b64_tr_b16 v[72:73], v234\n\t"
      "ds_read_b64_tr_b16 v[74:75], v234 offset:512\n\t"
      "ds_read_b64_tr_b16 v[76:77], v235\n\t"
      "ds_read_b64_tr_b16 v[78:79], v235 offset:512\n\t"
      "s_add_u32 m0, s12, 57344\n\t"
      "s_nop 0\n\t"
      "global_load_lds_dwordx4 v224, s[4:5]\n\t"
      "global_load_lds_dwordx4 v221, s[4:5] offset:2048\n\t"
      "s_add_u32 m0, s12, 61440\n\t"
      "s_nop 0\n\t"
      "global_load_lds_dwordx4 v222, s[4:5]\n\t"
      "s_cbranch_vccnz WR6_%=\n\t"
      "global_load_lds_dwordx4 v223, s[4:5] offset:2048\n\t"
      "WR6_%=:\n\t"
      "s_sub_u32 s14, s14, 1\n\t"
      "s_cselect_b32 s48, -13, 1\n\t"
      "s_cselect_b32 s49, -1, 0\n\t"
      "s_mul_i32 s48, s48, 28672\n\t"
      "s_add_u32 s4, s4, s48\n\t"
      "s_addc_u32 s5, s5, s49\n\t"
      "s_cbranch_vccnz WW5_%=\n\t"
      "s_waitcnt vmcnt(4)\n\t"
      "s_branch WX5_%=\n\t"
      "WW5_%=:\n\t"
      "s_waitcnt vmcnt(3)\n\t"
      "WX5_%=:\n\t"
      "s_waitcnt lgkmcnt(0)\n\t"
      "s_barrier\n\t"
      "ds_read_b64_tr_b16 v[80:81], v232 offset:28672\n\t"
      "ds_read_b64_tr_b16 v[82:83], v232 offset:29184\n\t"
      "ds_read_b64_tr_b16 v[84:85], v233 offset:28672\n\t"
      "ds_read_b64_tr_b16 v[86:87], v233 offset:29184\n\t"
      "ds_read_b64_tr_b16 v[88:89], v234 offset:28672\n\t"
      "ds_read_b64_tr_b16 v[90:91], v234 offset:29184\n\t"
      "ds_read_b64_tr_b16 v[92:93], v235 offset:28672\n\t"
      "ds_read_b64_tr_b16 v[94:95], v235 offset:29184\n\t"
      "s_add_u32 m0, s12, 86016\n\t"
      "s_nop 0\n\t"
      "global_load_lds_dwordx4 v224, s[4:5]\n\t"
      "global_load_lds_dwordx4 v221, s[4:5] offset:2048\n\t"
      "s_add_u32 m0, s12, 90112\n\t"
      "s_nop 0\n\t"
      "global_load_lds_dwordx4 v222, s[4:5]\n\t"
      "s_cbranch_vccnz WR7_%=\n\t"
      "global_load_lds_dwordx4 v223, s[4:5] offset:2048\n\t"
      "WR7_%=:\n\t"
      "s_sub_u32 s14, s14, 1\n\t"
      "s_cselect_b32 s48, -13, 1\n\t"
      "s_cselect_b32 s49, -1, 0\n\t"
      "s_mul_i32 s48, s48, 28672\n\t"
      "s_add_u32 s4, s4, s48\n\t"
      "s_addc_u32 s5, s5, s49\n\t"
      "v_add_u32_e32 v232, 57344, v232\n\t"
      "v_add_u32_e32 v233, 57344, v233\n\t"
      "v_add_u32_e32 v234, 57344, v234\n\t"
      "v_add_u32_e32 v235, 57344, v235\n\t"
      "s_cbranch_vccnz WW6_%=\n\t"
      "s_waitcnt vmcnt(4)\n\t"
      "s_branch WX6_%=\n\t"
      "WW6_%=:\n\t"
      "s_waitcnt vmcnt(3)\n\t"
      "WX6_%=:\n\t"
      "s_waitcnt lgkmcnt(0)\n\t"
      "s_barrier\n\t"
      "ds_read_b64_tr_b16 v[96:97], v232\n\t"
      "ds_read_b64_tr_b16 v[98:99], v232 offset:512\n\t"
      "ds_read_b64_tr_b16 v[100:101], v233\n\t"
      "ds_read_b64_tr_b16 v[102:103], v233 offset:512\n\t"
      "ds_read_b64_tr_b16 v[104:105], v234\n\t"
      "ds_read_b64_tr_b16 v[106:107], v234 offset:512\n\t"
      "ds_read_b64_tr_b16 v[108:109], v235\n\t"
      "ds_read_b64_tr_b16 v[110:111], v235 offset:512\n\t"
      "s_add_u32 m0, s12, 0\n\t"
      "s_nop 0\n\t"
      "global_load_lds_dwordx4 v224, s[4:5]\n\t"
      "global_load_lds_dwordx4 v221, s[4:5] offset:2048\n\t"
      "s_add_u32 m0, s12, 4096\n\t"
      "s_nop 0\n\t"
      "global_load_lds_dwordx4 v222, s[4:5]\n\t"
      "s_cbranch_vccnz WR8_%=\n\t"
      "global_load_lds_dwordx4 v223, s[4:5] offset:2048\n\t"
      "WR8_%=:\n\t"
      "s_sub_u32 s14, s14, 1\n\t"
      "s_cselect_b32 s48, -13, 1\n\t"
      "s_cselect_b32 s49, -1, 0\n\t"
      "s_mul_i32 s48, s48, 28672\n\t"
      "s_add_u32 s4, s4, s48\n\t"
      "s_addc_u32 s5, s5, s49\n\t"
      "s_cbranch_vccnz WW7_%=\n\t"
      "s_waitcnt vmcnt(4)\n\t"
      "s_branch WX7_%=\n\t"
      "WW7_%=:\n\t"
      "s_waitcnt vmcnt(3)\n\t"
      "WX7_%=:\n\t"
      "s_waitcnt lgkmcnt(0)\n\t"
      "s_barrier\n\t"
      "ds_read_b64_tr_b16 v[112:113], v232 offset:28672\n\t"
      "ds_read_b64_tr_b16 v[114:115], v232 offset:29184\n\t"
      "ds_read_b64_tr_b16 v[116:117], v233 offset:28672\n\t"
      "ds_read_b64_tr_b16 v[118:119], v233 offset:29184\n\t"
      "ds_read_b64_tr_b16 v[120:121], v234 offset:28672\n\t"
      "ds_read_b64_tr_b16 v[122:123], v234 offset:29184\n\t"
      "ds_read_b64_tr_b16 v[124:125], v235 offset:28672\n\t"
      "ds_read_b64_tr_b16 v[126:127], v235 offset:29184\n\t"
      "s_add_u32 m0, s12, 28672\n\t"
      "s_nop 0\n\t"
      "global_load_lds_dwordx4 v224, s[4:5]\n\t"
      "global_load_lds_dwordx4 v221, s[4:5] offset:2048\n\t"
      "s_add_u32 m0, s12, 32768\n\t"
      "s_nop 0\n\t"
      "global_load_lds_dwordx4 v222, s[4:5]\n\t"
      "s_cbranch_vccnz WR9_%=\n\t"
      "global_load_lds_dwordx4 v223, s[4:5] offset:2048\n\t"
      "WR9_%=:\n\t"
      "s_sub_u32 s14, s14, 1\n\t"
      "s_cselect_b32 s48, -13, 1\n\t"
      "s_cselect_b32 s49, -1, 0\n\t"
      "s_mul_i32 s48, s48, 28672\n\t"
      "s_add_u32 s4, s4, s48\n\t"
      "s_addc_u32 s5, s5, s49\n\t"
      "s_nop 0\n\t"
      "v_add_u32_e32 v232, 4294909952, v232\n\t"
      "v_add_u32_e32 v233, 4294909952, v233\n\t"
      "v_add_u32_e32 v234, 4294909952, v234\n\t"
      "v_add_u32_e32 v235, 4294909952, v235\n\t"
      "s_cbranch_vccnz WW8_%=\n\t"
      "s_waitcnt vmcnt(4)\n\t"
      "s_branch WX8_%=\n\t"
      "WW8_%=:\n\t"
      "s_waitcnt vmcnt(3)\n\t"
      "WX8_%=:\n\t"
      "s_waitcnt lgkmcnt(0)\n\t"
      "s_barrier\n\t"
      "ds_read_b64_tr_b16 v[128:129], v232\n\t"
      "ds_read_b64_tr_b16 v[130:131], v232 offset:512\n\t"
      "ds_read_b64_tr_b16 v[132:133], v233\n\t"
      "ds_read_b64_tr_b16 v[134:135], v233 offset:512\n\t"
      "ds_read_b64_tr_b16 v[136:137], v234\n\t"
      "ds_read_b64_tr_b16 v[138:139], v234 offset:512\n\t"
      "ds_read_b64_tr_b16 v[140:141], v235\n\t"
      "ds_read_b64_tr_b16 v[142:143], v235 offset:512\n\t"
      "s_add_u32 m0, s12, 57344\n\t"
      "s_nop 0\n\t"
      "global_load_lds_dwordx4 v224, s[4:5]\n\t"
      "global_load_lds_dwordx4 v221, s[4:5] offset:2048\n\t"
      "s_add_u32 m0, s12, 61440\n\t"
      "s_nop 0\n\t"
      "global_load_lds_dwordx4 v222, s[4:5]\n\t"
      "s_cbranch_vccnz WR10_%=\n\t"
      "global_load_lds_dwordx4 v223, s[4:5] offset:2048\n\t"
      "WR10_%=:\n\t"
      "s_sub_u32 s14, s14, 1\n\t"
      "s_cselect_b32 s48, -13, 1\n\t"
      "s_cselect_b32 s49, -1, 0\n\t"
      "s_mul_i32 s48, s48, 28672\n\t"
      "s_add_u32 s4, s4, s48\n\t"
      "s_addc_u32 s5, s5, s49\n\t"
      "s_cbranch_vccnz WW9_%=\n\t"
      "s_waitcnt vmcnt(4)\n\t"
      "s_branch WX9_%=\n\t"
      "WW9_%=:\n\t"
      "s_waitcnt vmcnt(3)\n\t"
      "WX9_%=:\n\t"
      "s_waitcnt lgkmcnt(0)\n\t"
      "s_barrier\n\t"
      "ds_read_b64_tr_b16 v[144:145], v232 offset:28672\n\t"
      "ds_read_b64_tr_b16 v[146:147], v232 offset:29184\n\t"
      "ds_read_b64_tr_b16 v[148:149], v233 offset:28672\n\t"
      "ds_read_b64_tr_b16 v[150:151], v233 offset:29184\n\t"
      "ds_read_b64_tr_b16 v[152:153], v234 offset:28672\n\t"
      "ds_read_b64_tr_b16 v[154:155], v234 offset:29184\n\t"
      "ds_read_b64_tr_b16 v[156:157], v235 offset:28672\n\t"
      "ds_read_b64_tr_b16 v[158:159], v235 offset:29184\n\t"
      "s_add_u32 m0, s12, 86016\n\t"
      "s_nop 0\n\t"
      "global_load_lds_dwordx4 v224, s[4:5]\n\t"
      "global_load_lds_dwordx4 v221, s[4:5] offset:2048\n\t"
      "s_add_u32 m0, s12, 90112\n\t"
      "s_nop 0\n\t"
      "global_load_lds_dwordx4 v222, s[4:5]\n\t"
      "s_cbranch_vccnz WR11_%=\n\t"
      "global_load_lds_dwordx4 v223, s[4:5] offset:2048\n\t"
      "WR11_%=:\n\t"
      "s_sub_u32 s14, s14, 1\n\t"
      "s_cselect_b32 s48, -13, 1\n\t"
      "s_cselect_b32 s49, -1, 0\n\t"
      "s_mul_i32 s48, s48, 28672\n\t"
      "s_add_u32 s4, s4, s48\n\t"
      "s_addc_u32 s5, s5, s49\n\t"
      "v_add_u32_e32 v232, 57344, v232\n\t"
      "v_add_u32_e32 v233, 57344, v233\n\t"
      "v_add_u32_e32 v234, 57344, v234\n\t"
      "v_add_u32_e32 v235, 57344, v235\n\t"
      "v_min_u32_e32 v184, 32, v230\n\t"
      "v_add_u32_e32 v184, s28, v184\n\t"
      "global_load_dwordx4 v[180:183], v184, s[6:7] sc1\n\t"
      "s_cbranch_vccnz WW10_%=\n\t"
      "s_waitcnt vmcnt(5)\n\t"
      "s_branch WX10_%=\n\t"
      "WW10_%=:\n\t"
      "s_waitcnt vmcnt(4)\n\t"
      "WX10_%=:\n\t"
      "s_waitcnt lgkmcnt(0)\n\t"
      "s_barrier\n\t"
      "ds_read_b64_tr_b16 v[188:189], v232\n\t"
      "ds_read_b64_tr_b16 v[190:191], v232 offset:512\n\t"
      "ds_read_b64_tr_b16 v[192:193], v233\n\t"
      "ds_read_b64_tr_b16 v[194:195], v233 offset:512\n\t"
      "ds_read_b64_tr_b16 v[196:197], v234\n\t"
      "ds_read_b64_tr_b16 v[198:199], v234 offset:512\n\t"
      "ds_read_b64_tr_b16 v[200:201], v235\n\t"
      "ds_read_b64_tr_b16 v[202:203], v235 offset:512\n\t"
      "s_add_u32 m0, s12, 0\n\t"
      "s_nop 0\n\t"
      "global_load_lds_dwordx4 v224, s[4:5]\n\t"
      "global_load_lds_dwordx4 v221, s[4:5] offset:2048\n\t"
      "s_add_u32 m0, s12, 4096\n\t"
      "s_nop 0\n\t"
      "global_load_lds_dwordx4 v222, s[4:5]\n\t"
      "s_cbranch_vccnz WR12_%=\n\t"
      "global_load_lds_dwordx4 v223, s[4:5] offset:2048\n\t"
      "WR12_%=:\n\t"
      "s_sub_u32 s14, s14, 1\n\t"
      "s_cselect_b32 s48, -13, 1\n\t"
      "s_cselect_b32 s49, -1, 0\n\t"
      "s_mul_i32 s48, s48, 28672\n\t"
      "s_add_u32 s4, s4, s48\n\t"
      "s_addc_u32 s5, s5, s49\n\t"
      "s_cbranch_vccnz WW11_%=\n\t"
      "s_waitcnt vmcnt(5)\n\t"
      "s_branch WX11_%=\n\t"
      "WW11_%=:\n\t"
      "s_waitcnt vmcnt(4)\n\t"
      "WX11_%=:\n\t"
      "s_waitcnt lgkmcnt(0)\n\t"
      "s_barrier\n\t"
      "ds_read_b64_tr_b16 v[204:205], v232 offset:28672\n\t"
      "ds_read_b64_tr_b16 v[206:207], v232 offset:29184\n\t"
      "ds_read_b64_tr_b16 v[208:209], v233 offset:28672\n\t"
      "ds_read_b64_tr_b16 v[210:211], v233 offset:29184\n\t"
      "ds_read_b64_tr_b16 v[212:213], v234 offset:28672\n\t"
      "ds_read_b64_tr_b16 v[214:215], v234 offset:29184\n\t"
      "ds_read_b64_tr_b16 v[216:217], v235 offset:28672\n\t"
      "ds_read_b64_tr_b16 v[218:219], v235 offset:29184\n\t"
      "s_add_u32 m0, s12, 28672\n\t"
      "s_nop 0\n\t"
      "global_load_lds_dwordx4 v224, s[4:5]\n\t"
      "global_load_lds_dwordx4 v221, s[4:5] offset:2048\n\t"
      "s_add_u32 m0, s12, 32768\n\t"
      "s_nop 0\n\t"
      "global_load_lds_dwordx4 v222, s[4:5]\n\t"
      "s_cbranch_vccnz WR13_%=\n\t"
      "global_load_lds_dwordx4 v223, s[4:5] offset:2048\n\t"
      "WR13_%=:\n\t"
      "v_add_u32_e32 v232, 4294909952, v232\n\t"
      "v_add_u32_e32 v233, 4294909952, v233\n\t"
      "v_add_u32_e32 v234, 4294909952, v234\n\t"
      "v_add_u32_e32 v235, 4294909952, v235\n\t"
      "s_waitcnt vmcnt(6)\n\t"
      "v_xor_b32_e32 v182, v182, v180\n\t"
      "v_xor_b32_e64 v183, v183, v181\n\t"
      "v_cmp_ne_u64_e32 vcc, s[8:9], v[182:183]\n\t"
      "s_cbranch_vccnz PWR_%=\n\t"
      "PWB_%=:\n\t"
      "v_readlane_b32 s48, v180, 0\n\t"
      "v_readlane_b32 s28, v180, 1\n\t"
      "v_readlane_b32 s29, v180, 2\n\t"
      "v_mov_b32_e32 v252, s8\n\t"
      "v_mov_b32_e32 v253, s9\n\t"
      "s_xor_b32 s22, s22, s48\n\t"
      "v_xor_b32_e32 v231, s28, v231\n\t"
      "v_xor_b32_e32 v238, s28, v238\n\t"
      "v_xor_b32_e32 v246, s28, v246\n\t"
      "v_xor_b32_e32 v247, s28, v247\n\t"
      "v_xor_b32_e32 v245, s29, v245\n\t"
      "s_waitcnt lgkmcnt(0)\n\t"
      "s_waitcnt vmcnt(0)\n\t"
      "s_mov_b32 s51, 0\n\t"
      "s_branch QE_%=\n\t"
      ".p2align 6\n\t"
      "QL_%=:\n\t"
      "s_sleep 2\n\t"
      "s_add_u32 s51, s51, 1\n\t"
      "s_cmp_lt_u32 s51, 262144\n\t"
      "s_cbranch_scc0 QX_%=\n\t"
      "QE_%=:\n\t"
      "s_mov_b64 exec, 3\n\t"
      "v_mov_b64_e32 v[176:177], 0\n\t"
      "global_atomic_or_x2 v[176:177], v246, v[176:177], s[6:7] sc0 sc1\n\t"
      "s_waitcnt vmcnt(0)\n\t"
      "v_cmp_ne_u64_e32 vcc, s[8:9], v[176:177]\n\t"
      "s_mov_b64 exec, -1\n\t"
      "s_cbranch_vccnz QL_%=\n\t"
      "QX_%=:\n\t"
      "s_mov_b32 s51, 0\n\t"
      "s_branch OE_%=\n\t"
      "OL_%=:\n\t"
      "s_sleep 2\n\t"
      "s_add_u32 s51, s51, 1\n\t"
      "s_cmp_lt_u32 s51, 262144\n\t"
      "s_cbranch_scc0 OX_%=\n\t"
      "OE_%=:\n\t"
      "global_load_dwordx4 v[176:179], v230, s[22:23] sc1\n\t"
      "global_load_dwordx4 v[180:183], v230, s[22:23] offset:1024 sc1\n\t"
      "global_load_dword v186, v247, s[6:7] sc1\n\t"
      "s_waitcnt vmcnt(2)\n\t"
      "ds_write_b64 v240, v[176:177]\n\t"
      "s_mov_b64 exec, s[44:45]\n\t"
      "ds_write_b64 v241, v[178:179]\n\t"
      "s_mov_b64 exec, -1\n\t"
      "v_add3_u32 v184, v176, v177, v178\n\t"
      "v_add_u32_e32 v184, v184, v179\n\t"
      "s_waitcnt vmcnt(1)\n\t"
      "ds_write_b64 v242, v[180:181]\n\t"
      "s_mov_b64 exec, s[44:45]\n\t"
      "ds_write_b64 v243, v[182:183]\n\t"
      "s_mov_b64 exec, -1\n\t"
      "v_add3_u32 v185, v180, v181, v182\n\t"
      "v_add_u32_e32 v185, v185, v183\n\t"
      "s_nop 1\n\t"
      "v_permlane32_swap_b32_e32 v184, v185\n\t"
      "v_add_u32_e32 v184, v184, v185\n\t"
      "v_mov_b32_e32 v185, v184\n\t"
      "s_nop 1\n\t"
      "v_permlane16_swap_b32_e32 v184, v185\n\t"
      "v_add3_u32 v184, v184, v185, s8\n\t"
      "s_waitcnt vmcnt(0) lgkmcnt(0)\n\t"
      "v_cmp_ne_u32_e32 vcc, v184, v186\n\t"
      "s_cbranch_vccnz OL_%=\n\t"
      "OX_%=:\n\t"
      "s_mov_b64 vcc, s[46:47]\n\t"
      "s_waitcnt lgkmcnt(0)\n\t"
      "s_barrier\n\t"
      "s_cbranch_vccnz GY_%=\n\t"
      "ds_read_b128 v[176:179], v236\n\t"
      "ds_read_b128 v[180:183], v236 offset:64\n\t"
      "ds_read_b128 v[184:187], v236 offset:128\n\t"
      "ds_read_b128 v[240:243], v236 offset:192\n\t"
      "ds_read_b128 v[220:223], v236 offset:256\n\t"
      "s_waitcnt lgkmcnt(4)\n\t"
      "v_mfma_f32_16x16x32_bf16 v[160:163], v[176:179], v[0:3], v[160:163]\n\t"
      "s_waitcnt lgkmcnt(3)\n\t"
      "v_mfma_f32_16x16x32_bf16 v[160:163], v[180:183], v[16:19], v[160:163]\n\t"
      "v_mfma_f32_16x16x32_bf16 v[164:167], v[176:179], v[4:7], v[164:167]\n\t"
      "v_mfma_f32_16x16x32_bf16 v[164:167], v[180:183], v[20:23], v[164:167]\n\t"
      "v_mfma_f32_16x16x32_bf16 v[168:171], v[176:179], v[8:11], v[168:171]\n\t"
      "v_mfma_f32_16x16x32_bf16 v[168:171], v[180:183], v[24:27], v[168:171]\n\t"
      "v_mfma_f32_16x16x32_bf16 v[172:175], v[176:179], v[12:15], v[172:175]\n\t"
      "ds_read_b128 v[176:179], v236 offset:320\n\t"
      "v_mfma_f32_16x16x32_bf16 v[172:175], v[180:183], v[28:31], v[172:175]\n\t"
      "ds_read_b128 v[180:183], v236 offset:384\n\t"
      "s_waitcnt lgkmcnt(4)\n\t"
      "v_mfma_f32_16x16x32_bf16 v[160:163], v[184:187], v[32:35], v[160:163]\n\t"
      "s_waitcnt lgkmcnt(3)\n\t"
      "v_mfma_f32_16x16x32_bf16 v[160:163], v[240:243], v[48:51], v[160:163]\n\t"
      "ds_read_b64_tr_b16 v[0:1], v232\n\t"
      "ds_read_b64_tr_b16 v[2:3], v232 offset:512\n\t"
      "v_mfma_f32_16x16x32_bf16 v[164:167], v[184:187], v[36:39], v[164:167]\n\t"
      "v_mfma_f32_16x16x32_bf16 v[164:167], v[240:243], v[52:55], v[164:167]\n\t"
      "v_mfma_f32_16x16x32_bf16 v[168:171], v[184:187], v[40:43], v[168:171]\n\t"
      "v_mfma_f32_16x16x32_bf16 v[168:171], v[240:243], v[56:59], v[168:171]\n\t"
      "ds_read_b64_tr_b16 v[4:5], v233\n\t"
      "ds_read_b64_tr_b16 v[6:7], v233 offset:512\n\t"
      "v_mfma_f32_16x16x32_bf16 v[172:175], v[184:187], v[44:47], v[172:175]\n\t"
      "ds_read_b128 v[184:187], v236 offset:448\n\t"
      "v_mfma_f32_16x16x32_bf16 v[172:175], v[240:243], v[60:63], v[172:175]\n\t"
      "ds_read_b128 v[240:243], v236 offset:512\n\t"
      "s_waitcnt lgkmcnt(8)\n\t"
      "v_mfma_f32_16x16x32_bf16 v[160:163], v[220:223], v[64:67], v[160:163]\n\t"
      "s_waitcnt lgkmcnt(7)\n\t"
      "v_mfma_f32_16x16x32_bf16 v[160:163], v[176:179], v[80:83], v[160:163]\n\t"
      "ds_read_b64_tr_b16 v[8:9], v234\n\t"
      "ds_read_b64_tr_b16 v[10:11], v234 offset:512\n\t"
      "v_mfma_f32_16x16x32_bf16 v[164:167], v[220:223], v[68:71], v[164:167]\n\t"
      "v_mfma_f32_16x16x32_bf16 v[164:167], v[176:179], v[84:87], v[164:167]\n\t"
      "v_mfma_f32_16x16x32_bf16 v[168:171], v[220:223], v[72:75], v[168:171]\n\t"
      "v_mfma_f32_16x16x32_bf16 v[168:171], v[176:179], v[88:91], v[168:171]\n\t"
      "ds_read_b64_tr_b16 v[12:13], v235\n\t"
      "ds_read_b64_tr_b16 v[14:15], v235 offset:512\n\t"
      "v_mfma_f32_16x16x32_bf16 v[172:175], v[220:223], v[76:79], v[172:175]\n\t"
      "ds_read_b128 v[220:223], v236 offset:576\n\t"
      "v_mfma_f32_16x16x32_bf16 v[172:175], v[176:179], v[92:95], v[172:175]\n\t"
      "ds_read_b128 v[176:179], v236 offset:640\n\t"
      "s_waitcnt lgkmcnt(12)\n\t"
      "v_mfma_f32_16x16x32_bf16 v[160:163], v[180:183], v[96:99], v[160:163]\n\t"
      "s_waitcnt lgkmcnt(7)\n\t"
      "v_mfma_f32_16x16x32_bf16 v[160:163], v[184:187], v[112:115], v[160:163]\n\t"
      "ds_read_b64_tr_b16 v[16:17], v232 offset:28672\n\t"
      "ds_read_b64_tr_b16 v[18:19], v232 offset:29184\n\t"
      "v_mfma_f32_16x16x32_bf16 v[164:167], v[180:183], v[100:103], v[164:167]\n\t"
      "v_mfma_f32_16x16x32_bf16 v[164:167], v[184:187], v[116:119], v[164:167]\n\t"
      "v_mfma_f32_16x16x32_bf16 v[168:171], v[180:183], v[104:107], v[168:171]\n\t"
      "v_mfma_f32_16x16x32_bf16 v[168:171], v[184:187], v[120:123], v[168:171]\n\t"
      "ds_read_b64_tr_b16 v[20:21], v233 offset:28672\n\t"
      "ds_read_b64_tr_b16 v[22:23], v233 offset:29184\n\t"
      "v_mfma_f32_16x16x32_bf16 v[172:175], v[180:183], v[108:111], v[172:175]\n\t"
      "ds_read_b128 v[180:183], v236 offset:704\n\t"
      "v_mfma_f32_16x16x32_bf16 v[172:175], v[184:187], v[124:127], v[172:175]\n\t"
      "ds_read_b128 v[184:187], v236 offset:768\n\t"
      "s_waitcnt lgkmcnt(12)\n\t"
      "v_mfma_f32_16x16x32_bf16 v[160:163], v[240:243], v[128:131], v[160:163]\n\t"
      "s_waitcnt lgkmcnt(7)\n\t"
      "v_mfma_f32_16x16x32_bf16 v[160:163], v[220:223], v[144:147], v[160:163]\n\t"
      "ds_read_b64_tr_b16 v[24:25], v234 offset:28672\n\t"
      "ds_read_b64_tr_b16 v[26:27], v234 offset:29184\n\t"
      "v_mfma_f32_16x16x32_bf16 v[164:167], v[240:243], v[132:135], v[164:167]\n\t"
      "v_mfma_f32_16x16x32_bf16 v[164:167], v[220:223], v[148:151], v[164:167]\n\t"
      "v_mfma_f32_16x16x32_bf16 v[168:171], v[240:243], v[136:139], v[168:171]\n\t"
      "v_mfma_f32_16x16x32_bf16 v[168:171], v[220:223], v[152:155], v[168:171]\n\t"
      "ds_read_b64_tr_b16 v[28:29], v235 offset:28672\n\t"
      "ds_read_b64_tr_b16 v[30:31], v235 offset:29184\n\t"
      "v_mfma_f32_16x16x32_bf16 v[172:175], v[240:243], v[140:143], v[172:175]\n\t"
      "ds_read_b128 v[240:243], v236 offset:832\n\t"
      "v_mfma_f32_16x16x32_bf16 v[172:175], v[220:223], v[156:159], v[172:175]\n\t"
      "s_waitcnt lgkmcnt(11)\n\t"
      "v_mfma_f32_16x16x32_bf16 v[160:163], v[176:179], v[188:191], v[160:163]\n\t"
      "s_waitcnt lgkmcnt(6)\n\t"
      "v_mfma_f32_16x16x32_bf16 v[160:163], v[180:183], v[204:207], v[160:163]\n\t"
      "v_mfma_f32_16x16x32_bf16 v[164:167], v[176:179], v[192:195], v[164:167]\n\t"
      "v_mfma_f32_16x16x32_bf16 v[164:167], v[180:183], v[208:211], v[164:167]\n\t"
      "v_mfma_f32_16x16x32_bf16 v[168:171], v[176:179], v[196:199], v[168:171]\n\t"
      "v_mfma_f32_16x16x32_bf16 v[168:171], v[180:183], v[212:215], v[168:171]\n\t"
      "v_mfma_f32_16x16x32_bf16 v[172:175], v[176:179], v[200:203], v[172:175]\n\t"
      "v_mfma_f32_16x16x32_bf16 v[172:175], v[180:183], v[216:219], v[172:175]\n\t"
      "s_waitcnt lgkmcnt(5)\n\t"
      "v_mfma_f32_16x16x32_bf16 v[160:163], v[184:187], v[0:3], v[160:163]\n\t"
      "s_waitcnt lgkmcnt(0)\n\t"
      "v_mfma_f32_16x16x32_bf16 v[160:163], v[240:243], v[16:19], v[160:163]\n\t"
      "v_mfma_f32_16x16x32_bf16 v[164:167], v[184:187], v[4:7], v[164:167]\n\t"
      "s_bitcmp1_b32 s16, 4\n\t"
      "s_cbranch_scc0 HI_%=\n\t"
      "s_mov_b64 exec, 255\n\t"
      "global_store_dwordx2 v245, v[252:253], s[6:7] sc1\n\t"
      "s_mov_b64 exec, -1\n\t"
      "HI_%=:\n\t"
      "v_mfma_f32_16x16x32_bf16 v[164:167], v[240:243], v[20:23], v[164:167]\n\t"
      "v_mfma_f32_16x16x32_bf16 v[168:171], v[184:187], v[8:11], v[168:171]\n\t"
      "v_mfma_f32_16x16x32_bf16 v[168:171], v[240:243], v[24:27], v[168:171]\n\t"
      "v_mfma_f32_16x16x32_bf16 v[172:175], v[184:187], v[12:15], v[172:175]\n\t"
      "v_mfma_f32_16x16x32_bf16 v[172:175], v[240:243], v[28:31], v[172:175]\n\t"
      "v_mul_f32_e32 v192, v160, v160\n\t"
      "v_mul_f32_e32 v193, v161, v161\n\t"
      "v_mul_f32_e32 v194, v162, v162\n\t"
      "v_mul_f32_e32 v195, v163, v163\n\t"
      "v_add_f32_e32 v188, v160, v164\n\t"
      "v_add_f32_e32 v189, v161, v165\n\t"
      "v_add_f32_e32 v190, v162, v166\n\t"
      "v_add_f32_e32 v191, v163, v167\n\t"
      "v_fmac_f32_e32 v192, v164, v164\n\t"
      "v_fmac_f32_e32 v193, v165, v165\n\t"
      "v_fmac_f32_e32 v194, v166, v166\n\t"
      "v_fmac_f32_e32 v195, v167, v167\n\t"
      "v_add_f32_e32 v188, v188, v168\n\t"
      "v_add_f32_e32 v189, v189, v169\n\t"
      "v_add_f32_e32 v190, v190, v170\n\t"
      "v_add_f32_e32 v191, v191, v171\n\t"
      "v_fmac_f32_e32 v192, v168, v168\n\t"
      "v_fmac_f32_e32 v193, v169, v169\n\t"
      "v_fmac_f32_e32 v194, v170, v170\n\t"
      "v_fmac_f32_e32 v195, v171, v171\n\t"
      "v_add_f32_e32 v188, v188, v172\n\t"
      "v_add_f32_e32 v189, v189, v173\n\t"
      "v_add_f32_e32 v190, v190, v174\n\t"
      "v_add_f32_e32 v191, v191, v175\n\t"
      "v_fmac_f32_e32 v192, v172, v172\n\t"
      "v_fmac_f32_e32 v193, v173, v173\n\t"
      "v_fmac_f32_e32 v194, v174, v174\n\t"
      "v_fmac_f32_e32 v195, v175, v175\n\t"
      "s_nop 1\n\t"
      "s_branch GJ_%=\n\t"
      "GY_%=:\n\t"
      "v_add_u32_e32 v240, 262144, v231\n\t"
      "global_store_dwordx4 v231, v[248:251], s[6:7] sc1\n\t"
      "global_store_dwordx4 v240, v[248:251], s[6:7] sc1\n\t"
      "s_mov_b64 exec, s[42:43]\n\t"
      "global_store_dwordx4 v238, v[248:251], s[6:7] sc1\n\t"
      "s_mov_b64 exec, -1\n\t"
      "ds_read_b128 v[176:179], v236\n\t"
      "ds_read_b128 v[180:183], v236 offset:64\n\t"
      "ds_read_b128 v[184:187], v236 offset:128\n\t"
      "ds_read_b128 v[12:15], v236 offset:192\n\t"
      "ds_read_b128 v[28:31], v236 offset:256\n\t"
      "ds_read_b128 v[44:47], v236 offset:320\n\t"
      "ds_read_b128 v[60:63], v236 offset:384\n\t"
      "ds_read_b128 v[76:79], v236 offset:448\n\t"
      "ds_read_b128 v[92:95], v236 offset:512\n\t"
      "ds_read_b128 v[108:111], v236 offset:576\n\t"
      "ds_read_b128 v[124:127], v236 offset:640\n\t"
      "ds_read_b128 v[140:143], v236 offset:704\n\t"
      "ds_read_b128 v[156:159], v236 offset:768\n\t"
      "ds_read_b128 v[200:203], v236 offset:832\n\t"
      "s_bitcmp1_b32 s16, 7\n\t"
      "s_cbranch_scc0 SY2_%=\n\t"
      "global_store_dwordx4 v237, v[248:251], s[6:7] offset:-4096 sc1\n\t"
      "global_store_dwordx4 v239, v[248:251], s[6:7] offset:-4096 sc1\n\t"
      "SY2_%=:\n\t"
      "s_bitcmp1_b32 s16, 6\n\t"
      "s_cbranch_scc0 SG2_%=\n\t"
      "global_store_dwordx4 v237, v[248:251], s[6:7] sc1\n\t"
      "global_store_dwordx4 v239, v[248:251], s[6:7] sc1\n\t"
      "SG2_%=:\n\t"
      "s_waitcnt lgkmcnt(13)\n\t"
      "v_mfma_f32_16x16x32_bf16 v[160:163], v[176:179], v[0:3], v[160:163]\n\t"
      "s_waitcnt lgkmcnt(12)\n\t"
      "v_mfma_f32_16x16x32_bf16 v[160:163], v[180:183], v[16:19], v[160:163]\n\t"
      "s_waitcnt lgkmcnt(11)\n\t"
      "v_mfma_f32_16x16x32_bf16 v[160:163], v[184:187], v[32:35], v[160:163]\n\t"
      "s_waitcnt lgkmcnt(10)\n\t"
      "v_mfma_f32_16x16x32_bf16 v[160:163], v[12:15], v[48:51], v[160:163]\n\t"
      "ds_read_b64_tr_b16 v[0:1], v232\n\t"
      "ds_read_b64_tr_b16 v[2:3], v232 offset:512\n\t"
      "s_waitcnt lgkmcnt(11)\n\t"
      "v_mfma_f32_16x16x32_bf16 v[160:163], v[28:31], v[64:67], v[160:163]\n\t"
      "ds_read_b64_tr_b16 v[16:17], v232 offset:28672\n\t"
      "ds_read_b64_tr_b16 v[18:19], v232 offset:29184\n\t"
      "s_waitcnt lgkmcnt(12)\n\t"
      "v_mfma_f32_16x16x32_bf16 v[160:163], v[44:47], v[80:83], v[160:163]\n\t"
      "s_waitcnt lgkmcnt(11)\n\t"
      "v_mfma_f32_16x16x32_bf16 v[160:163], v[60:63], v[96:99], v[160:163]\n\t"
      "s_waitcnt lgkmcnt(10)\n\t"
      "v_mfma_f32_16x16x32_bf16 v[160:163], v[76:79], v[112:115], v[160:163]\n\t"
      "s_waitcnt lgkmcnt(9)\n\t"
      "v_mfma_f32_16x16x32_bf16 v[160:163], v[92:95], v[128:131], v[160:163]\n\t"
      "s_waitcnt lgkmcnt(8)\n\t"
      "v_mfma_f32_16x16x32_bf16 v[160:163], v[108:111], v[144:147], v[160:163]\n\t"
      "s_waitcnt lgkmcnt(7)\n\t"
      "v_mfma_f32_16x16x32_bf16 v[160:163], v[124:127], v[188:191], v[160:163]\n\t"
      "s_waitcnt lgkmcnt(6)\n\t"
      "v_mfma_f32_16x16x32_bf16 v[160:163], v[140:143], v[204:207], v[160:163]\n\t"
      "s_waitcnt lgkmcnt(2)\n\t"
      "v_mfma_f32_16x16x32_bf16 v[160:163], v[156:159], v[0:3], v[160:163]\n\t"
      "s_waitcnt lgkmcnt(0)\n\t"
      "v_mfma_f32_16x16x32_bf16 v[160:163], v[200:203], v[16:19], v[160:163]\n\t"
      "v_mfma_f32_16x16x32_bf16 v[164:167], v[176:179], v[4:7], v[164:167]\n\t"
      "v_mfma_f32_16x16x32_bf16 v[164:167], v[180:183], v[20:23], v[164:167]\n\t"
      "v_mfma_f32_16x16x32_bf16 v[164:167], v[184:187], v[36:39], v[164:167]\n\t"
      "v_mfma_f32_16x16x32_bf16 v[164:167], v[12:15], v[52:55], v[164:167]\n\t"
      "ds_read_b64_tr_b16 v[4:5], v233\n\t"
      "ds_read_b64_tr_b16 v[6:7], v233 offset:512\n\t"
      "v_mfma_f32_16x16x32_bf16 v[164:167], v[28:31], v[68:71], v[164:167]\n\t"
      "ds_read_b64_tr_b16 v[20:21], v233 offset:28672\n\t"
      "ds_read_b64_tr_b16 v[22:23], v233 offset:29184\n\t"
      "v_mfma_f32_16x16x32_bf16 v[164:167], v[44:47], v[84:87], v[164:167]\n\t"
      "v_mfma_f32_16x16x32_bf16 v[164:167], v[60:63], v[100:103], v[164:167]\n\t"
      "v_mul_f32_e32 v80, v160, v160\n\t"
      "v_mul_f32_e32 v81, v161, v161\n\t"
      "v_mfma_f32_16x16x32_bf16 v[164:167], v[76:79], v[116:119], v[164:167]\n\t"
      "v_mul_f32_e32 v82, v162, v162\n\t"
      "v_mul_f32_e32 v83, v163, v163\n\t"
      "v_mfma_f32_16x16x32_bf16 v[164:167], v[92:95], v[132:135], v[164:167]\n\t"
      "v_mfma_f32_16x16x32_bf16 v[164:167], v[108:111], v[148:151], v[164:167]\n\t"
      "v_mfma_f32_16x16x32_bf16 v[164:167], v[124:127], v[192:195], v[164:167]\n\t"
      "v_mfma_f32_16x16x32_bf16 v[164:167], v[140:143], v[208:211], v[164:167]\n\t"
      "s_waitcnt lgkmcnt(2)\n\t"
      "v_mfma_f32_16x16x32_bf16 v[164:167], v[156:159], v[4:7], v[164:167]\n\t"
      "s_waitcnt lgkmcnt(0)\n\t"
      "v_mfma_f32_16x16x32_bf16 v[164:167], v[200:203], v[20:23], v[164:167]\n\t"
      "v_mfma_f32_16x16x32_bf16 v[168:171], v[176:179], v[8:11], v[168:171]\n\t"
      "v_mfma_f32_16x16x32_bf16 v[168:171], v[180:183], v[24:27], v[168:171]\n\t"
      "v_mfma_f32_16x16x32_bf16 v[168:171], v[184:187], v[40:43], v[168:171]\n\t"
      "v_mfma_f32_16x16x32_bf16 v[168:171], v[12:15], v[56:59], v[168:171]\n\t"
      "ds_read_b64_tr_b16 v[8:9], v234\n\t"
      "ds_read_b64_tr_b16 v[10:11], v234 offset:512\n\t"
      "v_mfma_f32_16x16x32_bf16 v[168:171], v[28:31], v[72:75], v[168:171]\n\t"
      "ds_read_b64_tr_b16 v[24:25], v234 offset:28672\n\t"
      "ds_read_b64_tr_b16 v[26:27], v234 offset:29184\n\t"
      "v_mfma_f32_16x16x32_bf16 v[168:171], v[44:47], v[88:91], v[168:171]\n\t"
      "v_mfma_f32_16x16x32_bf16 v[168:171], v[60:63], v[104:107], v[168:171]\n\t"
      "v_add_f32_e32 v64, v160, v164\n\t"
      "v_add_f32_e32 v65, v161, v165\n\t"
      "v_mfma_f32_16x16x32_bf16 v[168:171], v[76:79], v[120:123], v[168:171]\n\t"
      "v_add_f32_e32 v66, v162, v166\n\t"
      "v_add_f32_e32 v67, v163, v167\n\t"
      "v_mfma_f32_16x16x32_bf16 v[168:171], v[92:95], v[136:139], v[168:171]\n\t"
      "v_fmac_f32_e32 v80, v164, v164\n\t"
      "v_fmac_f32_e32 v81, v165, v165\n\t"
      "v_mfma_f32_16x16x32_bf16 v[168:171], v[108:111], v[152:155], v[168:171]\n\t"
      "v_fmac_f32_e64 v82, v166, v166\n\t"
      "v_fmac_f32_e32 v83, v167, v167\n\t"
      "v_mfma_f32_16x16x32_bf16 v[168:171], v[124:127], v[196:199], v[168:171]\n\t"
      "v_mfma_f32_16x16x32_bf16 v[168:171], v[140:143], v[212:215], v[168:171]\n\t"
      "s_waitcnt lgkmcnt(2)\n\t"
      "v_mfma_f32_16x16x32_bf16 v[168:171], v[156:159], v[8:11], v[168:171]\n\t"
      "s_waitcnt lgkmcnt(0)\n\t"
      "v_mfma_f32_16x16x32_bf16 v[168:171], v[200:203], v[24:27], v[168:171]\n\t"
      "s_nop 7\n\t"
      "v_pk_add_f32 v[188:189], v[64:65], v[168:169]\n\t"
      "v_pk_fma_f32 v[192:193], v[168:169], v[168:169], v[80:81]\n\t"
      "v_pk_add_f32 v[190:191], v[66:67], v[170:171]\n\t"
      "v_pk_fma_f32 v[194:195], v[170:171], v[170:171], v[82:83]\n\t"
      "GJ_%=:\n\t"
      "v_add_f32_dpp v196, v188, v188 row_ror:8 row_mask:0xf bank_mask:0x3\n\t"
      "v_add_f32_dpp v197, v189, v189 row_ror:8 row_mask:0xf bank_mask:0x3\n\t"
      "v_add_f32_dpp v198, v192, v192 row_ror:8 row_mask:0xf bank_mask:0x3\n\t"
      "v_add_f32_dpp v199, v193, v193 row_ror:8 row_mask:0xf bank_mask:0x3\n\t"
      "v_add_f32_dpp v196, v190, v190 row_ror:8 row_mask:0xf bank_mask:0xc\n\t"
      "v_add_f32_dpp v197, v191, v191 row_ror:8 row_mask:0xf bank_mask:0xc\n\t"
      "v_add_f32_dpp v198, v194, v194 row_ror:8 row_mask:0xf bank_mask:0xc\n\t"
      "v_add_f32_dpp v199, v195, v195 row_ror:8 row_mask:0xf bank_mask:0xc\n\t"
      "v_add_f32_dpp v188, v196, v196 row_shl:4 row_mask:0xf bank_mask:0x5\n\t"
      "v_add_f32_dpp v189, v198, v198 row_shl:4 row_mask:0xf bank_mask:0x5\n\t"
      "s_nop 0\n\t"
      "v_add_f32_dpp v188, v197, v197 row_shr:4 row_mask:0xf bank_mask:0xa\n\t"
      "v_add_f32_dpp v189, v199, v199 row_shr:4 row_mask:0xf bank_mask:0xa\n\t"
      "s_nop 0\n\t"
      "v_add_f32_dpp v188, v188, v188 quad_perm:[1,0,3,2] row_mask:0xf bank_mask:0xf\n\t"
      "v_add_f32_dpp v189, v189, v189 quad_perm:[1,0,3,2] row_mask:0xf bank_mask:0xf\n\t"
      "s_nop 0\n\t"
      "v_add_f32_dpp v188, v188, v188 quad_perm:[2,3,0,1] row_mask:0xf bank_mask:0xf\n\t"
      "v_add_f32_dpp v189, v189, v189 quad_perm:[2,3,0,1] row_mask:0xf bank_mask:0xf\n\t"
      "ds_write_b64 v226, v[188:189]\n\t"
      "s_cbranch_vccz DR_%=\n\t"
      "s_waitcnt vmcnt(0)\n\t"
      "DR_%=:\n\t"
      "s_waitcnt lgkmcnt(0)\n\t"
      "s_barrier\n\t"
      "ds_read_b128 v[188:191], v227\n\t"
      "s_waitcnt lgkmcnt(0)\n\t"
      "v_pk_add_f32 v[188:189], v[188:189], v[190:191]\n\t"
      "s_nop 1\n\t"
      "v_add_f32_dpp v188, v188, v188 row_ror:4 row_mask:0xf bank_mask:0xf\n\t"
      "v_add_f32_dpp v189, v189, v189 row_ror:4 row_mask:0xf bank_mask:0xf\n\t"
      "s_nop 0\n\t"
      "v_add_f32_dpp v188, v188, v188 row_ror:8 row_mask:0xf bank_mask:0xf\n\t"
      "v_add_f32_dpp v189, v189, v189 row_ror:8 row_mask:0xf bank_mask:0xf\n\t"
      "v_mul_f32_e32 v188, 0x3b124925, v188\n\t"
      "v_mul_f32_e32 v189, 0x3b124925, v189\n\t"
      "v_fma_f32 v189, -v188, v188, v189\n\t"
      "v_max_f32_e64 v189, 0, v189\n\t"
      "v_add_f32_e32 v190, 0x3727c5ac, v189\n\t"
      "v_fmac_f32_e32 v189, 0x3727c5ac, v190\n\t"
      "v_rsq_f32_e64 v191, v189\n\t"
      "s_nop 0\n\t"
      "v_mul_f32_e64 v190, -v188, v191\n\t"
      "v_mov_b32_dpp v192, v191 quad_perm:[0,0,0,0] row_mask:0xf bank_mask:0xf\n\t"
      "s_nop 0\n\t"
      "v_mov_b32_dpp v196, v190 quad_perm:[0,0,0,0] row_mask:0xf bank_mask:0xf\n\t"
      "v_mov_b32_dpp v193, v191 quad_perm:[1,1,1,1] row_mask:0xf bank_mask:0xf\n\t"
      "v_mov_b32_dpp v197, v190 quad_perm:[1,1,1,1] row_mask:0xf bank_mask:0xf\n\t"
      "v_mov_b32_dpp v194, v191 quad_perm:[2,2,2,2] row_mask:0xf bank_mask:0xf\n\t"
      "v_mov_b32_dpp v198, v190 quad_perm:[2,2,2,2] row_mask:0xf bank_mask:0xf\n\t"
      "v_mov_b32_dpp v195, v191 quad_perm:[3,3,3,3] row_mask:0xf bank_mask:0xf\n\t"
      "v_mov_b32_dpp v199, v190 quad_perm:[3,3,3,3] row_mask:0xf bank_mask:0xf\n\t"
      "v_pk_fma_f32 v[160:161], v[160:161], v[192:193], v[196:197]\n\t"
      "v_pk_fma_f32 v[162:163], v[162:163], v[194:195], v[198:199]\n\t"
      "v_pk_fma_f32 v[164:165], v[164:165], v[192:193], v[196:197]\n\t"
      "v_pk_fma_f32 v[166:167], v[166:167], v[194:195], v[198:199]\n\t"
      "v_cvt_pk_bf16_f32 v212, v160, v161\n\t"
      "v_cvt_pk_bf16_f32 v213, v162, v163\n\t"
      "v_cvt_pk_bf16_f32 v214, v164, v165\n\t"
      "v_cvt_pk_bf16_f32 v215, v166, v167\n\t"
      "v_min_u32_e32 v212, -2, v212\n\t"
      "v_min_u32_e32 v213, -2, v213\n\t"
      "v_min_u32_e64 v214, -2, v214\n\t"
      "v_min_u32_e32 v215, -2, v215\n\t"
      "s_nop 0\n\t"
      "v_permlane16_swap_b32_e32 v212, v214\n\t"
      "v_permlane16_swap_b32_e32 v213, v215\n\t"
      "global_store_dwordx4 v231, v[212:215], s[6:7] sc1\n\t"
      "v_pk_fma_f32 v[168:169], v[168:169], v[192:193], v[196:197]\n\t"
      "v_pk_fma_f32 v[170:171], v[170:171], v[194:195], v[198:199]\n\t"
      "v_pk_fma_f32 v[172:173], v[172:173], v[192:193], v[196:197]\n\t"
      "v_pk_fma_f32 v[174:175], v[174:175], v[194:195], v[198:199]\n\t"
      "v_cvt_pk_bf16_f32 v216, v168, v169\n\t"
      "v_cvt_pk_bf16_f32 v217, v170, v171\n\t"
      "v_cvt_pk_bf16_f32 v218, v172, v173\n\t"
      "v_cvt_pk_bf16_f32 v219, v174, v175\n\t"
      "v_min_u32_e32 v216, -2, v216\n\t"
      "v_min_u32_e32 v217, -2, v217\n\t"
      "v_min_u32_e32 v218, -2, v218\n\t"
      "v_min_u32_e32 v219, -2, v219\n\t"
      "s_nop 0\n\t"
      "v_permlane16_swap_b32_e32 v216, v218\n\t"
      "v_permlane16_swap_b32_e32 v217, v219\n\t"
      "s_mov_b64 exec, s[42:43]\n\t"
      "global_store_dwordx4 v238, v[216:219], s[6:7] sc1\n\t"
      "s_mov_b64 exec, -1\n\t"
      "s_nop 1\n\t"
      "v_mov_b32_e32 v240, v188\n\t"
      "v_mul_f32_e32 v241, v189, v191\n\t"
      "v_add_u32_e32 v246, 65536, v228\n\t"
      "v_xor_b32_e32 v242, s8, v188\n\t"
      "v_xor_b32_e32 v243, s9, v241\n\t"
      "s_nop 1\n\t"
      "s_branch BFX_%=\n\t"
      "PWR_%=:\n\t"
      "s_mov_b32 s51, 0\n\t"
      "PWL_%=:\n\t"
      "s_add_u32 s51, s51, 1\n\t"
      "s_cmp_lt_u32 s51, 262144\n\t"
      "s_cbranch_scc0 PWX_%=\n\t"
      "s_sleep 1\n\t"
      "v_min_u32_e32 v184, 32, v230\n\t"
      "v_add_u32_e32 v184, s28, v184\n\t"
      "global_load_dwordx4 v[180:183], v184, s[6:7] sc1\n\t"
      "s_waitcnt vmcnt(0)\n\t"
      "v_xor_b32_e32 v182, v182, v180\n\t"
      "v_xor_b32_e32 v183, v183, v181\n\t"
      "v_cmp_ne_u64_e32 vcc, s[8:9], v[182:183]\n\t"
      "s_cbranch_vccnz PWL_%=\n\t"
      "PWX_%=:\n\t"
      "v_and_b32_e32 v180, 0x2000, v180\n\t"
      "s_nop 1\n\t"
      "s_branch PWB_%=\n\t"
      "BFX_%=:\n\t"
      : "+{s[4:11]}"(s1), "+{s[12:15]}"(s3), "+{s16}"(bits), "+{s[20:23]}"(s), "+{s[28:29]}"(par), "+{v[160:175]}"(acc), "+{v[224:231]}"(v1), "+{v[232:239]}"(v), "+{v[240:243]}"(vo), "+{v[244:247]}"(v3)
      :
      : "memory", "vcc", "scc",
        "v0", "v1", "v2", "v3", "v4", "v5", "v6", "v7", "v8", "v9", "v10", "v11", "v12", "v13", "v14", "v15", "v16",
        "v17", "v18", "v19", "v20", "v21", "v22", "v23", "v24", "v25", "v26", "v27", "v28", "v29", "v30", "v31", "v32",
        "v33", "v34", "v35", "v36", "v37", "v38", "v39", "v40", "v41", "v42", "v43", "v44", "v45", "v46", "v47", "v48",
        "v49", "v50", "v51", "v52", "v53", "v54", "v55", "v56", "v57", "v58", "v59", "v60", "v61", "v62", "v63", "v64",
        "v65", "v66", "v67", "v68", "v69", "v70", "v71", "v72", "v73", "v74", "v75", "v76", "v77", "v78", "v79", "v80",
        "v81", "v82", "v83", "v84", "v85", "v86", "v87", "v88", "v89", "v90", "v91", "v92", "v93", "v94", "v95", "v96",
        "v97", "v98", "v99", "v100", "v101", "v102", "v103", "v104", "v105", "v106", "v107", "v108", "v109", "v110",
        "v111", "v112", "v113", "v114", "v115", "v116", "v117", "v118", "v119", "v120", "v121", "v122", "v123", "v124",
        "v125", "v126", "v127", "v128", "v129", "v130", "v131", "v132", "v133", "v134", "v135", "v136", "v137", "v138",
        "v139", "v140", "v141", "v142", "v143", "v144", "v145", "v146", "v147", "v148", "v149", "v150", "v151", "v152",
        "v153", "v154", "v155", "v156", "v157", "v158", "v159", "v176", "v177", "v178", "v179", "v180", "v181", "v182",
        "v183", "v184", "v185", "v186", "v187", "v188", "v189", "v190", "v191", "v192", "v193", "v194", "v195", "v196",
        "v197", "v198", "v199", "v200", "v201", "v202", "v203", "v204", "v205", "v206", "v207", "v208", "v209", "v210",
        "v211", "v212", "v213", "v214", "v215", "v216", "v217", "v218", "v219", "v220", "v221", "v222", "v223", "v248",
        "v249", "v250", "v251", "v252", "v253", "s40", "s41", "s42", "s43", "s44", "s45", "s46", "s47", "s48", "s49",
        "s50", "s51"
    );
    }
    unsigned vq = v3[2];
    PIN();
    LANE_BITS(__builtin_amdgcn_mbcnt_hi(~0u, __builtin_amdgcn_mbcnt_lo(~0u, 0u)));
    u32x8 v, s2;
    u32x16 s;
    v[0] = lane * 16;
    u64 kr = (u64)(6 - rn) | ((u64)rot << 32);
    v[1] = lr * 4096 + ((lc ^ swz_tr(lr)) << 4);
    v[2] = wave * 4096 + g * 1024 + q4 * 128 + (((2 * (wave & 3) + (p4 >> 1)) ^ swz_tr(8 * g + q4)) << 4) + (p4 & 1) * 8;
    v[3] = wave * 4096 + (wave & 3) * 1024 + ((lane * 16) ^ (g << 6));
    v[4] = (wave & 3) * 1024 + (((p4 * 16 + (lane >> 2)) * 16) ^ (p4 << 6));
    v[5] = (lane >> 2) * 896 + p4 * 8;
    v[6] = c * 2;
    const unsigned mh = (unsigned)par, mg = (unsigned)(par >> 32);
    v[7] = ST_H4 + xp * 16384 + ((side * 8192) ^ mg) + ((ib & 1) * 28 + ((7 * wave) >> 1) + p4) * 128;
    put(s, 0, (u64)(w2 + st * 128 + (wave * 224 + rn * 32) * 4096));
    put(s, 2, (u64)(ws + WS_G + (unit(blk0, wave, 0) ^ mg)));
    put(s, 4, (u64)(out + (64 * st + 16 * (wave & 3)) * 896 + dg * 128));
    unsigned od = (16 - dg) * 32;
    put(s, 6, (u64)(b2 + (64 * st + 16 * (wave & 3)) * 2));
    put(s, 8, (u64)(ws + WS_ST));
    s[12] = bits;
    s[13] = wave * G_WV;
    put(s2, 0, (u64)(ws + WS_HN + (unit(blk0, st >> 2, 0) ^ mh) + (2 * (st & 3) + ((wave >> 1) & 1)) * 1024));
    s2[2] = ST_FL + (64 * st + 16 * (wave & 3)) * 16;
    s2[3] = UNI(wave & 1 ? ~0u : 0u);
    put(s2, 4, (u64)(ws + WS_EP));
    asm volatile(
      "s_waitcnt lgkmcnt(0)\n\ts_nop 4\n\t"
      ".p2align 3\n\t"
      "s_lshr_b32 s31, s17, 2\n\t"
      "v_mov_b32_e32 v112, 163600\n\t"
      "ds_read_b128 v[112:115], v112\n\t"
      "v_xor_b32_e32 v248, 64, v241\n\t"
      "v_add_u32_e32 v249, 63488, v241\n\t"
      "v_add_u32_e32 v250, 95232, v248\n\t"
      "v_add_u32_e32 v248, 31744, v248\n\t"
      "v_xor_b32_e64 v251, 32, v242\n\t"
      "v_xor_b32_e32 v252, 64, v242\n\t"
      "v_xor_b32_e32 v253, 96, v242\n\t"
      "s_bitcmp1_b32 s16, 4\n\t"
      "s_cbranch_scc0 GW2_%=\n\t"
      "s_mov_b32 s48, 0xf000f\n\t"
      "s_mov_b32 s49, 0xf000f\n\t"
      "s_mov_b64 exec, s[48:49]\n\t"
      "global_store_dwordx4 v140, v[136:139], s[12:13] sc1\n\t"
      "s_mov_b64 exec, -1\n\t"
      "GW2_%=:\n\t"
      "s_add_u32 m0, s31, 66048\n\t"
      "s_nop 0\n\t"
      "global_load_lds_dwordx4 v241, s[4:5]\n\t"
      "global_load_lds_dwordx4 v248, s[4:5] offset:1024\n\t"
      "global_load_lds_dwordx4 v249, s[4:5] offset:2048\n\t"
      "global_load_lds_dwordx4 v250, s[4:5] offset:3072\n\t"
      "s_sub_u32 s28, s28, 1\n\t"
      "s_cselect_b32 s48, -6, 1\n\t"
      "s_cselect_b32 s49, -1, 0\n\t"
      "s_mul_i32 s48, s48, 131072\n\t"
      "s_add_u32 s4, s4, s48\n\t"
      "s_addc_u32 s5, s5, s49\n\t"
      "global_load_ushort v236, v246, s[10:11]\n\t"
      "s_waitcnt lgkmcnt(0)\n\t"
      "v_readfirstlane_b32 s14, v112\n\t"
      "v_readfirstlane_b32 s15, v113\n\t"
      "v_readfirstlane_b32 s26, v114\n\t"
      "v_readfirstlane_b32 s27, v115\n\t"
      "s_add_u32 s52, s6, 0\n\t"
      "s_addc_u32 s53, s7, 0\n\t"
      "s_add_u32 s54, s6, 131072\n\t"
      "s_addc_u32 s55, s7, 0\n\t"
      "s_add_u32 s56, s6, 262144\n\t"
      "s_addc_u32 s57, s7, 0\n\t"
      "s_add_u32 s58, s6, 393216\n\t"
      "s_addc_u32 s59, s7, 0\n\t"
      "s_add_u32 s60, s6, 524288\n\t"
      "s_addc_u32 s61, s7, 0\n\t"
      "s_add_u32 s62, s6, 655360\n\t"
      "s_addc_u32 s63, s7, 0\n\t"
      "s_add_u32 s64, s6, 786432\n\t"
      "s_addc_u32 s65, s7, 0\n\t"
      "s_add_u32 s48, s29, 0\n\t"
      "s_cmp_ge_u32 s48, 7\n\t"
      "s_cselect_b32 s49, 7, 0\n\t"
      "s_sub_u32 s48, s48, s49\n\t"
      "v_lshl_add_u32 v224, s48, 10, v240\n\t"
      "s_add_u32 s48, s29, 1\n\t"
      "s_cmp_ge_u32 s48, 7\n\t"
      "s_cselect_b32 s49, 7, 0\n\t"
      "s_sub_u32 s48, s48, s49\n\t"
      "v_lshl_add_u32 v225, s48, 10, v240\n\t"
      "s_add_u32 s48, s29, 2\n\t"
      "s_cmp_ge_u32 s48, 7\n\t"
      "s_cselect_b32 s49, 7, 0\n\t"
      "s_sub_u32 s48, s48, s49\n\t"
      "v_lshl_add_u32 v226, s48, 10, v240\n\t"
      "s_add_u32 s48, s29, 3\n\t"
      "s_cmp_ge_u32 s48, 7\n\t"
      "s_cselect_b32 s49, 7, 0\n\t"
      "s_sub_u32 s48, s48, s49\n\t"
      "v_lshl_add_u32 v227, s48, 10, v240\n\t"
      "s_add_u32 s48, s29, 4\n\t"
      "s_cmp_ge_u32 s48, 7\n\t"
      "s_cselect_b32 s49, 7, 0\n\t"
      "s_sub_u32 s48, s48, s49\n\t"
      "v_lshl_add_u32 v228, s48, 10, v240\n\t"
      "s_add_u32 s48, s29, 5\n\t"
      "s_cmp_ge_u32 s48, 7\n\t"
      "s_cselect_b32 s49, 7, 0\n\t"
      "s_sub_u32 s48, s48, s49\n\t"
      "v_lshl_add_u32 v229, s48, 10, v240\n\t"
      "s_add_u32 s48, s29, 6\n\t"
      "s_cmp_ge_u32 s48, 7\n\t"
      "s_cselect_b32 s49, 7, 0\n\t"
      "s_sub_u32 s48, s48, s49\n\t"
      "v_lshl_add_u32 v230, s48, 10, v240\n\t"
      "v_xor_b32_e32 v220, 1024, v243\n\t"
      "v_xor_b32_e32 v221, 2048, v243\n\t"
      "v_xor_b32_e32 v222, 3072, v243\n\t"
      "s_add_u32 s66, s8, s30\n\t"
      "s_addc_u32 s67, s9, 0\n\t"
      "v_lshrrev_b32_e32 v112, 4, v240\n\t"
      "v_and_b32_e32 v113, 15, v112\n\t"
      "v_lshrrev_b32_e32 v112, 4, v112\n\t"
      "s_lshr_b32 s48, s17, 10\n\t"
      "s_and_b32 s48, s48, 16\n\t"
      "v_lshlrev_b32_e32 v112, 3, v112\n\t"
      "v_sub_u32_e32 v112, v113, v112\n\t"
      "v_add_u32_e32 v112, s48, v112\n\t"
      "v_mov_b32_e32 v114, 0xffff\n\t"
      "v_mov_b32_e32 v115, 0xffff0000\n\t"
      "v_cmp_eq_u32_e32 vcc, 0, v112\n\t"
      "s_nop 1\n\t"
      "v_cndmask_b32_e32 v216, 0, v114, vcc\n\t"
      "v_cmp_eq_u32_e32 vcc, 1, v112\n\t"
      "s_nop 1\n\t"
      "v_cndmask_b32_e32 v216, v216, v115, vcc\n\t"
      "v_cmp_eq_u32_e32 vcc, 2, v112\n\t"
      "s_nop 1\n\t"
      "v_cndmask_b32_e32 v217, 0, v114, vcc\n\t"
      "v_cmp_eq_u32_e32 vcc, 3, v112\n\t"
      "s_nop 1\n\t"
      "v_cndmask_b32_e32 v217, v217, v115, vcc\n\t"
      "v_cmp_eq_u32_e32 vcc, 4, v112\n\t"
      "s_nop 1\n\t"
      "v_cndmask_b32_e32 v218, 0, v114, vcc\n\t"
      "v_cmp_eq_u32_e32 vcc, 5, v112\n\t"
      "s_nop 1\n\t"
      "v_cndmask_b32_e32 v218, v218, v115, vcc\n\t"
      "v_cmp_eq_u32_e32 vcc, 6, v112\n\t"
      "s_nop 1\n\t"
      "v_cndmask_b32_e32 v219, 0, v114, vcc\n\t"
      "v_cmp_eq_u32_e32 vcc, 7, v112\n\t"
      "s_nop 1\n\t"
      "v_cndmask_b32_e32 v219, v219, v115, vcc\n\t"
      "v_and_b32_e32 v116, 0xf0, v240\n\t"
      "v_add_u32_e32 v116, s22, v116\n\t"
      "v_mov_b64_e32 v[184:185], 0\n\t"
      "v_mov_b64_e32 v[186:187], 0\n\t"
      "v_mov_b64_e32 v[188:189], 0\n\t"
      "v_mov_b64_e32 v[190:191], 0\n\t"
      "v_mov_b64_e32 v[192:193], 0\n\t"
      "v_mov_b64_e32 v[194:195], 0\n\t"
      "v_mov_b64_e32 v[196:197], 0\n\t"
      "v_mov_b64_e32 v[198:199], 0\n\t"
      "v_mov_b64_e32 v[200:201], 0\n\t"
      "v_mov_b64_e32 v[202:203], 0\n\t"
      "v_mov_b64_e32 v[204:205], 0\n\t"
      "v_mov_b64_e32 v[206:207], 0\n\t"
      "v_mov_b64_e32 v[208:209], 0\n\t"
      "v_mov_b64_e32 v[210:211], 0\n\t"
      "s_waitcnt vmcnt(5)\n\t"
      "v_add_u32_e32 v242, 66048, v242\n\t"
      "v_add_u32_e32 v251, 66048, v251\n\t"
      "v_add_u32_e32 v252, 66048, v252\n\t"
      "v_add_u32_e32 v253, 66048, v253\n\t"
      "ds_read_b64_tr_b16 v[0:1], v242 offset:64512\n\t"
      "ds_read_b64_tr_b16 v[2:3], v242 offset:65024\n\t"
      "ds_read_b64_tr_b16 v[4:5], v251 offset:64512\n\t"
      "ds_read_b64_tr_b16 v[6:7], v251 offset:65024\n\t"
      "ds_read_b64_tr_b16 v[8:9], v252 offset:64512\n\t"
      "ds_read_b64_tr_b16 v[10:11], v252 offset:65024\n\t"
      "ds_read_b64_tr_b16 v[12:13], v253 offset:64512\n\t"
      "ds_read_b64_tr_b16 v[14:15], v253 offset:65024\n\t"
      "s_waitcnt vmcnt(1)\n\t"
      "s_add_u32 m0, s31, 0\n\t"
      "s_nop 0\n\t"
      "global_load_lds_dwordx4 v241, s[4:5]\n\t"
      "global_load_lds_dwordx4 v248, s[4:5] offset:1024\n\t"
      "global_load_lds_dwordx4 v249, s[4:5] offset:2048\n\t"
      "global_load_lds_dwordx4 v250, s[4:5] offset:3072\n\t"
      "s_sub_u32 s28, s28, 1\n\t"
      "s_cselect_b32 s48, -6, 1\n\t"
      "s_cselect_b32 s49, -1, 0\n\t"
      "s_mul_i32 s48, s48, 131072\n\t"
      "s_add_u32 s4, s4, s48\n\t"
      "s_addc_u32 s5, s5, s49\n\t"
      "ds_read_b64_tr_b16 v[16:17], v242\n\t"
      "ds_read_b64_tr_b16 v[18:19], v242 offset:512\n\t"
      "ds_read_b64_tr_b16 v[20:21], v251\n\t"
      "ds_read_b64_tr_b16 v[22:23], v251 offset:512\n\t"
      "ds_read_b64_tr_b16 v[24:25], v252\n\t"
      "ds_read_b64_tr_b16 v[26:27], v252 offset:512\n\t"
      "ds_read_b64_tr_b16 v[28:29], v253\n\t"
      "ds_read_b64_tr_b16 v[30:31], v253 offset:512\n\t"
      "s_waitcnt vmcnt(0)\n\t"
      "s_add_u32 m0, s31, 32768\n\t"
      "s_nop 0\n\t"
      "global_load_lds_dwordx4 v241, s[4:5]\n\t"
      "global_load_lds_dwordx4 v248, s[4:5] offset:1024\n\t"
      "global_load_lds_dwordx4 v249, s[4:5] offset:2048\n\t"
      "global_load_lds_dwordx4 v250, s[4:5] offset:3072\n\t"
      "s_sub_u32 s28, s28, 1\n\t"
      "s_cselect_b32 s48, -6, 1\n\t"
      "s_cselect_b32 s49, -1, 0\n\t"
      "s_mul_i32 s48, s48, 131072\n\t"
      "s_add_u32 s4, s4, s48\n\t"
      "s_addc_u32 s5, s5, s49\n\t"
      "s_nop 0\n\t"
      "v_add_u32_e32 v242, 4294901248, v242\n\t"
      "v_add_u32_e32 v251, 4294901248, v251\n\t"
      "v_add_u32_e32 v252, 4294901248, v252\n\t"
      "v_add_u32_e32 v253, 4294901248, v253\n\t"
      "ds_read_b64_tr_b16 v[32:33], v242\n\t"
      "ds_read_b64_tr_b16 v[34:35], v242 offset:512\n\t"
      "ds_read_b64_tr_b16 v[36:37], v251\n\t"
      "ds_read_b64_tr_b16 v[38:39], v251 offset:512\n\t"
      "ds_read_b64_tr_b16 v[40:41], v252\n\t"
      "ds_read_b64_tr_b16 v[42:43], v252 offset:512\n\t"
      "ds_read_b64_tr_b16 v[44:45], v253\n\t"
      "ds_read_b64_tr_b16 v[46:47], v253 offset:512\n\t"
      "s_waitcnt vmcnt(0)\n\t"
      "s_waitcnt lgkmcnt(14)\n\t"
      "s_add_u32 m0, s31, 130560\n\t"
      "s_nop 0\n\t"
      "global_load_lds_dwordx4 v241, s[4:5]\n\t"
      "global_load_lds_dwordx4 v248, s[4:5] offset:1024\n\t"
      "global_load_lds_dwordx4 v249, s[4:5] offset:2048\n\t"
      "global_load_lds_dwordx4 v250, s[4:5] offset:3072\n\t"
      "s_sub_u32 s28, s28, 1\n\t"
      "s_cselect_b32 s48, -6, 1\n\t"
      "s_cselect_b32 s49, -1, 0\n\t"
      "s_mul_i32 s48, s48, 131072\n\t"
      "s_add_u32 s4, s4, s48\n\t"
      "s_addc_u32 s5, s5, s49\n\t"
      "ds_read_b64_tr_b16 v[48:49], v242 offset:32768\n\t"
      "ds_read_b64_tr_b16 v[50:51], v242 offset:33280\n\t"
      "ds_read_b64_tr_b16 v[52:53], v251 offset:32768\n\t"
      "ds_read_b64_tr_b16 v[54:55], v251 offset:33280\n\t"
      "ds_read_b64_tr_b16 v[56:57], v252 offset:32768\n\t"
      "ds_read_b64_tr_b16 v[58:59], v252 offset:33280\n\t"
      "ds_read_b64_tr_b16 v[60:61], v253 offset:32768\n\t"
      "ds_read_b64_tr_b16 v[62:63], v253 offset:33280\n\t"
      "s_bitcmp1_b32 s16, 1\n\t"
      "s_cbranch_scc0 HWQ_%=\n\t"
      "global_load_dwordx4 v[112:115], v116, s[12:13] sc1\n\t"
      "HWQ_%=:\n\t"
      "s_waitcnt vmcnt(0)\n\t"
      "s_waitcnt lgkmcnt(14)\n\t"
      "s_add_u32 m0, s31, 66048\n\t"
      "s_nop 0\n\t"
      "global_load_lds_dwordx4 v241, s[4:5]\n\t"
      "global_load_lds_dwordx4 v248, s[4:5] offset:1024\n\t"
      "global_load_lds_dwordx4 v249, s[4:5] offset:2048\n\t"
      "global_load_lds_dwordx4 v250, s[4:5] offset:3072\n\t"
      "s_sub_u32 s28, s28, 1\n\t"
      "s_cselect_b32 s48, -6, 1\n\t"
      "s_cselect_b32 s49, -1, 0\n\t"
      "s_mul_i32 s48, s48, 131072\n\t"
      "s_add_u32 s4, s4, s48\n\t"
      "s_addc_u32 s5, s5, s49\n\t"
      "v_add_u32_e32 v242, 66048, v242\n\t"
      "v_add_u32_e32 v251, 66048, v251\n\t"
      "v_add_u32_e32 v252, 66048, v252\n\t"
      "v_add_u32_e32 v253, 66048, v253\n\t"
      "ds_read_b64_tr_b16 v[64:65], v242 offset:64512\n\t"
      "ds_read_b64_tr_b16 v[66:67], v242 offset:65024\n\t"
      "ds_read_b64_tr_b16 v[68:69], v251 offset:64512\n\t"
      "ds_read_b64_tr_b16 v[70:71], v251 offset:65024\n\t"
      "ds_read_b64_tr_b16 v[72:73], v252 offset:64512\n\t"
      "ds_read_b64_tr_b16 v[74:75], v252 offset:65024\n\t"
      "ds_read_b64_tr_b16 v[76:77], v253 offset:64512\n\t"
      "ds_read_b64_tr_b16 v[78:79], v253 offset:65024\n\t"
      "s_waitcnt vmcnt(0)\n\t"
      "s_waitcnt lgkmcnt(14)\n\t"
      "s_add_u32 m0, s31, 0\n\t"
      "s_nop 0\n\t"
      "global_load_lds_dwordx4 v241, s[4:5]\n\t"
      "global_load_lds_dwordx4 v248, s[4:5] offset:1024\n\t"
      "global_load_lds_dwordx4 v249, s[4:5] offset:2048\n\t"
      "global_load_lds_dwordx4 v250, s[4:5] offset:3072\n\t"
      "ds_read_b64_tr_b16 v[80:81], v242\n\t"
      "ds_read_b64_tr_b16 v[82:83], v242 offset:512\n\t"
      "ds_read_b64_tr_b16 v[84:85], v251\n\t"
      "ds_read_b64_tr_b16 v[86:87], v251 offset:512\n\t"
      "ds_read_b64_tr_b16 v[88:89], v252\n\t"
      "ds_read_b64_tr_b16 v[90:91], v252 offset:512\n\t"
      "ds_read_b64_tr_b16 v[92:93], v253\n\t"
      "ds_read_b64_tr_b16 v[94:95], v253 offset:512\n\t"
      "s_bitcmp1_b32 s16, 1\n\t"
      "s_cbranch_scc0 HWH_%=\n\t"
      "v_xor_b32_e32 v114, v114, v112\n\t"
      "v_xor_b32_e32 v115, v115, v113\n\t"
      "v_cmp_ne_u64_e32 vcc, s[14:15], v[114:115]\n\t"
      "s_cbranch_vccnz HWR_%=\n\t"
      "HWB_%=:\n\t"
      "s_mov_b32 exec_hi, s23\n\t"
      "s_not_b32 exec_lo, s23\n\t"
      "s_add_u32 s68, s20, 0\n\t"
      "s_addc_u32 s69, s21, 0\n\t"
      "global_load_dwordx4 v[184:187], v240, s[68:69] sc1\n\t"
      "s_add_u32 s68, s20, 131072\n\t"
      "s_addc_u32 s69, s21, 0\n\t"
      "global_load_dwordx4 v[188:191], v240, s[68:69] sc1\n\t"
      "s_add_u32 s68, s20, 262144\n\t"
      "s_addc_u32 s69, s21, 0\n\t"
      "global_load_dwordx4 v[192:195], v240, s[68:69] sc1\n\t"
      "s_add_u32 s68, s20, 393216\n\t"
      "s_addc_u32 s69, s21, 0\n\t"
      "global_load_dwordx4 v[196:199], v240, s[68:69] sc1\n\t"
      "s_add_u32 s68, s20, 524288\n\t"
      "s_addc_u32 s69, s21, 0\n\t"
      "global_load_dwordx4 v[200:203], v240, s[68:69] sc1\n\t"
      "s_add_u32 s68, s20, 655360\n\t"
      "s_addc_u32 s69, s21, 0\n\t"
      "global_load_dwordx4 v[204:207], v240, s[68:69] sc1\n\t"
      "s_add_u32 s68, s20, 786432\n\t"
      "s_addc_u32 s69, s21, 0\n\t"
      "global_load_dwordx4 v[208:211], v240, s[68:69] sc1\n\t"
      "s_mov_b64 exec, -1\n\t"
      "v_lshlrev_b32_e32 v236, 16, v236\n\t"
      "v_add_f32_e32 v236, v236, v112\n\t"
      "v_cvt_pk_bf16_f32 v114, v113, v113\n\t"
      "v_lshlrev_b32_e32 v115, 16, v114\n\t"
      "v_sub_f32_e32 v115, v113, v115\n\t"
      "v_cvt_pk_bf16_f32 v115, v115, v115\n\t"
      "v_mov_b32_e32 v237, v236\n\t"
      "v_mov_b32_e32 v238, v236\n\t"
      "v_mov_b32_e32 v239, v236\n\t"
      "v_and_b32_e32 v120, v216, v115\n\t"
      "v_and_b32_e32 v216, v216, v114\n\t"
      "v_and_b32_e32 v121, v217, v115\n\t"
      "v_and_b32_e32 v217, v217, v114\n\t"
      "v_and_b32_e32 v122, v218, v115\n\t"
      "v_and_b32_e32 v218, v218, v114\n\t"
      "v_and_b32_e32 v123, v219, v115\n\t"
      "v_and_b32_e32 v219, v219, v114\n\t"
      "HWH_%=:\n\t"
      "s_waitcnt vmcnt(0)\n\t"
      "v_add_u32_e32 v242, 4294901248, v242\n\t"
      "v_add_u32_e32 v251, 4294901248, v251\n\t"
      "v_add_u32_e32 v252, 4294901248, v252\n\t"
      "v_add_u32_e32 v253, 4294901248, v253\n\t"
      "ds_read_b64_tr_b16 v[96:97], v242\n\t"
      "ds_read_b64_tr_b16 v[98:99], v242 offset:512\n\t"
      "ds_read_b64_tr_b16 v[100:101], v251\n\t"
      "ds_read_b64_tr_b16 v[102:103], v251 offset:512\n\t"
      "ds_read_b64_tr_b16 v[104:105], v252\n\t"
      "ds_read_b64_tr_b16 v[106:107], v252 offset:512\n\t"
      "ds_read_b64_tr_b16 v[108:109], v253\n\t"
      "ds_read_b64_tr_b16 v[110:111], v253 offset:512\n\t"
      "s_mov_b64 exec, 15\n\t"
      "v_mov_b64_e32 v[232:233], 0\n\t"
      "global_atomic_or_x2 v[232:233], v247, v[232:233], s[12:13] sc0 sc1\n\t"
      "s_mov_b64 exec, -1\n\t"
      "s_waitcnt lgkmcnt(0)\n\t"
      "s_barrier\n\t"
      "s_mov_b64 exec, 15\n\t"
      "s_waitcnt vmcnt(0)\n\t"
      "v_subrev_u32_e32 v232, s14, v232\n\t"
      "v_cmp_ne_u32_e64 s[48:49], s15, v233\n\t"
      "v_subrev_u32_e32 v232, 0, v232\n\t"
      "v_cmp_lt_u32_e32 vcc, 7, v232\n\t"
      "s_mov_b64 exec, -1\n\t"
      "s_or_b64 vcc, vcc, s[48:49]\n\t"
      "s_cbranch_vccz PRY_%=\n\t"
      "s_mov_b32 s51, 0\n\t"
      "s_branch PRE_%=\n\t"
      "PRL_%=:\n\t"
      "s_sleep 4\n\t"
      "s_add_u32 s51, s51, 1\n\t"
      "s_cmp_lt_u32 s51, 262144\n\t"
      "s_cbranch_scc0 PRX_%=\n\t"
      "PRE_%=:\n\t"
      "s_mov_b64 exec, 15\n\t"
      "v_mov_b64_e32 v[232:233], 0\n\t"
      "global_atomic_or_x2 v[232:233], v247, v[232:233], s[12:13] sc0 sc1\n\t"
      "s_waitcnt vmcnt(0)\n\t"
      "v_subrev_u32_e32 v232, s14, v232\n\t"
      "v_cmp_ne_u32_e64 s[48:49], s15, v233\n\t"
      "v_subrev_u32_e32 v232, 0, v232\n\t"
      "v_cmp_lt_u32_e32 vcc, 7, v232\n\t"
      "s_mov_b64 exec, -1\n\t"
      "s_or_b64 vcc, vcc, s[48:49]\n\t"
      "s_cbranch_vccnz PRL_%=\n\t"
      "PRX_%=:\n\t"
      "PRY_%=:\n\t"
      "global_load_dwordx4 v[128:131], v224, s[52:53] sc1\n\t"
      "global_load_dwordx4 v[132:135], v225, s[52:53] sc1\n\t"
      "global_load_dwordx4 v[136:139], v226, s[52:53] sc1\n\t"
      "global_load_dwordx4 v[140:143], v227, s[52:53] sc1\n\t"
      "global_load_dwordx4 v[144:147], v228, s[52:53] sc1\n\t"
      "global_load_dwordx4 v[148:151], v229, s[52:53] sc1\n\t"
      "global_load_dwordx4 v[152:155], v230, s[52:53] sc1\n\t"
      "s_bitcmp1_b32 s16, 1\n\t"
      "s_cbranch_scc0 HWE_%=\n\t"
      "v_max3_u32 v234, v184, v185, v186\n\t"
      "v_max3_u32 v234, v234, v187, v188\n\t"
      "v_max3_u32 v234, v234, v189, v190\n\t"
      "v_max3_u32 v234, v234, v191, v192\n\t"
      "v_max3_u32 v234, v234, v193, v194\n\t"
      "v_max3_u32 v234, v234, v195, v196\n\t"
      "v_max3_u32 v234, v234, v197, v198\n\t"
      "v_max3_u32 v234, v234, v199, v200\n\t"
      "v_max3_u32 v234, v234, v201, v202\n\t"
      "v_max3_u32 v234, v234, v203, v204\n\t"
      "v_max3_u32 v234, v234, v205, v206\n\t"
      "v_max3_u32 v234, v234, v207, v208\n\t"
      "v_max3_u32 v234, v234, v209, v210\n\t"
      "v_max_u32_e32 v234, v234, v211\n\t"
      "v_cmp_eq_u32_e32 vcc, -1, v234\n\t"
      "s_cbranch_vccnz HXR_%=\n\t"
      "HXB_%=:\n\t"
      "v_mfma_f32_16x16x32_bf16 v[156:159], v[184:187], v[120:123], v[236:239]\n\t"
      "v_mfma_f32_16x16x32_bf16 v[160:163], v[188:191], v[120:123], v[236:239]\n\t"
      "v_mfma_f32_16x16x32_bf16 v[164:167], v[192:195], v[120:123], v[236:239]\n\t"
      "v_mfma_f32_16x16x32_bf16 v[168:171], v[196:199], v[120:123], v[236:239]\n\t"
      "v_mfma_f32_16x16x32_bf16 v[172:175], v[200:203], v[120:123], v[236:239]\n\t"
      "v_mfma_f32_16x16x32_bf16 v[176:179], v[204:207], v[120:123], v[236:239]\n\t"
      "v_mfma_f32_16x16x32_bf16 v[180:183], v[208:211], v[120:123], v[236:239]\n\t"
      "v_mfma_f32_16x16x32_bf16 v[156:159], v[184:187], v[216:219], v[156:159]\n\t"
      "v_mfma_f32_16x16x32_bf16 v[160:163], v[188:191], v[216:219], v[160:163]\n\t"
      "v_mfma_f32_16x16x32_bf16 v[164:167], v[192:195], v[216:219], v[164:167]\n\t"
      "v_mfma_f32_16x16x32_bf16 v[168:171], v[196:199], v[216:219], v[168:171]\n\t"
      "v_mfma_f32_16x16x32_bf16 v[172:175], v[200:203], v[216:219], v[172:175]\n\t"
      "v_mfma_f32_16x16x32_bf16 v[176:179], v[204:207], v[216:219], v[176:179]\n\t"
      "v_mfma_f32_16x16x32_bf16 v[180:183], v[208:211], v[216:219], v[180:183]\n\t"
      "HWE_%=:\n\t"
      "s_nop 7\n\t"
      "s_cmp_ge_u32 s17, 65536\n\t"
      "s_cbranch_scc1 GQ_%=\n\t"
      ".p2align 6\n\t"
      "s_setprio 1\n\t"
      "s_waitcnt vmcnt(6)\n\t"
      "v_mfma_f32_16x16x32_bf16 v[184:187], v[128:131], v[0:3], v[156:159]\n\t"
      "v_mfma_f32_16x16x32_bf16 v[188:191], v[128:131], v[4:7], 0\n\t"
      "v_mfma_f32_16x16x32_bf16 v[192:195], v[128:131], v[8:11], 0\n\t"
      "v_mfma_f32_16x16x32_bf16 v[196:199], v[128:131], v[12:15], 0\n\t"
      "global_load_dwordx4 v[128:131], v224, s[54:55] sc1\n\t"
      "s_waitcnt vmcnt(6)\n\t"
      "v_mfma_f32_16x16x32_bf16 v[184:187], v[132:135], v[16:19], v[184:187]\n\t"
      "s_waitcnt vmcnt(5)\n\t"
      "v_mfma_f32_16x16x32_bf16 v[184:187], v[136:139], v[32:35], v[184:187]\n\t"
      "v_mfma_f32_16x16x32_bf16 v[188:191], v[132:135], v[20:23], v[188:191]\n\t"
      "v_mfma_f32_16x16x32_bf16 v[188:191], v[136:139], v[36:39], v[188:191]\n\t"
      "v_mfma_f32_16x16x32_bf16 v[192:195], v[132:135], v[24:27], v[192:195]\n\t"
      "v_mfma_f32_16x16x32_bf16 v[192:195], v[136:139], v[40:43], v[192:195]\n\t"
      "v_mfma_f32_16x16x32_bf16 v[196:199], v[132:135], v[28:31], v[196:199]\n\t"
      "global_load_dwordx4 v[132:135], v225, s[54:55] sc1\n\t"
      "v_mfma_f32_16x16x32_bf16 v[196:199], v[136:139], v[44:47], v[196:199]\n\t"
      "global_load_dwordx4 v[136:139], v226, s[54:55] sc1\n\t"
      "s_waitcnt vmcnt(6)\n\t"
      "v_mfma_f32_16x16x32_bf16 v[184:187], v[140:143], v[48:51], v[184:187]\n\t"
      "s_waitcnt vmcnt(5)\n\t"
      "v_mfma_f32_16x16x32_bf16 v[184:187], v[144:147], v[64:67], v[184:187]\n\t"
      "v_mfma_f32_16x16x32_bf16 v[188:191], v[140:143], v[52:55], v[188:191]\n\t"
      "v_mfma_f32_16x16x32_bf16 v[188:191], v[144:147], v[68:71], v[188:191]\n\t"
      "v_mfma_f32_16x16x32_bf16 v[192:195], v[140:143], v[56:59], v[192:195]\n\t"
      "v_mfma_f32_16x16x32_bf16 v[192:195], v[144:147], v[72:75], v[192:195]\n\t"
      "v_mfma_f32_16x16x32_bf16 v[196:199], v[140:143], v[60:63], v[196:199]\n\t"
      "global_load_dwordx4 v[140:143], v227, s[54:55] sc1\n\t"
      "v_mfma_f32_16x16x32_bf16 v[196:199], v[144:147], v[76:79], v[196:199]\n\t"
      "global_load_dwordx4 v[144:147], v228, s[54:55] sc1\n\t"
      "s_waitcnt vmcnt(6)\n\t"
      "v_mfma_f32_16x16x32_bf16 v[184:187], v[148:151], v[80:83], v[184:187]\n\t"
      "s_waitcnt vmcnt(5)\n\t"
      "v_mfma_f32_16x16x32_bf16 v[184:187], v[152:155], v[96:99], v[184:187]\n\t"
      "v_mfma_f32_16x16x32_bf16 v[188:191], v[148:151], v[84:87], v[188:191]\n\t"
      "v_mfma_f32_16x16x32_bf16 v[188:191], v[152:155], v[100:103], v[188:191]\n\t"
      "s_nop 5\n\t"
      "v_cmp_u_f32_e32 vcc, v184, v185\n\t"
      "v_cmp_u_f32_e64 s[48:49], v186, v187\n\t"
      "v_mfma_f32_16x16x32_bf16 v[192:195], v[148:151], v[88:91], v[192:195]\n\t"
      "v_mfma_f32_16x16x32_bf16 v[192:195], v[152:155], v[104:107], v[192:195]\n\t"
      "s_or_b64 vcc, vcc, s[48:49]\n\t"
      "s_cbranch_scc1 NP0R_%=\n\t"
      "NP0B_%=:\n\t"
      "ds_write_b128 v243, v[184:187]\n\t"
      "v_mfma_f32_16x16x32_bf16 v[196:199], v[148:151], v[92:95], v[196:199]\n\t"
      "global_load_dwordx4 v[148:151], v229, s[54:55] sc1\n\t"
      "v_mfma_f32_16x16x32_bf16 v[196:199], v[152:155], v[108:111], v[196:199]\n\t"
      "global_load_dwordx4 v[152:155], v230, s[54:55] sc1\n\t"
      "ds_write_b128 v220, v[188:191]\n\t"
      "ds_write_b128 v221, v[192:195]\n\t"
      "s_nop 4\n\t"
      "ds_write_b128 v222, v[196:199]\n\t"
      "s_waitcnt lgkmcnt(0)\n\t"
      "s_barrier\n\t"
      "ds_read_b128 v[112:115], v244\n\t"
      "ds_read_b128 v[116:119], v244 offset:4096\n\t"
      "ds_read_b128 v[120:123], v244 offset:8192\n\t"
      "ds_read_b128 v[124:127], v244 offset:12288\n\t"
      "ds_read_b128 v[184:187], v244 offset:16384\n\t"
      "ds_read_b128 v[188:191], v244 offset:20480\n\t"
      "ds_read_b128 v[192:195], v244 offset:24576\n\t"
      "ds_read_b128 v[196:199], v244 offset:28672\n\t"
      "s_waitcnt vmcnt(6)\n\t"
      "v_mfma_f32_16x16x32_bf16 v[200:203], v[128:131], v[0:3], v[160:163]\n\t"
      "v_mfma_f32_16x16x32_bf16 v[204:207], v[128:131], v[4:7], 0\n\t"
      "s_waitcnt lgkmcnt(6)\n\t"
      "v_add_f32_e32 v112, v112, v116\n\t"
      "v_add_f32_e32 v113, v113, v117\n\t"
      "v_mfma_f32_16x16x32_bf16 v[208:211], v[128:131], v[8:11], 0\n\t"
      "v_add_f32_e32 v114, v114, v118\n\t"
      "v_add_f32_e32 v115, v115, v119\n\t"
      "v_mfma_f32_16x16x32_bf16 v[212:215], v[128:131], v[12:15], 0\n\t"
      "global_load_dwordx4 v[128:131], v224, s[56:57] sc1\n\t"
      "s_waitcnt vmcnt(6)\n\t"
      "v_mfma_f32_16x16x32_bf16 v[200:203], v[132:135], v[16:19], v[200:203]\n\t"
      "s_waitcnt lgkmcnt(4)\n\t"
      "v_add_f32_e32 v120, v120, v124\n\t"
      "s_waitcnt vmcnt(5)\n\t"
      "v_mfma_f32_16x16x32_bf16 v[200:203], v[136:139], v[32:35], v[200:203]\n\t"
      "v_add_f32_e32 v121, v121, v125\n\t"
      "v_add_f32_e32 v122, v122, v126\n\t"
      "v_mfma_f32_16x16x32_bf16 v[204:207], v[132:135], v[20:23], v[204:207]\n\t"
      "v_add_f32_e32 v123, v123, v127\n\t"
      "s_waitcnt lgkmcnt(2)\n\t"
      "v_add_f32_e32 v184, v184, v188\n\t"
      "v_mfma_f32_16x16x32_bf16 v[204:207], v[136:139], v[36:39], v[204:207]\n\t"
      "v_add_f32_e32 v185, v185, v189\n\t"
      "v_add_f32_e32 v186, v186, v190\n\t"
      "v_mfma_f32_16x16x32_bf16 v[208:211], v[132:135], v[24:27], v[208:211]\n\t"
      "v_add_f32_e32 v187, v187, v191\n\t"
      "v_add_f32_e32 v112, v112, v120\n\t"
      "v_mfma_f32_16x16x32_bf16 v[208:211], v[136:139], v[40:43], v[208:211]\n\t"
      "v_add_f32_e32 v113, v113, v121\n\t"
      "v_add_f32_e32 v114, v114, v122\n\t"
      "v_mfma_f32_16x16x32_bf16 v[212:215], v[132:135], v[28:31], v[212:215]\n\t"
      "global_load_dwordx4 v[132:135], v225, s[56:57] sc1\n\t"
      "v_add_f32_e32 v115, v115, v123\n\t"
      "v_mfma_f32_16x16x32_bf16 v[212:215], v[136:139], v[44:47], v[212:215]\n\t"
      "global_load_dwordx4 v[136:139], v226, s[56:57] sc1\n\t"
      "s_waitcnt vmcnt(6)\n\t"
      "v_mfma_f32_16x16x32_bf16 v[200:203], v[140:143], v[48:51], v[200:203]\n\t"
      "s_waitcnt lgkmcnt(0)\n\t"
      "v_add_f32_e32 v192, v192, v196\n\t"
      "s_waitcnt vmcnt(5)\n\t"
      "v_mfma_f32_16x16x32_bf16 v[200:203], v[144:147], v[64:67], v[200:203]\n\t"
      "v_add_f32_e32 v193, v193, v197\n\t"
      "v_add_f32_e32 v194, v194, v198\n\t"
      "v_mfma_f32_16x16x32_bf16 v[204:207], v[140:143], v[52:55], v[204:207]\n\t"
      "v_add_f32_e32 v195, v195, v199\n\t"
      "v_add_f32_e32 v184, v184, v192\n\t"
      "v_mfma_f32_16x16x32_bf16 v[204:207], v[144:147], v[68:71], v[204:207]\n\t"
      "v_add_f32_e32 v185, v185, v193\n\t"
      "v_add_f32_e32 v186, v186, v194\n\t"
      "v_mfma_f32_16x16x32_bf16 v[208:211], v[140:143], v[56:59], v[208:211]\n\t"
      "v_add_f32_e32 v187, v187, v195\n\t"
      "v_add_f32_e32 v112, v112, v184\n\t"
      "v_mfma_f32_16x16x32_bf16 v[208:211], v[144:147], v[72:75], v[208:211]\n\t"
      "v_add_f32_e32 v113, v113, v185\n\t"
      "v_add_f32_e32 v114, v114, v186\n\t"
      "v_mfma_f32_16x16x32_bf16 v[212:215], v[140:143], v[60:63], v[212:215]\n\t"
      "global_load_dwordx4 v[140:143], v227, s[56:57] sc1\n\t"
      "v_add_f32_e32 v115, v115, v187\n\t"
      "v_mfma_f32_16x16x32_bf16 v[212:215], v[144:147], v[76:79], v[212:215]\n\t"
      "global_load_dwordx4 v[144:147], v228, s[56:57] sc1\n\t"
      "s_waitcnt vmcnt(6)\n\t"
      "v_mfma_f32_16x16x32_bf16 v[200:203], v[148:151], v[80:83], v[200:203]\n\t"
      "v_cvt_pk_bf16_f32 v116, v112, v113\n\t"
      "s_waitcnt vmcnt(5)\n\t"
      "v_mfma_f32_16x16x32_bf16 v[200:203], v[152:155], v[96:99], v[200:203]\n\t"
      "v_cvt_pk_bf16_f32 v117, v114, v115\n\t"
      "global_store_dwordx2 v245, v[116:117], s[8:9]\n\t"
      "v_mfma_f32_16x16x32_bf16 v[204:207], v[148:151], v[84:87], v[204:207]\n\t"
      "v_mfma_f32_16x16x32_bf16 v[204:207], v[152:155], v[100:103], v[204:207]\n\t"
      "s_nop 3\n\t"
      "v_cmp_u_f32_e32 vcc, v200, v201\n\t"
      "v_cmp_u_f32_e64 s[48:49], v202, v203\n\t"
      "v_mfma_f32_16x16x32_bf16 v[208:211], v[148:151], v[88:91], v[208:211]\n\t"
      "v_mfma_f32_16x16x32_bf16 v[208:211], v[152:155], v[104:107], v[208:211]\n\t"
      "s_or_b64 vcc, vcc, s[48:49]\n\t"
      "s_cbranch_scc1 NP1R_%=\n\t"
      "NP1B_%=:\n\t"
      "ds_write_b128 v243, v[200:203] offset:32768\n\t"
      "v_mfma_f32_16x16x32_bf16 v[212:215], v[148:151], v[92:95], v[212:215]\n\t"
      "global_load_dwordx4 v[148:151], v229, s[56:57] sc1\n\t"
      "v_mfma_f32_16x16x32_bf16 v[212:215], v[152:155], v[108:111], v[212:215]\n\t"
      "global_load_dwordx4 v[152:155], v230, s[56:57] sc1\n\t"
      "ds_write_b128 v220, v[204:207] offset:32768\n\t"
      "ds_write_b128 v221, v[208:211] offset:32768\n\t"
      "s_nop 4\n\t"
      "ds_write_b128 v222, v[212:215] offset:32768\n\t"
      "s_waitcnt lgkmcnt(0)\n\t"
      "s_barrier\n\t"
      "ds_read_b128 v[112:115], v244 offset:32768\n\t"
      "ds_read_b128 v[116:119], v244 offset:36864\n\t"
      "ds_read_b128 v[120:123], v244 offset:40960\n\t"
      "ds_read_b128 v[124:127], v244 offset:45056\n\t"
      "ds_read_b128 v[200:203], v244 offset:49152\n\t"
      "ds_read_b128 v[204:207], v244 offset:53248\n\t"
      "ds_read_b128 v[208:211], v244 offset:57344\n\t"
      "ds_read_b128 v[212:215], v244 offset:61440\n\t"
      "s_waitcnt vmcnt(7)\n\t"
      "v_mfma_f32_16x16x32_bf16 v[184:187], v[128:131], v[0:3], v[164:167]\n\t"
      "v_mfma_f32_16x16x32_bf16 v[188:191], v[128:131], v[4:7], 0\n\t"
      "s_waitcnt lgkmcnt(6)\n\t"
      "v_add_f32_e32 v112, v112, v116\n\t"
      "v_add_f32_e32 v113, v113, v117\n\t"
      "v_mfma_f32_16x16x32_bf16 v[192:195], v[128:131], v[8:11], 0\n\t"
      "v_add_f32_e32 v114, v114, v118\n\t"
      "v_add_f32_e32 v115, v115, v119\n\t"
      "v_mfma_f32_16x16x32_bf16 v[196:199], v[128:131], v[12:15], 0\n\t"
      "global_load_dwordx4 v[128:131], v224, s[58:59] sc1\n\t"
      "s_waitcnt vmcnt(7)\n\t"
      "v_mfma_f32_16x16x32_bf16 v[184:187], v[132:135], v[16:19], v[184:187]\n\t"
      "s_waitcnt lgkmcnt(4)\n\t"
      "v_add_f32_e32 v120, v120, v124\n\t"
      "s_waitcnt vmcnt(6)\n\t"
      "v_mfma_f32_16x16x32_bf16 v[184:187], v[136:139], v[32:35], v[184:187]\n\t"
      "v_add_f32_e32 v121, v121, v125\n\t"
      "v_add_f32_e32 v122, v122, v126\n\t"
      "v_mfma_f32_16x16x32_bf16 v[188:191], v[132:135], v[20:23], v[188:191]\n\t"
      "v_add_f32_e32 v123, v123, v127\n\t"
      "s_waitcnt lgkmcnt(2)\n\t"
      "v_add_f32_e32 v200, v200, v204\n\t"
      "v_mfma_f32_16x16x32_bf16 v[188:191], v[136:139], v[36:39], v[188:191]\n\t"
      "v_add_f32_e32 v201, v201, v205\n\t"
      "v_add_f32_e32 v202, v202, v206\n\t"
      "v_mfma_f32_16x16x32_bf16 v[192:195], v[132:135], v[24:27], v[192:195]\n\t"
      "v_add_f32_e32 v203, v203, v207\n\t"
      "v_add_f32_e32 v112, v112, v120\n\t"
      "v_mfma_f32_16x16x32_bf16 v[192:195], v[136:139], v[40:43], v[192:195]\n\t"
      "v_add_f32_e32 v113, v113, v121\n\t"
      "v_add_f32_e32 v114, v114, v122\n\t"
      "v_mfma_f32_16x16x32_bf16 v[196:199], v[132:135], v[28:31], v[196:199]\n\t"
      "global_load_dwordx4 v[132:135], v225, s[58:59] sc1\n\t"
      "v_add_f32_e32 v115, v115, v123\n\t"
      "v_mfma_f32_16x16x32_bf16 v[196:199], v[136:139], v[44:47], v[196:199]\n\t"
      "global_load_dwordx4 v[136:139], v226, s[58:59] sc1\n\t"
      "s_waitcnt vmcnt(7)\n\t"
      "v_mfma_f32_16x16x32_bf16 v[184:187], v[140:143], v[48:51], v[184:187]\n\t"
      "s_waitcnt lgkmcnt(0)\n\t"
      "v_add_f32_e32 v208, v208, v212\n\t"
      "s_waitcnt vmcnt(6)\n\t"
      "v_mfma_f32_16x16x32_bf16 v[184:187], v[144:147], v[64:67], v[184:187]\n\t"
      "v_add_f32_e32 v209, v209, v213\n\t"
      "v_add_f32_e32 v210, v210, v214\n\t"
      "v_mfma_f32_16x16x32_bf16 v[188:191], v[140:143], v[52:55], v[188:191]\n\t"
      "v_add_f32_e32 v211, v211, v215\n\t"
      "v_add_f32_e32 v200, v200, v208\n\t"
      "v_mfma_f32_16x16x32_bf16 v[188:191], v[144:147], v[68:71], v[188:191]\n\t"
      "v_add_f32_e32 v201, v201, v209\n\t"
      "v_add_f32_e32 v202, v202, v210\n\t"
      "v_mfma_f32_16x16x32_bf16 v[192:195], v[140:143], v[56:59], v[192:195]\n\t"
      "v_add_f32_e32 v203, v203, v211\n\t"
      "v_add_f32_e32 v112, v112, v200\n\t"
      "v_mfma_f32_16x16x32_bf16 v[192:195], v[144:147], v[72:75], v[192:195]\n\t"
      "v_add_f32_e32 v113, v113, v201\n\t"
      "v_add_f32_e32 v114, v114, v202\n\t"
      "v_mfma_f32_16x16x32_bf16 v[196:199], v[140:143], v[60:63], v[196:199]\n\t"
      "global_load_dwordx4 v[140:143], v227, s[58:59] sc1\n\t"
      "v_add_f32_e32 v115, v115, v203\n\t"
      "v_mfma_f32_16x16x32_bf16 v[196:199], v[144:147], v[76:79], v[196:199]\n\t"
      "global_load_dwordx4 v[144:147], v228, s[58:59] sc1\n\t"
      "s_waitcnt vmcnt(6)\n\t"
      "v_mfma_f32_16x16x32_bf16 v[184:187], v[148:151], v[80:83], v[184:187]\n\t"
      "v_cvt_pk_bf16_f32 v116, v112, v113\n\t"
      "s_waitcnt vmcnt(5)\n\t"
      "v_mfma_f32_16x16x32_bf16 v[184:187], v[152:155], v[96:99], v[184:187]\n\t"
      "v_cvt_pk_bf16_f32 v117, v114, v115\n\t"
      "global_store_dwordx2 v245, v[116:117], s[8:9] offset:32\n\t"
      "v_mfma_f32_16x16x32_bf16 v[188:191], v[148:151], v[84:87], v[188:191]\n\t"
      "v_mfma_f32_16x16x32_bf16 v[188:191], v[152:155], v[100:103], v[188:191]\n\t"
      "s_nop 3\n\t"
      "v_cmp_u_f32_e32 vcc, v184, v185\n\t"
      "v_cmp_u_f32_e64 s[48:49], v186, v187\n\t"
      "v_mfma_f32_16x16x32_bf16 v[192:195], v[148:151], v[88:91], v[192:195]\n\t"
      "v_mfma_f32_16x16x32_bf16 v[192:195], v[152:155], v[104:107], v[192:195]\n\t"
      "s_or_b64 vcc, vcc, s[48:49]\n\t"
      "s_cbranch_scc1 NP2R_%=\n\t"
      "NP2B_%=:\n\t"
      "ds_write_b128 v243, v[184:187]\n\t"
      "v_mfma_f32_16x16x32_bf16 v[196:199], v[148:151], v[92:95], v[196:199]\n\t"
      "global_load_dwordx4 v[148:151], v229, s[58:59] sc1\n\t"
      "v_mfma_f32_16x16x32_bf16 v[196:199], v[152:155], v[108:111], v[196:199]\n\t"
      "global_load_dwordx4 v[152:155], v230, s[58:59] sc1\n\t"
      "ds_write_b128 v220, v[188:191]\n\t"
      "ds_write_b128 v221, v[192:195]\n\t"
      "s_nop 4\n\t"
      "ds_write_b128 v222, v[196:199]\n\t"
      "s_waitcnt lgkmcnt(0)\n\t"
      "s_barrier\n\t"
      "ds_read_b128 v[112:115], v244\n\t"
      "ds_read_b128 v[116:119], v244 offset:4096\n\t"
      "ds_read_b128 v[120:123], v244 offset:8192\n\t"
      "ds_read_b128 v[124:127], v244 offset:12288\n\t"
      "ds_read_b128 v[184:187], v244 offset:16384\n\t"
      "ds_read_b128 v[188:191], v244 offset:20480\n\t"
      "ds_read_b128 v[192:195], v244 offset:24576\n\t"
      "ds_read_b128 v[196:199], v244 offset:28672\n\t"
      "s_waitcnt vmcnt(7)\n\t"
      "v_mfma_f32_16x16x32_bf16 v[200:203], v[128:131], v[0:3], v[168:171]\n\t"
      "v_mfma_f32_16x16x32_bf16 v[204:207], v[128:131], v[4:7], 0\n\t"
      "s_waitcnt lgkmcnt(6)\n\t"
      "v_add_f32_e32 v112, v112, v116\n\t"
      "v_add_f32_e32 v113, v113, v117\n\t"
      "v_mfma_f32_16x16x32_bf16 v[208:211], v[128:131], v[8:11], 0\n\t"
      "v_add_f32_e32 v114, v114, v118\n\t"
      "v_add_f32_e32 v115, v115, v119\n\t"
      "v_mfma_f32_16x16x32_bf16 v[212:215], v[128:131], v[12:15], 0\n\t"
      "global_load_dwordx4 v[128:131], v224, s[60:61] sc1\n\t"
      "s_waitcnt vmcnt(7)\n\t"
      "v_mfma_f32_16x16x32_bf16 v[200:203], v[132:135], v[16:19], v[200:203]\n\t"
      "s_waitcnt lgkmcnt(4)\n\t"
      "v_add_f32_e32 v120, v120, v124\n\t"
      "s_waitcnt vmcnt(6)\n\t"
      "v_mfma_f32_16x16x32_bf16 v[200:203], v[136:139], v[32:35], v[200:203]\n\t"
      "v_add_f32_e32 v121, v121, v125\n\t"
      "v_add_f32_e32 v122, v122, v126\n\t"
      "v_mfma_f32_16x16x32_bf16 v[204:207], v[132:135], v[20:23], v[204:207]\n\t"
      "v_add_f32_e32 v123, v123, v127\n\t"
      "s_waitcnt lgkmcnt(2)\n\t"
      "v_add_f32_e32 v184, v184, v188\n\t"
      "v_mfma_f32_16x16x32_bf16 v[204:207], v[136:139], v[36:39], v[204:207]\n\t"
      "v_add_f32_e32 v185, v185, v189\n\t"
      "v_add_f32_e32 v186, v186, v190\n\t"
      "v_mfma_f32_16x16x32_bf16 v[208:211], v[132:135], v[24:27], v[208:211]\n\t"
      "v_add_f32_e32 v187, v187, v191\n\t"
      "v_add_f32_e32 v112, v112, v120\n\t"
      "v_mfma_f32_16x16x32_bf16 v[208:211], v[136:139], v[40:43], v[208:211]\n\t"
      "v_add_f32_e32 v113, v113, v121\n\t"
      "v_add_f32_e32 v114, v114, v122\n\t"
      "v_mfma_f32_16x16x32_bf16 v[212:215], v[132:135], v[28:31], v[212:215]\n\t"
      "global_load_dwordx4 v[132:135], v225, s[60:61] sc1\n\t"
      "v_add_f32_e32 v115, v115, v123\n\t"
      "v_mfma_f32_16x16x32_bf16 v[212:215], v[136:139], v[44:47], v[212:215]\n\t"
      "global_load_dwordx4 v[136:139], v226, s[60:61] sc1\n\t"
      "s_waitcnt vmcnt(7)\n\t"
      "v_mfma_f32_16x16x32_bf16 v[200:203], v[140:143], v[48:51], v[200:203]\n\t"
      "s_waitcnt lgkmcnt(0)\n\t"
      "v_add_f32_e32 v192, v192, v196\n\t"
      "s_waitcnt vmcnt(6)\n\t"
      "v_mfma_f32_16x16x32_bf16 v[200:203], v[144:147], v[64:67], v[200:203]\n\t"
      "v_add_f32_e32 v193, v193, v197\n\t"
      "v_add_f32_e32 v194, v194, v198\n\t"
      "v_mfma_f32_16x16x32_bf16 v[204:207], v[140:143], v[52:55], v[204:207]\n\t"
      "v_add_f32_e32 v195, v195, v199\n\t"
      "v_add_f32_e32 v184, v184, v192\n\t"
      "v_mfma_f32_16x16x32_bf16 v[204:207], v[144:147], v[68:71], v[204:207]\n\t"
      "v_add_f32_e32 v185, v185, v193\n\t"
      "v_add_f32_e32 v186, v186, v194\n\t"
      "v_mfma_f32_16x16x32_bf16 v[208:211], v[140:143], v[56:59], v[208:211]\n\t"
      "v_add_f32_e32 v187, v187, v195\n\t"
      "v_add_f32_e32 v112, v112, v184\n\t"
      "v_mfma_f32_16x16x32_bf16 v[208:211], v[144:147], v[72:75], v[208:211]\n\t"
      "v_add_f32_e32 v113, v113, v185\n\t"
      "v_add_f32_e32 v114, v114, v186\n\t"
      "v_mfma_f32_16x16x32_bf16 v[212:215], v[140:143], v[60:63], v[212:215]\n\t"
      "global_load_dwordx4 v[140:143], v227, s[60:61] sc1\n\t"
      "v_add_f32_e32 v115, v115, v187\n\t"
      "v_mfma_f32_16x16x32_bf16 v[212:215], v[144:147], v[76:79], v[212:215]\n\t"
      "global_load_dwordx4 v[144:147], v228, s[60:61] sc1\n\t"
      "s_waitcnt vmcnt(6)\n\t"
      "v_mfma_f32_16x16x32_bf16 v[200:203], v[148:151], v[80:83], v[200:203]\n\t"
      "v_cvt_pk_bf16_f32 v116, v112, v113\n\t"
      "s_waitcnt vmcnt(5)\n\t"
      "v_mfma_f32_16x16x32_bf16 v[200:203], v[152:155], v[96:99], v[200:203]\n\t"
      "v_cvt_pk_bf16_f32 v117, v114, v115\n\t"
      "global_store_dwordx2 v245, v[116:117], s[66:67]\n\t"
      "v_mfma_f32_16x16x32_bf16 v[204:207], v[148:151], v[84:87], v[204:207]\n\t"
      "v_mfma_f32_16x16x32_bf16 v[204:207], v[152:155], v[100:103], v[204:207]\n\t"
      "s_nop 3\n\t"
      "v_cmp_u_f32_e32 vcc, v200, v201\n\t"
      "v_cmp_u_f32_e64 s[48:49], v202, v203\n\t"
      "v_mfma_f32_16x16x32_bf16 v[208:211], v[148:151], v[88:91], v[208:211]\n\t"
      "v_mfma_f32_16x16x32_bf16 v[208:211], v[152:155], v[104:107], v[208:211]\n\t"
      "s_or_b64 vcc, vcc, s[48:49]\n\t"
      "s_cbranch_scc1 NP3R_%=\n\t"
      "NP3B_%=:\n\t"
      "ds_write_b128 v243, v[200:203] offset:32768\n\t"
      "v_mfma_f32_16x16x32_bf16 v[212:215], v[148:151], v[92:95], v[212:215]\n\t"
      "global_load_dwordx4 v[148:151], v229, s[60:61] sc1\n\t"
      "v_mfma_f32_16x16x32_bf16 v[212:215], v[152:155], v[108:111], v[212:215]\n\t"
      "global_load_dwordx4 v[152:155], v230, s[60:61] sc1\n\t"
      "ds_write_b128 v220, v[204:207] offset:32768\n\t"
      "ds_write_b128 v221, v[208:211] offset:32768\n\t"
      "s_nop 4\n\t"
      "ds_write_b128 v222, v[212:215] offset:32768\n\t"
      "s_waitcnt lgkmcnt(0)\n\t"
      "s_barrier\n\t"
      "ds_read_b128 v[112:115], v244 offset:32768\n\t"
      "ds_read_b128 v[116:119], v244 offset:36864\n\t"
      "ds_read_b128 v[120:123], v244 offset:40960\n\t"
      "ds_read_b128 v[124:127], v244 offset:45056\n\t"
      "ds_read_b128 v[200:203], v244 offset:49152\n\t"
      "ds_read_b128 v[204:207], v244 offset:53248\n\t"
      "ds_read_b128 v[208:211], v244 offset:57344\n\t"
      "ds_read_b128 v[212:215], v244 offset:61440\n\t"
      "s_waitcnt vmcnt(7)\n\t"
      "v_mfma_f32_16x16x32_bf16 v[184:187], v[128:131], v[0:3], v[172:175]\n\t"
      "v_mfma_f32_16x16x32_bf16 v[188:191], v[128:131], v[4:7], 0\n\t"
      "s_waitcnt lgkmcnt(6)\n\t"
      "v_add_f32_e32 v112, v112, v116\n\t"
      "v_add_f32_e32 v113, v113, v117\n\t"
      "v_mfma_f32_16x16x32_bf16 v[192:195], v[128:131], v[8:11], 0\n\t"
      "v_add_f32_e32 v114, v114, v118\n\t"
      "v_add_f32_e32 v115, v115, v119\n\t"
      "v_mfma_f32_16x16x32_bf16 v[196:199], v[128:131], v[12:15], 0\n\t"
      "global_load_dwordx4 v[128:131], v224, s[62:63] sc1\n\t"
      "s_waitcnt vmcnt(7)\n\t"
      "v_mfma_f32_16x16x32_bf16 v[184:187], v[132:135], v[16:19], v[184:187]\n\t"
      "s_waitcnt lgkmcnt(4)\n\t"
      "v_add_f32_e32 v120, v120, v124\n\t"
      "s_waitcnt vmcnt(6)\n\t"
      "v_mfma_f32_16x16x32_bf16 v[184:187], v[136:139], v[32:35], v[184:187]\n\t"
      "v_add_f32_e32 v121, v121, v125\n\t"
      "v_add_f32_e32 v122, v122, v126\n\t"
      "v_mfma_f32_16x16x32_bf16 v[188:191], v[132:135], v[20:23], v[188:191]\n\t"
      "v_add_f32_e32 v123, v123, v127\n\t"
      "s_waitcnt lgkmcnt(2)\n\t"
      "v_add_f32_e32 v200, v200, v204\n\t"
      "v_mfma_f32_16x16x32_bf16 v[188:191], v[136:139], v[36:39], v[188:191]\n\t"
      "v_add_f32_e32 v201, v201, v205\n\t"
      "v_add_f32_e32 v202, v202, v206\n\t"
      "v_mfma_f32_16x16x32_bf16 v[192:195], v[132:135], v[24:27], v[192:195]\n\t"
      "v_add_f32_e32 v203, v203, v207\n\t"
      "v_add_f32_e32 v112, v112, v120\n\t"
      "v_mfma_f32_16x16x32_bf16 v[192:195], v[136:139], v[40:43], v[192:195]\n\t"
      "v_add_f32_e32 v113, v113, v121\n\t"
      "v_add_f32_e32 v114, v114, v122\n\t"
      "v_mfma_f32_16x16x32_bf16 v[196:199], v[132:135], v[28:31], v[196:199]\n\t"
      "global_load_dwordx4 v[132:135], v225, s[62:63] sc1\n\t"
      "v_add_f32_e32 v115, v115, v123\n\t"
      "v_mfma_f32_16x16x32_bf16 v[196:199], v[136:139], v[44:47], v[196:199]\n\t"
      "global_load_dwordx4 v[136:139], v226, s[62:63] sc1\n\t"
      "s_waitcnt vmcnt(7)\n\t"
      "v_mfma_f32_16x16x32_bf16 v[184:187], v[140:143], v[48:51], v[184:187]\n\t"
      "s_waitcnt lgkmcnt(0)\n\t"
      "v_add_f32_e32 v208, v208, v212\n\t"
      "s_waitcnt vmcnt(6)\n\t"
      "v_mfma_f32_16x16x32_bf16 v[184:187], v[144:147], v[64:67], v[184:187]\n\t"
      "v_add_f32_e32 v209, v209, v213\n\t"
      "v_add_f32_e32 v210, v210, v214\n\t"
      "v_mfma_f32_16x16x32_bf16 v[188:191], v[140:143], v[52:55], v[188:191]\n\t"
      "v_add_f32_e32 v211, v211, v215\n\t"
      "v_add_f32_e32 v200, v200, v208\n\t"
      "v_mfma_f32_16x16x32_bf16 v[188:191], v[144:147], v[68:71], v[188:191]\n\t"
      "v_add_f32_e32 v201, v201, v209\n\t"
      "v_add_f32_e32 v202, v202, v210\n\t"
      "v_mfma_f32_16x16x32_bf16 v[192:195], v[140:143], v[56:59], v[192:195]\n\t"
      "v_add_f32_e32 v203, v203, v211\n\t"
      "v_add_f32_e32 v112, v112, v200\n\t"
      "v_mfma_f32_16x16x32_bf16 v[192:195], v[144:147], v[72:75], v[192:195]\n\t"
      "v_add_f32_e32 v113, v113, v201\n\t"
      "v_add_f32_e32 v114, v114, v202\n\t"
      "v_mfma_f32_16x16x32_bf16 v[196:199], v[140:143], v[60:63], v[196:199]\n\t"
      "global_load_dwordx4 v[140:143], v227, s[62:63] sc1\n\t"
      "v_add_f32_e32 v115, v115, v203\n\t"
      "v_mfma_f32_16x16x32_bf16 v[196:199], v[144:147], v[76:79], v[196:199]\n\t"
      "global_load_dwordx4 v[144:147], v228, s[62:63] sc1\n\t"
      "s_waitcnt vmcnt(6)\n\t"
      "v_mfma_f32_16x16x32_bf16 v[184:187], v[148:151], v[80:83], v[184:187]\n\t"
      "v_cvt_pk_bf16_f32 v116, v112, v113\n\t"
      "s_waitcnt vmcnt(5)\n\t"
      "v_mfma_f32_16x16x32_bf16 v[184:187], v[152:155], v[96:99], v[184:187]\n\t"
      "v_cvt_pk_bf16_f32 v117, v114, v115\n\t"
      "global_store_dwordx2 v245, v[116:117], s[66:67] offset:32\n\t"
      "v_mfma_f32_16x16x32_bf16 v[188:191], v[148:151], v[84:87], v[188:191]\n\t"
      "v_mfma_f32_16x16x32_bf16 v[188:191], v[152:155], v[100:103], v[188:191]\n\t"
      "s_nop 3\n\t"
      "v_cmp_u_f32_e32 vcc, v184, v185\n\t"
      "v_cmp_u_f32_e64 s[48:49], v186, v187\n\t"
      "v_mfma_f32_16x16x32_bf16 v[192:195], v[148:151], v[88:91], v[192:195]\n\t"
      "v_mfma_f32_16x16x32_bf16 v[192:195], v[152:155], v[104:107], v[192:195]\n\t"
      "s_or_b64 vcc, vcc, s[48:49]\n\t"
      "s_cbranch_scc1 NP4R_%=\n\t"
      "NP4B_%=:\n\t"
      "ds_write_b128 v243, v[184:187]\n\t"
      "v_mfma_f32_16x16x32_bf16 v[196:199], v[148:151], v[92:95], v[196:199]\n\t"
      "global_load_dwordx4 v[148:151], v229, s[62:63] sc1\n\t"
      "v_mfma_f32_16x16x32_bf16 v[196:199], v[152:155], v[108:111], v[196:199]\n\t"
      "global_load_dwordx4 v[152:155], v230, s[62:63] sc1\n\t"
      "ds_write_b128 v220, v[188:191]\n\t"
      "ds_write_b128 v221, v[192:195]\n\t"
      "s_nop 4\n\t"
      "ds_write_b128 v222, v[196:199]\n\t"
      "s_waitcnt lgkmcnt(0)\n\t"
      "s_barrier\n\t"
      "ds_read_b128 v[112:115], v244\n\t"
      "ds_read_b128 v[116:119], v244 offset:4096\n\t"
      "ds_read_b128 v[120:123], v244 offset:8192\n\t"
      "ds_read_b128 v[124:127], v244 offset:12288\n\t"
      "ds_read_b128 v[184:187], v244 offset:16384\n\t"
      "ds_read_b128 v[188:191], v244 offset:20480\n\t"
      "ds_read_b128 v[192:195], v244 offset:24576\n\t"
      "ds_read_b128 v[196:199], v244 offset:28672\n\t"
      "s_waitcnt vmcnt(7)\n\t"
      "v_mfma_f32_16x16x32_bf16 v[200:203], v[128:131], v[0:3], v[176:179]\n\t"
      "v_mfma_f32_16x16x32_bf16 v[204:207], v[128:131], v[4:7], 0\n\t"
      "s_waitcnt lgkmcnt(6)\n\t"
      "v_add_f32_e32 v112, v112, v116\n\t"
      "v_add_f32_e32 v113, v113, v117\n\t"
      "v_mfma_f32_16x16x32_bf16 v[208:211], v[128:131], v[8:11], 0\n\t"
      "v_add_f32_e32 v114, v114, v118\n\t"
      "v_add_f32_e32 v115, v115, v119\n\t"
      "v_mfma_f32_16x16x32_bf16 v[212:215], v[128:131], v[12:15], 0\n\t"
      "global_load_dwordx4 v[128:131], v224, s[64:65] sc1\n\t"
      "s_waitcnt vmcnt(7)\n\t"
      "v_mfma_f32_16x16x32_bf16 v[200:203], v[132:135], v[16:19], v[200:203]\n\t"
      "s_waitcnt lgkmcnt(4)\n\t"
      "v_add_f32_e32 v120, v120, v124\n\t"
      "s_waitcnt vmcnt(6)\n\t"
      "v_mfma_f32_16x16x32_bf16 v[200:203], v[136:139], v[32:35], v[200:203]\n\t"
      "v_add_f32_e32 v121, v121, v125\n\t"
      "v_add_f32_e32 v122, v122, v126\n\t"
      "v_mfma_f32_16x16x32_bf16 v[204:207], v[132:135], v[20:23], v[204:207]\n\t"
      "v_add_f32_e32 v123, v123, v127\n\t"
      "s_waitcnt lgkmcnt(2)\n\t"
      "v_add_f32_e32 v184, v184, v188\n\t"
      "v_mfma_f32_16x16x32_bf16 v[204:207], v[136:139], v[36:39], v[204:207]\n\t"
      "v_add_f32_e32 v185, v185, v189\n\t"
      "v_add_f32_e32 v186, v186, v190\n\t"
      "v_mfma_f32_16x16x32_bf16 v[208:211], v[132:135], v[24:27], v[208:211]\n\t"
      "v_add_f32_e32 v187, v187, v191\n\t"
      "v_add_f32_e32 v112, v112, v120\n\t"
      "v_mfma_f32_16x16x32_bf16 v[208:211], v[136:139], v[40:43], v[208:211]\n\t"
      "v_add_f32_e32 v113, v113, v121\n\t"
      "v_add_f32_e32 v114, v114, v122\n\t"
      "v_mfma_f32_16x16x32_bf16 v[212:215], v[132:135], v[28:31], v[212:215]\n\t"
      "global_load_dwordx4 v[132:135], v225, s[64:65] sc1\n\t"
      "v_add_f32_e32 v115, v115, v123\n\t"
      "v_mfma_f32_16x16x32_bf16 v[212:215], v[136:139], v[44:47], v[212:215]\n\t"
      "global_load_dwordx4 v[136:139], v226, s[64:65] sc1\n\t"
      "s_waitcnt vmcnt(7)\n\t"
      "v_mfma_f32_16x16x32_bf16 v[200:203], v[140:143], v[48:51], v[200:203]\n\t"
      "s_waitcnt lgkmcnt(0)\n\t"
      "v_add_f32_e32 v192, v192, v196\n\t"
      "s_waitcnt vmcnt(6)\n\t"
      "v_mfma_f32_16x16x32_bf16 v[200:203], v[144:147], v[64:67], v[200:203]\n\t"
      "v_add_f32_e32 v193, v193, v197\n\t"
      "v_add_f32_e32 v194, v194, v198\n\t"
      "v_mfma_f32_16x16x32_bf16 v[204:207], v[140:143], v[52:55], v[204:207]\n\t"
      "v_add_f32_e32 v195, v195, v199\n\t"
      "v_add_f32_e32 v184, v184, v192\n\t"
      "v_mfma_f32_16x16x32_bf16 v[204:207], v[144:147], v[68:71], v[204:207]\n\t"
      "v_add_f32_e32 v185, v185, v193\n\t"
      "v_add_f32_e32 v186, v186, v194\n\t"
      "v_mfma_f32_16x16x32_bf16 v[208:211], v[140:143], v[56:59], v[208:211]\n\t"
      "v_add_f32_e32 v187, v187, v195\n\t"
      "v_add_f32_e32 v112, v112, v184\n\t"
      "v_mfma_f32_16x16x32_bf16 v[208:211], v[144:147], v[72:75], v[208:211]\n\t"
      "v_add_f32_e32 v113, v113, v185\n\t"
      "v_add_f32_e32 v114, v114, v186\n\t"
      "v_mfma_f32_16x16x32_bf16 v[212:215], v[140:143], v[60:63], v[212:215]\n\t"
      "global_load_dwordx4 v[140:143], v227, s[64:65] sc1\n\t"
      "v_add_f32_e32 v115, v115, v187\n\t"
      "v_mfma_f32_16x16x32_bf16 v[212:215], v[144:147], v[76:79], v[212:215]\n\t"
      "global_load_dwordx4 v[144:147], v228, s[64:65] sc1\n\t"
      "s_waitcnt vmcnt(6)\n\t"
      "v_mfma_f32_16x16x32_bf16 v[200:203], v[148:151], v[80:83], v[200:203]\n\t"
      "v_cvt_pk_bf16_f32 v116, v112, v113\n\t"
      "s_waitcnt vmcnt(5)\n\t"
      "v_mfma_f32_16x16x32_bf16 v[200:203], v[152:155], v[96:99], v[200:203]\n\t"
      "v_cvt_pk_bf16_f32 v117, v114, v115\n\t"
      "global_store_dwordx2 v245, v[116:117], s[8:9] offset:64\n\t"
      "v_mfma_f32_16x16x32_bf16 v[204:207], v[148:151], v[84:87], v[204:207]\n\t"
      "v_mfma_f32_16x16x32_bf16 v[204:207], v[152:155], v[100:103], v[204:207]\n\t"
      "s_nop 3\n\t"
      "v_cmp_u_f32_e32 vcc, v200, v201\n\t"
      "v_cmp_u_f32_e64 s[48:49], v202, v203\n\t"
      "v_mfma_f32_16x16x32_bf16 v[208:211], v[148:151], v[88:91], v[208:211]\n\t"
      "v_mfma_f32_16x16x32_bf16 v[208:211], v[152:155], v[104:107], v[208:211]\n\t"
      "s_or_b64 vcc, vcc, s[48:49]\n\t"
      "s_cbranch_scc1 NP5R_%=\n\t"
      "NP5B_%=:\n\t"
      "ds_write_b128 v243, v[200:203] offset:32768\n\t"
      "v_mfma_f32_16x16x32_bf16 v[212:215], v[148:151], v[92:95], v[212:215]\n\t"
      "global_load_dwordx4 v[148:151], v229, s[64:65] sc1\n\t"
      "v_mfma_f32_16x16x32_bf16 v[212:215], v[152:155], v[108:111], v[212:215]\n\t"
      "global_load_dwordx4 v[152:155], v230, s[64:65] sc1\n\t"
      "ds_write_b128 v220, v[204:207] offset:32768\n\t"
      "ds_write_b128 v221, v[208:211] offset:32768\n\t"
      "s_nop 4\n\t"
      "ds_write_b128 v222, v[212:215] offset:32768\n\t"
      "s_waitcnt lgkmcnt(0)\n\t"
      "s_barrier\n\t"
      "ds_read_b128 v[112:115], v244 offset:32768\n\t"
      "ds_read_b128 v[116:119], v244 offset:36864\n\t"
      "ds_read_b128 v[120:123], v244 offset:40960\n\t"
      "ds_read_b128 v[124:127], v244 offset:45056\n\t"
      "ds_read_b128 v[200:203], v244 offset:49152\n\t"
      "ds_read_b128 v[204:207], v244 offset:53248\n\t"
      "ds_read_b128 v[208:211], v244 offset:57344\n\t"
      "ds_read_b128 v[212:215], v244 offset:61440\n\t"
      "s_waitcnt vmcnt(7)\n\t"
      "v_mfma_f32_16x16x32_bf16 v[184:187], v[128:131], v[0:3], v[180:183]\n\t"
      "v_mfma_f32_16x16x32_bf16 v[188:191], v[128:131], v[4:7], 0\n\t"
      "v_mfma_f32_16x16x32_bf16 v[192:195], v[128:131], v[8:11], 0\n\t"
      "v_mfma_f32_16x16x32_bf16 v[196:199], v[128:131], v[12:15], 0\n\t"
      "s_waitcnt vmcnt(6)\n\t"
      "v_mfma_f32_16x16x32_bf16 v[184:187], v[132:135], v[16:19], v[184:187]\n\t"
      "s_waitcnt lgkmcnt(6)\n\t"
      "v_add_f32_e32 v112, v112, v116\n\t"
      "s_waitcnt vmcnt(5)\n\t"
      "v_mfma_f32_16x16x32_bf16 v[184:187], v[136:139], v[32:35], v[184:187]\n\t"
      "v_add_f32_e32 v113, v113, v117\n\t"
      "v_add_f32_e32 v114, v114, v118\n\t"
      "v_mfma_f32_16x16x32_bf16 v[188:191], v[132:135], v[20:23], v[188:191]\n\t"
      "v_add_f32_e32 v115, v115, v119\n\t"
      "s_waitcnt lgkmcnt(4)\n\t"
      "v_add_f32_e32 v120, v120, v124\n\t"
      "v_mfma_f32_16x16x32_bf16 v[188:191], v[136:139], v[36:39], v[188:191]\n\t"
      "v_add_f32_e32 v121, v121, v125\n\t"
      "v_add_f32_e32 v122, v122, v126\n\t"
      "v_mfma_f32_16x16x32_bf16 v[192:195], v[132:135], v[24:27], v[192:195]\n\t"
      "v_add_f32_e32 v123, v123, v127\n\t"
      "s_waitcnt lgkmcnt(2)\n\t"
      "v_add_f32_e32 v200, v200, v204\n\t"
      "v_mfma_f32_16x16x32_bf16 v[192:195], v[136:139], v[40:43], v[192:195]\n\t"
      "v_add_f32_e32 v201, v201, v205\n\t"
      "v_add_f32_e32 v202, v202, v206\n\t"
      "v_mfma_f32_16x16x32_bf16 v[196:199], v[132:135], v[28:31], v[196:199]\n\t"
      "v_add_f32_e32 v203, v203, v207\n\t"
      "v_add_f32_e32 v112, v112, v120\n\t"
      "v_mfma_f32_16x16x32_bf16 v[196:199], v[136:139], v[44:47], v[196:199]\n\t"
      "v_add_f32_e32 v113, v113, v121\n\t"
      "s_waitcnt vmcnt(4)\n\t"
      "v_mfma_f32_16x16x32_bf16 v[184:187], v[140:143], v[48:51], v[184:187]\n\t"
      "v_add_f32_e32 v114, v114, v122\n\t"
      "s_waitcnt vmcnt(3)\n\t"
      "v_mfma_f32_16x16x32_bf16 v[184:187], v[144:147], v[64:67], v[184:187]\n\t"
      "v_add_f32_e32 v115, v115, v123\n\t"
      "s_waitcnt lgkmcnt(0)\n\t"
      "v_add_f32_e32 v208, v208, v212\n\t"
      "v_mfma_f32_16x16x32_bf16 v[188:191], v[140:143], v[52:55], v[188:191]\n\t"
      "v_add_f32_e32 v209, v209, v213\n\t"
      "v_add_f32_e32 v210, v210, v214\n\t"
      "v_mfma_f32_16x16x32_bf16 v[188:191], v[144:147], v[68:71], v[188:191]\n\t"
      "v_add_f32_e32 v211, v211, v215\n\t"
      "v_add_f32_e32 v200, v200, v208\n\t"
      "v_mfma_f32_16x16x32_bf16 v[192:195], v[140:143], v[56:59], v[192:195]\n\t"
      "v_add_f32_e32 v201, v201, v209\n\t"
      "v_add_f32_e32 v202, v202, v210\n\t"
      "v_mfma_f32_16x16x32_bf16 v[192:195], v[144:147], v[72:75], v[192:195]\n\t"
      "v_add_f32_e32 v203, v203, v211\n\t"
      "v_add_f32_e32 v112, v112, v200\n\t"
      "v_mfma_f32_16x16x32_bf16 v[196:199], v[140:143], v[60:63], v[196:199]\n\t"
      "v_add_f32_e32 v113, v113, v201\n\t"
      "v_add_f32_e32 v114, v114, v202\n\t"
      "v_mfma_f32_16x16x32_bf16 v[196:199], v[144:147], v[76:79], v[196:199]\n\t"
      "v_add_f32_e32 v115, v115, v203\n\t"
      "s_waitcnt vmcnt(1)\n\t"
      "v_mfma_f32_16x16x32_bf16 v[184:187], v[148:151], v[80:83], v[184:187]\n\t"
      "v_cvt_pk_bf16_f32 v116, v112, v113\n\t"
      "s_waitcnt vmcnt(0)\n\t"
      "v_mfma_f32_16x16x32_bf16 v[184:187], v[152:155], v[96:99], v[184:187]\n\t"
      "v_cvt_pk_bf16_f32 v117, v114, v115\n\t"
      "global_store_dwordx2 v245, v[116:117], s[8:9] offset:96\n\t"
      "v_mfma_f32_16x16x32_bf16 v[188:191], v[148:151], v[84:87], v[188:191]\n\t"
      "v_mfma_f32_16x16x32_bf16 v[188:191], v[152:155], v[100:103], v[188:191]\n\t"
      "s_nop 3\n\t"
      "v_cmp_u_f32_e32 vcc, v184, v185\n\t"
      "v_cmp_u_f32_e64 s[48:49], v186, v187\n\t"
      "v_mfma_f32_16x16x32_bf16 v[192:195], v[148:151], v[88:91], v[192:195]\n\t"
      "v_mfma_f32_16x16x32_bf16 v[192:195], v[152:155], v[104:107], v[192:195]\n\t"
      "s_or_b64 vcc, vcc, s[48:49]\n\t"
      "s_cbranch_scc1 NP6R_%=\n\t"
      "NP6B_%=:\n\t"
      "ds_write_b128 v243, v[184:187]\n\t"
      "v_mfma_f32_16x16x32_bf16 v[196:199], v[148:151], v[92:95], v[196:199]\n\t"
      "v_mfma_f32_16x16x32_bf16 v[196:199], v[152:155], v[108:111], v[196:199]\n\t"
      "ds_write_b128 v220, v[188:191]\n\t"
      "s_nop 1\n\t"
      "ds_write_b128 v221, v[192:195]\n\t"
      "s_nop 3\n\t"
      "ds_write_b128 v222, v[196:199]\n\t"
      "s_waitcnt lgkmcnt(0)\n\t"
      "s_barrier\n\t"
      "ds_read_b128 v[112:115], v244\n\t"
      "ds_read_b128 v[116:119], v244 offset:4096\n\t"
      "ds_read_b128 v[120:123], v244 offset:8192\n\t"
      "ds_read_b128 v[124:127], v244 offset:12288\n\t"
      "ds_read_b128 v[184:187], v244 offset:16384\n\t"
      "ds_read_b128 v[188:191], v244 offset:20480\n\t"
      "ds_read_b128 v[192:195], v244 offset:24576\n\t"
      "ds_read_b128 v[196:199], v244 offset:28672\n\t"
      "s_waitcnt lgkmcnt(6)\n\t"
      "v_add_f32_e32 v112, v112, v116\n\t"
      "v_add_f32_e32 v113, v113, v117\n\t"
      "v_add_f32_e32 v114, v114, v118\n\t"
      "v_add_f32_e32 v115, v115, v119\n\t"
      "s_waitcnt lgkmcnt(4)\n\t"
      "v_add_f32_e32 v120, v120, v124\n\t"
      "v_add_f32_e32 v121, v121, v125\n\t"
      "v_add_f32_e32 v122, v122, v126\n\t"
      "v_add_f32_e32 v123, v123, v127\n\t"
      "s_waitcnt lgkmcnt(2)\n\t"
      "v_add_f32_e32 v184, v184, v188\n\t"
      "v_add_f32_e32 v185, v185, v189\n\t"
      "v_add_f32_e32 v186, v186, v190\n\t"
      "v_add_f32_e32 v187, v187, v191\n\t"
      "v_add_f32_e32 v112, v112, v120\n\t"
      "v_add_f32_e32 v113, v113, v121\n\t"
      "v_add_f32_e32 v114, v114, v122\n\t"
      "v_add_f32_e32 v115, v115, v123\n\t"
      "s_waitcnt lgkmcnt(0)\n\t"
      "v_add_f32_e32 v192, v192, v196\n\t"
      "v_add_f32_e32 v193, v193, v197\n\t"
      "v_add_f32_e32 v194, v194, v198\n\t"
      "v_add_f32_e32 v195, v195, v199\n\t"
      "v_add_f32_e32 v184, v184, v192\n\t"
      "v_add_f32_e32 v185, v185, v193\n\t"
      "v_add_f32_e32 v186, v186, v194\n\t"
      "v_add_f32_e32 v187, v187, v195\n\t"
      "v_add_f32_e32 v112, v112, v184\n\t"
      "v_add_f32_e32 v113, v113, v185\n\t"
      "v_add_f32_e32 v114, v114, v186\n\t"
      "v_add_f32_e32 v115, v115, v187\n\t"
      "v_cvt_pk_bf16_f32 v116, v112, v113\n\t"
      "v_cvt_pk_bf16_f32 v117, v114, v115\n\t"
      "global_store_dwordx2 v245, v[116:117], s[66:67] offset:64\n\t"
      "GX_%=:\n\t"
      "s_endpgm\n\t"
      ".p2align 6\n\t"
      "GQ_%=:\n\t"
      "s_waitcnt vmcnt(6)\n\t"
      "v_mfma_f32_16x16x32_bf16 v[184:187], v[128:131], v[0:3], 0\n\t"
      "s_waitcnt vmcnt(5)\n\t"
      "v_cmp_eq_u32_e32 vcc, -1, v132\n\t"
      "v_mfma_f32_16x16x32_bf16 v[188:191], v[128:131], v[4:7], 0\n\t"
      "v_mfma_f32_16x16x32_bf16 v[192:195], v[128:131], v[8:11], 0\n\t"
      "s_cbranch_vccnz FQ1R_%=\n\t"
      "FQ1B_%=:\n\t"
      "v_mfma_f32_16x16x32_bf16 v[196:199], v[128:131], v[12:15], 0\n\t"
      "global_load_dwordx4 v[128:131], v224, s[54:55] sc1\n\t"
      "v_mfma_f32_16x16x32_bf16 v[184:187], v[132:135], v[16:19], v[184:187]\n\t"
      "s_waitcnt vmcnt(5)\n\t"
      "v_cmp_eq_u32_e32 vcc, -1, v136\n\t"
      "v_mfma_f32_16x16x32_bf16 v[188:191], v[132:135], v[20:23], v[188:191]\n\t"
      "v_mfma_f32_16x16x32_bf16 v[192:195], v[132:135], v[24:27], v[192:195]\n\t"
      "s_cbranch_vccnz FQ2R_%=\n\t"
      "FQ2B_%=:\n\t"
      "v_mfma_f32_16x16x32_bf16 v[196:199], v[132:135], v[28:31], v[196:199]\n\t"
      "global_load_dwordx4 v[132:135], v225, s[54:55] sc1\n\t"
      "v_mfma_f32_16x16x32_bf16 v[184:187], v[136:139], v[32:35], v[184:187]\n\t"
      "s_waitcnt vmcnt(5)\n\t"
      "v_cmp_eq_u32_e32 vcc, -1, v140\n\t"
      "v_mfma_f32_16x16x32_bf16 v[188:191], v[136:139], v[36:39], v[188:191]\n\t"
      "v_mfma_f32_16x16x32_bf16 v[192:195], v[136:139], v[40:43], v[192:195]\n\t"
      "s_cbranch_vccnz FQ3R_%=\n\t"
      "FQ3B_%=:\n\t"
      "v_mfma_f32_16x16x32_bf16 v[196:199], v[136:139], v[44:47], v[196:199]\n\t"
      "global_load_dwordx4 v[136:139], v226, s[54:55] sc1\n\t"
      "v_mfma_f32_16x16x32_bf16 v[184:187], v[140:143], v[48:51], v[184:187]\n\t"
      "s_waitcnt vmcnt(5)\n\t"
      "v_cmp_eq_u32_e32 vcc, -1, v144\n\t"
      "v_mfma_f32_16x16x32_bf16 v[188:191], v[140:143], v[52:55], v[188:191]\n\t"
      "v_mfma_f32_16x16x32_bf16 v[192:195], v[140:143], v[56:59], v[192:195]\n\t"
      "s_cbranch_vccnz FQ4R_%=\n\t"
      "FQ4B_%=:\n\t"
      "v_mfma_f32_16x16x32_bf16 v[196:199], v[140:143], v[60:63], v[196:199]\n\t"
      "global_load_dwordx4 v[140:143], v227, s[54:55] sc1\n\t"
      "v_mfma_f32_16x16x32_bf16 v[184:187], v[144:147], v[64:67], v[184:187]\n\t"
      "s_waitcnt vmcnt(5)\n\t"
      "v_cmp_eq_u32_e32 vcc, -1, v148\n\t"
      "v_mfma_f32_16x16x32_bf16 v[188:191], v[144:147], v[68:71], v[188:191]\n\t"
      "v_mfma_f32_16x16x32_bf16 v[192:195], v[144:147], v[72:75], v[192:195]\n\t"
      "s_cbranch_vccnz FQ5R_%=\n\t"
      "FQ5B_%=:\n\t"
      "v_mfma_f32_16x16x32_bf16 v[196:199], v[144:147], v[76:79], v[196:199]\n\t"
      "global_load_dwordx4 v[144:147], v228, s[54:55] sc1\n\t"
      "v_mfma_f32_16x16x32_bf16 v[184:187], v[148:151], v[80:83], v[184:187]\n\t"
      "s_waitcnt vmcnt(5)\n\t"
      "v_cmp_eq_u32_e32 vcc, -1, v152\n\t"
      "v_mfma_f32_16x16x32_bf16 v[188:191], v[148:151], v[84:87], v[188:191]\n\t"
      "v_mfma_f32_16x16x32_bf16 v[192:195], v[148:151], v[88:91], v[192:195]\n\t"
      "s_cbranch_vccnz FQ6R_%=\n\t"
      "FQ6B_%=:\n\t"
      "v_mfma_f32_16x16x32_bf16 v[196:199], v[148:151], v[92:95], v[196:199]\n\t"
      "global_load_dwordx4 v[148:151], v229, s[54:55] sc1\n\t"
      "v_mfma_f32_16x16x32_bf16 v[184:187], v[152:155], v[96:99], v[184:187]\n\t"
      "s_waitcnt vmcnt(5)\n\t"
      "v_cmp_eq_u32_e32 vcc, -1, v128\n\t"
      "v_mfma_f32_16x16x32_bf16 v[188:191], v[152:155], v[100:103], v[188:191]\n\t"
      "v_mfma_f32_16x16x32_bf16 v[192:195], v[152:155], v[104:107], v[192:195]\n\t"
      "s_cbranch_vccnz FQ7R_%=\n\t"
      "FQ7B_%=:\n\t"
      "v_mfma_f32_16x16x32_bf16 v[196:199], v[152:155], v[108:111], v[196:199]\n\t"
      "global_load_dwordx4 v[152:155], v230, s[54:55] sc1\n\t"
      "s_nop 0\n\t"
      "v_cmp_u_f32_e32 vcc, v184, v185\n\t"
      "v_cmp_u_f32_e64 s[48:49], v186, v187\n\t"
      "s_nop 0\n\t"
      "s_or_b64 vcc, vcc, s[48:49]\n\t"
      "s_cbranch_scc1 NQ0R_%=\n\t"
      "NQ0B_%=:\n\t"
      "ds_write_b128 v243, v[184:187]\n\t"
      "ds_write_b128 v220, v[188:191]\n\t"
      "ds_write_b128 v221, v[192:195]\n\t"
      "ds_write_b128 v222, v[196:199]\n\t"
      "v_mfma_f32_16x16x32_bf16 v[200:203], v[128:131], v[0:3], 0\n\t"
      "s_waitcnt vmcnt(5)\n\t"
      "v_cmp_eq_u32_e32 vcc, -1, v132\n\t"
      "v_mfma_f32_16x16x32_bf16 v[204:207], v[128:131], v[4:7], 0\n\t"
      "v_mfma_f32_16x16x32_bf16 v[208:211], v[128:131], v[8:11], 0\n\t"
      "s_cbranch_vccnz FQ8R_%=\n\t"
      "FQ8B_%=:\n\t"
      "v_mfma_f32_16x16x32_bf16 v[212:215], v[128:131], v[12:15], 0\n\t"
      "global_load_dwordx4 v[128:131], v224, s[56:57] sc1\n\t"
      "v_mfma_f32_16x16x32_bf16 v[200:203], v[132:135], v[16:19], v[200:203]\n\t"
      "s_waitcnt vmcnt(5)\n\t"
      "v_cmp_eq_u32_e32 vcc, -1, v136\n\t"
      "v_mfma_f32_16x16x32_bf16 v[204:207], v[132:135], v[20:23], v[204:207]\n\t"
      "v_mfma_f32_16x16x32_bf16 v[208:211], v[132:135], v[24:27], v[208:211]\n\t"
      "s_cbranch_vccnz FQ9R_%=\n\t"
      "FQ9B_%=:\n\t"
      "v_mfma_f32_16x16x32_bf16 v[212:215], v[132:135], v[28:31], v[212:215]\n\t"
      "global_load_dwordx4 v[132:135], v225, s[56:57] sc1\n\t"
      "v_mfma_f32_16x16x32_bf16 v[200:203], v[136:139], v[32:35], v[200:203]\n\t"
      "s_waitcnt vmcnt(5)\n\t"
      "v_cmp_eq_u32_e32 vcc, -1, v140\n\t"
      "v_mfma_f32_16x16x32_bf16 v[204:207], v[136:139], v[36:39], v[204:207]\n\t"
      "v_mfma_f32_16x16x32_bf16 v[208:211], v[136:139], v[40:43], v[208:211]\n\t"
      "s_cbranch_vccnz FQ10R_%=\n\t"
      "FQ10B_%=:\n\t"
      "v_mfma_f32_16x16x32_bf16 v[212:215], v[136:139], v[44:47], v[212:215]\n\t"
      "global_load_dwordx4 v[136:139], v226, s[56:57] sc1\n\t"
      "v_mfma_f32_16x16x32_bf16 v[200:203], v[140:143], v[48:51], v[200:203]\n\t"
      "s_waitcnt vmcnt(5)\n\t"
      "v_cmp_eq_u32_e32 vcc, -1, v144\n\t"
      "v_mfma_f32_16x16x32_bf16 v[204:207], v[140:143], v[52:55], v[204:207]\n\t"
      "v_mfma_f32_16x16x32_bf16 v[208:211], v[140:143], v[56:59], v[208:211]\n\t"
      "s_cbranch_vccnz FQ11R_%=\n\t"
      "FQ11B_%=:\n\t"
      "v_mfma_f32_16x16x32_bf16 v[212:215], v[140:143], v[60:63], v[212:215]\n\t"
      "global_load_dwordx4 v[140:143], v227, s[56:57] sc1\n\t"
      "v_mfma_f32_16x16x32_bf16 v[200:203], v[144:147], v[64:67], v[200:203]\n\t"
      "s_waitcnt vmcnt(5)\n\t"
      "v_cmp_eq_u32_e32 vcc, -1, v148\n\t"
      "v_mfma_f32_16x16x32_bf16 v[204:207], v[144:147], v[68:71], v[204:207]\n\t"
      "v_mfma_f32_16x16x32_bf16 v[208:211], v[144:147], v[72:75], v[208:211]\n\t"
      "s_cbranch_vccnz FQ12R_%=\n\t"
      "FQ12B_%=:\n\t"
      "v_mfma_f32_16x16x32_bf16 v[212:215], v[144:147], v[76:79], v[212:215]\n\t"
      "global_load_dwordx4 v[144:147], v228, s[56:57] sc1\n\t"
      "s_waitcnt lgkmcnt(0)\n\t"
      "s_barrier\n\t"
      "s_setprio 2\n\t"
      "v_mfma_f32_16x16x32_bf16 v[200:203], v[148:151], v[80:83], v[200:203]\n\t"
      "s_waitcnt vmcnt(5)\n\t"
      "v_cmp_eq_u32_e32 vcc, -1, v152\n\t"
      "v_mfma_f32_16x16x32_bf16 v[204:207], v[148:151], v[84:87], v[204:207]\n\t"
      "v_mfma_f32_16x16x32_bf16 v[208:211], v[148:151], v[88:91], v[208:211]\n\t"
      "s_cbranch_vccnz FQ13R_%=\n\t"
      "FQ13B_%=:\n\t"
      "v_mfma_f32_16x16x32_bf16 v[212:215], v[148:151], v[92:95], v[212:215]\n\t"
      "global_load_dwordx4 v[148:151], v229, s[56:57] sc1\n\t"
      "v_mfma_f32_16x16x32_bf16 v[200:203], v[152:155], v[96:99], v[200:203]\n\t"
      "s_waitcnt vmcnt(5)\n\t"
      "v_cmp_eq_u32_e32 vcc, -1, v128\n\t"
      "v_mfma_f32_16x16x32_bf16 v[204:207], v[152:155], v[100:103], v[204:207]\n\t"
      "v_mfma_f32_16x16x32_bf16 v[208:211], v[152:155], v[104:107], v[208:211]\n\t"
      "s_cbranch_vccnz FQ14R_%=\n\t"
      "FQ14B_%=:\n\t"
      "v_mfma_f32_16x16x32_bf16 v[212:215], v[152:155], v[108:111], v[212:215]\n\t"
      "global_load_dwordx4 v[152:155], v230, s[56:57] sc1\n\t"
      "s_setprio 0\n\t"
      "v_cmp_u_f32_e32 vcc, v200, v201\n\t"
      "v_cmp_u_f32_e64 s[48:49], v202, v203\n\t"
      "s_nop 0\n\t"
      "s_or_b64 vcc, vcc, s[48:49]\n\t"
      "s_cbranch_scc1 NQ1R_%=\n\t"
      "NQ1B_%=:\n\t"
      "ds_write_b128 v243, v[200:203] offset:32768\n\t"
      "ds_write_b128 v220, v[204:207] offset:32768\n\t"
      "ds_write_b128 v221, v[208:211] offset:32768\n\t"
      "ds_write_b128 v222, v[212:215] offset:32768\n\t"
      "v_mfma_f32_16x16x32_bf16 v[184:187], v[128:131], v[0:3], 0\n\t"
      "s_waitcnt vmcnt(5)\n\t"
      "v_cmp_eq_u32_e32 vcc, -1, v132\n\t"
      "v_mfma_f32_16x16x32_bf16 v[188:191], v[128:131], v[4:7], 0\n\t"
      "v_mfma_f32_16x16x32_bf16 v[192:195], v[128:131], v[8:11], 0\n\t"
      "s_cbranch_vccnz FQ15R_%=\n\t"
      "FQ15B_%=:\n\t"
      "v_mfma_f32_16x16x32_bf16 v[196:199], v[128:131], v[12:15], 0\n\t"
      "global_load_dwordx4 v[128:131], v224, s[58:59] sc1\n\t"
      "v_mfma_f32_16x16x32_bf16 v[184:187], v[132:135], v[16:19], v[184:187]\n\t"
      "s_waitcnt vmcnt(5)\n\t"
      "v_cmp_eq_u32_e32 vcc, -1, v136\n\t"
      "v_mfma_f32_16x16x32_bf16 v[188:191], v[132:135], v[20:23], v[188:191]\n\t"
      "v_mfma_f32_16x16x32_bf16 v[192:195], v[132:135], v[24:27], v[192:195]\n\t"
      "s_cbranch_vccnz FQ16R_%=\n\t"
      "FQ16B_%=:\n\t"
      "v_mfma_f32_16x16x32_bf16 v[196:199], v[132:135], v[28:31], v[196:199]\n\t"
      "global_load_dwordx4 v[132:135], v225, s[58:59] sc1\n\t"
      "v_mfma_f32_16x16x32_bf16 v[184:187], v[136:139], v[32:35], v[184:187]\n\t"
      "s_waitcnt vmcnt(5)\n\t"
      "v_cmp_eq_u32_e32 vcc, -1, v140\n\t"
      "v_mfma_f32_16x16x32_bf16 v[188:191], v[136:139], v[36:39], v[188:191]\n\t"
      "v_mfma_f32_16x16x32_bf16 v[192:195], v[136:139], v[40:43], v[192:195]\n\t"
      "s_cbranch_vccnz FQ17R_%=\n\t"
      "FQ17B_%=:\n\t"
      "v_mfma_f32_16x16x32_bf16 v[196:199], v[136:139], v[44:47], v[196:199]\n\t"
      "global_load_dwordx4 v[136:139], v226, s[58:59] sc1\n\t"
      "v_mfma_f32_16x16x32_bf16 v[184:187], v[140:143], v[48:51], v[184:187]\n\t"
      "s_waitcnt vmcnt(5)\n\t"
      "v_cmp_eq_u32_e32 vcc, -1, v144\n\t"
      "v_mfma_f32_16x16x32_bf16 v[188:191], v[140:143], v[52:55], v[188:191]\n\t"
      "v_mfma_f32_16x16x32_bf16 v[192:195], v[140:143], v[56:59], v[192:195]\n\t"
      "s_cbranch_vccnz FQ18R_%=\n\t"
      "FQ18B_%=:\n\t"
      "v_mfma_f32_16x16x32_bf16 v[196:199], v[140:143], v[60:63], v[196:199]\n\t"
      "global_load_dwordx4 v[140:143], v227, s[58:59] sc1\n\t"
      "v_mfma_f32_16x16x32_bf16 v[184:187], v[144:147], v[64:67], v[184:187]\n\t"
      "s_waitcnt vmcnt(5)\n\t"
      "v_cmp_eq_u32_e32 vcc, -1, v148\n\t"
      "v_mfma_f32_16x16x32_bf16 v[188:191], v[144:147], v[68:71], v[188:191]\n\t"
      "v_mfma_f32_16x16x32_bf16 v[192:195], v[144:147], v[72:75], v[192:195]\n\t"
      "s_cbranch_vccnz FQ19R_%=\n\t"
      "FQ19B_%=:\n\t"
      "v_mfma_f32_16x16x32_bf16 v[196:199], v[144:147], v[76:79], v[196:199]\n\t"
      "global_load_dwordx4 v[144:147], v228, s[58:59] sc1\n\t"
      "s_waitcnt lgkmcnt(0)\n\t"
      "s_barrier\n\t"
      "s_setprio 2\n\t"
      "v_mfma_f32_16x16x32_bf16 v[184:187], v[148:151], v[80:83], v[184:187]\n\t"
      "s_waitcnt vmcnt(5)\n\t"
      "v_cmp_eq_u32_e32 vcc, -1, v152\n\t"
      "v_mfma_f32_16x16x32_bf16 v[188:191], v[148:151], v[84:87], v[188:191]\n\t"
      "v_mfma_f32_16x16x32_bf16 v[192:195], v[148:151], v[88:91], v[192:195]\n\t"
      "s_cbranch_vccnz FQ20R_%=\n\t"
      "FQ20B_%=:\n\t"
      "v_mfma_f32_16x16x32_bf16 v[196:199], v[148:151], v[92:95], v[196:199]\n\t"
      "global_load_dwordx4 v[148:151], v229, s[58:59] sc1\n\t"
      "v_mfma_f32_16x16x32_bf16 v[184:187], v[152:155], v[96:99], v[184:187]\n\t"
      "s_waitcnt vmcnt(5)\n\t"
      "v_cmp_eq_u32_e32 vcc, -1, v128\n\t"
      "v_mfma_f32_16x16x32_bf16 v[188:191], v[152:155], v[100:103], v[188:191]\n\t"
      "v_mfma_f32_16x16x32_bf16 v[192:195], v[152:155], v[104:107], v[192:195]\n\t"
      "s_cbranch_vccnz FQ21R_%=\n\t"
      "FQ21B_%=:\n\t"
      "v_mfma_f32_16x16x32_bf16 v[196:199], v[152:155], v[108:111], v[196:199]\n\t"
      "global_load_dwordx4 v[152:155], v230, s[58:59] sc1\n\t"
      "s_setprio 0\n\t"
      "v_cmp_u_f32_e32 vcc, v184, v185\n\t"
      "v_cmp_u_f32_e64 s[48:49], v186, v187\n\t"
      "s_nop 0\n\t"
      "s_or_b64 vcc, vcc, s[48:49]\n\t"
      "s_cbranch_scc1 NQ2R_%=\n\t"
      "NQ2B_%=:\n\t"
      "ds_write_b128 v243, v[184:187]\n\t"
      "ds_write_b128 v220, v[188:191]\n\t"
      "ds_write_b128 v221, v[192:195]\n\t"
      "ds_write_b128 v222, v[196:199]\n\t"
      "v_mfma_f32_16x16x32_bf16 v[200:203], v[128:131], v[0:3], 0\n\t"
      "s_waitcnt vmcnt(5)\n\t"
      "v_cmp_eq_u32_e32 vcc, -1, v132\n\t"
      "v_mfma_f32_16x16x32_bf16 v[204:207], v[128:131], v[4:7], 0\n\t"
      "v_mfma_f32_16x16x32_bf16 v[208:211], v[128:131], v[8:11], 0\n\t"
      "s_cbranch_vccnz FQ22R_%=\n\t"
      "FQ22B_%=:\n\t"
      "v_mfma_f32_16x16x32_bf16 v[212:215], v[128:131], v[12:15], 0\n\t"
      "global_load_dwordx4 v[128:131], v224, s[60:61] sc1\n\t"
      "v_mfma_f32_16x16x32_bf16 v[200:203], v[132:135], v[16:19], v[200:203]\n\t"
      "s_waitcnt vmcnt(5)\n\t"
      "v_cmp_eq_u32_e32 vcc, -1, v136\n\t"
      "v_mfma_f32_16x16x32_bf16 v[204:207], v[132:135], v[20:23], v[204:207]\n\t"
      "v_mfma_f32_16x16x32_bf16 v[208:211], v[132:135], v[24:27], v[208:211]\n\t"
      "s_cbranch_vccnz FQ23R_%=\n\t"
      "FQ23B_%=:\n\t"
      "v_mfma_f32_16x16x32_bf16 v[212:215], v[132:135], v[28:31], v[212:215]\n\t"
      "global_load_dwordx4 v[132:135], v225, s[60:61] sc1\n\t"
      "v_mfma_f32_16x16x32_bf16 v[200:203], v[136:139], v[32:35], v[200:203]\n\t"
      "s_waitcnt vmcnt(5)\n\t"
      "v_cmp_eq_u32_e32 vcc, -1, v140\n\t"
      "v_mfma_f32_16x16x32_bf16 v[204:207], v[136:139], v[36:39], v[204:207]\n\t"
      "v_mfma_f32_16x16x32_bf16 v[208:211], v[136:139], v[40:43], v[208:211]\n\t"
      "s_cbranch_vccnz FQ24R_%=\n\t"
      "FQ24B_%=:\n\t"
      "v_mfma_f32_16x16x32_bf16 v[212:215], v[136:139], v[44:47], v[212:215]\n\t"
      "global_load_dwordx4 v[136:139], v226, s[60:61] sc1\n\t"
      "v_mfma_f32_16x16x32_bf16 v[200:203], v[140:143], v[48:51], v[200:203]\n\t"
      "s_waitcnt vmcnt(5)\n\t"
      "v_cmp_eq_u32_e32 vcc, -1, v144\n\t"
      "v_mfma_f32_16x16x32_bf16 v[204:207], v[140:143], v[52:55], v[204:207]\n\t"
      "v_mfma_f32_16x16x32_bf16 v[208:211], v[140:143], v[56:59], v[208:211]\n\t"
      "s_cbranch_vccnz FQ25R_%=\n\t"
      "FQ25B_%=:\n\t"
      "v_mfma_f32_16x16x32_bf16 v[212:215], v[140:143], v[60:63], v[212:215]\n\t"
      "global_load_dwordx4 v[140:143], v227, s[60:61] sc1\n\t"
      "v_mfma_f32_16x16x32_bf16 v[200:203], v[144:147], v[64:67], v[200:203]\n\t"
      "s_waitcnt vmcnt(5)\n\t"
      "v_cmp_eq_u32_e32 vcc, -1, v148\n\t"
      "v_mfma_f32_16x16x32_bf16 v[204:207], v[144:147], v[68:71], v[204:207]\n\t"
      "v_mfma_f32_16x16x32_bf16 v[208:211], v[144:147], v[72:75], v[208:211]\n\t"
      "s_cbranch_vccnz FQ26R_%=\n\t"
      "FQ26B_%=:\n\t"
      "v_mfma_f32_16x16x32_bf16 v[212:215], v[144:147], v[76:79], v[212:215]\n\t"
      "global_load_dwordx4 v[144:147], v228, s[60:61] sc1\n\t"
      "s_waitcnt lgkmcnt(0)\n\t"
      "s_barrier\n\t"
      "s_setprio 2\n\t"
      "v_mfma_f32_16x16x32_bf16 v[200:203], v[148:151], v[80:83], v[200:203]\n\t"
      "s_waitcnt vmcnt(5)\n\t"
      "v_cmp_eq_u32_e32 vcc, -1, v152\n\t"
      "v_mfma_f32_16x16x32_bf16 v[204:207], v[148:151], v[84:87], v[204:207]\n\t"
      "v_mfma_f32_16x16x32_bf16 v[208:211], v[148:151], v[88:91], v[208:211]\n\t"
      "s_cbranch_vccnz FQ27R_%=\n\t"
      "FQ27B_%=:\n\t"
      "v_mfma_f32_16x16x32_bf16 v[212:215], v[148:151], v[92:95], v[212:215]\n\t"
      "global_load_dwordx4 v[148:151], v229, s[60:61] sc1\n\t"
      "v_mfma_f32_16x16x32_bf16 v[200:203], v[152:155], v[96:99], v[200:203]\n\t"
      "s_waitcnt vmcnt(5)\n\t"
      "v_cmp_eq_u32_e32 vcc, -1, v128\n\t"
      "v_mfma_f32_16x16x32_bf16 v[204:207], v[152:155], v[100:103], v[204:207]\n\t"
      "v_mfma_f32_16x16x32_bf16 v[208:211], v[152:155], v[104:107], v[208:211]\n\t"
      "s_cbranch_vccnz FQ28R_%=\n\t"
      "FQ28B_%=:\n\t"
      "v_mfma_f32_16x16x32_bf16 v[212:215], v[152:155], v[108:111], v[212:215]\n\t"
      "global_load_dwordx4 v[152:155], v230, s[60:61] sc1\n\t"
      "s_setprio 0\n\t"
      "v_cmp_u_f32_e32 vcc, v200, v201\n\t"
      "v_cmp_u_f32_e64 s[48:49], v202, v203\n\t"
      "s_nop 0\n\t"
      "s_or_b64 vcc, vcc, s[48:49]\n\t"
      "s_cbranch_scc1 NQ3R_%=\n\t"
      "NQ3B_%=:\n\t"
      "ds_write_b128 v243, v[200:203] offset:32768\n\t"
      "ds_write_b128 v220, v[204:207] offset:32768\n\t"
      "ds_write_b128 v221, v[208:211] offset:32768\n\t"
      "ds_write_b128 v222, v[212:215] offset:32768\n\t"
      "v_mfma_f32_16x16x32_bf16 v[184:187], v[128:131], v[0:3], 0\n\t"
      "s_waitcnt vmcnt(5)\n\t"
      "v_cmp_eq_u32_e32 vcc, -1, v132\n\t"
      "v_mfma_f32_16x16x32_bf16 v[188:191], v[128:131], v[4:7], 0\n\t"
      "v_mfma_f32_16x16x32_bf16 v[192:195], v[128:131], v[8:11], 0\n\t"
      "s_cbranch_vccnz FQ29R_%=\n\t"
      "FQ29B_%=:\n\t"
      "v_mfma_f32_16x16x32_bf16 v[196:199], v[128:131], v[12:15], 0\n\t"
      "global_load_dwordx4 v[128:131], v224, s[62:63] sc1\n\t"
      "v_mfma_f32_16x16x32_bf16 v[184:187], v[132:135], v[16:19], v[184:187]\n\t"
      "s_waitcnt vmcnt(5)\n\t"
      "v_cmp_eq_u32_e32 vcc, -1, v136\n\t"
      "v_mfma_f32_16x16x32_bf16 v[188:191], v[132:135], v[20:23], v[188:191]\n\t"
      "v_mfma_f32_16x16x32_bf16 v[192:195], v[132:135], v[24:27], v[192:195]\n\t"
      "s_cbranch_vccnz FQ30R_%=\n\t"
      "FQ30B_%=:\n\t"
      "v_mfma_f32_16x16x32_bf16 v[196:199], v[132:135], v[28:31], v[196:199]\n\t"
      "global_load_dwordx4 v[132:135], v225, s[62:63] sc1\n\t"
      "v_mfma_f32_16x16x32_bf16 v[184:187], v[136:139], v[32:35], v[184:187]\n\t"
      "s_waitcnt vmcnt(5)\n\t"
      "v_cmp_eq_u32_e32 vcc, -1, v140\n\t"
      "v_mfma_f32_16x16x32_bf16 v[188:191], v[136:139], v[36:39], v[188:191]\n\t"
      "v_mfma_f32_16x16x32_bf16 v[192:195], v[136:139], v[40:43], v[192:195]\n\t"
      "s_cbranch_vccnz FQ31R_%=\n\t"
      "FQ31B_%=:\n\t"
      "v_mfma_f32_16x16x32_bf16 v[196:199], v[136:139], v[44:47], v[196:199]\n\t"
      "global_load_dwordx4 v[136:139], v226, s[62:63] sc1\n\t"
      "v_mfma_f32_16x16x32_bf16 v[184:187], v[140:143], v[48:51], v[184:187]\n\t"
      "s_waitcnt vmcnt(5)\n\t"
      "v_cmp_eq_u32_e32 vcc, -1, v144\n\t"
      "v_mfma_f32_16x16x32_bf16 v[188:191], v[140:143], v[52:55], v[188:191]\n\t"
      "v_mfma_f32_16x16x32_bf16 v[192:195], v[140:143], v[56:59], v[192:195]\n\t"
      "s_cbranch_vccnz FQ32R_%=\n\t"
      "FQ32B_%=:\n\t"
      "v_mfma_f32_16x16x32_bf16 v[196:199], v[140:143], v[60:63], v[196:199]\n\t"
      "global_load_dwordx4 v[140:143], v227, s[62:63] sc1\n\t"
      "v_mfma_f32_16x16x32_bf16 v[184:187], v[144:147], v[64:67], v[184:187]\n\t"
      "s_waitcnt vmcnt(5)\n\t"
      "v_cmp_eq_u32_e32 vcc, -1, v148\n\t"
      "v_mfma_f32_16x16x32_bf16 v[188:191], v[144:147], v[68:71], v[188:191]\n\t"
      "v_mfma_f32_16x16x32_bf16 v[192:195], v[144:147], v[72:75], v[192:195]\n\t"
      "s_cbranch_vccnz FQ33R_%=\n\t"
      "FQ33B_%=:\n\t"
      "v_mfma_f32_16x16x32_bf16 v[196:199], v[144:147], v[76:79], v[196:199]\n\t"
      "global_load_dwordx4 v[144:147], v228, s[62:63] sc1\n\t"
      "s_waitcnt lgkmcnt(0)\n\t"
      "s_barrier\n\t"
      "s_setprio 2\n\t"
      "v_mfma_f32_16x16x32_bf16 v[184:187], v[148:151], v[80:83], v[184:187]\n\t"
      "s_waitcnt vmcnt(5)\n\t"
      "v_cmp_eq_u32_e32 vcc, -1, v152\n\t"
      "v_mfma_f32_16x16x32_bf16 v[188:191], v[148:151], v[84:87], v[188:191]\n\t"
      "v_mfma_f32_16x16x32_bf16 v[192:195], v[148:151], v[88:91], v[192:195]\n\t"
      "s_cbranch_vccnz FQ34R_%=\n\t"
      "FQ34B_%=:\n\t"
      "v_mfma_f32_16x16x32_bf16 v[196:199], v[148:151], v[92:95], v[196:199]\n\t"
      "global_load_dwordx4 v[148:151], v229, s[62:63] sc1\n\t"
      "v_mfma_f32_16x16x32_bf16 v[184:187], v[152:155], v[96:99], v[184:187]\n\t"
      "s_waitcnt vmcnt(5)\n\t"
      "v_cmp_eq_u32_e32 vcc, -1, v128\n\t"
      "v_mfma_f32_16x16x32_bf16 v[188:191], v[152:155], v[100:103], v[188:191]\n\t"
      "v_mfma_f32_16x16x32_bf16 v[192:195], v[152:155], v[104:107], v[192:195]\n\t"
      "s_cbranch_vccnz FQ35R_%=\n\t"
      "FQ35B_%=:\n\t"
      "v_mfma_f32_16x16x32_bf16 v[196:199], v[152:155], v[108:111], v[196:199]\n\t"
      "global_load_dwordx4 v[152:155], v230, s[62:63] sc1\n\t"
      "s_setprio 0\n\t"
      "v_cmp_u_f32_e32 vcc, v184, v185\n\t"
      "v_cmp_u_f32_e64 s[48:49], v186, v187\n\t"
      "s_nop 0\n\t"
      "s_or_b64 vcc, vcc, s[48:49]\n\t"
      "s_cbranch_scc1 NQ4R_%=\n\t"
      "NQ4B_%=:\n\t"
      "ds_write_b128 v243, v[184:187]\n\t"
      "ds_write_b128 v220, v[188:191]\n\t"
      "ds_write_b128 v221, v[192:195]\n\t"
      "ds_write_b128 v222, v[196:199]\n\t"
      "v_mfma_f32_16x16x32_bf16 v[200:203], v[128:131], v[0:3], 0\n\t"
      "s_waitcnt vmcnt(5)\n\t"
      "v_cmp_eq_u32_e32 vcc, -1, v132\n\t"
      "v_mfma_f32_16x16x32_bf16 v[204:207], v[128:131], v[4:7], 0\n\t"
      "v_mfma_f32_16x16x32_bf16 v[208:211], v[128:131], v[8:11], 0\n\t"
      "s_cbranch_vccnz FQ36R_%=\n\t"
      "FQ36B_%=:\n\t"
      "v_mfma_f32_16x16x32_bf16 v[212:215], v[128:131], v[12:15], 0\n\t"
      "global_load_dwordx4 v[128:131], v224, s[64:65] sc1\n\t"
      "v_mfma_f32_16x16x32_bf16 v[200:203], v[132:135], v[16:19], v[200:203]\n\t"
      "s_waitcnt vmcnt(5)\n\t"
      "v_cmp_eq_u32_e32 vcc, -1, v136\n\t"
      "v_mfma_f32_16x16x32_bf16 v[204:207], v[132:135], v[20:23], v[204:207]\n\t"
      "v_mfma_f32_16x16x32_bf16 v[208:211], v[132:135], v[24:27], v[208:211]\n\t"
      "s_cbranch_vccnz FQ37R_%=\n\t"
      "FQ37B_%=:\n\t"
      "v_mfma_f32_16x16x32_bf16 v[212:215], v[132:135], v[28:31], v[212:215]\n\t"
      "global_load_dwordx4 v[132:135], v225, s[64:65] sc1\n\t"
      "v_mfma_f32_16x16x32_bf16 v[200:203], v[136:139], v[32:35], v[200:203]\n\t"
      "s_waitcnt vmcnt(5)\n\t"
      "v_cmp_eq_u32_e32 vcc, -1, v140\n\t"
      "v_mfma_f32_16x16x32_bf16 v[204:207], v[136:139], v[36:39], v[204:207]\n\t"
      "v_mfma_f32_16x16x32_bf16 v[208:211], v[136:139], v[40:43], v[208:211]\n\t"
      "s_cbranch_vccnz FQ38R_%=\n\t"
      "FQ38B_%=:\n\t"
      "v_mfma_f32_16x16x32_bf16 v[212:215], v[136:139], v[44:47], v[212:215]\n\t"
      "global_load_dwordx4 v[136:139], v226, s[64:65] sc1\n\t"
      "v_mfma_f32_16x16x32_bf16 v[200:203], v[140:143], v[48:51], v[200:203]\n\t"
      "s_waitcnt vmcnt(5)\n\t"
      "v_cmp_eq_u32_e32 vcc, -1, v144\n\t"
      "v_mfma_f32_16x16x32_bf16 v[204:207], v[140:143], v[52:55], v[204:207]\n\t"
      "v_mfma_f32_16x16x32_bf16 v[208:211], v[140:143], v[56:59], v[208:211]\n\t"
      "s_cbranch_vccnz FQ39R_%=\n\t"
      "FQ39B_%=:\n\t"
      "v_mfma_f32_16x16x32_bf16 v[212:215], v[140:143], v[60:63], v[212:215]\n\t"
      "global_load_dwordx4 v[140:143], v227, s[64:65] sc1\n\t"
      "v_mfma_f32_16x16x32_bf16 v[200:203], v[144:147], v[64:67], v[200:203]\n\t"
      "s_waitcnt vmcnt(5)\n\t"
      "v_cmp_eq_u32_e32 vcc, -1, v148\n\t"
      "v_mfma_f32_16x16x32_bf16 v[204:207], v[144:147], v[68:71], v[204:207]\n\t"
      "v_mfma_f32_16x16x32_bf16 v[208:211], v[144:147], v[72:75], v[208:211]\n\t"
      "s_cbranch_vccnz FQ40R_%=\n\t"
      "FQ40B_%=:\n\t"
      "v_mfma_f32_16x16x32_bf16 v[212:215], v[144:147], v[76:79], v[212:215]\n\t"
      "global_load_dwordx4 v[144:147], v228, s[64:65] sc1\n\t"
      "s_waitcnt lgkmcnt(0)\n\t"
      "s_barrier\n\t"
      "s_setprio 2\n\t"
      "v_mfma_f32_16x16x32_bf16 v[200:203], v[148:151], v[80:83], v[200:203]\n\t"
      "s_waitcnt vmcnt(5)\n\t"
      "v_cmp_eq_u32_e32 vcc, -1, v152\n\t"
      "v_mfma_f32_16x16x32_bf16 v[204:207], v[148:151], v[84:87], v[204:207]\n\t"
      "v_mfma_f32_16x16x32_bf16 v[208:211], v[148:151], v[88:91], v[208:211]\n\t"
      "s_cbranch_vccnz FQ41R_%=\n\t"
      "FQ41B_%=:\n\t"
      "v_mfma_f32_16x16x32_bf16 v[212:215], v[148:151], v[92:95], v[212:215]\n\t"
      "global_load_dwordx4 v[148:151], v229, s[64:65] sc1\n\t"
      "v_mfma_f32_16x16x32_bf16 v[200:203], v[152:155], v[96:99], v[200:203]\n\t"
      "s_waitcnt vmcnt(5)\n\t"
      "v_cmp_eq_u32_e32 vcc, -1, v128\n\t"
      "v_mfma_f32_16x16x32_bf16 v[204:207], v[152:155], v[100:103], v[204:207]\n\t"
      "v_mfma_f32_16x16x32_bf16 v[208:211], v[152:155], v[104:107], v[208:211]\n\t"
      "s_cbranch_vccnz FQ42R_%=\n\t"
      "FQ42B_%=:\n\t"
      "v_mfma_f32_16x16x32_bf16 v[212:215], v[152:155], v[108:111], v[212:215]\n\t"
      "global_load_dwordx4 v[152:155], v230, s[64:65] sc1\n\t"
      "s_setprio 0\n\t"
      "v_cmp_u_f32_e32 vcc, v200, v201\n\t"
      "v_cmp_u_f32_e64 s[48:49], v202, v203\n\t"
      "s_nop 0\n\t"
      "s_or_b64 vcc, vcc, s[48:49]\n\t"
      "s_cbranch_scc1 NQ5R_%=\n\t"
      "NQ5B_%=:\n\t"
      "ds_write_b128 v243, v[200:203] offset:32768\n\t"
      "ds_write_b128 v220, v[204:207] offset:32768\n\t"
      "ds_write_b128 v221, v[208:211] offset:32768\n\t"
      "ds_write_b128 v222, v[212:215] offset:32768\n\t"
      "v_mfma_f32_16x16x32_bf16 v[184:187], v[128:131], v[0:3], 0\n\t"
      "s_waitcnt vmcnt(5)\n\t"
      "v_cmp_eq_u32_e32 vcc, -1, v132\n\t"
      "v_mfma_f32_16x16x32_bf16 v[188:191], v[128:131], v[4:7], 0\n\t"
      "v_mfma_f32_16x16x32_bf16 v[192:195], v[128:131], v[8:11], 0\n\t"
      "s_cbranch_vccnz FQ43R_%=\n\t"
      "FQ43B_%=:\n\t"
      "v_mfma_f32_16x16x32_bf16 v[196:199], v[128:131], v[12:15], 0\n\t"
      "v_mfma_f32_16x16x32_bf16 v[184:187], v[132:135], v[16:19], v[184:187]\n\t"
      "s_waitcnt vmcnt(4)\n\t"
      "v_cmp_eq_u32_e32 vcc, -1, v136\n\t"
      "v_mfma_f32_16x16x32_bf16 v[188:191], v[132:135], v[20:23], v[188:191]\n\t"
      "v_mfma_f32_16x16x32_bf16 v[192:195], v[132:135], v[24:27], v[192:195]\n\t"
      "s_cbranch_vccnz FQ44R_%=\n\t"
      "FQ44B_%=:\n\t"
      "v_mfma_f32_16x16x32_bf16 v[196:199], v[132:135], v[28:31], v[196:199]\n\t"
      "v_mfma_f32_16x16x32_bf16 v[184:187], v[136:139], v[32:35], v[184:187]\n\t"
      "s_waitcnt vmcnt(3)\n\t"
      "v_cmp_eq_u32_e32 vcc, -1, v140\n\t"
      "v_mfma_f32_16x16x32_bf16 v[188:191], v[136:139], v[36:39], v[188:191]\n\t"
      "v_mfma_f32_16x16x32_bf16 v[192:195], v[136:139], v[40:43], v[192:195]\n\t"
      "s_cbranch_vccnz FQ45R_%=\n\t"
      "FQ45B_%=:\n\t"
      "v_mfma_f32_16x16x32_bf16 v[196:199], v[136:139], v[44:47], v[196:199]\n\t"
      "v_mfma_f32_16x16x32_bf16 v[184:187], v[140:143], v[48:51], v[184:187]\n\t"
      "s_waitcnt vmcnt(2)\n\t"
      "v_cmp_eq_u32_e32 vcc, -1, v144\n\t"
      "v_mfma_f32_16x16x32_bf16 v[188:191], v[140:143], v[52:55], v[188:191]\n\t"
      "v_mfma_f32_16x16x32_bf16 v[192:195], v[140:143], v[56:59], v[192:195]\n\t"
      "s_cbranch_vccnz FQ46R_%=\n\t"
      "FQ46B_%=:\n\t"
      "v_mfma_f32_16x16x32_bf16 v[196:199], v[140:143], v[60:63], v[196:199]\n\t"
      "v_mfma_f32_16x16x32_bf16 v[184:187], v[144:147], v[64:67], v[184:187]\n\t"
      "s_waitcnt vmcnt(1)\n\t"
      "v_cmp_eq_u32_e32 vcc, -1, v148\n\t"
      "v_mfma_f32_16x16x32_bf16 v[188:191], v[144:147], v[68:71], v[188:191]\n\t"
      "v_mfma_f32_16x16x32_bf16 v[192:195], v[144:147], v[72:75], v[192:195]\n\t"
      "s_cbranch_vccnz FQ47R_%=\n\t"
      "FQ47B_%=:\n\t"
      "v_mfma_f32_16x16x32_bf16 v[196:199], v[144:147], v[76:79], v[196:199]\n\t"
      "s_waitcnt lgkmcnt(0)\n\t"
      "s_barrier\n\t"
      "s_setprio 2\n\t"
      "v_mfma_f32_16x16x32_bf16 v[184:187], v[148:151], v[80:83], v[184:187]\n\t"
      "s_waitcnt vmcnt(0)\n\t"
      "v_cmp_eq_u32_e32 vcc, -1, v152\n\t"
      "v_mfma_f32_16x16x32_bf16 v[188:191], v[148:151], v[84:87], v[188:191]\n\t"
      "v_mfma_f32_16x16x32_bf16 v[192:195], v[148:151], v[88:91], v[192:195]\n\t"
      "s_cbranch_vccnz FQ48R_%=\n\t"
      "FQ48B_%=:\n\t"
      "v_mfma_f32_16x16x32_bf16 v[196:199], v[148:151], v[92:95], v[196:199]\n\t"
      "v_mfma_f32_16x16x32_bf16 v[184:187], v[152:155], v[96:99], v[184:187]\n\t"
      "v_mfma_f32_16x16x32_bf16 v[188:191], v[152:155], v[100:103], v[188:191]\n\t"
      "v_mfma_f32_16x16x32_bf16 v[192:195], v[152:155], v[104:107], v[192:195]\n\t"
      "v_mfma_f32_16x16x32_bf16 v[196:199], v[152:155], v[108:111], v[196:199]\n\t"
      "s_nop 4\n\t"
      "v_cmp_u_f32_e32 vcc, v184, v185\n\t"
      "v_cmp_u_f32_e64 s[48:49], v186, v187\n\t"
      "s_nop 0\n\t"
      "s_or_b64 vcc, vcc, s[48:49]\n\t"
      "s_cbranch_scc1 NQ6R_%=\n\t"
      "NQ6B_%=:\n\t"
      "ds_write_b128 v243, v[184:187]\n\t"
      "ds_write_b128 v220, v[188:191]\n\t"
      "ds_write_b128 v221, v[192:195]\n\t"
      "ds_write_b128 v222, v[196:199]\n\t"
      "s_waitcnt lgkmcnt(0)\n\t"
      "s_barrier\n\t"
      "s_branch GX_%=\n\t"
      "HWR_%=:\n\t"
      "s_mov_b32 s51, 0\n\t"
      "HWL_%=:\n\t"
      "s_add_u32 s51, s51, 1\n\t"
      "s_cmp_lt_u32 s51, 262144\n\t"
      "s_cbranch_scc0 HWX_%=\n\t"
      "s_sleep 2\n\t"
      "global_load_dwordx4 v[112:115], v116, s[12:13] sc1\n\t"
      "s_waitcnt vmcnt(0)\n\t"
      "v_xor_b32_e64 v114, v114, v112\n\t"
      "v_xor_b32_e32 v115, v115, v113\n\t"
      "v_cmp_ne_u64_e32 vcc, s[14:15], v[114:115]\n\t"
      "s_cbranch_vccnz HWL_%=\n\t"
      "HWX_%=:\n\t"
      "s_branch HWB_%=\n\t"
      "HXR_%=:\n\t"
      "s_mov_b32 s51, 0\n\t"
      "HXL_%=:\n\t"
      "s_add_u32 s51, s51, 1\n\t"
      "s_cmp_lt_u32 s51, 262144\n\t"
      "s_cbranch_scc0 HXX_%=\n\t"
      "s_sleep 2\n\t"
      "s_mov_b32 exec_hi, s23\n\t"
      "s_not_b32 exec_lo, s23\n\t"
      "s_add_u32 s68, s20, 0\n\t"
      "s_addc_u32 s69, s21, 0\n\t"
      "global_load_dwordx4 v[184:187], v240, s[68:69] sc1\n\t"
      "s_add_u32 s68, s20, 131072\n\t"
      "s_addc_u32 s69, s21, 0\n\t"
      "global_load_dwordx4 v[188:191], v240, s[68:69] sc1\n\t"
      "s_add_u32 s68, s20, 262144\n\t"
      "s_addc_u32 s69, s21, 0\n\t"
      "global_load_dwordx4 v[192:195], v240, s[68:69] sc1\n\t"
      "s_add_u32 s68, s20, 393216\n\t"
      "s_addc_u32 s69, s21, 0\n\t"
      "global_load_dwordx4 v[196:199], v240, s[68:69] sc1\n\t"
      "s_add_u32 s68, s20, 524288\n\t"
      "s_addc_u32 s69, s21, 0\n\t"
      "global_load_dwordx4 v[200:203], v240, s[68:69] sc1\n\t"
      "s_add_u32 s68, s20, 655360\n\t"
      "s_addc_u32 s69, s21, 0\n\t"
      "global_load_dwordx4 v[204:207], v240, s[68:69] sc1\n\t"
      "s_add_u32 s68, s20, 786432\n\t"
      "s_addc_u32 s69, s21, 0\n\t"
      "global_load_dwordx4 v[208:211], v240, s[68:69] sc1\n\t"
      "s_mov_b64 exec, -1\n\t"
      "s_waitcnt vmcnt(0)\n\t"
      "v_max3_u32 v234, v184, v185, v186\n\t"
      "v_max3_u32 v234, v234, v187, v188\n\t"
      "v_max3_u32 v234, v234, v189, v190\n\t"
      "v_max3_u32 v234, v234, v191, v192\n\t"
      "v_max3_u32 v234, v234, v193, v194\n\t"
      "v_max3_u32 v234, v234, v195, v196\n\t"
      "v_max3_u32 v234, v234, v197, v198\n\t"
      "v_max3_u32 v234, v234, v199, v200\n\t"
      "v_max3_u32 v234, v234, v201, v202\n\t"
      "v_max3_u32 v234, v234, v203, v204\n\t"
      "v_max3_u32 v234, v234, v205, v206\n\t"
      "v_max3_u32 v234, v234, v207, v208\n\t"
      "v_max3_u32 v234, v234, v209, v210\n\t"
      "v_max_u32_e32 v234, v234, v211\n\t"
      "v_cmp_eq_u32_e32 vcc, -1, v234\n\t"
      "s_cbranch_vccnz HXL_%=\n\t"
      "HXX_%=:\n\t"
      "s_nop 1\n\t"
      "s_branch HXB_%=\n\t"
      "FQ1R_%=:\n\t"
      "s_mov_b32 s51, 0\n\t"
      "FQ1L_%=:\n\t"
      "s_add_u32 s51, s51, 1\n\t"
      "s_cmp_lt_u32 s51, 262144\n\t"
      "s_cbranch_scc0 FQ1X_%=\n\t"
      "s_waitcnt vmcnt(0)\n\t"
      "s_sleep 1\n\t"
      "global_load_dwordx4 v[132:135], v225, s[52:53] sc1\n\t"
      "global_load_dwordx4 v[136:139], v226, s[52:53] sc1\n\t"
      "global_load_dwordx4 v[140:143], v227, s[52:53] sc1\n\t"
      "global_load_dwordx4 v[144:147], v228, s[52:53] sc1\n\t"
      "global_load_dwordx4 v[148:151], v229, s[52:53] sc1\n\t"
      "global_load_dwordx4 v[152:155], v230, s[52:53] sc1\n\t"
      "s_waitcnt vmcnt(5)\n\t"
      "v_max3_u32 v234, v132, v133, v134\n\t"
      "v_max_u32_e32 v234, v234, v135\n\t"
      "v_cmp_eq_u32_e32 vcc, -1, v234\n\t"
      "s_cbranch_vccnz FQ1L_%=\n\t"
      "FQ1X_%=:\n\t"
      "s_branch FQ1B_%=\n\t"
      "FQ2R_%=:\n\t"
      "s_mov_b32 s51, 0\n\t"
      "FQ2L_%=:\n\t"
      "s_add_u32 s51, s51, 1\n\t"
      "s_cmp_lt_u32 s51, 262144\n\t"
      "s_cbranch_scc0 FQ2X_%=\n\t"
      "s_waitcnt vmcnt(0)\n\t"
      "s_sleep 1\n\t"
      "global_load_dwordx4 v[136:139], v226, s[52:53] sc1\n\t"
      "global_load_dwordx4 v[140:143], v227, s[52:53] sc1\n\t"
      "global_load_dwordx4 v[144:147], v228, s[52:53] sc1\n\t"
      "global_load_dwordx4 v[148:151], v229, s[52:53] sc1\n\t"
      "global_load_dwordx4 v[152:155], v230, s[52:53] sc1\n\t"
      "global_load_dwordx4 v[128:131], v224, s[54:55] sc1\n\t"
      "s_waitcnt vmcnt(5)\n\t"
      "v_max3_u32 v234, v136, v137, v138\n\t"
      "v_max_u32_e32 v234, v234, v139\n\t"
      "v_cmp_eq_u32_e32 vcc, -1, v234\n\t"
      "s_cbranch_vccnz FQ2L_%=\n\t"
      "FQ2X_%=:\n\t"
      "s_branch FQ2B_%=\n\t"
      "FQ3R_%=:\n\t"
      "s_mov_b32 s51, 0\n\t"
      "FQ3L_%=:\n\t"
      "s_add_u32 s51, s51, 1\n\t"
      "s_cmp_lt_u32 s51, 262144\n\t"
      "s_cbranch_scc0 FQ3X_%=\n\t"
      "s_waitcnt vmcnt(0)\n\t"
      "s_sleep 1\n\t"
      "global_load_dwordx4 v[140:143], v227, s[52:53] sc1\n\t"
      "global_load_dwordx4 v[144:147], v228, s[52:53] sc1\n\t"
      "global_load_dwordx4 v[148:151], v229, s[52:53] sc1\n\t"
      "global_load_dwordx4 v[152:155], v230, s[52:53] sc1\n\t"
      "global_load_dwordx4 v[128:131], v224, s[54:55] sc1\n\t"
      "global_load_dwordx4 v[132:135], v225, s[54:55] sc1\n\t"
      "s_waitcnt vmcnt(5)\n\t"
      "v_max3_u32 v234, v140, v141, v142\n\t"
      "v_max_u32_e32 v234, v234, v143\n\t"
      "v_cmp_eq_u32_e32 vcc, -1, v234\n\t"
      "s_cbranch_vccnz FQ3L_%=\n\t"
      "FQ3X_%=:\n\t"
      "s_branch FQ3B_%=\n\t"
      "FQ4R_%=:\n\t"
      "s_mov_b32 s51, 0\n\t"
      "FQ4L_%=:\n\t"
      "s_add_u32 s51, s51, 1\n\t"
      "s_cmp_lt_u32 s51, 262144\n\t"
      "s_cbranch_scc0 FQ4X_%=\n\t"
      "s_waitcnt vmcnt(0)\n\t"
      "s_sleep 1\n\t"
      "global_load_dwordx4 v[144:147], v228, s[52:53] sc1\n\t"
      "global_load_dwordx4 v[148:151], v229, s[52:53] sc1\n\t"
      "global_load_dwordx4 v[152:155], v230, s[52:53] sc1\n\t"
      "global_load_dwordx4 v[128:131], v224, s[54:55] sc1\n\t"
      "global_load_dwordx4 v[132:135], v225, s[54:55] sc1\n\t"
      "global_load_dwordx4 v[136:139], v226, s[54:55] sc1\n\t"
      "s_waitcnt vmcnt(5)\n\t"
      "v_max3_u32 v234, v144, v145, v146\n\t"
      "v_max_u32_e32 v234, v234, v147\n\t"
      "v_cmp_eq_u32_e32 vcc, -1, v234\n\t"
      "s_cbranch_vccnz FQ4L_%=\n\t"
      "FQ4X_%=:\n\t"
      "s_branch FQ4B_%=\n\t"
      "FQ5R_%=:\n\t"
      "s_mov_b32 s51, 0\n\t"
      "FQ5L_%=:\n\t"
      "s_add_u32 s51, s51, 1\n\t"
      "s_cmp_lt_u32 s51, 262144\n\t"
      "s_cbranch_scc0 FQ5X_%=\n\t"
      "s_waitcnt vmcnt(0)\n\t"
      "s_sleep 1\n\t"
      "global_load_dwordx4 v[148:151], v229, s[52:53] sc1\n\t"
      "global_load_dwordx4 v[152:155], v230, s[52:53] sc1\n\t"
      "global_load_dwordx4 v[128:131], v224, s[54:55] sc1\n\t"
      "global_load_dwordx4 v[132:135], v225, s[54:55] sc1\n\t"
      "global_load_dwordx4 v[136:139], v226, s[54:55] sc1\n\t"
      "global_load_dwordx4 v[140:143], v227, s[54:55] sc1\n\t"
      "s_waitcnt vmcnt(5)\n\t"
      "v_max3_u32 v234, v148, v149, v150\n\t"
      "v_max_u32_e32 v234, v234, v151\n\t"
      "v_cmp_eq_u32_e32 vcc, -1, v234\n\t"
      "s_cbranch_vccnz FQ5L_%=\n\t"
      "FQ5X_%=:\n\t"
      "s_branch FQ5B_%=\n\t"
      "FQ6R_%=:\n\t"
      "s_mov_b32 s51, 0\n\t"
      "FQ6L_%=:\n\t"
      "s_add_u32 s51, s51, 1\n\t"
      "s_cmp_lt_u32 s51, 262144\n\t"
      "s_cbranch_scc0 FQ6X_%=\n\t"
      "s_waitcnt vmcnt(0)\n\t"
      "s_sleep 1\n\t"
      "global_load_dwordx4 v[152:155], v230, s[52:53] sc1\n\t"
      "global_load_dwordx4 v[128:131], v224, s[54:55] sc1\n\t"
      "global_load_dwordx4 v[132:135], v225, s[54:55] sc1\n\t"
      "global_load_dwordx4 v[136:139], v226, s[54:55] sc1\n\t"
      "global_load_dwordx4 v[140:143], v227, s[54:55] sc1\n\t"
      "global_load_dwordx4 v[144:147], v228, s[54:55] sc1\n\t"
      "s_waitcnt vmcnt(5)\n\t"
      "v_max3_u32 v234, v152, v153, v154\n\t"
      "v_max_u32_e32 v234, v234, v155\n\t"
      "v_cmp_eq_u32_e32 vcc, -1, v234\n\t"
      "s_cbranch_vccnz FQ6L_%=\n\t"
      "FQ6X_%=:\n\t"
      "s_branch FQ6B_%=\n\t"
      "FQ7R_%=:\n\t"
      "s_mov_b32 s51, 0\n\t"
      "FQ7L_%=:\n\t"
      "s_add_u32 s51, s51, 1\n\t"
      "s_cmp_lt_u32 s51, 262144\n\t"
      "s_cbranch_scc0 FQ7X_%=\n\t"
      "s_waitcnt vmcnt(0)\n\t"
      "s_sleep 4\n\t"
      "global_load_dwordx4 v[128:131], v224, s[54:55] sc1\n\t"
      "global_load_dwordx4 v[132:135], v225, s[54:55] sc1\n\t"
      "global_load_dwordx4 v[136:139], v226, s[54:55] sc1\n\t"
      "global_load_dwordx4 v[140:143], v227, s[54:55] sc1\n\t"
      "global_load_dwordx4 v[144:147], v228, s[54:55] sc1\n\t"
      "global_load_dwordx4 v[148:151], v229, s[54:55] sc1\n\t"
      "s_waitcnt vmcnt(5)\n\t"
      "v_max3_u32 v234, v128, v129, v130\n\t"
      "v_max_u32_e32 v234, v234, v131\n\t"
      "v_cmp_eq_u32_e32 vcc, -1, v234\n\t"
      "s_cbranch_vccnz FQ7L_%=\n\t"
      "FQ7X_%=:\n\t"
      "s_branch FQ7B_%=\n\t"
      "NQ0R_%=:\n\t"
      "s_mov_b32 s51, 0\n\t"
      "NQ0L_%=:\n\t"
      "s_add_u32 s51, s51, 1\n\t"
      "s_cmp_lt_u32 s51, 262144\n\t"
      "s_cbranch_scc0 NQ0X_%=\n\t"
      "s_waitcnt vmcnt(0)\n\t"
      "s_sleep 1\n\t"
      "global_load_dwordx4 v[128:131], v224, s[52:53] sc1\n\t"
      "global_load_dwordx4 v[132:135], v225, s[52:53] sc1\n\t"
      "global_load_dwordx4 v[136:139], v226, s[52:53] sc1\n\t"
      "global_load_dwordx4 v[140:143], v227, s[52:53] sc1\n\t"
      "global_load_dwordx4 v[144:147], v228, s[52:53] sc1\n\t"
      "global_load_dwordx4 v[148:151], v229, s[52:53] sc1\n\t"
      "global_load_dwordx4 v[152:155], v230, s[52:53] sc1\n\t"
      "s_waitcnt vmcnt(0)\n\t"
      "s_nop 0\n\t"
      "v_max3_u32 v234, v128, v129, v130\n\t"
      "v_max3_u32 v234, v234, v131, v132\n\t"
      "v_max3_u32 v234, v234, v133, v134\n\t"
      "v_max3_u32 v234, v234, v135, v136\n\t"
      "v_max3_u32 v234, v234, v137, v138\n\t"
      "v_max3_u32 v234, v234, v139, v140\n\t"
      "v_max3_u32 v234, v234, v141, v142\n\t"
      "v_max3_u32 v234, v234, v143, v144\n\t"
      "v_max3_u32 v234, v234, v145, v146\n\t"
      "v_max3_u32 v234, v234, v147, v148\n\t"
      "v_max3_u32 v234, v234, v149, v150\n\t"
      "v_max3_u32 v234, v234, v151, v152\n\t"
      "v_max3_u32 v234, v234, v153, v154\n\t"
      "v_max_u32_e32 v234, v234, v155\n\t"
      "v_cmp_eq_u32_e32 vcc, -1, v234\n\t"
      "s_cbranch_vccnz NQ0L_%=\n\t"
      "NQ0X_%=:\n\t"
      "v_mfma_f32_16x16x32_bf16 v[184:187], v[128:131], v[0:3], 0\n\t"
      "v_mfma_f32_16x16x32_bf16 v[188:191], v[128:131], v[4:7], 0\n\t"
      "v_mfma_f32_16x16x32_bf16 v[192:195], v[128:131], v[8:11], 0\n\t"
      "v_mfma_f32_16x16x32_bf16 v[196:199], v[128:131], v[12:15], 0\n\t"
      "v_mfma_f32_16x16x32_bf16 v[184:187], v[132:135], v[16:19], v[184:187]\n\t"
      "v_mfma_f32_16x16x32_bf16 v[188:191], v[132:135], v[20:23], v[188:191]\n\t"
      "v_mfma_f32_16x16x32_bf16 v[192:195], v[132:135], v[24:27], v[192:195]\n\t"
      "v_mfma_f32_16x16x32_bf16 v[196:199], v[132:135], v[28:31], v[196:199]\n\t"
      "v_mfma_f32_16x16x32_bf16 v[184:187], v[136:139], v[32:35], v[184:187]\n\t"
      "v_mfma_f32_16x16x32_bf16 v[188:191], v[136:139], v[36:39], v[188:191]\n\t"
      "v_mfma_f32_16x16x32_bf16 v[192:195], v[136:139], v[40:43], v[192:195]\n\t"
      "v_mfma_f32_16x16x32_bf16 v[196:199], v[136:139], v[44:47], v[196:199]\n\t"
      "v_mfma_f32_16x16x32_bf16 v[184:187], v[140:143], v[48:51], v[184:187]\n\t"
      "v_mfma_f32_16x16x32_bf16 v[188:191], v[140:143], v[52:55], v[188:191]\n\t"
      "v_mfma_f32_16x16x32_bf16 v[192:195], v[140:143], v[56:59], v[192:195]\n\t"
      "v_mfma_f32_16x16x32_bf16 v[196:199], v[140:143], v[60:63], v[196:199]\n\t"
      "v_mfma_f32_16x16x32_bf16 v[184:187], v[144:147], v[64:67], v[184:187]\n\t"
      "v_mfma_f32_16x16x32_bf16 v[188:191], v[144:147], v[68:71], v[188:191]\n\t"
      "v_mfma_f32_16x16x32_bf16 v[192:195], v[144:147], v[72:75], v[192:195]\n\t"
      "v_mfma_f32_16x16x32_bf16 v[196:199], v[144:147], v[76:79], v[196:199]\n\t"
      "v_mfma_f32_16x16x32_bf16 v[184:187], v[148:151], v[80:83], v[184:187]\n\t"
      "v_mfma_f32_16x16x32_bf16 v[188:191], v[148:151], v[84:87], v[188:191]\n\t"
      "v_mfma_f32_16x16x32_bf16 v[192:195], v[148:151], v[88:91], v[192:195]\n\t"
      "v_mfma_f32_16x16x32_bf16 v[196:199], v[148:151], v[92:95], v[196:199]\n\t"
      "v_mfma_f32_16x16x32_bf16 v[184:187], v[152:155], v[96:99], v[184:187]\n\t"
      "v_mfma_f32_16x16x32_bf16 v[188:191], v[152:155], v[100:103], v[188:191]\n\t"
      "v_mfma_f32_16x16x32_bf16 v[192:195], v[152:155], v[104:107], v[192:195]\n\t"
      "v_mfma_f32_16x16x32_bf16 v[196:199], v[152:155], v[108:111], v[196:199]\n\t"
      "global_load_dwordx4 v[128:131], v224, s[54:55] sc1\n\t"
      "global_load_dwordx4 v[132:135], v225, s[54:55] sc1\n\t"
      "global_load_dwordx4 v[136:139], v226, s[54:55] sc1\n\t"
      "global_load_dwordx4 v[140:143], v227, s[54:55] sc1\n\t"
      "global_load_dwordx4 v[144:147], v228, s[54:55] sc1\n\t"
      "global_load_dwordx4 v[148:151], v229, s[54:55] sc1\n\t"
      "global_load_dwordx4 v[152:155], v230, s[54:55] sc1\n\t"
      "s_waitcnt vmcnt(0)\n\t"
      "s_nop 7\n\t"
      "s_branch NQ0B_%=\n\t"
      "FQ8R_%=:\n\t"
      "s_mov_b32 s51, 0\n\t"
      "FQ8L_%=:\n\t"
      "s_add_u32 s51, s51, 1\n\t"
      "s_cmp_lt_u32 s51, 262144\n\t"
      "s_cbranch_scc0 FQ8X_%=\n\t"
      "s_waitcnt vmcnt(0)\n\t"
      "s_sleep 4\n\t"
      "global_load_dwordx4 v[132:135], v225, s[54:55] sc1\n\t"
      "global_load_dwordx4 v[136:139], v226, s[54:55] sc1\n\t"
      "global_load_dwordx4 v[140:143], v227, s[54:55] sc1\n\t"
      "global_load_dwordx4 v[144:147], v228, s[54:55] sc1\n\t"
      "global_load_dwordx4 v[148:151], v229, s[54:55] sc1\n\t"
      "global_load_dwordx4 v[152:155], v230, s[54:55] sc1\n\t"
      "s_waitcnt vmcnt(5)\n\t"
      "v_max3_u32 v234, v132, v133, v134\n\t"
      "v_max_u32_e32 v234, v234, v135\n\t"
      "v_cmp_eq_u32_e32 vcc, -1, v234\n\t"
      "s_cbranch_vccnz FQ8L_%=\n\t"
      "FQ8X_%=:\n\t"
      "s_branch FQ8B_%=\n\t"
      "FQ9R_%=:\n\t"
      "s_mov_b32 s51, 0\n\t"
      "FQ9L_%=:\n\t"
      "s_add_u32 s51, s51, 1\n\t"
      "s_cmp_lt_u32 s51, 262144\n\t"
      "s_cbranch_scc0 FQ9X_%=\n\t"
      "s_waitcnt vmcnt(0)\n\t"
      "s_sleep 4\n\t"
      "global_load_dwordx4 v[136:139], v226, s[54:55] sc1\n\t"
      "global_load_dwordx4 v[140:143], v227, s[54:55] sc1\n\t"
      "global_load_dwordx4 v[144:147], v228, s[54:55] sc1\n\t"
      "global_load_dwordx4 v[148:151], v229, s[54:55] sc1\n\t"
      "global_load_dwordx4 v[152:155], v230, s[54:55] sc1\n\t"
      "global_load_dwordx4 v[128:131], v224, s[56:57] sc1\n\t"
      "s_waitcnt vmcnt(5)\n\t"
      "v_max3_u32 v234, v136, v137, v138\n\t"
      "v_max_u32_e32 v234, v234, v139\n\t"
      "v_cmp_eq_u32_e32 vcc, -1, v234\n\t"
      "s_cbranch_vccnz FQ9L_%=\n\t"
      "FQ9X_%=:\n\t"
      "s_branch FQ9B_%=\n\t"
      "FQ10R_%=:\n\t"
      "s_mov_b32 s51, 0\n\t"
      "FQ10L_%=:\n\t"
      "s_add_u32 s51, s51, 1\n\t"
      "s_cmp_lt_u32 s51, 262144\n\t"
      "s_cbranch_scc0 FQ10X_%=\n\t"
      "s_waitcnt vmcnt(0)\n\t"
      "s_sleep 4\n\t"
      "global_load_dwordx4 v[140:143], v227, s[54:55] sc1\n\t"
      "global_load_dwordx4 v[144:147], v228, s[54:55] sc1\n\t"
      "global_load_dwordx4 v[148:151], v229, s[54:55] sc1\n\t"
      "global_load_dwordx4 v[152:155], v230, s[54:55] sc1\n\t"
      "global_load_dwordx4 v[128:131], v224, s[56:57] sc1\n\t"
      "global_load_dwordx4 v[132:135], v225, s[56:57] sc1\n\t"
      "s_waitcnt vmcnt(5)\n\t"
      "v_max3_u32 v234, v140, v141, v142\n\t"
      "v_max_u32_e32 v234, v234, v143\n\t"
      "v_cmp_eq_u32_e32 vcc, -1, v234\n\t"
      "s_cbranch_vccnz FQ10L_%=\n\t"
      "FQ10X_%=:\n\t"
      "s_branch FQ10B_%=\n\t"
      "FQ11R_%=:\n\t"
      "s_mov_b32 s51, 0\n\t"
      "FQ11L_%=:\n\t"
      "s_add_u32 s51, s51, 1\n\t"
      "s_cmp_lt_u32 s51, 262144\n\t"
      "s_cbranch_scc0 FQ11X_%=\n\t"
      "s_waitcnt vmcnt(0)\n\t"
      "s_sleep 4\n\t"
      "global_load_dwordx4 v[144:147], v228, s[54:55] sc1\n\t"
      "global_load_dwordx4 v[148:151], v229, s[54:55] sc1\n\t"
      "global_load_dwordx4 v[152:155], v230, s[54:55] sc1\n\t"
      "global_load_dwordx4 v[128:131], v224, s[56:57] sc1\n\t"
      "global_load_dwordx4 v[132:135], v225, s[56:57] sc1\n\t"
      "global_load_dwordx4 v[136:139], v226, s[56:57] sc1\n\t"
      "s_waitcnt vmcnt(5)\n\t"
      "v_max3_u32 v234, v144, v145, v146\n\t"
      "v_max_u32_e32 v234, v234, v147\n\t"
      "v_cmp_eq_u32_e32 vcc, -1, v234\n\t"
      "s_cbranch_vccnz FQ11L_%=\n\t"
      "FQ11X_%=:\n\t"
      "s_branch FQ11B_%=\n\t"
      "FQ12R_%=:\n\t"
      "s_mov_b32 s51, 0\n\t"
      "FQ12L_%=:\n\t"
      "s_add_u32 s51, s51, 1\n\t"
      "s_cmp_lt_u32 s51, 262144\n\t"
      "s_cbranch_scc0 FQ12X_%=\n\t"
      "s_waitcnt vmcnt(0)\n\t"
      "s_sleep 4\n\t"
      "global_load_dwordx4 v[148:151], v229, s[54:55] sc1\n\t"
      "global_load_dwordx4 v[152:155], v230, s[54:55] sc1\n\t"
      "global_load_dwordx4 v[128:131], v224, s[56:57] sc1\n\t"
      "global_load_dwordx4 v[132:135], v225, s[56:57] sc1\n\t"
      "global_load_dwordx4 v[136:139], v226, s[56:57] sc1\n\t"
      "global_load_dwordx4 v[140:143], v227, s[56:57] sc1\n\t"
      "s_waitcnt vmcnt(5)\n\t"
      "v_max3_u32 v234, v148, v149, v150\n\t"
      "v_max_u32_e32 v234, v234, v151\n\t"
      "v_cmp_eq_u32_e32 vcc, -1, v234\n\t"
      "s_cbranch_vccnz FQ12L_%=\n\t"
      "FQ12X_%=:\n\t"
      "s_branch FQ12B_%=\n\t"
      "FQ13R_%=:\n\t"
      "s_mov_b32 s51, 0\n\t"
      "FQ13L_%=:\n\t"
      "s_add_u32 s51, s51, 1\n\t"
      "s_cmp_lt_u32 s51, 262144\n\t"
      "s_cbranch_scc0 FQ13X_%=\n\t"
      "s_waitcnt vmcnt(0)\n\t"
      "s_sleep 4\n\t"
      "global_load_dwordx4 v[152:155], v230, s[54:55] sc1\n\t"
      "global_load_dwordx4 v[128:131], v224, s[56:57] sc1\n\t"
      "global_load_dwordx4 v[132:135], v225, s[56:57] sc1\n\t"
      "global_load_dwordx4 v[136:139], v226, s[56:57] sc1\n\t"
      "global_load_dwordx4 v[140:143], v227, s[56:57] sc1\n\t"
      "global_load_dwordx4 v[144:147], v228, s[56:57] sc1\n\t"
      "s_waitcnt vmcnt(5)\n\t"
      "v_max3_u32 v234, v152, v153, v154\n\t"
      "v_max_u32_e32 v234, v234, v155\n\t"
      "v_cmp_eq_u32_e32 vcc, -1, v234\n\t"
      "s_cbranch_vccnz FQ13L_%=\n\t"
      "FQ13X_%=:\n\t"
      "s_branch FQ13B_%=\n\t"
      "FQ14R_%=:\n\t"
      "s_mov_b32 s51, 0\n\t"
      "FQ14L_%=:\n\t"
      "s_add_u32 s51, s51, 1\n\t"
      "s_cmp_lt_u32 s51, 262144\n\t"
      "s_cbranch_scc0 FQ14X_%=\n\t"
      "s_waitcnt vmcnt(0)\n\t"
      "s_sleep 4\n\t"
      "global_load_dwordx4 v[128:131], v224, s[56:57] sc1\n\t"
      "global_load_dwordx4 v[132:135], v225, s[56:57] sc1\n\t"
      "global_load_dwordx4 v[136:139], v226, s[56:57] sc1\n\t"
      "global_load_dwordx4 v[140:143], v227, s[56:57] sc1\n\t"
      "global_load_dwordx4 v[144:147], v228, s[56:57] sc1\n\t"
      "global_load_dwordx4 v[148:151], v229, s[56:57] sc1\n\t"
      "s_waitcnt vmcnt(5)\n\t"
      "v_max3_u32 v234, v128, v129, v130\n\t"
      "v_max_u32_e32 v234, v234, v131\n\t"
      "v_cmp_eq_u32_e32 vcc, -1, v234\n\t"
      "s_cbranch_vccnz FQ14L_%=\n\t"
      "FQ14X_%=:\n\t"
      "s_branch FQ14B_%=\n\t"
      "NQ1R_%=:\n\t"
      "s_mov_b32 s51, 0\n\t"
      "NQ1L_%=:\n\t"
      "s_add_u32 s51, s51, 1\n\t"
      "s_cmp_lt_u32 s51, 262144\n\t"
      "s_cbranch_scc0 NQ1X_%=\n\t"
      "s_waitcnt vmcnt(0)\n\t"
      "s_sleep 1\n\t"
      "global_load_dwordx4 v[128:131], v224, s[54:55] sc1\n\t"
      "global_load_dwordx4 v[132:135], v225, s[54:55] sc1\n\t"
      "global_load_dwordx4 v[136:139], v226, s[54:55] sc1\n\t"
      "global_load_dwordx4 v[140:143], v227, s[54:55] sc1\n\t"
      "global_load_dwordx4 v[144:147], v228, s[54:55] sc1\n\t"
      "global_load_dwordx4 v[148:151], v229, s[54:55] sc1\n\t"
      "global_load_dwordx4 v[152:155], v230, s[54:55] sc1\n\t"
      "s_waitcnt vmcnt(0)\n\t"
      "v_max3_u32 v234, v128, v129, v130\n\t"
      "v_max3_u32 v234, v234, v131, v132\n\t"
      "v_max3_u32 v234, v234, v133, v134\n\t"
      "v_max3_u32 v234, v234, v135, v136\n\t"
      "v_max3_u32 v234, v234, v137, v138\n\t"
      "v_max3_u32 v234, v234, v139, v140\n\t"
      "v_max3_u32 v234, v234, v141, v142\n\t"
      "v_max3_u32 v234, v234, v143, v144\n\t"
      "v_max3_u32 v234, v234, v145, v146\n\t"
      "v_max3_u32 v234, v234, v147, v148\n\t"
      "v_max3_u32 v234, v234, v149, v150\n\t"
      "v_max3_u32 v234, v234, v151, v152\n\t"
      "v_max3_u32 v234, v234, v153, v154\n\t"
      "v_max_u32_e32 v234, v234, v155\n\t"
      "v_cmp_eq_u32_e32 vcc, -1, v234\n\t"
      "s_cbranch_vccnz NQ1L_%=\n\t"
      "NQ1X_%=:\n\t"
      "v_mfma_f32_16x16x32_bf16 v[200:203], v[128:131], v[0:3], 0\n\t"
      "v_mfma_f32_16x16x32_bf16 v[204:207], v[128:131], v[4:7], 0\n\t"
      "v_mfma_f32_16x16x32_bf16 v[208:211], v[128:131], v[8:11], 0\n\t"
      "v_mfma_f32_16x16x32_bf16 v[212:215], v[128:131], v[12:15], 0\n\t"
      "v_mfma_f32_16x16x32_bf16 v[200:203], v[132:135], v[16:19], v[200:203]\n\t"
      "v_mfma_f32_16x16x32_bf16 v[204:207], v[132:135], v[20:23], v[204:207]\n\t"
      "v_mfma_f32_16x16x32_bf16 v[208:211], v[132:135], v[24:27], v[208:211]\n\t"
      "v_mfma_f32_16x16x32_bf16 v[212:215], v[132:135], v[28:31], v[212:215]\n\t"
      "v_mfma_f32_16x16x32_bf16 v[200:203], v[136:139], v[32:35], v[200:203]\n\t"
      "v_mfma_f32_16x16x32_bf16 v[204:207], v[136:139], v[36:39], v[204:207]\n\t"
      "v_mfma_f32_16x16x32_bf16 v[208:211], v[136:139], v[40:43], v[208:211]\n\t"
      "v_mfma_f32_16x16x32_bf16 v[212:215], v[136:139], v[44:47], v[212:215]\n\t"
      "v_mfma_f32_16x16x32_bf16 v[200:203], v[140:143], v[48:51], v[200:203]\n\t"
      "v_mfma_f32_16x16x32_bf16 v[204:207], v[140:143], v[52:55], v[204:207]\n\t"
      "v_mfma_f32_16x16x32_bf16 v[208:211], v[140:143], v[56:59], v[208:211]\n\t"
      "v_mfma_f32_16x16x32_bf16 v[212:215], v[140:143], v[60:63], v[212:215]\n\t"
      "v_mfma_f32_16x16x32_bf16 v[200:203], v[144:147], v[64:67], v[200:203]\n\t"
      "v_mfma_f32_16x16x32_bf16 v[204:207], v[144:147], v[68:71], v[204:207]\n\t"
      "v_mfma_f32_16x16x32_bf16 v[208:211], v[144:147], v[72:75], v[208:211]\n\t"
      "v_mfma_f32_16x16x32_bf16 v[212:215], v[144:147], v[76:79], v[212:215]\n\t"
      "v_mfma_f32_16x16x32_bf16 v[200:203], v[148:151], v[80:83], v[200:203]\n\t"
      "v_mfma_f32_16x16x32_bf16 v[204:207], v[148:151], v[84:87], v[204:207]\n\t"
      "v_mfma_f32_16x16x32_bf16 v[208:211], v[148:151], v[88:91], v[208:211]\n\t"
      "v_mfma_f32_16x16x32_bf16 v[212:215], v[148:151], v[92:95], v[212:215]\n\t"
      "v_mfma_f32_16x16x32_bf16 v[200:203], v[152:155], v[96:99], v[200:203]\n\t"
      "v_mfma_f32_16x16x32_bf16 v[204:207], v[152:155], v[100:103], v[204:207]\n\t"
      "v_mfma_f32_16x16x32_bf16 v[208:211], v[152:155], v[104:107], v[208:211]\n\t"
      "v_mfma_f32_16x16x32_bf16 v[212:215], v[152:155], v[108:111], v[212:215]\n\t"
      "global_load_dwordx4 v[128:131], v224, s[56:57] sc1\n\t"
      "global_load_dwordx4 v[132:135], v225, s[56:57] sc1\n\t"
      "global_load_dwordx4 v[136:139], v226, s[56:57] sc1\n\t"
      "global_load_dwordx4 v[140:143], v227, s[56:57] sc1\n\t"
      "global_load_dwordx4 v[144:147], v228, s[56:57] sc1\n\t"
      "global_load_dwordx4 v[148:151], v229, s[56:57] sc1\n\t"
      "global_load_dwordx4 v[152:155], v230, s[56:57] sc1\n\t"
      "s_waitcnt vmcnt(0)\n\t"
      "s_nop 7\n\t"
      "s_branch NQ1B_%=\n\t"
      "FQ15R_%=:\n\t"
      "s_mov_b32 s51, 0\n\t"
      "FQ15L_%=:\n\t"
      "s_add_u32 s51, s51, 1\n\t"
      "s_cmp_lt_u32 s51, 262144\n\t"
      "s_cbranch_scc0 FQ15X_%=\n\t"
      "s_waitcnt vmcnt(0)\n\t"
      "s_sleep 4\n\t"
      "global_load_dwordx4 v[132:135], v225, s[56:57] sc1\n\t"
      "global_load_dwordx4 v[136:139], v226, s[56:57] sc1\n\t"
      "global_load_dwordx4 v[140:143], v227, s[56:57] sc1\n\t"
      "global_load_dwordx4 v[144:147], v228, s[56:57] sc1\n\t"
      "global_load_dwordx4 v[148:151], v229, s[56:57] sc1\n\t"
      "global_load_dwordx4 v[152:155], v230, s[56:57] sc1\n\t"
      "s_waitcnt vmcnt(5)\n\t"
      "v_max3_u32 v234, v132, v133, v134\n\t"
      "v_max_u32_e32 v234, v234, v135\n\t"
      "v_cmp_eq_u32_e32 vcc, -1, v234\n\t"
      "s_cbranch_vccnz FQ15L_%=\n\t"
      "FQ15X_%=:\n\t"
      "s_branch FQ15B_%=\n\t"
      "FQ16R_%=:\n\t"
      "s_mov_b32 s51, 0\n\t"
      "FQ16L_%=:\n\t"
      "s_add_u32 s51, s51, 1\n\t"
      "s_cmp_lt_u32 s51, 262144\n\t"
      "s_cbranch_scc0 FQ16X_%=\n\t"
      "s_waitcnt vmcnt(0)\n\t"
      "s_sleep 4\n\t"
      "global_load_dwordx4 v[136:139], v226, s[56:57] sc1\n\t"
      "global_load_dwordx4 v[140:143], v227, s[56:57] sc1\n\t"
      "global_load_dwordx4 v[144:147], v228, s[56:57] sc1\n\t"
      "global_load_dwordx4 v[148:151], v229, s[56:57] sc1\n\t"
      "global_load_dwordx4 v[152:155], v230, s[56:57] sc1\n\t"
      "global_load_dwordx4 v[128:131], v224, s[58:59] sc1\n\t"
      "s_waitcnt vmcnt(5)\n\t"
      "v_max3_u32 v234, v136, v137, v138\n\t"
      "v_max_u32_e32 v234, v234, v139\n\t"
      "v_cmp_eq_u32_e32 vcc, -1, v234\n\t"
      "s_cbranch_vccnz FQ16L_%=\n\t"
      "FQ16X_%=:\n\t"
      "s_branch FQ16B_%=\n\t"
      "FQ17R_%=:\n\t"
      "s_mov_b32 s51, 0\n\t"
      "FQ17L_%=:\n\t"
      "s_add_u32 s51, s51, 1\n\t"
      "s_cmp_lt_u32 s51, 262144\n\t"
      "s_cbranch_scc0 FQ17X_%=\n\t"
      "s_waitcnt vmcnt(0)\n\t"
      "s_sleep 4\n\t"
      "global_load_dwordx4 v[140:143], v227, s[56:57] sc1\n\t"
      "global_load_dwordx4 v[144:147], v228, s[56:57] sc1\n\t"
      "global_load_dwordx4 v[148:151], v229, s[56:57] sc1\n\t"
      "global_load_dwordx4 v[152:155], v230, s[56:57] sc1\n\t"
      "global_load_dwordx4 v[128:131], v224, s[58:59] sc1\n\t"
      "global_load_dwordx4 v[132:135], v225, s[58:59] sc1\n\t"
      "s_waitcnt vmcnt(5)\n\t"
      "v_max3_u32 v234, v140, v141, v142\n\t"
      "v_max_u32_e32 v234, v234, v143\n\t"
      "v_cmp_eq_u32_e32 vcc, -1, v234\n\t"
      "s_cbranch_vccnz FQ17L_%=\n\t"
      "FQ17X_%=:\n\t"
      "s_branch FQ17B_%=\n\t"
      "FQ18R_%=:\n\t"
      "s_mov_b32 s51, 0\n\t"
      "FQ18L_%=:\n\t"
      "s_add_u32 s51, s51, 1\n\t"
      "s_cmp_lt_u32 s51, 262144\n\t"
      "s_cbranch_scc0 FQ18X_%=\n\t"
      "s_waitcnt vmcnt(0)\n\t"
      "s_sleep 4\n\t"
      "global_load_dwordx4 v[144:147], v228, s[56:57] sc1\n\t"
      "global_load_dwordx4 v[148:151], v229, s[56:57] sc1\n\t"
      "global_load_dwordx4 v[152:155], v230, s[56:57] sc1\n\t"
      "global_load_dwordx4 v[128:131], v224, s[58:59] sc1\n\t"
      "global_load_dwordx4 v[132:135], v225, s[58:59] sc1\n\t"
      "global_load_dwordx4 v[136:139], v226, s[58:59] sc1\n\t"
      "s_waitcnt vmcnt(5)\n\t"
      "v_max3_u32 v234, v144, v145, v146\n\t"
      "v_max_u32_e32 v234, v234, v147\n\t"
      "v_cmp_eq_u32_e32 vcc, -1, v234\n\t"
      "s_cbranch_vccnz FQ18L_%=\n\t"
      "FQ18X_%=:\n\t"
      "s_branch FQ18B_%=\n\t"
      "FQ19R_%=:\n\t"
      "s_mov_b32 s51, 0\n\t"
      "FQ19L_%=:\n\t"
      "s_add_u32 s51, s51, 1\n\t"
      "s_cmp_lt_u32 s51, 262144\n\t"
      "s_cbranch_scc0 FQ19X_%=\n\t"
      "s_waitcnt vmcnt(0)\n\t"
      "s_sleep 4\n\t"
      "global_load_dwordx4 v[148:151], v229, s[56:57] sc1\n\t"
      "global_load_dwordx4 v[152:155], v230, s[56:57] sc1\n\t"
      "global_load_dwordx4 v[128:131], v224, s[58:59] sc1\n\t"
      "global_load_dwordx4 v[132:135], v225, s[58:59] sc1\n\t"
      "global_load_dwordx4 v[136:139], v226, s[58:59] sc1\n\t"
      "global_load_dwordx4 v[140:143], v227, s[58:59] sc1\n\t"
      "s_waitcnt vmcnt(5)\n\t"
      "v_max3_u32 v234, v148, v149, v150\n\t"
      "v_max_u32_e32 v234, v234, v151\n\t"
      "v_cmp_eq_u32_e32 vcc, -1, v234\n\t"
      "s_cbranch_vccnz FQ19L_%=\n\t"
      "FQ19X_%=:\n\t"
      "s_branch FQ19B_%=\n\t"
      "FQ20R_%=:\n\t"
      "s_mov_b32 s51, 0\n\t"
      "FQ20L_%=:\n\t"
      "s_add_u32 s51, s51, 1\n\t"
      "s_cmp_lt_u32 s51, 262144\n\t"
      "s_cbranch_scc0 FQ20X_%=\n\t"
      "s_waitcnt vmcnt(0)\n\t"
      "s_sleep 4\n\t"
      "global_load_dwordx4 v[152:155], v230, s[56:57] sc1\n\t"
      "global_load_dwordx4 v[128:131], v224, s[58:59] sc1\n\t"
      "global_load_dwordx4 v[132:135], v225, s[58:59] sc1\n\t"
      "global_load_dwordx4 v[136:139], v226, s[58:59] sc1\n\t"
      "global_load_dwordx4 v[140:143], v227, s[58:59] sc1\n\t"
      "global_load_dwordx4 v[144:147], v228, s[58:59] sc1\n\t"
      "s_waitcnt vmcnt(5)\n\t"
      "v_max3_u32 v234, v152, v153, v154\n\t"
      "v_max_u32_e32 v234, v234, v155\n\t"
      "v_cmp_eq_u32_e32 vcc, -1, v234\n\t"
      "s_cbranch_vccnz FQ20L_%=\n\t"
      "FQ20X_%=:\n\t"
      "s_branch FQ20B_%=\n\t"
      "FQ21R_%=:\n\t"
      "s_mov_b32 s51, 0\n\t"
      "FQ21L_%=:\n\t"
      "s_add_u32 s51, s51, 1\n\t"
      "s_cmp_lt_u32 s51, 262144\n\t"
      "s_cbranch_scc0 FQ21X_%=\n\t"
      "s_waitcnt vmcnt(0)\n\t"
      "s_sleep 4\n\t"
      "global_load_dwordx4 v[128:131], v224, s[58:59] sc1\n\t"
      "global_load_dwordx4 v[132:135], v225, s[58:59] sc1\n\t"
      "global_load_dwordx4 v[136:139], v226, s[58:59] sc1\n\t"
      "global_load_dwordx4 v[140:143], v227, s[58:59] sc1\n\t"
      "global_load_dwordx4 v[144:147], v228, s[58:59] sc1\n\t"
      "global_load_dwordx4 v[148:151], v229, s[58:59] sc1\n\t"
      "s_waitcnt vmcnt(5)\n\t"
      "v_max3_u32 v234, v128, v129, v130\n\t"
      "v_max_u32_e32 v234, v234, v131\n\t"
      "v_cmp_eq_u32_e32 vcc, -1, v234\n\t"
      "s_cbranch_vccnz FQ21L_%=\n\t"
      "FQ21X_%=:\n\t"
      "s_branch FQ21B_%=\n\t"
      "NQ2R_%=:\n\t"
      "s_mov_b32 s51, 0\n\t"
      "NQ2L_%=:\n\t"
      "s_add_u32 s51, s51, 1\n\t"
      "s_cmp_lt_u32 s51, 262144\n\t"
      "s_cbranch_scc0 NQ2X_%=\n\t"
      "s_waitcnt vmcnt(0)\n\t"
      "s_sleep 1\n\t"
      "global_load_dwordx4 v[128:131], v224, s[56:57] sc1\n\t"
      "global_load_dwordx4 v[132:135], v225, s[56:57] sc1\n\t"
      "global_load_dwordx4 v[136:139], v226, s[56:57] sc1\n\t"
      "global_load_dwordx4 v[140:143], v227, s[56:57] sc1\n\t"
      "global_load_dwordx4 v[144:147], v228, s[56:57] sc1\n\t"
      "global_load_dwordx4 v[148:151], v229, s[56:57] sc1\n\t"
      "global_load_dwordx4 v[152:155], v230, s[56:57] sc1\n\t"
      "s_waitcnt vmcnt(0)\n\t"
      "v_max3_u32 v234, v128, v129, v130\n\t"
      "v_max3_u32 v234, v234, v131, v132\n\t"
      "v_max3_u32 v234, v234, v133, v134\n\t"
      "v_max3_u32 v234, v234, v135, v136\n\t"
      "v_max3_u32 v234, v234, v137, v138\n\t"
      "v_max3_u32 v234, v234, v139, v140\n\t"
      "v_max3_u32 v234, v234, v141, v142\n\t"
      "v_max3_u32 v234, v234, v143, v144\n\t"
      "v_max3_u32 v234, v234, v145, v146\n\t"
      "v_max3_u32 v234, v234, v147, v148\n\t"
      "v_max3_u32 v234, v234, v149, v150\n\t"
      "v_max3_u32 v234, v234, v151, v152\n\t"
      "v_max3_u32 v234, v234, v153, v154\n\t"
      "v_max_u32_e32 v234, v234, v155\n\t"
      "v_cmp_eq_u32_e32 vcc, -1, v234\n\t"
      "s_cbranch_vccnz NQ2L_%=\n\t"
      "NQ2X_%=:\n\t"
      "v_mfma_f32_16x16x32_bf16 v[184:187], v[128:131], v[0:3], 0\n\t"
      "v_mfma_f32_16x16x32_bf16 v[188:191], v[128:131], v[4:7], 0\n\t"
      "v_mfma_f32_16x16x32_bf16 v[192:195], v[128:131], v[8:11], 0\n\t"
      "v_mfma_f32_16x16x32_bf16 v[196:199], v[128:131], v[12:15], 0\n\t"
      "v_mfma_f32_16x16x32_bf16 v[184:187], v[132:135], v[16:19], v[184:187]\n\t"
      "v_mfma_f32_16x16x32_bf16 v[188:191], v[132:135], v[20:23], v[188:191]\n\t"
      "v_mfma_f32_16x16x32_bf16 v[192:195], v[132:135], v[24:27], v[192:195]\n\t"
      "v_mfma_f32_16x16x32_bf16 v[196:199], v[132:135], v[28:31], v[196:199]\n\t"
      "v_mfma_f32_16x16x32_bf16 v[184:187], v[136:139], v[32:35], v[184:187]\n\t"
      "v_mfma_f32_16x16x32_bf16 v[188:191], v[136:139], v[36:39], v[188:191]\n\t"
      "v_mfma_f32_16x16x32_bf16 v[192:195], v[136:139], v[40:43], v[192:195]\n\t"
      "v_mfma_f32_16x16x32_bf16 v[196:199], v[136:139], v[44:47], v[196:199]\n\t"
      "v_mfma_f32_16x16x32_bf16 v[184:187], v[140:143], v[48:51], v[184:187]\n\t"
      "v_mfma_f32_16x16x32_bf16 v[188:191], v[140:143], v[52:55], v[188:191]\n\t"
      "v_mfma_f32_16x16x32_bf16 v[192:195], v[140:143], v[56:59], v[192:195]\n\t"
      "v_mfma_f32_16x16x32_bf16 v[196:199], v[140:143], v[60:63], v[196:199]\n\t"
      "v_mfma_f32_16x16x32_bf16 v[184:187], v[144:147], v[64:67], v[184:187]\n\t"
      "v_mfma_f32_16x16x32_bf16 v[188:191], v[144:147], v[68:71], v[188:191]\n\t"
      "v_mfma_f32_16x16x32_bf16 v[192:195], v[144:147], v[72:75], v[192:195]\n\t"
      "v_mfma_f32_16x16x32_bf16 v[196:199], v[144:147], v[76:79], v[196:199]\n\t"
      "v_mfma_f32_16x16x32_bf16 v[184:187], v[148:151], v[80:83], v[184:187]\n\t"
      "v_mfma_f32_16x16x32_bf16 v[188:191], v[148:151], v[84:87], v[188:191]\n\t"
      "v_mfma_f32_16x16x32_bf16 v[192:195], v[148:151], v[88:91], v[192:195]\n\t"
      "v_mfma_f32_16x16x32_bf16 v[196:199], v[148:151], v[92:95], v[196:199]\n\t"
      "v_mfma_f32_16x16x32_bf16 v[184:187], v[152:155], v[96:99], v[184:187]\n\t"
      "v_mfma_f32_16x16x32_bf16 v[188:191], v[152:155], v[100:103], v[188:191]\n\t"
      "v_mfma_f32_16x16x32_bf16 v[192:195], v[152:155], v[104:107], v[192:195]\n\t"
      "v_mfma_f32_16x16x32_bf16 v[196:199], v[152:155], v[108:111], v[196:199]\n\t"
      "global_load_dwordx4 v[128:131], v224, s[58:59] sc1\n\t"
      "global_load_dwordx4 v[132:135], v225, s[58:59] sc1\n\t"
      "global_load_dwordx4 v[136:139], v226, s[58:59] sc1\n\t"
      "global_load_dwordx4 v[140:143], v227, s[58:59] sc1\n\t"
      "global_load_dwordx4 v[144:147], v228, s[58:59] sc1\n\t"
      "global_load_dwordx4 v[148:151], v229, s[58:59] sc1\n\t"
      "global_load_dwordx4 v[152:155], v230, s[58:59] sc1\n\t"
      "s_waitcnt vmcnt(0)\n\t"
      "s_nop 7\n\t"
      "s_branch NQ2B_%=\n\t"
      "FQ22R_%=:\n\t"
      "s_mov_b32 s51, 0\n\t"
      "FQ22L_%=:\n\t"
      "s_add_u32 s51, s51, 1\n\t"
      "s_cmp_lt_u32 s51, 262144\n\t"
      "s_cbranch_scc0 FQ22X_%=\n\t"
      "s_waitcnt vmcnt(0)\n\t"
      "s_sleep 4\n\t"
      "global_load_dwordx4 v[132:135], v225, s[58:59] sc1\n\t"
      "global_load_dwordx4 v[136:139], v226, s[58:59] sc1\n\t"
      "global_load_dwordx4 v[140:143], v227, s[58:59] sc1\n\t"
      "global_load_dwordx4 v[144:147], v228, s[58:59] sc1\n\t"
      "global_load_dwordx4 v[148:151], v229, s[58:59] sc1\n\t"
      "global_load_dwordx4 v[152:155], v230, s[58:59] sc1\n\t"
      "s_waitcnt vmcnt(5)\n\t"
      "v_max3_u32 v234, v132, v133, v134\n\t"
      "v_max_u32_e32 v234, v234, v135\n\t"
      "v_cmp_eq_u32_e32 vcc, -1, v234\n\t"
      "s_cbranch_vccnz FQ22L_%=\n\t"
      "FQ22X_%=:\n\t"
      "s_branch FQ22B_%=\n\t"
      "FQ23R_%=:\n\t"
      "s_mov_b32 s51, 0\n\t"
      "FQ23L_%=:\n\t"
      "s_add_u32 s51, s51, 1\n\t"
      "s_cmp_lt_u32 s51, 262144\n\t"
      "s_cbranch_scc0 FQ23X_%=\n\t"
      "s_waitcnt vmcnt(0)\n\t"
      "s_sleep 4\n\t"
      "global_load_dwordx4 v[136:139], v226, s[58:59] sc1\n\t"
      "global_load_dwordx4 v[140:143], v227, s[58:59] sc1\n\t"
      "global_load_dwordx4 v[144:147], v228, s[58:59] sc1\n\t"
      "global_load_dwordx4 v[148:151], v229, s[58:59] sc1\n\t"
      "global_load_dwordx4 v[152:155], v230, s[58:59] sc1\n\t"
      "global_load_dwordx4 v[128:131], v224, s[60:61] sc1\n\t"
      "s_waitcnt vmcnt(5)\n\t"
      "v_max3_u32 v234, v136, v137, v138\n\t"
      "v_max_u32_e32 v234, v234, v139\n\t"
      "v_cmp_eq_u32_e32 vcc, -1, v234\n\t"
      "s_cbranch_vccnz FQ23L_%=\n\t"
      "FQ23X_%=:\n\t"
      "s_branch FQ23B_%=\n\t"
      "FQ24R_%=:\n\t"
      "s_mov_b32 s51, 0\n\t"
      "FQ24L_%=:\n\t"
      "s_add_u32 s51, s51, 1\n\t"
      "s_cmp_lt_u32 s51, 262144\n\t"
      "s_cbranch_scc0 FQ24X_%=\n\t"
      "s_waitcnt vmcnt(0)\n\t"
      "s_sleep 4\n\t"
      "global_load_dwordx4 v[140:143], v227, s[58:59] sc1\n\t"
      "global_load_dwordx4 v[144:147], v228, s[58:59] sc1\n\t"
      "global_load_dwordx4 v[148:151], v229, s[58:59] sc1\n\t"
      "global_load_dwordx4 v[152:155], v230, s[58:59] sc1\n\t"
      "global_load_dwordx4 v[128:131], v224, s[60:61] sc1\n\t"
      "global_load_dwordx4 v[132:135], v225, s[60:61] sc1\n\t"
      "s_waitcnt vmcnt(5)\n\t"
      "v_max3_u32 v234, v140, v141, v142\n\t"
      "v_max_u32_e32 v234, v234, v143\n\t"
      "v_cmp_eq_u32_e32 vcc, -1, v234\n\t"
      "s_cbranch_vccnz FQ24L_%=\n\t"
      "FQ24X_%=:\n\t"
      "s_branch FQ24B_%=\n\t"
      "FQ25R_%=:\n\t"
      "s_mov_b32 s51, 0\n\t"
      "FQ25L_%=:\n\t"
      "s_add_u32 s51, s51, 1\n\t"
      "s_cmp_lt_u32 s51, 262144\n\t"
      "s_cbranch_scc0 FQ25X_%=\n\t"
      "s_waitcnt vmcnt(0)\n\t"
      "s_sleep 4\n\t"
      "global_load_dwordx4 v[144:147], v228, s[58:59] sc1\n\t"
      "global_load_dwordx4 v[148:151], v229, s[58:59] sc1\n\t"
      "global_load_dwordx4 v[152:155], v230, s[58:59] sc1\n\t"
      "global_load_dwordx4 v[128:131], v224, s[60:61] sc1\n\t"
      "global_load_dwordx4 v[132:135], v225, s[60:61] sc1\n\t"
      "global_load_dwordx4 v[136:139], v226, s[60:61] sc1\n\t"
      "s_waitcnt vmcnt(5)\n\t"
      "v_max3_u32 v234, v144, v145, v146\n\t"
      "v_max_u32_e32 v234, v234, v147\n\t"
      "v_cmp_eq_u32_e32 vcc, -1, v234\n\t"
      "s_cbranch_vccnz FQ25L_%=\n\t"
      "FQ25X_%=:\n\t"
      "s_branch FQ25B_%=\n\t"
      "FQ26R_%=:\n\t"
      "s_mov_b32 s51, 0\n\t"
      "FQ26L_%=:\n\t"
      "s_add_u32 s51, s51, 1\n\t"
      "s_cmp_lt_u32 s51, 262144\n\t"
      "s_cbranch_scc0 FQ26X_%=\n\t"
      "s_waitcnt vmcnt(0)\n\t"
      "s_sleep 4\n\t"
      "global_load_dwordx4 v[148:151], v229, s[58:59] sc1\n\t"
      "global_load_dwordx4 v[152:155], v230, s[58:59] sc1\n\t"
      "global_load_dwordx4 v[128:131], v224, s[60:61] sc1\n\t"
      "global_load_dwordx4 v[132:135], v225, s[60:61] sc1\n\t"
      "global_load_dwordx4 v[136:139], v226, s[60:61] sc1\n\t"
      "global_load_dwordx4 v[140:143], v227, s[60:61] sc1\n\t"
      "s_waitcnt vmcnt(5)\n\t"
      "v_max3_u32 v234, v148, v149, v150\n\t"
      "v_max_u32_e32 v234, v234, v151\n\t"
      "v_cmp_eq_u32_e32 vcc, -1, v234\n\t"
      "s_cbranch_vccnz FQ26L_%=\n\t"
      "FQ26X_%=:\n\t"
      "s_branch FQ26B_%=\n\t"
      "FQ27R_%=:\n\t"
      "s_mov_b32 s51, 0\n\t"
      "FQ27L_%=:\n\t"
      "s_add_u32 s51, s51, 1\n\t"
      "s_cmp_lt_u32 s51, 262144\n\t"
      "s_cbranch_scc0 FQ27X_%=\n\t"
      "s_waitcnt vmcnt(0)\n\t"
      "s_sleep 4\n\t"
      "global_load_dwordx4 v[152:155], v230, s[58:59] sc1\n\t"
      "global_load_dwordx4 v[128:131], v224, s[60:61] sc1\n\t"
      "global_load_dwordx4 v[132:135], v225, s[60:61] sc1\n\t"
      "global_load_dwordx4 v[136:139], v226, s[60:61] sc1\n\t"
      "global_load_dwordx4 v[140:143], v227, s[60:61] sc1\n\t"
      "global_load_dwordx4 v[144:147], v228, s[60:61] sc1\n\t"
      "s_waitcnt vmcnt(5)\n\t"
      "v_max3_u32 v234, v152, v153, v154\n\t"
      "v_max_u32_e32 v234, v234, v155\n\t"
      "v_cmp_eq_u32_e32 vcc, -1, v234\n\t"
      "s_cbranch_vccnz FQ27L_%=\n\t"
      "FQ27X_%=:\n\t"
      "s_branch FQ27B_%=\n\t"
      "FQ28R_%=:\n\t"
      "s_mov_b32 s51, 0\n\t"
      "FQ28L_%=:\n\t"
      "s_add_u32 s51, s51, 1\n\t"
      "s_cmp_lt_u32 s51, 262144\n\t"
      "s_cbranch_scc0 FQ28X_%=\n\t"
      "s_waitcnt vmcnt(0)\n\t"
      "s_sleep 4\n\t"
      "global_load_dwordx4 v[128:131], v224, s[60:61] sc1\n\t"
      "global_load_dwordx4 v[132:135], v225, s[60:61] sc1\n\t"
      "global_load_dwordx4 v[136:139], v226, s[60:61] sc1\n\t"
      "global_load_dwordx4 v[140:143], v227, s[60:61] sc1\n\t"
      "global_load_dwordx4 v[144:147], v228, s[60:61] sc1\n\t"
      "global_load_dwordx4 v[148:151], v229, s[60:61] sc1\n\t"
      "s_waitcnt vmcnt(5)\n\t"
      "v_max3_u32 v234, v128, v129, v130\n\t"
      "v_max_u32_e32 v234, v234, v131\n\t"
      "v_cmp_eq_u32_e32 vcc, -1, v234\n\t"
      "s_cbranch_vccnz FQ28L_%=\n\t"
      "FQ28X_%=:\n\t"
      "s_branch FQ28B_%=\n\t"
      "NQ3R_%=:\n\t"
      "s_mov_b32 s51, 0\n\t"
      "NQ3L_%=:\n\t"
      "s_add_u32 s51, s51, 1\n\t"
      "s_cmp_lt_u32 s51, 262144\n\t"
      "s_cbranch_scc0 NQ3X_%=\n\t"
      "s_waitcnt vmcnt(0)\n\t"
      "s_sleep 1\n\t"
      "global_load_dwordx4 v[128:131], v224, s[58:59] sc1\n\t"
      "global_load_dwordx4 v[132:135], v225, s[58:59] sc1\n\t"
      "global_load_dwordx4 v[136:139], v226, s[58:59] sc1\n\t"
      "global_load_dwordx4 v[140:143], v227, s[58:59] sc1\n\t"
      "global_load_dwordx4 v[144:147], v228, s[58:59] sc1\n\t"
      "global_load_dwordx4 v[148:151], v229, s[58:59] sc1\n\t"
      "global_load_dwordx4 v[152:155], v230, s[58:59] sc1\n\t"
      "s_waitcnt vmcnt(0)\n\t"
      "v_max3_u32 v234, v128, v129, v130\n\t"
      "v_max3_u32 v234, v234, v131, v132\n\t"
      "v_max3_u32 v234, v234, v133, v134\n\t"
      "v_max3_u32 v234, v234, v135, v136\n\t"
      "v_max3_u32 v234, v234, v137, v138\n\t"
      "v_max3_u32 v234, v234, v139, v140\n\t"
      "v_max3_u32 v234, v234, v141, v142\n\t"
      "v_max3_u32 v234, v234, v143, v144\n\t"
      "v_max3_u32 v234, v234, v145, v146\n\t"
      "v_max3_u32 v234, v234, v147, v148\n\t"
      "v_max3_u32 v234, v234, v149, v150\n\t"
      "v_max3_u32 v234, v234, v151, v152\n\t"
      "v_max3_u32 v234, v234, v153, v154\n\t"
      "v_max_u32_e32 v234, v234, v155\n\t"
      "v_cmp_eq_u32_e32 vcc, -1, v234\n\t"
      "s_cbranch_vccnz NQ3L_%=\n\t"
      "NQ3X_%=:\n\t"
      "v_mfma_f32_16x16x32_bf16 v[200:203], v[128:131], v[0:3], 0\n\t"
      "v_mfma_f32_16x16x32_bf16 v[204:207], v[128:131], v[4:7], 0\n\t"
      "v_mfma_f32_16x16x32_bf16 v[208:211], v[128:131], v[8:11], 0\n\t"
      "v_mfma_f32_16x16x32_bf16 v[212:215], v[128:131], v[12:15], 0\n\t"
      "v_mfma_f32_16x16x32_bf16 v[200:203], v[132:135], v[16:19], v[200:203]\n\t"
      "v_mfma_f32_16x16x32_bf16 v[204:207], v[132:135], v[20:23], v[204:207]\n\t"
      "v_mfma_f32_16x16x32_bf16 v[208:211], v[132:135], v[24:27], v[208:211]\n\t"
      "v_mfma_f32_16x16x32_bf16 v[212:215], v[132:135], v[28:31], v[212:215]\n\t"
      "v_mfma_f32_16x16x32_bf16 v[200:203], v[136:139], v[32:35], v[200:203]\n\t"
      "v_mfma_f32_16x16x32_bf16 v[204:207], v[136:139], v[36:39], v[204:207]\n\t"
      "v_mfma_f32_16x16x32_bf16 v[208:211], v[136:139], v[40:43], v[208:211]\n\t"
      "v_mfma_f32_16x16x32_bf16 v[212:215], v[136:139], v[44:47], v[212:215]\n\t"
      "v_mfma_f32_16x16x32_bf16 v[200:203], v[140:143], v[48:51], v[200:203]\n\t"
      "v_mfma_f32_16x16x32_bf16 v[204:207], v[140:143], v[52:55], v[204:207]\n\t"
      "v_mfma_f32_16x16x32_bf16 v[208:211], v[140:143], v[56:59], v[208:211]\n\t"
      "v_mfma_f32_16x16x32_bf16 v[212:215], v[140:143], v[60:63], v[212:215]\n\t"
      "v_mfma_f32_16x16x32_bf16 v[200:203], v[144:147], v[64:67], v[200:203]\n\t"
      "v_mfma_f32_16x16x32_bf16 v[204:207], v[144:147], v[68:71], v[204:207]\n\t"
      "v_mfma_f32_16x16x32_bf16 v[208:211], v[144:147], v[72:75], v[208:211]\n\t"
      "v_mfma_f32_16x16x32_bf16 v[212:215], v[144:147], v[76:79], v[212:215]\n\t"
      "v_mfma_f32_16x16x32_bf16 v[200:203], v[148:151], v[80:83], v[200:203]\n\t"
      "v_mfma_f32_16x16x32_bf16 v[204:207], v[148:151], v[84:87], v[204:207]\n\t"
      "v_mfma_f32_16x16x32_bf16 v[208:211], v[148:151], v[88:91], v[208:211]\n\t"
      "v_mfma_f32_16x16x32_bf16 v[212:215], v[148:151], v[92:95], v[212:215]\n\t"
      "v_mfma_f32_16x16x32_bf16 v[200:203], v[152:155], v[96:99], v[200:203]\n\t"
      "v_mfma_f32_16x16x32_bf16 v[204:207], v[152:155], v[100:103], v[204:207]\n\t"
      "v_mfma_f32_16x16x32_bf16 v[208:211], v[152:155], v[104:107], v[208:211]\n\t"
      "v_mfma_f32_16x16x32_bf16 v[212:215], v[152:155], v[108:111], v[212:215]\n\t"
      "global_load_dwordx4 v[128:131], v224, s[60:61] sc1\n\t"
      "global_load_dwordx4 v[132:135], v225, s[60:61] sc1\n\t"
      "global_load_dwordx4 v[136:139], v226, s[60:61] sc1\n\t"
      "global_load_dwordx4 v[140:143], v227, s[60:61] sc1\n\t"
      "global_load_dwordx4 v[144:147], v228, s[60:61] sc1\n\t"
      "global_load_dwordx4 v[148:151], v229, s[60:61] sc1\n\t"
      "global_load_dwordx4 v[152:155], v230, s[60:61] sc1\n\t"
      "s_waitcnt vmcnt(0)\n\t"
      "s_nop 7\n\t"
      "s_branch NQ3B_%=\n\t"
      "FQ29R_%=:\n\t"
      "s_mov_b32 s51, 0\n\t"
      "FQ29L_%=:\n\t"
      "s_add_u32 s51, s51, 1\n\t"
      "s_cmp_lt_u32 s51, 262144\n\t"
      "s_cbranch_scc0 FQ29X_%=\n\t"
      "s_waitcnt vmcnt(0)\n\t"
      "s_sleep 4\n\t"
      "global_load_dwordx4 v[132:135], v225, s[60:61] sc1\n\t"
      "global_load_dwordx4 v[136:139], v226, s[60:61] sc1\n\t"
      "global_load_dwordx4 v[140:143], v227, s[60:61] sc1\n\t"
      "global_load_dwordx4 v[144:147], v228, s[60:61] sc1\n\t"
      "global_load_dwordx4 v[148:151], v229, s[60:61] sc1\n\t"
      "global_load_dwordx4 v[152:155], v230, s[60:61] sc1\n\t"
      "s_waitcnt vmcnt(5)\n\t"
      "v_max3_u32 v234, v132, v133, v134\n\t"
      "v_max_u32_e32 v234, v234, v135\n\t"
      "v_cmp_eq_u32_e32 vcc, -1, v234\n\t"
      "s_cbranch_vccnz FQ29L_%=\n\t"
      "FQ29X_%=:\n\t"
      "s_branch FQ29B_%=\n\t"
      "FQ30R_%=:\n\t"
      "s_mov_b32 s51, 0\n\t"
      "FQ30L_%=:\n\t"
      "s_add_u32 s51, s51, 1\n\t"
      "s_cmp_lt_u32 s51, 262144\n\t"
      "s_cbranch_scc0 FQ30X_%=\n\t"
      "s_waitcnt vmcnt(0)\n\t"
      "s_sleep 4\n\t"
      "global_load_dwordx4 v[136:139], v226, s[60:61] sc1\n\t"
      "global_load_dwordx4 v[140:143], v227, s[60:61] sc1\n\t"
      "global_load_dwordx4 v[144:147], v228, s[60:61] sc1\n\t"
      "global_load_dwordx4 v[148:151], v229, s[60:61] sc1\n\t"
      "global_load_dwordx4 v[152:155], v230, s[60:61] sc1\n\t"
      "global_load_dwordx4 v[128:131], v224, s[62:63] sc1\n\t"
      "s_waitcnt vmcnt(5)\n\t"
      "v_max3_u32 v234, v136, v137, v138\n\t"
      "v_max_u32_e32 v234, v234, v139\n\t"
      "v_cmp_eq_u32_e32 vcc, -1, v234\n\t"
      "s_cbranch_vccnz FQ30L_%=\n\t"
      "FQ30X_%=:\n\t"
      "s_branch FQ30B_%=\n\t"
      "FQ31R_%=:\n\t"
      "s_mov_b32 s51, 0\n\t"
      "FQ31L_%=:\n\t"
      "s_add_u32 s51, s51, 1\n\t"
      "s_cmp_lt_u32 s51, 262144\n\t"
      "s_cbranch_scc0 FQ31X_%=\n\t"
      "s_waitcnt vmcnt(0)\n\t"
      "s_sleep 4\n\t"
      "global_load_dwordx4 v[140:143], v227, s[60:61] sc1\n\t"
      "global_load_dwordx4 v[144:147], v228, s[60:61] sc1\n\t"
      "global_load_dwordx4 v[148:151], v229, s[60:61] sc1\n\t"
      "global_load_dwordx4 v[152:155], v230, s[60:61] sc1\n\t"
      "global_load_dwordx4 v[128:131], v224, s[62:63] sc1\n\t"
      "global_load_dwordx4 v[132:135], v225, s[62:63] sc1\n\t"
      "s_waitcnt vmcnt(5)\n\t"
      "v_max3_u32 v234, v140, v141, v142\n\t"
      "v_max_u32_e32 v234, v234, v143\n\t"
      "v_cmp_eq_u32_e32 vcc, -1, v234\n\t"
      "s_cbranch_vccnz FQ31L_%=\n\t"
      "FQ31X_%=:\n\t"
      "s_branch FQ31B_%=\n\t"
      "FQ32R_%=:\n\t"
      "s_mov_b32 s51, 0\n\t"
      "FQ32L_%=:\n\t"
      "s_add_u32 s51, s51, 1\n\t"
      "s_cmp_lt_u32 s51, 262144\n\t"
      "s_cbranch_scc0 FQ32X_%=\n\t"
      "s_waitcnt vmcnt(0)\n\t"
      "s_sleep 4\n\t"
      "global_load_dwordx4 v[144:147], v228, s[60:61] sc1\n\t"
      "global_load_dwordx4 v[148:151], v229, s[60:61] sc1\n\t"
      "global_load_dwordx4 v[152:155], v230, s[60:61] sc1\n\t"
      "global_load_dwordx4 v[128:131], v224, s[62:63] sc1\n\t"
      "global_load_dwordx4 v[132:135], v225, s[62:63] sc1\n\t"
      "global_load_dwordx4 v[136:139], v226, s[62:63] sc1\n\t"
      "s_waitcnt vmcnt(5)\n\t"
      "v_max3_u32 v234, v144, v145, v146\n\t"
      "v_max_u32_e32 v234, v234, v147\n\t"
      "v_cmp_eq_u32_e32 vcc, -1, v234\n\t"
      "s_cbranch_vccnz FQ32L_%=\n\t"
      "FQ32X_%=:\n\t"
      "s_branch FQ32B_%=\n\t"
      "FQ33R_%=:\n\t"
      "s_mov_b32 s51, 0\n\t"
      "FQ33L_%=:\n\t"
      "s_add_u32 s51, s51, 1\n\t"
      "s_cmp_lt_u32 s51, 262144\n\t"
      "s_cbranch_scc0 FQ33X_%=\n\t"
      "s_waitcnt vmcnt(0)\n\t"
      "s_sleep 4\n\t"
      "global_load_dwordx4 v[148:151], v229, s[60:61] sc1\n\t"
      "global_load_dwordx4 v[152:155], v230, s[60:61] sc1\n\t"
      "global_load_dwordx4 v[128:131], v224, s[62:63] sc1\n\t"
      "global_load_dwordx4 v[132:135], v225, s[62:63] sc1\n\t"
      "global_load_dwordx4 v[136:139], v226, s[62:63] sc1\n\t"
      "global_load_dwordx4 v[140:143], v227, s[62:63] sc1\n\t"
      "s_waitcnt vmcnt(5)\n\t"
      "v_max3_u32 v234, v148, v149, v150\n\t"
      "v_max_u32_e32 v234, v234, v151\n\t"
      "v_cmp_eq_u32_e32 vcc, -1, v234\n\t"
      "s_cbranch_vccnz FQ33L_%=\n\t"
      "FQ33X_%=:\n\t"
      "s_branch FQ33B_%=\n\t"
      "FQ34R_%=:\n\t"
      "s_mov_b32 s51, 0\n\t"
      "FQ34L_%=:\n\t"
      "s_add_u32 s51, s51, 1\n\t"
      "s_cmp_lt_u32 s51, 262144\n\t"
      "s_cbranch_scc0 FQ34X_%=\n\t"
      "s_waitcnt vmcnt(0)\n\t"
      "s_sleep 4\n\t"
      "global_load_dwordx4 v[152:155], v230, s[60:61] sc1\n\t"
      "global_load_dwordx4 v[128:131], v224, s[62:63] sc1\n\t"
      "global_load_dwordx4 v[132:135], v225, s[62:63] sc1\n\t"
      "global_load_dwordx4 v[136:139], v226, s[62:63] sc1\n\t"
      "global_load_dwordx4 v[140:143], v227, s[62:63] sc1\n\t"
      "global_load_dwordx4 v[144:147], v228, s[62:63] sc1\n\t"
      "s_waitcnt vmcnt(5)\n\t"
      "v_max3_u32 v234, v152, v153, v154\n\t"
      "v_max_u32_e32 v234, v234, v155\n\t"
      "v_cmp_eq_u32_e32 vcc, -1, v234\n\t"
      "s_cbranch_vccnz FQ34L_%=\n\t"
      "FQ34X_%=:\n\t"
      "s_branch FQ34B_%=\n\t"
      "FQ35R_%=:\n\t"
      "s_mov_b32 s51, 0\n\t"
      "FQ35L_%=:\n\t"
      "s_add_u32 s51, s51, 1\n\t"
      "s_cmp_lt_u32 s51, 262144\n\t"
      "s_cbranch_scc0 FQ35X_%=\n\t"
      "s_waitcnt vmcnt(0)\n\t"
      "s_sleep 4\n\t"
      "global_load_dwordx4 v[128:131], v224, s[62:63] sc1\n\t"
      "global_load_dwordx4 v[132:135], v225, s[62:63] sc1\n\t"
      "global_load_dwordx4 v[136:139], v226, s[62:63] sc1\n\t"
      "global_load_dwordx4 v[140:143], v227, s[62:63] sc1\n\t"
      "global_load_dwordx4 v[144:147], v228, s[62:63] sc1\n\t"
      "global_load_dwordx4 v[148:151], v229, s[62:63] sc1\n\t"
      "s_waitcnt vmcnt(5)\n\t"
      "v_max3_u32 v234, v128, v129, v130\n\t"
      "v_max_u32_e32 v234, v234, v131\n\t"
      "v_cmp_eq_u32_e32 vcc, -1, v234\n\t"
      "s_cbranch_vccnz FQ35L_%=\n\t"
      "FQ35X_%=:\n\t"
      "s_branch FQ35B_%=\n\t"
      "NQ4R_%=:\n\t"
      "s_mov_b32 s51, 0\n\t"
      "NQ4L_%=:\n\t"
      "s_add_u32 s51, s51, 1\n\t"
      "s_cmp_lt_u32 s51, 262144\n\t"
      "s_cbranch_scc0 NQ4X_%=\n\t"
      "s_waitcnt vmcnt(0)\n\t"
      "s_sleep 1\n\t"
      "global_load_dwordx4 v[128:131], v224, s[60:61] sc1\n\t"
      "global_load_dwordx4 v[132:135], v225, s[60:61] sc1\n\t"
      "global_load_dwordx4 v[136:139], v226, s[60:61] sc1\n\t"
      "global_load_dwordx4 v[140:143], v227, s[60:61] sc1\n\t"
      "global_load_dwordx4 v[144:147], v228, s[60:61] sc1\n\t"
      "global_load_dwordx4 v[148:151], v229, s[60:61] sc1\n\t"
      "global_load_dwordx4 v[152:155], v230, s[60:61] sc1\n\t"
      "s_waitcnt vmcnt(0)\n\t"
      "v_max3_u32 v234, v128, v129, v130\n\t"
      "v_max3_u32 v234, v234, v131, v132\n\t"
      "v_max3_u32 v234, v234, v133, v134\n\t"
      "v_max3_u32 v234, v234, v135, v136\n\t"
      "v_max3_u32 v234, v234, v137, v138\n\t"
      "v_max3_u32 v234, v234, v139, v140\n\t"
      "v_max3_u32 v234, v234, v141, v142\n\t"
      "v_max3_u32 v234, v234, v143, v144\n\t"
      "v_max3_u32 v234, v234, v145, v146\n\t"
      "v_max3_u32 v234, v234, v147, v148\n\t"
      "v_max3_u32 v234, v234, v149, v150\n\t"
      "v_max3_u32 v234, v234, v151, v152\n\t"
      "v_max3_u32 v234, v234, v153, v154\n\t"
      "v_max_u32_e32 v234, v234, v155\n\t"
      "v_cmp_eq_u32_e32 vcc, -1, v234\n\t"
      "s_cbranch_vccnz NQ4L_%=\n\t"
      "NQ4X_%=:\n\t"
      "v_mfma_f32_16x16x32_bf16 v[184:187], v[128:131], v[0:3], 0\n\t"
      "v_mfma_f32_16x16x32_bf16 v[188:191], v[128:131], v[4:7], 0\n\t"
      "v_mfma_f32_16x16x32_bf16 v[192:195], v[128:131], v[8:11], 0\n\t"
      "v_mfma_f32_16x16x32_bf16 v[196:199], v[128:131], v[12:15], 0\n\t"
      "v_mfma_f32_16x16x32_bf16 v[184:187], v[132:135], v[16:19], v[184:187]\n\t"
      "v_mfma_f32_16x16x32_bf16 v[188:191], v[132:135], v[20:23], v[188:191]\n\t"
      "v_mfma_f32_16x16x32_bf16 v[192:195], v[132:135], v[24:27], v[192:195]\n\t"
      "v_mfma_f32_16x16x32_bf16 v[196:199], v[132:135], v[28:31], v[196:199]\n\t"
      "v_mfma_f32_16x16x32_bf16 v[184:187], v[136:139], v[32:35], v[184:187]\n\t"
      "v_mfma_f32_16x16x32_bf16 v[188:191], v[136:139], v[36:39], v[188:191]\n\t"
      "v_mfma_f32_16x16x32_bf16 v[192:195], v[136:139], v[40:43], v[192:195]\n\t"
      "v_mfma_f32_16x16x32_bf16 v[196:199], v[136:139], v[44:47], v[196:199]\n\t"
      "v_mfma_f32_16x16x32_bf16 v[184:187], v[140:143], v[48:51], v[184:187]\n\t"
      "v_mfma_f32_16x16x32_bf16 v[188:191], v[140:143], v[52:55], v[188:191]\n\t"
      "v_mfma_f32_16x16x32_bf16 v[192:195], v[140:143], v[56:59], v[192:195]\n\t"
      "v_mfma_f32_16x16x32_bf16 v[196:199], v[140:143], v[60:63], v[196:199]\n\t"
      "v_mfma_f32_16x16x32_bf16 v[184:187], v[144:147], v[64:67], v[184:187]\n\t"
      "v_mfma_f32_16x16x32_bf16 v[188:191], v[144:147], v[68:71], v[188:191]\n\t"
      "v_mfma_f32_16x16x32_bf16 v[192:195], v[144:147], v[72:75], v[192:195]\n\t"
      "v_mfma_f32_16x16x32_bf16 v[196:199], v[144:147], v[76:79], v[196:199]\n\t"
      "v_mfma_f32_16x16x32_bf16 v[184:187], v[148:151], v[80:83], v[184:187]\n\t"
      "v_mfma_f32_16x16x32_bf16 v[188:191], v[148:151], v[84:87], v[188:191]\n\t"
      "v_mfma_f32_16x16x32_bf16 v[192:195], v[148:151], v[88:91], v[192:195]\n\t"
      "v_mfma_f32_16x16x32_bf16 v[196:199], v[148:151], v[92:95], v[196:199]\n\t"
      "v_mfma_f32_16x16x32_bf16 v[184:187], v[152:155], v[96:99], v[184:187]\n\t"
      "v_mfma_f32_16x16x32_bf16 v[188:191], v[152:155], v[100:103], v[188:191]\n\t"
      "v_mfma_f32_16x16x32_bf16 v[192:195], v[152:155], v[104:107], v[192:195]\n\t"
      "v_mfma_f32_16x16x32_bf16 v[196:199], v[152:155], v[108:111], v[196:199]\n\t"
      "global_load_dwordx4 v[128:131], v224, s[62:63] sc1\n\t"
      "global_load_dwordx4 v[132:135], v225, s[62:63] sc1\n\t"
      "global_load_dwordx4 v[136:139], v226, s[62:63] sc1\n\t"
      "global_load_dwordx4 v[140:143], v227, s[62:63] sc1\n\t"
      "global_load_dwordx4 v[144:147], v228, s[62:63] sc1\n\t"
      "global_load_dwordx4 v[148:151], v229, s[62:63] sc1\n\t"
      "global_load_dwordx4 v[152:155], v230, s[62:63] sc1\n\t"
      "s_waitcnt vmcnt(0)\n\t"
      "s_nop 7\n\t"
      "s_branch NQ4B_%=\n\t"
      "FQ36R_%=:\n\t"
      "s_mov_b32 s51, 0\n\t"
      "FQ36L_%=:\n\t"
      "s_add_u32 s51, s51, 1\n\t"
      "s_cmp_lt_u32 s51, 262144\n\t"
      "s_cbranch_scc0 FQ36X_%=\n\t"
      "s_waitcnt vmcnt(0)\n\t"
      "s_sleep 4\n\t"
      "global_load_dwordx4 v[132:135], v225, s[62:63] sc1\n\t"
      "global_load_dwordx4 v[136:139], v226, s[62:63] sc1\n\t"
      "global_load_dwordx4 v[140:143], v227, s[62:63] sc1\n\t"
      "global_load_dwordx4 v[144:147], v228, s[62:63] sc1\n\t"
      "global_load_dwordx4 v[148:151], v229, s[62:63] sc1\n\t"
      "global_load_dwordx4 v[152:155], v230, s[62:63] sc1\n\t"
      "s_waitcnt vmcnt(5)\n\t"
      "v_max3_u32 v234, v132, v133, v134\n\t"
      "v_max_u32_e32 v234, v234, v135\n\t"
      "v_cmp_eq_u32_e32 vcc, -1, v234\n\t"
      "s_cbranch_vccnz FQ36L_%=\n\t"
      "FQ36X_%=:\n\t"
      "s_branch FQ36B_%=\n\t"
      "FQ37R_%=:\n\t"
      "s_mov_b32 s51, 0\n\t"
      "FQ37L_%=:\n\t"
      "s_add_u32 s51, s51, 1\n\t"
      "s_cmp_lt_u32 s51, 262144\n\t"
      "s_cbranch_scc0 FQ37X_%=\n\t"
      "s_waitcnt vmcnt(0)\n\t"
      "s_sleep 4\n\t"
      "global_load_dwordx4 v[136:139], v226, s[62:63] sc1\n\t"
      "global_load_dwordx4 v[140:143], v227, s[62:63] sc1\n\t"
      "global_load_dwordx4 v[144:147], v228, s[62:63] sc1\n\t"
      "global_load_dwordx4 v[148:151], v229, s[62:63] sc1\n\t"
      "global_load_dwordx4 v[152:155], v230, s[62:63] sc1\n\t"
      "global_load_dwordx4 v[128:131], v224, s[64:65] sc1\n\t"
      "s_waitcnt vmcnt(5)\n\t"
      "v_max3_u32 v234, v136, v137, v138\n\t"
      "v_max_u32_e32 v234, v234, v139\n\t"
      "v_cmp_eq_u32_e32 vcc, -1, v234\n\t"
      "s_cbranch_vccnz FQ37L_%=\n\t"
      "FQ37X_%=:\n\t"
      "s_branch FQ37B_%=\n\t"
      "FQ38R_%=:\n\t"
      "s_mov_b32 s51, 0\n\t"
      "FQ38L_%=:\n\t"
      "s_add_u32 s51, s51, 1\n\t"
      "s_cmp_lt_u32 s51, 262144\n\t"
      "s_cbranch_scc0 FQ38X_%=\n\t"
      "s_waitcnt vmcnt(0)\n\t"
      "s_sleep 4\n\t"
      "global_load_dwordx4 v[140:143], v227, s[62:63] sc1\n\t"
      "global_load_dwordx4 v[144:147], v228, s[62:63] sc1\n\t"
      "global_load_dwordx4 v[148:151], v229, s[62:63] sc1\n\t"
      "global_load_dwordx4 v[152:155], v230, s[62:63] sc1\n\t"
      "global_load_dwordx4 v[128:131], v224, s[64:65] sc1\n\t"
      "global_load_dwordx4 v[132:135], v225, s[64:65] sc1\n\t"
      "s_waitcnt vmcnt(5)\n\t"
      "v_max3_u32 v234, v140, v141, v142\n\t"
      "v_max_u32_e32 v234, v234, v143\n\t"
      "v_cmp_eq_u32_e32 vcc, -1, v234\n\t"
      "s_cbranch_vccnz FQ38L_%=\n\t"
      "FQ38X_%=:\n\t"
      "s_branch FQ38B_%=\n\t"
      "FQ39R_%=:\n\t"
      "s_mov_b32 s51, 0\n\t"
      "FQ39L_%=:\n\t"
      "s_add_u32 s51, s51, 1\n\t"
      "s_cmp_lt_u32 s51, 262144\n\t"
      "s_cbranch_scc0 FQ39X_%=\n\t"
      "s_waitcnt vmcnt(0)\n\t"
      "s_sleep 4\n\t"
      "global_load_dwordx4 v[144:147], v228, s[62:63] sc1\n\t"
      "global_load_dwordx4 v[148:151], v229, s[62:63] sc1\n\t"
      "global_load_dwordx4 v[152:155], v230, s[62:63] sc1\n\t"
      "global_load_dwordx4 v[128:131], v224, s[64:65] sc1\n\t"
      "global_load_dwordx4 v[132:135], v225, s[64:65] sc1\n\t"
      "global_load_dwordx4 v[136:139], v226, s[64:65] sc1\n\t"
      "s_waitcnt vmcnt(5)\n\t"
      "v_max3_u32 v234, v144, v145, v146\n\t"
      "v_max_u32_e32 v234, v234, v147\n\t"
      "v_cmp_eq_u32_e32 vcc, -1, v234\n\t"
      "s_cbranch_vccnz FQ39L_%=\n\t"
      "FQ39X_%=:\n\t"
      "s_branch FQ39B_%=\n\t"
      "FQ40R_%=:\n\t"
      "s_mov_b32 s51, 0\n\t"
      "FQ40L_%=:\n\t"
      "s_add_u32 s51, s51, 1\n\t"
      "s_cmp_lt_u32 s51, 262144\n\t"
      "s_cbranch_scc0 FQ40X_%=\n\t"
      "s_waitcnt vmcnt(0)\n\t"
      "s_sleep 4\n\t"
      "global_load_dwordx4 v[148:151], v229, s[62:63] sc1\n\t"
      "global_load_dwordx4 v[152:155], v230, s[62:63] sc1\n\t"
      "global_load_dwordx4 v[128:131], v224, s[64:65] sc1\n\t"
      "global_load_dwordx4 v[132:135], v225, s[64:65] sc1\n\t"
      "global_load_dwordx4 v[136:139], v226, s[64:65] sc1\n\t"
      "global_load_dwordx4 v[140:143], v227, s[64:65] sc1\n\t"
      "s_waitcnt vmcnt(5)\n\t"
      "v_max3_u32 v234, v148, v149, v150\n\t"
      "v_max_u32_e32 v234, v234, v151\n\t"
      "v_cmp_eq_u32_e32 vcc, -1, v234\n\t"
      "s_cbranch_vccnz FQ40L_%=\n\t"
      "FQ40X_%=:\n\t"
      "s_branch FQ40B_%=\n\t"
      "FQ41R_%=:\n\t"
      "s_mov_b32 s51, 0\n\t"
      "FQ41L_%=:\n\t"
      "s_add_u32 s51, s51, 1\n\t"
      "s_cmp_lt_u32 s51, 262144\n\t"
      "s_cbranch_scc0 FQ41X_%=\n\t"
      "s_waitcnt vmcnt(0)\n\t"
      "s_sleep 4\n\t"
      "global_load_dwordx4 v[152:155], v230, s[62:63] sc1\n\t"
      "global_load_dwordx4 v[128:131], v224, s[64:65] sc1\n\t"
      "global_load_dwordx4 v[132:135], v225, s[64:65] sc1\n\t"
      "global_load_dwordx4 v[136:139], v226, s[64:65] sc1\n\t"
      "global_load_dwordx4 v[140:143], v227, s[64:65] sc1\n\t"
      "global_load_dwordx4 v[144:147], v228, s[64:65] sc1\n\t"
      "s_waitcnt vmcnt(5)\n\t"
      "v_max3_u32 v234, v152, v153, v154\n\t"
      "v_max_u32_e32 v234, v234, v155\n\t"
      "v_cmp_eq_u32_e32 vcc, -1, v234\n\t"
      "s_cbranch_vccnz FQ41L_%=\n\t"
      "FQ41X_%=:\n\t"
      "s_branch FQ41B_%=\n\t"
      "FQ42R_%=:\n\t"
      "s_mov_b32 s51, 0\n\t"
      "FQ42L_%=:\n\t"
      "s_add_u32 s51, s51, 1\n\t"
      "s_cmp_lt_u32 s51, 262144\n\t"
      "s_cbranch_scc0 FQ42X_%=\n\t"
      "s_sleep 1\n\t"
      "s_mov_b64 exec, 15\n\t"
      "v_mov_b64_e32 v[232:233], 0\n\t"
      "global_atomic_or_x2 v[232:233], v247, v[232:233], s[12:13] sc0 sc1\n\t"
      "s_waitcnt vmcnt(0)\n\t"
      "v_subrev_u32_e32 v232, s14, v232\n\t"
      "v_cmp_ne_u32_e64 s[48:49], s15, v233\n\t"
      "v_subrev_u32_e32 v232, 6, v232\n\t"
      "v_cmp_lt_u32_e32 vcc, 7, v232\n\t"
      "s_mov_b64 exec, -1\n\t"
      "s_or_b64 vcc, vcc, s[48:49]\n\t"
      "s_cbranch_vccnz FQ42L_%=\n\t"
      "FQ42X_%=:\n\t"
      "FQ42DR_%=:\n\t"
      "s_mov_b32 s51, 0\n\t"
      "FQ42DL_%=:\n\t"
      "s_add_u32 s51, s51, 1\n\t"
      "s_cmp_lt_u32 s51, 262144\n\t"
      "s_cbranch_scc0 FQ42DX_%=\n\t"
      "s_waitcnt vmcnt(0)\n\t"
      "s_sleep 1\n\t"
      "global_load_dwordx4 v[128:131], v224, s[64:65] sc1\n\t"
      "global_load_dwordx4 v[132:135], v225, s[64:65] sc1\n\t"
      "global_load_dwordx4 v[136:139], v226, s[64:65] sc1\n\t"
      "global_load_dwordx4 v[140:143], v227, s[64:65] sc1\n\t"
      "global_load_dwordx4 v[144:147], v228, s[64:65] sc1\n\t"
      "global_load_dwordx4 v[148:151], v229, s[64:65] sc1\n\t"
      "s_waitcnt vmcnt(5)\n\t"
      "v_max3_u32 v234, v128, v129, v130\n\t"
      "v_max_u32_e32 v234, v234, v131\n\t"
      "v_cmp_eq_u32_e32 vcc, -1, v234\n\t"
      "s_cbranch_vccnz FQ42DL_%=\n\t"
      "FQ42DX_%=:\n\t"
      "s_branch FQ42B_%=\n\t"
      "NQ5R_%=:\n\t"
      "s_mov_b32 s51, 0\n\t"
      "NQ5L_%=:\n\t"
      "s_add_u32 s51, s51, 1\n\t"
      "s_cmp_lt_u32 s51, 262144\n\t"
      "s_cbranch_scc0 NQ5X_%=\n\t"
      "s_waitcnt vmcnt(0)\n\t"
      "s_sleep 1\n\t"
      "global_load_dwordx4 v[128:131], v224, s[62:63] sc1\n\t"
      "global_load_dwordx4 v[132:135], v225, s[62:63] sc1\n\t"
      "global_load_dwordx4 v[136:139], v226, s[62:63] sc1\n\t"
      "global_load_dwordx4 v[140:143], v227, s[62:63] sc1\n\t"
      "global_load_dwordx4 v[144:147], v228, s[62:63] sc1\n\t"
      "global_load_dwordx4 v[148:151], v229, s[62:63] sc1\n\t"
      "global_load_dwordx4 v[152:155], v230, s[62:63] sc1\n\t"
      "s_waitcnt vmcnt(0)\n\t"
      "s_nop 0\n\t"
      "v_max3_u32 v234, v128, v129, v130\n\t"
      "v_max3_u32 v234, v234, v131, v132\n\t"
      "v_max3_u32 v234, v234, v133, v134\n\t"
      "v_max3_u32 v234, v234, v135, v136\n\t"
      "v_max3_u32 v234, v234, v137, v138\n\t"
      "v_max3_u32 v234, v234, v139, v140\n\t"
      "v_max3_u32 v234, v234, v141, v142\n\t"
      "v_max3_u32 v234, v234, v143, v144\n\t"
      "v_max3_u32 v234, v234, v145, v146\n\t"
      "v_max3_u32 v234, v234, v147, v148\n\t"
      "v_max3_u32 v234, v234, v149, v150\n\t"
      "v_max3_u32 v234, v234, v151, v152\n\t"
      "v_max3_u32 v234, v234, v153, v154\n\t"
      "v_max_u32_e32 v234, v234, v155\n\t"
      "v_cmp_eq_u32_e32 vcc, -1, v234\n\t"
      "s_cbranch_vccnz NQ5L_%=\n\t"
      "NQ5X_%=:\n\t"
      "v_mfma_f32_16x16x32_bf16 v[200:203], v[128:131], v[0:3], 0\n\t"
      "v_mfma_f32_16x16x32_bf16 v[204:207], v[128:131], v[4:7], 0\n\t"
      "v_mfma_f32_16x16x32_bf16 v[208:211], v[128:131], v[8:11], 0\n\t"
      "v_mfma_f32_16x16x32_bf16 v[212:215], v[128:131], v[12:15], 0\n\t"
      "v_mfma_f32_16x16x32_bf16 v[200:203], v[132:135], v[16:19], v[200:203]\n\t"
      "v_mfma_f32_16x16x32_bf16 v[204:207], v[132:135], v[20:23], v[204:207]\n\t"
      "v_mfma_f32_16x16x32_bf16 v[208:211], v[132:135], v[24:27], v[208:211]\n\t"
      "v_mfma_f32_16x16x32_bf16 v[212:215], v[132:135], v[28:31], v[212:215]\n\t"
      "v_mfma_f32_16x16x32_bf16 v[200:203], v[136:139], v[32:35], v[200:203]\n\t"
      "v_mfma_f32_16x16x32_bf16 v[204:207], v[136:139], v[36:39], v[204:207]\n\t"
      "v_mfma_f32_16x16x32_bf16 v[208:211], v[136:139], v[40:43], v[208:211]\n\t"
      "v_mfma_f32_16x16x32_bf16 v[212:215], v[136:139], v[44:47], v[212:215]\n\t"
      "v_mfma_f32_16x16x32_bf16 v[200:203], v[140:143], v[48:51], v[200:203]\n\t"
      "v_mfma_f32_16x16x32_bf16 v[204:207], v[140:143], v[52:55], v[204:207]\n\t"
      "v_mfma_f32_16x16x32_bf16 v[208:211], v[140:143], v[56:59], v[208:211]\n\t"
      "v_mfma_f32_16x16x32_bf16 v[212:215], v[140:143], v[60:63], v[212:215]\n\t"
      "v_mfma_f32_16x16x32_bf16 v[200:203], v[144:147], v[64:67], v[200:203]\n\t"
      "v_mfma_f32_16x16x32_bf16 v[204:207], v[144:147], v[68:71], v[204:207]\n\t"
      "v_mfma_f32_16x16x32_bf16 v[208:211], v[144:147], v[72:75], v[208:211]\n\t"
      "v_mfma_f32_16x16x32_bf16 v[212:215], v[144:147], v[76:79], v[212:215]\n\t"
      "v_mfma_f32_16x16x32_bf16 v[200:203], v[148:151], v[80:83], v[200:203]\n\t"
      "v_mfma_f32_16x16x32_bf16 v[204:207], v[148:151], v[84:87], v[204:207]\n\t"
      "v_mfma_f32_16x16x32_bf16 v[208:211], v[148:151], v[88:91], v[208:211]\n\t"
      "v_mfma_f32_16x16x32_bf16 v[212:215], v[148:151], v[92:95], v[212:215]\n\t"
      "v_mfma_f32_16x16x32_bf16 v[200:203], v[152:155], v[96:99], v[200:203]\n\t"
      "v_mfma_f32_16x16x32_bf16 v[204:207], v[152:155], v[100:103], v[204:207]\n\t"
      "v_mfma_f32_16x16x32_bf16 v[208:211], v[152:155], v[104:107], v[208:211]\n\t"
      "v_mfma_f32_16x16x32_bf16 v[212:215], v[152:155], v[108:111], v[212:215]\n\t"
      "global_load_dwordx4 v[128:131], v224, s[64:65] sc1\n\t"
      "global_load_dwordx4 v[132:135], v225, s[64:65] sc1\n\t"
      "global_load_dwordx4 v[136:139], v226, s[64:65] sc1\n\t"
      "global_load_dwordx4 v[140:143], v227, s[64:65] sc1\n\t"
      "global_load_dwordx4 v[144:147], v228, s[64:65] sc1\n\t"
      "global_load_dwordx4 v[148:151], v229, s[64:65] sc1\n\t"
      "global_load_dwordx4 v[152:155], v230, s[64:65] sc1\n\t"
      "s_waitcnt vmcnt(0)\n\t"
      "s_nop 7\n\t"
      "s_branch NQ5B_%=\n\t"
      "FQ43R_%=:\n\t"
      "s_mov_b32 s51, 0\n\t"
      "FQ43L_%=:\n\t"
      "s_add_u32 s51, s51, 1\n\t"
      "s_cmp_lt_u32 s51, 262144\n\t"
      "s_cbranch_scc0 FQ43X_%=\n\t"
      "s_sleep 1\n\t"
      "s_mov_b64 exec, 15\n\t"
      "v_mov_b64_e32 v[232:233], 0\n\t"
      "global_atomic_or_x2 v[232:233], v247, v[232:233], s[12:13] sc0 sc1\n\t"
      "s_waitcnt vmcnt(0)\n\t"
      "v_subrev_u32_e32 v232, s14, v232\n\t"
      "v_cmp_ne_u32_e64 s[48:49], s15, v233\n\t"
      "v_subrev_u32_e32 v232, 6, v232\n\t"
      "v_cmp_lt_u32_e32 vcc, 7, v232\n\t"
      "s_mov_b64 exec, -1\n\t"
      "s_or_b64 vcc, vcc, s[48:49]\n\t"
      "s_cbranch_vccnz FQ43L_%=\n\t"
      "FQ43X_%=:\n\t"
      "FQ43DR_%=:\n\t"
      "s_mov_b32 s51, 0\n\t"
      "FQ43DL_%=:\n\t"
      "s_add_u32 s51, s51, 1\n\t"
      "s_cmp_lt_u32 s51, 262144\n\t"
      "s_cbranch_scc0 FQ43DX_%=\n\t"
      "s_waitcnt vmcnt(0)\n\t"
      "s_sleep 1\n\t"
      "global_load_dwordx4 v[132:135], v225, s[64:65] sc1\n\t"
      "global_load_dwordx4 v[136:139], v226, s[64:65] sc1\n\t"
      "global_load_dwordx4 v[140:143], v227, s[64:65] sc1\n\t"
      "global_load_dwordx4 v[144:147], v228, s[64:65] sc1\n\t"
      "global_load_dwordx4 v[148:151], v229, s[64:65] sc1\n\t"
      "global_load_dwordx4 v[152:155], v230, s[64:65] sc1\n\t"
      "s_waitcnt vmcnt(5)\n\t"
      "v_max3_u32 v234, v132, v133, v134\n\t"
      "v_max_u32_e32 v234, v234, v135\n\t"
      "v_cmp_eq_u32_e32 vcc, -1, v234\n\t"
      "s_cbranch_vccnz FQ43DL_%=\n\t"
      "FQ43DX_%=:\n\t"
      "s_branch FQ43B_%=\n\t"
      "FQ44R_%=:\n\t"
      "s_mov_b32 s51, 0\n\t"
      "FQ44L_%=:\n\t"
      "s_add_u32 s51, s51, 1\n\t"
      "s_cmp_lt_u32 s51, 262144\n\t"
      "s_cbranch_scc0 FQ44X_%=\n\t"
      "s_sleep 1\n\t"
      "s_mov_b64 exec, 15\n\t"
      "v_mov_b64_e32 v[232:233], 0\n\t"
      "global_atomic_or_x2 v[232:233], v247, v[232:233], s[12:13] sc0 sc1\n\t"
      "s_waitcnt vmcnt(0)\n\t"
      "v_subrev_u32_e32 v232, s14, v232\n\t"
      "v_cmp_ne_u32_e64 s[48:49], s15, v233\n\t"
      "v_subrev_u32_e32 v232, 6, v232\n\t"
      "v_cmp_lt_u32_e32 vcc, 7, v232\n\t"
      "s_mov_b64 exec, -1\n\t"
      "s_or_b64 vcc, vcc, s[48:49]\n\t"
      "s_cbranch_vccnz FQ44L_%=\n\t"
      "FQ44X_%=:\n\t"
      "FQ44DR_%=:\n\t"
      "s_mov_b32 s51, 0\n\t"
      "FQ44DL_%=:\n\t"
      "s_add_u32 s51, s51, 1\n\t"
      "s_cmp_lt_u32 s51, 262144\n\t"
      "s_cbranch_scc0 FQ44DX_%=\n\t"
      "s_waitcnt vmcnt(0)\n\t"
      "s_sleep 1\n\t"
      "global_load_dwordx4 v[136:139], v226, s[64:65] sc1\n\t"
      "global_load_dwordx4 v[140:143], v227, s[64:65] sc1\n\t"
      "global_load_dwordx4 v[144:147], v228, s[64:65] sc1\n\t"
      "global_load_dwordx4 v[148:151], v229, s[64:65] sc1\n\t"
      "global_load_dwordx4 v[152:155], v230, s[64:65] sc1\n\t"
      "s_waitcnt vmcnt(4)\n\t"
      "v_max3_u32 v234, v136, v137, v138\n\t"
      "v_max_u32_e32 v234, v234, v139\n\t"
      "v_cmp_eq_u32_e32 vcc, -1, v234\n\t"
      "s_cbranch_vccnz FQ44DL_%=\n\t"
      "FQ44DX_%=:\n\t"
      "s_branch FQ44B_%=\n\t"
      "FQ45R_%=:\n\t"
      "s_mov_b32 s51, 0\n\t"
      "FQ45L_%=:\n\t"
      "s_add_u32 s51, s51, 1\n\t"
      "s_cmp_lt_u32 s51, 262144\n\t"
      "s_cbranch_scc0 FQ45X_%=\n\t"
      "s_sleep 1\n\t"
      "s_mov_b64 exec, 15\n\t"
      "v_mov_b64_e32 v[232:233], 0\n\t"
      "global_atomic_or_x2 v[232:233], v247, v[232:233], s[12:13] sc0 sc1\n\t"
      "s_waitcnt vmcnt(0)\n\t"
      "v_subrev_u32_e32 v232, s14, v232\n\t"
      "v_cmp_ne_u32_e64 s[48:49], s15, v233\n\t"
      "v_subrev_u32_e32 v232, 6, v232\n\t"
      "v_cmp_lt_u32_e32 vcc, 7, v232\n\t"
      "s_mov_b64 exec, -1\n\t"
      "s_or_b64 vcc, vcc, s[48:49]\n\t"
      "s_cbranch_vccnz FQ45L_%=\n\t"
      "FQ45X_%=:\n\t"
      "FQ45DR_%=:\n\t"
      "s_mov_b32 s51, 0\n\t"
      "FQ45DL_%=:\n\t"
      "s_add_u32 s51, s51, 1\n\t"
      "s_cmp_lt_u32 s51, 262144\n\t"
      "s_cbranch_scc0 FQ45DX_%=\n\t"
      "s_waitcnt vmcnt(0)\n\t"
      "s_sleep 1\n\t"
      "global_load_dwordx4 v[140:143], v227, s[64:65] sc1\n\t"
      "global_load_dwordx4 v[144:147], v228, s[64:65] sc1\n\t"
      "global_load_dwordx4 v[148:151], v229, s[64:65] sc1\n\t"
      "global_load_dwordx4 v[152:155], v230, s[64:65] sc1\n\t"
      "s_waitcnt vmcnt(3)\n\t"
      "v_max3_u32 v234, v140, v141, v142\n\t"
      "v_max_u32_e32 v234, v234, v143\n\t"
      "v_cmp_eq_u32_e32 vcc, -1, v234\n\t"
      "s_cbranch_vccnz FQ45DL_%=\n\t"
      "FQ45DX_%=:\n\t"
      "s_branch FQ45B_%=\n\t"
      "FQ46R_%=:\n\t"
      "s_mov_b32 s51, 0\n\t"
      "FQ46L_%=:\n\t"
      "s_add_u32 s51, s51, 1\n\t"
      "s_cmp_lt_u32 s51, 262144\n\t"
      "s_cbranch_scc0 FQ46X_%=\n\t"
      "s_sleep 1\n\t"
      "s_mov_b64 exec, 15\n\t"
      "v_mov_b64_e32 v[232:233], 0\n\t"
      "global_atomic_or_x2 v[232:233], v247, v[232:233], s[12:13] sc0 sc1\n\t"
      "s_waitcnt vmcnt(0)\n\t"
      "v_subrev_u32_e32 v232, s14, v232\n\t"
      "v_cmp_ne_u32_e64 s[48:49], s15, v233\n\t"
      "v_subrev_u32_e32 v232, 6, v232\n\t"
      "v_cmp_lt_u32_e32 vcc, 7, v232\n\t"
      "s_mov_b64 exec, -1\n\t"
      "s_or_b64 vcc, vcc, s[48:49]\n\t"
      "s_cbranch_vccnz FQ46L_%=\n\t"
      "FQ46X_%=:\n\t"
      "FQ46DR_%=:\n\t"
      "s_mov_b32 s51, 0\n\t"
      "FQ46DL_%=:\n\t"
      "s_add_u32 s51, s51, 1\n\t"
      "s_cmp_lt_u32 s51, 262144\n\t"
      "s_cbranch_scc0 FQ46DX_%=\n\t"
      "s_waitcnt vmcnt(0)\n\t"
      "s_sleep 1\n\t"
      "global_load_dwordx4 v[144:147], v228, s[64:65] sc1\n\t"
      "global_load_dwordx4 v[148:151], v229, s[64:65] sc1\n\t"
      "global_load_dwordx4 v[152:155], v230, s[64:65] sc1\n\t"
      "s_waitcnt vmcnt(2)\n\t"
      "v_max3_u32 v234, v144, v145, v146\n\t"
      "v_max_u32_e32 v234, v234, v147\n\t"
      "v_cmp_eq_u32_e32 vcc, -1, v234\n\t"
      "s_cbranch_vccnz FQ46DL_%=\n\t"
      "FQ46DX_%=:\n\t"
      "s_branch FQ46B_%=\n\t"
      "FQ47R_%=:\n\t"
      "s_mov_b32 s51, 0\n\t"
      "FQ47L_%=:\n\t"
      "s_add_u32 s51, s51, 1\n\t"
      "s_cmp_lt_u32 s51, 262144\n\t"
      "s_cbranch_scc0 FQ47X_%=\n\t"
      "s_sleep 1\n\t"
      "s_mov_b64 exec, 15\n\t"
      "v_mov_b64_e32 v[232:233], 0\n\t"
      "global_atomic_or_x2 v[232:233], v247, v[232:233], s[12:13] sc0 sc1\n\t"
      "s_waitcnt vmcnt(0)\n\t"
      "v_subrev_u32_e32 v232, s14, v232\n\t"
      "v_cmp_ne_u32_e64 s[48:49], s15, v233\n\t"
      "v_subrev_u32_e32 v232, 6, v232\n\t"
      "v_cmp_lt_u32_e32 vcc, 7, v232\n\t"
      "s_mov_b64 exec, -1\n\t"
      "s_or_b64 vcc, vcc, s[48:49]\n\t"
      "s_cbranch_vccnz FQ47L_%=\n\t"
      "FQ47X_%=:\n\t"
      "FQ47DR_%=:\n\t"
      "s_mov_b32 s51, 0\n\t"
      "FQ47DL_%=:\n\t"
      "s_add_u32 s51, s51, 1\n\t"
      "s_cmp_lt_u32 s51, 262144\n\t"
      "s_cbranch_scc0 FQ47DX_%=\n\t"
      "s_waitcnt vmcnt(0)\n\t"
      "s_sleep 1\n\t"
      "global_load_dwordx4 v[148:151], v229, s[64:65] sc1\n\t"
      "global_load_dwordx4 v[152:155], v230, s[64:65] sc1\n\t"
      "s_waitcnt vmcnt(1)\n\t"
      "v_max3_u32 v234, v148, v149, v150\n\t"
      "v_max_u32_e32 v234, v234, v151\n\t"
      "v_cmp_eq_u32_e32 vcc, -1, v234\n\t"
      "s_cbranch_vccnz FQ47DL_%=\n\t"
      "FQ47DX_%=:\n\t"
      "s_branch FQ47B_%=\n\t"
      "FQ48R_%=:\n\t"
      "s_mov_b32 s51, 0\n\t"
      "FQ48L_%=:\n\t"
      "s_add_u32 s51, s51, 1\n\t"
      "s_cmp_lt_u32 s51, 262144\n\t"
      "s_cbranch_scc0 FQ48X_%=\n\t"
      "s_sleep 1\n\t"
      "s_mov_b64 exec, 15\n\t"
      "v_mov_b64_e32 v[232:233], 0\n\t"
      "global_atomic_or_x2 v[232:233], v247, v[232:233], s[12:13] sc0 sc1\n\t"
      "s_waitcnt vmcnt(0)\n\t"
      "v_subrev_u32_e32 v232, s14, v232\n\t"
      "v_cmp_ne_u32_e64 s[48:49], s15, v233\n\t"
      "v_subrev_u32_e32 v232, 6, v232\n\t"
      "v_cmp_lt_u32_e32 vcc, 7, v232\n\t"
      "s_mov_b64 exec, -1\n\t"
      "s_or_b64 vcc, vcc, s[48:49]\n\t"
      "s_cbranch_vccnz FQ48L_%=\n\t"
      "FQ48X_%=:\n\t"
      "FQ48DR_%=:\n\t"
      "s_mov_b32 s51, 0\n\t"
      "FQ48DL_%=:\n\t"
      "s_add_u32 s51, s51, 1\n\t"
      "s_cmp_lt_u32 s51, 262144\n\t"
      "s_cbranch_scc0 FQ48DX_%=\n\t"
      "s_waitcnt vmcnt(0)\n\t"
      "s_sleep 1\n\t"
      "global_load_dwordx4 v[152:155], v230, s[64:65] sc1\n\t"
      "s_waitcnt vmcnt(0)\n\t"
      "v_max3_u32 v234, v152, v153, v154\n\t"
      "v_max_u32_e32 v234, v234, v155\n\t"
      "v_cmp_eq_u32_e32 vcc, -1, v234\n\t"
      "s_cbranch_vccnz FQ48DL_%=\n\t"
      "FQ48DX_%=:\n\t"
      "s_branch FQ48B_%=\n\t"
      "NQ6R_%=:\n\t"
      "s_mov_b32 s51, 0\n\t"
      "NQ6L_%=:\n\t"
      "s_add_u32 s51, s51, 1\n\t"
      "s_cmp_lt_u32 s51, 262144\n\t"
      "s_cbranch_scc0 NQ6X_%=\n\t"
      "s_waitcnt vmcnt(0)\n\t"
      "s_sleep 1\n\t"
      "global_load_dwordx4 v[128:131], v224, s[64:65] sc1\n\t"
      "global_load_dwordx4 v[132:135], v225, s[64:65] sc1\n\t"
      "global_load_dwordx4 v[136:139], v226, s[64:65] sc1\n\t"
      "global_load_dwordx4 v[140:143], v227, s[64:65] sc1\n\t"
      "global_load_dwordx4 v[144:147], v228, s[64:65] sc1\n\t"
      "global_load_dwordx4 v[148:151], v229, s[64:65] sc1\n\t"
      "global_load_dwordx4 v[152:155], v230, s[64:65] sc1\n\t"
      "s_waitcnt vmcnt(0)\n\t"
      "v_max3_u32 v234, v128, v129, v130\n\t"
      "v_max3_u32 v234, v234, v131, v132\n\t"
      "v_max3_u32 v234, v234, v133, v134\n\t"
      "v_max3_u32 v234, v234, v135, v136\n\t"
      "v_max3_u32 v234, v234, v137, v138\n\t"
      "v_max3_u32 v234, v234, v139, v140\n\t"
      "v_max3_u32 v234, v234, v141, v142\n\t"
      "v_max3_u32 v234, v234, v143, v144\n\t"
      "v_max3_u32 v234, v234, v145, v146\n\t"
      "v_max3_u32 v234, v234, v147, v148\n\t"
      "v_max3_u32 v234, v234, v149, v150\n\t"
      "v_max3_u32 v234, v234, v151, v152\n\t"
      "v_max3_u32 v234, v234, v153, v154\n\t"
      "v_max_u32_e32 v234, v234, v155\n\t"
      "v_cmp_eq_u32_e32 vcc, -1, v234\n\t"
      "s_cbranch_vccnz NQ6L_%=\n\t"
      "NQ6X_%=:\n\t"
      "v_mfma_f32_16x16x32_bf16 v[184:187], v[128:131], v[0:3], 0\n\t"
      "v_mfma_f32_16x16x32_bf16 v[188:191], v[128:131], v[4:7], 0\n\t"
      "v_mfma_f32_16x16x32_bf16 v[192:195], v[128:131], v[8:11], 0\n\t"
      "v_mfma_f32_16x16x32_bf16 v[196:199], v[128:131], v[12:15], 0\n\t"
      "v_mfma_f32_16x16x32_bf16 v[184:187], v[132:135], v[16:19], v[184:187]\n\t"
      "v_mfma_f32_16x16x32_bf16 v[188:191], v[132:135], v[20:23], v[188:191]\n\t"
      "v_mfma_f32_16x16x32_bf16 v[192:195], v[132:135], v[24:27], v[192:195]\n\t"
      "v_mfma_f32_16x16x32_bf16 v[196:199], v[132:135], v[28:31], v[196:199]\n\t"
      "v_mfma_f32_16x16x32_bf16 v[184:187], v[136:139], v[32:35], v[184:187]\n\t"
      "v_mfma_f32_16x16x32_bf16 v[188:191], v[136:139], v[36:39], v[188:191]\n\t"
      "v_mfma_f32_16x16x32_bf16 v[192:195], v[136:139], v[40:43], v[192:195]\n\t"
      "v_mfma_f32_16x16x32_bf16 v[196:199], v[136:139], v[44:47], v[196:199]\n\t"
      "v_mfma_f32_16x16x32_bf16 v[184:187], v[140:143], v[48:51], v[184:187]\n\t"
      "v_mfma_f32_16x16x32_bf16 v[188:191], v[140:143], v[52:55], v[188:191]\n\t"
      "v_mfma_f32_16x16x32_bf16 v[192:195], v[140:143], v[56:59], v[192:195]\n\t"
      "v_mfma_f32_16x16x32_bf16 v[196:199], v[140:143], v[60:63], v[196:199]\n\t"
      "v_mfma_f32_16x16x32_bf16 v[184:187], v[144:147], v[64:67], v[184:187]\n\t"
      "v_mfma_f32_16x16x32_bf16 v[188:191], v[144:147], v[68:71], v[188:191]\n\t"
      "v_mfma_f32_16x16x32_bf16 v[192:195], v[144:147], v[72:75], v[192:195]\n\t"
      "v_mfma_f32_16x16x32_bf16 v[196:199], v[144:147], v[76:79], v[196:199]\n\t"
      "v_mfma_f32_16x16x32_bf16 v[184:187], v[148:151], v[80:83], v[184:187]\n\t"
      "v_mfma_f32_16x16x32_bf16 v[188:191], v[148:151], v[84:87], v[188:191]\n\t"
      "v_mfma_f32_16x16x32_bf16 v[192:195], v[148:151], v[88:91], v[192:195]\n\t"
      "v_mfma_f32_16x16x32_bf16 v[196:199], v[148:151], v[92:95], v[196:199]\n\t"
      "v_mfma_f32_16x16x32_bf16 v[184:187], v[152:155], v[96:99], v[184:187]\n\t"
      "v_mfma_f32_16x16x32_bf16 v[188:191], v[152:155], v[100:103], v[188:191]\n\t"
      "v_mfma_f32_16x16x32_bf16 v[192:195], v[152:155], v[104:107], v[192:195]\n\t"
      "v_mfma_f32_16x16x32_bf16 v[196:199], v[152:155], v[108:111], v[196:199]\n\t"
      "s_waitcnt vmcnt(0)\n\t"
      "s_nop 7\n\t"
      "s_branch NQ6B_%=\n\t"
      "NP0R_%=:\n\t"
      "s_mov_b32 s51, 0\n\t"
      "NP0L_%=:\n\t"
      "s_add_u32 s51, s51, 1\n\t"
      "s_cmp_lt_u32 s51, 262144\n\t"
      "s_cbranch_scc0 NP0X_%=\n\t"
      "s_waitcnt vmcnt(0)\n\t"
      "s_sleep 1\n\t"
      "global_load_dwordx4 v[128:131], v224, s[52:53] sc1\n\t"
      "global_load_dwordx4 v[132:135], v225, s[52:53] sc1\n\t"
      "global_load_dwordx4 v[136:139], v226, s[52:53] sc1\n\t"
      "global_load_dwordx4 v[140:143], v227, s[52:53] sc1\n\t"
      "global_load_dwordx4 v[144:147], v228, s[52:53] sc1\n\t"
      "global_load_dwordx4 v[148:151], v229, s[52:53] sc1\n\t"
      "global_load_dwordx4 v[152:155], v230, s[52:53] sc1\n\t"
      "s_waitcnt vmcnt(0)\n\t"
      "v_max3_u32 v234, v128, v129, v130\n\t"
      "v_max3_u32 v234, v234, v131, v132\n\t"
      "v_max3_u32 v234, v234, v133, v134\n\t"
      "v_max3_u32 v234, v234, v135, v136\n\t"
      "v_max3_u32 v234, v234, v137, v138\n\t"
      "v_max3_u32 v234, v234, v139, v140\n\t"
      "v_max3_u32 v234, v234, v141, v142\n\t"
      "v_max3_u32 v234, v234, v143, v144\n\t"
      "v_max3_u32 v234, v234, v145, v146\n\t"
      "v_max3_u32 v234, v234, v147, v148\n\t"
      "v_max3_u32 v234, v234, v149, v150\n\t"
      "v_max3_u32 v234, v234, v151, v152\n\t"
      "v_max3_u32 v234, v234, v153, v154\n\t"
      "v_max_u32_e32 v234, v234, v155\n\t"
      "v_cmp_eq_u32_e32 vcc, -1, v234\n\t"
      "s_cbranch_vccnz NP0L_%=\n\t"
      "NP0X_%=:\n\t"
      "v_mfma_f32_16x16x32_bf16 v[184:187], v[128:131], v[0:3], v[156:159]\n\t"
      "v_mfma_f32_16x16x32_bf16 v[188:191], v[128:131], v[4:7], 0\n\t"
      "v_mfma_f32_16x16x32_bf16 v[192:195], v[128:131], v[8:11], 0\n\t"
      "v_mfma_f32_16x16x32_bf16 v[196:199], v[128:131], v[12:15], 0\n\t"
      "v_mfma_f32_16x16x32_bf16 v[184:187], v[132:135], v[16:19], v[184:187]\n\t"
      "v_mfma_f32_16x16x32_bf16 v[188:191], v[132:135], v[20:23], v[188:191]\n\t"
      "v_mfma_f32_16x16x32_bf16 v[192:195], v[132:135], v[24:27], v[192:195]\n\t"
      "v_mfma_f32_16x16x32_bf16 v[196:199], v[132:135], v[28:31], v[196:199]\n\t"
      "v_mfma_f32_16x16x32_bf16 v[184:187], v[136:139], v[32:35], v[184:187]\n\t"
      "v_mfma_f32_16x16x32_bf16 v[188:191], v[136:139], v[36:39], v[188:191]\n\t"
      "v_mfma_f32_16x16x32_bf16 v[192:195], v[136:139], v[40:43], v[192:195]\n\t"
      "v_mfma_f32_16x16x32_bf16 v[196:199], v[136:139], v[44:47], v[196:199]\n\t"
      "v_mfma_f32_16x16x32_bf16 v[184:187], v[140:143], v[48:51], v[184:187]\n\t"
      "v_mfma_f32_16x16x32_bf16 v[188:191], v[140:143], v[52:55], v[188:191]\n\t"
      "v_mfma_f32_16x16x32_bf16 v[192:195], v[140:143], v[56:59], v[192:195]\n\t"
      "v_mfma_f32_16x16x32_bf16 v[196:199], v[140:143], v[60:63], v[196:199]\n\t"
      "v_mfma_f32_16x16x32_bf16 v[184:187], v[144:147], v[64:67], v[184:187]\n\t"
      "v_mfma_f32_16x16x32_bf16 v[188:191], v[144:147], v[68:71], v[188:191]\n\t"
      "v_mfma_f32_16x16x32_bf16 v[192:195], v[144:147], v[72:75], v[192:195]\n\t"
      "v_mfma_f32_16x16x32_bf16 v[196:199], v[144:147], v[76:79], v[196:199]\n\t"
      "v_mfma_f32_16x16x32_bf16 v[184:187], v[148:151], v[80:83], v[184:187]\n\t"
      "v_mfma_f32_16x16x32_bf16 v[188:191], v[148:151], v[84:87], v[188:191]\n\t"
      "v_mfma_f32_16x16x32_bf16 v[192:195], v[148:151], v[88:91], v[192:195]\n\t"
      "v_mfma_f32_16x16x32_bf16 v[184:187], v[152:155], v[96:99], v[184:187]\n\t"
      "v_mfma_f32_16x16x32_bf16 v[188:191], v[152:155], v[100:103], v[188:191]\n\t"
      "v_mfma_f32_16x16x32_bf16 v[192:195], v[152:155], v[104:107], v[192:195]\n\t"
      "global_load_dwordx4 v[128:131], v224, s[54:55] sc1\n\t"
      "global_load_dwordx4 v[132:135], v225, s[54:55] sc1\n\t"
      "global_load_dwordx4 v[136:139], v226, s[54:55] sc1\n\t"
      "global_load_dwordx4 v[140:143], v227, s[54:55] sc1\n\t"
      "global_load_dwordx4 v[144:147], v228, s[54:55] sc1\n\t"
      "s_waitcnt vmcnt(0)\n\t"
      "s_nop 7\n\t"
      "s_branch NP0B_%=\n\t"
      "NP1R_%=:\n\t"
      "s_mov_b32 s51, 0\n\t"
      "NP1L_%=:\n\t"
      "s_add_u32 s51, s51, 1\n\t"
      "s_cmp_lt_u32 s51, 262144\n\t"
      "s_cbranch_scc0 NP1X_%=\n\t"
      "s_waitcnt vmcnt(0)\n\t"
      "s_sleep 1\n\t"
      "global_load_dwordx4 v[128:131], v224, s[54:55] sc1\n\t"
      "global_load_dwordx4 v[132:135], v225, s[54:55] sc1\n\t"
      "global_load_dwordx4 v[136:139], v226, s[54:55] sc1\n\t"
      "global_load_dwordx4 v[140:143], v227, s[54:55] sc1\n\t"
      "global_load_dwordx4 v[144:147], v228, s[54:55] sc1\n\t"
      "global_load_dwordx4 v[148:151], v229, s[54:55] sc1\n\t"
      "global_load_dwordx4 v[152:155], v230, s[54:55] sc1\n\t"
      "s_waitcnt vmcnt(0)\n\t"
      "v_max3_u32 v234, v128, v129, v130\n\t"
      "v_max3_u32 v234, v234, v131, v132\n\t"
      "v_max3_u32 v234, v234, v133, v134\n\t"
      "v_max3_u32 v234, v234, v135, v136\n\t"
      "v_max3_u32 v234, v234, v137, v138\n\t"
      "v_max3_u32 v234, v234, v139, v140\n\t"
      "v_max3_u32 v234, v234, v141, v142\n\t"
      "v_max3_u32 v234, v234, v143, v144\n\t"
      "v_max3_u32 v234, v234, v145, v146\n\t"
      "v_max3_u32 v234, v234, v147, v148\n\t"
      "v_max3_u32 v234, v234, v149, v150\n\t"
      "v_max3_u32 v234, v234, v151, v152\n\t"
      "v_max3_u32 v234, v234, v153, v154\n\t"
      "v_max_u32_e32 v234, v234, v155\n\t"
      "v_cmp_eq_u32_e32 vcc, -1, v234\n\t"
      "s_cbranch_vccnz NP1L_%=\n\t"
      "NP1X_%=:\n\t"
      "v_mfma_f32_16x16x32_bf16 v[200:203], v[128:131], v[0:3], v[160:163]\n\t"
      "v_mfma_f32_16x16x32_bf16 v[204:207], v[128:131], v[4:7], 0\n\t"
      "v_mfma_f32_16x16x32_bf16 v[208:211], v[128:131], v[8:11], 0\n\t"
      "v_mfma_f32_16x16x32_bf16 v[212:215], v[128:131], v[12:15], 0\n\t"
      "v_mfma_f32_16x16x32_bf16 v[200:203], v[132:135], v[16:19], v[200:203]\n\t"
      "v_mfma_f32_16x16x32_bf16 v[204:207], v[132:135], v[20:23], v[204:207]\n\t"
      "v_mfma_f32_16x16x32_bf16 v[208:211], v[132:135], v[24:27], v[208:211]\n\t"
      "v_mfma_f32_16x16x32_bf16 v[212:215], v[132:135], v[28:31], v[212:215]\n\t"
      "v_mfma_f32_16x16x32_bf16 v[200:203], v[136:139], v[32:35], v[200:203]\n\t"
      "v_mfma_f32_16x16x32_bf16 v[204:207], v[136:139], v[36:39], v[204:207]\n\t"
      "v_mfma_f32_16x16x32_bf16 v[208:211], v[136:139], v[40:43], v[208:211]\n\t"
      "v_mfma_f32_16x16x32_bf16 v[212:215], v[136:139], v[44:47], v[212:215]\n\t"
      "v_mfma_f32_16x16x32_bf16 v[200:203], v[140:143], v[48:51], v[200:203]\n\t"
      "v_mfma_f32_16x16x32_bf16 v[204:207], v[140:143], v[52:55], v[204:207]\n\t"
      "v_mfma_f32_16x16x32_bf16 v[208:211], v[140:143], v[56:59], v[208:211]\n\t"
      "v_mfma_f32_16x16x32_bf16 v[212:215], v[140:143], v[60:63], v[212:215]\n\t"
      "v_mfma_f32_16x16x32_bf16 v[200:203], v[144:147], v[64:67], v[200:203]\n\t"
      "v_mfma_f32_16x16x32_bf16 v[204:207], v[144:147], v[68:71], v[204:207]\n\t"
      "v_mfma_f32_16x16x32_bf16 v[208:211], v[144:147], v[72:75], v[208:211]\n\t"
      "v_mfma_f32_16x16x32_bf16 v[212:215], v[144:147], v[76:79], v[212:215]\n\t"
      "v_mfma_f32_16x16x32_bf16 v[200:203], v[148:151], v[80:83], v[200:203]\n\t"
      "v_mfma_f32_16x16x32_bf16 v[204:207], v[148:151], v[84:87], v[204:207]\n\t"
      "v_mfma_f32_16x16x32_bf16 v[208:211], v[148:151], v[88:91], v[208:211]\n\t"
      "v_mfma_f32_16x16x32_bf16 v[200:203], v[152:155], v[96:99], v[200:203]\n\t"
      "v_mfma_f32_16x16x32_bf16 v[204:207], v[152:155], v[100:103], v[204:207]\n\t"
      "v_mfma_f32_16x16x32_bf16 v[208:211], v[152:155], v[104:107], v[208:211]\n\t"
      "global_load_dwordx4 v[128:131], v224, s[56:57] sc1\n\t"
      "global_load_dwordx4 v[132:135], v225, s[56:57] sc1\n\t"
      "global_load_dwordx4 v[136:139], v226, s[56:57] sc1\n\t"
      "global_load_dwordx4 v[140:143], v227, s[56:57] sc1\n\t"
      "global_load_dwordx4 v[144:147], v228, s[56:57] sc1\n\t"
      "s_waitcnt vmcnt(0)\n\t"
      "s_nop 7\n\t"
      "s_branch NP1B_%=\n\t"
      "NP2R_%=:\n\t"
      "s_mov_b32 s51, 0\n\t"
      "NP2L_%=:\n\t"
      "s_add_u32 s51, s51, 1\n\t"
      "s_cmp_lt_u32 s51, 262144\n\t"
      "s_cbranch_scc0 NP2X_%=\n\t"
      "s_waitcnt vmcnt(0)\n\t"
      "s_sleep 1\n\t"
      "global_load_dwordx4 v[128:131], v224, s[56:57] sc1\n\t"
      "global_load_dwordx4 v[132:135], v225, s[56:57] sc1\n\t"
      "global_load_dwordx4 v[136:139], v226, s[56:57] sc1\n\t"
      "global_load_dwordx4 v[140:143], v227, s[56:57] sc1\n\t"
      "global_load_dwordx4 v[144:147], v228, s[56:57] sc1\n\t"
      "global_load_dwordx4 v[148:151], v229, s[56:57] sc1\n\t"
      "global_load_dwordx4 v[152:155], v230, s[56:57] sc1\n\t"
      "s_waitcnt vmcnt(0)\n\t"
      "v_max3_u32 v234, v128, v129, v130\n\t"
      "v_max3_u32 v234, v234, v131, v132\n\t"
      "v_max3_u32 v234, v234, v133, v134\n\t"
      "v_max3_u32 v234, v234, v135, v136\n\t"
      "v_max3_u32 v234, v234, v137, v138\n\t"
      "v_max3_u32 v234, v234, v139, v140\n\t"
      "v_max3_u32 v234, v234, v141, v142\n\t"
      "v_max3_u32 v234, v234, v143, v144\n\t"
      "v_max3_u32 v234, v234, v145, v146\n\t"
      "v_max3_u32 v234, v234, v147, v148\n\t"
      "v_max3_u32 v234, v234, v149, v150\n\t"
      "v_max3_u32 v234, v234, v151, v152\n\t"
      "v_max3_u32 v234, v234, v153, v154\n\t"
      "v_max_u32_e32 v234, v234, v155\n\t"
      "v_cmp_eq_u32_e32 vcc, -1, v234\n\t"
      "s_cbranch_vccnz NP2L_%=\n\t"
      "NP2X_%=:\n\t"
      "v_mfma_f32_16x16x32_bf16 v[184:187], v[128:131], v[0:3], v[164:167]\n\t"
      "v_mfma_f32_16x16x32_bf16 v[188:191], v[128:131], v[4:7], 0\n\t"
      "v_mfma_f32_16x16x32_bf16 v[192:195], v[128:131], v[8:11], 0\n\t"
      "v_mfma_f32_16x16x32_bf16 v[196:199], v[128:131], v[12:15], 0\n\t"
      "v_mfma_f32_16x16x32_bf16 v[184:187], v[132:135], v[16:19], v[184:187]\n\t"
      "v_mfma_f32_16x16x32_bf16 v[188:191], v[132:135], v[20:23], v[188:191]\n\t"
      "v_mfma_f32_16x16x32_bf16 v[192:195], v[132:135], v[24:27], v[192:195]\n\t"
      "v_mfma_f32_16x16x32_bf16 v[196:199], v[132:135], v[28:31], v[196:199]\n\t"
      "v_mfma_f32_16x16x32_bf16 v[184:187], v[136:139], v[32:35], v[184:187]\n\t"
      "v_mfma_f32_16x16x32_bf16 v[188:191], v[136:139], v[36:39], v[188:191]\n\t"
      "v_mfma_f32_16x16x32_bf16 v[192:195], v[136:139], v[40:43], v[192:195]\n\t"
      "v_mfma_f32_16x16x32_bf16 v[196:199], v[136:139], v[44:47], v[196:199]\n\t"
      "v_mfma_f32_16x16x32_bf16 v[184:187], v[140:143], v[48:51], v[184:187]\n\t"
      "v_mfma_f32_16x16x32_bf16 v[188:191], v[140:143], v[52:55], v[188:191]\n\t"
      "v_mfma_f32_16x16x32_bf16 v[192:195], v[140:143], v[56:59], v[192:195]\n\t"
      "v_mfma_f32_16x16x32_bf16 v[196:199], v[140:143], v[60:63], v[196:199]\n\t"
      "v_mfma_f32_16x16x32_bf16 v[184:187], v[144:147], v[64:67], v[184:187]\n\t"
      "v_mfma_f32_16x16x32_bf16 v[188:191], v[144:147], v[68:71], v[188:191]\n\t"
      "v_mfma_f32_16x16x32_bf16 v[192:195], v[144:147], v[72:75], v[192:195]\n\t"
      "v_mfma_f32_16x16x32_bf16 v[196:199], v[144:147], v[76:79], v[196:199]\n\t"
      "v_mfma_f32_16x16x32_bf16 v[184:187], v[148:151], v[80:83], v[184:187]\n\t"
      "v_mfma_f32_16x16x32_bf16 v[188:191], v[148:151], v[84:87], v[188:191]\n\t"
      "v_mfma_f32_16x16x32_bf16 v[192:195], v[148:151], v[88:91], v[192:195]\n\t"
      "v_mfma_f32_16x16x32_bf16 v[184:187], v[152:155], v[96:99], v[184:187]\n\t"
      "v_mfma_f32_16x16x32_bf16 v[188:191], v[152:155], v[100:103], v[188:191]\n\t"
      "v_mfma_f32_16x16x32_bf16 v[192:195], v[152:155], v[104:107], v[192:195]\n\t"
      "global_load_dwordx4 v[128:131], v224, s[58:59] sc1\n\t"
      "global_load_dwordx4 v[132:135], v225, s[58:59] sc1\n\t"
      "global_load_dwordx4 v[136:139], v226, s[58:59] sc1\n\t"
      "global_load_dwordx4 v[140:143], v227, s[58:59] sc1\n\t"
      "global_load_dwordx4 v[144:147], v228, s[58:59] sc1\n\t"
      "s_waitcnt vmcnt(0)\n\t"
      "s_nop 7\n\t"
      "s_branch NP2B_%=\n\t"
      "NP3R_%=:\n\t"
      "s_mov_b32 s51, 0\n\t"
      "NP3L_%=:\n\t"
      "s_add_u32 s51, s51, 1\n\t"
      "s_cmp_lt_u32 s51, 262144\n\t"
      "s_cbranch_scc0 NP3X_%=\n\t"
      "s_waitcnt vmcnt(0)\n\t"
      "s_sleep 1\n\t"
      "global_load_dwordx4 v[128:131], v224, s[58:59] sc1\n\t"
      "global_load_dwordx4 v[132:135], v225, s[58:59] sc1\n\t"
      "global_load_dwordx4 v[136:139], v226, s[58:59] sc1\n\t"
      "global_load_dwordx4 v[140:143], v227, s[58:59] sc1\n\t"
      "global_load_dwordx4 v[144:147], v228, s[58:59] sc1\n\t"
      "global_load_dwordx4 v[148:151], v229, s[58:59] sc1\n\t"
      "global_load_dwordx4 v[152:155], v230, s[58:59] sc1\n\t"
      "s_waitcnt vmcnt(0)\n\t"
      "v_max3_u32 v234, v128, v129, v130\n\t"
      "v_max3_u32 v234, v234, v131, v132\n\t"
      "v_max3_u32 v234, v234, v133, v134\n\t"
      "v_max3_u32 v234, v234, v135, v136\n\t"
      "v_max3_u32 v234, v234, v137, v138\n\t"
      "v_max3_u32 v234, v234, v139, v140\n\t"
      "v_max3_u32 v234, v234, v141, v142\n\t"
      "v_max3_u32 v234, v234, v143, v144\n\t"
      "v_max3_u32 v234, v234, v145, v146\n\t"
      "v_max3_u32 v234, v234, v147, v148\n\t"
      "v_max3_u32 v234, v234, v149, v150\n\t"
      "v_max3_u32 v234, v234, v151, v152\n\t"
      "v_max3_u32 v234, v234, v153, v154\n\t"
      "v_max_u32_e32 v234, v234, v155\n\t"
      "v_cmp_eq_u32_e32 vcc, -1, v234\n\t"
      "s_cbranch_vccnz NP3L_%=\n\t"
      "NP3X_%=:\n\t"
      "v_mfma_f32_16x16x32_bf16 v[200:203], v[128:131], v[0:3], v[168:171]\n\t"
      "v_mfma_f32_16x16x32_bf16 v[204:207], v[128:131], v[4:7], 0\n\t"
      "v_mfma_f32_16x16x32_bf16 v[208:211], v[128:131], v[8:11], 0\n\t"
      "v_mfma_f32_16x16x32_bf16 v[212:215], v[128:131], v[12:15], 0\n\t"
      "v_mfma_f32_16x16x32_bf16 v[200:203], v[132:135], v[16:19], v[200:203]\n\t"
      "v_mfma_f32_16x16x32_bf16 v[204:207], v[132:135], v[20:23], v[204:207]\n\t"
      "v_mfma_f32_16x16x32_bf16 v[208:211], v[132:135], v[24:27], v[208:211]\n\t"
      "v_mfma_f32_16x16x32_bf16 v[212:215], v[132:135], v[28:31], v[212:215]\n\t"
      "v_mfma_f32_16x16x32_bf16 v[200:203], v[136:139], v[32:35], v[200:203]\n\t"
      "v_mfma_f32_16x16x32_bf16 v[204:207], v[136:139], v[36:39], v[204:207]\n\t"
      "v_mfma_f32_16x16x32_bf16 v[208:211], v[136:139], v[40:43], v[208:211]\n\t"
      "v_mfma_f32_16x16x32_bf16 v[212:215], v[136:139], v[44:47], v[212:215]\n\t"
      "v_mfma_f32_16x16x32_bf16 v[200:203], v[140:143], v[48:51], v[200:203]\n\t"
      "v_mfma_f32_16x16x32_bf16 v[204:207], v[140:143], v[52:55], v[204:207]\n\t"
      "v_mfma_f32_16x16x32_bf16 v[208:211], v[140:143], v[56:59], v[208:211]\n\t"
      "v_mfma_f32_16x16x32_bf16 v[212:215], v[140:143], v[60:63], v[212:215]\n\t"
      "v_mfma_f32_16x16x32_bf16 v[200:203], v[144:147], v[64:67], v[200:203]\n\t"
      "v_mfma_f32_16x16x32_bf16 v[204:207], v[144:147], v[68:71], v[204:207]\n\t"
      "v_mfma_f32_16x16x32_bf16 v[208:211], v[144:147], v[72:75], v[208:211]\n\t"
      "v_mfma_f32_16x16x32_bf16 v[212:215], v[144:147], v[76:79], v[212:215]\n\t"
      "v_mfma_f32_16x16x32_bf16 v[200:203], v[148:151], v[80:83], v[200:203]\n\t"
      "v_mfma_f32_16x16x32_bf16 v[204:207], v[148:151], v[84:87], v[204:207]\n\t"
      "v_mfma_f32_16x16x32_bf16 v[208:211], v[148:151], v[88:91], v[208:211]\n\t"
      "v_mfma_f32_16x16x32_bf16 v[200:203], v[152:155], v[96:99], v[200:203]\n\t"
      "v_mfma_f32_16x16x32_bf16 v[204:207], v[152:155], v[100:103], v[204:207]\n\t"
      "v_mfma_f32_16x16x32_bf16 v[208:211], v[152:155], v[104:107], v[208:211]\n\t"
      "global_load_dwordx4 v[128:131], v224, s[60:61] sc1\n\t"
      "global_load_dwordx4 v[132:135], v225, s[60:61] sc1\n\t"
      "global_load_dwordx4 v[136:139], v226, s[60:61] sc1\n\t"
      "global_load_dwordx4 v[140:143], v227, s[60:61] sc1\n\t"
      "global_load_dwordx4 v[144:147], v228, s[60:61] sc1\n\t"
      "s_waitcnt vmcnt(0)\n\t"
      "s_nop 7\n\t"
      "s_branch NP3B_%=\n\t"
      "NP4R_%=:\n\t"
      "s_mov_b32 s51, 0\n\t"
      "NP4L_%=:\n\t"
      "s_add_u32 s51, s51, 1\n\t"
      "s_cmp_lt_u32 s51, 262144\n\t"
      "s_cbranch_scc0 NP4X_%=\n\t"
      "s_waitcnt vmcnt(0)\n\t"
      "s_sleep 1\n\t"
      "global_load_dwordx4 v[128:131], v224, s[60:61] sc1\n\t"
      "global_load_dwordx4 v[132:135], v225, s[60:61] sc1\n\t"
      "global_load_dwordx4 v[136:139], v226, s[60:61] sc1\n\t"
      "global_load_dwordx4 v[140:143], v227, s[60:61] sc1\n\t"
      "global_load_dwordx4 v[144:147], v228, s[60:61] sc1\n\t"
      "global_load_dwordx4 v[148:151], v229, s[60:61] sc1\n\t"
      "global_load_dwordx4 v[152:155], v230, s[60:61] sc1\n\t"
      "s_waitcnt vmcnt(0)\n\t"
      "v_max3_u32 v234, v128, v129, v130\n\t"
      "v_max3_u32 v234, v234, v131, v132\n\t"
      "v_max3_u32 v234, v234, v133, v134\n\t"
      "v_max3_u32 v234, v234, v135, v136\n\t"
      "v_max3_u32 v234, v234, v137, v138\n\t"
      "v_max3_u32 v234, v234, v139, v140\n\t"
      "v_max3_u32 v234, v234, v141, v142\n\t"
      "v_max3_u32 v234, v234, v143, v144\n\t"
      "v_max3_u32 v234, v234, v145, v146\n\t"
      "v_max3_u32 v234, v234, v147, v148\n\t"
      "v_max3_u32 v234, v234, v149, v150\n\t"
      "v_max3_u32 v234, v234, v151, v152\n\t"
      "v_max3_u32 v234, v234, v153, v154\n\t"
      "v_max_u32_e32 v234, v234, v155\n\t"
      "v_cmp_eq_u32_e32 vcc, -1, v234\n\t"
      "s_cbranch_vccnz NP4L_%=\n\t"
      "NP4X_%=:\n\t"
      "v_mfma_f32_16x16x32_bf16 v[184:187], v[128:131], v[0:3], v[172:175]\n\t"
      "v_mfma_f32_16x16x32_bf16 v[188:191], v[128:131], v[4:7], 0\n\t"
      "v_mfma_f32_16x16x32_bf16 v[192:195], v[128:131], v[8:11], 0\n\t"
      "v_mfma_f32_16x16x32_bf16 v[196:199], v[128:131], v[12:15], 0\n\t"
      "v_mfma_f32_16x16x32_bf16 v[184:187], v[132:135], v[16:19], v[184:187]\n\t"
      "v_mfma_f32_16x16x32_bf16 v[188:191], v[132:135], v[20:23], v[188:191]\n\t"
      "v_mfma_f32_16x16x32_bf16 v[192:195], v[132:135], v[24:27], v[192:195]\n\t"
      "v_mfma_f32_16x16x32_bf16 v[196:199], v[132:135], v[28:31], v[196:199]\n\t"
      "v_mfma_f32_16x16x32_bf16 v[184:187], v[136:139], v[32:35], v[184:187]\n\t"
      "v_mfma_f32_16x16x32_bf16 v[188:191], v[136:139], v[36:39], v[188:191]\n\t"
      "v_mfma_f32_16x16x32_bf16 v[192:195], v[136:139], v[40:43], v[192:195]\n\t"
      "v_mfma_f32_16x16x32_bf16 v[196:199], v[136:139], v[44:47], v[196:199]\n\t"
      "v_mfma_f32_16x16x32_bf16 v[184:187], v[140:143], v[48:51], v[184:187]\n\t"
      "v_mfma_f32_16x16x32_bf16 v[188:191], v[140:143], v[52:55], v[188:191]\n\t"
      "v_mfma_f32_16x16x32_bf16 v[192:195], v[140:143], v[56:59], v[192:195]\n\t"
      "v_mfma_f32_16x16x32_bf16 v[196:199], v[140:143], v[60:63], v[196:199]\n\t"
      "v_mfma_f32_16x16x32_bf16 v[184:187], v[144:147], v[64:67], v[184:187]\n\t"
      "v_mfma_f32_16x16x32_bf16 v[188:191], v[144:147], v[68:71], v[188:191]\n\t"
      "v_mfma_f32_16x16x32_bf16 v[192:195], v[144:147], v[72:75], v[192:195]\n\t"
      "v_mfma_f32_16x16x32_bf16 v[196:199], v[144:147], v[76:79], v[196:199]\n\t"
      "v_mfma_f32_16x16x32_bf16 v[184:187], v[148:151], v[80:83], v[184:187]\n\t"
      "v_mfma_f32_16x16x32_bf16 v[188:191], v[148:151], v[84:87], v[188:191]\n\t"
      "v_mfma_f32_16x16x32_bf16 v[192:195], v[148:151], v[88:91], v[192:195]\n\t"
      "v_mfma_f32_16x16x32_bf16 v[184:187], v[152:155], v[96:99], v[184:187]\n\t"
      "v_mfma_f32_16x16x32_bf16 v[188:191], v[152:155], v[100:103], v[188:191]\n\t"
      "v_mfma_f32_16x16x32_bf16 v[192:195], v[152:155], v[104:107], v[192:195]\n\t"
      "global_load_dwordx4 v[128:131], v224, s[62:63] sc1\n\t"
      "global_load_dwordx4 v[132:135], v225, s[62:63] sc1\n\t"
      "global_load_dwordx4 v[136:139], v226, s[62:63] sc1\n\t"
      "global_load_dwordx4 v[140:143], v227, s[62:63] sc1\n\t"
      "global_load_dwordx4 v[144:147], v228, s[62:63] sc1\n\t"
      "s_waitcnt vmcnt(0)\n\t"
      "s_nop 7\n\t"
      "s_branch NP4B_%=\n\t"
      "NP5R_%=:\n\t"
      "s_mov_b32 s51, 0\n\t"
      "NP5L_%=:\n\t"
      "s_add_u32 s51, s51, 1\n\t"
      "s_cmp_lt_u32 s51, 262144\n\t"
      "s_cbranch_scc0 NP5X_%=\n\t"
      "s_waitcnt vmcnt(0)\n\t"
      "s_sleep 1\n\t"
      "global_load_dwordx4 v[128:131], v224, s[62:63] sc1\n\t"
      "global_load_dwordx4 v[132:135], v225, s[62:63] sc1\n\t"
      "global_load_dwordx4 v[136:139], v226, s[62:63] sc1\n\t"
      "global_load_dwordx4 v[140:143], v227, s[62:63] sc1\n\t"
      "global_load_dwordx4 v[144:147], v228, s[62:63] sc1\n\t"
      "global_load_dwordx4 v[148:151], v229, s[62:63] sc1\n\t"
      "global_load_dwordx4 v[152:155], v230, s[62:63] sc1\n\t"
      "s_waitcnt vmcnt(0)\n\t"
      "v_max3_u32 v234, v128, v129, v130\n\t"
      "v_max3_u32 v234, v234, v131, v132\n\t"
      "v_max3_u32 v234, v234, v133, v134\n\t"
      "v_max3_u32 v234, v234, v135, v136\n\t"
      "v_max3_u32 v234, v234, v137, v138\n\t"
      "v_max3_u32 v234, v234, v139, v140\n\t"
      "v_max3_u32 v234, v234, v141, v142\n\t"
      "v_max3_u32 v234, v234, v143, v144\n\t"
      "v_max3_u32 v234, v234, v145, v146\n\t"
      "v_max3_u32 v234, v234, v147, v148\n\t"
      "v_max3_u32 v234, v234, v149, v150\n\t"
      "v_max3_u32 v234, v234, v151, v152\n\t"
      "v_max3_u32 v234, v234, v153, v154\n\t"
      "v_max_u32_e32 v234, v234, v155\n\t"
      "v_cmp_eq_u32_e32 vcc, -1, v234\n\t"
      "s_cbranch_vccnz NP5L_%=\n\t"
      "NP5X_%=:\n\t"
      "v_mfma_f32_16x16x32_bf16 v[200:203], v[128:131], v[0:3], v[176:179]\n\t"
      "v_mfma_f32_16x16x32_bf16 v[204:207], v[128:131], v[4:7], 0\n\t"
      "v_mfma_f32_16x16x32_bf16 v[208:211], v[128:131], v[8:11], 0\n\t"
      "v_mfma_f32_16x16x32_bf16 v[212:215], v[128:131], v[12:15], 0\n\t"
      "v_mfma_f32_16x16x32_bf16 v[200:203], v[132:135], v[16:19], v[200:203]\n\t"
      "v_mfma_f32_16x16x32_bf16 v[204:207], v[132:135], v[20:23], v[204:207]\n\t"
      "v_mfma_f32_16x16x32_bf16 v[208:211], v[132:135], v[24:27], v[208:211]\n\t"
      "v_mfma_f32_16x16x32_bf16 v[212:215], v[132:135], v[28:31], v[212:215]\n\t"
      "v_mfma_f32_16x16x32_bf16 v[200:203], v[136:139], v[32:35], v[200:203]\n\t"
      "v_mfma_f32_16x16x32_bf16 v[204:207], v[136:139], v[36:39], v[204:207]\n\t"
      "v_mfma_f32_16x16x32_bf16 v[208:211], v[136:139], v[40:43], v[208:211]\n\t"
      "v_mfma_f32_16x16x32_bf16 v[212:215], v[136:139], v[44:47], v[212:215]\n\t"
      "v_mfma_f32_16x16x32_bf16 v[200:203], v[140:143], v[48:51], v[200:203]\n\t"
      "v_mfma_f32_16x16x32_bf16 v[204:207], v[140:143], v[52:55], v[204:207]\n\t"
      "v_mfma_f32_16x16x32_bf16 v[208:211], v[140:143], v[56:59], v[208:211]\n\t"
      "v_mfma_f32_16x16x32_bf16 v[212:215], v[140:143], v[60:63], v[212:215]\n\t"
      "v_mfma_f32_16x16x32_bf16 v[200:203], v[144:147], v[64:67], v[200:203]\n\t"
      "v_mfma_f32_16x16x32_bf16 v[204:207], v[144:147], v[68:71], v[204:207]\n\t"
      "v_mfma_f32_16x16x32_bf16 v[208:211], v[144:147], v[72:75], v[208:211]\n\t"
      "v_mfma_f32_16x16x32_bf16 v[212:215], v[144:147], v[76:79], v[212:215]\n\t"
      "v_mfma_f32_16x16x32_bf16 v[200:203], v[148:151], v[80:83], v[200:203]\n\t"
      "v_mfma_f32_16x16x32_bf16 v[204:207], v[148:151], v[84:87], v[204:207]\n\t"
      "v_mfma_f32_16x16x32_bf16 v[208:211], v[148:151], v[88:91], v[208:211]\n\t"
      "v_mfma_f32_16x16x32_bf16 v[200:203], v[152:155], v[96:99], v[200:203]\n\t"
      "v_mfma_f32_16x16x32_bf16 v[204:207], v[152:155], v[100:103], v[204:207]\n\t"
      "v_mfma_f32_16x16x32_bf16 v[208:211], v[152:155], v[104:107], v[208:211]\n\t"
      "global_load_dwordx4 v[128:131], v224, s[64:65] sc1\n\t"
      "global_load_dwordx4 v[132:135], v225, s[64:65] sc1\n\t"
      "global_load_dwordx4 v[136:139], v226, s[64:65] sc1\n\t"
      "global_load_dwordx4 v[140:143], v227, s[64:65] sc1\n\t"
      "global_load_dwordx4 v[144:147], v228, s[64:65] sc1\n\t"
      "s_waitcnt vmcnt(0)\n\t"
      "s_nop 7\n\t"
      "s_branch NP5B_%=\n\t"
      "NP6R_%=:\n\t"
      "s_mov_b32 s51, 0\n\t"
      "NP6L_%=:\n\t"
      "s_add_u32 s51, s51, 1\n\t"
      "s_cmp_lt_u32 s51, 262144\n\t"
      "s_cbranch_scc0 NP6X_%=\n\t"
      "s_waitcnt vmcnt(0)\n\t"
      "s_sleep 1\n\t"
      "global_load_dwordx4 v[128:131], v224, s[64:65] sc1\n\t"
      "global_load_dwordx4 v[132:135], v225, s[64:65] sc1\n\t"
      "global_load_dwordx4 v[136:139], v226, s[64:65] sc1\n\t"
      "global_load_dwordx4 v[140:143], v227, s[64:65] sc1\n\t"
      "global_load_dwordx4 v[144:147], v228, s[64:65] sc1\n\t"
      "global_load_dwordx4 v[148:151], v229, s[64:65] sc1\n\t"
      "global_load_dwordx4 v[152:155], v230, s[64:65] sc1\n\t"
      "s_waitcnt vmcnt(0)\n\t"
      "v_max3_u32 v234, v128, v129, v130\n\t"
      "v_max3_u32 v234, v234, v131, v132\n\t"
      "v_max3_u32 v234, v234, v133, v134\n\t"
      "v_max3_u32 v234, v234, v135, v136\n\t"
      "v_max3_u32 v234, v234, v137, v138\n\t"
      "v_max3_u32 v234, v234, v139, v140\n\t"
      "v_max3_u32 v234, v234, v141, v142\n\t"
      "v_max3_u32 v234, v234, v143, v144\n\t"
      "v_max3_u32 v234, v234, v145, v146\n\t"
      "v_max3_u32 v234, v234, v147, v148\n\t"
      "v_max3_u32 v234, v234, v149, v150\n\t"
      "v_max3_u32 v234, v234, v151, v152\n\t"
      "v_max3_u32 v234, v234, v153, v154\n\t"
      "v_max_u32_e32 v234, v234, v155\n\t"
      "v_cmp_eq_u32_e32 vcc, -1, v234\n\t"
      "s_cbranch_vccnz NP6L_%=\n\t"
      "NP6X_%=:\n\t"
      "v_mfma_f32_16x16x32_bf16 v[184:187], v[128:131], v[0:3], v[180:183]\n\t"
      "v_mfma_f32_16x16x32_bf16 v[188:191], v[128:131], v[4:7], 0\n\t"
      "v_mfma_f32_16x16x32_bf16 v[192:195], v[128:131], v[8:11], 0\n\t"
      "v_mfma_f32_16x16x32_bf16 v[196:199], v[128:131], v[12:15], 0\n\t"
      "v_mfma_f32_16x16x32_bf16 v[184:187], v[132:135], v[16:19], v[184:187]\n\t"
      "v_mfma_f32_16x16x32_bf16 v[188:191], v[132:135], v[20:23], v[188:191]\n\t"
      "v_mfma_f32_16x16x32_bf16 v[192:195], v[132:135], v[24:27], v[192:195]\n\t"
      "v_mfma_f32_16x16x32_bf16 v[196:199], v[132:135], v[28:31], v[196:199]\n\t"
      "v_mfma_f32_16x16x32_bf16 v[184:187], v[136:139], v[32:35], v[184:187]\n\t"
      "v_mfma_f32_16x16x32_bf16 v[188:191], v[136:139], v[36:39], v[188:191]\n\t"
      "v_mfma_f32_16x16x32_bf16 v[192:195], v[136:139], v[40:43], v[192:195]\n\t"
      "v_mfma_f32_16x16x32_bf16 v[196:199], v[136:139], v[44:47], v[196:199]\n\t"
      "v_mfma_f32_16x16x32_bf16 v[184:187], v[140:143], v[48:51], v[184:187]\n\t"
      "v_mfma_f32_16x16x32_bf16 v[188:191], v[140:143], v[52:55], v[188:191]\n\t"
      "v_mfma_f32_16x16x32_bf16 v[192:195], v[140:143], v[56:59], v[192:195]\n\t"
      "v_mfma_f32_16x16x32_bf16 v[196:199], v[140:143], v[60:63], v[196:199]\n\t"
      "v_mfma_f32_16x16x32_bf16 v[184:187], v[144:147], v[64:67], v[184:187]\n\t"
      "v_mfma_f32_16x16x32_bf16 v[188:191], v[144:147], v[68:71], v[188:191]\n\t"
      "v_mfma_f32_16x16x32_bf16 v[192:195], v[144:147], v[72:75], v[192:195]\n\t"
      "v_mfma_f32_16x16x32_bf16 v[196:199], v[144:147], v[76:79], v[196:199]\n\t"
      "v_mfma_f32_16x16x32_bf16 v[184:187], v[148:151], v[80:83], v[184:187]\n\t"
      "v_mfma_f32_16x16x32_bf16 v[188:191], v[148:151], v[84:87], v[188:191]\n\t"
      "v_mfma_f32_16x16x32_bf16 v[192:195], v[148:151], v[88:91], v[192:195]\n\t"
      "v_mfma_f32_16x16x32_bf16 v[184:187], v[152:155], v[96:99], v[184:187]\n\t"
      "v_mfma_f32_16x16x32_bf16 v[188:191], v[152:155], v[100:103], v[188:191]\n\t"
      "v_mfma_f32_16x16x32_bf16 v[192:195], v[152:155], v[104:107], v[192:195]\n\t"
      "s_waitcnt vmcnt(0)\n\t"
      "s_nop 7\n\t"
      "s_branch NP6B_%=\n\t"
      : "+{s[4:19]}"(s), "+{s[20:27]}"(s2), "+{s[28:29]}"(kr), "+{s30}"(od), "+{v[240:247]}"(v), "+{v[136:139]}"(vo), "+{v140}"(vq)
      :
      : "memory", "vcc", "scc",
        "v0", "v1", "v2", "v3", "v4", "v5", "v6", "v7", "v8", "v9", "v10", "v11", "v12", "v13", "v14", "v15", "v16",
        "v17", "v18", "v19", "v20", "v21", "v22", "v23", "v24", "v25", "v26", "v27", "v28", "v29", "v30", "v31", "v32",
        "v33", "v34", "v35", "v36", "v37", "v38", "v39", "v40", "v41", "v42", "v43", "v44", "v45", "v46", "v47", "v48",
        "v49", "v50", "v51", "v52", "v53", "v54", "v55", "v56", "v57", "v58", "v59", "v60", "v61", "v62", "v63", "v64",
        "v65", "v66", "v67", "v68", "v69", "v70", "v71", "v72", "v73", "v74", "v75", "v76", "v77", "v78", "v79", "v80",
        "v81", "v82", "v83", "v84", "v85", "v86", "v87", "v88", "v89", "v90", "v91", "v92", "v93", "v94", "v95", "v96",
        "v97", "v98", "v99", "v100", "v101", "v102", "v103", "v104", "v105", "v106", "v107", "v108", "v109", "v110",
        "v111", "v112", "v113", "v114", "v115", "v116", "v117", "v118", "v119", "v120", "v121", "v122", "v123", "v124",
        "v125", "v126", "v127", "v128", "v129", "v130", "v131", "v132", "v133", "v134", "v135", "v141", "v142", "v143",
        "v144", "v145", "v146", "v147", "v148", "v149", "v150", "v151", "v152", "v153", "v154", "v155", "v156", "v157",
        "v158", "v159", "v160", "v161", "v162", "v163", "v164", "v165", "v166", "v167", "v168", "v169", "v170", "v171",
        "v172", "v173", "v174", "v175", "v176", "v177", "v178", "v179", "v180", "v181", "v182", "v183", "v184", "v185",
        "v186", "v187", "v188", "v189", "v190", "v191", "v192", "v193", "v194", "v195", "v196", "v197", "v198", "v199",
        "v200", "v201", "v202", "v203", "v204", "v205", "v206", "v207", "v208", "v209", "v210", "v211", "v212", "v213",
        "v214", "v215", "v216", "v217", "v218", "v219", "v220", "v221", "v222", "v223", "v224", "v225", "v226", "v227",
        "v228", "v229", "v230", "v231", "v232", "v233", "v234", "v235", "v236", "v237", "v238", "v239", "v248", "v249",
        "v250", "v251", "v252", "v253", "s31", "s44", "s45", "s46", "s47", "s48", "s49", "s50", "s51", "s52", "s53",
        "s54", "s55", "s56", "s57", "s58", "s59", "s60", "s61", "s62", "s63", "s64", "s65", "s66", "s67", "s68", "s69",
        "s70"
    );
    __builtin_amdgcn_endpgm();
    }
  }
}

extern "C" void kernel_launch(void* const* d_in, const int* in_sizes, int n_in, void* d_out, int out_size,
                              void* d_ws, size_t ws_size, hipStream_t stream) {
  (void)hipFuncSetAttribute((const void*)fused_block, hipFuncAttributeMaxDynamicSharedMemorySize, LDS_BYTES);
  fused_block<<<256, 512, LDS_BYTES, stream>>>((const char*)d_in[0], (const char*)d_in[1], (const char*)d_in[2],
                                               (const char*)d_in[4], (const char*)d_in[5], (const char*)d_in[6],
                                               (const char*)d_in[7], (char*)d_out, (char*)d_ws);
}
```

```cpp
#include <hip/hip_runtime.h>
#include <stdint.h>

#define WS_ST 0
#define WS_HN 2097152
#define WS_O 4194304
#define WS_G 6291456
#define ST_FL 65536
#define ST_PW 114688
#define WS_EP 131072
#define PROBE 1835008
#define ST_CK 3981312
#define ST_H2 4112384
#define ST_H3 8175616
#define ST_H4 8306688
#define A_UNIT 5120
#define A_HALO 40960
#define A_SLOT 43008
#define A_CS 131072
#define A_ST 132096
#define A_EX 142336
#define A_FL 154624
#define A_MK 154752
#define B_UNIT 7168
#define B_SLOT 57344
#define B_X 114688
#define B_OP 928
#define B_PS 129536
#define B_WX 130560
#define B_WY 66048
#define G_WV 16384
#define A_W0 49152
#define A_WD 40960
#define G_RED 0
#define G_RED2 32768
#define LDS_KEY 163600
#define LDS_SINK 163328
#define LDS_BYTES 163840
#define XO 0
#define M44 1
#define PRE 2
#define GKM 1
#define PF 0
#define KROT 14
#define KMUL 1
#define HP4 1
#define W2E 1
#define R4 0
#define SP3 1

typedef unsigned short u16;
typedef unsigned long long u64;
typedef __attribute__((ext_vector_type(4))) unsigned int u32x4;
typedef __attribute__((ext_vector_type(8))) unsigned int u32x8;
typedef __attribute__((ext_vector_type(16))) unsigned int u32x16;
#define UNI(x) (unsigned)__builtin_amdgcn_readfirstlane((int)(x))
#define PIN() __builtin_amdgcn_sched_barrier(0)

__device__ __forceinline__ int swz_tr(int row) { return (((row >> 1) & 1) | (((row >> 3) & 1) << 1)) << 1; }
__device__ __forceinline__ int swz_row(int row) { return (row >> 1) & 7; }
__device__ __forceinline__ int unit(int blk, int slice, int par) { return (((blk < 14 ? blk : blk - 14) * 8 + slice) * 2 + ((blk < 14 ? 0 : 1) ^ par)) * 8192; }
__device__ __forceinline__ int part(int n, int side) { return (n >> 13) * 16384 + side * 8192 + (n & 8191); }
template <class T>
__device__ __forceinline__ void put(T& s, int i, u64 p) { s[i] = (unsigned)p; s[i + 1] = (unsigned)(p >> 32); }
template <class T>
__device__ __forceinline__ void put2(T& s, int i, unsigned lo, unsigned hi) { s[i] = UNI(lo); s[i + 1] = UNI(hi); }

__global__ __launch_bounds__(512, 2) __attribute__((aligned(4096))) void fused_block(const char* __restrict__ x, const char* __restrict__ wqkv,
                                                   const char* __restrict__ wo, const char* __restrict__ w1,
                                                   const char* __restrict__ b1, const char* __restrict__ w2,
                                                   const char* __restrict__ b2, char* __restrict__ out,
                                                   char* __restrict__ ws0) {
  u32x8 f;
  asm volatile(
      "s_load_dwordx4 s[4:7], s[0:1], 0x0\n\t"
      "v_readfirstlane_b32 s3, v0\n\t"
      "s_getpc_b64 s[40:41]\n\t"
      "s_lshr_b32 s12, s2, 3\n\t"
      "s_or_b32 s13, s3, s12\n\t"
      "s_and_b32 s13, s13, 0x1d0\n\t"
      "s_cmp_lg_u32 s13, 16\n\t"
      "s_cbranch_scc1 ET2_%=\n\t"
      "s_and_b32 s12, s12, 7\n\t"
      "s_mov_b32 m0, 163328\n\t"
      "v_lshl_add_u32 v3, s12, 6, v0\n\t"
      "v_min_u32_e32 v3, 495, v3\n\t"
      "v_lshlrev_b32_e32 v3, 7, v3\n\t"
      "global_load_lds_dword v3, s[40:41]\n\t"
      "ET2_%=:\n\t"
      "v_lshrrev_b32_e32 v1, 3, v0\n\t"
      "v_and_b32_e32 v2, 7, v0\n\t"
      "s_and_b32 s17, s2, 7\n\t"
      "s_lshr_b32 s18, s2, 3\n\t"
      "s_lshr_b32 s3, s3, 6\n\t"
      "s_cmpk_gt_u32 s2, 0x7f\n\t"
      "s_cbranch_scc1 EB_%=\n\t"
      "v_and_b32_e32 v3, 2, v1\n\t"
      "v_lshrrev_b32_e32 v4, 1, v1\n\t"
      "s_lshr_b32 s19, s18, 2\n\t"
      "s_and_b32 s12, s17, 6\n\t"
      "s_lshl1_add_u32 s19, s12, s19\n\t"
      "v_and_or_b32 v3, v4, 4, v3\n\t"
      "v_xor_b32_e32 v3, v3, v2\n\t"
      "v_min_u32_e32 v3, 6, v3\n\t"
      "v_mul_u32_u24_e32 v5, 2688, v1\n\t"
      "v_lshl_add_u32 v3, v3, 4, v5\n\t"
      "v_add_u32_e32 v195, 2048, v3\n\t"
      "v_add_u32_e32 v196, 1920, v3\n\t"
      "v_add_u32_e32 v197, 5888, v3\n\t"
      "s_mul_i32 s12, s19, 114688\n\t"
      "v_and_b32_e32 v5, 0x38, v1\n\t"
      "v_add_u32_e32 v5, v5, v1\n\t"
      "v_mul_u32_u24_e32 v5, 896, v5\n\t"
      "s_and_b32 s14, s17, 1\n\t"
      "s_add_u32 s14, s14, 3\n\t"
      "s_and_b32 s13, s18, 3\n\t"
      "s_xor_b32 s13, s13, s14\n\t"
      "s_mul_i32 s13, s13, 112\n\t"
      "v_and_b32_e32 v6, 3, v4\n\t"
      "v_xor_b32_e32 v6, v6, v2\n\t"
      "v_lshl_add_u32 v5, v6, 4, v5\n\t"
      "s_sub_u32 s12, s12, 15360\n\t"
      "s_ashr_i32 s14, s12, 31\n\t"
      "s_sub_u32 s13, s13, 4096\n\t"
      "v_add_u32_e32 v192, 15360, v5\n\t"
      "v_xor_b32_e32 v5, 64, v5\n\t"
      "v_add_u32_e32 v193, 21504, v5\n\t"
      "s_mul_i32 s9, s3, 5120\n\t"
      "s_sub_u32 s3, s3, 4\n\t"
      "s_cbranch_scc0 EH_%=\n\t"
      "s_waitcnt lgkmcnt(0)\n\t"
      "s_add_u32 s4, s4, s12\n\t"
      "s_addc_u32 s5, s5, s14\n\t"
      "s_add_u32 s6, s6, s13\n\t"
      "s_addc_u32 s7, s7, -1\n\t"
      "s_add_u32 m0, s9, 0\n\t"
      "s_nop 0\n\t"
      "global_load_lds_dwordx4 v195, s[6:7] offset:2048\n\t"
      "global_load_lds_dwordx4 v192, s[4:5]\n\t"
      "global_load_lds_dwordx4 v193, s[4:5] offset:1024\n\t"
      "global_load_lds_dwordx4 v196, s[6:7] offset:3072\n\t"
      "s_add_u32 m0, s9, 4096\n\t"
      "s_nop 0\n\t"
      "global_load_lds_dwordx4 v197, s[6:7]\n\t"
      "s_branch EX_%=\n\t"
      "EH_%=:\n\t"
      "s_cmp_eq_u32 s19, 0\n\t"
      "s_cselect_b32 s13, 16, 0\n\t"
      "s_lshl2_add_u32 s13, s3, s13\n\t"
      "v_and_or_b32 v3, v1, 3, s13\n\t"
      "v_bfe_u32 v4, v3, 1, 3\n\t"
      "v_mul_u32_u24_e32 v3, 896, v3\n\t"
      "v_xor_b32_e32 v4, v4, v2\n\t"
      "v_lshl_add_u32 v3, v4, 4, v3\n\t"
      "v_add_u32_e32 v194, 1024, v3\n\t"
      "s_waitcnt lgkmcnt(0)\n\t"
      "s_branch EX_%=\n\t"
      "EB_%=:\n\t"
      "v_and_b32_e32 v3, 63, v0\n\t"
      "v_lshlrev_b32_e32 v3, 4, v3\n\t"
      "s_sub_u32 s18, s18, 16\n\t"
      "s_min_u32 s16, s3, 6\n\t"
      "s_xor_b32 s15, s17, 1\n\t"
      "s_lshl4_add_u32 s15, s15, s18\n\t"
      "s_mul_i32 s15, s15, 14336\n\t"
      "s_lshl_b32 s12, s16, 7\n\t"
      "s_add_u32 s15, s15, s12\n\t"
      "v_mul_u32_u24_e32 v6, 56, v3\n\t"
      "v_add_u32_e32 v6, s15, v6\n\t"
      "s_add_u32 s19, s17, 0\n\t"
      "s_and_b32 s19, s19, 3\n\t"
      "s_and_b32 s12, s17, 4\n\t"
      "s_or_b32 s19, s19, s12\n\t"
      "s_lshl4_add_u32 s19, s19, s18\n\t"
      "s_lshl3_add_u32 s12, s19, s3\n\t"
      "s_lshl_b32 s12, s12, 11\n\t"
      "s_mul_i32 s19, s19, 14336\n\t"
      "s_lshl_b32 s16, s16, 11\n\t"
      "s_add_u32 s19, s19, s16\n\t"
      "s_add_u32 s8, s16, 114688\n\t"
      "v_add_u32_e32 v1, s19, v3\n\t"
      "s_lshl4_add_u32 s16, s3, s18\n\t"
      "v_bfe_u32 v4, v0, 2, 2\n\t"
      "v_mul_u32_u24_e32 v4, 896, v4\n\t"
      "v_and_b32_e32 v5, 48, v3\n\t"
      "v_lshl_add_u32 v4, v5, 3, v4\n\t"
      "s_and_b32 s13, s17, 1\n\t"
      "s_mul_i32 s13, s13, 384\n\t"
      "v_add_u32_e32 v4, s13, v4\n\t"
      "v_lshrrev_b32_e32 v2, 8, v3\n\t"
      "s_mul_i32 s13, s16, 1\n\t"
      "s_mul_i32 s14, s16, 3\n\t"
      "s_add_u32 s14, s14, 128\n\t"
      "v_add_u32_e32 v5, s13, v2\n\t"
      "v_add_u32_e32 v2, s14, v2\n\t"
      "v_min_u32_e32 v2, 447, v2\n\t"
      "s_movk_i32 s13, 2688\n\t"
      "v_mad_u32_u24 v5, v5, s13, v4\n\t"
      "v_mad_u32_u24 v4, v2, s13, v4\n\t"
      "s_mov_b32 m0, 163328\n\t"
      "s_waitcnt lgkmcnt(0)\n\t"
      "s_mov_b32 m0, s8\n\t"
      "s_nop 0\n\t"
      "global_load_lds_dwordx4 v1, s[4:5]\n\t"
      "global_load_lds_dwordx4 v1, s[4:5] offset:1024\n\t"
      "s_mov_b32 m0, 163328\n\t"
      "s_mov_b32 exec_lo, 0xfff\n\t"
      "s_mov_b32 exec_hi, 0x0\n\t"
      "global_load_lds_dword v5, s[6:7]\n\t"
      "s_mov_b64 exec, -1\n\t"
      "s_barrier\n\t"
      "s_mov_b32 exec_lo, 0xfff0fff\n\t"
      "s_mov_b32 exec_hi, 0xfff\n\t"
      "global_load_lds_dword v4, s[6:7]\n\t"
      "s_mov_b64 exec, -1\n\t"
      "s_mov_b64 exec, 0xffff\n\t"
      "global_load_lds_dword v6, s[4:5]\n\t"
      "s_mov_b64 exec, -1\n\t"
      "EX_%=:\n\t"
      : "={v[192:199]}"(f)
      : "{s[0:1]}"(__builtin_amdgcn_kernarg_segment_ptr()), "{s2}"(blockIdx.x), "{v0}"(threadIdx.x)
      : "memory", "scc", "s3", "s4", "s5", "s6", "s7", "s8", "s9", "s10", "s11", "s12", "s13", "s14", "s15", "s16", "s17", "s18", "s19", "v1", "v2",
        "v3", "v4", "v5", "v6", "s40", "s41", "s42", "s43", "s48", "s49", "s50"
      );
  PIN();
  char* const ws = ws0 + (-(unsigned)(uintptr_t)ws0 & ((2u << 20) - 1));
  const int wave = __builtin_amdgcn_readfirstlane(threadIdx.x >> 6);
  const int xcd = blockIdx.x & 7, idx = blockIdx.x >> 3, side = xcd >> 2, xp = xcd & 3;
#define LANE_BITS(l) \
  const int lane = (l), g = lane >> 4, c = lane & 15, q4 = c >> 2, p4 = lane & 3, lr = lane >> 3, lc = lane & 7

  if (__builtin_expect(idx < 16, 1)) {
    const int rt = M44 ? 2 * (xcd & 6) + (idx >> 2) : 2 * xcd + (idx >> 3), hp = M44 ? (idx & 3) ^ (3 + (xcd & 1)) : idx & 7, hh = (wave >> 1) & 1, half = wave & 1;
    const unsigned sta = A_ST + half * 1024;
    if (wave < 4) {
      LANE_BITS(threadIdx.x & 63);
      u32x16 v;
      u32x8 s;
      v[0] = 4 * half * A_UNIT + c * 128 + ((g ^ swz_row(c)) << 4);
      v[1] = A_HALO + c * 128 + ((g ^ swz_row(c)) << 4);
#pragma unroll
      for (int j = 0; j < 2; ++j) {
        const int by = 56 * hh + 32 * j + 8 * p4;
        v[2 + j] = g * A_UNIT + 2048 + q4 * 128 + (((by >> 4) ^ swz_tr(8 * g + q4)) << 4) + (by & 8);
      }
      const unsigned csa = A_CS + hh * 384, exa = A_EX + hh * 3 * 2048;
      v[4] = csa + g * 16;
      v[5] = csa + c * 4;
      v[6] = v[4] + 64 * half;
      v[7] = v[5] + 64 * half;
      v[8] = A_FL + (wave ^ 1) * 8;
      v[9] = sta + g * 16;
      v[10] = sta + c * 4;
      const int key = 4 * g + (c & 1);
      v[11] = sta + 384 + (key <= c ? 128 : 64) + key * 4;
      unsigned vk1 = sta + 384 + (key + 2 <= c ? 128 : 64) + key * 4 + 8;
      v[12] = exa + half * 2048 + lane * 16;
      v[13] = exa + half * 2048 + lane * 8;
      v[14] = exa + lane * 16 + 8 * half;
      v[15] = lane * 16;
      put(s, 0, (u64)(ws + WS_O + part(((8 * (rt & 7) + 4 * half) * 16 + 2 * hp + hh) * 1024, 0)));
      s[2] = half;
      s[3] = UNI(rt == 0 && half == 0 ? 0xff800000u : 0u);
      s[4] = exa + 1024 + 512 * half;
      s[5] = A_FL + 32 + 4 * half;
      put(s, 6, (u64)(ws + WS_ST));
      unsigned hint = ST_H2 + part(((((rt & 7) * 8 + hp) * 2 + half) * 2 + hh) * 64, side);
      unsigned chk = ST_CK + part((((rt & 7) * 2 + half) * 16 + 2 * hp + hh) * 256, side);
      unsigned vn = (((wave ^ (4 * side)) * 256 + (idx & 1) * 128 + lr) * 28 + 7 * xp + min(idx >> 1, 6)) * 128 + ((lc ^ swz_tr(lr)) << 4);
      u64 tw = (u64)w1;
      unsigned lw = wave * 4096;
      u64 pf = tw;
    asm volatile(
      "s_waitcnt lgkmcnt(0)\n\ts_nop 4\n\t"
      ".p2align 3\n\t"
      "v_xor_b32_e64 v248, 64, v224\n\t"
      "v_xor_b32_e32 v249, 64, v225\n\t"
      "s_mov_b32 s36, 0xaaaaaaaa\n\t"
      "s_mov_b32 s37, 0xaaaaaaaa\n\t"
      "s_mov_b32 s38, 0xfff0ffff\n\t"
      "s_mov_b32 s39, 0xf000ff00\n\t"
      "s_mov_b32 s40, 0xffc0fffc\n\t"
      "s_mov_b32 s41, 0xc000fc00\n\t"
      "s_mov_b32 s42, 0x0\n\t"
      "s_mov_b32 s43, 0xffff0000\n\t"
      "s_mov_b32 s44, 0x10001\n\t"
      "s_mov_b32 s45, 0x10001\n\t"
      "v_mov_b32_e32 v218, 0x0c0c0c0c\n\t"
      "v_mov_b32_e32 v216, 0xc0c0100\n\t"
      "v_mov_b32_e32 v217, 0x1000c0c\n\t"
      "v_cndmask_b32_e64 v216, v216, v217, s[36:37]\n\t"
      "v_cndmask_b32_e64 v240, v216, v218, s[38:39]\n\t"
      "v_cndmask_b32_e64 v242, v218, v216, s[38:39]\n\t"
      "v_mov_b32_e32 v216, 0xc0c0302\n\t"
      "v_mov_b32_e32 v217, 0x3020c0c\n\t"
      "v_cndmask_b32_e64 v216, v216, v217, s[36:37]\n\t"
      "v_cndmask_b32_e64 v241, v216, v218, s[40:41]\n\t"
      "v_cndmask_b32_e64 v243, v218, v216, s[40:41]\n\t"
      "v_xor_b32_e32 v213, 8, v232\n\t"
      "v_lshrrev_b32_e32 v214, 1, v239\n\t"
      "v_add_u32_e32 v214, s8, v214\n\t"
      "v_mov_b32_e32 v215, s9\n\t"
      "v_mov_b32_e32 v220, 154752\n\t"
      "v_lshrrev_b32_e32 v221, 2, v239\n\t"
      "s_add_u32 s56, s4, 32768\n\t"
      "s_addc_u32 s57, s5, 0\n\t"
      "s_add_u32 s58, s4, 65536\n\t"
      "s_addc_u32 s59, s5, 0\n\t"
      "s_add_u32 s60, s4, 98304\n\t"
      "s_addc_u32 s61, s5, 0\n\t"
      "s_and_b32 s52, s12, 0x2000\n\t"
      "v_mov_b32_e32 v244, s7\n\t"
      "v_mov_b32_e32 v245, s7\n\t"
      "v_mov_b32_e32 v246, s7\n\t"
      "v_mov_b32_e32 v247, s7\n\t"
      "v_mov_b64_e32 v[0:1], 0\n\t"
      "v_mov_b64_e32 v[2:3], 0\n\t"
      "v_mov_b64_e32 v[4:5], 0\n\t"
      "v_mov_b64_e32 v[6:7], 0\n\t"
      "v_mov_b64_e32 v[8:9], 0\n\t"
      "v_mov_b64_e32 v[10:11], 0\n\t"
      "v_mov_b64_e32 v[12:13], 0\n\t"
      "v_mov_b64_e32 v[14:15], 0\n\t"
      "v_mov_b64_e32 v[16:17], 0\n\t"
      "v_mov_b64_e32 v[18:19], 0\n\t"
      "v_mov_b64_e32 v[20:21], 0\n\t"
      "v_mov_b64_e32 v[22:23], 0\n\t"
      "v_mov_b64_e32 v[24:25], 0\n\t"
      "v_mov_b64_e32 v[26:27], 0\n\t"
      "v_mov_b64_e32 v[28:29], 0\n\t"
      "v_mov_b64_e32 v[30:31], 0\n\t"
      "v_mov_b64_e32 v[32:33], 0\n\t"
      "v_mov_b64_e32 v[34:35], 0\n\t"
      "v_mov_b64_e32 v[36:37], 0\n\t"
      "v_mov_b64_e32 v[38:39], 0\n\t"
      "v_mov_b64_e32 v[40:41], 0\n\t"
      "v_mov_b64_e32 v[42:43], 0\n\t"
      "v_mov_b64_e32 v[44:45], 0\n\t"
      "v_mov_b64_e32 v[46:47], 0\n\t"
      "v_mov_b64_e32 v[48:49], 0\n\t"
      "v_mov_b64_e32 v[50:51], 0\n\t"
      "v_mov_b64_e32 v[52:53], 0\n\t"
      "v_mov_b64_e32 v[54:55], 0\n\t"
      "v_mov_b64_e32 v[56:57], 0\n\t"
      "v_mov_b64_e32 v[58:59], 0\n\t"
      "v_mov_b64_e32 v[60:61], 0\n\t"
      "v_mov_b64_e32 v[62:63], 0\n\t"
      "v_mov_b64_e32 v[64:65], 0\n\t"
      "v_mov_b64_e32 v[66:67], 0\n\t"
      "v_mov_b64_e32 v[68:69], 0\n\t"
      "v_mov_b64_e32 v[70:71], 0\n\t"
      "v_mov_b64_e32 v[72:73], 0\n\t"
      "v_mov_b64_e32 v[74:75], 0\n\t"
      "v_mov_b64_e32 v[76:77], 0\n\t"
      "v_mov_b64_e32 v[78:79], 0\n\t"
      "v_mov_b64_e32 v[80:81], 0\n\t"
      "v_mov_b64_e32 v[82:83], 0\n\t"
      "v_mov_b64_e32 v[84:85], 0\n\t"
      "v_mov_b64_e32 v[86:87], 0\n\t"
      "v_mov_b64_e32 v[88:89], 0\n\t"
      "v_mov_b64_e32 v[90:91], 0\n\t"
      "v_mov_b64_e32 v[92:93], 0\n\t"
      "v_mov_b64_e32 v[94:95], 0\n\t"
      "v_mov_b64_e32 v[96:97], 0\n\t"
      "v_mov_b64_e32 v[98:99], 0\n\t"
      "v_mov_b64_e32 v[100:101], 0\n\t"
      "v_mov_b64_e32 v[102:103], 0\n\t"
      "v_mov_b64_e32 v[104:105], 0\n\t"
      "v_mov_b64_e32 v[106:107], 0\n\t"
      "s_mov_b32 s47, 6\n\t"
      "s_mov_b64 exec, 0\n\t"
      "s_cmpk_eq_u32 s16, 0x0\n\t"
      "s_cbranch_scc1 .LhopA0\n\t"
      "s_cmpk_eq_u32 s16, 0x1000\n\t"
      "s_cbranch_scc1 .LhopA1\n\t"
      "s_cmpk_eq_u32 s16, 0x2000\n\t"
      "s_cbranch_scc1 .LhopA2\n\t"
      "s_cmpk_eq_u32 s16, 0x3000\n\t"
      "s_cbranch_scc1 .LhopA3\n\t"
      ".LbackA:\n\t"
      "s_mov_b64 exec, -1\n\t"
      "s_cmp_eq_u32 s6, 0\n\t"
      "s_cbranch_scc0 LOOP1_%=\n\t"
      "s_waitcnt vmcnt(0)\n\t"
      "s_barrier\n\t"
      "ds_read_b64_tr_b16 v[108:109], v226\n\t"
      "ds_read_b64_tr_b16 v[110:111], v226 offset:512\n\t"
      "ds_read_b128 v[132:135], v224\n\t"
      "ds_read_b128 v[136:139], v224 offset:5120\n\t"
      "ds_read_b128 v[140:143], v224 offset:10240\n\t"
      "ds_read_b128 v[144:147], v224 offset:15360\n\t"
      "ds_read_b64_tr_b16 v[112:113], v227\n\t"
      "ds_read_b64_tr_b16 v[114:115], v227 offset:512\n\t"
      "ds_read_b128 v[148:151], v225\n\t"
      "ds_read_b64_tr_b16 v[116:117], v226 offset:1024\n\t"
      "ds_read_b64_tr_b16 v[118:119], v226 offset:1536\n\t"
      "ds_read_b64_tr_b16 v[120:121], v227 offset:1024\n\t"
      "ds_read_b64_tr_b16 v[122:123], v227 offset:1536\n\t"
      "ds_read_b64_tr_b16 v[124:125], v226 offset:2048\n\t"
      "ds_read_b64_tr_b16 v[126:127], v226 offset:2560\n\t"
      "ds_read_b64_tr_b16 v[128:129], v227 offset:2048\n\t"
      "ds_read_b64_tr_b16 v[130:131], v227 offset:2560\n\t"
      "s_branch EVEN0_%=\n\t"
      "s_waitcnt lgkmcnt(0)\n\t"
      ".p2align 6\n\t"
      ".LhopA1:\n\t"
      "s_cbranch_execz .LhopA5\n\t"
      "s_nop 0\n\t"
      "ODD0_%=:\n\t"
      "s_waitcnt lgkmcnt(0)\n\t"
      "v_mfma_f32_16x16x32_bf16 v[0:3], v[152:155], v[176:179], v[0:3]\n\t"
      "v_add_u32_e32 v224, s46, v224\n\t"
      "v_add_u32_e32 v226, s46, v226\n\t"
      "v_mfma_f32_16x16x32_bf16 v[24:27], v[152:155], v[180:183], v[24:27]\n\t"
      "v_add_u32_e32 v227, s46, v227\n\t"
      "v_add_u32_e32 v225, s46, v225\n\t"
      "v_mfma_f32_16x16x32_bf16 v[48:51], v[152:155], v[184:187], v[48:51]\n\t"
      "s_barrier\n\t"
      "ds_read_b64_tr_b16 v[108:109], v226\n\t"
      "v_mfma_f32_16x16x32_bf16 v[72:75], v[152:155], v[188:191], v[72:75]\n\t"
      "ds_read_b64_tr_b16 v[110:111], v226 offset:512\n\t"
      "v_mfma_f32_16x16x32_bf16 v[4:7], v[156:159], v[176:179], v[4:7]\n\t"
      "ds_read_b128 v[132:135], v224\n\t"
      "v_mfma_f32_16x16x32_bf16 v[28:31], v[156:159], v[180:183], v[28:31]\n\t"
      "ds_read_b128 v[136:139], v224 offset:5120\n\t"
      "v_mfma_f32_16x16x32_bf16 v[52:55], v[156:159], v[184:187], v[52:55]\n\t"
      "ds_read_b128 v[140:143], v224 offset:10240\n\t"
      "v_mfma_f32_16x16x32_bf16 v[76:79], v[156:159], v[188:191], v[76:79]\n\t"
      "ds_read_b128 v[144:147], v224 offset:15360\n\t"
      "v_mfma_f32_16x16x32_bf16 v[96:99], v[152:155], v[192:195], v[96:99]\n\t"
      "v_mfma_f32_16x16x32_bf16 v[8:11], v[160:163], v[176:179], v[8:11]\n\t"
      "ds_read_b64_tr_b16 v[112:113], v227\n\t"
      "v_mfma_f32_16x16x32_bf16 v[32:35], v[160:163], v[180:183], v[32:35]\n\t"
      "ds_read_b64_tr_b16 v[114:115], v227 offset:512\n\t"
      "v_mfma_f32_16x16x32_bf16 v[56:59], v[160:163], v[184:187], v[56:59]\n\t"
      "ds_read_b128 v[148:151], v225\n\t"
      "v_mfma_f32_16x16x32_bf16 v[80:83], v[160:163], v[188:191], v[80:83]\n\t"
      "ds_read_b64_tr_b16 v[116:117], v226 offset:1024\n\t"
      "v_mfma_f32_16x16x32_bf16 v[12:15], v[164:167], v[176:179], v[12:15]\n\t"
      "ds_read_b64_tr_b16 v[118:119], v226 offset:1536\n\t"
      "v_mfma_f32_16x16x32_bf16 v[36:39], v[164:167], v[180:183], v[36:39]\n\t"
      "ds_read_b64_tr_b16 v[120:121], v227 offset:1024\n\t"
      "v_mfma_f32_16x16x32_bf16 v[60:63], v[164:167], v[184:187], v[60:63]\n\t"
      "v_mfma_f32_16x16x32_bf16 v[84:87], v[164:167], v[188:191], v[84:87]\n\t"
      "ds_read_b64_tr_b16 v[122:123], v227 offset:1536\n\t"
      "v_mfma_f32_16x16x32_bf16 v[100:103], v[160:163], v[192:195], v[100:103]\n\t"
      "ds_read_b64_tr_b16 v[124:125], v226 offset:2048\n\t"
      "v_mfma_f32_16x16x32_bf16 v[16:19], v[176:179], v[168:171], v[16:19]\n\t"
      "ds_read_b64_tr_b16 v[126:127], v226 offset:2560\n\t"
      "v_mfma_f32_16x16x32_bf16 v[40:43], v[180:183], v[168:171], v[40:43]\n\t"
      "ds_read_b64_tr_b16 v[128:129], v227 offset:2048\n\t"
      "v_mfma_f32_16x16x32_bf16 v[64:67], v[184:187], v[168:171], v[64:67]\n\t"
      "ds_read_b64_tr_b16 v[130:131], v227 offset:2560\n\t"
      "v_mfma_f32_16x16x32_bf16 v[88:91], v[188:191], v[168:171], v[88:91]\n\t"
      "v_mfma_f32_16x16x32_bf16 v[20:23], v[176:179], v[172:175], v[20:23]\n\t"
      "v_mfma_f32_16x16x32_bf16 v[44:47], v[180:183], v[172:175], v[44:47]\n\t"
      "v_add_u32_e32 v248, s46, v248\n\t"
      "v_add_u32_e32 v249, s46, v249\n\t"
      "v_mfma_f32_16x16x32_bf16 v[68:71], v[184:187], v[172:175], v[68:71]\n\t"
      "v_mfma_f32_16x16x32_bf16 v[92:95], v[188:191], v[172:175], v[92:95]\n\t"
      "v_mfma_f32_16x16x32_bf16 v[104:107], v[192:195], v[168:171], v[104:107]\n\t"
      ".LhopA2:\n\t"
      "s_cbranch_execz .LhopA6\n\t"
      "EVEN0_%=:\n\t"
      "s_waitcnt lgkmcnt(0)\n\t"
      "v_mfma_f32_16x16x32_bf16 v[0:3], v[108:111], v[132:135], v[0:3]\n\t"
      "ds_read_b64_tr_b16 v[152:153], v226 offset:20480\n\t"
      "v_mfma_f32_16x16x32_bf16 v[24:27], v[108:111], v[136:139], v[24:27]\n\t"
      "ds_read_b64_tr_b16 v[154:155], v226 offset:20992\n\t"
      "v_mfma_f32_16x16x32_bf16 v[48:51], v[108:111], v[140:143], v[48:51]\n\t"
      "ds_read_b128 v[176:179], v248\n\t"
      "v_mfma_f32_16x16x32_bf16 v[72:75], v[108:111], v[144:147], v[72:75]\n\t"
      "ds_read_b128 v[180:183], v248 offset:5120\n\t"
      "v_mfma_f32_16x16x32_bf16 v[4:7], v[112:115], v[132:135], v[4:7]\n\t"
      "v_mfma_f32_16x16x32_bf16 v[28:31], v[112:115], v[136:139], v[28:31]\n\t"
      "ds_read_b128 v[184:187], v248 offset:10240\n\t"
      "v_mfma_f32_16x16x32_bf16 v[52:55], v[112:115], v[140:143], v[52:55]\n\t"
      "ds_read_b128 v[188:191], v248 offset:15360\n\t"
      "v_mfma_f32_16x16x32_bf16 v[76:79], v[112:115], v[144:147], v[76:79]\n\t"
      "ds_read_b64_tr_b16 v[156:157], v227 offset:20480\n\t"
      "v_mfma_f32_16x16x32_bf16 v[96:99], v[108:111], v[148:151], v[96:99]\n\t"
      "v_mfma_f32_16x16x32_bf16 v[8:11], v[116:119], v[132:135], v[8:11]\n\t"
      "ds_read_b64_tr_b16 v[158:159], v227 offset:20992\n\t"
      "v_mfma_f32_16x16x32_bf16 v[32:35], v[116:119], v[136:139], v[32:35]\n\t"
      "ds_read_b128 v[192:195], v249\n\t"
      "v_mfma_f32_16x16x32_bf16 v[56:59], v[116:119], v[140:143], v[56:59]\n\t"
      "ds_read_b64_tr_b16 v[160:161], v226 offset:21504\n\t"
      "v_mfma_f32_16x16x32_bf16 v[80:83], v[116:119], v[144:147], v[80:83]\n\t"
      "ds_read_b64_tr_b16 v[162:163], v226 offset:22016\n\t"
      "v_mfma_f32_16x16x32_bf16 v[12:15], v[120:123], v[132:135], v[12:15]\n\t"
      "v_mfma_f32_16x16x32_bf16 v[36:39], v[120:123], v[136:139], v[36:39]\n\t"
      "ds_read_b64_tr_b16 v[164:165], v227 offset:21504\n\t"
      "v_mfma_f32_16x16x32_bf16 v[60:63], v[120:123], v[140:143], v[60:63]\n\t"
      "ds_read_b64_tr_b16 v[166:167], v227 offset:22016\n\t"
      "v_mfma_f32_16x16x32_bf16 v[84:87], v[120:123], v[144:147], v[84:87]\n\t"
      "ds_read_b64_tr_b16 v[168:169], v226 offset:22528\n\t"
      "v_mfma_f32_16x16x32_bf16 v[100:103], v[116:119], v[148:151], v[100:103]\n\t"
      "v_mfma_f32_16x16x32_bf16 v[16:19], v[132:135], v[124:127], v[16:19]\n\t"
      "ds_read_b64_tr_b16 v[170:171], v226 offset:23040\n\t"
      "v_mfma_f32_16x16x32_bf16 v[40:43], v[136:139], v[124:127], v[40:43]\n\t"
      "ds_read_b64_tr_b16 v[172:173], v227 offset:22528\n\t"
      "v_mfma_f32_16x16x32_bf16 v[64:67], v[140:143], v[124:127], v[64:67]\n\t"
      "ds_read_b64_tr_b16 v[174:175], v227 offset:23040\n\t"
      "s_cmp_eq_u32 s47, 4\n\t"
      "s_cselect_b32 s46, -2, 1\n\t"
      "v_mfma_f32_16x16x32_bf16 v[88:91], v[144:147], v[124:127], v[88:91]\n\t"
      "s_cmp_eq_u32 s47, 1\n\t"
      "s_cselect_b32 s46, -2, s46\n\t"
      "v_mfma_f32_16x16x32_bf16 v[20:23], v[132:135], v[128:131], v[20:23]\n\t"
      "s_mul_i32 s46, s46, 43008\n\t"
      "v_mfma_f32_16x16x32_bf16 v[44:47], v[136:139], v[128:131], v[44:47]\n\t"
      "v_mfma_f32_16x16x32_bf16 v[68:71], v[140:143], v[128:131], v[68:71]\n\t"
      "v_mfma_f32_16x16x32_bf16 v[92:95], v[144:147], v[128:131], v[92:95]\n\t"
      "s_sub_u32 s47, s47, 1\n\t"
      "v_mfma_f32_16x16x32_bf16 v[104:107], v[148:151], v[124:127], v[104:107]\n\t"
      "s_cbranch_scc0 ODD0_%=\n\t"
      "s_waitcnt lgkmcnt(0)\n\t"
      "v_mfma_f32_16x16x32_bf16 v[96:99], v[152:155], v[192:195], v[96:99]\n\t"
      "ds_read_b32 v212, v215\n\t"
      "v_mfma_f32_16x16x32_bf16 v[100:103], v[160:163], v[192:195], v[100:103]\n\t"
      "ds_read2_b32 v[134:135], v234 offset0:80 offset1:80\n\t"
      "v_mfma_f32_16x16x32_bf16 v[104:107], v[192:195], v[168:171], v[104:107]\n\t"
      "ds_read_b128 v[148:151], v233 offset:320\n\t"
      "v_mfma_f32_16x16x32_bf16 v[80:83], v[160:163], v[188:191], v[80:83]\n\t"
      "ds_read2_b32 v[248:249], v234 offset0:0 offset1:0\n\t"
      "v_mfma_f32_16x16x32_bf16 v[84:87], v[164:167], v[188:191], v[84:87]\n\t"
      "ds_read_b128 v[196:199], v233\n\t"
      "v_mfma_f32_16x16x32_bf16 v[88:91], v[188:191], v[168:171], v[88:91]\n\t"
      "ds_read2_b32 v[132:133], v234 offset0:64 offset1:64\n\t"
      "v_mfma_f32_16x16x32_bf16 v[92:95], v[188:191], v[172:175], v[92:95]\n\t"
      "s_mov_b64 exec, s[44:45]\n\t"
      "ds_write_b128 v230, v[96:99]\n\t"
      "ds_write_b128 v230, v[100:103] offset:128\n\t"
      "s_mov_b64 exec, 0xffff\n\t"
      "ds_write_b32 v231, v104 offset:256\n\t"
      "ds_write_b32 v213, v213\n\t"
      "s_mov_b64 exec, -1\n\t"
      "v_mfma_f32_16x16x32_bf16 v[56:59], v[160:163], v[184:187], v[56:59]\n\t"
      "ds_read2_b32 v[130:131], v234 offset0:48 offset1:48\n\t"
      "v_mfma_f32_16x16x32_bf16 v[60:63], v[164:167], v[184:187], v[60:63]\n\t"
      "ds_read2_b32 v[128:129], v234 offset0:32 offset1:32\n\t"
      "v_mfma_f32_16x16x32_bf16 v[64:67], v[184:187], v[168:171], v[64:67]\n\t"
      "s_waitcnt lgkmcnt(0)\n\t"
      "v_mfma_f32_16x16x32_bf16 v[68:71], v[184:187], v[172:175], v[68:71]\n\t"
      "ds_read_b32 v226, v232\n\t"
      "ds_read_b128 v[116:119], v228 offset:128\n\t"
      "v_mfma_f32_16x16x32_bf16 v[32:35], v[160:163], v[180:183], v[32:35]\n\t"
      "ds_read_b128 v[120:123], v228 offset:192\n\t"
      "v_mfma_f32_16x16x32_bf16 v[36:39], v[164:167], v[180:183], v[36:39]\n\t"
      "ds_read2_b32 v[250:251], v229 offset0:64 offset1:64\n\t"
      "v_mfma_f32_16x16x32_bf16 v[40:43], v[180:183], v[168:171], v[40:43]\n\t"
      "ds_read2_b32 v[252:253], v229 offset0:80 offset1:80\n\t"
      "v_mfma_f32_16x16x32_bf16 v[44:47], v[180:183], v[172:175], v[44:47]\n\t"
      "ds_read_b128 v[124:127], v230 offset:128\n\t"
      "v_mfma_f32_16x16x32_bf16 v[8:11], v[160:163], v[176:179], v[8:11]\n\t"
      "ds_read2_b32 v[224:225], v231 offset0:64 offset1:64\n\t"
      "v_mfma_f32_16x16x32_bf16 v[12:15], v[164:167], v[176:179], v[12:15]\n\t"
      "v_mfma_f32_16x16x32_bf16 v[16:19], v[176:179], v[168:171], v[16:19]\n\t"
      "s_waitcnt lgkmcnt(6)\n\t"
      "v_min_u32_e32 v226, v226, v212\n\t"
      "v_cmp_eq_u32_e32 vcc, 0, v226\n\t"
      "v_mfma_f32_16x16x32_bf16 v[20:23], v[176:179], v[172:175], v[20:23]\n\t"
      "LOOPX_%=:\n\t"
      "s_cbranch_vccnz XAR_%=\n\t"
      "XAB_%=:\n\t"
      "v_mfma_f32_16x16x32_bf16 v[72:75], v[152:155], v[188:191], v[72:75]\n\t"
      "s_waitcnt lgkmcnt(4)\n\t"
      "v_fmac_f32_e32 v80, v134, v116\n\t"
      "v_fmac_f32_e32 v81, v134, v117\n\t"
      "v_mfma_f32_16x16x32_bf16 v[76:79], v[156:159], v[188:191], v[76:79]\n\t"
      "v_fmac_f32_e32 v82, v134, v118\n\t"
      "v_fmac_f32_e32 v83, v134, v119\n\t"
      "v_mfma_f32_16x16x32_bf16 v[48:51], v[152:155], v[184:187], v[48:51]\n\t"
      "v_fmac_f32_e32 v84, v134, v120\n\t"
      "v_fmac_f32_e32 v85, v134, v121\n\t"
      "v_mfma_f32_16x16x32_bf16 v[52:55], v[156:159], v[184:187], v[52:55]\n\t"
      "v_fmac_f32_e32 v86, v134, v122\n\t"
      "v_fmac_f32_e32 v87, v134, v123\n\t"
      "v_mfma_f32_16x16x32_bf16 v[24:27], v[152:155], v[180:183], v[24:27]\n\t"
      "v_cvt_pk_bf16_f32 v80, v80, v81\n\t"
      "v_cvt_pk_bf16_f32 v81, v82, v83\n\t"
      "v_mfma_f32_16x16x32_bf16 v[28:31], v[156:159], v[180:183], v[28:31]\n\t"
      "v_cvt_pk_bf16_f32 v82, v84, v85\n\t"
      "v_cvt_pk_bf16_f32 v83, v86, v87\n\t"
      "v_mfma_f32_16x16x32_bf16 v[0:3], v[152:155], v[176:179], v[0:3]\n\t"
      "s_waitcnt lgkmcnt(2)\n\t"
      "v_fmac_f32_e32 v88, v148, v250\n\t"
      "v_fmac_f32_e32 v89, v149, v250\n\t"
      "v_mfma_f32_16x16x32_bf16 v[4:7], v[156:159], v[176:179], v[4:7]\n\t"
      "v_fmac_f32_e32 v90, v150, v250\n\t"
      "v_fmac_f32_e32 v91, v151, v250\n\t"
      "ds_write_b128 v236, v[80:83] offset:2048\n\t"
      "v_fmac_f32_e32 v92, v148, v252\n\t"
      "v_fmac_f32_e32 v93, v149, v252\n\t"
      "v_fmac_f32_e32 v94, v150, v252\n\t"
      "v_fmac_f32_e32 v95, v151, v252\n\t"
      "v_cvt_pk_bf16_f32 v164, v88, v89\n\t"
      "v_cvt_pk_bf16_f32 v165, v90, v91\n\t"
      "v_cvt_pk_bf16_f32 v174, v92, v93\n\t"
      "v_cvt_pk_bf16_f32 v175, v94, v95\n\t"
      "ds_read_b128 v[108:111], v228\n\t"
      "ds_read_b128 v[112:115], v228 offset:64\n\t"
      "s_waitcnt lgkmcnt(3)\n\t"
      "s_nop 0\n\t"
      "v_pk_fma_f32 v[100:101], v[248:249], v[124:125], v[100:101]\n\t"
      "v_pk_fma_f32 v[104:105], v[196:197], v[224:225], v[104:105]\n\t"
      "v_pk_fma_f32 v[102:103], v[248:249], v[126:127], v[102:103]\n\t"
      "v_pk_fma_f32 v[106:107], v[198:199], v[224:225], v[106:107]\n\t"
      "ds_write_b64 v237, v[164:165] offset:3072\n\t"
      "ds_write_b64 v237, v[174:175] offset:3584\n\t"
      "v_cvt_pk_bf16_f32 v100, v100, v101\n\t"
      "v_cvt_pk_bf16_f32 v101, v102, v103\n\t"
      "v_cvt_pk_bf16_f32 v102, v104, v105\n\t"
      "v_cvt_pk_bf16_f32 v103, v106, v107\n\t"
      "v_pk_fma_f32 v[56:57], v[132:133], v[116:117], v[56:57]\n\t"
      "v_pk_fma_f32 v[58:59], v[132:133], v[118:119], v[58:59]\n\t"
      "v_pk_fma_f32 v[60:61], v[132:133], v[120:121], v[60:61]\n\t"
      "v_pk_fma_f32 v[62:63], v[132:133], v[122:123], v[62:63]\n\t"
      "ds_write_b64 v238, v[100:101]\n\t"
      "ds_write_b64 v214, v[102:103]\n\t"
      "ds_write_b32 v213, v213 offset:4\n\t"
      "v_cvt_pk_bf16_f32 v56, v56, v57\n\t"
      "v_cvt_pk_bf16_f32 v57, v58, v59\n\t"
      "v_cvt_pk_bf16_f32 v58, v60, v61\n\t"
      "v_cvt_pk_bf16_f32 v59, v62, v63\n\t"
      "ds_read_b128 v[144:147], v233 offset:256\n\t"
      "ds_read_b128 v[140:143], v233 offset:192\n\t"
      "ds_read_b128 v[136:139], v233 offset:128\n\t"
      "v_pk_fma_f32 v[32:33], v[130:131], v[116:117], v[32:33]\n\t"
      "v_pk_fma_f32 v[34:35], v[130:131], v[118:119], v[34:35]\n\t"
      "v_pk_fma_f32 v[36:37], v[130:131], v[120:121], v[36:37]\n\t"
      "v_pk_fma_f32 v[38:39], v[130:131], v[122:123], v[38:39]\n\t"
      "v_pk_fma_f32 v[8:9], v[128:129], v[116:117], v[8:9]\n\t"
      "v_pk_fma_f32 v[10:11], v[128:129], v[118:119], v[10:11]\n\t"
      "v_pk_fma_f32 v[12:13], v[128:129], v[120:121], v[12:13]\n\t"
      "v_pk_fma_f32 v[14:15], v[128:129], v[122:123], v[14:15]\n\t"
      "s_waitcnt lgkmcnt(8)\n\t"
      "s_nop 0\n\t"
      "v_pk_fma_f32 v[72:73], v[134:135], v[108:109], v[72:73]\n\t"
      "v_pk_fma_f32 v[74:75], v[134:135], v[110:111], v[74:75]\n\t"
      "v_pk_fma_f32 v[76:77], v[134:135], v[112:113], v[76:77]\n\t"
      "v_pk_fma_f32 v[78:79], v[134:135], v[114:115], v[78:79]\n\t"
      "v_pk_fma_f32 v[48:49], v[132:133], v[108:109], v[48:49]\n\t"
      "v_pk_fma_f32 v[50:51], v[132:133], v[110:111], v[50:51]\n\t"
      "v_pk_fma_f32 v[52:53], v[132:133], v[112:113], v[52:53]\n\t"
      "v_pk_fma_f32 v[54:55], v[132:133], v[114:115], v[54:55]\n\t"
      "s_waitcnt lgkmcnt(3)\n\t"
      "ds_read2_b32 v[204:205], v235 offset0:0 offset1:16\n\t"
      "ds_read2_b32 v[206:207], v235 offset0:32 offset1:48\n\t"
      "v_pk_fma_f32 v[24:25], v[130:131], v[108:109], v[24:25]\n\t"
      "v_pk_fma_f32 v[26:27], v[130:131], v[110:111], v[26:27]\n\t"
      "v_pk_fma_f32 v[28:29], v[130:131], v[112:113], v[28:29]\n\t"
      "v_pk_fma_f32 v[30:31], v[130:131], v[114:115], v[30:31]\n\t"
      "ds_read2_b32 v[208:209], v254 offset0:0 offset1:16\n\t"
      "ds_read2_b32 v[210:211], v254 offset0:32 offset1:48\n\t"
      "v_pk_fma_f32 v[0:1], v[128:129], v[108:109], v[0:1]\n\t"
      "v_pk_fma_f32 v[2:3], v[128:129], v[110:111], v[2:3]\n\t"
      "v_pk_fma_f32 v[4:5], v[128:129], v[112:113], v[4:5]\n\t"
      "v_pk_fma_f32 v[6:7], v[128:129], v[114:115], v[6:7]\n\t"
      "ds_read2_b32 v[200:201], v234 offset0:192 offset1:208\n\t"
      "ds_read2_b32 v[202:203], v234 offset0:224 offset1:240\n\t"
      "ds_read_b32 v226, v232 offset:4\n\t"
      "ds_read_b128 v[152:155], v236\n\t"
      "ds_read_b64 v[156:157], v237 offset:1024\n\t"
      "ds_read_b64 v[166:167], v237 offset:1536\n\t"
      "s_waitcnt lgkmcnt(12)\n\t"
      "v_pk_fma_f32 v[64:65], v[144:145], v[250:251], v[64:65]\n\t"
      "v_pk_fma_f32 v[66:67], v[146:147], v[250:251], v[66:67]\n\t"
      "v_pk_fma_f32 v[68:69], v[144:145], v[252:253], v[68:69]\n\t"
      "v_pk_fma_f32 v[70:71], v[146:147], v[252:253], v[70:71]\n\t"
      "s_waitcnt lgkmcnt(11)\n\t"
      "v_pk_fma_f32 v[40:41], v[140:141], v[250:251], v[40:41]\n\t"
      "v_pk_fma_f32 v[42:43], v[142:143], v[250:251], v[42:43]\n\t"
      "v_pk_fma_f32 v[44:45], v[140:141], v[252:253], v[44:45]\n\t"
      "v_pk_fma_f32 v[46:47], v[142:143], v[252:253], v[46:47]\n\t"
      "s_waitcnt lgkmcnt(10)\n\t"
      "v_pk_fma_f32 v[16:17], v[136:137], v[250:251], v[16:17]\n\t"
      "v_pk_fma_f32 v[18:19], v[138:139], v[250:251], v[18:19]\n\t"
      "v_pk_fma_f32 v[20:21], v[136:137], v[252:253], v[20:21]\n\t"
      "v_pk_fma_f32 v[22:23], v[138:139], v[252:253], v[22:23]\n\t"
      "ds_read_b64 v[250:251], v220 offset:8848\n\t"
      "ds_read_b64 v[196:197], v220\n\t"
      "v_cvt_pk_bf16_f32 v72, v72, v73\n\t"
      "v_cvt_pk_bf16_f32 v73, v74, v75\n\t"
      "v_cvt_pk_bf16_f32 v74, v76, v77\n\t"
      "v_cvt_pk_bf16_f32 v75, v78, v79\n\t"
      "v_cndmask_b32_e64 v74, v74, 0, s[42:43]\n\t"
      "v_cndmask_b32_e64 v75, v75, 0, s[42:43]\n\t"
      "v_cvt_pk_bf16_f32 v48, v48, v49\n\t"
      "v_cvt_pk_bf16_f32 v49, v50, v51\n\t"
      "v_cvt_pk_bf16_f32 v50, v52, v53\n\t"
      "v_cvt_pk_bf16_f32 v51, v54, v55\n\t"
      "v_cndmask_b32_e64 v50, v50, 0, s[42:43]\n\t"
      "v_cndmask_b32_e64 v51, v51, 0, s[42:43]\n\t"
      "v_cvt_pk_bf16_f32 v32, v32, v33\n\t"
      "v_cvt_pk_bf16_f32 v33, v34, v35\n\t"
      "v_cvt_pk_bf16_f32 v34, v36, v37\n\t"
      "v_cvt_pk_bf16_f32 v35, v38, v39\n\t"
      "v_cvt_pk_bf16_f32 v8, v8, v9\n\t"
      "v_cvt_pk_bf16_f32 v9, v10, v11\n\t"
      "v_cvt_pk_bf16_f32 v10, v12, v13\n\t"
      "v_cvt_pk_bf16_f32 v11, v14, v15\n\t"
      "v_mfma_f32_16x16x32_bf16 v[76:79], v[56:59], v[72:75], 0\n\t"
      "v_cvt_pk_bf16_f32 v24, v24, v25\n\t"
      "v_cvt_pk_bf16_f32 v25, v26, v27\n\t"
      "v_mfma_f32_16x16x32_bf16 v[84:87], v[80:83], v[72:75], 0\n\t"
      "v_cvt_pk_bf16_f32 v26, v28, v29\n\t"
      "v_cvt_pk_bf16_f32 v27, v30, v31\n\t"
      "v_mfma_f32_16x16x32_bf16 v[52:55], v[32:35], v[48:51], 0\n\t"
      "v_cndmask_b32_e64 v26, v26, 0, s[42:43]\n\t"
      "v_cndmask_b32_e64 v27, v27, 0, s[42:43]\n\t"
      "v_mfma_f32_16x16x32_bf16 v[60:63], v[56:59], v[48:51], 0\n\t"
      "s_waitcnt lgkmcnt(5)\n\t"
      "v_cmp_eq_u32_e32 vcc, 0, v226\n\t"
      "v_cvt_pk_bf16_f32 v0, v0, v1\n\t"
      "v_mfma_f32_16x16x32_bf16 v[28:31], v[8:11], v[24:27], 0\n\t"
      "v_cvt_pk_bf16_f32 v1, v2, v3\n\t"
      "v_cvt_pk_bf16_f32 v2, v4, v5\n\t"
      "v_mfma_f32_16x16x32_bf16 v[36:39], v[32:35], v[24:27], 0\n\t"
      "v_cvt_pk_bf16_f32 v3, v6, v7\n\t"
      "v_cndmask_b32_e64 v2, v2, 0, s[42:43]\n\t"
      "v_cndmask_b32_e64 v3, v3, 0, s[42:43]\n\t"
      "s_cbranch_vccnz XBR_%=\n\t"
      "XBB_%=:\n\t"
      "v_cvt_pk_bf16_f32 v162, v64, v65\n\t"
      "s_waitcnt lgkmcnt(4)\n\t"
      "v_mfma_f32_16x16x32_bf16 v[4:7], v[152:155], v[0:3], v[244:247]\n\t"
      "v_cvt_pk_bf16_f32 v163, v66, v67\n\t"
      "v_cvt_pk_bf16_f32 v172, v68, v69\n\t"
      "v_mfma_f32_16x16x32_bf16 v[12:15], v[8:11], v[0:3], 0\n\t"
      "v_cvt_pk_bf16_f32 v173, v70, v71\n\t"
      "v_cvt_pk_bf16_f32 v160, v40, v41\n\t"
      "v_cndmask_b32_e64 v76, v76, v77, s[36:37]\n\t"
      "v_cndmask_b32_e64 v84, v84, v85, s[36:37]\n\t"
      "v_cndmask_b32_e64 v78, v78, v79, s[36:37]\n\t"
      "v_cndmask_b32_e64 v86, v86, v87, s[36:37]\n\t"
      "v_cndmask_b32_e64 v179, v76, v84, s[38:39]\n\t"
      "v_cndmask_b32_e64 v183, v78, v86, s[40:41]\n\t"
      "v_cndmask_b32_e64 v52, v52, v53, s[36:37]\n\t"
      "v_cndmask_b32_e64 v60, v60, v61, s[36:37]\n\t"
      "v_cndmask_b32_e64 v54, v54, v55, s[36:37]\n\t"
      "v_cndmask_b32_e64 v62, v62, v63, s[36:37]\n\t"
      "v_cndmask_b32_e64 v178, v52, v60, s[38:39]\n\t"
      "v_cndmask_b32_e64 v182, v54, v62, s[40:41]\n\t"
      "v_cndmask_b32_e64 v28, v28, v29, s[36:37]\n\t"
      "v_cndmask_b32_e64 v36, v36, v37, s[36:37]\n\t"
      "v_cndmask_b32_e64 v30, v30, v31, s[36:37]\n\t"
      "v_cndmask_b32_e64 v38, v38, v39, s[36:37]\n\t"
      "v_cndmask_b32_e64 v177, v28, v36, s[38:39]\n\t"
      "v_cndmask_b32_e64 v181, v30, v38, s[40:41]\n\t"
      "v_cndmask_b32_e64 v4, v4, v5, s[36:37]\n\t"
      "v_cndmask_b32_e64 v12, v12, v13, s[36:37]\n\t"
      "v_cndmask_b32_e64 v6, v6, v7, s[36:37]\n\t"
      "v_cndmask_b32_e64 v14, v14, v15, s[36:37]\n\t"
      "v_cndmask_b32_e64 v176, v4, v12, s[38:39]\n\t"
      "v_cndmask_b32_e64 v180, v6, v14, s[40:41]\n\t"
      "v_pk_mul_f32 v[178:179], v[178:179], v[206:207]\n\t"
      "v_pk_mul_f32 v[182:183], v[182:183], v[210:211]\n\t"
      "v_pk_mul_f32 v[176:177], v[176:177], v[204:205]\n\t"
      "v_pk_mul_f32 v[180:181], v[180:181], v[208:209]\n\t"
      "v_max_f32_e32 v219, v179, v183\n\t"
      "v_max_f32_e32 v217, v177, v181\n\t"
      "v_max_f32_e32 v218, v178, v182\n\t"
      "v_max_f32_e32 v216, v176, v180\n\t"
      "v_cvt_pk_bf16_f32 v161, v42, v43\n\t"
      "v_permlane32_swap_b32_e32 v217, v219\n\t"
      "v_permlane32_swap_b32_e32 v216, v218\n\t"
      "v_max_f32_e32 v217, v217, v219\n\t"
      "v_max_f32_e32 v216, v216, v218\n\t"
      "v_cvt_pk_bf16_f32 v170, v44, v45\n\t"
      "v_cvt_pk_bf16_f32 v171, v46, v47\n\t"
      "v_permlane16_swap_b32_e32 v216, v217\n\t"
      "v_max_f32_e32 v216, v216, v217\n\t"
      "v_mov_b32_e32 v217, v216\n\t"
      "v_cvt_pk_bf16_f32 v158, v16, v17\n\t"
      "s_waitcnt lgkmcnt(0)\n\t"
      "v_xad_u32 v252, s13, v197, v221\n\t"
      "v_permlane16_swap_b32_e32 v216, v217\n\t"
      "v_mov_b32_e32 v218, v216\n\t"
      "v_mov_b32_e64 v219, v217\n\t"
      "v_xad_u32 v196, s52, v196, v239\n\t"
      "v_permlane32_swap_b32_e32 v216, v218\n\t"
      "v_permlane32_swap_b32_e32 v217, v219\n\t"
      "v_pk_add_f32 v[178:179], v[178:179], v[218:219] neg_lo:[0,1] neg_hi:[0,1]\n\t"
      "v_pk_add_f32 v[182:183], v[182:183], v[218:219] neg_lo:[0,1] neg_hi:[0,1]\n\t"
      "v_pk_add_f32 v[176:177], v[176:177], v[216:217] neg_lo:[0,1] neg_hi:[0,1]\n\t"
      "v_pk_add_f32 v[180:181], v[180:181], v[216:217] neg_lo:[0,1] neg_hi:[0,1]\n\t"
      "v_pk_mul_f32 v[178:179], v[178:179], v[202:203]\n\t"
      "v_pk_mul_f32 v[182:183], v[182:183], v[202:203]\n\t"
      "v_pk_mul_f32 v[176:177], v[176:177], v[200:201]\n\t"
      "v_pk_mul_f32 v[180:181], v[180:181], v[200:201]\n\t"
      "v_exp_f32_e32 v179, v179\n\t"
      "v_exp_f32_e32 v183, v183\n\t"
      "v_exp_f32_e32 v178, v178\n\t"
      "v_exp_f32_e32 v182, v182\n\t"
      "v_exp_f32_e32 v177, v177\n\t"
      "v_exp_f32_e32 v181, v181\n\t"
      "v_exp_f32_e32 v176, v176\n\t"
      "v_exp_f32_e32 v180, v180\n\t"
      "v_xor_b32_e64 v253, s12, v197\n\t"
      "s_mov_b64 exec, 1\n\t"
      "global_store_dwordx2 v253, v[250:251], s[10:11] sc1\n\t"
      "s_mov_b64 exec, -1\n\t"
      "v_pk_add_f32 v[218:219], v[178:179], v[182:183]\n\t"
      "v_pk_add_f32 v[216:217], v[176:177], v[180:181]\n\t"
      "v_cvt_pk_bf16_f32 v159, v18, v19\n\t"
      "v_cvt_pk_bf16_f32 v168, v20, v21\n\t"
      "v_permlane32_swap_b32_e32 v217, v219\n\t"
      "v_permlane32_swap_b32_e32 v216, v218\n\t"
      "v_add_f32_e32 v217, v217, v219\n\t"
      "v_add_f32_e32 v216, v216, v218\n\t"
      "v_pk_mul_f32 v[178:179], v[178:179], v[206:207]\n\t"
      "v_pk_mul_f32 v[182:183], v[182:183], v[210:211]\n\t"
      "v_permlane16_swap_b32_e32 v216, v217\n\t"
      "v_add_f32_e32 v216, v216, v217\n\t"
      "v_rcp_f32_e32 v216, v216\n\t"
      "v_pk_mul_f32 v[176:177], v[176:177], v[204:205]\n\t"
      "v_mov_b32_e32 v217, v216\n\t"
      "v_pk_mul_f32 v[180:181], v[180:181], v[208:209]\n\t"
      "v_cvt_pk_bf16_f32 v169, v22, v23\n\t"
      "v_permlane16_swap_b32_e32 v216, v217\n\t"
      "v_mov_b32_e32 v218, v216\n\t"
      "v_mov_b32_e32 v219, v217\n\t"
      "s_nop 0\n\t"
      "v_permlane32_swap_b32_e32 v216, v218\n\t"
      "v_permlane32_swap_b32_e32 v217, v219\n\t"
      "v_pk_mul_f32 v[178:179], v[178:179], v[218:219]\n\t"
      "v_pk_mul_f32 v[182:183], v[182:183], v[218:219]\n\t"
      ".LhopA3:\n\t"
      "s_cbranch_execz .LhopA7\n\t"
      "v_pk_mul_f32 v[176:177], v[176:177], v[216:217]\n\t"
      "v_pk_mul_f32 v[180:181], v[180:181], v[216:217]\n\t"
      "v_cvt_pk_bf16_f32 v179, v179, v183\n\t"
      "v_cvt_pk_bf16_f32 v178, v178, v182\n\t"
      "v_cvt_pk_bf16_f32 v177, v177, v181\n\t"
      "v_cvt_pk_bf16_f32 v176, v176, v180\n\t"
      "v_perm_b32 v72, v179, v179, v240\n\t"
      "v_perm_b32 v73, v179, v179, v241\n\t"
      ".LhopA4:\n\t"
      "s_cbranch_execz .LbackA\n\t"
      "v_perm_b32 v74, v179, v179, v242\n\t"
      "v_perm_b32 v75, v179, v179, v243\n\t"
      "v_perm_b32 v48, v178, v178, v240\n\t"
      "v_perm_b32 v49, v178, v178, v241\n\t"
      "v_mfma_f32_16x16x32_bf16 v[88:91], v[162:165], v[72:75], 0\n\t"
      "v_perm_b32 v50, v178, v178, v242\n\t"
      "v_perm_b32 v51, v178, v178, v243\n\t"
      "v_mfma_f32_16x16x32_bf16 v[92:95], v[172:175], v[72:75], 0\n\t"
      "v_perm_b32 v24, v177, v177, v240\n\t"
      "v_perm_b32 v25, v177, v177, v241\n\t"
      "v_mfma_f32_16x16x32_bf16 v[64:67], v[160:163], v[48:51], 0\n\t"
      "v_perm_b32 v26, v177, v177, v242\n\t"
      "v_perm_b32 v27, v177, v177, v243\n\t"
      "v_mfma_f32_16x16x32_bf16 v[68:71], v[170:173], v[48:51], 0\n\t"
      "v_perm_b32 v0, v176, v176, v240\n\t"
      "v_perm_b32 v1, v176, v176, v241\n\t"
      "v_mfma_f32_16x16x32_bf16 v[40:43], v[158:161], v[24:27], 0\n\t"
      "v_perm_b32 v2, v176, v176, v242\n\t"
      "v_perm_b32 v3, v176, v176, v243\n\t"
      "v_mfma_f32_16x16x32_bf16 v[44:47], v[168:171], v[24:27], 0\n\t"
      "s_nop 0\n\t"
      "v_mfma_f32_16x16x32_bf16 v[16:19], v[156:159], v[0:3], 0\n\t"
      "v_cvt_pk_bf16_f32 v80, v88, v89\n\t"
      "v_cvt_pk_bf16_f32 v81, v90, v91\n\t"
      "v_mfma_f32_16x16x32_bf16 v[20:23], v[166:169], v[0:3], 0\n\t"
      "v_cvt_pk_bf16_f32 v82, v92, v93\n\t"
      "v_cvt_pk_bf16_f32 v83, v94, v95\n\t"
      "global_store_dwordx4 v196, v[80:83], s[60:61] sc1\n\t"
      "v_add3_u32 v219, v80, v81, v82\n\t"
      "v_add_u32_e32 v219, v219, v83\n\t"
      "v_cvt_pk_bf16_f32 v56, v64, v65\n\t"
      "v_cvt_pk_bf16_f32 v57, v66, v67\n\t"
      "v_cvt_pk_bf16_f32 v58, v68, v69\n\t"
      "v_cvt_pk_bf16_f32 v59, v70, v71\n\t"
      "global_store_dwordx4 v196, v[56:59], s[58:59] sc1\n\t"
      "v_add3_u32 v218, v56, v57, v58\n\t"
      "v_add_u32_e64 v218, v218, v59\n\t"
      "v_cvt_pk_bf16_f32 v32, v40, v41\n\t"
      "v_cvt_pk_bf16_f32 v33, v42, v43\n\t"
      "v_cvt_pk_bf16_f32 v34, v44, v45\n\t"
      "v_cvt_pk_bf16_f32 v35, v46, v47\n\t"
      "global_store_dwordx4 v196, v[32:35], s[56:57] sc1\n\t"
      "v_add3_u32 v217, v32, v33, v34\n\t"
      "v_add_u32_e64 v217, v217, v35\n\t"
      "v_cvt_pk_bf16_f32 v8, v16, v17\n\t"
      "v_cvt_pk_bf16_f32 v9, v18, v19\n\t"
      "v_cvt_pk_bf16_f32 v10, v20, v21\n\t"
      "v_cvt_pk_bf16_f32 v11, v22, v23\n\t"
      "global_store_dwordx4 v196, v[8:11], s[4:5] sc1\n\t"
      "v_add3_u32 v216, v8, v9, v10\n\t"
      "v_add_u32_e32 v216, v216, v11\n\t"
      "v_permlane32_swap_b32_e32 v217, v219\n\t"
      "v_add_u32_e32 v217, v217, v219\n\t"
      "v_permlane32_swap_b32_e32 v216, v218\n\t"
      "v_add_u32_e64 v216, v216, v218\n\t"
      "s_nop 1\n\t"
      "v_permlane16_swap_b32_e32 v216, v217\n\t"
      "v_add3_u32 v216, v216, v217, v250\n\t"
      "global_store_dword v252, v216, s[10:11] sc1\n\t"
      "v_xor_b32_e64 v120, 64, v222\n\t"
      "v_add_u32_e32 v121, 55296, v222\n\t"
      "v_add_u32_e32 v122, 82944, v120\n\t"
      "v_add_u32_e32 v120, 27648, v120\n\t"
      "s_barrier\n\t"
      "s_add_u32 m0, s16, 65536\n\t"
      "s_add_u32 s48, s14, 0\n\t"
      "s_addc_u32 s49, s15, 0\n\t"
      "global_load_lds_dwordx4 v222, s[48:49]\n\t"
      "global_load_lds_dwordx4 v120, s[48:49] offset:1024\n\t"
      "global_load_lds_dwordx4 v121, s[48:49] offset:2048\n\t"
      "global_load_lds_dwordx4 v122, s[48:49] offset:3072\n\t"
      "s_add_u32 m0, s16, 98304\n\t"
      "s_add_u32 s48, s14, 114688\n\t"
      "s_addc_u32 s49, s15, 0\n\t"
      "global_load_lds_dwordx4 v222, s[48:49]\n\t"
      "global_load_lds_dwordx4 v120, s[48:49] offset:1024\n\t"
      "global_load_lds_dwordx4 v121, s[48:49] offset:2048\n\t"
      "global_load_lds_dwordx4 v122, s[48:49] offset:3072\n\t"
      "s_nop 1\n\t"
      "s_branch CEND_%=\n\t"
      "LOOP1_%=:\n\t"
      "s_waitcnt vmcnt(0)\n\t"
      "s_barrier\n\t"
      "ds_read_b64_tr_b16 v[108:109], v226\n\t"
      "ds_read_b64_tr_b16 v[110:111], v226 offset:512\n\t"
      "ds_read_b128 v[132:135], v224\n\t"
      "ds_read_b128 v[136:139], v224 offset:5120\n\t"
      "ds_read_b128 v[140:143], v224 offset:10240\n\t"
      "ds_read_b128 v[144:147], v224 offset:15360\n\t"
      "ds_read_b64_tr_b16 v[112:113], v227\n\t"
      "ds_read_b64_tr_b16 v[114:115], v227 offset:512\n\t"
      "ds_read_b128 v[148:151], v225\n\t"
      "ds_read_b64_tr_b16 v[116:117], v226 offset:1024\n\t"
      "ds_read_b64_tr_b16 v[118:119], v226 offset:1536\n\t"
      "ds_read_b64_tr_b16 v[120:121], v227 offset:1024\n\t"
      "ds_read_b64_tr_b16 v[122:123], v227 offset:1536\n\t"
      "ds_read_b64_tr_b16 v[124:125], v226 offset:2048\n\t"
      "ds_read_b64_tr_b16 v[126:127], v226 offset:2560\n\t"
      "ds_read_b64_tr_b16 v[128:129], v227 offset:2048\n\t"
      "ds_read_b64_tr_b16 v[130:131], v227 offset:2560\n\t"
      "s_branch EVEN1_%=\n\t"
      "s_waitcnt lgkmcnt(0)\n\t"
      ".p2align 6\n\t"
      ".LhopA5:\n\t"
      "s_cbranch_execz .LbackA\n\t"
      "s_nop 0\n\t"
      "ODD1_%=:\n\t"
      "s_waitcnt lgkmcnt(0)\n\t"
      "v_mfma_f32_16x16x32_bf16 v[0:3], v[152:155], v[176:179], v[0:3]\n\t"
      "v_add_u32_e32 v224, s46, v224\n\t"
      "v_add_u32_e32 v226, s46, v226\n\t"
      "v_mfma_f32_16x16x32_bf16 v[24:27], v[152:155], v[180:183], v[24:27]\n\t"
      "v_add_u32_e32 v227, s46, v227\n\t"
      "v_add_u32_e32 v225, s46, v225\n\t"
      "v_mfma_f32_16x16x32_bf16 v[48:51], v[152:155], v[184:187], v[48:51]\n\t"
      "s_barrier\n\t"
      "ds_read_b64_tr_b16 v[108:109], v226\n\t"
      "v_mfma_f32_16x16x32_bf16 v[72:75], v[152:155], v[188:191], v[72:75]\n\t"
      "ds_read_b64_tr_b16 v[110:111], v226 offset:512\n\t"
      "v_mfma_f32_16x16x32_bf16 v[4:7], v[156:159], v[176:179], v[4:7]\n\t"
      "ds_read_b128 v[132:135], v224\n\t"
      "v_mfma_f32_16x16x32_bf16 v[28:31], v[156:159], v[180:183], v[28:31]\n\t"
      "ds_read_b128 v[136:139], v224 offset:5120\n\t"
      "v_mfma_f32_16x16x32_bf16 v[52:55], v[156:159], v[184:187], v[52:55]\n\t"
      "ds_read_b128 v[140:143], v224 offset:10240\n\t"
      "v_mfma_f32_16x16x32_bf16 v[76:79], v[156:159], v[188:191], v[76:79]\n\t"
      "ds_read_b128 v[144:147], v224 offset:15360\n\t"
      "v_mfma_f32_16x16x32_bf16 v[96:99], v[156:159], v[192:195], v[96:99]\n\t"
      "v_mfma_f32_16x16x32_bf16 v[8:11], v[160:163], v[176:179], v[8:11]\n\t"
      "ds_read_b64_tr_b16 v[112:113], v227\n\t"
      "v_mfma_f32_16x16x32_bf16 v[32:35], v[160:163], v[180:183], v[32:35]\n\t"
      "ds_read_b64_tr_b16 v[114:115], v227 offset:512\n\t"
      "v_mfma_f32_16x16x32_bf16 v[56:59], v[160:163], v[184:187], v[56:59]\n\t"
      "ds_read_b128 v[148:151], v225\n\t"
      "v_mfma_f32_16x16x32_bf16 v[80:83], v[160:163], v[188:191], v[80:83]\n\t"
      "ds_read_b64_tr_b16 v[116:117], v226 offset:1024\n\t"
      "v_mfma_f32_16x16x32_bf16 v[12:15], v[164:167], v[176:179], v[12:15]\n\t"
      "ds_read_b64_tr_b16 v[118:119], v226 offset:1536\n\t"
      "v_mfma_f32_16x16x32_bf16 v[36:39], v[164:167], v[180:183], v[36:39]\n\t"
      "ds_read_b64_tr_b16 v[120:121], v227 offset:1024\n\t"
      "v_mfma_f32_16x16x32_bf16 v[60:63], v[164:167], v[184:187], v[60:63]\n\t"
      "v_mfma_f32_16x16x32_bf16 v[84:87], v[164:167], v[188:191], v[84:87]\n\t"
      "ds_read_b64_tr_b16 v[122:123], v227 offset:1536\n\t"
      "v_mfma_f32_16x16x32_bf16 v[100:103], v[164:167], v[192:195], v[100:103]\n\t"
      "ds_read_b64_tr_b16 v[124:125], v226 offset:2048\n\t"
      "v_mfma_f32_16x16x32_bf16 v[16:19], v[176:179], v[168:171], v[16:19]\n\t"
      "ds_read_b64_tr_b16 v[126:127], v226 offset:2560\n\t"
      "v_mfma_f32_16x16x32_bf16 v[40:43], v[180:183], v[168:171], v[40:43]\n\t"
      "ds_read_b64_tr_b16 v[128:129], v227 offset:2048\n\t"
      "v_mfma_f32_16x16x32_bf16 v[64:67], v[184:187], v[168:171], v[64:67]\n\t"
      "ds_read_b64_tr_b16 v[130:131], v227 offset:2560\n\t"
      "v_mfma_f32_16x16x32_bf16 v[88:91], v[188:191], v[168:171], v[88:91]\n\t"
      "v_mfma_f32_16x16x32_bf16 v[20:23], v[176:179], v[172:175], v[20:23]\n\t"
      "v_mfma_f32_16x16x32_bf16 v[44:47], v[180:183], v[172:175], v[44:47]\n\t"
      "v_add_u32_e32 v248, s46, v248\n\t"
      "v_add_u32_e32 v249, s46, v249\n\t"
      "v_mfma_f32_16x16x32_bf16 v[68:71], v[184:187], v[172:175], v[68:71]\n\t"
      "v_mfma_f32_16x16x32_bf16 v[92:95], v[188:191], v[172:175], v[92:95]\n\t"
      "v_mfma_f32_16x16x32_bf16 v[104:107], v[192:195], v[172:175], v[104:107]\n\t"
      ".LhopA6:\n\t"
      "s_cbranch_execz .LbackA\n\t"
      "EVEN1_%=:\n\t"
      "s_waitcnt lgkmcnt(0)\n\t"
      "v_mfma_f32_16x16x32_bf16 v[0:3], v[108:111], v[132:135], v[0:3]\n\t"
      "ds_read_b64_tr_b16 v[152:153], v226 offset:20480\n\t"
      "v_mfma_f32_16x16x32_bf16 v[24:27], v[108:111], v[136:139], v[24:27]\n\t"
      "ds_read_b64_tr_b16 v[154:155], v226 offset:20992\n\t"
      "v_mfma_f32_16x16x32_bf16 v[48:51], v[108:111], v[140:143], v[48:51]\n\t"
      "ds_read_b128 v[176:179], v248\n\t"
      "v_mfma_f32_16x16x32_bf16 v[72:75], v[108:111], v[144:147], v[72:75]\n\t"
      "ds_read_b128 v[180:183], v248 offset:5120\n\t"
      "v_mfma_f32_16x16x32_bf16 v[4:7], v[112:115], v[132:135], v[4:7]\n\t"
      "v_mfma_f32_16x16x32_bf16 v[28:31], v[112:115], v[136:139], v[28:31]\n\t"
      "ds_read_b128 v[184:187], v248 offset:10240\n\t"
      "v_mfma_f32_16x16x32_bf16 v[52:55], v[112:115], v[140:143], v[52:55]\n\t"
      "ds_read_b128 v[188:191], v248 offset:15360\n\t"
      "v_mfma_f32_16x16x32_bf16 v[76:79], v[112:115], v[144:147], v[76:79]\n\t"
      "ds_read_b64_tr_b16 v[156:157], v227 offset:20480\n\t"
      "v_mfma_f32_16x16x32_bf16 v[96:99], v[112:115], v[148:151], v[96:99]\n\t"
      "v_mfma_f32_16x16x32_bf16 v[8:11], v[116:119], v[132:135], v[8:11]\n\t"
      "ds_read_b64_tr_b16 v[158:159], v227 offset:20992\n\t"
      "v_mfma_f32_16x16x32_bf16 v[32:35], v[116:119], v[136:139], v[32:35]\n\t"
      "ds_read_b128 v[192:195], v249\n\t"
      "v_mfma_f32_16x16x32_bf16 v[56:59], v[116:119], v[140:143], v[56:59]\n\t"
      "ds_read_b64_tr_b16 v[160:161], v226 offset:21504\n\t"
      "v_mfma_f32_16x16x32_bf16 v[80:83], v[116:119], v[144:147], v[80:83]\n\t"
      "ds_read_b64_tr_b16 v[162:163], v226 offset:22016\n\t"
      "v_mfma_f32_16x16x32_bf16 v[12:15], v[120:123], v[132:135], v[12:15]\n\t"
      "v_mfma_f32_16x16x32_bf16 v[36:39], v[120:123], v[136:139], v[36:39]\n\t"
      "ds_read_b64_tr_b16 v[164:165], v227 offset:21504\n\t"
      "v_mfma_f32_16x16x32_bf16 v[60:63], v[120:123], v[140:143], v[60:63]\n\t"
      "ds_read_b64_tr_b16 v[166:167], v227 offset:22016\n\t"
      "v_mfma_f32_16x16x32_bf16 v[84:87], v[120:123], v[144:147], v[84:87]\n\t"
      "ds_read_b64_tr_b16 v[168:169], v226 offset:22528\n\t"
      "v_mfma_f32_16x16x32_bf16 v[100:103], v[120:123], v[148:151], v[100:103]\n\t"
      "v_mfma_f32_16x16x32_bf16 v[16:19], v[132:135], v[124:127], v[16:19]\n\t"
      "ds_read_b64_tr_b16 v[170:171], v226 offset:23040\n\t"
      "v_mfma_f32_16x16x32_bf16 v[40:43], v[136:139], v[124:127], v[40:43]\n\t"
      "ds_read_b64_tr_b16 v[172:173], v227 offset:22528\n\t"
      "v_mfma_f32_16x16x32_bf16 v[64:67], v[140:143], v[124:127], v[64:67]\n\t"
      "ds_read_b64_tr_b16 v[174:175], v227 offset:23040\n\t"
      "s_cmp_eq_u32 s47, 4\n\t"
      "s_cselect_b32 s46, -2, 1\n\t"
      "v_mfma_f32_16x16x32_bf16 v[88:91], v[144:147], v[124:127], v[88:91]\n\t"
      "s_cmp_eq_u32 s47, 1\n\t"
      "s_cselect_b32 s46, -2, s46\n\t"
      "v_mfma_f32_16x16x32_bf16 v[20:23], v[132:135], v[128:131], v[20:23]\n\t"
      "s_mul_i32 s46, s46, 43008\n\t"
      "v_mfma_f32_16x16x32_bf16 v[44:47], v[136:139], v[128:131], v[44:47]\n\t"
      "v_mfma_f32_16x16x32_bf16 v[68:71], v[140:143], v[128:131], v[68:71]\n\t"
      "v_mfma_f32_16x16x32_bf16 v[92:95], v[144:147], v[128:131], v[92:95]\n\t"
      "s_sub_u32 s47, s47, 1\n\t"
      "v_mfma_f32_16x16x32_bf16 v[104:107], v[148:151], v[128:131], v[104:107]\n\t"
      "s_cbranch_scc0 ODD1_%=\n\t"
      "s_waitcnt lgkmcnt(0)\n\t"
      "v_mfma_f32_16x16x32_bf16 v[96:99], v[156:159], v[192:195], v[96:99]\n\t"
      "ds_read_b32 v212, v215\n\t"
      "v_mfma_f32_16x16x32_bf16 v[100:103], v[164:167], v[192:195], v[100:103]\n\t"
      "ds_read2_b32 v[134:135], v234 offset0:80 offset1:80\n\t"
      "v_mfma_f32_16x16x32_bf16 v[104:107], v[192:195], v[172:175], v[104:107]\n\t"
      "ds_read_b128 v[148:151], v233 offset:320\n\t"
      "v_mfma_f32_16x16x32_bf16 v[80:83], v[160:163], v[188:191], v[80:83]\n\t"
      "ds_read2_b32 v[248:249], v234 offset0:0 offset1:0\n\t"
      "v_mfma_f32_16x16x32_bf16 v[84:87], v[164:167], v[188:191], v[84:87]\n\t"
      "ds_read_b128 v[196:199], v233\n\t"
      "v_mfma_f32_16x16x32_bf16 v[88:91], v[188:191], v[168:171], v[88:91]\n\t"
      "ds_read2_b32 v[132:133], v234 offset0:64 offset1:64\n\t"
      "v_mfma_f32_16x16x32_bf16 v[92:95], v[188:191], v[172:175], v[92:95]\n\t"
      "s_mov_b64 exec, s[44:45]\n\t"
      "ds_write_b128 v230, v[96:99]\n\t"
      "ds_write_b128 v230, v[100:103] offset:128\n\t"
      "s_mov_b64 exec, 0xffff\n\t"
      "ds_write_b32 v231, v104 offset:256\n\t"
      "ds_write_b32 v213, v213\n\t"
      "s_mov_b64 exec, -1\n\t"
      "v_mfma_f32_16x16x32_bf16 v[56:59], v[160:163], v[184:187], v[56:59]\n\t"
      "ds_read2_b32 v[130:131], v234 offset0:48 offset1:48\n\t"
      "v_mfma_f32_16x16x32_bf16 v[60:63], v[164:167], v[184:187], v[60:63]\n\t"
      "ds_read2_b32 v[128:129], v234 offset0:32 offset1:32\n\t"
      "v_mfma_f32_16x16x32_bf16 v[64:67], v[184:187], v[168:171], v[64:67]\n\t"
      "s_waitcnt lgkmcnt(0)\n\t"
      "v_mfma_f32_16x16x32_bf16 v[68:71], v[184:187], v[172:175], v[68:71]\n\t"
      "ds_read_b32 v226, v232\n\t"
      "ds_read_b128 v[116:119], v228 offset:128\n\t"
      "v_mfma_f32_16x16x32_bf16 v[32:35], v[160:163], v[180:183], v[32:35]\n\t"
      "ds_read_b128 v[120:123], v228 offset:192\n\t"
      "v_mfma_f32_16x16x32_bf16 v[36:39], v[164:167], v[180:183], v[36:39]\n\t"
      "ds_read2_b32 v[250:251], v229 offset0:64 offset1:64\n\t"
      "v_mfma_f32_16x16x32_bf16 v[40:43], v[180:183], v[168:171], v[40:43]\n\t"
      "ds_read2_b32 v[252:253], v229 offset0:80 offset1:80\n\t"
      "v_mfma_f32_16x16x32_bf16 v[44:47], v[180:183], v[172:175], v[44:47]\n\t"
      "ds_read_b128 v[124:127], v230 offset:128\n\t"
      "v_mfma_f32_16x16x32_bf16 v[8:11], v[160:163], v[176:179], v[8:11]\n\t"
      "ds_read2_b32 v[224:225], v231 offset0:64 offset1:64\n\t"
      "v_mfma_f32_16x16x32_bf16 v[12:15], v[164:167], v[176:179], v[12:15]\n\t"
      "v_mfma_f32_16x16x32_bf16 v[16:19], v[176:179], v[168:171], v[16:19]\n\t"
      "s_waitcnt lgkmcnt(6)\n\t"
      "v_min_u32_e32 v226, v226, v212\n\t"
      "v_cmp_eq_u32_e32 vcc, 0, v226\n\t"
      "v_mfma_f32_16x16x32_bf16 v[20:23], v[176:179], v[172:175], v[20:23]\n\t"
      "s_branch LOOPX_%=\n\t"
      "XAR_%=:\n\t"
      "s_mov_b32 s51, 0\n\t"
      "XAL_%=:\n\t"
      "s_add_u32 s51, s51, 1\n\t"
      "s_cmp_lt_u32 s51, 262144\n\t"
      "s_cbranch_scc0 XAX_%=\n\t"
      "s_waitcnt lgkmcnt(0)\n\t"
      "ds_read_b32 v212, v215\n\t"
      "ds_read2_b32 v[134:135], v234 offset0:80 offset1:80\n\t"
      "ds_read_b128 v[148:151], v233 offset:320\n\t"
      "ds_read2_b32 v[248:249], v234 offset0:0 offset1:0\n\t"
      "ds_read_b128 v[196:199], v233\n\t"
      "ds_read2_b32 v[132:133], v234 offset0:64 offset1:64\n\t"
      "ds_read2_b32 v[130:131], v234 offset0:48 offset1:48\n\t"
      "ds_read2_b32 v[128:129], v234 offset0:32 offset1:32\n\t"
      "s_waitcnt lgkmcnt(0)\n\t"
      "ds_read_b32 v226, v232\n\t"
      "ds_read_b128 v[116:119], v228 offset:128\n\t"
      "ds_read_b128 v[120:123], v228 offset:192\n\t"
      "ds_read2_b32 v[250:251], v229 offset0:64 offset1:64\n\t"
      "ds_read2_b32 v[252:253], v229 offset0:80 offset1:80\n\t"
      "ds_read_b128 v[124:127], v230 offset:128\n\t"
      "ds_read2_b32 v[224:225], v231 offset0:64 offset1:64\n\t"
      "s_waitcnt lgkmcnt(0)\n\t"
      "v_min_u32_e32 v226, v226, v212\n\t"
      "v_cmp_eq_u32_e32 vcc, 0, v226\n\t"
      "s_cbranch_vccnz XAL_%=\n\t"
      "XAX_%=:\n\t"
      "s_branch XAB_%=\n\t"
      "XBR_%=:\n\t"
      "s_mov_b32 s51, 0\n\t"
      "XBL_%=:\n\t"
      "s_add_u32 s51, s51, 1\n\t"
      "s_cmp_lt_u32 s51, 262144\n\t"
      "s_cbranch_scc0 XBX_%=\n\t"
      "s_waitcnt lgkmcnt(0)\n\t"
      "ds_read_b32 v226, v232 offset:4\n\t"
      "ds_read_b128 v[152:155], v236\n\t"
      "ds_read_b64 v[156:157], v237 offset:1024\n\t"
      "ds_read_b64 v[166:167], v237 offset:1536\n\t"
      "s_waitcnt lgkmcnt(0)\n\t"
      "v_cmp_eq_u32_e32 vcc, 0, v226\n\t"
      "s_cbranch_vccnz XBL_%=\n\t"
      "XBX_%=:\n\t"
      "s_branch XBB_%=\n\t"
      "CEND_%=:\n\t"
      ".LhopA7:\n\t"
      "s_cbranch_execz .LbackA\n\t"
      : "+{s[4:11]}"(s), "+{s12}"(hint), "+{s13}"(chk), "+{s[14:15]}"(tw), "+{s16}"(lw), "+{s[18:19]}"(pf), "+{v222}"(vn), "+{v[224:239]}"(v), "+{v254}"(vk1)
      :
      : "memory", "vcc", "scc",
        "v0", "v1", "v2", "v3", "v4", "v5", "v6", "v7", "v8", "v9", "v10", "v11", "v12", "v13", "v14", "v15", "v16",
        "v17", "v18", "v19", "v20", "v21", "v22", "v23", "v24", "v25", "v26", "v27", "v28", "v29", "v30", "v31", "v32",
        "v33", "v34", "v35", "v36", "v37", "v38", "v39", "v40", "v41", "v42", "v43", "v44", "v45", "v46", "v47", "v48",
        "v49", "v50", "v51", "v52", "v53", "v54", "v55", "v56", "v57", "v58", "v59", "v60", "v61", "v62", "v63", "v64",
        "v65", "v66", "v67", "v68", "v69", "v70", "v71", "v72", "v73", "v74", "v75", "v76", "v77", "v78", "v79", "v80",
        "v81", "v82", "v83", "v84", "v85", "v86", "v87", "v88", "v89", "v90", "v91", "v92", "v93", "v94", "v95", "v96",
        "v97", "v98", "v99", "v100", "v101", "v102", "v103", "v104", "v105", "v106", "v107", "v108", "v109", "v110",
        "v111", "v112", "v113", "v114", "v115", "v116", "v117", "v118", "v119", "v120", "v121", "v122", "v123", "v124",
        "v125", "v126", "v127", "v128", "v129", "v130", "v131", "v132", "v133", "v134", "v135", "v136", "v137", "v138",
        "v139", "v140", "v141", "v142", "v143", "v144", "v145", "v146", "v147", "v148", "v149", "v150", "v151", "v152",
        "v153", "v154", "v155", "v156", "v157", "v158", "v159", "v160", "v161", "v162", "v163", "v164", "v165", "v166",
        "v167", "v168", "v169", "v170", "v171", "v172", "v173", "v174", "v175", "v176", "v177", "v178", "v179", "v180",
        "v181", "v182", "v183", "v184", "v185", "v186", "v187", "v188", "v189", "v190", "v191", "v192", "v193", "v194",
        "v195", "v196", "v197", "v198", "v199", "v200", "v201", "v202", "v203", "v204", "v205", "v206", "v207", "v208",
        "v209", "v210", "v211", "v212", "v213", "v214", "v215", "v216", "v217", "v218", "v219", "v220", "v221", "v223",
        "v240", "v241", "v242", "v243", "v244", "v245", "v246", "v247", "v248", "v249", "v250", "v251", "v252", "v253",
        "s36", "s37", "s38", "s39", "s40", "s41", "s42", "s43", "s44", "s45", "s46", "s47", "s48", "s49", "s50", "s51",
        "s52", "s53", "s54", "s55", "s56", "s57", "s58", "s59", "s60", "s61"
    );
    } else {
      LANE_BITS(threadIdx.x & 63);
      u32x8 v;
      u32x16 s;
      v[0] = ((8 * rt + 4 * half + g) * 16 + c) * 16;
      const int hi = -(lane >> 5), m3 = min(c, 2);
      v[1] = (((max(8 * rt + ((g & 1) ? 0 : 4 * half) - 1, 0) * 16 + c) * 16) & ~hi) | ((ST_PW + xcd * 128 + m3 * 16) & hi);
      v[2] = sta + 128 + lane * 4;
      v[3] = ((sta + ((g & 1) ? 0 : 64) + c * 4) & ~hi) | ((A_MK + m3 * 4) & hi);
      v[4] = lr * A_SLOT + A_HALO + lc * 16;
      v[6] = (side * 1024 + (idx * 4 + wave - 4) * 8 + lr) * 3584 + (7 * xp + min(lc, 6)) * 128;
      v[7] = v[6] + (1 - 2 * side) * 1024 * 3584;
      u64 tw = (u64)w1;
      unsigned vn = (((wave ^ (4 * side)) * 256 + (idx & 1) * 128 + lr) * 28 + 7 * xp + min(idx >> 1, 6)) * 128 + ((lc ^ swz_tr(lr)) << 4);
      s[4] = UNI(idx == 15 && xcd < 3 && wave == 4 ? (xcd == 0 ? WS_O : xcd == 1 ? WS_HN + 16 : WS_G + 32) + PROBE : 0);
      v[5] = lane * (8192 + 640);
      put(s, 0, (u64)(x + (rt * 128 * 896 + 128) - (16 * 896 + 1024)));
      put(s, 2, (u64)(wqkv + (hp * 112 + 64 * 2688) - 4096));
      s[5] = wave * A_UNIT;
      put2(s, 6, wave > 4 ? 0xffffffffu : 0xffffff00u, 0u);
      s[8] = A_HALO + (wave - 4) * 512;
      s[9] = A_FL + 32 + 4 * half;
      put(s, 10, (u64)(ws + WS_ST));
    asm volatile(
      "s_waitcnt lgkmcnt(0)\n\ts_nop 4\n\t"
      ".p2align 3\n\t"
      "s_load_dwordx2 s[18:19], s[14:15], 0x20000\n\t"
      "s_sub_u32 s40, s4, 57344\n\t"
      "s_subb_u32 s41, s5, 0\n\t"
      "s_sub_u32 s42, s6, 86016\n\t"
      "s_subb_u32 s43, s7, 0\n\t"
      "v_mov_b32_e32 v0, 0x3f803f80\n\t"
      "v_mov_b32_e32 v1, 0x3f803f80\n\t"
      "v_mov_b32_e32 v2, 0x3f803f80\n\t"
      "v_mov_b32_e32 v3, 0x3f803f80\n\t"
      "s_mov_b64 exec, 0xffffff\n\t"
      "ds_write_b128 v204, v[0:3]\n\t"
      "s_mov_b64 exec, -1\n\t"
      "v_mov_b32_e32 v32, 154624\n\t"
      "v_mov_b32_e32 v33, s13\n\t"
      "v_mov_b32_e32 v36, 0\n\t"
      "v_mov_b32_e32 v37, 0\n\t"
      "v_mov_b32_e32 v38, 0\n\t"
      "v_mov_b32_e32 v39, 0\n\t"
      "ds_write_b128 v32, v[36:39]\n\t"
      "ds_write_b128 v32, v[36:39] offset:16\n\t"
      "ds_write_b128 v32, v[36:39] offset:32\n\t"
      "v_xor_b32_e64 v21, 64, v20\n\t"
      "v_add_u32_e32 v22, 55296, v20\n\t"
      "v_add_u32_e32 v23, 82944, v21\n\t"
      "v_add_u32_e32 v21, 27648, v21\n\t"
      "s_sub_u32 s4, s4, 128\n\t"
      "s_subb_u32 s5, s5, 0\n\t"
      "s_sub_u32 s6, s6, 172032\n\t"
      "s_subb_u32 s7, s7, 0\n\t"
      "s_add_u32 m0, s9, 0\n\t"
      "s_nop 0\n\t"
      "global_load_lds_dwordx4 v195, s[6:7] offset:2048\n\t"
      "global_load_lds_dwordx4 v192, s[4:5]\n\t"
      "global_load_lds_dwordx4 v193, s[4:5] offset:1024\n\t"
      "global_load_lds_dwordx4 v196, s[6:7] offset:3072\n\t"
      "s_add_u32 m0, s9, 4096\n\t"
      "s_nop 0\n\t"
      "global_load_lds_dwordx4 v197, s[6:7]\n\t"
      "s_add_u32 m0, s12, 0\n\t"
      "s_mov_b64 exec, s[10:11]\n\t"
      "global_load_lds_dwordx4 v194, s[4:5]\n\t"
      "s_mov_b64 exec, -1\n\t"
      "s_add_u32 s4, s4, 128\n\t"
      "s_addc_u32 s5, s5, 0\n\t"
      "s_add_u32 s6, s6, 172032\n\t"
      "s_addc_u32 s7, s7, 0\n\t"
      "s_waitcnt lgkmcnt(0)\n\t"
      "s_add_u32 s18, s18, 1\n\t"
      "s_addc_u32 s19, s19, 0\n\t"
      "s_mul_i32 s48, s18, 0x9e3779b9\n\t"
      "s_mul_hi_u32 s17, s18, 0x7f4a7c15\n\t"
      "s_add_u32 s17, s17, s48\n\t"
      "s_mul_i32 s48, s19, 0x7f4a7c15\n\t"
      "s_add_u32 s17, s17, s48\n\t"
      "s_mul_i32 s16, s18, 0x7f4a7c15\n\t"
      "s_add_u32 s16, s16, 0xd192ed03\n\t"
      "s_addc_u32 s17, s17, 0xd1b54a32\n\t"
      "v_mov_b32_e32 v12, s16\n\t"
      "v_mov_b32_e32 v13, s17\n\t"
      "v_mov_b32_e32 v14, s18\n\t"
      "v_mov_b32_e32 v15, s19\n\t"
      "v_mov_b32_e32 v16, 163600\n\t"
      "s_mov_b64 exec, 1\n\t"
      "ds_write_b128 v16, v[12:15]\n\t"
      "s_mov_b64 exec, -1\n\t"
      "s_and_b32 s47, s8, 48\n\t"
      "s_add_u32 s47, s47, 114688\n\t"
      "v_mbcnt_lo_u32_b32 v28, -1, 0\n\t"
      "v_mov_b32_e32 v25, 0\n\t"
      "v_mov_b32_e32 v26, s16\n\t"
      "v_mov_b32_e32 v27, s17\n\t"
      "v_lshl_add_u32 v28, v28, 7, s47\n\t"
      "s_cmp_eq_u32 s8, 0\n\t"
      "s_cbranch_scc1 PRt_%=\n\t"
      "s_add_u32 s52, s14, s8\n\t"
      "s_addc_u32 s53, s15, 0\n\t"
      "s_mov_b32 s54, 0\n\t"
      "s_mov_b64 s[56:57], -1\n\t"
      "s_load_dword s50, s[52:53], 0x1f80\n\t"
      "PRt_%=:\n\t"
      "s_cmp_eq_u32 s8, 0\n\t"
      "s_cbranch_scc1 PRu_%=\n\t"
      "s_mov_b64 exec, 3\n\t"
      "global_store_dword v205, v0, s[52:53] sc1\n\t"
      "s_mov_b64 exec, -1\n\t"
      "PRu_%=:\n\t"
      "s_add_u32 m0, s9, 22528\n\t"
      "s_nop 0\n\t"
      "global_load_lds_dwordx4 v195, s[42:43] offset:2048\n\t"
      "global_load_lds_dwordx4 v192, s[40:41]\n\t"
      "global_load_lds_dwordx4 v193, s[40:41] offset:1024\n\t"
      "global_load_lds_dwordx4 v196, s[42:43] offset:3072\n\t"
      "s_add_u32 m0, s9, 26624\n\t"
      "s_nop 0\n\t"
      "global_load_lds_dwordx4 v197, s[42:43]\n\t"
      "s_add_u32 m0, s9, 43008\n\t"
      "s_nop 0\n\t"
      "global_load_lds_dwordx4 v195, s[6:7] offset:2048\n\t"
      "global_load_lds_dwordx4 v192, s[4:5]\n\t"
      "global_load_lds_dwordx4 v193, s[4:5] offset:1024\n\t"
      "global_load_lds_dwordx4 v196, s[6:7] offset:3072\n\t"
      "s_add_u32 m0, s9, 47104\n\t"
      "s_nop 0\n\t"
      "global_load_lds_dwordx4 v197, s[6:7]\n\t"
      "s_add_u32 m0, s12, 43008\n\t"
      "s_mov_b64 exec, s[10:11]\n\t"
      "global_load_lds_dwordx4 v194, s[4:5]\n\t"
      "s_mov_b64 exec, -1\n\t"
      "s_waitcnt vmcnt(11) lgkmcnt(0)\n\t"
      "s_barrier\n\t"
      "s_cmp_eq_u32 s8, 0\n\t"
      "s_cbranch_scc1 PRC0_%=\n\t"
      "s_mov_b32 s55, 8832\n\t"
      "s_call_b64 s[44:45], PRB_%=\n\t"
      "PRC0_%=:\n\t"
      "s_add_u32 s4, s4, 128\n\t"
      "s_addc_u32 s5, s5, 0\n\t"
      "s_add_u32 s6, s6, 172032\n\t"
      "s_addc_u32 s7, s7, 0\n\t"
      "s_add_u32 s40, s40, 128\n\t"
      "s_addc_u32 s41, s41, 0\n\t"
      "s_add_u32 s42, s42, 172032\n\t"
      "s_addc_u32 s43, s43, 0\n\t"
      "s_add_u32 m0, s9, 65536\n\t"
      "s_nop 0\n\t"
      "global_load_lds_dwordx4 v195, s[42:43] offset:2048\n\t"
      "global_load_lds_dwordx4 v192, s[40:41]\n\t"
      "global_load_lds_dwordx4 v193, s[40:41] offset:1024\n\t"
      "global_load_lds_dwordx4 v196, s[42:43] offset:3072\n\t"
      "s_add_u32 m0, s9, 69632\n\t"
      "s_nop 0\n\t"
      "global_load_lds_dwordx4 v197, s[42:43]\n\t"
      "s_add_u32 m0, s9, 86016\n\t"
      "s_nop 0\n\t"
      "global_load_lds_dwordx4 v195, s[6:7] offset:2048\n\t"
      "global_load_lds_dwordx4 v192, s[4:5]\n\t"
      "global_load_lds_dwordx4 v193, s[4:5] offset:1024\n\t"
      "global_load_lds_dwordx4 v196, s[6:7] offset:3072\n\t"
      "s_add_u32 m0, s9, 90112\n\t"
      "s_nop 0\n\t"
      "global_load_lds_dwordx4 v197, s[6:7]\n\t"
      "s_add_u32 m0, s12, 86016\n\t"
      "s_mov_b64 exec, s[10:11]\n\t"
      "global_load_lds_dwordx4 v194, s[4:5]\n\t"
      "s_mov_b64 exec, -1\n\t"
      "s_waitcnt vmcnt(11)\n\t"
      "s_barrier\n\t"
      "s_cmp_eq_u32 s8, 0\n\t"
      "s_cbranch_scc1 PRC1_%=\n\t"
      "s_mov_b32 s55, 17664\n\t"
      "s_call_b64 s[44:45], PRB_%=\n\t"
      "PRC1_%=:\n\t"
      "s_add_u32 s4, s4, 128\n\t"
      "s_addc_u32 s5, s5, 0\n\t"
      "s_add_u32 s6, s6, 172032\n\t"
      "s_addc_u32 s7, s7, 0\n\t"
      "s_add_u32 s40, s40, 128\n\t"
      "s_addc_u32 s41, s41, 0\n\t"
      "s_add_u32 s42, s42, 172032\n\t"
      "s_addc_u32 s43, s43, 0\n\t"
      "s_add_u32 m0, s9, 4294946816\n\t"
      "s_nop 0\n\t"
      "global_load_lds_dwordx4 v195, s[42:43] offset:2048\n\t"
      "global_load_lds_dwordx4 v192, s[40:41]\n\t"
      "global_load_lds_dwordx4 v193, s[40:41] offset:1024\n\t"
      "global_load_lds_dwordx4 v196, s[42:43] offset:3072\n\t"
      "s_add_u32 m0, s9, 4294950912\n\t"
      "s_nop 0\n\t"
      "global_load_lds_dwordx4 v197, s[42:43]\n\t"
      "s_add_u32 m0, s9, 0\n\t"
      "s_nop 0\n\t"
      "global_load_lds_dwordx4 v195, s[6:7] offset:2048\n\t"
      "global_load_lds_dwordx4 v192, s[4:5]\n\t"
      "global_load_lds_dwordx4 v193, s[4:5] offset:1024\n\t"
      "global_load_lds_dwordx4 v196, s[6:7] offset:3072\n\t"
      "s_add_u32 m0, s9, 4096\n\t"
      "s_nop 0\n\t"
      "global_load_lds_dwordx4 v197, s[6:7]\n\t"
      "s_add_u32 m0, s12, 0\n\t"
      "s_mov_b64 exec, s[10:11]\n\t"
      "global_load_lds_dwordx4 v194, s[4:5]\n\t"
      "s_mov_b64 exec, -1\n\t"
      "s_cmp_eq_u32 s8, 0\n\t"
      "s_cbranch_scc1 PRS_%=\n\t"
      "s_cmp_lt_u32 s57, s56\n\t"
      "s_cselect_b32 s46, 0x2000, 0\n\t"
      "v_mov_b32_e32 v24, s46\n\t"
      "v_xor_b32_e32 v26, s46, v26\n\t"
      "s_mov_b64 exec, 0xff\n\t"
      "global_store_dwordx4 v28, v[24:27], s[14:15] sc1\n\t"
      "s_mov_b64 exec, -1\n\t"
      "PRS_%=:\n\t"
      "s_waitcnt vmcnt(11)\n\t"
      "s_barrier\n\t"
      "s_add_u32 s4, s4, 128\n\t"
      "s_addc_u32 s5, s5, 0\n\t"
      "s_add_u32 s6, s6, 172032\n\t"
      "s_addc_u32 s7, s7, 0\n\t"
      "s_add_u32 s40, s40, 128\n\t"
      "s_addc_u32 s41, s41, 0\n\t"
      "s_add_u32 s42, s42, 172032\n\t"
      "s_addc_u32 s43, s43, 0\n\t"
      "s_add_u32 m0, s9, 22528\n\t"
      "s_nop 0\n\t"
      "global_load_lds_dwordx4 v195, s[42:43] offset:2048\n\t"
      "global_load_lds_dwordx4 v192, s[40:41]\n\t"
      "global_load_lds_dwordx4 v193, s[40:41] offset:1024\n\t"
      "global_load_lds_dwordx4 v196, s[42:43] offset:3072\n\t"
      "s_add_u32 m0, s9, 26624\n\t"
      "s_nop 0\n\t"
      "global_load_lds_dwordx4 v197, s[42:43]\n\t"
      "s_add_u32 m0, s9, 43008\n\t"
      "s_nop 0\n\t"
      "global_load_lds_dwordx4 v195, s[6:7] offset:2048\n\t"
      "global_load_lds_dwordx4 v192, s[4:5]\n\t"
      "global_load_lds_dwordx4 v193, s[4:5] offset:1024\n\t"
      "global_load_lds_dwordx4 v196, s[6:7] offset:3072\n\t"
      "s_add_u32 m0, s9, 47104\n\t"
      "s_nop 0\n\t"
      "global_load_lds_dwordx4 v197, s[6:7]\n\t"
      "s_add_u32 m0, s12, 43008\n\t"
      "s_mov_b64 exec, s[10:11]\n\t"
      "global_load_lds_dwordx4 v194, s[4:5]\n\t"
      "s_mov_b64 exec, -1\n\t"
      "s_waitcnt vmcnt(11)\n\t"
      "s_barrier\n\t"
      "s_add_u32 s4, s4, 128\n\t"
      "s_addc_u32 s5, s5, 0\n\t"
      "s_add_u32 s6, s6, 172032\n\t"
      "s_addc_u32 s7, s7, 0\n\t"
      "s_add_u32 s40, s40, 128\n\t"
      "s_addc_u32 s41, s41, 0\n\t"
      "s_add_u32 s42, s42, 172032\n\t"
      "s_addc_u32 s43, s43, 0\n\t"
      "s_add_u32 m0, s9, 65536\n\t"
      "s_nop 0\n\t"
      "global_load_lds_dwordx4 v195, s[42:43] offset:2048\n\t"
      "global_load_lds_dwordx4 v192, s[40:41]\n\t"
      "global_load_lds_dwordx4 v193, s[40:41] offset:1024\n\t"
      "global_load_lds_dwordx4 v196, s[42:43] offset:3072\n\t"
      "s_add_u32 m0, s9, 69632\n\t"
      "s_nop 0\n\t"
      "global_load_lds_dwordx4 v197, s[42:43]\n\t"
      "s_add_u32 m0, s9, 86016\n\t"
      "s_nop 0\n\t"
      "global_load_lds_dwordx4 v195, s[6:7] offset:2048\n\t"
      "global_load_lds_dwordx4 v192, s[4:5]\n\t"
      "global_load_lds_dwordx4 v193, s[4:5] offset:1024\n\t"
      "global_load_lds_dwordx4 v196, s[6:7] offset:3072\n\t"
      "s_add_u32 m0, s9, 90112\n\t"
      "s_nop 0\n\t"
      "global_load_lds_dwordx4 v197, s[6:7]\n\t"
      "s_add_u32 m0, s12, 86016\n\t"
      "s_mov_b64 exec, s[10:11]\n\t"
      "global_load_lds_dwordx4 v194, s[4:5]\n\t"
      "s_mov_b64 exec, -1\n\t"
      "global_load_dwordx4 v[4:7], v200, s[14:15] sc1\n\t"
      "global_load_dwordx4 v[8:11], v201, s[14:15] sc1\n\t"
      "s_waitcnt vmcnt(13)\n\t"
      "s_barrier\n\t"
      "s_add_u32 s4, s4, 128\n\t"
      "s_addc_u32 s5, s5, 0\n\t"
      "s_add_u32 s6, s6, 172032\n\t"
      "s_addc_u32 s7, s7, 0\n\t"
      "s_add_u32 s40, s40, 128\n\t"
      "s_addc_u32 s41, s41, 0\n\t"
      "s_add_u32 s42, s42, 172032\n\t"
      "s_addc_u32 s43, s43, 0\n\t"
      "s_add_u32 m0, s9, 4294946816\n\t"
      "s_nop 0\n\t"
      "global_load_lds_dwordx4 v195, s[42:43] offset:2048\n\t"
      "global_load_lds_dwordx4 v192, s[40:41]\n\t"
      "global_load_lds_dwordx4 v193, s[40:41] offset:1024\n\t"
      "global_load_lds_dwordx4 v196, s[42:43] offset:3072\n\t"
      "s_add_u32 m0, s9, 4294950912\n\t"
      "s_nop 0\n\t"
      "global_load_lds_dwordx4 v197, s[42:43]\n\t"
      "s_add_u32 m0, s9, 0\n\t"
      "s_nop 0\n\t"
      "global_load_lds_dwordx4 v195, s[6:7] offset:2048\n\t"
      "global_load_lds_dwordx4 v192, s[4:5]\n\t"
      "global_load_lds_dwordx4 v193, s[4:5] offset:1024\n\t"
      "global_load_lds_dwordx4 v196, s[6:7] offset:3072\n\t"
      "s_add_u32 m0, s9, 4096\n\t"
      "s_nop 0\n\t"
      "global_load_lds_dwordx4 v197, s[6:7]\n\t"
      "s_add_u32 m0, s12, 0\n\t"
      "s_mov_b64 exec, s[10:11]\n\t"
      "global_load_lds_dwordx4 v194, s[4:5]\n\t"
      "s_mov_b64 exec, -1\n\t"
      "s_waitcnt vmcnt(13)\n\t"
      "s_barrier\n\t"
      "s_mov_b32 m0, 163328\n\t"
      "s_add_u32 s44, s20, 0\n\t"
      "s_addc_u32 s45, s21, 0\n\t"
      "global_load_lds_dword v206, s[44:45]\n\t"
      "s_mov_b32 m0, 163328\n\t"
      "s_add_u32 s44, s20, 1835008\n\t"
      "s_addc_u32 s45, s21, 0\n\t"
      "global_load_lds_dword v206, s[44:45]\n\t"
      "s_waitcnt vmcnt(2)\n\t"
      "s_barrier\n\t"
      "v_xor_b32_e32 v6, v6, v4\n\t"
      "v_xor_b32_e32 v7, v7, v5\n\t"
      "v_xor_b32_e32 v10, v10, v8\n\t"
      "v_xor_b32_e32 v11, v11, v9\n\t"
      "v_cmp_ne_u64_e64 s[48:49], s[16:17], v[6:7]\n\t"
      "v_cmp_ne_u64_e32 vcc, s[16:17], v[10:11]\n\t"
      "s_nop 0\n\t"
      "s_or_b64 vcc, vcc, s[48:49]\n\t"
      "s_cbranch_scc1 GRR_%=\n\t"
      "GRB_%=:\n\t"
      "v_mul_f32_e32 v6, 0x3e8b97f6, v5\n\t"
      "ds_write2_b32 v202, v4, v5 offset0:0 offset1:96\n\t"
      "ds_write2_b32 v203, v8, v9 offset0:0 offset1:96\n\t"
      "ds_write_b32 v202, v6 offset:640\n\t"
      "s_mov_b64 exec, 1\n\t"
      "ds_write_b32 v33, v33\n\t"
      "s_mov_b64 exec, -1\n\t"
      "s_sub_u32 s46, s12, 38912\n\t"
      "s_lshl_b32 s46, s46, 3\n\t"
      "s_add_u32 m0, s46, 65536\n\t"
      "s_add_u32 s44, s20, 0\n\t"
      "s_addc_u32 s45, s21, 0\n\t"
      "global_load_lds_dwordx4 v20, s[44:45]\n\t"
      "global_load_lds_dwordx4 v21, s[44:45] offset:1024\n\t"
      "global_load_lds_dwordx4 v22, s[44:45] offset:2048\n\t"
      "global_load_lds_dwordx4 v23, s[44:45] offset:3072\n\t"
      "s_add_u32 m0, s46, 98304\n\t"
      "s_add_u32 s44, s20, 114688\n\t"
      "s_addc_u32 s45, s21, 0\n\t"
      "global_load_lds_dwordx4 v20, s[44:45]\n\t"
      "global_load_lds_dwordx4 v21, s[44:45] offset:1024\n\t"
      "global_load_lds_dwordx4 v22, s[44:45] offset:2048\n\t"
      "global_load_lds_dwordx4 v23, s[44:45] offset:3072\n\t"
      "s_waitcnt lgkmcnt(0)\n\t"
      "s_branch HEND_%=\n\t"
      ".p2align 6\n\t"
      "PRB_%=:\n\t"
      "s_waitcnt lgkmcnt(0)\n\t"
      "s_memtime s[58:59]\n\t"
      "s_waitcnt lgkmcnt(0)\n\t"
      "s_load_dword s50, s[52:53], s54\n\t"
      "s_waitcnt lgkmcnt(0)\n\t"
      "s_memtime s[46:47]\n\t"
      "s_add_u32 s54, s54, 8832\n\t"
      "s_waitcnt lgkmcnt(0)\n\t"
      "s_sub_u32 s46, s46, s58\n\t"
      "s_min_u32 s46, s56, s46\n\t"
      "s_mov_b32 s56, s57\n\t"
      "s_mov_b32 s57, s46\n\t"
      "s_cmp_lt_u32 s54, s55\n\t"
      "s_cbranch_scc1 PRB_%=\n\t"
      "s_setpc_b64 s[44:45]\n\t"
      "GRR_%=:\n\t"
      "s_mov_b32 s51, 0\n\t"
      "GRL_%=:\n\t"
      "s_add_u32 s51, s51, 1\n\t"
      "s_cmp_lt_u32 s51, 262144\n\t"
      "s_cbranch_scc0 GRX_%=\n\t"
      "s_sleep 2\n\t"
      "global_load_dwordx4 v[4:7], v200, s[14:15] sc1\n\t"
      "global_load_dwordx4 v[8:11], v201, s[14:15] sc1\n\t"
      "s_waitcnt vmcnt(0)\n\t"
      "v_xor_b32_e32 v6, v6, v4\n\t"
      "v_xor_b32_e32 v7, v7, v5\n\t"
      "v_xor_b32_e32 v10, v10, v8\n\t"
      "v_xor_b32_e32 v11, v11, v9\n\t"
      "v_cmp_ne_u64_e64 s[48:49], s[16:17], v[6:7]\n\t"
      "v_cmp_ne_u64_e32 vcc, s[16:17], v[10:11]\n\t"
      "s_nop 0\n\t"
      "s_or_b64 vcc, vcc, s[48:49]\n\t"
      "s_cbranch_scc1 GRL_%=\n\t"
      "GRX_%=:\n\t"
      "s_mov_b32 exec_lo, 0\n\t"
      "v_and_b32_e32 v8, 0x2000, v8\n\t"
      "s_mov_b32 exec_lo, -1\n\t"
      "s_branch GRB_%=\n\t"
      "HEND_%=:\n\t"
      ".LhopA0:\n\t"
      "s_cbranch_execz .LhopA4\n\t"
      : "+{s[4:19]}"(s), "+{s[20:21]}"(tw), "+{v[192:199]}"(f), "+{v[200:207]}"(v), "+{v20}"(vn)
      :
      : "memory", "vcc", "scc",
        "v0", "v1", "v2", "v3", "v4", "v5", "v6", "v7", "v8", "v9", "v10", "v11", "v12", "v13", "v14", "v15", "v16",
        "v17", "v18", "v19", "v21", "v22", "v23", "v24", "v25", "v26", "v27", "v28", "v29", "v30", "v31", "v32", "v33",
        "v34", "v35", "v36", "v37", "v38", "v39", "s40", "s41", "s42", "s43", "s44", "s45", "s46", "s47", "s48", "s49",
        "s50", "s51", "s52", "s53", "s54", "s55", "s56", "s57", "s58", "s59"
    );
    }
    PIN();
    LANE_BITS(__builtin_amdgcn_mbcnt_hi(~0u, __builtin_amdgcn_mbcnt_lo(~0u, 0u)));
    const bool has3 = idx < 14;
    const int ft = has3 ? 7 * xp + (idx >> 1) : 0, blk0 = 7 * (2 * side + (idx & 1));
    u32x8 v;
    u32x16 s;
    v[0] = lane * 16;
    const int rot = 4 * (idx & 1), kg = 2 * ft + ((wave >> 1) & 1), ks = wave ^ (4 * side), rn = rot + 2;
    u64 kr = (u64)(7 - rn) | ((u64)rot << 32);
    v[1] = has3 ? lr * 3584 + ((lc ^ swz_tr(lr)) << 4) : SP3 ? (min(lane, 55) + 96 * (idx & 1)) * 4096 + 384 + xp * 1024 : (lr + 8 * side) * 4096 + lc * 128 + xp * 1024;
    v[2] = wave * 4096 + g * 1024 + q4 * 128 + (((2 * (wave & 3) + (p4 >> 1)) ^ swz_tr(8 * g + q4)) << 4) + (p4 & 1) * 8;
    v[3] = wave * 4096 + (wave & 3) * 1024 + ((lane * 16) ^ ((g & 1) << 7));
    const int gg = 2 * (lane >> 5) + (lane & 1);
    v[4] = (wave & 3) * 1024 + (((gg * 16 + ((lane >> 1) & 15)) * 16) ^ ((gg & 1) << 7));
    v[5] = (wave & 1) * 512 + lane * 8;
    v[6] = has3 ? g * 8 : (160 + 32 * side + lr) * 4096 + lc * 128 + xp * 1024;
    v[7] = ST_H3 + part(xp * 16384 + (ks * 16 + c) * 128, side);
    put(s, 0, has3 ? (u64)(w1 + ft * 128 + (ks * 256 + rn * 32) * 3584) : (u64)(w2 + (wave * 224 + (idx & 1) * 16) * 4096));
    put(s, 2, (u64)(ws + WS_HN + unit(blk0, ks, 0)));
    put(s, 4, (u64)(ws + WS_G + unit(blk0, kg / 7, 0) + kg % 7 * 1024));
    put(s, 6, (u64)(b1 + (64 * ft + 16 * (wave & 3)) * 2));
    put(s, 8, (u64)(ws + WS_ST));
    unsigned hint = ST_H4 + side * 8192 + ((idx & 1) * 28 + ft) * 128;
    s[12] = UNI((has3 ? 4 : 0) | (blockIdx.x == 0 && wave == 0 ? 32 : 0) |  (ks == 4 * side + ((xp + XO) & 3) ? 16 : 0));
    s[13] = wave * G_WV;
    asm volatile(
      "s_waitcnt lgkmcnt(0)\n\ts_nop 4\n\t"
      ".p2align 3\n\t"
      "s_bitcmp0_b32 s16, 2\n\t"
      "s_cbranch_scc1 ASP_%=\n\t"
      "s_lshr_b32 s23, s17, 2\n\t"
      "v_mov_b32_e32 v216, 163600\n\t"
      "ds_read_b128 v[216:219], v216\n\t"
      "v_mov_b32_e32 v220, 154756\n\t"
      "ds_read2_b32 v[220:221], v220 offset0:0 offset1:1\n\t"
      "v_xor_b32_e32 v248, 64, v241\n\t"
      "v_add_u32_e32 v249, 55296, v241\n\t"
      "v_add_u32_e32 v250, 82944, v248\n\t"
      "v_add_u32_e32 v248, 27648, v248\n\t"
      "v_xor_b32_e32 v251, 32, v242\n\t"
      "v_xor_b32_e32 v252, 64, v242\n\t"
      "v_xor_b32_e32 v253, 96, v242\n\t"
      "global_load_dwordx2 v[238:239], v246, s[10:11]\n\t"
      "s_waitcnt lgkmcnt(0)\n\t"
      "v_readfirstlane_b32 s14, v216\n\t"
      "v_readfirstlane_b32 s15, v217\n\t"
      "v_readfirstlane_b32 s46, v218\n\t"
      "v_readfirstlane_b32 s47, v219\n\t"
      "v_readfirstlane_b32 s48, v220\n\t"
      "v_readfirstlane_b32 s49, v221\n\t"
      "v_xor_b32_e32 v247, v247, v221\n\t"
      "s_nop 0\n\t"
      "s_xor_b32 s6, s6, s48\n\t"
      "s_xor_b32 s8, s8, s49\n\t"
      "s_xor_b32 s22, s22, s49\n\t"
      "s_add_u32 s52, s6, 0\n\t"
      "s_addc_u32 s53, s7, 0\n\t"
      "s_add_u32 s54, s6, 131072\n\t"
      "s_addc_u32 s55, s7, 0\n\t"
      "s_add_u32 s56, s6, 262144\n\t"
      "s_addc_u32 s57, s7, 0\n\t"
      "s_add_u32 s58, s6, 393216\n\t"
      "s_addc_u32 s59, s7, 0\n\t"
      "s_add_u32 s60, s6, 524288\n\t"
      "s_addc_u32 s61, s7, 0\n\t"
      "s_add_u32 s62, s6, 655360\n\t"
      "s_addc_u32 s63, s7, 0\n\t"
      "s_add_u32 s64, s6, 786432\n\t"
      "s_addc_u32 s65, s7, 0\n\t"
      "s_add_u32 s48, s21, 0\n\t"
      "s_cmp_ge_u32 s48, 8\n\t"
      "s_cselect_b32 s49, 8, 0\n\t"
      "s_sub_u32 s48, s48, s49\n\t"
      "v_lshl_add_u32 v176, s48, 10, v240\n\t"
      "s_add_u32 s48, s21, 1\n\t"
      "s_cmp_ge_u32 s48, 8\n\t"
      "s_cselect_b32 s49, 8, 0\n\t"
      "s_sub_u32 s48, s48, s49\n\t"
      "v_lshl_add_u32 v177, s48, 10, v240\n\t"
      "s_add_u32 s48, s21, 2\n\t"
      "s_cmp_ge_u32 s48, 8\n\t"
      "s_cselect_b32 s49, 8, 0\n\t"
      "s_sub_u32 s48, s48, s49\n\t"
      "v_lshl_add_u32 v178, s48, 10, v240\n\t"
      "s_add_u32 s48, s21, 3\n\t"
      "s_cmp_ge_u32 s48, 8\n\t"
      "s_cselect_b32 s49, 8, 0\n\t"
      "s_sub_u32 s48, s48, s49\n\t"
      "v_lshl_add_u32 v179, s48, 10, v240\n\t"
      "s_add_u32 s48, s21, 4\n\t"
      "s_cmp_ge_u32 s48, 8\n\t"
      "s_cselect_b32 s49, 8, 0\n\t"
      "s_sub_u32 s48, s48, s49\n\t"
      "v_lshl_add_u32 v180, s48, 10, v240\n\t"
      "s_add_u32 s48, s21, 5\n\t"
      "s_cmp_ge_u32 s48, 8\n\t"
      "s_cselect_b32 s49, 8, 0\n\t"
      "s_sub_u32 s48, s48, s49\n\t"
      "v_lshl_add_u32 v181, s48, 10, v240\n\t"
      "s_add_u32 s48, s21, 6\n\t"
      "s_cmp_ge_u32 s48, 8\n\t"
      "s_cselect_b32 s49, 8, 0\n\t"
      "s_sub_u32 s48, s48, s49\n\t"
      "v_lshl_add_u32 v182, s48, 10, v240\n\t"
      "s_add_u32 s48, s21, 7\n\t"
      "s_cmp_ge_u32 s48, 8\n\t"
      "s_cselect_b32 s49, 8, 0\n\t"
      "s_sub_u32 s48, s48, s49\n\t"
      "v_lshl_add_u32 v183, s48, 10, v240\n\t"
      "s_cmp_ge_u32 s17, 65536\n\t"
      "s_cbranch_scc0 BOH_%=\n\t"
      "s_barrier\n\t"
      "BOH_%=:\n\t"
      "s_waitcnt vmcnt(5)\n\t"
      "s_add_u32 m0, s23, 0\n\t"
      "s_nop 0\n\t"
      "global_load_lds_dwordx4 v241, s[4:5]\n\t"
      "global_load_lds_dwordx4 v248, s[4:5] offset:1024\n\t"
      "global_load_lds_dwordx4 v249, s[4:5] offset:2048\n\t"
      "global_load_lds_dwordx4 v250, s[4:5] offset:3072\n\t"
      "s_sub_u32 s20, s20, 1\n\t"
      "s_cselect_b32 s48, -7, 1\n\t"
      "s_cselect_b32 s49, -1, 0\n\t"
      "s_mul_i32 s48, s48, 114688\n\t"
      "s_add_u32 s4, s4, s48\n\t"
      "s_addc_u32 s5, s5, s49\n\t"
      "v_add_u32_e32 v242, 65536, v242\n\t"
      "v_add_u32_e32 v251, 65536, v251\n\t"
      "v_add_u32_e32 v252, 65536, v252\n\t"
      "v_add_u32_e32 v253, 65536, v253\n\t"
      "ds_read_b64_tr_b16 v[0:1], v242\n\t"
      "ds_read_b64_tr_b16 v[2:3], v242 offset:512\n\t"
      "ds_read_b64_tr_b16 v[4:5], v251\n\t"
      "ds_read_b64_tr_b16 v[6:7], v251 offset:512\n\t"
      "ds_read_b64_tr_b16 v[8:9], v252\n\t"
      "ds_read_b64_tr_b16 v[10:11], v252 offset:512\n\t"
      "ds_read_b64_tr_b16 v[12:13], v253\n\t"
      "ds_read_b64_tr_b16 v[14:15], v253 offset:512\n\t"
      "s_waitcnt vmcnt(5)\n\t"
      "s_add_u32 m0, s23, 32768\n\t"
      "s_nop 0\n\t"
      "global_load_lds_dwordx4 v241, s[4:5]\n\t"
      "global_load_lds_dwordx4 v248, s[4:5] offset:1024\n\t"
      "global_load_lds_dwordx4 v249, s[4:5] offset:2048\n\t"
      "global_load_lds_dwordx4 v250, s[4:5] offset:3072\n\t"
      "s_sub_u32 s20, s20, 1\n\t"
      "s_cselect_b32 s48, -7, 1\n\t"
      "s_cselect_b32 s49, -1, 0\n\t"
      "s_mul_i32 s48, s48, 114688\n\t"
      "s_add_u32 s4, s4, s48\n\t"
      "s_addc_u32 s5, s5, s49\n\t"
      "ds_read_b64_tr_b16 v[16:17], v242 offset:32768\n\t"
      "ds_read_b64_tr_b16 v[18:19], v242 offset:33280\n\t"
      "ds_read_b64_tr_b16 v[20:21], v251 offset:32768\n\t"
      "ds_read_b64_tr_b16 v[22:23], v251 offset:33280\n\t"
      "ds_read_b64_tr_b16 v[24:25], v252 offset:32768\n\t"
      "ds_read_b64_tr_b16 v[26:27], v252 offset:33280\n\t"
      "ds_read_b64_tr_b16 v[28:29], v253 offset:32768\n\t"
      "ds_read_b64_tr_b16 v[30:31], v253 offset:33280\n\t"
      "s_waitcnt vmcnt(8)\n\t"
      "v_xor_b32_e32 v164, 1024, v243\n\t"
      "v_xor_b32_e32 v165, 2048, v243\n\t"
      "v_xor_b32_e32 v166, 3072, v243\n\t"
      "v_lshlrev_b32_e32 v236, 16, v238\n\t"
      "v_and_b32_e32 v237, 0xffff0000, v238\n\t"
      "v_lshlrev_b32_e32 v238, 16, v239\n\t"
      "v_and_b32_e32 v239, 0xffff0000, v239\n\t"
      "v_mov_b32_e32 v235, 0xbdd2d3e8\n\t"
      "s_waitcnt vmcnt(4)\n\t"
      "s_waitcnt lgkmcnt(8)\n\t"
      "s_add_u32 m0, s23, 65536\n\t"
      "s_nop 0\n\t"
      "global_load_lds_dwordx4 v241, s[4:5]\n\t"
      "global_load_lds_dwordx4 v248, s[4:5] offset:1024\n\t"
      "global_load_lds_dwordx4 v249, s[4:5] offset:2048\n\t"
      "global_load_lds_dwordx4 v250, s[4:5] offset:3072\n\t"
      "s_sub_u32 s20, s20, 1\n\t"
      "s_cselect_b32 s48, -7, 1\n\t"
      "s_cselect_b32 s49, -1, 0\n\t"
      "s_mul_i32 s48, s48, 114688\n\t"
      "s_add_u32 s4, s4, s48\n\t"
      "s_addc_u32 s5, s5, s49\n\t"
      "v_add_u32_e32 v242, 4294901760, v242\n\t"
      "v_add_u32_e32 v251, 4294901760, v251\n\t"
      "v_add_u32_e32 v252, 4294901760, v252\n\t"
      "v_add_u32_e32 v253, 4294901760, v253\n\t"
      "ds_read_b64_tr_b16 v[32:33], v242\n\t"
      "ds_read_b64_tr_b16 v[34:35], v242 offset:512\n\t"
      "ds_read_b64_tr_b16 v[36:37], v251\n\t"
      "ds_read_b64_tr_b16 v[38:39], v251 offset:512\n\t"
      "ds_read_b64_tr_b16 v[40:41], v252\n\t"
      "ds_read_b64_tr_b16 v[42:43], v252 offset:512\n\t"
      "ds_read_b64_tr_b16 v[44:45], v253\n\t"
      "ds_read_b64_tr_b16 v[46:47], v253 offset:512\n\t"
      "s_waitcnt vmcnt(4)\n\t"
      "s_waitcnt lgkmcnt(8)\n\t"
      "s_add_u32 m0, s23, 98304\n\t"
      "s_nop 0\n\t"
      "global_load_lds_dwordx4 v241, s[4:5]\n\t"
      "global_load_lds_dwordx4 v248, s[4:5] offset:1024\n\t"
      "global_load_lds_dwordx4 v249, s[4:5] offset:2048\n\t"
      ".LhopD0:\n\t"
      "s_cbranch_execz .LbackD\n\t"
      "global_load_lds_dwordx4 v250, s[4:5] offset:3072\n\t"
      "s_sub_u32 s20, s20, 1\n\t"
      "s_cselect_b32 s48, -7, 1\n\t"
      "s_cselect_b32 s49, -1, 0\n\t"
      "s_mul_i32 s48, s48, 114688\n\t"
      "s_add_u32 s4, s4, s48\n\t"
      "s_addc_u32 s5, s5, s49\n\t"
      "ds_read_b64_tr_b16 v[48:49], v242 offset:32768\n\t"
      "ds_read_b64_tr_b16 v[50:51], v242 offset:33280\n\t"
      "ds_read_b64_tr_b16 v[52:53], v251 offset:32768\n\t"
      ".LhopD1:\n\t"
      "s_cbranch_execz .LbackD\n\t"
      "ds_read_b64_tr_b16 v[54:55], v251 offset:33280\n\t"
      "ds_read_b64_tr_b16 v[56:57], v252 offset:32768\n\t"
      "ds_read_b64_tr_b16 v[58:59], v252 offset:33280\n\t"
      "ds_read_b64_tr_b16 v[60:61], v253 offset:32768\n\t"
      "ds_read_b64_tr_b16 v[62:63], v253 offset:33280\n\t"
      "s_waitcnt vmcnt(4)\n\t"
      "s_waitcnt lgkmcnt(8)\n\t"
      "s_add_u32 m0, s23, 0\n\t"
      "s_nop 0\n\t"
      "global_load_lds_dwordx4 v241, s[4:5]\n\t"
      "global_load_lds_dwordx4 v248, s[4:5] offset:1024\n\t"
      "global_load_lds_dwordx4 v249, s[4:5] offset:2048\n\t"
      "global_load_lds_dwordx4 v250, s[4:5] offset:3072\n\t"
      "s_sub_u32 s20, s20, 1\n\t"
      "s_cselect_b32 s48, -7, 1\n\t"
      "s_cselect_b32 s49, -1, 0\n\t"
      "s_mul_i32 s48, s48, 114688\n\t"
      "s_add_u32 s4, s4, s48\n\t"
      "s_addc_u32 s5, s5, s49\n\t"
      "s_nop 0\n\t"
      "v_add_u32_e32 v242, 65536, v242\n\t"
      "v_add_u32_e32 v251, 65536, v251\n\t"
      "v_add_u32_e32 v252, 65536, v252\n\t"
      "v_add_u32_e32 v253, 65536, v253\n\t"
      "ds_read_b64_tr_b16 v[64:65], v242\n\t"
      "ds_read_b64_tr_b16 v[66:67], v242 offset:512\n\t"
      "ds_read_b64_tr_b16 v[68:69], v251\n\t"
      "ds_read_b64_tr_b16 v[70:71], v251 offset:512\n\t"
      "ds_read_b64_tr_b16 v[72:73], v252\n\t"
      "ds_read_b64_tr_b16 v[74:75], v252 offset:512\n\t"
      "ds_read_b64_tr_b16 v[76:77], v253\n\t"
      "ds_read_b64_tr_b16 v[78:79], v253 offset:512\n\t"
      "s_waitcnt vmcnt(4)\n\t"
      "s_waitcnt lgkmcnt(8)\n\t"
      "s_add_u32 m0, s23, 32768\n\t"
      "s_nop 0\n\t"
      "global_load_lds_dwordx4 v241, s[4:5]\n\t"
      "global_load_lds_dwordx4 v248, s[4:5] offset:1024\n\t"
      "global_load_lds_dwordx4 v249, s[4:5] offset:2048\n\t"
      "global_load_lds_dwordx4 v250, s[4:5] offset:3072\n\t"
      "ds_read_b64_tr_b16 v[80:81], v242 offset:32768\n\t"
      "ds_read_b64_tr_b16 v[82:83], v242 offset:33280\n\t"
      "ds_read_b64_tr_b16 v[84:85], v251 offset:32768\n\t"
      "ds_read_b64_tr_b16 v[86:87], v251 offset:33280\n\t"
      "ds_read_b64_tr_b16 v[88:89], v252 offset:32768\n\t"
      "ds_read_b64_tr_b16 v[90:91], v252 offset:33280\n\t"
      "ds_read_b64_tr_b16 v[92:93], v253 offset:32768\n\t"
      "ds_read_b64_tr_b16 v[94:95], v253 offset:33280\n\t"
      "s_waitcnt vmcnt(4)\n\t"
      "v_add_u32_e32 v242, 4294901760, v242\n\t"
      "v_add_u32_e32 v251, 4294901760, v251\n\t"
      "v_add_u32_e32 v252, 4294901760, v252\n\t"
      "v_add_u32_e32 v253, 4294901760, v253\n\t"
      "ds_read_b64_tr_b16 v[96:97], v242\n\t"
      "ds_read_b64_tr_b16 v[98:99], v242 offset:512\n\t"
      "ds_read_b64_tr_b16 v[100:101], v251\n\t"
      "ds_read_b64_tr_b16 v[102:103], v251 offset:512\n\t"
      "ds_read_b64_tr_b16 v[104:105], v252\n\t"
      "ds_read_b64_tr_b16 v[106:107], v252 offset:512\n\t"
      "ds_read_b64_tr_b16 v[108:109], v253\n\t"
      "ds_read_b64_tr_b16 v[110:111], v253 offset:512\n\t"
      "s_waitcnt vmcnt(0)\n\t"
      "ds_read_b64_tr_b16 v[112:113], v242 offset:32768\n\t"
      "ds_read_b64_tr_b16 v[114:115], v242 offset:33280\n\t"
      "ds_read_b64_tr_b16 v[116:117], v251 offset:32768\n\t"
      "ds_read_b64_tr_b16 v[118:119], v251 offset:33280\n\t"
      "ds_read_b64_tr_b16 v[120:121], v252 offset:32768\n\t"
      "ds_read_b64_tr_b16 v[122:123], v252 offset:33280\n\t"
      "ds_read_b64_tr_b16 v[124:125], v253 offset:32768\n\t"
      "ds_read_b64_tr_b16 v[126:127], v253 offset:33280\n\t"
      "s_mov_b64 exec, 0\n\t"
      "s_cmpk_eq_u32 s23, 0x0\n\t"
      "s_cbranch_scc1 .LhopD0\n\t"
      "s_cmpk_eq_u32 s23, 0x1000\n\t"
      "s_cbranch_scc1 .LhopD1\n\t"
      "s_cmpk_eq_u32 s23, 0x2000\n\t"
      "s_cbranch_scc1 .LhopD2\n\t"
      "s_cmpk_eq_u32 s23, 0x3000\n\t"
      "s_cbranch_scc1 .LhopD3\n\t"
      "s_cmpk_eq_u32 s23, 0x4000\n\t"
      "s_cbranch_scc1 .LhopD4\n\t"
      "s_cmpk_eq_u32 s23, 0x5000\n\t"
      "s_cbranch_scc1 .LhopD5\n\t"
      "s_cmpk_eq_u32 s23, 0x6000\n\t"
      "s_cbranch_scc1 .LhopD6\n\t"
      "s_cmpk_eq_u32 s23, 0x7000\n\t"
      "s_cbranch_scc1 .LbackD\n\t"
      ".LbackD:\n\t"
      "s_mov_b64 exec, -1\n\t"
      "s_waitcnt lgkmcnt(0)\n\t"
      "s_barrier\n\t"
      "s_mov_b32 s51, 0\n\t"
      "s_branch PRE_%=\n\t"
      "PRL_%=:\n\t"
      "s_sleep 4\n\t"
      "s_add_u32 s51, s51, 1\n\t"
      "s_cmp_lt_u32 s51, 262144\n\t"
      "s_cbranch_scc0 PRX_%=\n\t"
      "PRE_%=:\n\t"
      "s_mov_b64 exec, 65535\n\t"
      "s_bitcmp1_b32 s16, 4\n\t"
      "s_cbranch_scc1 PM_%=\n\t"
      "global_load_dwordx2 v[232:233], v247, s[12:13] sc1\n\t"
      "s_waitcnt vmcnt(0)\n\t"
      "PM_%=:\n\t"
      "s_cbranch_scc0 PN_%=\n\t"
      "v_mov_b64_e32 v[232:233], 0\n\t"
      "global_atomic_or_x2 v[232:233], v247, v[232:233], s[12:13] sc0 sc1\n\t"
      "s_waitcnt vmcnt(0)\n\t"
      "PN_%=:\n\t"
      "v_subrev_u32_e32 v232, s14, v232\n\t"
      "v_cmp_ne_u32_e64 s[48:49], s15, v233\n\t"
      "v_subrev_u32_e32 v232, 0, v232\n\t"
      "v_cmp_lt_u32_e32 vcc, 7, v232\n\t"
      "s_mov_b64 exec, -1\n\t"
      "s_or_b64 vcc, vcc, s[48:49]\n\t"
      "s_cbranch_vccnz PRL_%=\n\t"
      "PRX_%=:\n\t"
      "PRY_%=:\n\t"
      "global_load_dwordx4 v[128:131], v176, s[52:53] sc1\n\t"
      "global_load_dwordx4 v[132:135], v177, s[52:53] sc1\n\t"
      "global_load_dwordx4 v[136:139], v178, s[52:53] sc1\n\t"
      "global_load_dwordx4 v[140:143], v179, s[52:53] sc1\n\t"
      "global_load_dwordx4 v[144:147], v180, s[52:53] sc1\n\t"
      "global_load_dwordx4 v[148:151], v181, s[52:53] sc1\n\t"
      "global_load_dwordx4 v[152:155], v182, s[52:53] sc1\n\t"
      "global_load_dwordx4 v[156:159], v183, s[52:53] sc1\n\t"
      "s_nop 7\n\t"
      "s_cmp_ge_u32 s17, 65536\n\t"
      "s_cbranch_scc1 GQ_%=\n\t"
      ".p2align 6\n\t"
      "s_setprio 1\n\t"
      "v_mov_b32_e32 v232, s14\n\t"
      "v_mov_b32_e32 v233, s15\n\t"
      "v_lshl_add_u32 v247, v240, 10, s22\n\t"
      "s_waitcnt vmcnt(7)\n\t"
      "v_max3_u32 v234, v128, v129, v130\n\t"
      "v_max_u32_e32 v234, v234, v131\n\t"
      "v_cmp_eq_u32_e32 vcc, -1, v234\n\t"
      "s_cbranch_vccnz FP0R_%=\n\t"
      "FP0B_%=:\n\t"
      "v_mfma_f32_16x16x32_bf16 v[184:187], v[0:3], v[128:131], 0\n\t"
      "s_waitcnt vmcnt(6)\n\t"
      "v_max3_u32 v234, v132, v133, v134\n\t"
      "v_max_u32_e32 v234, v234, v135\n\t"
      "v_cmp_eq_u32_e32 vcc, -1, v234\n\t"
      "s_cbranch_vccnz FP1R_%=\n\t"
      "FP1B_%=:\n\t"
      "v_mfma_f32_16x16x32_bf16 v[184:187], v[16:19], v[132:135], v[184:187]\n\t"
      "v_mfma_f32_16x16x32_bf16 v[188:191], v[4:7], v[128:131], 0\n\t"
      "v_mfma_f32_16x16x32_bf16 v[188:191], v[20:23], v[132:135], v[188:191]\n\t"
      "v_mfma_f32_16x16x32_bf16 v[192:195], v[8:11], v[128:131], 0\n\t"
      "v_mfma_f32_16x16x32_bf16 v[192:195], v[24:27], v[132:135], v[192:195]\n\t"
      "v_mfma_f32_16x16x32_bf16 v[196:199], v[12:15], v[128:131], 0\n\t"
      "global_load_dwordx4 v[128:131], v176, s[54:55] sc1\n\t"
      "v_mfma_f32_16x16x32_bf16 v[196:199], v[28:31], v[132:135], v[196:199]\n\t"
      "global_load_dwordx4 v[132:135], v177, s[54:55] sc1\n\t"
      "s_waitcnt vmcnt(7)\n\t"
      "v_max3_u32 v234, v136, v137, v138\n\t"
      "v_max_u32_e32 v234, v234, v139\n\t"
      "v_cmp_eq_u32_e32 vcc, -1, v234\n\t"
      "s_cbranch_vccnz FP2R_%=\n\t"
      "FP2B_%=:\n\t"
      "v_mfma_f32_16x16x32_bf16 v[184:187], v[32:35], v[136:139], v[184:187]\n\t"
      "s_waitcnt vmcnt(6)\n\t"
      "v_max3_u32 v234, v140, v141, v142\n\t"
      "v_max_u32_e32 v234, v234, v143\n\t"
      "v_cmp_eq_u32_e32 vcc, -1, v234\n\t"
      "s_cbranch_vccnz FP3R_%=\n\t"
      "FP3B_%=:\n\t"
      "v_mfma_f32_16x16x32_bf16 v[184:187], v[48:51], v[140:143], v[184:187]\n\t"
      "v_mfma_f32_16x16x32_bf16 v[188:191], v[36:39], v[136:139], v[188:191]\n\t"
      "v_mfma_f32_16x16x32_bf16 v[188:191], v[52:55], v[140:143], v[188:191]\n\t"
      "v_mfma_f32_16x16x32_bf16 v[192:195], v[40:43], v[136:139], v[192:195]\n\t"
      "v_mfma_f32_16x16x32_bf16 v[192:195], v[56:59], v[140:143], v[192:195]\n\t"
      "v_mfma_f32_16x16x32_bf16 v[196:199], v[44:47], v[136:139], v[196:199]\n\t"
      "global_load_dwordx4 v[136:139], v178, s[54:55] sc1\n\t"
      "v_mfma_f32_16x16x32_bf16 v[196:199], v[60:63], v[140:143], v[196:199]\n\t"
      "global_load_dwordx4 v[140:143], v179, s[54:55] sc1\n\t"
      "s_waitcnt vmcnt(7)\n\t"
      "v_max3_u32 v234, v144, v145, v146\n\t"
      "v_max_u32_e32 v234, v234, v147\n\t"
      "v_cmp_eq_u32_e32 vcc, -1, v234\n\t"
      "s_cbranch_vccnz FP4R_%=\n\t"
      "FP4B_%=:\n\t"
      "v_mfma_f32_16x16x32_bf16 v[184:187], v[64:67], v[144:147], v[184:187]\n\t"
      "s_waitcnt vmcnt(6)\n\t"
      "v_max3_u32 v234, v148, v149, v150\n\t"
      "v_max_u32_e32 v234, v234, v151\n\t"
      "v_cmp_eq_u32_e32 vcc, -1, v234\n\t"
      "s_cbranch_vccnz FP5R_%=\n\t"
      "FP5B_%=:\n\t"
      "v_mfma_f32_16x16x32_bf16 v[184:187], v[80:83], v[148:151], v[184:187]\n\t"
      "v_mfma_f32_16x16x32_bf16 v[188:191], v[68:71], v[144:147], v[188:191]\n\t"
      "v_mfma_f32_16x16x32_bf16 v[188:191], v[84:87], v[148:151], v[188:191]\n\t"
      "v_mfma_f32_16x16x32_bf16 v[192:195], v[72:75], v[144:147], v[192:195]\n\t"
      "v_mfma_f32_16x16x32_bf16 v[192:195], v[88:91], v[148:151], v[192:195]\n\t"
      "v_mfma_f32_16x16x32_bf16 v[196:199], v[76:79], v[144:147], v[196:199]\n\t"
      "global_load_dwordx4 v[144:147], v180, s[54:55] sc1\n\t"
      "v_mfma_f32_16x16x32_bf16 v[196:199], v[92:95], v[148:151], v[196:199]\n\t"
      "global_load_dwordx4 v[148:151], v181, s[54:55] sc1\n\t"
      "s_waitcnt vmcnt(7)\n\t"
      "v_max3_u32 v234, v152, v153, v154\n\t"
      "v_max_u32_e32 v234, v234, v155\n\t"
      "v_cmp_eq_u32_e32 vcc, -1, v234\n\t"
      "s_cbranch_vccnz FP6R_%=\n\t"
      "FP6B_%=:\n\t"
      "v_mfma_f32_16x16x32_bf16 v[184:187], v[96:99], v[152:155], v[184:187]\n\t"
      "s_waitcnt vmcnt(6)\n\t"
      "v_max3_u32 v234, v156, v157, v158\n\t"
      "v_max_u32_e32 v234, v234, v159\n\t"
      "v_cmp_eq_u32_e32 vcc, -1, v234\n\t"
      "s_cbranch_vccnz FP7R_%=\n\t"
      "FP7B_%=:\n\t"
      "v_mfma_f32_16x16x32_bf16 v[184:187], v[112:115], v[156:159], v[184:187]\n\t"
      "v_mfma_f32_16x16x32_bf16 v[188:191], v[100:103], v[152:155], v[188:191]\n\t"
      "v_mfma_f32_16x16x32_bf16 v[188:191], v[116:119], v[156:159], v[188:191]\n\t"
      "v_mfma_f32_16x16x32_bf16 v[192:195], v[104:107], v[152:155], v[192:195]\n\t"
      "v_mfma_f32_16x16x32_bf16 v[192:195], v[120:123], v[156:159], v[192:195]\n\t"
      "s_nop 3\n\t"
      "ds_write_b128 v243, v[184:187]\n\t"
      "v_mfma_f32_16x16x32_bf16 v[196:199], v[108:111], v[152:155], v[196:199]\n\t"
      "global_load_dwordx4 v[152:155], v182, s[54:55] sc1\n\t"
      "v_mfma_f32_16x16x32_bf16 v[196:199], v[124:127], v[156:159], v[196:199]\n\t"
      "global_load_dwordx4 v[156:159], v183, s[54:55] sc1\n\t"
      "ds_write_b128 v164, v[188:191]\n\t"
      "ds_write_b128 v165, v[192:195]\n\t"
      "s_nop 4\n\t"
      "ds_write_b128 v166, v[196:199]\n\t"
      "s_waitcnt lgkmcnt(0)\n\t"
      "s_barrier\n\t"
      "ds_read_b128 v[216:219], v244\n\t"
      "ds_read_b128 v[220:223], v244 offset:4096\n\t"
      "ds_read_b128 v[224:227], v244 offset:8192\n\t"
      "ds_read_b128 v[228:231], v244 offset:12288\n\t"
      "ds_read_b128 v[184:187], v244 offset:16384\n\t"
      "ds_read_b128 v[188:191], v244 offset:20480\n\t"
      "ds_read_b128 v[192:195], v244 offset:24576\n\t"
      "ds_read_b128 v[196:199], v244 offset:28672\n\t"
      "s_waitcnt lgkmcnt(6)\n\t"
      "v_add_f32_e32 v216, v216, v220\n\t"
      "v_add_f32_e32 v217, v217, v221\n\t"
      "v_add_f32_e32 v218, v218, v222\n\t"
      "v_add_f32_e32 v219, v219, v223\n\t"
      "s_waitcnt lgkmcnt(4)\n\t"
      "v_add_f32_e32 v224, v224, v228\n\t"
      "v_add_f32_e32 v225, v225, v229\n\t"
      "v_add_f32_e32 v226, v226, v230\n\t"
      "v_add_f32_e32 v227, v227, v231\n\t"
      "s_waitcnt lgkmcnt(2)\n\t"
      "v_add_f32_e32 v184, v184, v188\n\t"
      "v_add_f32_e32 v185, v185, v189\n\t"
      "v_add_f32_e32 v186, v186, v190\n\t"
      "v_add_f32_e32 v187, v187, v191\n\t"
      "v_add_f32_e32 v216, v216, v224\n\t"
      "v_add_f32_e32 v217, v217, v225\n\t"
      "v_add_f32_e32 v218, v218, v226\n\t"
      "v_add_f32_e32 v219, v219, v227\n\t"
      "s_waitcnt lgkmcnt(0)\n\t"
      "v_add_f32_e32 v192, v192, v196\n\t"
      "v_add_f32_e32 v193, v193, v197\n\t"
      "v_add_f32_e32 v194, v194, v198\n\t"
      "v_add_f32_e32 v195, v195, v199\n\t"
      "v_add_f32_e32 v184, v184, v192\n\t"
      "v_add_f32_e32 v185, v185, v193\n\t"
      "v_add_f32_e32 v186, v186, v194\n\t"
      "v_add_f32_e32 v187, v187, v195\n\t"
      "v_add_f32_e32 v216, v216, v184\n\t"
      "s_waitcnt vmcnt(7)\n\t"
      "v_mfma_f32_16x16x32_bf16 v[200:203], v[0:3], v[128:131], 0\n\t"
      "v_add_f32_e32 v217, v217, v185\n\t"
      "s_waitcnt vmcnt(6)\n\t"
      "v_mfma_f32_16x16x32_bf16 v[200:203], v[16:19], v[132:135], v[200:203]\n\t"
      "v_add_f32_e32 v218, v218, v186\n\t"
      "v_add_f32_e32 v219, v219, v187\n\t"
      "v_mfma_f32_16x16x32_bf16 v[204:207], v[4:7], v[128:131], 0\n\t"
      "v_mul_f32_e32 v220, v216, v216\n\t"
      "v_mul_f32_e32 v221, v217, v217\n\t"
      "v_mfma_f32_16x16x32_bf16 v[204:207], v[20:23], v[132:135], v[204:207]\n\t"
      "v_mul_f32_e32 v222, v218, v218\n\t"
      "v_mul_f32_e32 v223, v219, v219\n\t"
      "v_mfma_f32_16x16x32_bf16 v[208:211], v[8:11], v[128:131], 0\n\t"
      "v_fmaak_f32 v220, v235, v220, 0xc0135761\n\t"
      "v_fmaak_f32 v221, v235, v221, 0xc0135761\n\t"
      "v_mfma_f32_16x16x32_bf16 v[208:211], v[24:27], v[132:135], v[208:211]\n\t"
      "v_fmaak_f32 v222, v235, v222, 0xc0135761\n\t"
      "v_fmaak_f32 v223, v235, v223, 0xc0135761\n\t"
      "v_mfma_f32_16x16x32_bf16 v[212:215], v[12:15], v[128:131], 0\n\t"
      "global_load_dwordx4 v[128:131], v176, s[56:57] sc1\n\t"
      "v_mul_f32_e32 v220, v216, v220\n\t"
      "v_mfma_f32_16x16x32_bf16 v[212:215], v[28:31], v[132:135], v[212:215]\n\t"
      "global_load_dwordx4 v[132:135], v177, s[56:57] sc1\n\t"
      "s_waitcnt vmcnt(7)\n\t"
      "v_mfma_f32_16x16x32_bf16 v[200:203], v[32:35], v[136:139], v[200:203]\n\t"
      "v_mul_f32_e32 v221, v217, v221\n\t"
      "s_waitcnt vmcnt(6)\n\t"
      "v_mfma_f32_16x16x32_bf16 v[200:203], v[48:51], v[140:143], v[200:203]\n\t"
      "v_mul_f32_e32 v222, v218, v222\n\t"
      "v_mul_f32_e32 v223, v219, v223\n\t"
      "v_mfma_f32_16x16x32_bf16 v[204:207], v[36:39], v[136:139], v[204:207]\n\t"
      "v_exp_f32_e32 v220, v220\n\t"
      "v_mfma_f32_16x16x32_bf16 v[204:207], v[52:55], v[140:143], v[204:207]\n\t"
      "v_exp_f32_e32 v221, v221\n\t"
      "v_mfma_f32_16x16x32_bf16 v[208:211], v[40:43], v[136:139], v[208:211]\n\t"
      "v_exp_f32_e32 v222, v222\n\t"
      "v_mfma_f32_16x16x32_bf16 v[208:211], v[56:59], v[140:143], v[208:211]\n\t"
      "v_exp_f32_e32 v223, v223\n\t"
      "v_mfma_f32_16x16x32_bf16 v[212:215], v[44:47], v[136:139], v[212:215]\n\t"
      "global_load_dwordx4 v[136:139], v178, s[56:57] sc1\n\t"
      "v_add_f32_e32 v220, 1.0, v220\n\t"
      "v_mfma_f32_16x16x32_bf16 v[212:215], v[60:63], v[140:143], v[212:215]\n\t"
      "global_load_dwordx4 v[140:143], v179, s[56:57] sc1\n\t"
      "s_waitcnt vmcnt(7)\n\t"
      "v_mfma_f32_16x16x32_bf16 v[200:203], v[64:67], v[144:147], v[200:203]\n\t"
      "v_add_f32_e32 v221, 1.0, v221\n\t"
      "s_waitcnt vmcnt(6)\n\t"
      "v_mfma_f32_16x16x32_bf16 v[200:203], v[80:83], v[148:151], v[200:203]\n\t"
      "v_add_f32_e32 v222, 1.0, v222\n\t"
      "v_add_f32_e32 v223, 1.0, v223\n\t"
      "v_mfma_f32_16x16x32_bf16 v[204:207], v[68:71], v[144:147], v[204:207]\n\t"
      "v_rcp_f32_e32 v220, v220\n\t"
      "v_mfma_f32_16x16x32_bf16 v[204:207], v[84:87], v[148:151], v[204:207]\n\t"
      "v_rcp_f32_e32 v221, v221\n\t"
      "v_mfma_f32_16x16x32_bf16 v[208:211], v[72:75], v[144:147], v[208:211]\n\t"
      "v_rcp_f32_e32 v222, v222\n\t"
      "v_mfma_f32_16x16x32_bf16 v[208:211], v[88:91], v[148:151], v[208:211]\n\t"
      "v_rcp_f32_e32 v223, v223\n\t"
      "v_mfma_f32_16x16x32_bf16 v[212:215], v[76:79], v[144:147], v[212:215]\n\t"
      "global_load_dwordx4 v[144:147], v180, s[56:57] sc1\n\t"
      "v_mul_f32_e32 v216, v216, v220\n\t"
      "v_mfma_f32_16x16x32_bf16 v[212:215], v[92:95], v[148:151], v[212:215]\n\t"
      "global_load_dwordx4 v[148:151], v181, s[56:57] sc1\n\t"
      "s_waitcnt vmcnt(7)\n\t"
      "v_mfma_f32_16x16x32_bf16 v[200:203], v[96:99], v[152:155], v[200:203]\n\t"
      "v_mul_f32_e32 v217, v217, v221\n\t"
      "s_waitcnt vmcnt(6)\n\t"
      "v_mfma_f32_16x16x32_bf16 v[200:203], v[112:115], v[156:159], v[200:203]\n\t"
      "v_mul_f32_e32 v218, v218, v222\n\t"
      "v_mul_f32_e32 v219, v219, v223\n\t"
      "v_mfma_f32_16x16x32_bf16 v[204:207], v[100:103], v[152:155], v[204:207]\n\t"
      "v_cvt_pk_bf16_f32 v220, v216, v217\n\t"
      "v_cvt_pk_bf16_f32 v221, v218, v219\n\t"
      "v_mfma_f32_16x16x32_bf16 v[204:207], v[116:119], v[156:159], v[204:207]\n\t"
      "s_nop 1\n\t"
      "v_cmp_u_f32_e32 vcc, v200, v200\n\t"
      "v_min_u32_e32 v220, -2, v220\n\t"
      "v_mfma_f32_16x16x32_bf16 v[208:211], v[104:107], v[152:155], v[208:211]\n\t"
      "v_min_u32_e32 v221, -2, v221\n\t"
      "global_store_dwordx2 v245, v[220:221], s[8:9] sc1\n\t"
      "s_add_u32 s8, s8, 131072\n\t"
      "s_addc_u32 s9, s9, 0\n\t"
      "v_mfma_f32_16x16x32_bf16 v[208:211], v[120:123], v[156:159], v[208:211]\n\t"
      "s_cbranch_vccnz NP1R_%=\n\t"
      "NP1B_%=:\n\t"
      "ds_write_b128 v243, v[200:203] offset:32768\n\t"
      "v_mfma_f32_16x16x32_bf16 v[212:215], v[108:111], v[152:155], v[212:215]\n\t"
      "global_load_dwordx4 v[152:155], v182, s[56:57] sc1\n\t"
      "v_mfma_f32_16x16x32_bf16 v[212:215], v[124:127], v[156:159], v[212:215]\n\t"
      "global_load_dwordx4 v[156:159], v183, s[56:57] sc1\n\t"
      "ds_write_b128 v164, v[204:207] offset:32768\n\t"
      "s_nop 0\n\t"
      "ds_write_b128 v165, v[208:211] offset:32768\n\t"
      "s_nop 3\n\t"
      "ds_write_b128 v166, v[212:215] offset:32768\n\t"
      "s_waitcnt lgkmcnt(0)\n\t"
      "s_barrier\n\t"
      "ds_read_b128 v[216:219], v244 offset:32768\n\t"
      "ds_read_b128 v[220:223], v244 offset:36864\n\t"
      "ds_read_b128 v[224:227], v244 offset:40960\n\t"
      "ds_read_b128 v[228:231], v244 offset:45056\n\t"
      "ds_read_b128 v[200:203], v244 offset:49152\n\t"
      "ds_read_b128 v[204:207], v244 offset:53248\n\t"
      "ds_read_b128 v[208:211], v244 offset:57344\n\t"
      "ds_read_b128 v[212:215], v244 offset:61440\n\t"
      "s_waitcnt lgkmcnt(6)\n\t"
      "v_add_f32_e32 v216, v216, v220\n\t"
      "v_add_f32_e32 v217, v217, v221\n\t"
      "v_add_f32_e32 v218, v218, v222\n\t"
      "v_add_f32_e32 v219, v219, v223\n\t"
      "s_waitcnt lgkmcnt(4)\n\t"
      "v_add_f32_e32 v224, v224, v228\n\t"
      "v_add_f32_e32 v225, v225, v229\n\t"
      "v_add_f32_e32 v226, v226, v230\n\t"
      "v_add_f32_e32 v227, v227, v231\n\t"
      "s_waitcnt lgkmcnt(2)\n\t"
      "v_add_f32_e32 v200, v200, v204\n\t"
      "v_add_f32_e32 v201, v201, v205\n\t"
      "v_add_f32_e32 v202, v202, v206\n\t"
      "v_add_f32_e32 v203, v203, v207\n\t"
      "v_add_f32_e32 v216, v216, v224\n\t"
      "v_add_f32_e32 v217, v217, v225\n\t"
      "v_add_f32_e32 v218, v218, v226\n\t"
      "v_add_f32_e32 v219, v219, v227\n\t"
      "s_waitcnt lgkmcnt(0)\n\t"
      "v_add_f32_e32 v208, v208, v212\n\t"
      "v_add_f32_e32 v209, v209, v213\n\t"
      "v_add_f32_e32 v210, v210, v214\n\t"
      "v_add_f32_e32 v211, v211, v215\n\t"
      "v_add_f32_e32 v200, v200, v208\n\t"
      "v_add_f32_e32 v201, v201, v209\n\t"
      "v_add_f32_e32 v202, v202, v210\n\t"
      "v_add_f32_e32 v203, v203, v211\n\t"
      "v_add_f32_e32 v216, v216, v200\n\t"
      "s_waitcnt vmcnt(8)\n\t"
      "v_mfma_f32_16x16x32_bf16 v[184:187], v[0:3], v[128:131], 0\n\t"
      "v_add_f32_e32 v217, v217, v201\n\t"
      "s_cmp_eq_u32 s17, 0\n\t"
      "s_cbranch_scc0 HI0_%=\n\t"
      "s_mov_b64 exec, 0xf\n\t"
      "global_store_dwordx2 v247, v[232:233], s[12:13] sc1\n\t"
      "s_mov_b64 exec, -1\n\t"
      "s_nop 0\n\t"
      "v_add_u32_e32 v232, 6, v232\n\t"
      "HI0_%=:\n\t"
      "s_waitcnt vmcnt(7)\n\t"
      "v_mfma_f32_16x16x32_bf16 v[184:187], v[16:19], v[132:135], v[184:187]\n\t"
      "v_add_f32_e32 v218, v218, v202\n\t"
      "v_add_f32_e32 v219, v219, v203\n\t"
      "v_mfma_f32_16x16x32_bf16 v[188:191], v[4:7], v[128:131], 0\n\t"
      "v_mul_f32_e32 v220, v216, v216\n\t"
      "v_mul_f32_e32 v221, v217, v217\n\t"
      "v_mfma_f32_16x16x32_bf16 v[188:191], v[20:23], v[132:135], v[188:191]\n\t"
      "v_mul_f32_e32 v222, v218, v218\n\t"
      "v_mul_f32_e32 v223, v219, v219\n\t"
      "v_mfma_f32_16x16x32_bf16 v[192:195], v[8:11], v[128:131], 0\n\t"
      "v_fmaak_f32 v220, v235, v220, 0xc0135761\n\t"
      "v_fmaak_f32 v221, v235, v221, 0xc0135761\n\t"
      "v_mfma_f32_16x16x32_bf16 v[192:195], v[24:27], v[132:135], v[192:195]\n\t"
      "v_fmaak_f32 v222, v235, v222, 0xc0135761\n\t"
      "v_fmaak_f32 v223, v235, v223, 0xc0135761\n\t"
      "v_mfma_f32_16x16x32_bf16 v[196:199], v[12:15], v[128:131], 0\n\t"
      "global_load_dwordx4 v[128:131], v176, s[58:59] sc1\n\t"
      "v_mul_f32_e32 v220, v216, v220\n\t"
      "v_mfma_f32_16x16x32_bf16 v[196:199], v[28:31], v[132:135], v[196:199]\n\t"
      "global_load_dwordx4 v[132:135], v177, s[58:59] sc1\n\t"
      "s_waitcnt vmcnt(8)\n\t"
      "v_mfma_f32_16x16x32_bf16 v[184:187], v[32:35], v[136:139], v[184:187]\n\t"
      "v_mul_f32_e32 v221, v217, v221\n\t"
      "s_waitcnt vmcnt(7)\n\t"
      "v_mfma_f32_16x16x32_bf16 v[184:187], v[48:51], v[140:143], v[184:187]\n\t"
      "v_mul_f32_e32 v222, v218, v222\n\t"
      "v_mul_f32_e32 v223, v219, v223\n\t"
      "v_mfma_f32_16x16x32_bf16 v[188:191], v[36:39], v[136:139], v[188:191]\n\t"
      "v_exp_f32_e32 v220, v220\n\t"
      "v_mfma_f32_16x16x32_bf16 v[188:191], v[52:55], v[140:143], v[188:191]\n\t"
      "v_exp_f32_e32 v221, v221\n\t"
      "v_mfma_f32_16x16x32_bf16 v[192:195], v[40:43], v[136:139], v[192:195]\n\t"
      "v_exp_f32_e32 v222, v222\n\t"
      "v_mfma_f32_16x16x32_bf16 v[192:195], v[56:59], v[140:143], v[192:195]\n\t"
      "v_exp_f32_e32 v223, v223\n\t"
      "v_mfma_f32_16x16x32_bf16 v[196:199], v[44:47], v[136:139], v[196:199]\n\t"
      "global_load_dwordx4 v[136:139], v178, s[58:59] sc1\n\t"
      "v_add_f32_e32 v220, 1.0, v220\n\t"
      "v_mfma_f32_16x16x32_bf16 v[196:199], v[60:63], v[140:143], v[196:199]\n\t"
      "global_load_dwordx4 v[140:143], v179, s[58:59] sc1\n\t"
      "s_waitcnt vmcnt(8)\n\t"
      "v_mfma_f32_16x16x32_bf16 v[184:187], v[64:67], v[144:147], v[184:187]\n\t"
      "v_add_f32_e32 v221, 1.0, v221\n\t"
      "s_waitcnt vmcnt(7)\n\t"
      "v_mfma_f32_16x16x32_bf16 v[184:187], v[80:83], v[148:151], v[184:187]\n\t"
      "v_add_f32_e32 v222, 1.0, v222\n\t"
      "v_add_f32_e32 v223, 1.0, v223\n\t"
      "v_mfma_f32_16x16x32_bf16 v[188:191], v[68:71], v[144:147], v[188:191]\n\t"
      "v_rcp_f32_e32 v220, v220\n\t"
      "v_mfma_f32_16x16x32_bf16 v[188:191], v[84:87], v[148:151], v[188:191]\n\t"
      "v_rcp_f32_e32 v221, v221\n\t"
      "v_mfma_f32_16x16x32_bf16 v[192:195], v[72:75], v[144:147], v[192:195]\n\t"
      "v_rcp_f32_e32 v222, v222\n\t"
      "v_mfma_f32_16x16x32_bf16 v[192:195], v[88:91], v[148:151], v[192:195]\n\t"
      "v_rcp_f32_e32 v223, v223\n\t"
      "v_mfma_f32_16x16x32_bf16 v[196:199], v[76:79], v[144:147], v[196:199]\n\t"
      "global_load_dwordx4 v[144:147], v180, s[58:59] sc1\n\t"
      "v_mul_f32_e32 v216, v216, v220\n\t"
      "v_mfma_f32_16x16x32_bf16 v[196:199], v[92:95], v[148:151], v[196:199]\n\t"
      "global_load_dwordx4 v[148:151], v181, s[58:59] sc1\n\t"
      "s_waitcnt vmcnt(7)\n\t"
      "v_mfma_f32_16x16x32_bf16 v[184:187], v[96:99], v[152:155], v[184:187]\n\t"
      "v_mul_f32_e32 v217, v217, v221\n\t"
      "s_waitcnt vmcnt(6)\n\t"
      "v_mfma_f32_16x16x32_bf16 v[184:187], v[112:115], v[156:159], v[184:187]\n\t"
      "v_mul_f32_e32 v218, v218, v222\n\t"
      "v_mul_f32_e32 v219, v219, v223\n\t"
      "v_mfma_f32_16x16x32_bf16 v[188:191], v[100:103], v[152:155], v[188:191]\n\t"
      "v_cvt_pk_bf16_f32 v220, v216, v217\n\t"
      "v_cvt_pk_bf16_f32 v221, v218, v219\n\t"
      "v_mfma_f32_16x16x32_bf16 v[188:191], v[116:119], v[156:159], v[188:191]\n\t"
      "s_nop 1\n\t"
      "v_cmp_u_f32_e32 vcc, v184, v184\n\t"
      "v_min_u32_e32 v220, -2, v220\n\t"
      "v_mfma_f32_16x16x32_bf16 v[192:195], v[104:107], v[152:155], v[192:195]\n\t"
      "v_min_u32_e32 v221, -2, v221\n\t"
      "global_store_dwordx2 v245, v[220:221], s[8:9] sc1\n\t"
      "s_add_u32 s8, s8, 131072\n\t"
      "s_addc_u32 s9, s9, 0\n\t"
      "v_mfma_f32_16x16x32_bf16 v[192:195], v[120:123], v[156:159], v[192:195]\n\t"
      "s_cbranch_vccnz NP2R_%=\n\t"
      "NP2B_%=:\n\t"
      "ds_write_b128 v243, v[184:187]\n\t"
      "v_mfma_f32_16x16x32_bf16 v[196:199], v[108:111], v[152:155], v[196:199]\n\t"
      "global_load_dwordx4 v[152:155], v182, s[58:59] sc1\n\t"
      "v_mfma_f32_16x16x32_bf16 v[196:199], v[124:127], v[156:159], v[196:199]\n\t"
      "global_load_dwordx4 v[156:159], v183, s[58:59] sc1\n\t"
      "ds_write_b128 v164, v[188:191]\n\t"
      "s_nop 0\n\t"
      "ds_write_b128 v165, v[192:195]\n\t"
      "s_nop 3\n\t"
      "ds_write_b128 v166, v[196:199]\n\t"
      "s_waitcnt lgkmcnt(0)\n\t"
      "s_barrier\n\t"
      "ds_read_b128 v[216:219], v244\n\t"
      "ds_read_b128 v[220:223], v244 offset:4096\n\t"
      "ds_read_b128 v[224:227], v244 offset:8192\n\t"
      "ds_read_b128 v[228:231], v244 offset:12288\n\t"
      "ds_read_b128 v[184:187], v244 offset:16384\n\t"
      "ds_read_b128 v[188:191], v244 offset:20480\n\t"
      "ds_read_b128 v[192:195], v244 offset:24576\n\t"
      "ds_read_b128 v[196:199], v244 offset:28672\n\t"
      "s_waitcnt lgkmcnt(6)\n\t"
      "v_add_f32_e32 v216, v216, v220\n\t"
      "v_add_f32_e32 v217, v217, v221\n\t"
      "v_add_f32_e32 v218, v218, v222\n\t"
      "v_add_f32_e32 v219, v219, v223\n\t"
      "s_waitcnt lgkmcnt(4)\n\t"
      "v_add_f32_e32 v224, v224, v228\n\t"
      "v_add_f32_e32 v225, v225, v229\n\t"
      "v_add_f32_e32 v226, v226, v230\n\t"
      "v_add_f32_e32 v227, v227, v231\n\t"
      "s_waitcnt lgkmcnt(2)\n\t"
      "v_add_f32_e32 v184, v184, v188\n\t"
      "v_add_f32_e32 v185, v185, v189\n\t"
      "v_add_f32_e32 v186, v186, v190\n\t"
      "v_add_f32_e32 v187, v187, v191\n\t"
      "v_add_f32_e32 v216, v216, v224\n\t"
      "v_add_f32_e32 v217, v217, v225\n\t"
      "v_add_f32_e32 v218, v218, v226\n\t"
      "v_add_f32_e32 v219, v219, v227\n\t"
      "s_waitcnt lgkmcnt(0)\n\t"
      "v_add_f32_e32 v192, v192, v196\n\t"
      "v_add_f32_e32 v193, v193, v197\n\t"
      "v_add_f32_e32 v194, v194, v198\n\t"
      "v_add_f32_e32 v195, v195, v199\n\t"
      "v_add_f32_e32 v184, v184, v192\n\t"
      "v_add_f32_e32 v185, v185, v193\n\t"
      "v_add_f32_e32 v186, v186, v194\n\t"
      "v_add_f32_e32 v187, v187, v195\n\t"
      "v_add_f32_e32 v216, v216, v184\n\t"
      "s_waitcnt vmcnt(8)\n\t"
      "v_mfma_f32_16x16x32_bf16 v[200:203], v[0:3], v[128:131], 0\n\t"
      "v_add_f32_e32 v217, v217, v185\n\t"
      "s_waitcnt vmcnt(7)\n\t"
      "v_mfma_f32_16x16x32_bf16 v[200:203], v[16:19], v[132:135], v[200:203]\n\t"
      "v_add_f32_e32 v218, v218, v186\n\t"
      "v_add_f32_e32 v219, v219, v187\n\t"
      "v_mfma_f32_16x16x32_bf16 v[204:207], v[4:7], v[128:131], 0\n\t"
      "v_mul_f32_e32 v220, v216, v216\n\t"
      "v_mul_f32_e32 v221, v217, v217\n\t"
      "v_mfma_f32_16x16x32_bf16 v[204:207], v[20:23], v[132:135], v[204:207]\n\t"
      "v_mul_f32_e32 v222, v218, v218\n\t"
      "v_mul_f32_e32 v223, v219, v219\n\t"
      "v_mfma_f32_16x16x32_bf16 v[208:211], v[8:11], v[128:131], 0\n\t"
      "v_fmaak_f32 v220, v235, v220, 0xc0135761\n\t"
      "v_fmaak_f32 v221, v235, v221, 0xc0135761\n\t"
      "v_mfma_f32_16x16x32_bf16 v[208:211], v[24:27], v[132:135], v[208:211]\n\t"
      "v_fmaak_f32 v222, v235, v222, 0xc0135761\n\t"
      "v_fmaak_f32 v223, v235, v223, 0xc0135761\n\t"
      "v_mfma_f32_16x16x32_bf16 v[212:215], v[12:15], v[128:131], 0\n\t"
      "global_load_dwordx4 v[128:131], v176, s[60:61] sc1\n\t"
      "v_mul_f32_e32 v220, v216, v220\n\t"
      "v_mfma_f32_16x16x32_bf16 v[212:215], v[28:31], v[132:135], v[212:215]\n\t"
      "global_load_dwordx4 v[132:135], v177, s[60:61] sc1\n\t"
      "s_waitcnt vmcnt(8)\n\t"
      "v_mfma_f32_16x16x32_bf16 v[200:203], v[32:35], v[136:139], v[200:203]\n\t"
      "v_mul_f32_e32 v221, v217, v221\n\t"
      "s_waitcnt vmcnt(7)\n\t"
      "v_mfma_f32_16x16x32_bf16 v[200:203], v[48:51], v[140:143], v[200:203]\n\t"
      "v_mul_f32_e32 v222, v218, v222\n\t"
      "v_mul_f32_e32 v223, v219, v223\n\t"
      "v_mfma_f32_16x16x32_bf16 v[204:207], v[36:39], v[136:139], v[204:207]\n\t"
      "v_exp_f32_e32 v220, v220\n\t"
      "v_mfma_f32_16x16x32_bf16 v[204:207], v[52:55], v[140:143], v[204:207]\n\t"
      "v_exp_f32_e32 v221, v221\n\t"
      "v_mfma_f32_16x16x32_bf16 v[208:211], v[40:43], v[136:139], v[208:211]\n\t"
      "v_exp_f32_e32 v222, v222\n\t"
      "v_mfma_f32_16x16x32_bf16 v[208:211], v[56:59], v[140:143], v[208:211]\n\t"
      "v_exp_f32_e32 v223, v223\n\t"
      "v_mfma_f32_16x16x32_bf16 v[212:215], v[44:47], v[136:139], v[212:215]\n\t"
      "global_load_dwordx4 v[136:139], v178, s[60:61] sc1\n\t"
      "v_add_f32_e32 v220, 1.0, v220\n\t"
      "v_mfma_f32_16x16x32_bf16 v[212:215], v[60:63], v[140:143], v[212:215]\n\t"
      "global_load_dwordx4 v[140:143], v179, s[60:61] sc1\n\t"
      "s_waitcnt vmcnt(8)\n\t"
      "v_mfma_f32_16x16x32_bf16 v[200:203], v[64:67], v[144:147], v[200:203]\n\t"
      "v_add_f32_e32 v221, 1.0, v221\n\t"
      "s_waitcnt vmcnt(7)\n\t"
      "v_mfma_f32_16x16x32_bf16 v[200:203], v[80:83], v[148:151], v[200:203]\n\t"
      "v_add_f32_e32 v222, 1.0, v222\n\t"
      "v_add_f32_e32 v223, 1.0, v223\n\t"
      "v_mfma_f32_16x16x32_bf16 v[204:207], v[68:71], v[144:147], v[204:207]\n\t"
      "v_rcp_f32_e32 v220, v220\n\t"
      "v_mfma_f32_16x16x32_bf16 v[204:207], v[84:87], v[148:151], v[204:207]\n\t"
      "v_rcp_f32_e32 v221, v221\n\t"
      "v_mfma_f32_16x16x32_bf16 v[208:211], v[72:75], v[144:147], v[208:211]\n\t"
      "v_rcp_f32_e32 v222, v222\n\t"
      "v_mfma_f32_16x16x32_bf16 v[208:211], v[88:91], v[148:151], v[208:211]\n\t"
      "v_rcp_f32_e32 v223, v223\n\t"
      "v_mfma_f32_16x16x32_bf16 v[212:215], v[76:79], v[144:147], v[212:215]\n\t"
      "global_load_dwordx4 v[144:147], v180, s[60:61] sc1\n\t"
      "v_mul_f32_e32 v216, v216, v220\n\t"
      "v_mfma_f32_16x16x32_bf16 v[212:215], v[92:95], v[148:151], v[212:215]\n\t"
      "global_load_dwordx4 v[148:151], v181, s[60:61] sc1\n\t"
      "s_waitcnt vmcnt(7)\n\t"
      "v_mfma_f32_16x16x32_bf16 v[200:203], v[96:99], v[152:155], v[200:203]\n\t"
      "v_mul_f32_e32 v217, v217, v221\n\t"
      "s_waitcnt vmcnt(6)\n\t"
      "v_mfma_f32_16x16x32_bf16 v[200:203], v[112:115], v[156:159], v[200:203]\n\t"
      "v_mul_f32_e32 v218, v218, v222\n\t"
      "v_mul_f32_e32 v219, v219, v223\n\t"
      "v_mfma_f32_16x16x32_bf16 v[204:207], v[100:103], v[152:155], v[204:207]\n\t"
      "v_cvt_pk_bf16_f32 v220, v216, v217\n\t"
      "v_cvt_pk_bf16_f32 v221, v218, v219\n\t"
      "v_mfma_f32_16x16x32_bf16 v[204:207], v[116:119], v[156:159], v[204:207]\n\t"
      "s_nop 1\n\t"
      "v_cmp_u_f32_e32 vcc, v200, v200\n\t"
      "v_min_u32_e32 v220, -2, v220\n\t"
      "v_mfma_f32_16x16x32_bf16 v[208:211], v[104:107], v[152:155], v[208:211]\n\t"
      "v_min_u32_e32 v221, -2, v221\n\t"
      "global_store_dwordx2 v245, v[220:221], s[8:9] sc1\n\t"
      "s_add_u32 s8, s8, 131072\n\t"
      "s_addc_u32 s9, s9, 0\n\t"
      "v_mfma_f32_16x16x32_bf16 v[208:211], v[120:123], v[156:159], v[208:211]\n\t"
      "s_cbranch_vccnz NP3R_%=\n\t"
      "NP3B_%=:\n\t"
      "ds_write_b128 v243, v[200:203] offset:32768\n\t"
      "v_mfma_f32_16x16x32_bf16 v[212:215], v[108:111], v[152:155], v[212:215]\n\t"
      "global_load_dwordx4 v[152:155], v182, s[60:61] sc1\n\t"
      "v_mfma_f32_16x16x32_bf16 v[212:215], v[124:127], v[156:159], v[212:215]\n\t"
      "global_load_dwordx4 v[156:159], v183, s[60:61] sc1\n\t"
      "ds_write_b128 v164, v[204:207] offset:32768\n\t"
      "s_nop 0\n\t"
      "ds_write_b128 v165, v[208:211] offset:32768\n\t"
      "s_nop 3\n\t"
      "ds_write_b128 v166, v[212:215] offset:32768\n\t"
      "s_waitcnt lgkmcnt(0)\n\t"
      "s_barrier\n\t"
      "ds_read_b128 v[216:219], v244 offset:32768\n\t"
      "ds_read_b128 v[220:223], v244 offset:36864\n\t"
      "ds_read_b128 v[224:227], v244 offset:40960\n\t"
      "ds_read_b128 v[228:231], v244 offset:45056\n\t"
      "ds_read_b128 v[200:203], v244 offset:49152\n\t"
      "ds_read_b128 v[204:207], v244 offset:53248\n\t"
      "ds_read_b128 v[208:211], v244 offset:57344\n\t"
      "ds_read_b128 v[212:215], v244 offset:61440\n\t"
      "s_waitcnt lgkmcnt(6)\n\t"
      "v_add_f32_e32 v216, v216, v220\n\t"
      "v_add_f32_e32 v217, v217, v221\n\t"
      "v_add_f32_e32 v218, v218, v222\n\t"
      "v_add_f32_e32 v219, v219, v223\n\t"
      "s_waitcnt lgkmcnt(4)\n\t"
      "v_add_f32_e32 v224, v224, v228\n\t"
      "v_add_f32_e32 v225, v225, v229\n\t"
      "v_add_f32_e32 v226, v226, v230\n\t"
      "v_add_f32_e32 v227, v227, v231\n\t"
      "s_waitcnt lgkmcnt(2)\n\t"
      "v_add_f32_e32 v200, v200, v204\n\t"
      "v_add_f32_e32 v201, v201, v205\n\t"
      "v_add_f32_e32 v202, v202, v206\n\t"
      "v_add_f32_e32 v203, v203, v207\n\t"
      "v_add_f32_e32 v216, v216, v224\n\t"
      "v_add_f32_e32 v217, v217, v225\n\t"
      "v_add_f32_e32 v218, v218, v226\n\t"
      "v_add_f32_e32 v219, v219, v227\n\t"
      "s_waitcnt lgkmcnt(0)\n\t"
      "v_add_f32_e32 v208, v208, v212\n\t"
      "v_add_f32_e32 v209, v209, v213\n\t"
      "v_add_f32_e32 v210, v210, v214\n\t"
      "v_add_f32_e32 v211, v211, v215\n\t"
      "v_add_f32_e32 v200, v200, v208\n\t"
      "v_add_f32_e32 v201, v201, v209\n\t"
      "v_add_f32_e32 v202, v202, v210\n\t"
      "v_add_f32_e32 v203, v203, v211\n\t"
      "v_add_f32_e32 v216, v216, v200\n\t"
      "s_waitcnt vmcnt(8)\n\t"
      "v_mfma_f32_16x16x32_bf16 v[184:187], v[0:3], v[128:131], 0\n\t"
      "v_add_f32_e32 v217, v217, v201\n\t"
      "s_waitcnt vmcnt(7)\n\t"
      "v_mfma_f32_16x16x32_bf16 v[184:187], v[16:19], v[132:135], v[184:187]\n\t"
      "v_add_f32_e32 v218, v218, v202\n\t"
      "v_add_f32_e32 v219, v219, v203\n\t"
      "v_mfma_f32_16x16x32_bf16 v[188:191], v[4:7], v[128:131], 0\n\t"
      "v_mul_f32_e32 v220, v216, v216\n\t"
      "v_mul_f32_e32 v221, v217, v217\n\t"
      "v_mfma_f32_16x16x32_bf16 v[188:191], v[20:23], v[132:135], v[188:191]\n\t"
      "v_mul_f32_e32 v222, v218, v218\n\t"
      "v_mul_f32_e32 v223, v219, v219\n\t"
      "v_mfma_f32_16x16x32_bf16 v[192:195], v[8:11], v[128:131], 0\n\t"
      "v_fmaak_f32 v220, v235, v220, 0xc0135761\n\t"
      ".LhopD2:\n\t"
      "s_cbranch_execz .LbackD\n\t"
      "v_fmaak_f32 v221, v235, v221, 0xc0135761\n\t"
      "v_mfma_f32_16x16x32_bf16 v[192:195], v[24:27], v[132:135], v[192:195]\n\t"
      "v_fmaak_f32 v222, v235, v222, 0xc0135761\n\t"
      "v_fmaak_f32 v223, v235, v223, 0xc0135761\n\t"
      "v_mfma_f32_16x16x32_bf16 v[196:199], v[12:15], v[128:131], 0\n\t"
      "global_load_dwordx4 v[128:131], v176, s[62:63] sc1\n\t"
      "v_mul_f32_e32 v220, v216, v220\n\t"
      "v_mfma_f32_16x16x32_bf16 v[196:199], v[28:31], v[132:135], v[196:199]\n\t"
      ".LhopD3:\n\t"
      "s_cbranch_execz .LbackD\n\t"
      "global_load_dwordx4 v[132:135], v177, s[62:63] sc1\n\t"
      "s_waitcnt vmcnt(8)\n\t"
      "v_mfma_f32_16x16x32_bf16 v[184:187], v[32:35], v[136:139], v[184:187]\n\t"
      "v_mul_f32_e32 v221, v217, v221\n\t"
      "s_waitcnt vmcnt(7)\n\t"
      "v_mfma_f32_16x16x32_bf16 v[184:187], v[48:51], v[140:143], v[184:187]\n\t"
      "v_mul_f32_e32 v222, v218, v222\n\t"
      "v_mul_f32_e32 v223, v219, v223\n\t"
      "v_mfma_f32_16x16x32_bf16 v[188:191], v[36:39], v[136:139], v[188:191]\n\t"
      "v_exp_f32_e32 v220, v220\n\t"
      "v_mfma_f32_16x16x32_bf16 v[188:191], v[52:55], v[140:143], v[188:191]\n\t"
      "v_exp_f32_e32 v221, v221\n\t"
      "v_mfma_f32_16x16x32_bf16 v[192:195], v[40:43], v[136:139], v[192:195]\n\t"
      "v_exp_f32_e32 v222, v222\n\t"
      "v_mfma_f32_16x16x32_bf16 v[192:195], v[56:59], v[140:143], v[192:195]\n\t"
      "v_exp_f32_e32 v223, v223\n\t"
      "v_mfma_f32_16x16x32_bf16 v[196:199], v[44:47], v[136:139], v[196:199]\n\t"
      "global_load_dwordx4 v[136:139], v178, s[62:63] sc1\n\t"
      "v_add_f32_e32 v220, 1.0, v220\n\t"
      "v_mfma_f32_16x16x32_bf16 v[196:199], v[60:63], v[140:143], v[196:199]\n\t"
      "global_load_dwordx4 v[140:143], v179, s[62:63] sc1\n\t"
      "s_waitcnt vmcnt(8)\n\t"
      "v_mfma_f32_16x16x32_bf16 v[184:187], v[64:67], v[144:147], v[184:187]\n\t"
      "v_add_f32_e32 v221, 1.0, v221\n\t"
      "s_waitcnt vmcnt(7)\n\t"
      "v_mfma_f32_16x16x32_bf16 v[184:187], v[80:83], v[148:151], v[184:187]\n\t"
      "v_add_f32_e32 v222, 1.0, v222\n\t"
      "v_add_f32_e32 v223, 1.0, v223\n\t"
      "v_mfma_f32_16x16x32_bf16 v[188:191], v[68:71], v[144:147], v[188:191]\n\t"
      "v_rcp_f32_e32 v220, v220\n\t"
      "v_mfma_f32_16x16x32_bf16 v[188:191], v[84:87], v[148:151], v[188:191]\n\t"
      "v_rcp_f32_e32 v221, v221\n\t"
      "v_mfma_f32_16x16x32_bf16 v[192:195], v[72:75], v[144:147], v[192:195]\n\t"
      "v_rcp_f32_e32 v222, v222\n\t"
      "v_mfma_f32_16x16x32_bf16 v[192:195], v[88:91], v[148:151], v[192:195]\n\t"
      "v_rcp_f32_e32 v223, v223\n\t"
      "v_mfma_f32_16x16x32_bf16 v[196:199], v[76:79], v[144:147], v[196:199]\n\t"
      "global_load_dwordx4 v[144:147], v180, s[62:63] sc1\n\t"
      "v_mul_f32_e32 v216, v216, v220\n\t"
      "v_mfma_f32_16x16x32_bf16 v[196:199], v[92:95], v[148:151], v[196:199]\n\t"
      "global_load_dwordx4 v[148:151], v181, s[62:63] sc1\n\t"
      "s_waitcnt vmcnt(7)\n\t"
      "v_mfma_f32_16x16x32_bf16 v[184:187], v[96:99], v[152:155], v[184:187]\n\t"
      "v_mul_f32_e32 v217, v217, v221\n\t"
      "s_waitcnt vmcnt(6)\n\t"
      "v_mfma_f32_16x16x32_bf16 v[184:187], v[112:115], v[156:159], v[184:187]\n\t"
      "v_mul_f32_e32 v218, v218, v222\n\t"
      "v_mul_f32_e32 v219, v219, v223\n\t"
      "v_mfma_f32_16x16x32_bf16 v[188:191], v[100:103], v[152:155], v[188:191]\n\t"
      "v_cvt_pk_bf16_f32 v220, v216, v217\n\t"
      "v_cvt_pk_bf16_f32 v221, v218, v219\n\t"
      "v_mfma_f32_16x16x32_bf16 v[188:191], v[116:119], v[156:159], v[188:191]\n\t"
      "s_nop 1\n\t"
      "v_cmp_u_f32_e32 vcc, v184, v184\n\t"
      "v_min_u32_e32 v220, -2, v220\n\t"
      "v_mfma_f32_16x16x32_bf16 v[192:195], v[104:107], v[152:155], v[192:195]\n\t"
      "v_min_u32_e32 v221, -2, v221\n\t"
      "global_store_dwordx2 v245, v[220:221], s[8:9] sc1\n\t"
      "s_add_u32 s8, s8, 131072\n\t"
      "s_addc_u32 s9, s9, 0\n\t"
      "v_mfma_f32_16x16x32_bf16 v[192:195], v[120:123], v[156:159], v[192:195]\n\t"
      "s_cbranch_vccnz NP4R_%=\n\t"
      "NP4B_%=:\n\t"
      "ds_write_b128 v243, v[184:187]\n\t"
      "v_mfma_f32_16x16x32_bf16 v[196:199], v[108:111], v[152:155], v[196:199]\n\t"
      "global_load_dwordx4 v[152:155], v182, s[62:63] sc1\n\t"
      "v_mfma_f32_16x16x32_bf16 v[196:199], v[124:127], v[156:159], v[196:199]\n\t"
      "global_load_dwordx4 v[156:159], v183, s[62:63] sc1\n\t"
      "ds_write_b128 v164, v[188:191]\n\t"
      "s_nop 0\n\t"
      "ds_write_b128 v165, v[192:195]\n\t"
      "s_nop 3\n\t"
      "ds_write_b128 v166, v[196:199]\n\t"
      "s_waitcnt lgkmcnt(0)\n\t"
      "s_barrier\n\t"
      "ds_read_b128 v[216:219], v244\n\t"
      "ds_read_b128 v[220:223], v244 offset:4096\n\t"
      "ds_read_b128 v[224:227], v244 offset:8192\n\t"
      "ds_read_b128 v[228:231], v244 offset:12288\n\t"
      "ds_read_b128 v[184:187], v244 offset:16384\n\t"
      "ds_read_b128 v[188:191], v244 offset:20480\n\t"
      "ds_read_b128 v[192:195], v244 offset:24576\n\t"
      "ds_read_b128 v[196:199], v244 offset:28672\n\t"
      "s_waitcnt lgkmcnt(6)\n\t"
      "v_add_f32_e32 v216, v216, v220\n\t"
      "v_add_f32_e32 v217, v217, v221\n\t"
      "v_add_f32_e32 v218, v218, v222\n\t"
      "v_add_f32_e32 v219, v219, v223\n\t"
      "s_waitcnt lgkmcnt(4)\n\t"
      "v_add_f32_e32 v224, v224, v228\n\t"
      "v_add_f32_e32 v225, v225, v229\n\t"
      "v_add_f32_e32 v226, v226, v230\n\t"
      "v_add_f32_e32 v227, v227, v231\n\t"
      "s_waitcnt lgkmcnt(2)\n\t"
      "v_add_f32_e32 v184, v184, v188\n\t"
      "v_add_f32_e32 v185, v185, v189\n\t"
      "v_add_f32_e32 v186, v186, v190\n\t"
      "v_add_f32_e32 v187, v187, v191\n\t"
      "v_add_f32_e32 v216, v216, v224\n\t"
      "v_add_f32_e32 v217, v217, v225\n\t"
      "v_add_f32_e32 v218, v218, v226\n\t"
      "v_add_f32_e32 v219, v219, v227\n\t"
      "s_waitcnt lgkmcnt(0)\n\t"
      "v_add_f32_e32 v192, v192, v196\n\t"
      "v_add_f32_e32 v193, v193, v197\n\t"
      "v_add_f32_e32 v194, v194, v198\n\t"
      "v_add_f32_e32 v195, v195, v199\n\t"
      "v_add_f32_e32 v184, v184, v192\n\t"
      "v_add_f32_e32 v185, v185, v193\n\t"
      "v_add_f32_e32 v186, v186, v194\n\t"
      "v_add_f32_e32 v187, v187, v195\n\t"
      "v_add_f32_e32 v216, v216, v184\n\t"
      "s_waitcnt vmcnt(8)\n\t"
      "v_mfma_f32_16x16x32_bf16 v[200:203], v[0:3], v[128:131], 0\n\t"
      "v_add_f32_e32 v217, v217, v185\n\t"
      "s_waitcnt vmcnt(7)\n\t"
      "v_mfma_f32_16x16x32_bf16 v[200:203], v[16:19], v[132:135], v[200:203]\n\t"
      "v_add_f32_e32 v218, v218, v186\n\t"
      "v_add_f32_e32 v219, v219, v187\n\t"
      "v_mfma_f32_16x16x32_bf16 v[204:207], v[4:7], v[128:131], 0\n\t"
      "v_mul_f32_e32 v220, v216, v216\n\t"
      "v_mul_f32_e32 v221, v217, v217\n\t"
      "v_mfma_f32_16x16x32_bf16 v[204:207], v[20:23], v[132:135], v[204:207]\n\t"
      "v_mul_f32_e32 v222, v218, v218\n\t"
      "v_mul_f32_e32 v223, v219, v219\n\t"
      "v_mfma_f32_16x16x32_bf16 v[208:211], v[8:11], v[128:131], 0\n\t"
      "v_fmaak_f32 v220, v235, v220, 0xc0135761\n\t"
      "v_fmaak_f32 v221, v235, v221, 0xc0135761\n\t"
      "v_mfma_f32_16x16x32_bf16 v[208:211], v[24:27], v[132:135], v[208:211]\n\t"
      "v_fmaak_f32 v222, v235, v222, 0xc0135761\n\t"
      "v_fmaak_f32 v223, v235, v223, 0xc0135761\n\t"
      "v_mfma_f32_16x16x32_bf16 v[212:215], v[12:15], v[128:131], 0\n\t"
      "global_load_dwordx4 v[128:131], v176, s[64:65] sc1\n\t"
      "v_mul_f32_e32 v220, v216, v220\n\t"
      "v_mfma_f32_16x16x32_bf16 v[212:215], v[28:31], v[132:135], v[212:215]\n\t"
      "global_load_dwordx4 v[132:135], v177, s[64:65] sc1\n\t"
      "s_waitcnt vmcnt(8)\n\t"
      "v_mfma_f32_16x16x32_bf16 v[200:203], v[32:35], v[136:139], v[200:203]\n\t"
      "v_mul_f32_e32 v221, v217, v221\n\t"
      "s_waitcnt vmcnt(7)\n\t"
      "v_mfma_f32_16x16x32_bf16 v[200:203], v[48:51], v[140:143], v[200:203]\n\t"
      "v_mul_f32_e32 v222, v218, v222\n\t"
      "v_mul_f32_e32 v223, v219, v223\n\t"
      "v_mfma_f32_16x16x32_bf16 v[204:207], v[36:39], v[136:139], v[204:207]\n\t"
      "v_exp_f32_e32 v220, v220\n\t"
      "v_mfma_f32_16x16x32_bf16 v[204:207], v[52:55], v[140:143], v[204:207]\n\t"
      "v_exp_f32_e32 v221, v221\n\t"
      "v_mfma_f32_16x16x32_bf16 v[208:211], v[40:43], v[136:139], v[208:211]\n\t"
      "v_exp_f32_e32 v222, v222\n\t"
      "v_mfma_f32_16x16x32_bf16 v[208:211], v[56:59], v[140:143], v[208:211]\n\t"
      "v_exp_f32_e32 v223, v223\n\t"
      "v_mfma_f32_16x16x32_bf16 v[212:215], v[44:47], v[136:139], v[212:215]\n\t"
      "global_load_dwordx4 v[136:139], v178, s[64:65] sc1\n\t"
      "v_add_f32_e32 v220, 1.0, v220\n\t"
      "v_mfma_f32_16x16x32_bf16 v[212:215], v[60:63], v[140:143], v[212:215]\n\t"
      "global_load_dwordx4 v[140:143], v179, s[64:65] sc1\n\t"
      "s_waitcnt vmcnt(8)\n\t"
      "v_mfma_f32_16x16x32_bf16 v[200:203], v[64:67], v[144:147], v[200:203]\n\t"
      "v_add_f32_e32 v221, 1.0, v221\n\t"
      "s_waitcnt vmcnt(7)\n\t"
      "v_mfma_f32_16x16x32_bf16 v[200:203], v[80:83], v[148:151], v[200:203]\n\t"
      "v_add_f32_e32 v222, 1.0, v222\n\t"
      "v_add_f32_e32 v223, 1.0, v223\n\t"
      "v_mfma_f32_16x16x32_bf16 v[204:207], v[68:71], v[144:147], v[204:207]\n\t"
      "v_rcp_f32_e32 v220, v220\n\t"
      "v_mfma_f32_16x16x32_bf16 v[204:207], v[84:87], v[148:151], v[204:207]\n\t"
      "v_rcp_f32_e32 v221, v221\n\t"
      "v_mfma_f32_16x16x32_bf16 v[208:211], v[72:75], v[144:147], v[208:211]\n\t"
      "v_rcp_f32_e32 v222, v222\n\t"
      "v_mfma_f32_16x16x32_bf16 v[208:211], v[88:91], v[148:151], v[208:211]\n\t"
      "v_rcp_f32_e32 v223, v223\n\t"
      "v_mfma_f32_16x16x32_bf16 v[212:215], v[76:79], v[144:147], v[212:215]\n\t"
      "global_load_dwordx4 v[144:147], v180, s[64:65] sc1\n\t"
      "v_mul_f32_e32 v216, v216, v220\n\t"
      "v_mfma_f32_16x16x32_bf16 v[212:215], v[92:95], v[148:151], v[212:215]\n\t"
      "global_load_dwordx4 v[148:151], v181, s[64:65] sc1\n\t"
      "s_waitcnt vmcnt(7)\n\t"
      "v_mfma_f32_16x16x32_bf16 v[200:203], v[96:99], v[152:155], v[200:203]\n\t"
      "v_mul_f32_e32 v217, v217, v221\n\t"
      "s_waitcnt vmcnt(6)\n\t"
      "v_mfma_f32_16x16x32_bf16 v[200:203], v[112:115], v[156:159], v[200:203]\n\t"
      "v_mul_f32_e32 v218, v218, v222\n\t"
      "v_mul_f32_e32 v219, v219, v223\n\t"
      "v_mfma_f32_16x16x32_bf16 v[204:207], v[100:103], v[152:155], v[204:207]\n\t"
      "v_cvt_pk_bf16_f32 v220, v216, v217\n\t"
      "v_cvt_pk_bf16_f32 v221, v218, v219\n\t"
      "v_mfma_f32_16x16x32_bf16 v[204:207], v[116:119], v[156:159], v[204:207]\n\t"
      "s_nop 1\n\t"
      "v_cmp_u_f32_e32 vcc, v200, v200\n\t"
      "v_min_u32_e32 v220, -2, v220\n\t"
      "v_mfma_f32_16x16x32_bf16 v[208:211], v[104:107], v[152:155], v[208:211]\n\t"
      "v_min_u32_e32 v221, -2, v221\n\t"
      "global_store_dwordx2 v245, v[220:221], s[8:9] sc1\n\t"
      "s_add_u32 s8, s8, 131072\n\t"
      "s_addc_u32 s9, s9, 0\n\t"
      "v_mfma_f32_16x16x32_bf16 v[208:211], v[120:123], v[156:159], v[208:211]\n\t"
      "s_cbranch_vccnz NP5R_%=\n\t"
      "NP5B_%=:\n\t"
      "ds_write_b128 v243, v[200:203] offset:32768\n\t"
      "v_mfma_f32_16x16x32_bf16 v[212:215], v[108:111], v[152:155], v[212:215]\n\t"
      "global_load_dwordx4 v[152:155], v182, s[64:65] sc1\n\t"
      "v_mfma_f32_16x16x32_bf16 v[212:215], v[124:127], v[156:159], v[212:215]\n\t"
      "global_load_dwordx4 v[156:159], v183, s[64:65] sc1\n\t"
      "ds_write_b128 v164, v[204:207] offset:32768\n\t"
      "s_nop 0\n\t"
      "ds_write_b128 v165, v[208:211] offset:32768\n\t"
      "s_nop 3\n\t"
      "ds_write_b128 v166, v[212:215] offset:32768\n\t"
      "s_waitcnt lgkmcnt(0)\n\t"
      "s_barrier\n\t"
      "ds_read_b128 v[216:219], v244 offset:32768\n\t"
      "ds_read_b128 v[220:223], v244 offset:36864\n\t"
      "ds_read_b128 v[224:227], v244 offset:40960\n\t"
      "ds_read_b128 v[228:231], v244 offset:45056\n\t"
      "ds_read_b128 v[200:203], v244 offset:49152\n\t"
      "ds_read_b128 v[204:207], v244 offset:53248\n\t"
      "ds_read_b128 v[208:211], v244 offset:57344\n\t"
      "ds_read_b128 v[212:215], v244 offset:61440\n\t"
      "s_waitcnt lgkmcnt(6)\n\t"
      "v_add_f32_e32 v216, v216, v220\n\t"
      "v_add_f32_e32 v217, v217, v221\n\t"
      "v_add_f32_e32 v218, v218, v222\n\t"
      "v_add_f32_e32 v219, v219, v223\n\t"
      "s_waitcnt lgkmcnt(4)\n\t"
      "v_add_f32_e32 v224, v224, v228\n\t"
      "v_add_f32_e32 v225, v225, v229\n\t"
      "v_add_f32_e32 v226, v226, v230\n\t"
      "v_add_f32_e32 v227, v227, v231\n\t"
      "s_waitcnt lgkmcnt(2)\n\t"
      "v_add_f32_e32 v200, v200, v204\n\t"
      "v_add_f32_e32 v201, v201, v205\n\t"
      "v_add_f32_e32 v202, v202, v206\n\t"
      "v_add_f32_e32 v203, v203, v207\n\t"
      "v_add_f32_e32 v216, v216, v224\n\t"
      "v_add_f32_e32 v217, v217, v225\n\t"
      "v_add_f32_e32 v218, v218, v226\n\t"
      "v_add_f32_e32 v219, v219, v227\n\t"
      "s_waitcnt lgkmcnt(0)\n\t"
      "v_add_f32_e32 v208, v208, v212\n\t"
      "v_add_f32_e32 v209, v209, v213\n\t"
      "v_add_f32_e32 v210, v210, v214\n\t"
      "v_add_f32_e32 v211, v211, v215\n\t"
      "v_add_f32_e32 v200, v200, v208\n\t"
      "s_waitcnt vmcnt(8)\n\t"
      "v_mfma_f32_16x16x32_bf16 v[184:187], v[0:3], v[128:131], 0\n\t"
      "v_add_f32_e32 v201, v201, v209\n\t"
      "s_waitcnt vmcnt(7)\n\t"
      "v_mfma_f32_16x16x32_bf16 v[184:187], v[16:19], v[132:135], v[184:187]\n\t"
      "v_add_f32_e32 v202, v202, v210\n\t"
      "v_add_f32_e32 v203, v203, v211\n\t"
      "v_mfma_f32_16x16x32_bf16 v[188:191], v[4:7], v[128:131], 0\n\t"
      "v_add_f32_e32 v216, v216, v200\n\t"
      "v_add_f32_e32 v217, v217, v201\n\t"
      "v_mfma_f32_16x16x32_bf16 v[188:191], v[20:23], v[132:135], v[188:191]\n\t"
      "v_add_f32_e32 v218, v218, v202\n\t"
      "v_add_f32_e32 v219, v219, v203\n\t"
      "v_mfma_f32_16x16x32_bf16 v[192:195], v[8:11], v[128:131], 0\n\t"
      "v_mul_f32_e32 v220, v216, v216\n\t"
      "v_mul_f32_e32 v221, v217, v217\n\t"
      "v_mfma_f32_16x16x32_bf16 v[192:195], v[24:27], v[132:135], v[192:195]\n\t"
      "v_mul_f32_e32 v222, v218, v218\n\t"
      "v_mul_f32_e32 v223, v219, v219\n\t"
      "v_mfma_f32_16x16x32_bf16 v[196:199], v[12:15], v[128:131], 0\n\t"
      "v_fmaak_f32 v220, v235, v220, 0xc0135761\n\t"
      "v_fmaak_f32 v221, v235, v221, 0xc0135761\n\t"
      "v_mfma_f32_16x16x32_bf16 v[196:199], v[28:31], v[132:135], v[196:199]\n\t"
      "v_fmaak_f32 v222, v235, v222, 0xc0135761\n\t"
      "s_waitcnt vmcnt(6)\n\t"
      "v_mfma_f32_16x16x32_bf16 v[184:187], v[32:35], v[136:139], v[184:187]\n\t"
      "v_fmaak_f32 v223, v235, v223, 0xc0135761\n\t"
      "s_waitcnt vmcnt(5)\n\t"
      "v_mfma_f32_16x16x32_bf16 v[184:187], v[48:51], v[140:143], v[184:187]\n\t"
      "v_mul_f32_e32 v220, v216, v220\n\t"
      "v_mul_f32_e32 v221, v217, v221\n\t"
      "v_mfma_f32_16x16x32_bf16 v[188:191], v[36:39], v[136:139], v[188:191]\n\t"
      "v_mul_f32_e32 v222, v218, v222\n\t"
      "v_mul_f32_e32 v223, v219, v223\n\t"
      "v_mfma_f32_16x16x32_bf16 v[188:191], v[52:55], v[140:143], v[188:191]\n\t"
      "v_exp_f32_e32 v220, v220\n\t"
      "v_mfma_f32_16x16x32_bf16 v[192:195], v[40:43], v[136:139], v[192:195]\n\t"
      "v_exp_f32_e32 v221, v221\n\t"
      "v_mfma_f32_16x16x32_bf16 v[192:195], v[56:59], v[140:143], v[192:195]\n\t"
      "v_exp_f32_e32 v222, v222\n\t"
      "v_mfma_f32_16x16x32_bf16 v[196:199], v[44:47], v[136:139], v[196:199]\n\t"
      "v_exp_f32_e32 v223, v223\n\t"
      "v_mfma_f32_16x16x32_bf16 v[196:199], v[60:63], v[140:143], v[196:199]\n\t"
      "s_waitcnt vmcnt(4)\n\t"
      "v_mfma_f32_16x16x32_bf16 v[184:187], v[64:67], v[144:147], v[184:187]\n\t"
      "s_waitcnt vmcnt(3)\n\t"
      "v_mfma_f32_16x16x32_bf16 v[184:187], v[80:83], v[148:151], v[184:187]\n\t"
      "v_add_f32_e32 v220, 1.0, v220\n\t"
      "v_add_f32_e32 v221, 1.0, v221\n\t"
      "v_mfma_f32_16x16x32_bf16 v[188:191], v[68:71], v[144:147], v[188:191]\n\t"
      "v_add_f32_e32 v222, 1.0, v222\n\t"
      "v_add_f32_e32 v223, 1.0, v223\n\t"
      "v_mfma_f32_16x16x32_bf16 v[188:191], v[84:87], v[148:151], v[188:191]\n\t"
      "v_rcp_f32_e32 v220, v220\n\t"
      "v_mfma_f32_16x16x32_bf16 v[192:195], v[72:75], v[144:147], v[192:195]\n\t"
      "v_rcp_f32_e32 v221, v221\n\t"
      "v_mfma_f32_16x16x32_bf16 v[192:195], v[88:91], v[148:151], v[192:195]\n\t"
      "v_rcp_f32_e32 v222, v222\n\t"
      "v_mfma_f32_16x16x32_bf16 v[196:199], v[76:79], v[144:147], v[196:199]\n\t"
      "v_rcp_f32_e32 v223, v223\n\t"
      "v_mfma_f32_16x16x32_bf16 v[196:199], v[92:95], v[148:151], v[196:199]\n\t"
      "v_mul_f32_e32 v216, v216, v220\n\t"
      "s_waitcnt vmcnt(1)\n\t"
      "v_mfma_f32_16x16x32_bf16 v[184:187], v[96:99], v[152:155], v[184:187]\n\t"
      "v_mul_f32_e32 v217, v217, v221\n\t"
      "s_waitcnt vmcnt(0)\n\t"
      "v_mfma_f32_16x16x32_bf16 v[184:187], v[112:115], v[156:159], v[184:187]\n\t"
      "v_mul_f32_e32 v218, v218, v222\n\t"
      "v_mul_f32_e32 v219, v219, v223\n\t"
      "v_mfma_f32_16x16x32_bf16 v[188:191], v[100:103], v[152:155], v[188:191]\n\t"
      "v_cvt_pk_bf16_f32 v220, v216, v217\n\t"
      "v_cvt_pk_bf16_f32 v221, v218, v219\n\t"
      "v_mfma_f32_16x16x32_bf16 v[188:191], v[116:119], v[156:159], v[188:191]\n\t"
      "s_nop 1\n\t"
      "v_cmp_u_f32_e32 vcc, v184, v184\n\t"
      "v_min_u32_e32 v220, -2, v220\n\t"
      "v_mfma_f32_16x16x32_bf16 v[192:195], v[104:107], v[152:155], v[192:195]\n\t"
      "v_min_u32_e32 v221, -2, v221\n\t"
      "global_store_dwordx2 v245, v[220:221], s[8:9] sc1\n\t"
      "s_add_u32 s8, s8, 131072\n\t"
      "s_addc_u32 s9, s9, 0\n\t"
      "v_mfma_f32_16x16x32_bf16 v[192:195], v[120:123], v[156:159], v[192:195]\n\t"
      "s_cbranch_vccnz NP6R_%=\n\t"
      "NP6B_%=:\n\t"
      "ds_write_b128 v243, v[184:187]\n\t"
      "v_mfma_f32_16x16x32_bf16 v[196:199], v[108:111], v[152:155], v[196:199]\n\t"
      "v_mfma_f32_16x16x32_bf16 v[196:199], v[124:127], v[156:159], v[196:199]\n\t"
      "ds_write_b128 v164, v[188:191]\n\t"
      "s_nop 2\n\t"
      "ds_write_b128 v165, v[192:195]\n\t"
      "s_nop 2\n\t"
      "ds_write_b128 v166, v[196:199]\n\t"
      "s_waitcnt lgkmcnt(0)\n\t"
      "s_barrier\n\t"
      "ds_read_b128 v[216:219], v244\n\t"
      "ds_read_b128 v[220:223], v244 offset:4096\n\t"
      "ds_read_b128 v[224:227], v244 offset:8192\n\t"
      "ds_read_b128 v[228:231], v244 offset:12288\n\t"
      "ds_read_b128 v[184:187], v244 offset:16384\n\t"
      "ds_read_b128 v[188:191], v244 offset:20480\n\t"
      "ds_read_b128 v[192:195], v244 offset:24576\n\t"
      "ds_read_b128 v[196:199], v244 offset:28672\n\t"
      "s_waitcnt lgkmcnt(6)\n\t"
      "v_add_f32_e32 v216, v216, v220\n\t"
      "v_add_f32_e32 v217, v217, v221\n\t"
      "v_add_f32_e32 v218, v218, v222\n\t"
      "v_add_f32_e32 v219, v219, v223\n\t"
      "s_waitcnt lgkmcnt(4)\n\t"
      "v_add_f32_e32 v224, v224, v228\n\t"
      "v_add_f32_e32 v225, v225, v229\n\t"
      "v_add_f32_e32 v226, v226, v230\n\t"
      "v_add_f32_e32 v227, v227, v231\n\t"
      "s_waitcnt lgkmcnt(2)\n\t"
      "v_add_f32_e32 v184, v184, v188\n\t"
      "v_add_f32_e32 v185, v185, v189\n\t"
      "v_add_f32_e32 v186, v186, v190\n\t"
      "v_add_f32_e32 v187, v187, v191\n\t"
      "v_add_f32_e32 v216, v216, v224\n\t"
      "v_add_f32_e32 v217, v217, v225\n\t"
      "v_add_f32_e32 v218, v218, v226\n\t"
      "v_add_f32_e32 v219, v219, v227\n\t"
      "s_waitcnt lgkmcnt(0)\n\t"
      "v_add_f32_e32 v192, v192, v196\n\t"
      "v_add_f32_e32 v193, v193, v197\n\t"
      "v_add_f32_e32 v194, v194, v198\n\t"
      "v_add_f32_e32 v195, v195, v199\n\t"
      "v_add_f32_e32 v184, v184, v192\n\t"
      "v_add_f32_e32 v185, v185, v193\n\t"
      "v_add_f32_e32 v186, v186, v194\n\t"
      "v_add_f32_e32 v187, v187, v195\n\t"
      "v_add_f32_e32 v216, v216, v184\n\t"
      "v_add_f32_e32 v217, v217, v185\n\t"
      "v_add_f32_e32 v218, v218, v186\n\t"
      "v_add_f32_e32 v219, v219, v187\n\t"
      "v_mul_f32_e32 v220, v216, v216\n\t"
      "v_mul_f32_e32 v221, v217, v217\n\t"
      "v_mul_f32_e32 v222, v218, v218\n\t"
      "v_mul_f32_e64 v223, v219, v219\n\t"
      "v_fmaak_f32 v220, v235, v220, 0xc0135761\n\t"
      "v_fmaak_f32 v221, v235, v221, 0xc0135761\n\t"
      "v_fmaak_f32 v222, v235, v222, 0xc0135761\n\t"
      "v_fmaak_f32 v223, v235, v223, 0xc0135761\n\t"
      "v_mul_f32_e32 v220, v216, v220\n\t"
      "v_mul_f32_e32 v221, v217, v221\n\t"
      "v_mul_f32_e32 v222, v218, v222\n\t"
      "v_mul_f32_e32 v223, v219, v223\n\t"
      "v_exp_f32_e32 v220, v220\n\t"
      "v_exp_f32_e32 v221, v221\n\t"
      "v_exp_f32_e32 v222, v222\n\t"
      "v_exp_f32_e32 v223, v223\n\t"
      "v_add_f32_e32 v220, 1.0, v220\n\t"
      "v_add_f32_e32 v221, 1.0, v221\n\t"
      "v_add_f32_e32 v222, 1.0, v222\n\t"
      "v_add_f32_e32 v223, 1.0, v223\n\t"
      "v_rcp_f32_e32 v220, v220\n\t"
      "v_rcp_f32_e32 v221, v221\n\t"
      "v_rcp_f32_e32 v222, v222\n\t"
      "v_rcp_f32_e32 v223, v223\n\t"
      "v_mul_f32_e32 v216, v216, v220\n\t"
      "v_mul_f32_e32 v217, v217, v221\n\t"
      "v_mul_f32_e32 v218, v218, v222\n\t"
      "v_mul_f32_e32 v219, v219, v223\n\t"
      "v_cvt_pk_bf16_f32 v220, v216, v217\n\t"
      "v_cvt_pk_bf16_f32 v221, v218, v219\n\t"
      "v_min_u32_e32 v220, -2, v220\n\t"
      "v_min_u32_e32 v221, -2, v221\n\t"
      "s_cmp_eq_u32 s17, 0\n\t"
      "s_cbranch_scc0 HI6_%=\n\t"
      "s_mov_b64 exec, 0xf\n\t"
      "global_store_dwordx2 v247, v[232:233], s[12:13] sc1\n\t"
      "s_mov_b64 exec, -1\n\t"
      "HI6_%=:\n\t"
      "global_store_dwordx2 v245, v[220:221], s[8:9] sc1\n\t"
      "GX_%=:\n\t"
      "s_bitcmp1_b32 s16, 5\n\t"
      "s_cbranch_scc0 EP_%=\n\t"
      "v_mov_b32_e32 v0, s46\n\t"
      "v_mov_b32_e32 v1, s47\n\t"
      "v_mov_b32_e32 v2, 131072\n\t"
      "s_mov_b64 exec, 1\n\t"
      "global_store_dwordx2 v2, v[0:1], s[12:13] sc1\n\t"
      "s_mov_b64 exec, -1\n\t"
      "EP_%=:\n\t"
      "AEND_%=:\n\t"
      "s_endpgm\n\t"
      ".p2align 6\n\t"
      "GQ_%=:\n\t"
      ".LhopD4:\n\t"
      "s_cbranch_execz .LbackD\n\t"
      "s_waitcnt vmcnt(7)\n\t"
      "v_max3_u32 v234, v128, v129, v130\n\t"
      "v_max_u32_e32 v234, v234, v131\n\t"
      "v_cmp_eq_u32_e32 vcc, -1, v234\n\t"
      "s_cbranch_vccnz FQ0R_%=\n\t"
      "FQ0B_%=:\n\t"
      "v_mfma_f32_16x16x32_bf16 v[184:187], v[0:3], v[128:131], v[236:239]\n\t"
      "s_waitcnt vmcnt(6)\n\t"
      "v_max3_u32 v234, v132, v133, v134\n\t"
      "v_max_u32_e32 v234, v234, v135\n\t"
      "v_cmp_eq_u32_e32 vcc, -1, v234\n\t"
      "s_cbranch_vccnz FQ1R_%=\n\t"
      "FQ1B_%=:\n\t"
      "v_mfma_f32_16x16x32_bf16 v[184:187], v[16:19], v[132:135], v[184:187]\n\t"
      "v_mfma_f32_16x16x32_bf16 v[188:191], v[4:7], v[128:131], 0\n\t"
      "v_mfma_f32_16x16x32_bf16 v[188:191], v[20:23], v[132:135], v[188:191]\n\t"
      "v_mfma_f32_16x16x32_bf16 v[192:195], v[8:11], v[128:131], 0\n\t"
      "v_mfma_f32_16x16x32_bf16 v[192:195], v[24:27], v[132:135], v[192:195]\n\t"
      "v_mfma_f32_16x16x32_bf16 v[196:199], v[12:15], v[128:131], 0\n\t"
      "global_load_dwordx4 v[128:131], v176, s[54:55] sc1\n\t"
      "v_mfma_f32_16x16x32_bf16 v[196:199], v[28:31], v[132:135], v[196:199]\n\t"
      "global_load_dwordx4 v[132:135], v177, s[54:55] sc1\n\t"
      "s_waitcnt vmcnt(7)\n\t"
      "v_max3_u32 v234, v136, v137, v138\n\t"
      "v_max_u32_e32 v234, v234, v139\n\t"
      "v_cmp_eq_u32_e32 vcc, -1, v234\n\t"
      "s_cbranch_vccnz FQ2R_%=\n\t"
      "FQ2B_%=:\n\t"
      "v_mfma_f32_16x16x32_bf16 v[184:187], v[32:35], v[136:139], v[184:187]\n\t"
      "s_waitcnt vmcnt(6)\n\t"
      "v_max3_u32 v234, v140, v141, v142\n\t"
      "v_max_u32_e32 v234, v234, v143\n\t"
      "v_cmp_eq_u32_e32 vcc, -1, v234\n\t"
      "s_cbranch_vccnz FQ3R_%=\n\t"
      "FQ3B_%=:\n\t"
      "v_mfma_f32_16x16x32_bf16 v[184:187], v[48:51], v[140:143], v[184:187]\n\t"
      "v_mfma_f32_16x16x32_bf16 v[188:191], v[36:39], v[136:139], v[188:191]\n\t"
      "v_mfma_f32_16x16x32_bf16 v[188:191], v[52:55], v[140:143], v[188:191]\n\t"
      "v_mfma_f32_16x16x32_bf16 v[192:195], v[40:43], v[136:139], v[192:195]\n\t"
      "v_mfma_f32_16x16x32_bf16 v[192:195], v[56:59], v[140:143], v[192:195]\n\t"
      "v_mfma_f32_16x16x32_bf16 v[196:199], v[44:47], v[136:139], v[196:199]\n\t"
      "global_load_dwordx4 v[136:139], v178, s[54:55] sc1\n\t"
      "v_mfma_f32_16x16x32_bf16 v[196:199], v[60:63], v[140:143], v[196:199]\n\t"
      "global_load_dwordx4 v[140:143], v179, s[54:55] sc1\n\t"
      "s_waitcnt vmcnt(7)\n\t"
      "v_max3_u32 v234, v144, v145, v146\n\t"
      "v_max_u32_e32 v234, v234, v147\n\t"
      "v_cmp_eq_u32_e32 vcc, -1, v234\n\t"
      "s_cbranch_vccnz FQ4R_%=\n\t"
      "FQ4B_%=:\n\t"
      "v_mfma_f32_16x16x32_bf16 v[184:187], v[64:67], v[144:147], v[184:187]\n\t"
      "s_waitcnt vmcnt(6)\n\t"
      "v_max3_u32 v234, v148, v149, v150\n\t"
      "v_max_u32_e32 v234, v234, v151\n\t"
      "v_cmp_eq_u32_e32 vcc, -1, v234\n\t"
      "s_cbranch_vccnz FQ5R_%=\n\t"
      "FQ5B_%=:\n\t"
      "v_mfma_f32_16x16x32_bf16 v[184:187], v[80:83], v[148:151], v[184:187]\n\t"
      "v_mfma_f32_16x16x32_bf16 v[188:191], v[68:71], v[144:147], v[188:191]\n\t"
      "v_mfma_f32_16x16x32_bf16 v[188:191], v[84:87], v[148:151], v[188:191]\n\t"
      "v_mfma_f32_16x16x32_bf16 v[192:195], v[72:75], v[144:147], v[192:195]\n\t"
      "v_mfma_f32_16x16x32_bf16 v[192:195], v[88:91], v[148:151], v[192:195]\n\t"
      "v_mfma_f32_16x16x32_bf16 v[196:199], v[76:79], v[144:147], v[196:199]\n\t"
      "global_load_dwordx4 v[144:147], v180, s[54:55] sc1\n\t"
      "v_mfma_f32_16x16x32_bf16 v[196:199], v[92:95], v[148:151], v[196:199]\n\t"
      "global_load_dwordx4 v[148:151], v181, s[54:55] sc1\n\t"
      "s_waitcnt vmcnt(7)\n\t"
      "v_max3_u32 v234, v152, v153, v154\n\t"
      "v_max_u32_e32 v234, v234, v155\n\t"
      "v_cmp_eq_u32_e32 vcc, -1, v234\n\t"
      "s_cbranch_vccnz FQ6R_%=\n\t"
      "FQ6B_%=:\n\t"
      "v_mfma_f32_16x16x32_bf16 v[184:187], v[96:99], v[152:155], v[184:187]\n\t"
      "s_waitcnt vmcnt(6)\n\t"
      "v_max3_u32 v234, v156, v157, v158\n\t"
      "v_max_u32_e32 v234, v234, v159\n\t"
      "v_cmp_eq_u32_e32 vcc, -1, v234\n\t"
      "s_cbranch_vccnz FQ7R_%=\n\t"
      "FQ7B_%=:\n\t"
      "v_mfma_f32_16x16x32_bf16 v[184:187], v[112:115], v[156:159], v[184:187]\n\t"
      "v_mfma_f32_16x16x32_bf16 v[188:191], v[100:103], v[152:155], v[188:191]\n\t"
      "v_mfma_f32_16x16x32_bf16 v[188:191], v[116:119], v[156:159], v[188:191]\n\t"
      "v_mfma_f32_16x16x32_bf16 v[192:195], v[104:107], v[152:155], v[192:195]\n\t"
      "v_mfma_f32_16x16x32_bf16 v[192:195], v[120:123], v[156:159], v[192:195]\n\t"
      "v_mfma_f32_16x16x32_bf16 v[196:199], v[108:111], v[152:155], v[196:199]\n\t"
      "global_load_dwordx4 v[152:155], v182, s[54:55] sc1\n\t"
      "v_mfma_f32_16x16x32_bf16 v[196:199], v[124:127], v[156:159], v[196:199]\n\t"
      "global_load_dwordx4 v[156:159], v183, s[54:55] sc1\n\t"
      "ds_write_b128 v243, v[184:187]\n\t"
      "s_nop 0\n\t"
      "ds_write_b128 v164, v[188:191]\n\t"
      "s_nop 0\n\t"
      "ds_write_b128 v165, v[192:195]\n\t"
      "s_nop 1\n\t"
      "ds_write_b128 v166, v[196:199]\n\t"
      "s_waitcnt vmcnt(7)\n\t"
      "v_mfma_f32_16x16x32_bf16 v[200:203], v[0:3], v[128:131], v[236:239]\n\t"
      "s_waitcnt vmcnt(6)\n\t"
      "v_mfma_f32_16x16x32_bf16 v[200:203], v[16:19], v[132:135], v[200:203]\n\t"
      "v_mfma_f32_16x16x32_bf16 v[204:207], v[4:7], v[128:131], 0\n\t"
      "v_mfma_f32_16x16x32_bf16 v[204:207], v[20:23], v[132:135], v[204:207]\n\t"
      "v_mfma_f32_16x16x32_bf16 v[208:211], v[8:11], v[128:131], 0\n\t"
      "v_mfma_f32_16x16x32_bf16 v[208:211], v[24:27], v[132:135], v[208:211]\n\t"
      "v_mfma_f32_16x16x32_bf16 v[212:215], v[12:15], v[128:131], 0\n\t"
      "global_load_dwordx4 v[128:131], v176, s[56:57] sc1\n\t"
      "v_mfma_f32_16x16x32_bf16 v[212:215], v[28:31], v[132:135], v[212:215]\n\t"
      "global_load_dwordx4 v[132:135], v177, s[56:57] sc1\n\t"
      "s_waitcnt vmcnt(7)\n\t"
      "v_mfma_f32_16x16x32_bf16 v[200:203], v[32:35], v[136:139], v[200:203]\n\t"
      "s_waitcnt vmcnt(6)\n\t"
      "v_mfma_f32_16x16x32_bf16 v[200:203], v[48:51], v[140:143], v[200:203]\n\t"
      "v_mfma_f32_16x16x32_bf16 v[204:207], v[36:39], v[136:139], v[204:207]\n\t"
      "v_mfma_f32_16x16x32_bf16 v[204:207], v[52:55], v[140:143], v[204:207]\n\t"
      "v_mfma_f32_16x16x32_bf16 v[208:211], v[40:43], v[136:139], v[208:211]\n\t"
      "v_mfma_f32_16x16x32_bf16 v[208:211], v[56:59], v[140:143], v[208:211]\n\t"
      "v_mfma_f32_16x16x32_bf16 v[212:215], v[44:47], v[136:139], v[212:215]\n\t"
      "global_load_dwordx4 v[136:139], v178, s[56:57] sc1\n\t"
      "v_mfma_f32_16x16x32_bf16 v[212:215], v[60:63], v[140:143], v[212:215]\n\t"
      "global_load_dwordx4 v[140:143], v179, s[56:57] sc1\n\t"
      "s_waitcnt vmcnt(7)\n\t"
      "v_mfma_f32_16x16x32_bf16 v[200:203], v[64:67], v[144:147], v[200:203]\n\t"
      "s_waitcnt vmcnt(6)\n\t"
      "v_mfma_f32_16x16x32_bf16 v[200:203], v[80:83], v[148:151], v[200:203]\n\t"
      "v_mfma_f32_16x16x32_bf16 v[204:207], v[68:71], v[144:147], v[204:207]\n\t"
      "v_mfma_f32_16x16x32_bf16 v[204:207], v[84:87], v[148:151], v[204:207]\n\t"
      "v_mfma_f32_16x16x32_bf16 v[208:211], v[72:75], v[144:147], v[208:211]\n\t"
      "v_mfma_f32_16x16x32_bf16 v[208:211], v[88:91], v[148:151], v[208:211]\n\t"
      "s_waitcnt lgkmcnt(0)\n\t"
      "s_barrier\n\t"
      "s_setprio 2\n\t"
      "v_mfma_f32_16x16x32_bf16 v[212:215], v[76:79], v[144:147], v[212:215]\n\t"
      "global_load_dwordx4 v[144:147], v180, s[56:57] sc1\n\t"
      "v_mfma_f32_16x16x32_bf16 v[212:215], v[92:95], v[148:151], v[212:215]\n\t"
      "global_load_dwordx4 v[148:151], v181, s[56:57] sc1\n\t"
      "s_waitcnt vmcnt(7)\n\t"
      "v_mfma_f32_16x16x32_bf16 v[200:203], v[96:99], v[152:155], v[200:203]\n\t"
      "s_waitcnt vmcnt(6)\n\t"
      "v_mfma_f32_16x16x32_bf16 v[200:203], v[112:115], v[156:159], v[200:203]\n\t"
      "v_mfma_f32_16x16x32_bf16 v[204:207], v[100:103], v[152:155], v[204:207]\n\t"
      "v_mfma_f32_16x16x32_bf16 v[204:207], v[116:119], v[156:159], v[204:207]\n\t"
      "v_mfma_f32_16x16x32_bf16 v[208:211], v[104:107], v[152:155], v[208:211]\n\t"
      "v_mfma_f32_16x16x32_bf16 v[208:211], v[120:123], v[156:159], v[208:211]\n\t"
      "v_mfma_f32_16x16x32_bf16 v[212:215], v[108:111], v[152:155], v[212:215]\n\t"
      "global_load_dwordx4 v[152:155], v182, s[56:57] sc1\n\t"
      "v_mfma_f32_16x16x32_bf16 v[212:215], v[124:127], v[156:159], v[212:215]\n\t"
      "global_load_dwordx4 v[156:159], v183, s[56:57] sc1\n\t"
      "s_setprio 0\n\t"
      "v_cmp_u_f32_e32 vcc, v200, v200\n\t"
      "s_cbranch_vccnz NQ1R_%=\n\t"
      "NQ1B_%=:\n\t"
      "ds_write_b128 v243, v[200:203] offset:32768\n\t"
      "ds_write_b128 v164, v[204:207] offset:32768\n\t"
      "ds_write_b128 v165, v[208:211] offset:32768\n\t"
      "s_nop 0\n\t"
      "ds_write_b128 v166, v[212:215] offset:32768\n\t"
      "s_waitcnt vmcnt(7)\n\t"
      "v_mfma_f32_16x16x32_bf16 v[184:187], v[0:3], v[128:131], v[236:239]\n\t"
      "s_waitcnt vmcnt(6)\n\t"
      "v_mfma_f32_16x16x32_bf16 v[184:187], v[16:19], v[132:135], v[184:187]\n\t"
      "v_mfma_f32_16x16x32_bf16 v[188:191], v[4:7], v[128:131], 0\n\t"
      "v_mfma_f32_16x16x32_bf16 v[188:191], v[20:23], v[132:135], v[188:191]\n\t"
      "v_mfma_f32_16x16x32_bf16 v[192:195], v[8:11], v[128:131], 0\n\t"
      "v_mfma_f32_16x16x32_bf16 v[192:195], v[24:27], v[132:135], v[192:195]\n\t"
      "v_mfma_f32_16x16x32_bf16 v[196:199], v[12:15], v[128:131], 0\n\t"
      "global_load_dwordx4 v[128:131], v176, s[58:59] sc1\n\t"
      "v_mfma_f32_16x16x32_bf16 v[196:199], v[28:31], v[132:135], v[196:199]\n\t"
      "global_load_dwordx4 v[132:135], v177, s[58:59] sc1\n\t"
      "s_waitcnt vmcnt(7)\n\t"
      "v_mfma_f32_16x16x32_bf16 v[184:187], v[32:35], v[136:139], v[184:187]\n\t"
      "s_waitcnt vmcnt(6)\n\t"
      "v_mfma_f32_16x16x32_bf16 v[184:187], v[48:51], v[140:143], v[184:187]\n\t"
      "v_mfma_f32_16x16x32_bf16 v[188:191], v[36:39], v[136:139], v[188:191]\n\t"
      "v_mfma_f32_16x16x32_bf16 v[188:191], v[52:55], v[140:143], v[188:191]\n\t"
      "v_mfma_f32_16x16x32_bf16 v[192:195], v[40:43], v[136:139], v[192:195]\n\t"
      "v_mfma_f32_16x16x32_bf16 v[192:195], v[56:59], v[140:143], v[192:195]\n\t"
      "v_mfma_f32_16x16x32_bf16 v[196:199], v[44:47], v[136:139], v[196:199]\n\t"
      "global_load_dwordx4 v[136:139], v178, s[58:59] sc1\n\t"
      "v_mfma_f32_16x16x32_bf16 v[196:199], v[60:63], v[140:143], v[196:199]\n\t"
      "global_load_dwordx4 v[140:143], v179, s[58:59] sc1\n\t"
      "s_waitcnt vmcnt(7)\n\t"
      "v_mfma_f32_16x16x32_bf16 v[184:187], v[64:67], v[144:147], v[184:187]\n\t"
      "s_waitcnt vmcnt(6)\n\t"
      "v_mfma_f32_16x16x32_bf16 v[184:187], v[80:83], v[148:151], v[184:187]\n\t"
      "v_mfma_f32_16x16x32_bf16 v[188:191], v[68:71], v[144:147], v[188:191]\n\t"
      "v_mfma_f32_16x16x32_bf16 v[188:191], v[84:87], v[148:151], v[188:191]\n\t"
      "v_mfma_f32_16x16x32_bf16 v[192:195], v[72:75], v[144:147], v[192:195]\n\t"
      "v_mfma_f32_16x16x32_bf16 v[192:195], v[88:91], v[148:151], v[192:195]\n\t"
      "s_waitcnt lgkmcnt(0)\n\t"
      "s_barrier\n\t"
      "s_setprio 2\n\t"
      "v_mfma_f32_16x16x32_bf16 v[196:199], v[76:79], v[144:147], v[196:199]\n\t"
      "global_load_dwordx4 v[144:147], v180, s[58:59] sc1\n\t"
      "v_mfma_f32_16x16x32_bf16 v[196:199], v[92:95], v[148:151], v[196:199]\n\t"
      "global_load_dwordx4 v[148:151], v181, s[58:59] sc1\n\t"
      "s_waitcnt vmcnt(7)\n\t"
      "v_mfma_f32_16x16x32_bf16 v[184:187], v[96:99], v[152:155], v[184:187]\n\t"
      "s_waitcnt vmcnt(6)\n\t"
      "v_mfma_f32_16x16x32_bf16 v[184:187], v[112:115], v[156:159], v[184:187]\n\t"
      "v_mfma_f32_16x16x32_bf16 v[188:191], v[100:103], v[152:155], v[188:191]\n\t"
      "v_mfma_f32_16x16x32_bf16 v[188:191], v[116:119], v[156:159], v[188:191]\n\t"
      "v_mfma_f32_16x16x32_bf16 v[192:195], v[104:107], v[152:155], v[192:195]\n\t"
      "v_mfma_f32_16x16x32_bf16 v[192:195], v[120:123], v[156:159], v[192:195]\n\t"
      "v_mfma_f32_16x16x32_bf16 v[196:199], v[108:111], v[152:155], v[196:199]\n\t"
      "global_load_dwordx4 v[152:155], v182, s[58:59] sc1\n\t"
      "v_mfma_f32_16x16x32_bf16 v[196:199], v[124:127], v[156:159], v[196:199]\n\t"
      "global_load_dwordx4 v[156:159], v183, s[58:59] sc1\n\t"
      "s_setprio 0\n\t"
      "v_cmp_u_f32_e32 vcc, v184, v184\n\t"
      "s_cbranch_vccnz NQ2R_%=\n\t"
      "NQ2B_%=:\n\t"
      "ds_write_b128 v243, v[184:187]\n\t"
      "ds_write_b128 v164, v[188:191]\n\t"
      "ds_write_b128 v165, v[192:195]\n\t"
      "s_nop 0\n\t"
      "ds_write_b128 v166, v[196:199]\n\t"
      "s_waitcnt vmcnt(7)\n\t"
      "v_mfma_f32_16x16x32_bf16 v[200:203], v[0:3], v[128:131], v[236:239]\n\t"
      "s_waitcnt vmcnt(6)\n\t"
      "v_mfma_f32_16x16x32_bf16 v[200:203], v[16:19], v[132:135], v[200:203]\n\t"
      "v_mfma_f32_16x16x32_bf16 v[204:207], v[4:7], v[128:131], 0\n\t"
      "v_mfma_f32_16x16x32_bf16 v[204:207], v[20:23], v[132:135], v[204:207]\n\t"
      "v_mfma_f32_16x16x32_bf16 v[208:211], v[8:11], v[128:131], 0\n\t"
      "v_mfma_f32_16x16x32_bf16 v[208:211], v[24:27], v[132:135], v[208:211]\n\t"
      "v_mfma_f32_16x16x32_bf16 v[212:215], v[12:15], v[128:131], 0\n\t"
      "global_load_dwordx4 v[128:131], v176, s[60:61] sc1\n\t"
      "v_mfma_f32_16x16x32_bf16 v[212:215], v[28:31], v[132:135], v[212:215]\n\t"
      "global_load_dwordx4 v[132:135], v177, s[60:61] sc1\n\t"
      "s_waitcnt vmcnt(7)\n\t"
      "v_mfma_f32_16x16x32_bf16 v[200:203], v[32:35], v[136:139], v[200:203]\n\t"
      "s_waitcnt vmcnt(6)\n\t"
      "v_mfma_f32_16x16x32_bf16 v[200:203], v[48:51], v[140:143], v[200:203]\n\t"
      "v_mfma_f32_16x16x32_bf16 v[204:207], v[36:39], v[136:139], v[204:207]\n\t"
      "v_mfma_f32_16x16x32_bf16 v[204:207], v[52:55], v[140:143], v[204:207]\n\t"
      "v_mfma_f32_16x16x32_bf16 v[208:211], v[40:43], v[136:139], v[208:211]\n\t"
      "v_mfma_f32_16x16x32_bf16 v[208:211], v[56:59], v[140:143], v[208:211]\n\t"
      "v_mfma_f32_16x16x32_bf16 v[212:215], v[44:47], v[136:139], v[212:215]\n\t"
      "global_load_dwordx4 v[136:139], v178, s[60:61] sc1\n\t"
      "v_mfma_f32_16x16x32_bf16 v[212:215], v[60:63], v[140:143], v[212:215]\n\t"
      "global_load_dwordx4 v[140:143], v179, s[60:61] sc1\n\t"
      "s_waitcnt vmcnt(7)\n\t"
      "v_mfma_f32_16x16x32_bf16 v[200:203], v[64:67], v[144:147], v[200:203]\n\t"
      "s_waitcnt vmcnt(6)\n\t"
      "v_mfma_f32_16x16x32_bf16 v[200:203], v[80:83], v[148:151], v[200:203]\n\t"
      "v_mfma_f32_16x16x32_bf16 v[204:207], v[68:71], v[144:147], v[204:207]\n\t"
      "v_mfma_f32_16x16x32_bf16 v[204:207], v[84:87], v[148:151], v[204:207]\n\t"
      ".LhopD5:\n\t"
      "s_cbranch_execz .LbackD\n\t"
      "v_mfma_f32_16x16x32_bf16 v[208:211], v[72:75], v[144:147], v[208:211]\n\t"
      "v_mfma_f32_16x16x32_bf16 v[208:211], v[88:91], v[148:151], v[208:211]\n\t"
      "s_waitcnt lgkmcnt(0)\n\t"
      "s_barrier\n\t"
      "s_setprio 2\n\t"
      "v_mfma_f32_16x16x32_bf16 v[212:215], v[76:79], v[144:147], v[212:215]\n\t"
      "global_load_dwordx4 v[144:147], v180, s[60:61] sc1\n\t"
      "v_mfma_f32_16x16x32_bf16 v[212:215], v[92:95], v[148:151], v[212:215]\n\t"
      "global_load_dwordx4 v[148:151], v181, s[60:61] sc1\n\t"
      ".LhopD6:\n\t"
      "s_cbranch_execz .LbackD\n\t"
      "s_waitcnt vmcnt(7)\n\t"
      "v_mfma_f32_16x16x32_bf16 v[200:203], v[96:99], v[152:155], v[200:203]\n\t"
      "s_waitcnt vmcnt(6)\n\t"
      "v_mfma_f32_16x16x32_bf16 v[200:203], v[112:115], v[156:159], v[200:203]\n\t"
      "v_mfma_f32_16x16x32_bf16 v[204:207], v[100:103], v[152:155], v[204:207]\n\t"
      "v_mfma_f32_16x16x32_bf16 v[204:207], v[116:119], v[156:159], v[204:207]\n\t"
      "v_mfma_f32_16x16x32_bf16 v[208:211], v[104:107], v[152:155], v[208:211]\n\t"
      "v_mfma_f32_16x16x32_bf16 v[208:211], v[120:123], v[156:159], v[208:211]\n\t"
      "v_mfma_f32_16x16x32_bf16 v[212:215], v[108:111], v[152:155], v[212:215]\n\t"
      "global_load_dwordx4 v[152:155], v182, s[60:61] sc1\n\t"
      "v_mfma_f32_16x16x32_bf16 v[212:215], v[124:127], v[156:159], v[212:215]\n\t"
      "global_load_dwordx4 v[156:159], v183, s[60:61] sc1\n\t"
      "s_setprio 0\n\t"
      "v_cmp_u_f32_e32 vcc, v200, v200\n\t"
      "s_cbranch_vccnz NQ3R_%=\n\t"
      "NQ3B_%=:\n\t"
      "ds_write_b128 v243, v[200:203] offset:32768\n\t"
      "ds_write_b128 v164, v[204:207] offset:32768\n\t"
      "ds_write_b128 v165, v[208:211] offset:32768\n\t"
      "s_nop 0\n\t"
      "ds_write_b128 v166, v[212:215] offset:32768\n\t"
      "s_waitcnt vmcnt(7)\n\t"
      "v_mfma_f32_16x16x32_bf16 v[184:187], v[0:3], v[128:131], v[236:239]\n\t"
      "s_waitcnt vmcnt(6)\n\t"
      "v_mfma_f32_16x16x32_bf16 v[184:187], v[16:19], v[132:135], v[184:187]\n\t"
      "v_mfma_f32_16x16x32_bf16 v[188:191], v[4:7], v[128:131], 0\n\t"
      "v_mfma_f32_16x16x32_bf16 v[188:191], v[20:23], v[132:135], v[188:191]\n\t"
      "v_mfma_f32_16x16x32_bf16 v[192:195], v[8:11], v[128:131], 0\n\t"
      "v_mfma_f32_16x16x32_bf16 v[192:195], v[24:27], v[132:135], v[192:195]\n\t"
      "v_mfma_f32_16x16x32_bf16 v[196:199], v[12:15], v[128:131], 0\n\t"
      "global_load_dwordx4 v[128:131], v176, s[62:63] sc1\n\t"
      "v_mfma_f32_16x16x32_bf16 v[196:199], v[28:31], v[132:135], v[196:199]\n\t"
      "global_load_dwordx4 v[132:135], v177, s[62:63] sc1\n\t"
      "s_waitcnt vmcnt(7)\n\t"
      "v_mfma_f32_16x16x32_bf16 v[184:187], v[32:35], v[136:139], v[184:187]\n\t"
      "s_waitcnt vmcnt(6)\n\t"
      "v_mfma_f32_16x16x32_bf16 v[184:187], v[48:51], v[140:143], v[184:187]\n\t"
      "v_mfma_f32_16x16x32_bf16 v[188:191], v[36:39], v[136:139], v[188:191]\n\t"
      "v_mfma_f32_16x16x32_bf16 v[188:191], v[52:55], v[140:143], v[188:191]\n\t"
      "v_mfma_f32_16x16x32_bf16 v[192:195], v[40:43], v[136:139], v[192:195]\n\t"
      "v_mfma_f32_16x16x32_bf16 v[192:195], v[56:59], v[140:143], v[192:195]\n\t"
      "v_mfma_f32_16x16x32_bf16 v[196:199], v[44:47], v[136:139], v[196:199]\n\t"
      "global_load_dwordx4 v[136:139], v178, s[62:63] sc1\n\t"
      "v_mfma_f32_16x16x32_bf16 v[196:199], v[60:63], v[140:143], v[196:199]\n\t"
      "global_load_dwordx4 v[140:143], v179, s[62:63] sc1\n\t"
      "s_waitcnt vmcnt(7)\n\t"
      "v_mfma_f32_16x16x32_bf16 v[184:187], v[64:67], v[144:147], v[184:187]\n\t"
      "s_waitcnt vmcnt(6)\n\t"
      "v_mfma_f32_16x16x32_bf16 v[184:187], v[80:83], v[148:151], v[184:187]\n\t"
      "v_mfma_f32_16x16x32_bf16 v[188:191], v[68:71], v[144:147], v[188:191]\n\t"
      "v_mfma_f32_16x16x32_bf16 v[188:191], v[84:87], v[148:151], v[188:191]\n\t"
      "v_mfma_f32_16x16x32_bf16 v[192:195], v[72:75], v[144:147], v[192:195]\n\t"
      "v_mfma_f32_16x16x32_bf16 v[192:195], v[88:91], v[148:151], v[192:195]\n\t"
      "s_waitcnt lgkmcnt(0)\n\t"
      "s_barrier\n\t"
      "s_setprio 2\n\t"
      "v_mfma_f32_16x16x32_bf16 v[196:199], v[76:79], v[144:147], v[196:199]\n\t"
      "global_load_dwordx4 v[144:147], v180, s[62:63] sc1\n\t"
      "v_mfma_f32_16x16x32_bf16 v[196:199], v[92:95], v[148:151], v[196:199]\n\t"
      "global_load_dwordx4 v[148:151], v181, s[62:63] sc1\n\t"
      "s_waitcnt vmcnt(7)\n\t"
      "v_mfma_f32_16x16x32_bf16 v[184:187], v[96:99], v[152:155], v[184:187]\n\t"
      "s_waitcnt vmcnt(6)\n\t"
      "v_mfma_f32_16x16x32_bf16 v[184:187], v[112:115], v[156:159], v[184:187]\n\t"
      "v_mfma_f32_16x16x32_bf16 v[188:191], v[100:103], v[152:155], v[188:191]\n\t"
      "v_mfma_f32_16x16x32_bf16 v[188:191], v[116:119], v[156:159], v[188:191]\n\t"
      "v_mfma_f32_16x16x32_bf16 v[192:195], v[104:107], v[152:155], v[192:195]\n\t"
      "v_mfma_f32_16x16x32_bf16 v[192:195], v[120:123], v[156:159], v[192:195]\n\t"
      "v_mfma_f32_16x16x32_bf16 v[196:199], v[108:111], v[152:155], v[196:199]\n\t"
      "global_load_dwordx4 v[152:155], v182, s[62:63] sc1\n\t"
      "v_mfma_f32_16x16x32_bf16 v[196:199], v[124:127], v[156:159], v[196:199]\n\t"
      "global_load_dwordx4 v[156:159], v183, s[62:63] sc1\n\t"
      "s_setprio 0\n\t"
      "v_cmp_u_f32_e32 vcc, v184, v184\n\t"
      "s_cbranch_vccnz NQ4R_%=\n\t"
      "NQ4B_%=:\n\t"
      "ds_write_b128 v243, v[184:187]\n\t"
      "ds_write_b128 v164, v[188:191]\n\t"
      "ds_write_b128 v165, v[192:195]\n\t"
      "s_nop 0\n\t"
      "ds_write_b128 v166, v[196:199]\n\t"
      "s_waitcnt vmcnt(7)\n\t"
      "v_mfma_f32_16x16x32_bf16 v[200:203], v[0:3], v[128:131], v[236:239]\n\t"
      "s_waitcnt vmcnt(6)\n\t"
      "v_mfma_f32_16x16x32_bf16 v[200:203], v[16:19], v[132:135], v[200:203]\n\t"
      "v_mfma_f32_16x16x32_bf16 v[204:207], v[4:7], v[128:131], 0\n\t"
      "v_mfma_f32_16x16x32_bf16 v[204:207], v[20:23], v[132:135], v[204:207]\n\t"
      "v_mfma_f32_16x16x32_bf16 v[208:211], v[8:11], v[128:131], 0\n\t"
      "v_mfma_f32_16x16x32_bf16 v[208:211], v[24:27], v[132:135], v[208:211]\n\t"
      "v_mfma_f32_16x16x32_bf16 v[212:215], v[12:15], v[128:131], 0\n\t"
      "global_load_dwordx4 v[128:131], v176, s[64:65] sc1\n\t"
      "v_mfma_f32_16x16x32_bf16 v[212:215], v[28:31], v[132:135], v[212:215]\n\t"
      "global_load_dwordx4 v[132:135], v177, s[64:65] sc1\n\t"
      "s_waitcnt vmcnt(7)\n\t"
      "v_mfma_f32_16x16x32_bf16 v[200:203], v[32:35], v[136:139], v[200:203]\n\t"
      "s_waitcnt vmcnt(6)\n\t"
      "v_mfma_f32_16x16x32_bf16 v[200:203], v[48:51], v[140:143], v[200:203]\n\t"
      "v_mfma_f32_16x16x32_bf16 v[204:207], v[36:39], v[136:139], v[204:207]\n\t"
      "v_mfma_f32_16x16x32_bf16 v[204:207], v[52:55], v[140:143], v[204:207]\n\t"
      "v_mfma_f32_16x16x32_bf16 v[208:211], v[40:43], v[136:139], v[208:211]\n\t"
      "v_mfma_f32_16x16x32_bf16 v[208:211], v[56:59], v[140:143], v[208:211]\n\t"
      "v_mfma_f32_16x16x32_bf16 v[212:215], v[44:47], v[136:139], v[212:215]\n\t"
      "global_load_dwordx4 v[136:139], v178, s[64:65] sc1\n\t"
      "v_mfma_f32_16x16x32_bf16 v[212:215], v[60:63], v[140:143], v[212:215]\n\t"
      "global_load_dwordx4 v[140:143], v179, s[64:65] sc1\n\t"
      "s_waitcnt vmcnt(7)\n\t"
      "v_mfma_f32_16x16x32_bf16 v[200:203], v[64:67], v[144:147], v[200:203]\n\t"
      "s_waitcnt vmcnt(6)\n\t"
      "v_mfma_f32_16x16x32_bf16 v[200:203], v[80:83], v[148:151], v[200:203]\n\t"
      "v_mfma_f32_16x16x32_bf16 v[204:207], v[68:71], v[144:147], v[204:207]\n\t"
      "v_mfma_f32_16x16x32_bf16 v[204:207], v[84:87], v[148:151], v[204:207]\n\t"
      "v_mfma_f32_16x16x32_bf16 v[208:211], v[72:75], v[144:147], v[208:211]\n\t"
      "v_mfma_f32_16x16x32_bf16 v[208:211], v[88:91], v[148:151], v[208:211]\n\t"
      "s_waitcnt lgkmcnt(0)\n\t"
      "s_barrier\n\t"
      "s_setprio 2\n\t"
      "v_mfma_f32_16x16x32_bf16 v[212:215], v[76:79], v[144:147], v[212:215]\n\t"
      "global_load_dwordx4 v[144:147], v180, s[64:65] sc1\n\t"
      "v_mfma_f32_16x16x32_bf16 v[212:215], v[92:95], v[148:151], v[212:215]\n\t"
      "global_load_dwordx4 v[148:151], v181, s[64:65] sc1\n\t"
      "s_waitcnt vmcnt(7)\n\t"
      "v_mfma_f32_16x16x32_bf16 v[200:203], v[96:99], v[152:155], v[200:203]\n\t"
      "s_waitcnt vmcnt(6)\n\t"
      "v_mfma_f32_16x16x32_bf16 v[200:203], v[112:115], v[156:159], v[200:203]\n\t"
      "v_mfma_f32_16x16x32_bf16 v[204:207], v[100:103], v[152:155], v[204:207]\n\t"
      "v_mfma_f32_16x16x32_bf16 v[204:207], v[116:119], v[156:159], v[204:207]\n\t"
      "v_mfma_f32_16x16x32_bf16 v[208:211], v[104:107], v[152:155], v[208:211]\n\t"
      "v_mfma_f32_16x16x32_bf16 v[208:211], v[120:123], v[156:159], v[208:211]\n\t"
      "v_mfma_f32_16x16x32_bf16 v[212:215], v[108:111], v[152:155], v[212:215]\n\t"
      "global_load_dwordx4 v[152:155], v182, s[64:65] sc1\n\t"
      "v_mfma_f32_16x16x32_bf16 v[212:215], v[124:127], v[156:159], v[212:215]\n\t"
      "global_load_dwordx4 v[156:159], v183, s[64:65] sc1\n\t"
      "s_setprio 0\n\t"
      "v_cmp_u_f32_e32 vcc, v200, v200\n\t"
      "s_cbranch_vccnz NQ5R_%=\n\t"
      "NQ5B_%=:\n\t"
      "ds_write_b128 v243, v[200:203] offset:32768\n\t"
      "ds_write_b128 v164, v[204:207] offset:32768\n\t"
      "ds_write_b128 v165, v[208:211] offset:32768\n\t"
      "s_nop 0\n\t"
      "ds_write_b128 v166, v[212:215] offset:32768\n\t"
      "s_waitcnt vmcnt(7)\n\t"
      "v_mfma_f32_16x16x32_bf16 v[184:187], v[0:3], v[128:131], v[236:239]\n\t"
      "s_waitcnt vmcnt(6)\n\t"
      "v_mfma_f32_16x16x32_bf16 v[184:187], v[16:19], v[132:135], v[184:187]\n\t"
      "v_mfma_f32_16x16x32_bf16 v[188:191], v[4:7], v[128:131], 0\n\t"
      "v_mfma_f32_16x16x32_bf16 v[188:191], v[20:23], v[132:135], v[188:191]\n\t"
      "v_mfma_f32_16x16x32_bf16 v[192:195], v[8:11], v[128:131], 0\n\t"
      "v_mfma_f32_16x16x32_bf16 v[192:195], v[24:27], v[132:135], v[192:195]\n\t"
      "v_mfma_f32_16x16x32_bf16 v[196:199], v[12:15], v[128:131], 0\n\t"
      "v_mfma_f32_16x16x32_bf16 v[196:199], v[28:31], v[132:135], v[196:199]\n\t"
      "s_waitcnt vmcnt(5)\n\t"
      "v_mfma_f32_16x16x32_bf16 v[184:187], v[32:35], v[136:139], v[184:187]\n\t"
      "s_waitcnt vmcnt(4)\n\t"
      "v_mfma_f32_16x16x32_bf16 v[184:187], v[48:51], v[140:143], v[184:187]\n\t"
      "v_mfma_f32_16x16x32_bf16 v[188:191], v[36:39], v[136:139], v[188:191]\n\t"
      "v_mfma_f32_16x16x32_bf16 v[188:191], v[52:55], v[140:143], v[188:191]\n\t"
      "v_mfma_f32_16x16x32_bf16 v[192:195], v[40:43], v[136:139], v[192:195]\n\t"
      "v_mfma_f32_16x16x32_bf16 v[192:195], v[56:59], v[140:143], v[192:195]\n\t"
      "v_mfma_f32_16x16x32_bf16 v[196:199], v[44:47], v[136:139], v[196:199]\n\t"
      "v_mfma_f32_16x16x32_bf16 v[196:199], v[60:63], v[140:143], v[196:199]\n\t"
      "s_waitcnt vmcnt(3)\n\t"
      "v_mfma_f32_16x16x32_bf16 v[184:187], v[64:67], v[144:147], v[184:187]\n\t"
      "s_waitcnt vmcnt(2)\n\t"
      "v_mfma_f32_16x16x32_bf16 v[184:187], v[80:83], v[148:151], v[184:187]\n\t"
      "v_mfma_f32_16x16x32_bf16 v[188:191], v[68:71], v[144:147], v[188:191]\n\t"
      "v_mfma_f32_16x16x32_bf16 v[188:191], v[84:87], v[148:151], v[188:191]\n\t"
      "v_mfma_f32_16x16x32_bf16 v[192:195], v[72:75], v[144:147], v[192:195]\n\t"
      "v_mfma_f32_16x16x32_bf16 v[192:195], v[88:91], v[148:151], v[192:195]\n\t"
      "s_waitcnt lgkmcnt(0)\n\t"
      "s_barrier\n\t"
      "s_setprio 2\n\t"
      "v_mfma_f32_16x16x32_bf16 v[196:199], v[76:79], v[144:147], v[196:199]\n\t"
      "v_mfma_f32_16x16x32_bf16 v[196:199], v[92:95], v[148:151], v[196:199]\n\t"
      "s_waitcnt vmcnt(1)\n\t"
      "v_mfma_f32_16x16x32_bf16 v[184:187], v[96:99], v[152:155], v[184:187]\n\t"
      "s_waitcnt vmcnt(0)\n\t"
      "v_mfma_f32_16x16x32_bf16 v[184:187], v[112:115], v[156:159], v[184:187]\n\t"
      "v_mfma_f32_16x16x32_bf16 v[188:191], v[100:103], v[152:155], v[188:191]\n\t"
      "v_mfma_f32_16x16x32_bf16 v[188:191], v[116:119], v[156:159], v[188:191]\n\t"
      "v_mfma_f32_16x16x32_bf16 v[192:195], v[104:107], v[152:155], v[192:195]\n\t"
      "v_mfma_f32_16x16x32_bf16 v[192:195], v[120:123], v[156:159], v[192:195]\n\t"
      "v_mfma_f32_16x16x32_bf16 v[196:199], v[108:111], v[152:155], v[196:199]\n\t"
      "v_mfma_f32_16x16x32_bf16 v[196:199], v[124:127], v[156:159], v[196:199]\n\t"
      "s_nop 1\n\t"
      "v_cmp_u_f32_e32 vcc, v184, v184\n\t"
      "s_cbranch_vccnz NQ6R_%=\n\t"
      "NQ6B_%=:\n\t"
      "ds_write_b128 v243, v[184:187]\n\t"
      "ds_write_b128 v164, v[188:191]\n\t"
      "ds_write_b128 v165, v[192:195]\n\t"
      "s_nop 0\n\t"
      "ds_write_b128 v166, v[196:199]\n\t"
      "s_waitcnt lgkmcnt(0)\n\t"
      "s_barrier\n\t"
      "s_branch GX_%=\n\t"
      "ASP_%=:\n\t"
      "s_mov_b32 m0, 163328\n\t"
      "s_cmp_ge_u32 s17, 65536\n\t"
      "s_cbranch_scc0 BOT_%=\n\t"
      "s_barrier\n\t"
      "BOT_%=:\n\t"
      "v_add_u32_e32 v1, 229376, v241\n\t"
      "v_add_u32_e32 v2, 32768, v246\n\t"
      "global_load_lds_dword v241, s[4:5]\n\t"
      "global_load_lds_dword v1, s[4:5]\n\t"
      "s_waitcnt vmcnt(0)\n\t"
      "s_sleep 1\n\t"
      "s_getpc_b64 s[44:45]\n\t"
      "s_add_u32 s44, s44, _Z11fused_blockPKcS0_S0_S0_S0_S0_S0_PcS1_@rel32@lo+4\n\t"
      "s_addc_u32 s45, s45, _Z11fused_blockPKcS0_S0_S0_S0_S0_S0_PcS1_@rel32@hi+12\n\t"
      "s_lshr_b32 s46, s17, 8\n\t"
      "v_lshrrev_b32_e32 v0, 4, v240\n\t"
      "v_add_u32_e32 v0, s46, v0\n\t"
      "v_min_u32_e32 v0, 495, v0\n\t"
      "v_lshlrev_b32_e32 v0, 7, v0\n\t"
      "global_load_lds_dword v0, s[44:45] sc1\n\t"
      "s_branch AEND_%=\n\t"
      "FQ0R_%=:\n\t"
      "s_mov_b32 s51, 0\n\t"
      "FQ0L_%=:\n\t"
      "s_add_u32 s51, s51, 1\n\t"
      "s_cmp_lt_u32 s51, 262144\n\t"
      "s_cbranch_scc0 FQ0X_%=\n\t"
      "s_waitcnt vmcnt(0)\n\t"
      "s_sleep 1\n\t"
      "global_load_dwordx4 v[128:131], v176, s[52:53] sc1\n\t"
      "global_load_dwordx4 v[132:135], v177, s[52:53] sc1\n\t"
      "global_load_dwordx4 v[136:139], v178, s[52:53] sc1\n\t"
      "global_load_dwordx4 v[140:143], v179, s[52:53] sc1\n\t"
      "global_load_dwordx4 v[144:147], v180, s[52:53] sc1\n\t"
      "global_load_dwordx4 v[148:151], v181, s[52:53] sc1\n\t"
      "global_load_dwordx4 v[152:155], v182, s[52:53] sc1\n\t"
      "global_load_dwordx4 v[156:159], v183, s[52:53] sc1\n\t"
      "s_waitcnt vmcnt(7)\n\t"
      "v_max3_u32 v234, v128, v129, v130\n\t"
      "v_max_u32_e32 v234, v234, v131\n\t"
      "v_cmp_eq_u32_e32 vcc, -1, v234\n\t"
      "s_cbranch_vccnz FQ0L_%=\n\t"
      "FQ0X_%=:\n\t"
      "s_branch FQ0B_%=\n\t"
      "FQ1R_%=:\n\t"
      "s_mov_b32 s51, 0\n\t"
      "FQ1L_%=:\n\t"
      "s_add_u32 s51, s51, 1\n\t"
      "s_cmp_lt_u32 s51, 262144\n\t"
      "s_cbranch_scc0 FQ1X_%=\n\t"
      "s_waitcnt vmcnt(0)\n\t"
      "s_sleep 1\n\t"
      "global_load_dwordx4 v[132:135], v177, s[52:53] sc1\n\t"
      "global_load_dwordx4 v[136:139], v178, s[52:53] sc1\n\t"
      "global_load_dwordx4 v[140:143], v179, s[52:53] sc1\n\t"
      "global_load_dwordx4 v[144:147], v180, s[52:53] sc1\n\t"
      "global_load_dwordx4 v[148:151], v181, s[52:53] sc1\n\t"
      "global_load_dwordx4 v[152:155], v182, s[52:53] sc1\n\t"
      "global_load_dwordx4 v[156:159], v183, s[52:53] sc1\n\t"
      "s_waitcnt vmcnt(6)\n\t"
      "v_max3_u32 v234, v132, v133, v134\n\t"
      "v_max_u32_e32 v234, v234, v135\n\t"
      "v_cmp_eq_u32_e32 vcc, -1, v234\n\t"
      "s_cbranch_vccnz FQ1L_%=\n\t"
      "FQ1X_%=:\n\t"
      "s_branch FQ1B_%=\n\t"
      "FQ2R_%=:\n\t"
      "s_mov_b32 s51, 0\n\t"
      "FQ2L_%=:\n\t"
      "s_add_u32 s51, s51, 1\n\t"
      "s_cmp_lt_u32 s51, 262144\n\t"
      "s_cbranch_scc0 FQ2X_%=\n\t"
      "s_waitcnt vmcnt(0)\n\t"
      "s_sleep 1\n\t"
      "global_load_dwordx4 v[136:139], v178, s[52:53] sc1\n\t"
      "global_load_dwordx4 v[140:143], v179, s[52:53] sc1\n\t"
      "global_load_dwordx4 v[144:147], v180, s[52:53] sc1\n\t"
      "global_load_dwordx4 v[148:151], v181, s[52:53] sc1\n\t"
      "global_load_dwordx4 v[152:155], v182, s[52:53] sc1\n\t"
      "global_load_dwordx4 v[156:159], v183, s[52:53] sc1\n\t"
      "global_load_dwordx4 v[128:131], v176, s[54:55] sc1\n\t"
      "global_load_dwordx4 v[132:135], v177, s[54:55] sc1\n\t"
      "s_waitcnt vmcnt(7)\n\t"
      "v_max3_u32 v234, v136, v137, v138\n\t"
      "v_max_u32_e32 v234, v234, v139\n\t"
      "v_cmp_eq_u32_e32 vcc, -1, v234\n\t"
      "s_cbranch_vccnz FQ2L_%=\n\t"
      "FQ2X_%=:\n\t"
      "s_branch FQ2B_%=\n\t"
      "FQ3R_%=:\n\t"
      "s_mov_b32 s51, 0\n\t"
      "FQ3L_%=:\n\t"
      "s_add_u32 s51, s51, 1\n\t"
      "s_cmp_lt_u32 s51, 262144\n\t"
      "s_cbranch_scc0 FQ3X_%=\n\t"
      "s_waitcnt vmcnt(0)\n\t"
      "s_sleep 1\n\t"
      "global_load_dwordx4 v[140:143], v179, s[52:53] sc1\n\t"
      "global_load_dwordx4 v[144:147], v180, s[52:53] sc1\n\t"
      "global_load_dwordx4 v[148:151], v181, s[52:53] sc1\n\t"
      "global_load_dwordx4 v[152:155], v182, s[52:53] sc1\n\t"
      "global_load_dwordx4 v[156:159], v183, s[52:53] sc1\n\t"
      "global_load_dwordx4 v[128:131], v176, s[54:55] sc1\n\t"
      "global_load_dwordx4 v[132:135], v177, s[54:55] sc1\n\t"
      "s_waitcnt vmcnt(6)\n\t"
      "v_max3_u32 v234, v140, v141, v142\n\t"
      "v_max_u32_e32 v234, v234, v143\n\t"
      "v_cmp_eq_u32_e32 vcc, -1, v234\n\t"
      "s_cbranch_vccnz FQ3L_%=\n\t"
      "FQ3X_%=:\n\t"
      "s_branch FQ3B_%=\n\t"
      "FQ4R_%=:\n\t"
      "s_mov_b32 s51, 0\n\t"
      "FQ4L_%=:\n\t"
      "s_add_u32 s51, s51, 1\n\t"
      "s_cmp_lt_u32 s51, 262144\n\t"
      "s_cbranch_scc0 FQ4X_%=\n\t"
      "s_waitcnt vmcnt(0)\n\t"
      "s_sleep 1\n\t"
      "global_load_dwordx4 v[144:147], v180, s[52:53] sc1\n\t"
      "global_load_dwordx4 v[148:151], v181, s[52:53] sc1\n\t"
      "global_load_dwordx4 v[152:155], v182, s[52:53] sc1\n\t"
      "global_load_dwordx4 v[156:159], v183, s[52:53] sc1\n\t"
      "global_load_dwordx4 v[128:131], v176, s[54:55] sc1\n\t"
      "global_load_dwordx4 v[132:135], v177, s[54:55] sc1\n\t"
      "global_load_dwordx4 v[136:139], v178, s[54:55] sc1\n\t"
      "global_load_dwordx4 v[140:143], v179, s[54:55] sc1\n\t"
      "s_waitcnt vmcnt(7)\n\t"
      "v_max3_u32 v234, v144, v145, v146\n\t"
      "v_max_u32_e32 v234, v234, v147\n\t"
      "v_cmp_eq_u32_e32 vcc, -1, v234\n\t"
      "s_cbranch_vccnz FQ4L_%=\n\t"
      "FQ4X_%=:\n\t"
      "s_branch FQ4B_%=\n\t"
      "FQ5R_%=:\n\t"
      "s_mov_b32 s51, 0\n\t"
      "FQ5L_%=:\n\t"
      "s_add_u32 s51, s51, 1\n\t"
      "s_cmp_lt_u32 s51, 262144\n\t"
      "s_cbranch_scc0 FQ5X_%=\n\t"
      "s_waitcnt vmcnt(0)\n\t"
      "s_sleep 1\n\t"
      "global_load_dwordx4 v[148:151], v181, s[52:53] sc1\n\t"
      "global_load_dwordx4 v[152:155], v182, s[52:53] sc1\n\t"
      "global_load_dwordx4 v[156:159], v183, s[52:53] sc1\n\t"
      "global_load_dwordx4 v[128:131], v176, s[54:55] sc1\n\t"
      "global_load_dwordx4 v[132:135], v177, s[54:55] sc1\n\t"
      "global_load_dwordx4 v[136:139], v178, s[54:55] sc1\n\t"
      "global_load_dwordx4 v[140:143], v179, s[54:55] sc1\n\t"
      "s_waitcnt vmcnt(6)\n\t"
      "v_max3_u32 v234, v148, v149, v150\n\t"
      "v_max_u32_e32 v234, v234, v151\n\t"
      "v_cmp_eq_u32_e32 vcc, -1, v234\n\t"
      "s_cbranch_vccnz FQ5L_%=\n\t"
      "FQ5X_%=:\n\t"
      "s_branch FQ5B_%=\n\t"
      "FQ6R_%=:\n\t"
      "s_mov_b32 s51, 0\n\t"
      "FQ6L_%=:\n\t"
      "s_add_u32 s51, s51, 1\n\t"
      "s_cmp_lt_u32 s51, 262144\n\t"
      "s_cbranch_scc0 FQ6X_%=\n\t"
      "s_waitcnt vmcnt(0)\n\t"
      "s_sleep 1\n\t"
      "global_load_dwordx4 v[152:155], v182, s[52:53] sc1\n\t"
      "global_load_dwordx4 v[156:159], v183, s[52:53] sc1\n\t"
      "global_load_dwordx4 v[128:131], v176, s[54:55] sc1\n\t"
      "global_load_dwordx4 v[132:135], v177, s[54:55] sc1\n\t"
      "global_load_dwordx4 v[136:139], v178, s[54:55] sc1\n\t"
      "global_load_dwordx4 v[140:143], v179, s[54:55] sc1\n\t"
      "global_load_dwordx4 v[144:147], v180, s[54:55] sc1\n\t"
      "global_load_dwordx4 v[148:151], v181, s[54:55] sc1\n\t"
      "s_waitcnt vmcnt(7)\n\t"
      "v_max3_u32 v234, v152, v153, v154\n\t"
      "v_max_u32_e32 v234, v234, v155\n\t"
      "v_cmp_eq_u32_e32 vcc, -1, v234\n\t"
      "s_cbranch_vccnz FQ6L_%=\n\t"
      "FQ6X_%=:\n\t"
      "s_branch FQ6B_%=\n\t"
      "FQ7R_%=:\n\t"
      "s_mov_b32 s51, 0\n\t"
      "FQ7L_%=:\n\t"
      "s_add_u32 s51, s51, 1\n\t"
      "s_cmp_lt_u32 s51, 262144\n\t"
      "s_cbranch_scc0 FQ7X_%=\n\t"
      "s_waitcnt vmcnt(0)\n\t"
      "s_sleep 1\n\t"
      "global_load_dwordx4 v[156:159], v183, s[52:53] sc1\n\t"
      "global_load_dwordx4 v[128:131], v176, s[54:55] sc1\n\t"
      "global_load_dwordx4 v[132:135], v177, s[54:55] sc1\n\t"
      "global_load_dwordx4 v[136:139], v178, s[54:55] sc1\n\t"
      "global_load_dwordx4 v[140:143], v179, s[54:55] sc1\n\t"
      "global_load_dwordx4 v[144:147], v180, s[54:55] sc1\n\t"
      "global_load_dwordx4 v[148:151], v181, s[54:55] sc1\n\t"
      "s_waitcnt vmcnt(6)\n\t"
      "v_max3_u32 v234, v156, v157, v158\n\t"
      "v_max_u32_e32 v234, v234, v159\n\t"
      "v_cmp_eq_u32_e32 vcc, -1, v234\n\t"
      "s_cbranch_vccnz FQ7L_%=\n\t"
      "FQ7X_%=:\n\t"
      "s_branch FQ7B_%=\n\t"
      "NQ1R_%=:\n\t"
      "s_mov_b32 s51, 0\n\t"
      "NQ1L_%=:\n\t"
      "s_add_u32 s51, s51, 1\n\t"
      "s_cmp_lt_u32 s51, 262144\n\t"
      "s_cbranch_scc0 NQ1X_%=\n\t"
      "s_waitcnt vmcnt(0)\n\t"
      "s_sleep 1\n\t"
      "global_load_dwordx4 v[128:131], v176, s[54:55] sc1\n\t"
      "global_load_dwordx4 v[132:135], v177, s[54:55] sc1\n\t"
      "global_load_dwordx4 v[136:139], v178, s[54:55] sc1\n\t"
      "global_load_dwordx4 v[140:143], v179, s[54:55] sc1\n\t"
      "global_load_dwordx4 v[144:147], v180, s[54:55] sc1\n\t"
      "global_load_dwordx4 v[148:151], v181, s[54:55] sc1\n\t"
      "global_load_dwordx4 v[152:155], v182, s[54:55] sc1\n\t"
      "global_load_dwordx4 v[156:159], v183, s[54:55] sc1\n\t"
      "s_waitcnt vmcnt(0)\n\t"
      "v_max3_u32 v234, v128, v129, v130\n\t"
      "v_max3_u32 v234, v234, v131, v132\n\t"
      "v_max3_u32 v234, v234, v133, v134\n\t"
      "v_max3_u32 v234, v234, v135, v136\n\t"
      "v_max3_u32 v234, v234, v137, v138\n\t"
      "v_max3_u32 v234, v234, v139, v140\n\t"
      "v_max3_u32 v234, v234, v141, v142\n\t"
      "v_max3_u32 v234, v234, v143, v144\n\t"
      "v_max3_u32 v234, v234, v145, v146\n\t"
      "v_max3_u32 v234, v234, v147, v148\n\t"
      "v_max3_u32 v234, v234, v149, v150\n\t"
      "v_max3_u32 v234, v234, v151, v152\n\t"
      "v_max3_u32 v234, v234, v153, v154\n\t"
      "v_max3_u32 v234, v234, v155, v156\n\t"
      "v_max3_u32 v234, v234, v157, v158\n\t"
      "v_max_u32_e32 v234, v234, v159\n\t"
      "v_cmp_eq_u32_e32 vcc, -1, v234\n\t"
      "s_cbranch_vccnz NQ1L_%=\n\t"
      "NQ1X_%=:\n\t"
      "v_mfma_f32_16x16x32_bf16 v[200:203], v[0:3], v[128:131], v[236:239]\n\t"
      "v_mfma_f32_16x16x32_bf16 v[204:207], v[4:7], v[128:131], 0\n\t"
      "v_mfma_f32_16x16x32_bf16 v[208:211], v[8:11], v[128:131], 0\n\t"
      "v_mfma_f32_16x16x32_bf16 v[212:215], v[12:15], v[128:131], 0\n\t"
      "v_mfma_f32_16x16x32_bf16 v[200:203], v[16:19], v[132:135], v[200:203]\n\t"
      "v_mfma_f32_16x16x32_bf16 v[204:207], v[20:23], v[132:135], v[204:207]\n\t"
      "v_mfma_f32_16x16x32_bf16 v[208:211], v[24:27], v[132:135], v[208:211]\n\t"
      "v_mfma_f32_16x16x32_bf16 v[212:215], v[28:31], v[132:135], v[212:215]\n\t"
      "v_mfma_f32_16x16x32_bf16 v[200:203], v[32:35], v[136:139], v[200:203]\n\t"
      "v_mfma_f32_16x16x32_bf16 v[204:207], v[36:39], v[136:139], v[204:207]\n\t"
      "v_mfma_f32_16x16x32_bf16 v[208:211], v[40:43], v[136:139], v[208:211]\n\t"
      "v_mfma_f32_16x16x32_bf16 v[212:215], v[44:47], v[136:139], v[212:215]\n\t"
      "v_mfma_f32_16x16x32_bf16 v[200:203], v[48:51], v[140:143], v[200:203]\n\t"
      "v_mfma_f32_16x16x32_bf16 v[204:207], v[52:55], v[140:143], v[204:207]\n\t"
      "v_mfma_f32_16x16x32_bf16 v[208:211], v[56:59], v[140:143], v[208:211]\n\t"
      "v_mfma_f32_16x16x32_bf16 v[212:215], v[60:63], v[140:143], v[212:215]\n\t"
      "v_mfma_f32_16x16x32_bf16 v[200:203], v[64:67], v[144:147], v[200:203]\n\t"
      "v_mfma_f32_16x16x32_bf16 v[204:207], v[68:71], v[144:147], v[204:207]\n\t"
      "v_mfma_f32_16x16x32_bf16 v[208:211], v[72:75], v[144:147], v[208:211]\n\t"
      "v_mfma_f32_16x16x32_bf16 v[212:215], v[76:79], v[144:147], v[212:215]\n\t"
      "v_mfma_f32_16x16x32_bf16 v[200:203], v[80:83], v[148:151], v[200:203]\n\t"
      "v_mfma_f32_16x16x32_bf16 v[204:207], v[84:87], v[148:151], v[204:207]\n\t"
      "v_mfma_f32_16x16x32_bf16 v[208:211], v[88:91], v[148:151], v[208:211]\n\t"
      "v_mfma_f32_16x16x32_bf16 v[212:215], v[92:95], v[148:151], v[212:215]\n\t"
      "v_mfma_f32_16x16x32_bf16 v[200:203], v[96:99], v[152:155], v[200:203]\n\t"
      "v_mfma_f32_16x16x32_bf16 v[204:207], v[100:103], v[152:155], v[204:207]\n\t"
      "v_mfma_f32_16x16x32_bf16 v[208:211], v[104:107], v[152:155], v[208:211]\n\t"
      "v_mfma_f32_16x16x32_bf16 v[212:215], v[108:111], v[152:155], v[212:215]\n\t"
      "v_mfma_f32_16x16x32_bf16 v[200:203], v[112:115], v[156:159], v[200:203]\n\t"
      "v_mfma_f32_16x16x32_bf16 v[204:207], v[116:119], v[156:159], v[204:207]\n\t"
      "v_mfma_f32_16x16x32_bf16 v[208:211], v[120:123], v[156:159], v[208:211]\n\t"
      "v_mfma_f32_16x16x32_bf16 v[212:215], v[124:127], v[156:159], v[212:215]\n\t"
      "global_load_dwordx4 v[128:131], v176, s[56:57] sc1\n\t"
      "global_load_dwordx4 v[132:135], v177, s[56:57] sc1\n\t"
      "global_load_dwordx4 v[136:139], v178, s[56:57] sc1\n\t"
      "global_load_dwordx4 v[140:143], v179, s[56:57] sc1\n\t"
      "global_load_dwordx4 v[144:147], v180, s[56:57] sc1\n\t"
      "global_load_dwordx4 v[148:151], v181, s[56:57] sc1\n\t"
      "global_load_dwordx4 v[152:155], v182, s[56:57] sc1\n\t"
      "global_load_dwordx4 v[156:159], v183, s[56:57] sc1\n\t"
      "s_waitcnt vmcnt(0)\n\t"
      "s_nop 7\n\t"
      "s_branch NQ1B_%=\n\t"
      "NQ2R_%=:\n\t"
      "s_mov_b32 s51, 0\n\t"
      "NQ2L_%=:\n\t"
      "s_add_u32 s51, s51, 1\n\t"
      "s_cmp_lt_u32 s51, 262144\n\t"
      "s_cbranch_scc0 NQ2X_%=\n\t"
      "s_waitcnt vmcnt(0)\n\t"
      "s_sleep 1\n\t"
      "global_load_dwordx4 v[128:131], v176, s[56:57] sc1\n\t"
      "global_load_dwordx4 v[132:135], v177, s[56:57] sc1\n\t"
      "global_load_dwordx4 v[136:139], v178, s[56:57] sc1\n\t"
      "global_load_dwordx4 v[140:143], v179, s[56:57] sc1\n\t"
      "global_load_dwordx4 v[144:147], v180, s[56:57] sc1\n\t"
      "global_load_dwordx4 v[148:151], v181, s[56:57] sc1\n\t"
      "global_load_dwordx4 v[152:155], v182, s[56:57] sc1\n\t"
      "global_load_dwordx4 v[156:159], v183, s[56:57] sc1\n\t"
      "s_waitcnt vmcnt(0)\n\t"
      "v_max3_u32 v234, v128, v129, v130\n\t"
      "v_max3_u32 v234, v234, v131, v132\n\t"
      "v_max3_u32 v234, v234, v133, v134\n\t"
      "v_max3_u32 v234, v234, v135, v136\n\t"
      "v_max3_u32 v234, v234, v137, v138\n\t"
      "v_max3_u32 v234, v234, v139, v140\n\t"
      "v_max3_u32 v234, v234, v141, v142\n\t"
      "v_max3_u32 v234, v234, v143, v144\n\t"
      "v_max3_u32 v234, v234, v145, v146\n\t"
      "v_max3_u32 v234, v234, v147, v148\n\t"
      "v_max3_u32 v234, v234, v149, v150\n\t"
      "v_max3_u32 v234, v234, v151, v152\n\t"
      "v_max3_u32 v234, v234, v153, v154\n\t"
      "v_max3_u32 v234, v234, v155, v156\n\t"
      "v_max3_u32 v234, v234, v157, v158\n\t"
      "v_max_u32_e32 v234, v234, v159\n\t"
      "v_cmp_eq_u32_e32 vcc, -1, v234\n\t"
      "s_cbranch_vccnz NQ2L_%=\n\t"
      "NQ2X_%=:\n\t"
      "v_mfma_f32_16x16x32_bf16 v[184:187], v[0:3], v[128:131], v[236:239]\n\t"
      "v_mfma_f32_16x16x32_bf16 v[188:191], v[4:7], v[128:131], 0\n\t"
      "v_mfma_f32_16x16x32_bf16 v[192:195], v[8:11], v[128:131], 0\n\t"
      "v_mfma_f32_16x16x32_bf16 v[196:199], v[12:15], v[128:131], 0\n\t"
      "v_mfma_f32_16x16x32_bf16 v[184:187], v[16:19], v[132:135], v[184:187]\n\t"
      "v_mfma_f32_16x16x32_bf16 v[188:191], v[20:23], v[132:135], v[188:191]\n\t"
      "v_mfma_f32_16x16x32_bf16 v[192:195], v[24:27], v[132:135], v[192:195]\n\t"
      "v_mfma_f32_16x16x32_bf16 v[196:199], v[28:31], v[132:135], v[196:199]\n\t"
      "v_mfma_f32_16x16x32_bf16 v[184:187], v[32:35], v[136:139], v[184:187]\n\t"
      "v_mfma_f32_16x16x32_bf16 v[188:191], v[36:39], v[136:139], v[188:191]\n\t"
      "v_mfma_f32_16x16x32_bf16 v[192:195], v[40:43], v[136:139], v[192:195]\n\t"
      "v_mfma_f32_16x16x32_bf16 v[196:199], v[44:47], v[136:139], v[196:199]\n\t"
      "v_mfma_f32_16x16x32_bf16 v[184:187], v[48:51], v[140:143], v[184:187]\n\t"
      "v_mfma_f32_16x16x32_bf16 v[188:191], v[52:55], v[140:143], v[188:191]\n\t"
      "v_mfma_f32_16x16x32_bf16 v[192:195], v[56:59], v[140:143], v[192:195]\n\t"
      "v_mfma_f32_16x16x32_bf16 v[196:199], v[60:63], v[140:143], v[196:199]\n\t"
      "v_mfma_f32_16x16x32_bf16 v[184:187], v[64:67], v[144:147], v[184:187]\n\t"
      "v_mfma_f32_16x16x32_bf16 v[188:191], v[68:71], v[144:147], v[188:191]\n\t"
      "v_mfma_f32_16x16x32_bf16 v[192:195], v[72:75], v[144:147], v[192:195]\n\t"
      "v_mfma_f32_16x16x32_bf16 v[196:199], v[76:79], v[144:147], v[196:199]\n\t"
      "v_mfma_f32_16x16x32_bf16 v[184:187], v[80:83], v[148:151], v[184:187]\n\t"
      "v_mfma_f32_16x16x32_bf16 v[188:191], v[84:87], v[148:151], v[188:191]\n\t"
      "v_mfma_f32_16x16x32_bf16 v[192:195], v[88:91], v[148:151], v[192:195]\n\t"
      "v_mfma_f32_16x16x32_bf16 v[196:199], v[92:95], v[148:151], v[196:199]\n\t"
      "v_mfma_f32_16x16x32_bf16 v[184:187], v[96:99], v[152:155], v[184:187]\n\t"
      "v_mfma_f32_16x16x32_bf16 v[188:191], v[100:103], v[152:155], v[188:191]\n\t"
      "v_mfma_f32_16x16x32_bf16 v[192:195], v[104:107], v[152:155], v[192:195]\n\t"
      "v_mfma_f32_16x16x32_bf16 v[196:199], v[108:111], v[152:155], v[196:199]\n\t"
      "v_mfma_f32_16x16x32_bf16 v[184:187], v[112:115], v[156:159], v[184:187]\n\t"
      "v_mfma_f32_16x16x32_bf16 v[188:191], v[116:119], v[156:159], v[188:191]\n\t"
      "v_mfma_f32_16x16x32_bf16 v[192:195], v[120:123], v[156:159], v[192:195]\n\t"
      "v_mfma_f32_16x16x32_bf16 v[196:199], v[124:127], v[156:159], v[196:199]\n\t"
      "global_load_dwordx4 v[128:131], v176, s[58:59] sc1\n\t"
      "global_load_dwordx4 v[132:135], v177, s[58:59] sc1\n\t"
      "global_load_dwordx4 v[136:139], v178, s[58:59] sc1\n\t"
      "global_load_dwordx4 v[140:143], v179, s[58:59] sc1\n\t"
      "global_load_dwordx4 v[144:147], v180, s[58:59] sc1\n\t"
      "global_load_dwordx4 v[148:151], v181, s[58:59] sc1\n\t"
      "global_load_dwordx4 v[152:155], v182, s[58:59] sc1\n\t"
      "global_load_dwordx4 v[156:159], v183, s[58:59] sc1\n\t"
      "s_waitcnt vmcnt(0)\n\t"
      "s_nop 7\n\t"
      "s_branch NQ2B_%=\n\t"
      "NQ3R_%=:\n\t"
      "s_mov_b32 s51, 0\n\t"
      "NQ3L_%=:\n\t"
      "s_add_u32 s51, s51, 1\n\t"
      "s_cmp_lt_u32 s51, 262144\n\t"
      "s_cbranch_scc0 NQ3X_%=\n\t"
      "s_waitcnt vmcnt(0)\n\t"
      "s_sleep 1\n\t"
      "global_load_dwordx4 v[128:131], v176, s[58:59] sc1\n\t"
      "global_load_dwordx4 v[132:135], v177, s[58:59] sc1\n\t"
      "global_load_dwordx4 v[136:139], v178, s[58:59] sc1\n\t"
      "global_load_dwordx4 v[140:143], v179, s[58:59] sc1\n\t"
      "global_load_dwordx4 v[144:147], v180, s[58:59] sc1\n\t"
      "global_load_dwordx4 v[148:151], v181, s[58:59] sc1\n\t"
      "global_load_dwordx4 v[152:155], v182, s[58:59] sc1\n\t"
      "global_load_dwordx4 v[156:159], v183, s[58:59] sc1\n\t"
      "s_waitcnt vmcnt(0)\n\t"
      "v_max3_u32 v234, v128, v129, v130\n\t"
      "v_max3_u32 v234, v234, v131, v132\n\t"
      "v_max3_u32 v234, v234, v133, v134\n\t"
      "v_max3_u32 v234, v234, v135, v136\n\t"
      "v_max3_u32 v234, v234, v137, v138\n\t"
      "v_max3_u32 v234, v234, v139, v140\n\t"
      "v_max3_u32 v234, v234, v141, v142\n\t"
      "v_max3_u32 v234, v234, v143, v144\n\t"
      "v_max3_u32 v234, v234, v145, v146\n\t"
      "v_max3_u32 v234, v234, v147, v148\n\t"
      "v_max3_u32 v234, v234, v149, v150\n\t"
      "v_max3_u32 v234, v234, v151, v152\n\t"
      "v_max3_u32 v234, v234, v153, v154\n\t"
      "v_max3_u32 v234, v234, v155, v156\n\t"
      "v_max3_u32 v234, v234, v157, v158\n\t"
      "v_max_u32_e32 v234, v234, v159\n\t"
      "v_cmp_eq_u32_e32 vcc, -1, v234\n\t"
      "s_cbranch_vccnz NQ3L_%=\n\t"
      "NQ3X_%=:\n\t"
      "v_mfma_f32_16x16x32_bf16 v[200:203], v[0:3], v[128:131], v[236:239]\n\t"
      "v_mfma_f32_16x16x32_bf16 v[204:207], v[4:7], v[128:131], 0\n\t"
      "v_mfma_f32_16x16x32_bf16 v[208:211], v[8:11], v[128:131], 0\n\t"
      "v_mfma_f32_16x16x32_bf16 v[212:215], v[12:15], v[128:131], 0\n\t"
      "v_mfma_f32_16x16x32_bf16 v[200:203], v[16:19], v[132:135], v[200:203]\n\t"
      "v_mfma_f32_16x16x32_bf16 v[204:207], v[20:23], v[132:135], v[204:207]\n\t"
      "v_mfma_f32_16x16x32_bf16 v[208:211], v[24:27], v[132:135], v[208:211]\n\t"
      "v_mfma_f32_16x16x32_bf16 v[212:215], v[28:31], v[132:135], v[212:215]\n\t"
      "v_mfma_f32_16x16x32_bf16 v[200:203], v[32:35], v[136:139], v[200:203]\n\t"
      "v_mfma_f32_16x16x32_bf16 v[204:207], v[36:39], v[136:139], v[204:207]\n\t"
      "v_mfma_f32_16x16x32_bf16 v[208:211], v[40:43], v[136:139], v[208:211]\n\t"
      "v_mfma_f32_16x16x32_bf16 v[212:215], v[44:47], v[136:139], v[212:215]\n\t"
      "v_mfma_f32_16x16x32_bf16 v[200:203], v[48:51], v[140:143], v[200:203]\n\t"
      "v_mfma_f32_16x16x32_bf16 v[204:207], v[52:55], v[140:143], v[204:207]\n\t"
      "v_mfma_f32_16x16x32_bf16 v[208:211], v[56:59], v[140:143], v[208:211]\n\t"
      "v_mfma_f32_16x16x32_bf16 v[212:215], v[60:63], v[140:143], v[212:215]\n\t"
      "v_mfma_f32_16x16x32_bf16 v[200:203], v[64:67], v[144:147], v[200:203]\n\t"
      "v_mfma_f32_16x16x32_bf16 v[204:207], v[68:71], v[144:147], v[204:207]\n\t"
      "v_mfma_f32_16x16x32_bf16 v[208:211], v[72:75], v[144:147], v[208:211]\n\t"
      "v_mfma_f32_16x16x32_bf16 v[212:215], v[76:79], v[144:147], v[212:215]\n\t"
      "v_mfma_f32_16x16x32_bf16 v[200:203], v[80:83], v[148:151], v[200:203]\n\t"
      "v_mfma_f32_16x16x32_bf16 v[204:207], v[84:87], v[148:151], v[204:207]\n\t"
      "v_mfma_f32_16x16x32_bf16 v[208:211], v[88:91], v[148:151], v[208:211]\n\t"
      "v_mfma_f32_16x16x32_bf16 v[212:215], v[92:95], v[148:151], v[212:215]\n\t"
      "v_mfma_f32_16x16x32_bf16 v[200:203], v[96:99], v[152:155], v[200:203]\n\t"
      "v_mfma_f32_16x16x32_bf16 v[204:207], v[100:103], v[152:155], v[204:207]\n\t"
      "v_mfma_f32_16x16x32_bf16 v[208:211], v[104:107], v[152:155], v[208:211]\n\t"
      "v_mfma_f32_16x16x32_bf16 v[212:215], v[108:111], v[152:155], v[212:215]\n\t"
      "v_mfma_f32_16x16x32_bf16 v[200:203], v[112:115], v[156:159], v[200:203]\n\t"
      "v_mfma_f32_16x16x32_bf16 v[204:207], v[116:119], v[156:159], v[204:207]\n\t"
      "v_mfma_f32_16x16x32_bf16 v[208:211], v[120:123], v[156:159], v[208:211]\n\t"
      "v_mfma_f32_16x16x32_bf16 v[212:215], v[124:127], v[156:159], v[212:215]\n\t"
      "global_load_dwordx4 v[128:131], v176, s[60:61] sc1\n\t"
      "global_load_dwordx4 v[132:135], v177, s[60:61] sc1\n\t"
      "global_load_dwordx4 v[136:139], v178, s[60:61] sc1\n\t"
      "global_load_dwordx4 v[140:143], v179, s[60:61] sc1\n\t"
      "global_load_dwordx4 v[144:147], v180, s[60:61] sc1\n\t"
      "global_load_dwordx4 v[148:151], v181, s[60:61] sc1\n\t"
      "global_load_dwordx4 v[152:155], v182, s[60:61] sc1\n\t"
      "global_load_dwordx4 v[156:159], v183, s[60:61] sc1\n\t"
      "s_waitcnt vmcnt(0)\n\t"
      "s_nop 7\n\t"
      "s_branch NQ3B_%=\n\t"
      "NQ4R_%=:\n\t"
      "s_mov_b32 s51, 0\n\t"
      "NQ4L_%=:\n\t"
      "s_add_u32 s51, s51, 1\n\t"
      "s_cmp_lt_u32 s51, 262144\n\t"
      "s_cbranch_scc0 NQ4X_%=\n\t"
      "s_waitcnt vmcnt(0)\n\t"
      "s_sleep 1\n\t"
      "global_load_dwordx4 v[128:131], v176, s[60:61] sc1\n\t"
      "global_load_dwordx4 v[132:135], v177, s[60:61] sc1\n\t"
      "global_load_dwordx4 v[136:139], v178, s[60:61] sc1\n\t"
      "global_load_dwordx4 v[140:143], v179, s[60:61] sc1\n\t"
      "global_load_dwordx4 v[144:147], v180, s[60:61] sc1\n\t"
      "global_load_dwordx4 v[148:151], v181, s[60:61] sc1\n\t"
      "global_load_dwordx4 v[152:155], v182, s[60:61] sc1\n\t"
      "global_load_dwordx4 v[156:159], v183, s[60:61] sc1\n\t"
      "s_waitcnt vmcnt(0)\n\t"
      "v_max3_u32 v234, v128, v129, v130\n\t"
      "v_max3_u32 v234, v234, v131, v132\n\t"
      "v_max3_u32 v234, v234, v133, v134\n\t"
      "v_max3_u32 v234, v234, v135, v136\n\t"
      "v_max3_u32 v234, v234, v137, v138\n\t"
      "v_max3_u32 v234, v234, v139, v140\n\t"
      "v_max3_u32 v234, v234, v141, v142\n\t"
      "v_max3_u32 v234, v234, v143, v144\n\t"
      "v_max3_u32 v234, v234, v145, v146\n\t"
      "v_max3_u32 v234, v234, v147, v148\n\t"
      "v_max3_u32 v234, v234, v149, v150\n\t"
      "v_max3_u32 v234, v234, v151, v152\n\t"
      "v_max3_u32 v234, v234, v153, v154\n\t"
      "v_max3_u32 v234, v234, v155, v156\n\t"
      "v_max3_u32 v234, v234, v157, v158\n\t"
      "v_max_u32_e32 v234, v234, v159\n\t"
      "v_cmp_eq_u32_e32 vcc, -1, v234\n\t"
      "s_cbranch_vccnz NQ4L_%=\n\t"
      "NQ4X_%=:\n\t"
      "v_mfma_f32_16x16x32_bf16 v[184:187], v[0:3], v[128:131], v[236:239]\n\t"
      "v_mfma_f32_16x16x32_bf16 v[188:191], v[4:7], v[128:131], 0\n\t"
      "v_mfma_f32_16x16x32_bf16 v[192:195], v[8:11], v[128:131], 0\n\t"
      "v_mfma_f32_16x16x32_bf16 v[196:199], v[12:15], v[128:131], 0\n\t"
      "v_mfma_f32_16x16x32_bf16 v[184:187], v[16:19], v[132:135], v[184:187]\n\t"
      "v_mfma_f32_16x16x32_bf16 v[188:191], v[20:23], v[132:135], v[188:191]\n\t"
      "v_mfma_f32_16x16x32_bf16 v[192:195], v[24:27], v[132:135], v[192:195]\n\t"
      "v_mfma_f32_16x16x32_bf16 v[196:199], v[28:31], v[132:135], v[196:199]\n\t"
      "v_mfma_f32_16x16x32_bf16 v[184:187], v[32:35], v[136:139], v[184:187]\n\t"
      "v_mfma_f32_16x16x32_bf16 v[188:191], v[36:39], v[136:139], v[188:191]\n\t"
      "v_mfma_f32_16x16x32_bf16 v[192:195], v[40:43], v[136:139], v[192:195]\n\t"
      "v_mfma_f32_16x16x32_bf16 v[196:199], v[44:47], v[136:139], v[196:199]\n\t"
      "v_mfma_f32_16x16x32_bf16 v[184:187], v[48:51], v[140:143], v[184:187]\n\t"
      "v_mfma_f32_16x16x32_bf16 v[188:191], v[52:55], v[140:143], v[188:191]\n\t"
      "v_mfma_f32_16x16x32_bf16 v[192:195], v[56:59], v[140:143], v[192:195]\n\t"
      "v_mfma_f32_16x16x32_bf16 v[196:199], v[60:63], v[140:143], v[196:199]\n\t"
      "v_mfma_f32_16x16x32_bf16 v[184:187], v[64:67], v[144:147], v[184:187]\n\t"
      "v_mfma_f32_16x16x32_bf16 v[188:191], v[68:71], v[144:147], v[188:191]\n\t"
      "v_mfma_f32_16x16x32_bf16 v[192:195], v[72:75], v[144:147], v[192:195]\n\t"
      "v_mfma_f32_16x16x32_bf16 v[196:199], v[76:79], v[144:147], v[196:199]\n\t"
      "v_mfma_f32_16x16x32_bf16 v[184:187], v[80:83], v[148:151], v[184:187]\n\t"
      "v_mfma_f32_16x16x32_bf16 v[188:191], v[84:87], v[148:151], v[188:191]\n\t"
      "v_mfma_f32_16x16x32_bf16 v[192:195], v[88:91], v[148:151], v[192:195]\n\t"
      "v_mfma_f32_16x16x32_bf16 v[196:199], v[92:95], v[148:151], v[196:199]\n\t"
      "v_mfma_f32_16x16x32_bf16 v[184:187], v[96:99], v[152:155], v[184:187]\n\t"
      "v_mfma_f32_16x16x32_bf16 v[188:191], v[100:103], v[152:155], v[188:191]\n\t"
      "v_mfma_f32_16x16x32_bf16 v[192:195], v[104:107], v[152:155], v[192:195]\n\t"
      "v_mfma_f32_16x16x32_bf16 v[196:199], v[108:111], v[152:155], v[196:199]\n\t"
      "v_mfma_f32_16x16x32_bf16 v[184:187], v[112:115], v[156:159], v[184:187]\n\t"
      "v_mfma_f32_16x16x32_bf16 v[188:191], v[116:119], v[156:159], v[188:191]\n\t"
      "v_mfma_f32_16x16x32_bf16 v[192:195], v[120:123], v[156:159], v[192:195]\n\t"
      "v_mfma_f32_16x16x32_bf16 v[196:199], v[124:127], v[156:159], v[196:199]\n\t"
      "global_load_dwordx4 v[128:131], v176, s[62:63] sc1\n\t"
      "global_load_dwordx4 v[132:135], v177, s[62:63] sc1\n\t"
      "global_load_dwordx4 v[136:139], v178, s[62:63] sc1\n\t"
      "global_load_dwordx4 v[140:143], v179, s[62:63] sc1\n\t"
      "global_load_dwordx4 v[144:147], v180, s[62:63] sc1\n\t"
      "global_load_dwordx4 v[148:151], v181, s[62:63] sc1\n\t"
      "global_load_dwordx4 v[152:155], v182, s[62:63] sc1\n\t"
      "global_load_dwordx4 v[156:159], v183, s[62:63] sc1\n\t"
      "s_waitcnt vmcnt(0)\n\t"
      "s_nop 7\n\t"
      "s_branch NQ4B_%=\n\t"
      "NQ5R_%=:\n\t"
      "s_mov_b32 s51, 0\n\t"
      "NQ5L_%=:\n\t"
      "s_add_u32 s51, s51, 1\n\t"
      "s_cmp_lt_u32 s51, 262144\n\t"
      "s_cbranch_scc0 NQ5X_%=\n\t"
      "s_waitcnt vmcnt(0)\n\t"
      "s_sleep 1\n\t"
      "global_load_dwordx4 v[128:131], v176, s[62:63] sc1\n\t"
      "global_load_dwordx4 v[132:135], v177, s[62:63] sc1\n\t"
      "global_load_dwordx4 v[136:139], v178, s[62:63] sc1\n\t"
      "global_load_dwordx4 v[140:143], v179, s[62:63] sc1\n\t"
      "global_load_dwordx4 v[144:147], v180, s[62:63] sc1\n\t"
      "global_load_dwordx4 v[148:151], v181, s[62:63] sc1\n\t"
      "global_load_dwordx4 v[152:155], v182, s[62:63] sc1\n\t"
      "global_load_dwordx4 v[156:159], v183, s[62:63] sc1\n\t"
      "s_waitcnt vmcnt(0)\n\t"
      "v_max3_u32 v234, v128, v129, v130\n\t"
      "v_max3_u32 v234, v234, v131, v132\n\t"
      "v_max3_u32 v234, v234, v133, v134\n\t"
      "v_max3_u32 v234, v234, v135, v136\n\t"
      "v_max3_u32 v234, v234, v137, v138\n\t"
      "v_max3_u32 v234, v234, v139, v140\n\t"
      "v_max3_u32 v234, v234, v141, v142\n\t"
      "v_max3_u32 v234, v234, v143, v144\n\t"
      "v_max3_u32 v234, v234, v145, v146\n\t"
      "v_max3_u32 v234, v234, v147, v148\n\t"
      "v_max3_u32 v234, v234, v149, v150\n\t"
      "v_max3_u32 v234, v234, v151, v152\n\t"
      "v_max3_u32 v234, v234, v153, v154\n\t"
      "v_max3_u32 v234, v234, v155, v156\n\t"
      "v_max3_u32 v234, v234, v157, v158\n\t"
      "v_max_u32_e32 v234, v234, v159\n\t"
      "v_cmp_eq_u32_e32 vcc, -1, v234\n\t"
      "s_cbranch_vccnz NQ5L_%=\n\t"
      "NQ5X_%=:\n\t"
      "v_mfma_f32_16x16x32_bf16 v[200:203], v[0:3], v[128:131], v[236:239]\n\t"
      "v_mfma_f32_16x16x32_bf16 v[204:207], v[4:7], v[128:131], 0\n\t"
      "v_mfma_f32_16x16x32_bf16 v[208:211], v[8:11], v[128:131], 0\n\t"
      "v_mfma_f32_16x16x32_bf16 v[212:215], v[12:15], v[128:131], 0\n\t"
      "v_mfma_f32_16x16x32_bf16 v[200:203], v[16:19], v[132:135], v[200:203]\n\t"
      "v_mfma_f32_16x16x32_bf16 v[204:207], v[20:23], v[132:135], v[204:207]\n\t"
      "v_mfma_f32_16x16x32_bf16 v[208:211], v[24:27], v[132:135], v[208:211]\n\t"
      "v_mfma_f32_16x16x32_bf16 v[212:215], v[28:31], v[132:135], v[212:215]\n\t"
      "v_mfma_f32_16x16x32_bf16 v[200:203], v[32:35], v[136:139], v[200:203]\n\t"
      "v_mfma_f32_16x16x32_bf16 v[204:207], v[36:39], v[136:139], v[204:207]\n\t"
      "v_mfma_f32_16x16x32_bf16 v[208:211], v[40:43], v[136:139], v[208:211]\n\t"
      "v_mfma_f32_16x16x32_bf16 v[212:215], v[44:47], v[136:139], v[212:215]\n\t"
      "v_mfma_f32_16x16x32_bf16 v[200:203], v[48:51], v[140:143], v[200:203]\n\t"
      "v_mfma_f32_16x16x32_bf16 v[204:207], v[52:55], v[140:143], v[204:207]\n\t"
      "v_mfma_f32_16x16x32_bf16 v[208:211], v[56:59], v[140:143], v[208:211]\n\t"
      "v_mfma_f32_16x16x32_bf16 v[212:215], v[60:63], v[140:143], v[212:215]\n\t"
      "v_mfma_f32_16x16x32_bf16 v[200:203], v[64:67], v[144:147], v[200:203]\n\t"
      "v_mfma_f32_16x16x32_bf16 v[204:207], v[68:71], v[144:147], v[204:207]\n\t"
      "v_mfma_f32_16x16x32_bf16 v[208:211], v[72:75], v[144:147], v[208:211]\n\t"
      "v_mfma_f32_16x16x32_bf16 v[212:215], v[76:79], v[144:147], v[212:215]\n\t"
      "v_mfma_f32_16x16x32_bf16 v[200:203], v[80:83], v[148:151], v[200:203]\n\t"
      "v_mfma_f32_16x16x32_bf16 v[204:207], v[84:87], v[148:151], v[204:207]\n\t"
      "v_mfma_f32_16x16x32_bf16 v[208:211], v[88:91], v[148:151], v[208:211]\n\t"
      "v_mfma_f32_16x16x32_bf16 v[212:215], v[92:95], v[148:151], v[212:215]\n\t"
      "v_mfma_f32_16x16x32_bf16 v[200:203], v[96:99], v[152:155], v[200:203]\n\t"
      "v_mfma_f32_16x16x32_bf16 v[204:207], v[100:103], v[152:155], v[204:207]\n\t"
      "v_mfma_f32_16x16x32_bf16 v[208:211], v[104:107], v[152:155], v[208:211]\n\t"
      "v_mfma_f32_16x16x32_bf16 v[212:215], v[108:111], v[152:155], v[212:215]\n\t"
      "v_mfma_f32_16x16x32_bf16 v[200:203], v[112:115], v[156:159], v[200:203]\n\t"
      "v_mfma_f32_16x16x32_bf16 v[204:207], v[116:119], v[156:159], v[204:207]\n\t"
      "v_mfma_f32_16x16x32_bf16 v[208:211], v[120:123], v[156:159], v[208:211]\n\t"
      "v_mfma_f32_16x16x32_bf16 v[212:215], v[124:127], v[156:159], v[212:215]\n\t"
      "global_load_dwordx4 v[128:131], v176, s[64:65] sc1\n\t"
      "global_load_dwordx4 v[132:135], v177, s[64:65] sc1\n\t"
      "global_load_dwordx4 v[136:139], v178, s[64:65] sc1\n\t"
      "global_load_dwordx4 v[140:143], v179, s[64:65] sc1\n\t"
      "global_load_dwordx4 v[144:147], v180, s[64:65] sc1\n\t"
      "global_load_dwordx4 v[148:151], v181, s[64:65] sc1\n\t"
      "global_load_dwordx4 v[152:155], v182, s[64:65] sc1\n\t"
      "global_load_dwordx4 v[156:159], v183, s[64:65] sc1\n\t"
      "s_waitcnt vmcnt(0)\n\t"
      "s_nop 7\n\t"
      "s_branch NQ5B_%=\n\t"
      "NQ6R_%=:\n\t"
      "s_mov_b32 s51, 0\n\t"
      "NQ6L_%=:\n\t"
      "s_add_u32 s51, s51, 1\n\t"
      "s_cmp_lt_u32 s51, 262144\n\t"
      "s_cbranch_scc0 NQ6X_%=\n\t"
      "s_waitcnt vmcnt(0)\n\t"
      "s_sleep 1\n\t"
      "global_load_dwordx4 v[128:131], v176, s[64:65] sc1\n\t"
      "global_load_dwordx4 v[132:135], v177, s[64:65] sc1\n\t"
      "global_load_dwordx4 v[136:139], v178, s[64:65] sc1\n\t"
      "global_load_dwordx4 v[140:143], v179, s[64:65] sc1\n\t"
      "global_load_dwordx4 v[144:147], v180, s[64:65] sc1\n\t"
      "global_load_dwordx4 v[148:151], v181, s[64:65] sc1\n\t"
      "global_load_dwordx4 v[152:155], v182, s[64:65] sc1\n\t"
      "global_load_dwordx4 v[156:159], v183, s[64:65] sc1\n\t"
      "s_waitcnt vmcnt(0)\n\t"
      "v_max3_u32 v234, v128, v129, v130\n\t"
      "v_max3_u32 v234, v234, v131, v132\n\t"
      "v_max3_u32 v234, v234, v133, v134\n\t"
      "v_max3_u32 v234, v234, v135, v136\n\t"
      "v_max3_u32 v234, v234, v137, v138\n\t"
      "v_max3_u32 v234, v234, v139, v140\n\t"
      "v_max3_u32 v234, v234, v141, v142\n\t"
      "v_max3_u32 v234, v234, v143, v144\n\t"
      "v_max3_u32 v234, v234, v145, v146\n\t"
      "v_max3_u32 v234, v234, v147, v148\n\t"
      "v_max3_u32 v234, v234, v149, v150\n\t"
      "v_max3_u32 v234, v234, v151, v152\n\t"
      "v_max3_u32 v234, v234, v153, v154\n\t"
      "v_max3_u32 v234, v234, v155, v156\n\t"
      "v_max3_u32 v234, v234, v157, v158\n\t"
      "v_max_u32_e32 v234, v234, v159\n\t"
      "v_cmp_eq_u32_e32 vcc, -1, v234\n\t"
      "s_cbranch_vccnz NQ6L_%=\n\t"
      "NQ6X_%=:\n\t"
      "v_mfma_f32_16x16x32_bf16 v[184:187], v[0:3], v[128:131], v[236:239]\n\t"
      "v_mfma_f32_16x16x32_bf16 v[188:191], v[4:7], v[128:131], 0\n\t"
      "v_mfma_f32_16x16x32_bf16 v[192:195], v[8:11], v[128:131], 0\n\t"
      "v_mfma_f32_16x16x32_bf16 v[196:199], v[12:15], v[128:131], 0\n\t"
      "v_mfma_f32_16x16x32_bf16 v[184:187], v[16:19], v[132:135], v[184:187]\n\t"
      "v_mfma_f32_16x16x32_bf16 v[188:191], v[20:23], v[132:135], v[188:191]\n\t"
      "v_mfma_f32_16x16x32_bf16 v[192:195], v[24:27], v[132:135], v[192:195]\n\t"
      "v_mfma_f32_16x16x32_bf16 v[196:199], v[28:31], v[132:135], v[196:199]\n\t"
      "v_mfma_f32_16x16x32_bf16 v[184:187], v[32:35], v[136:139], v[184:187]\n\t"
      "v_mfma_f32_16x16x32_bf16 v[188:191], v[36:39], v[136:139], v[188:191]\n\t"
      "v_mfma_f32_16x16x32_bf16 v[192:195], v[40:43], v[136:139], v[192:195]\n\t"
      "v_mfma_f32_16x16x32_bf16 v[196:199], v[44:47], v[136:139], v[196:199]\n\t"
      "v_mfma_f32_16x16x32_bf16 v[184:187], v[48:51], v[140:143], v[184:187]\n\t"
      "v_mfma_f32_16x16x32_bf16 v[188:191], v[52:55], v[140:143], v[188:191]\n\t"
      "v_mfma_f32_16x16x32_bf16 v[192:195], v[56:59], v[140:143], v[192:195]\n\t"
      "v_mfma_f32_16x16x32_bf16 v[196:199], v[60:63], v[140:143], v[196:199]\n\t"
      "v_mfma_f32_16x16x32_bf16 v[184:187], v[64:67], v[144:147], v[184:187]\n\t"
      "v_mfma_f32_16x16x32_bf16 v[188:191], v[68:71], v[144:147], v[188:191]\n\t"
      "v_mfma_f32_16x16x32_bf16 v[192:195], v[72:75], v[144:147], v[192:195]\n\t"
      "v_mfma_f32_16x16x32_bf16 v[196:199], v[76:79], v[144:147], v[196:199]\n\t"
      "v_mfma_f32_16x16x32_bf16 v[184:187], v[80:83], v[148:151], v[184:187]\n\t"
      "v_mfma_f32_16x16x32_bf16 v[188:191], v[84:87], v[148:151], v[188:191]\n\t"
      "v_mfma_f32_16x16x32_bf16 v[192:195], v[88:91], v[148:151], v[192:195]\n\t"
      "v_mfma_f32_16x16x32_bf16 v[196:199], v[92:95], v[148:151], v[196:199]\n\t"
      "v_mfma_f32_16x16x32_bf16 v[184:187], v[96:99], v[152:155], v[184:187]\n\t"
      "v_mfma_f32_16x16x32_bf16 v[188:191], v[100:103], v[152:155], v[188:191]\n\t"
      "v_mfma_f32_16x16x32_bf16 v[192:195], v[104:107], v[152:155], v[192:195]\n\t"
      "v_mfma_f32_16x16x32_bf16 v[196:199], v[108:111], v[152:155], v[196:199]\n\t"
      "v_mfma_f32_16x16x32_bf16 v[184:187], v[112:115], v[156:159], v[184:187]\n\t"
      "v_mfma_f32_16x16x32_bf16 v[188:191], v[116:119], v[156:159], v[188:191]\n\t"
      "v_mfma_f32_16x16x32_bf16 v[192:195], v[120:123], v[156:159], v[192:195]\n\t"
      "v_mfma_f32_16x16x32_bf16 v[196:199], v[124:127], v[156:159], v[196:199]\n\t"
      "s_waitcnt vmcnt(0)\n\t"
      "s_nop 7\n\t"
      "s_branch NQ6B_%=\n\t"
      "FP0R_%=:\n\t"
      "s_mov_b32 s51, 0\n\t"
      "FP0L_%=:\n\t"
      "s_add_u32 s51, s51, 1\n\t"
      "s_cmp_lt_u32 s51, 262144\n\t"
      "s_cbranch_scc0 FP0X_%=\n\t"
      "s_waitcnt vmcnt(0)\n\t"
      "s_sleep 1\n\t"
      "global_load_dwordx4 v[128:131], v176, s[52:53] sc1\n\t"
      "global_load_dwordx4 v[132:135], v177, s[52:53] sc1\n\t"
      "global_load_dwordx4 v[136:139], v178, s[52:53] sc1\n\t"
      "global_load_dwordx4 v[140:143], v179, s[52:53] sc1\n\t"
      "global_load_dwordx4 v[144:147], v180, s[52:53] sc1\n\t"
      "global_load_dwordx4 v[148:151], v181, s[52:53] sc1\n\t"
      "global_load_dwordx4 v[152:155], v182, s[52:53] sc1\n\t"
      "global_load_dwordx4 v[156:159], v183, s[52:53] sc1\n\t"
      "s_waitcnt vmcnt(7)\n\t"
      "v_max3_u32 v234, v128, v129, v130\n\t"
      "v_max_u32_e32 v234, v234, v131\n\t"
      "v_cmp_eq_u32_e32 vcc, -1, v234\n\t"
      "s_cbranch_vccnz FP0L_%=\n\t"
      "FP0X_%=:\n\t"
      "s_branch FP0B_%=\n\t"
      "FP1R_%=:\n\t"
      "s_mov_b32 s51, 0\n\t"
      "FP1L_%=:\n\t"
      "s_add_u32 s51, s51, 1\n\t"
      "s_cmp_lt_u32 s51, 262144\n\t"
      "s_cbranch_scc0 FP1X_%=\n\t"
      "s_waitcnt vmcnt(0)\n\t"
      "s_sleep 1\n\t"
      "global_load_dwordx4 v[132:135], v177, s[52:53] sc1\n\t"
      "global_load_dwordx4 v[136:139], v178, s[52:53] sc1\n\t"
      "global_load_dwordx4 v[140:143], v179, s[52:53] sc1\n\t"
      "global_load_dwordx4 v[144:147], v180, s[52:53] sc1\n\t"
      "global_load_dwordx4 v[148:151], v181, s[52:53] sc1\n\t"
      "global_load_dwordx4 v[152:155], v182, s[52:53] sc1\n\t"
      "global_load_dwordx4 v[156:159], v183, s[52:53] sc1\n\t"
      "s_waitcnt vmcnt(6)\n\t"
      "v_max3_u32 v234, v132, v133, v134\n\t"
      "v_max_u32_e32 v234, v234, v135\n\t"
      "v_cmp_eq_u32_e32 vcc, -1, v234\n\t"
      "s_cbranch_vccnz FP1L_%=\n\t"
      "FP1X_%=:\n\t"
      "s_branch FP1B_%=\n\t"
      "FP2R_%=:\n\t"
      "s_mov_b32 s51, 0\n\t"
      "FP2L_%=:\n\t"
      "s_add_u32 s51, s51, 1\n\t"
      "s_cmp_lt_u32 s51, 262144\n\t"
      "s_cbranch_scc0 FP2X_%=\n\t"
      "s_waitcnt vmcnt(0)\n\t"
      "s_sleep 1\n\t"
      "global_load_dwordx4 v[136:139], v178, s[52:53] sc1\n\t"
      "global_load_dwordx4 v[140:143], v179, s[52:53] sc1\n\t"
      "global_load_dwordx4 v[144:147], v180, s[52:53] sc1\n\t"
      "global_load_dwordx4 v[148:151], v181, s[52:53] sc1\n\t"
      "global_load_dwordx4 v[152:155], v182, s[52:53] sc1\n\t"
      "global_load_dwordx4 v[156:159], v183, s[52:53] sc1\n\t"
      "global_load_dwordx4 v[128:131], v176, s[54:55] sc1\n\t"
      "global_load_dwordx4 v[132:135], v177, s[54:55] sc1\n\t"
      "s_waitcnt vmcnt(7)\n\t"
      "v_max3_u32 v234, v136, v137, v138\n\t"
      "v_max_u32_e32 v234, v234, v139\n\t"
      "v_cmp_eq_u32_e32 vcc, -1, v234\n\t"
      "s_cbranch_vccnz FP2L_%=\n\t"
      "FP2X_%=:\n\t"
      "s_branch FP2B_%=\n\t"
      "FP3R_%=:\n\t"
      "s_mov_b32 s51, 0\n\t"
      "FP3L_%=:\n\t"
      "s_add_u32 s51, s51, 1\n\t"
      "s_cmp_lt_u32 s51, 262144\n\t"
      "s_cbranch_scc0 FP3X_%=\n\t"
      "s_waitcnt vmcnt(0)\n\t"
      "s_sleep 1\n\t"
      "global_load_dwordx4 v[140:143], v179, s[52:53] sc1\n\t"
      "global_load_dwordx4 v[144:147], v180, s[52:53] sc1\n\t"
      "global_load_dwordx4 v[148:151], v181, s[52:53] sc1\n\t"
      "global_load_dwordx4 v[152:155], v182, s[52:53] sc1\n\t"
      "global_load_dwordx4 v[156:159], v183, s[52:53] sc1\n\t"
      "global_load_dwordx4 v[128:131], v176, s[54:55] sc1\n\t"
      "global_load_dwordx4 v[132:135], v177, s[54:55] sc1\n\t"
      "s_waitcnt vmcnt(6)\n\t"
      "v_max3_u32 v234, v140, v141, v142\n\t"
      "v_max_u32_e32 v234, v234, v143\n\t"
      "v_cmp_eq_u32_e32 vcc, -1, v234\n\t"
      "s_cbranch_vccnz FP3L_%=\n\t"
      "FP3X_%=:\n\t"
      "s_branch FP3B_%=\n\t"
      "FP4R_%=:\n\t"
      "s_mov_b32 s51, 0\n\t"
      "FP4L_%=:\n\t"
      "s_add_u32 s51, s51, 1\n\t"
      "s_cmp_lt_u32 s51, 262144\n\t"
      "s_cbranch_scc0 FP4X_%=\n\t"
      "s_waitcnt vmcnt(0)\n\t"
      "s_sleep 1\n\t"
      "global_load_dwordx4 v[144:147], v180, s[52:53] sc1\n\t"
      "global_load_dwordx4 v[148:151], v181, s[52:53] sc1\n\t"
      "global_load_dwordx4 v[152:155], v182, s[52:53] sc1\n\t"
      "global_load_dwordx4 v[156:159], v183, s[52:53] sc1\n\t"
      "global_load_dwordx4 v[128:131], v176, s[54:55] sc1\n\t"
      "global_load_dwordx4 v[132:135], v177, s[54:55] sc1\n\t"
      "global_load_dwordx4 v[136:139], v178, s[54:55] sc1\n\t"
      "global_load_dwordx4 v[140:143], v179, s[54:55] sc1\n\t"
      "s_waitcnt vmcnt(7)\n\t"
      "v_max3_u32 v234, v144, v145, v146\n\t"
      "v_max_u32_e32 v234, v234, v147\n\t"
      "v_cmp_eq_u32_e32 vcc, -1, v234\n\t"
      "s_cbranch_vccnz FP4L_%=\n\t"
      "FP4X_%=:\n\t"
      "s_branch FP4B_%=\n\t"
      "FP5R_%=:\n\t"
      "s_mov_b32 s51, 0\n\t"
      "FP5L_%=:\n\t"
      "s_add_u32 s51, s51, 1\n\t"
      "s_cmp_lt_u32 s51, 262144\n\t"
      "s_cbranch_scc0 FP5X_%=\n\t"
      "s_waitcnt vmcnt(0)\n\t"
      "s_sleep 1\n\t"
      "global_load_dwordx4 v[148:151], v181, s[52:53] sc1\n\t"
      "global_load_dwordx4 v[152:155], v182, s[52:53] sc1\n\t"
      "global_load_dwordx4 v[156:159], v183, s[52:53] sc1\n\t"
      "global_load_dwordx4 v[128:131], v176, s[54:55] sc1\n\t"
      "global_load_dwordx4 v[132:135], v177, s[54:55] sc1\n\t"
      "global_load_dwordx4 v[136:139], v178, s[54:55] sc1\n\t"
      "global_load_dwordx4 v[140:143], v179, s[54:55] sc1\n\t"
      "s_waitcnt vmcnt(6)\n\t"
      "v_max3_u32 v234, v148, v149, v150\n\t"
      "v_max_u32_e32 v234, v234, v151\n\t"
      "v_cmp_eq_u32_e32 vcc, -1, v234\n\t"
      "s_cbranch_vccnz FP5L_%=\n\t"
      "FP5X_%=:\n\t"
      "s_branch FP5B_%=\n\t"
      "FP6R_%=:\n\t"
      "s_mov_b32 s51, 0\n\t"
      "FP6L_%=:\n\t"
      "s_add_u32 s51, s51, 1\n\t"
      "s_cmp_lt_u32 s51, 262144\n\t"
      "s_cbranch_scc0 FP6X_%=\n\t"
      "s_waitcnt vmcnt(0)\n\t"
      "s_sleep 1\n\t"
      "global_load_dwordx4 v[152:155], v182, s[52:53] sc1\n\t"
      "global_load_dwordx4 v[156:159], v183, s[52:53] sc1\n\t"
      "global_load_dwordx4 v[128:131], v176, s[54:55] sc1\n\t"
      "global_load_dwordx4 v[132:135], v177, s[54:55] sc1\n\t"
      "global_load_dwordx4 v[136:139], v178, s[54:55] sc1\n\t"
      "global_load_dwordx4 v[140:143], v179, s[54:55] sc1\n\t"
      "global_load_dwordx4 v[144:147], v180, s[54:55] sc1\n\t"
      "global_load_dwordx4 v[148:151], v181, s[54:55] sc1\n\t"
      "s_waitcnt vmcnt(7)\n\t"
      "v_max3_u32 v234, v152, v153, v154\n\t"
      "v_max_u32_e32 v234, v234, v155\n\t"
      "v_cmp_eq_u32_e32 vcc, -1, v234\n\t"
      "s_cbranch_vccnz FP6L_%=\n\t"
      "FP6X_%=:\n\t"
      "s_branch FP6B_%=\n\t"
      "FP7R_%=:\n\t"
      "s_mov_b32 s51, 0\n\t"
      "FP7L_%=:\n\t"
      "s_add_u32 s51, s51, 1\n\t"
      "s_cmp_lt_u32 s51, 262144\n\t"
      "s_cbranch_scc0 FP7X_%=\n\t"
      "s_waitcnt vmcnt(0)\n\t"
      "s_sleep 1\n\t"
      "global_load_dwordx4 v[156:159], v183, s[52:53] sc1\n\t"
      "global_load_dwordx4 v[128:131], v176, s[54:55] sc1\n\t"
      "global_load_dwordx4 v[132:135], v177, s[54:55] sc1\n\t"
      "global_load_dwordx4 v[136:139], v178, s[54:55] sc1\n\t"
      "global_load_dwordx4 v[140:143], v179, s[54:55] sc1\n\t"
      "global_load_dwordx4 v[144:147], v180, s[54:55] sc1\n\t"
      "global_load_dwordx4 v[148:151], v181, s[54:55] sc1\n\t"
      "s_waitcnt vmcnt(6)\n\t"
      "v_max3_u32 v234, v156, v157, v158\n\t"
      "v_max_u32_e32 v234, v234, v159\n\t"
      "v_cmp_eq_u32_e32 vcc, -1, v234\n\t"
      "s_cbranch_vccnz FP7L_%=\n\t"
      "FP7X_%=:\n\t"
      "s_branch FP7B_%=\n\t"
      "NP1R_%=:\n\t"
      "s_mov_b32 s51, 0\n\t"
      "NP1L_%=:\n\t"
      "s_add_u32 s51, s51, 1\n\t"
      "s_cmp_lt_u32 s51, 262144\n\t"
      "s_cbranch_scc0 NP1X_%=\n\t"
      "s_waitcnt vmcnt(0)\n\t"
      "s_sleep 1\n\t"
      "global_load_dwordx4 v[128:131], v176, s[54:55] sc1\n\t"
      "global_load_dwordx4 v[132:135], v177, s[54:55] sc1\n\t"
      "global_load_dwordx4 v[136:139], v178, s[54:55] sc1\n\t"
      "global_load_dwordx4 v[140:143], v179, s[54:55] sc1\n\t"
      "global_load_dwordx4 v[144:147], v180, s[54:55] sc1\n\t"
      "global_load_dwordx4 v[148:151], v181, s[54:55] sc1\n\t"
      "global_load_dwordx4 v[152:155], v182, s[54:55] sc1\n\t"
      "global_load_dwordx4 v[156:159], v183, s[54:55] sc1\n\t"
      "s_waitcnt vmcnt(0)\n\t"
      "v_max3_u32 v234, v128, v129, v130\n\t"
      "v_max3_u32 v234, v234, v131, v132\n\t"
      "v_max3_u32 v234, v234, v133, v134\n\t"
      "v_max3_u32 v234, v234, v135, v136\n\t"
      "v_max3_u32 v234, v234, v137, v138\n\t"
      "v_max3_u32 v234, v234, v139, v140\n\t"
      "v_max3_u32 v234, v234, v141, v142\n\t"
      "v_max3_u32 v234, v234, v143, v144\n\t"
      "v_max3_u32 v234, v234, v145, v146\n\t"
      "v_max3_u32 v234, v234, v147, v148\n\t"
      "v_max3_u32 v234, v234, v149, v150\n\t"
      "v_max3_u32 v234, v234, v151, v152\n\t"
      "v_max3_u32 v234, v234, v153, v154\n\t"
      "v_max3_u32 v234, v234, v155, v156\n\t"
      "v_max3_u32 v234, v234, v157, v158\n\t"
      "v_max_u32_e32 v234, v234, v159\n\t"
      "v_cmp_eq_u32_e32 vcc, -1, v234\n\t"
      "s_cbranch_vccnz NP1L_%=\n\t"
      "NP1X_%=:\n\t"
      "v_mfma_f32_16x16x32_bf16 v[200:203], v[0:3], v[128:131], 0\n\t"
      "v_mfma_f32_16x16x32_bf16 v[204:207], v[4:7], v[128:131], 0\n\t"
      "v_mfma_f32_16x16x32_bf16 v[208:211], v[8:11], v[128:131], 0\n\t"
      "v_mfma_f32_16x16x32_bf16 v[212:215], v[12:15], v[128:131], 0\n\t"
      "v_mfma_f32_16x16x32_bf16 v[200:203], v[16:19], v[132:135], v[200:203]\n\t"
      "v_mfma_f32_16x16x32_bf16 v[204:207], v[20:23], v[132:135], v[204:207]\n\t"
      "v_mfma_f32_16x16x32_bf16 v[208:211], v[24:27], v[132:135], v[208:211]\n\t"
      "v_mfma_f32_16x16x32_bf16 v[212:215], v[28:31], v[132:135], v[212:215]\n\t"
      "v_mfma_f32_16x16x32_bf16 v[200:203], v[32:35], v[136:139], v[200:203]\n\t"
      "v_mfma_f32_16x16x32_bf16 v[204:207], v[36:39], v[136:139], v[204:207]\n\t"
      "v_mfma_f32_16x16x32_bf16 v[208:211], v[40:43], v[136:139], v[208:211]\n\t"
      "v_mfma_f32_16x16x32_bf16 v[212:215], v[44:47], v[136:139], v[212:215]\n\t"
      "v_mfma_f32_16x16x32_bf16 v[200:203], v[48:51], v[140:143], v[200:203]\n\t"
      "v_mfma_f32_16x16x32_bf16 v[204:207], v[52:55], v[140:143], v[204:207]\n\t"
      "v_mfma_f32_16x16x32_bf16 v[208:211], v[56:59], v[140:143], v[208:211]\n\t"
      "v_mfma_f32_16x16x32_bf16 v[212:215], v[60:63], v[140:143], v[212:215]\n\t"
      "v_mfma_f32_16x16x32_bf16 v[200:203], v[64:67], v[144:147], v[200:203]\n\t"
      "v_mfma_f32_16x16x32_bf16 v[204:207], v[68:71], v[144:147], v[204:207]\n\t"
      "v_mfma_f32_16x16x32_bf16 v[208:211], v[72:75], v[144:147], v[208:211]\n\t"
      "v_mfma_f32_16x16x32_bf16 v[212:215], v[76:79], v[144:147], v[212:215]\n\t"
      "v_mfma_f32_16x16x32_bf16 v[200:203], v[80:83], v[148:151], v[200:203]\n\t"
      "v_mfma_f32_16x16x32_bf16 v[204:207], v[84:87], v[148:151], v[204:207]\n\t"
      "v_mfma_f32_16x16x32_bf16 v[208:211], v[88:91], v[148:151], v[208:211]\n\t"
      "v_mfma_f32_16x16x32_bf16 v[212:215], v[92:95], v[148:151], v[212:215]\n\t"
      "v_mfma_f32_16x16x32_bf16 v[200:203], v[96:99], v[152:155], v[200:203]\n\t"
      "v_mfma_f32_16x16x32_bf16 v[204:207], v[100:103], v[152:155], v[204:207]\n\t"
      "v_mfma_f32_16x16x32_bf16 v[208:211], v[104:107], v[152:155], v[208:211]\n\t"
      "v_mfma_f32_16x16x32_bf16 v[200:203], v[112:115], v[156:159], v[200:203]\n\t"
      "v_mfma_f32_16x16x32_bf16 v[204:207], v[116:119], v[156:159], v[204:207]\n\t"
      "v_mfma_f32_16x16x32_bf16 v[208:211], v[120:123], v[156:159], v[208:211]\n\t"
      "global_load_dwordx4 v[128:131], v176, s[56:57] sc1\n\t"
      "global_load_dwordx4 v[132:135], v177, s[56:57] sc1\n\t"
      "global_load_dwordx4 v[136:139], v178, s[56:57] sc1\n\t"
      "global_load_dwordx4 v[140:143], v179, s[56:57] sc1\n\t"
      "global_load_dwordx4 v[144:147], v180, s[56:57] sc1\n\t"
      "global_load_dwordx4 v[148:151], v181, s[56:57] sc1\n\t"
      "s_waitcnt vmcnt(0)\n\t"
      "s_nop 7\n\t"
      "s_branch NP1B_%=\n\t"
      "NP2R_%=:\n\t"
      "s_mov_b32 s51, 0\n\t"
      "NP2L_%=:\n\t"
      "s_add_u32 s51, s51, 1\n\t"
      "s_cmp_lt_u32 s51, 262144\n\t"
      "s_cbranch_scc0 NP2X_%=\n\t"
      "s_waitcnt vmcnt(0)\n\t"
      "s_sleep 1\n\t"
      "global_load_dwordx4 v[128:131], v176, s[56:57] sc1\n\t"
      "global_load_dwordx4 v[132:135], v177, s[56:57] sc1\n\t"
      "global_load_dwordx4 v[136:139], v178, s[56:57] sc1\n\t"
      "global_load_dwordx4 v[140:143], v179, s[56:57] sc1\n\t"
      "global_load_dwordx4 v[144:147], v180, s[56:57] sc1\n\t"
      "global_load_dwordx4 v[148:151], v181, s[56:57] sc1\n\t"
      "global_load_dwordx4 v[152:155], v182, s[56:57] sc1\n\t"
      "global_load_dwordx4 v[156:159], v183, s[56:57] sc1\n\t"
      "s_waitcnt vmcnt(0)\n\t"
      "v_max3_u32 v234, v128, v129, v130\n\t"
      "v_max3_u32 v234, v234, v131, v132\n\t"
      "v_max3_u32 v234, v234, v133, v134\n\t"
      "v_max3_u32 v234, v234, v135, v136\n\t"
      "v_max3_u32 v234, v234, v137, v138\n\t"
      "v_max3_u32 v234, v234, v139, v140\n\t"
      "v_max3_u32 v234, v234, v141, v142\n\t"
      "v_max3_u32 v234, v234, v143, v144\n\t"
      "v_max3_u32 v234, v234, v145, v146\n\t"
      "v_max3_u32 v234, v234, v147, v148\n\t"
      "v_max3_u32 v234, v234, v149, v150\n\t"
      "v_max3_u32 v234, v234, v151, v152\n\t"
      "v_max3_u32 v234, v234, v153, v154\n\t"
      "v_max3_u32 v234, v234, v155, v156\n\t"
      "v_max3_u32 v234, v234, v157, v158\n\t"
      "v_max_u32_e32 v234, v234, v159\n\t"
      "v_cmp_eq_u32_e32 vcc, -1, v234\n\t"
      "s_cbranch_vccnz NP2L_%=\n\t"
      "NP2X_%=:\n\t"
      "v_mfma_f32_16x16x32_bf16 v[184:187], v[0:3], v[128:131], 0\n\t"
      "v_mfma_f32_16x16x32_bf16 v[188:191], v[4:7], v[128:131], 0\n\t"
      "v_mfma_f32_16x16x32_bf16 v[192:195], v[8:11], v[128:131], 0\n\t"
      "v_mfma_f32_16x16x32_bf16 v[196:199], v[12:15], v[128:131], 0\n\t"
      "v_mfma_f32_16x16x32_bf16 v[184:187], v[16:19], v[132:135], v[184:187]\n\t"
      "v_mfma_f32_16x16x32_bf16 v[188:191], v[20:23], v[132:135], v[188:191]\n\t"
      "v_mfma_f32_16x16x32_bf16 v[192:195], v[24:27], v[132:135], v[192:195]\n\t"
      "v_mfma_f32_16x16x32_bf16 v[196:199], v[28:31], v[132:135], v[196:199]\n\t"
      "v_mfma_f32_16x16x32_bf16 v[184:187], v[32:35], v[136:139], v[184:187]\n\t"
      "v_mfma_f32_16x16x32_bf16 v[188:191], v[36:39], v[136:139], v[188:191]\n\t"
      "v_mfma_f32_16x16x32_bf16 v[192:195], v[40:43], v[136:139], v[192:195]\n\t"
      "v_mfma_f32_16x16x32_bf16 v[196:199], v[44:47], v[136:139], v[196:199]\n\t"
      "v_mfma_f32_16x16x32_bf16 v[184:187], v[48:51], v[140:143], v[184:187]\n\t"
      "v_mfma_f32_16x16x32_bf16 v[188:191], v[52:55], v[140:143], v[188:191]\n\t"
      "v_mfma_f32_16x16x32_bf16 v[192:195], v[56:59], v[140:143], v[192:195]\n\t"
      "v_mfma_f32_16x16x32_bf16 v[196:199], v[60:63], v[140:143], v[196:199]\n\t"
      "v_mfma_f32_16x16x32_bf16 v[184:187], v[64:67], v[144:147], v[184:187]\n\t"
      "v_mfma_f32_16x16x32_bf16 v[188:191], v[68:71], v[144:147], v[188:191]\n\t"
      "v_mfma_f32_16x16x32_bf16 v[192:195], v[72:75], v[144:147], v[192:195]\n\t"
      "v_mfma_f32_16x16x32_bf16 v[196:199], v[76:79], v[144:147], v[196:199]\n\t"
      "v_mfma_f32_16x16x32_bf16 v[184:187], v[80:83], v[148:151], v[184:187]\n\t"
      "v_mfma_f32_16x16x32_bf16 v[188:191], v[84:87], v[148:151], v[188:191]\n\t"
      "v_mfma_f32_16x16x32_bf16 v[192:195], v[88:91], v[148:151], v[192:195]\n\t"
      "v_mfma_f32_16x16x32_bf16 v[196:199], v[92:95], v[148:151], v[196:199]\n\t"
      "v_mfma_f32_16x16x32_bf16 v[184:187], v[96:99], v[152:155], v[184:187]\n\t"
      "v_mfma_f32_16x16x32_bf16 v[188:191], v[100:103], v[152:155], v[188:191]\n\t"
      "v_mfma_f32_16x16x32_bf16 v[192:195], v[104:107], v[152:155], v[192:195]\n\t"
      "v_mfma_f32_16x16x32_bf16 v[184:187], v[112:115], v[156:159], v[184:187]\n\t"
      "v_mfma_f32_16x16x32_bf16 v[188:191], v[116:119], v[156:159], v[188:191]\n\t"
      "v_mfma_f32_16x16x32_bf16 v[192:195], v[120:123], v[156:159], v[192:195]\n\t"
      "global_load_dwordx4 v[128:131], v176, s[58:59] sc1\n\t"
      "global_load_dwordx4 v[132:135], v177, s[58:59] sc1\n\t"
      "global_load_dwordx4 v[136:139], v178, s[58:59] sc1\n\t"
      "global_load_dwordx4 v[140:143], v179, s[58:59] sc1\n\t"
      "global_load_dwordx4 v[144:147], v180, s[58:59] sc1\n\t"
      "global_load_dwordx4 v[148:151], v181, s[58:59] sc1\n\t"
      "s_waitcnt vmcnt(0)\n\t"
      "s_nop 7\n\t"
      "s_branch NP2B_%=\n\t"
      "NP3R_%=:\n\t"
      "s_mov_b32 s51, 0\n\t"
      "NP3L_%=:\n\t"
      "s_add_u32 s51, s51, 1\n\t"
      "s_cmp_lt_u32 s51, 262144\n\t"
      "s_cbranch_scc0 NP3X_%=\n\t"
      "s_waitcnt vmcnt(0)\n\t"
      "s_sleep 1\n\t"
      "global_load_dwordx4 v[128:131], v176, s[58:59] sc1\n\t"
      "global_load_dwordx4 v[132:135], v177, s[58:59] sc1\n\t"
      "global_load_dwordx4 v[136:139], v178, s[58:59] sc1\n\t"
      "global_load_dwordx4 v[140:143], v179, s[58:59] sc1\n\t"
      "global_load_dwordx4 v[144:147], v180, s[58:59] sc1\n\t"
      "global_load_dwordx4 v[148:151], v181, s[58:59] sc1\n\t"
      "global_load_dwordx4 v[152:155], v182, s[58:59] sc1\n\t"
      "global_load_dwordx4 v[156:159], v183, s[58:59] sc1\n\t"
      "s_waitcnt vmcnt(0)\n\t"
      "v_max3_u32 v234, v128, v129, v130\n\t"
      "v_max3_u32 v234, v234, v131, v132\n\t"
      "v_max3_u32 v234, v234, v133, v134\n\t"
      "v_max3_u32 v234, v234, v135, v136\n\t"
      "v_max3_u32 v234, v234, v137, v138\n\t"
      "v_max3_u32 v234, v234, v139, v140\n\t"
      "v_max3_u32 v234, v234, v141, v142\n\t"
      "v_max3_u32 v234, v234, v143, v144\n\t"
      "v_max3_u32 v234, v234, v145, v146\n\t"
      "v_max3_u32 v234, v234, v147, v148\n\t"
      "v_max3_u32 v234, v234, v149, v150\n\t"
      "v_max3_u32 v234, v234, v151, v152\n\t"
      "v_max3_u32 v234, v234, v153, v154\n\t"
      "v_max3_u32 v234, v234, v155, v156\n\t"
      "v_max3_u32 v234, v234, v157, v158\n\t"
      "v_max_u32_e32 v234, v234, v159\n\t"
      "v_cmp_eq_u32_e32 vcc, -1, v234\n\t"
      "s_cbranch_vccnz NP3L_%=\n\t"
      "NP3X_%=:\n\t"
      "v_mfma_f32_16x16x32_bf16 v[200:203], v[0:3], v[128:131], 0\n\t"
      "v_mfma_f32_16x16x32_bf16 v[204:207], v[4:7], v[128:131], 0\n\t"
      "v_mfma_f32_16x16x32_bf16 v[208:211], v[8:11], v[128:131], 0\n\t"
      "v_mfma_f32_16x16x32_bf16 v[212:215], v[12:15], v[128:131], 0\n\t"
      "v_mfma_f32_16x16x32_bf16 v[200:203], v[16:19], v[132:135], v[200:203]\n\t"
      "v_mfma_f32_16x16x32_bf16 v[204:207], v[20:23], v[132:135], v[204:207]\n\t"
      "v_mfma_f32_16x16x32_bf16 v[208:211], v[24:27], v[132:135], v[208:211]\n\t"
      "v_mfma_f32_16x16x32_bf16 v[212:215], v[28:31], v[132:135], v[212:215]\n\t"
      "v_mfma_f32_16x16x32_bf16 v[200:203], v[32:35], v[136:139], v[200:203]\n\t"
      "v_mfma_f32_16x16x32_bf16 v[204:207], v[36:39], v[136:139], v[204:207]\n\t"
      "v_mfma_f32_16x16x32_bf16 v[208:211], v[40:43], v[136:139], v[208:211]\n\t"
      "v_mfma_f32_16x16x32_bf16 v[212:215], v[44:47], v[136:139], v[212:215]\n\t"
      "v_mfma_f32_16x16x32_bf16 v[200:203], v[48:51], v[140:143], v[200:203]\n\t"
      "v_mfma_f32_16x16x32_bf16 v[204:207], v[52:55], v[140:143], v[204:207]\n\t"
      "v_mfma_f32_16x16x32_bf16 v[208:211], v[56:59], v[140:143], v[208:211]\n\t"
      "v_mfma_f32_16x16x32_bf16 v[212:215], v[60:63], v[140:143], v[212:215]\n\t"
      "v_mfma_f32_16x16x32_bf16 v[200:203], v[64:67], v[144:147], v[200:203]\n\t"
      "v_mfma_f32_16x16x32_bf16 v[204:207], v[68:71], v[144:147], v[204:207]\n\t"
      "v_mfma_f32_16x16x32_bf16 v[208:211], v[72:75], v[144:147], v[208:211]\n\t"
      "v_mfma_f32_16x16x32_bf16 v[212:215], v[76:79], v[144:147], v[212:215]\n\t"
      "v_mfma_f32_16x16x32_bf16 v[200:203], v[80:83], v[148:151], v[200:203]\n\t"
      "v_mfma_f32_16x16x32_bf16 v[204:207], v[84:87], v[148:151], v[204:207]\n\t"
      "v_mfma_f32_16x16x32_bf16 v[208:211], v[88:91], v[148:151], v[208:211]\n\t"
      "v_mfma_f32_16x16x32_bf16 v[212:215], v[92:95], v[148:151], v[212:215]\n\t"
      "v_mfma_f32_16x16x32_bf16 v[200:203], v[96:99], v[152:155], v[200:203]\n\t"
      "v_mfma_f32_16x16x32_bf16 v[204:207], v[100:103], v[152:155], v[204:207]\n\t"
      "v_mfma_f32_16x16x32_bf16 v[208:211], v[104:107], v[152:155], v[208:211]\n\t"
      "v_mfma_f32_16x16x32_bf16 v[200:203], v[112:115], v[156:159], v[200:203]\n\t"
      "v_mfma_f32_16x16x32_bf16 v[204:207], v[116:119], v[156:159], v[204:207]\n\t"
      "v_mfma_f32_16x16x32_bf16 v[208:211], v[120:123], v[156:159], v[208:211]\n\t"
      "global_load_dwordx4 v[128:131], v176, s[60:61] sc1\n\t"
      "global_load_dwordx4 v[132:135], v177, s[60:61] sc1\n\t"
      "global_load_dwordx4 v[136:139], v178, s[60:61] sc1\n\t"
      "global_load_dwordx4 v[140:143], v179, s[60:61] sc1\n\t"
      "global_load_dwordx4 v[144:147], v180, s[60:61] sc1\n\t"
      "global_load_dwordx4 v[148:151], v181, s[60:61] sc1\n\t"
      "s_waitcnt vmcnt(0)\n\t"
      "s_nop 7\n\t"
      "s_branch NP3B_%=\n\t"
      "NP4R_%=:\n\t"
      "s_mov_b32 s51, 0\n\t"
      "NP4L_%=:\n\t"
      "s_add_u32 s51, s51, 1\n\t"
      "s_cmp_lt_u32 s51, 262144\n\t"
      "s_cbranch_scc0 NP4X_%=\n\t"
      "s_waitcnt vmcnt(0)\n\t"
      "s_sleep 1\n\t"
      "global_load_dwordx4 v[128:131], v176, s[60:61] sc1\n\t"
      "global_load_dwordx4 v[132:135], v177, s[60:61] sc1\n\t"
      "global_load_dwordx4 v[136:139], v178, s[60:61] sc1\n\t"
      "global_load_dwordx4 v[140:143], v179, s[60:61] sc1\n\t"
      "global_load_dwordx4 v[144:147], v180, s[60:61] sc1\n\t"
      "global_load_dwordx4 v[148:151], v181, s[60:61] sc1\n\t"
      "global_load_dwordx4 v[152:155], v182, s[60:61] sc1\n\t"
      "global_load_dwordx4 v[156:159], v183, s[60:61] sc1\n\t"
      "s_waitcnt vmcnt(0)\n\t"
      "v_max3_u32 v234, v128, v129, v130\n\t"
      "v_max3_u32 v234, v234, v131, v132\n\t"
      "v_max3_u32 v234, v234, v133, v134\n\t"
      "v_max3_u32 v234, v234, v135, v136\n\t"
      "v_max3_u32 v234, v234, v137, v138\n\t"
      "v_max3_u32 v234, v234, v139, v140\n\t"
      "v_max3_u32 v234, v234, v141, v142\n\t"
      "v_max3_u32 v234, v234, v143, v144\n\t"
      "v_max3_u32 v234, v234, v145, v146\n\t"
      "v_max3_u32 v234, v234, v147, v148\n\t"
      "v_max3_u32 v234, v234, v149, v150\n\t"
      "v_max3_u32 v234, v234, v151, v152\n\t"
      "v_max3_u32 v234, v234, v153, v154\n\t"
      "v_max3_u32 v234, v234, v155, v156\n\t"
      "v_max3_u32 v234, v234, v157, v158\n\t"
      "v_max_u32_e32 v234, v234, v159\n\t"
      "v_cmp_eq_u32_e32 vcc, -1, v234\n\t"
      "s_cbranch_vccnz NP4L_%=\n\t"
      "NP4X_%=:\n\t"
      "v_mfma_f32_16x16x32_bf16 v[184:187], v[0:3], v[128:131], 0\n\t"
      "v_mfma_f32_16x16x32_bf16 v[188:191], v[4:7], v[128:131], 0\n\t"
      "v_mfma_f32_16x16x32_bf16 v[192:195], v[8:11], v[128:131], 0\n\t"
      "v_mfma_f32_16x16x32_bf16 v[196:199], v[12:15], v[128:131], 0\n\t"
      "v_mfma_f32_16x16x32_bf16 v[184:187], v[16:19], v[132:135], v[184:187]\n\t"
      "v_mfma_f32_16x16x32_bf16 v[188:191], v[20:23], v[132:135], v[188:191]\n\t"
      "v_mfma_f32_16x16x32_bf16 v[192:195], v[24:27], v[132:135], v[192:195]\n\t"
      "v_mfma_f32_16x16x32_bf16 v[196:199], v[28:31], v[132:135], v[196:199]\n\t"
      "v_mfma_f32_16x16x32_bf16 v[184:187], v[32:35], v[136:139], v[184:187]\n\t"
      "v_mfma_f32_16x16x32_bf16 v[188:191], v[36:39], v[136:139], v[188:191]\n\t"
      "v_mfma_f32_16x16x32_bf16 v[192:195], v[40:43], v[136:139], v[192:195]\n\t"
      "v_mfma_f32_16x16x32_bf16 v[196:199], v[44:47], v[136:139], v[196:199]\n\t"
      "v_mfma_f32_16x16x32_bf16 v[184:187], v[48:51], v[140:143], v[184:187]\n\t"
      "v_mfma_f32_16x16x32_bf16 v[188:191], v[52:55], v[140:143], v[188:191]\n\t"
      "v_mfma_f32_16x16x32_bf16 v[192:195], v[56:59], v[140:143], v[192:195]\n\t"
      "v_mfma_f32_16x16x32_bf16 v[196:199], v[60:63], v[140:143], v[196:199]\n\t"
      "v_mfma_f32_16x16x32_bf16 v[184:187], v[64:67], v[144:147], v[184:187]\n\t"
      "v_mfma_f32_16x16x32_bf16 v[188:191], v[68:71], v[144:147], v[188:191]\n\t"
      "v_mfma_f32_16x16x32_bf16 v[192:195], v[72:75], v[144:147], v[192:195]\n\t"
      "v_mfma_f32_16x16x32_bf16 v[196:199], v[76:79], v[144:147], v[196:199]\n\t"
      "v_mfma_f32_16x16x32_bf16 v[184:187], v[80:83], v[148:151], v[184:187]\n\t"
      "v_mfma_f32_16x16x32_bf16 v[188:191], v[84:87], v[148:151], v[188:191]\n\t"
      "v_mfma_f32_16x16x32_bf16 v[192:195], v[88:91], v[148:151], v[192:195]\n\t"
      "v_mfma_f32_16x16x32_bf16 v[196:199], v[92:95], v[148:151], v[196:199]\n\t"
      "v_mfma_f32_16x16x32_bf16 v[184:187], v[96:99], v[152:155], v[184:187]\n\t"
      "v_mfma_f32_16x16x32_bf16 v[188:191], v[100:103], v[152:155], v[188:191]\n\t"
      "v_mfma_f32_16x16x32_bf16 v[192:195], v[104:107], v[152:155], v[192:195]\n\t"
      "v_mfma_f32_16x16x32_bf16 v[184:187], v[112:115], v[156:159], v[184:187]\n\t"
      "v_mfma_f32_16x16x32_bf16 v[188:191], v[116:119], v[156:159], v[188:191]\n\t"
      "v_mfma_f32_16x16x32_bf16 v[192:195], v[120:123], v[156:159], v[192:195]\n\t"
      "global_load_dwordx4 v[128:131], v176, s[62:63] sc1\n\t"
      "global_load_dwordx4 v[132:135], v177, s[62:63] sc1\n\t"
      "global_load_dwordx4 v[136:139], v178, s[62:63] sc1\n\t"
      "global_load_dwordx4 v[140:143], v179, s[62:63] sc1\n\t"
      "global_load_dwordx4 v[144:147], v180, s[62:63] sc1\n\t"
      "global_load_dwordx4 v[148:151], v181, s[62:63] sc1\n\t"
      "s_waitcnt vmcnt(0)\n\t"
      "s_nop 7\n\t"
      "s_branch NP4B_%=\n\t"
      "NP5R_%=:\n\t"
      "s_mov_b32 s51, 0\n\t"
      "NP5L_%=:\n\t"
      "s_add_u32 s51, s51, 1\n\t"
      "s_cmp_lt_u32 s51, 262144\n\t"
      "s_cbranch_scc0 NP5X_%=\n\t"
      "s_waitcnt vmcnt(0)\n\t"
      "s_sleep 1\n\t"
      "global_load_dwordx4 v[128:131], v176, s[62:63] sc1\n\t"
      "global_load_dwordx4 v[132:135], v177, s[62:63] sc1\n\t"
      "global_load_dwordx4 v[136:139], v178, s[62:63] sc1\n\t"
      "global_load_dwordx4 v[140:143], v179, s[62:63] sc1\n\t"
      "global_load_dwordx4 v[144:147], v180, s[62:63] sc1\n\t"
      "global_load_dwordx4 v[148:151], v181, s[62:63] sc1\n\t"
      "global_load_dwordx4 v[152:155], v182, s[62:63] sc1\n\t"
      "global_load_dwordx4 v[156:159], v183, s[62:63] sc1\n\t"
      "s_waitcnt vmcnt(0)\n\t"
      "v_max3_u32 v234, v128, v129, v130\n\t"
      "v_max3_u32 v234, v234, v131, v132\n\t"
      "v_max3_u32 v234, v234, v133, v134\n\t"
      "v_max3_u32 v234, v234, v135, v136\n\t"
      "v_max3_u32 v234, v234, v137, v138\n\t"
      "v_max3_u32 v234, v234, v139, v140\n\t"
      "v_max3_u32 v234, v234, v141, v142\n\t"
      "v_max3_u32 v234, v234, v143, v144\n\t"
      "v_max3_u32 v234, v234, v145, v146\n\t"
      "v_max3_u32 v234, v234, v147, v148\n\t"
      "v_max3_u32 v234, v234, v149, v150\n\t"
      "v_max3_u32 v234, v234, v151, v152\n\t"
      "v_max3_u32 v234, v234, v153, v154\n\t"
      "v_max3_u32 v234, v234, v155, v156\n\t"
      "v_max3_u32 v234, v234, v157, v158\n\t"
      "v_max_u32_e32 v234, v234, v159\n\t"
      "v_cmp_eq_u32_e32 vcc, -1, v234\n\t"
      "s_cbranch_vccnz NP5L_%=\n\t"
      "NP5X_%=:\n\t"
      "v_mfma_f32_16x16x32_bf16 v[200:203], v[0:3], v[128:131], 0\n\t"
      "v_mfma_f32_16x16x32_bf16 v[204:207], v[4:7], v[128:131], 0\n\t"
      "v_mfma_f32_16x16x32_bf16 v[208:211], v[8:11], v[128:131], 0\n\t"
      "v_mfma_f32_16x16x32_bf16 v[212:215], v[12:15], v[128:131], 0\n\t"
      "v_mfma_f32_16x16x32_bf16 v[200:203], v[16:19], v[132:135], v[200:203]\n\t"
      "v_mfma_f32_16x16x32_bf16 v[204:207], v[20:23], v[132:135], v[204:207]\n\t"
      "v_mfma_f32_16x16x32_bf16 v[208:211], v[24:27], v[132:135], v[208:211]\n\t"
      "v_mfma_f32_16x16x32_bf16 v[212:215], v[28:31], v[132:135], v[212:215]\n\t"
      "v_mfma_f32_16x16x32_bf16 v[200:203], v[32:35], v[136:139], v[200:203]\n\t"
      "v_mfma_f32_16x16x32_bf16 v[204:207], v[36:39], v[136:139], v[204:207]\n\t"
      "v_mfma_f32_16x16x32_bf16 v[208:211], v[40:43], v[136:139], v[208:211]\n\t"
      "v_mfma_f32_16x16x32_bf16 v[212:215], v[44:47], v[136:139], v[212:215]\n\t"
      "v_mfma_f32_16x16x32_bf16 v[200:203], v[48:51], v[140:143], v[200:203]\n\t"
      "v_mfma_f32_16x16x32_bf16 v[204:207], v[52:55], v[140:143], v[204:207]\n\t"
      "v_mfma_f32_16x16x32_bf16 v[208:211], v[56:59], v[140:143], v[208:211]\n\t"
      "v_mfma_f32_16x16x32_bf16 v[212:215], v[60:63], v[140:143], v[212:215]\n\t"
      "v_mfma_f32_16x16x32_bf16 v[200:203], v[64:67], v[144:147], v[200:203]\n\t"
      "v_mfma_f32_16x16x32_bf16 v[204:207], v[68:71], v[144:147], v[204:207]\n\t"
      "v_mfma_f32_16x16x32_bf16 v[208:211], v[72:75], v[144:147], v[208:211]\n\t"
      "v_mfma_f32_16x16x32_bf16 v[212:215], v[76:79], v[144:147], v[212:215]\n\t"
      "v_mfma_f32_16x16x32_bf16 v[200:203], v[80:83], v[148:151], v[200:203]\n\t"
      "v_mfma_f32_16x16x32_bf16 v[204:207], v[84:87], v[148:151], v[204:207]\n\t"
      "v_mfma_f32_16x16x32_bf16 v[208:211], v[88:91], v[148:151], v[208:211]\n\t"
      "v_mfma_f32_16x16x32_bf16 v[212:215], v[92:95], v[148:151], v[212:215]\n\t"
      "v_mfma_f32_16x16x32_bf16 v[200:203], v[96:99], v[152:155], v[200:203]\n\t"
      "v_mfma_f32_16x16x32_bf16 v[204:207], v[100:103], v[152:155], v[204:207]\n\t"
      "v_mfma_f32_16x16x32_bf16 v[208:211], v[104:107], v[152:155], v[208:211]\n\t"
      "v_mfma_f32_16x16x32_bf16 v[200:203], v[112:115], v[156:159], v[200:203]\n\t"
      "v_mfma_f32_16x16x32_bf16 v[204:207], v[116:119], v[156:159], v[204:207]\n\t"
      "v_mfma_f32_16x16x32_bf16 v[208:211], v[120:123], v[156:159], v[208:211]\n\t"
      "global_load_dwordx4 v[128:131], v176, s[64:65] sc1\n\t"
      "global_load_dwordx4 v[132:135], v177, s[64:65] sc1\n\t"
      "global_load_dwordx4 v[136:139], v178, s[64:65] sc1\n\t"
      "global_load_dwordx4 v[140:143], v179, s[64:65] sc1\n\t"
      "global_load_dwordx4 v[144:147], v180, s[64:65] sc1\n\t"
      "global_load_dwordx4 v[148:151], v181, s[64:65] sc1\n\t"
      "s_waitcnt vmcnt(0)\n\t"
      "s_nop 7\n\t"
      "s_branch NP5B_%=\n\t"
      "NP6R_%=:\n\t"
      "s_mov_b32 s51, 0\n\t"
      "NP6L_%=:\n\t"
      "s_add_u32 s51, s51, 1\n\t"
      "s_cmp_lt_u32 s51, 262144\n\t"
      "s_cbranch_scc0 NP6X_%=\n\t"
      "s_waitcnt vmcnt(0)\n\t"
      "s_sleep 1\n\t"
      "global_load_dwordx4 v[128:131], v176, s[64:65] sc1\n\t"
      "global_load_dwordx4 v[132:135], v177, s[64:65] sc1\n\t"
      "global_load_dwordx4 v[136:139], v178, s[64:65] sc1\n\t"
      "global_load_dwordx4 v[140:143], v179, s[64:65] sc1\n\t"
      "global_load_dwordx4 v[144:147], v180, s[64:65] sc1\n\t"
      "global_load_dwordx4 v[148:151], v181, s[64:65] sc1\n\t"
      "global_load_dwordx4 v[152:155], v182, s[64:65] sc1\n\t"
      "global_load_dwordx4 v[156:159], v183, s[64:65] sc1\n\t"
      "s_waitcnt vmcnt(0)\n\t"
      "v_max3_u32 v234, v128, v129, v130\n\t"
      "v_max3_u32 v234, v234, v131, v132\n\t"
      "v_max3_u32 v234, v234, v133, v134\n\t"
      "v_max3_u32 v234, v234, v135, v136\n\t"
      "v_max3_u32 v234, v234, v137, v138\n\t"
      "v_max3_u32 v234, v234, v139, v140\n\t"
      "v_max3_u32 v234, v234, v141, v142\n\t"
      "v_max3_u32 v234, v234, v143, v144\n\t"
      "v_max3_u32 v234, v234, v145, v146\n\t"
      "v_max3_u32 v234, v234, v147, v148\n\t"
      "v_max3_u32 v234, v234, v149, v150\n\t"
      "v_max3_u32 v234, v234, v151, v152\n\t"
      "v_max3_u32 v234, v234, v153, v154\n\t"
      "v_max3_u32 v234, v234, v155, v156\n\t"
      "v_max3_u32 v234, v234, v157, v158\n\t"
      "v_max_u32_e32 v234, v234, v159\n\t"
      "v_cmp_eq_u32_e32 vcc, -1, v234\n\t"
      "s_cbranch_vccnz NP6L_%=\n\t"
      "NP6X_%=:\n\t"
      "v_mfma_f32_16x16x32_bf16 v[184:187], v[0:3], v[128:131], 0\n\t"
      "v_mfma_f32_16x16x32_bf16 v[188:191], v[4:7], v[128:131], 0\n\t"
      "v_mfma_f32_16x16x32_bf16 v[192:195], v[8:11], v[128:131], 0\n\t"
      "v_mfma_f32_16x16x32_bf16 v[196:199], v[12:15], v[128:131], 0\n\t"
      "v_mfma_f32_16x16x32_bf16 v[184:187], v[16:19], v[132:135], v[184:187]\n\t"
      "v_mfma_f32_16x16x32_bf16 v[188:191], v[20:23], v[132:135], v[188:191]\n\t"
      "v_mfma_f32_16x16x32_bf16 v[192:195], v[24:27], v[132:135], v[192:195]\n\t"
      "v_mfma_f32_16x16x32_bf16 v[196:199], v[28:31], v[132:135], v[196:199]\n\t"
      "v_mfma_f32_16x16x32_bf16 v[184:187], v[32:35], v[136:139], v[184:187]\n\t"
      "v_mfma_f32_16x16x32_bf16 v[188:191], v[36:39], v[136:139], v[188:191]\n\t"
      "v_mfma_f32_16x16x32_bf16 v[192:195], v[40:43], v[136:139], v[192:195]\n\t"
      "v_mfma_f32_16x16x32_bf16 v[196:199], v[44:47], v[136:139], v[196:199]\n\t"
      "v_mfma_f32_16x16x32_bf16 v[184:187], v[48:51], v[140:143], v[184:187]\n\t"
      "v_mfma_f32_16x16x32_bf16 v[188:191], v[52:55], v[140:143], v[188:191]\n\t"
      "v_mfma_f32_16x16x32_bf16 v[192:195], v[56:59], v[140:143], v[192:195]\n\t"
      "v_mfma_f32_16x16x32_bf16 v[196:199], v[60:63], v[140:143], v[196:199]\n\t"
      "v_mfma_f32_16x16x32_bf16 v[184:187], v[64:67], v[144:147], v[184:187]\n\t"
      "v_mfma_f32_16x16x32_bf16 v[188:191], v[68:71], v[144:147], v[188:191]\n\t"
      "v_mfma_f32_16x16x32_bf16 v[192:195], v[72:75], v[144:147], v[192:195]\n\t"
      "v_mfma_f32_16x16x32_bf16 v[196:199], v[76:79], v[144:147], v[196:199]\n\t"
      "v_mfma_f32_16x16x32_bf16 v[184:187], v[80:83], v[148:151], v[184:187]\n\t"
      "v_mfma_f32_16x16x32_bf16 v[188:191], v[84:87], v[148:151], v[188:191]\n\t"
      "v_mfma_f32_16x16x32_bf16 v[192:195], v[88:91], v[148:151], v[192:195]\n\t"
      "v_mfma_f32_16x16x32_bf16 v[196:199], v[92:95], v[148:151], v[196:199]\n\t"
      "v_mfma_f32_16x16x32_bf16 v[184:187], v[96:99], v[152:155], v[184:187]\n\t"
      "v_mfma_f32_16x16x32_bf16 v[188:191], v[100:103], v[152:155], v[188:191]\n\t"
      "v_mfma_f32_16x16x32_bf16 v[192:195], v[104:107], v[152:155], v[192:195]\n\t"
      "v_mfma_f32_16x16x32_bf16 v[184:187], v[112:115], v[156:159], v[184:187]\n\t"
      "v_mfma_f32_16x16x32_bf16 v[188:191], v[116:119], v[156:159], v[188:191]\n\t"
      "v_mfma_f32_16x16x32_bf16 v[192:195], v[120:123], v[156:159], v[192:195]\n\t"
      "s_waitcnt vmcnt(0)\n\t"
      "s_nop 7\n\t"
      "s_branch NP6B_%=\n\t"
      "s_nop 0\n\t"
      "s_nop 0\n\t"
      "s_nop 0\n\t"
      "s_nop 0\n\t"
      "s_nop 0\n\t"
      "s_nop 0\n\t"
      "s_nop 0\n\t"
      "s_nop 0\n\t"
      "s_nop 0\n\t"
      "s_nop 0\n\t"
      "s_nop 0\n\t"
      "s_nop 0\n\t"
      "s_nop 0\n\t"
      "s_nop 0\n\t"
      "s_nop 0\n\t"
      "s_nop 0\n\t"
      "s_nop 0\n\t"
      "s_nop 0\n\t"
      "s_nop 0\n\t"
      "s_nop 0\n\t"
      "s_nop 0\n\t"
      "s_nop 0\n\t"
      "s_nop 0\n\t"
      "s_nop 0\n\t"
      "s_nop 0\n\t"
      "s_nop 0\n\t"
      "s_nop 0\n\t"
      "s_nop 0\n\t"
      "s_nop 0\n\t"
      "s_nop 0\n\t"
      "s_nop 0\n\t"
      "s_nop 0\n\t"
      "s_nop 0\n\t"
      "s_nop 0\n\t"
      "s_nop 0\n\t"
      "s_nop 0\n\t"
      "s_nop 0\n\t"
      "s_nop 0\n\t"
      "s_nop 0\n\t"
      "s_nop 0\n\t"
      "s_nop 0\n\t"
      "s_nop 0\n\t"
      "s_nop 0\n\t"
      "s_nop 0\n\t"
      "s_nop 0\n\t"
      "s_nop 0\n\t"
      "s_nop 0\n\t"
      "s_nop 0\n\t"
      "s_nop 0\n\t"
      "s_nop 0\n\t"
      "s_nop 0\n\t"
      "s_nop 0\n\t"
      "s_nop 0\n\t"
      "s_nop 0\n\t"
      "s_nop 0\n\t"
      "s_nop 0\n\t"
      "s_nop 0\n\t"
      "s_nop 0\n\t"
      "s_nop 0\n\t"
      "s_nop 0\n\t"
      "s_nop 0\n\t"
      "s_nop 0\n\t"
      "s_nop 0\n\t"
      "s_nop 0\n\t"
      "s_nop 0\n\t"
      "s_nop 0\n\t"
      "s_nop 0\n\t"
      "s_nop 0\n\t"
      "s_nop 0\n\t"
      "s_nop 0\n\t"
      "s_nop 0\n\t"
      "s_nop 0\n\t"
      "s_nop 0\n\t"
      "s_nop 0\n\t"
      "s_nop 0\n\t"
      "s_nop 0\n\t"
      "s_nop 0\n\t"
      "s_nop 0\n\t"
      "s_nop 0\n\t"
      "s_nop 0\n\t"
      "s_nop 0\n\t"
      "s_nop 0\n\t"
      "s_nop 0\n\t"
      "s_nop 0\n\t"
      "s_nop 0\n\t"
      "s_nop 0\n\t"
      "s_nop 0\n\t"
      "s_nop 0\n\t"
      "s_nop 0\n\t"
      "s_nop 0\n\t"
      "s_nop 0\n\t"
      "s_nop 0\n\t"
      "s_nop 0\n\t"
      "s_nop 0\n\t"
      "s_nop 0\n\t"
      "s_nop 0\n\t"
      "s_nop 0\n\t"
      "s_nop 0\n\t"
      "s_nop 0\n\t"
      "s_nop 0\n\t"
      "s_nop 0\n\t"
      "s_nop 0\n\t"
      "s_nop 0\n\t"
      "s_nop 0\n\t"
      "s_nop 0\n\t"
      "s_nop 0\n\t"
      "s_nop 0\n\t"
      "s_nop 0\n\t"
      "s_nop 0\n\t"
      "s_nop 0\n\t"
      "s_nop 0\n\t"
      "s_nop 0\n\t"
      "s_nop 0\n\t"
      "s_nop 0\n\t"
      "s_nop 0\n\t"
      "s_nop 0\n\t"
      "s_nop 0\n\t"
      "s_nop 0\n\t"
      "s_nop 0\n\t"
      "s_nop 0\n\t"
      "s_nop 0\n\t"
      "s_nop 0\n\t"
      "s_nop 0\n\t"
      "s_nop 0\n\t"
      "s_nop 0\n\t"
      "s_nop 0\n\t"
      "s_nop 0\n\t"
      "s_nop 0\n\t"
      "s_nop 0\n\t"
      "s_nop 0\n\t"
      "s_nop 0\n\t"
      "s_nop 0\n\t"
      "s_nop 0\n\t"
      "s_nop 0\n\t"
      "s_nop 0\n\t"
      "s_nop 0\n\t"
      "s_nop 0\n\t"
      "s_nop 0\n\t"
      "s_nop 0\n\t"
      "s_nop 0\n\t"
      "s_nop 0\n\t"
      "s_nop 0\n\t"
      "s_nop 0\n\t"
      "s_nop 0\n\t"
      "s_nop 0\n\t"
      "s_nop 0\n\t"
      "s_nop 0\n\t"
      "s_nop 0\n\t"
      "s_nop 0\n\t"
      "s_nop 0\n\t"
      "s_nop 0\n\t"
      "s_nop 0\n\t"
      "s_nop 0\n\t"
      "s_nop 0\n\t"
      "s_nop 0\n\t"
      "s_nop 0\n\t"
      "s_nop 0\n\t"
      "s_nop 0\n\t"
      "s_nop 0\n\t"
      "s_nop 0\n\t"
      "s_nop 0\n\t"
      "s_nop 0\n\t"
      "s_nop 0\n\t"
      "s_nop 0\n\t"
      "s_nop 0\n\t"
      "s_nop 0\n\t"
      "s_nop 0\n\t"
      "s_nop 0\n\t"
      "s_nop 0\n\t"
      "s_nop 0\n\t"
      "s_nop 0\n\t"
      "s_nop 0\n\t"
      "s_nop 0\n\t"
      "s_nop 0\n\t"
      "s_nop 0\n\t"
      "s_nop 0\n\t"
      "s_nop 0\n\t"
      "s_nop 0\n\t"
      "s_nop 0\n\t"
      "s_nop 0\n\t"
      "s_nop 0\n\t"
      "s_nop 0\n\t"
      "s_nop 0\n\t"
      "s_nop 0\n\t"
      "s_nop 0\n\t"
      "s_nop 0\n\t"
      "s_nop 0\n\t"
      "s_nop 0\n\t"
      "s_nop 0\n\t"
      "s_nop 0\n\t"
      "s_nop 0\n\t"
      "s_nop 0\n\t"
      "s_nop 0\n\t"
      "s_nop 0\n\t"
      "s_nop 0\n\t"
      "s_nop 0\n\t"
      "s_nop 0\n\t"
      "s_nop 0\n\t"
      "s_nop 0\n\t"
      "s_nop 0\n\t"
      "s_nop 0\n\t"
      "s_nop 0\n\t"
      "s_nop 0\n\t"
      "s_nop 0\n\t"
      "s_nop 0\n\t"
      "s_nop 0\n\t"
      "s_nop 0\n\t"
      "s_nop 0\n\t"
      "s_nop 0\n\t"
      "s_nop 0\n\t"
      "s_nop 0\n\t"
      "s_nop 0\n\t"
      "s_nop 0\n\t"
      "s_nop 0\n\t"
      "s_nop 0\n\t"
      "s_nop 0\n\t"
      "s_nop 0\n\t"
      "s_nop 0\n\t"
      "s_nop 0\n\t"
      "s_nop 0\n\t"
      "s_nop 0\n\t"
      "s_nop 0\n\t"
      "s_nop 0\n\t"
      "s_nop 0\n\t"
      "s_nop 0\n\t"
      "s_nop 0\n\t"
      "s_nop 0\n\t"
      "s_nop 0\n\t"
      "s_nop 0\n\t"
      "s_nop 0\n\t"
      "s_nop 0\n\t"
      "s_nop 0\n\t"
      "s_nop 0\n\t"
      "s_nop 0\n\t"
      "s_nop 0\n\t"
      "s_nop 0\n\t"
      "s_nop 0\n\t"
      "s_nop 0\n\t"
      "s_nop 0\n\t"
      "s_nop 0\n\t"
      "s_nop 0\n\t"
      "s_nop 0\n\t"
      "s_nop 0\n\t"
      "s_nop 0\n\t"
      "s_nop 0\n\t"
      "s_nop 0\n\t"
      "s_nop 0\n\t"
      "s_nop 0\n\t"
      "s_nop 0\n\t"
      "s_nop 0\n\t"
      "s_nop 0\n\t"
      "s_nop 0\n\t"
      "s_nop 0\n\t"
      "s_nop 0\n\t"
      "s_nop 0\n\t"
      "s_nop 0\n\t"
      "s_nop 0\n\t"
      "s_nop 0\n\t"
      "s_nop 0\n\t"
      "s_nop 0\n\t"
      "s_nop 0\n\t"
      "s_nop 0\n\t"
      "s_nop 0\n\t"
      "s_nop 0\n\t"
      "s_nop 0\n\t"
      "s_nop 0\n\t"
      "s_nop 0\n\t"
      "s_nop 0\n\t"
      "s_nop 0\n\t"
      "s_nop 0\n\t"
      "s_nop 0\n\t"
      "s_nop 0\n\t"
      "s_nop 0\n\t"
      "s_nop 0\n\t"
      "s_nop 0\n\t"
      "s_nop 0\n\t"
      "s_nop 0\n\t"
      "s_nop 0\n\t"
      "s_nop 0\n\t"
      "s_nop 0\n\t"
      "s_nop 0\n\t"
      "s_nop 0\n\t"
      "s_nop 0\n\t"
      "s_nop 0\n\t"
      "s_nop 0\n\t"
      "s_nop 0\n\t"
      "s_nop 0\n\t"
      "s_nop 0\n\t"
      "s_nop 0\n\t"
      "s_nop 0\n\t"
      "s_nop 0\n\t"
      "s_nop 0\n\t"
      "s_nop 0\n\t"
      "s_nop 0\n\t"
      "s_nop 0\n\t"
      "s_nop 0\n\t"
      "s_nop 0\n\t"
      "s_nop 0\n\t"
      "s_nop 0\n\t"
      "s_nop 0\n\t"
      "s_nop 0\n\t"
      "s_nop 0\n\t"
      "s_nop 0\n\t"
      "s_nop 0\n\t"
      "s_nop 0\n\t"
      "s_nop 0\n\t"
      "s_nop 0\n\t"
      "s_nop 0\n\t"
      "s_nop 0\n\t"
      "s_nop 0\n\t"
      "s_nop 0\n\t"
      "s_nop 0\n\t"
      "s_nop 0\n\t"
      "s_nop 0\n\t"
      "s_nop 0\n\t"
      "s_nop 0\n\t"
      "s_nop 0\n\t"
      "s_nop 0\n\t"
      "s_nop 0\n\t"
      "s_nop 0\n\t"
      "s_nop 0\n\t"
      "s_nop 0\n\t"
      "s_nop 0\n\t"
      "s_nop 0\n\t"
      "s_nop 0\n\t"
      "s_nop 0\n\t"
      "s_nop 0\n\t"
      "s_nop 0\n\t"
      "s_nop 0\n\t"
      "s_nop 0\n\t"
      "s_nop 0\n\t"
      "s_nop 0\n\t"
      "s_nop 0\n\t"
      "s_nop 0\n\t"
      "s_nop 0\n\t"
      "s_nop 0\n\t"
      "s_nop 0\n\t"
      "s_nop 0\n\t"
      "s_nop 0\n\t"
      "s_nop 0\n\t"
      "s_nop 0\n\t"
      "s_nop 0\n\t"
      "s_nop 0\n\t"
      "s_nop 0\n\t"
      "s_nop 0\n\t"
      "s_nop 0\n\t"
      "s_nop 0\n\t"
      "s_nop 0\n\t"
      "s_nop 0\n\t"
      "s_nop 0\n\t"
      "s_nop 0\n\t"
      "s_nop 0\n\t"
      "s_nop 0\n\t"
      "s_nop 0\n\t"
      "s_nop 0\n\t"
      "s_nop 0\n\t"
      "s_nop 0\n\t"
      "s_nop 0\n\t"
      "s_nop 0\n\t"
      "s_nop 0\n\t"
      "s_nop 0\n\t"
      "s_nop 0\n\t"
      "s_nop 0\n\t"
      "s_nop 0\n\t"
      "s_nop 0\n\t"
      "s_nop 0\n\t"
      "s_nop 0\n\t"
      "s_nop 0\n\t"
      "s_nop 0\n\t"
      "s_nop 0\n\t"
      "s_nop 0\n\t"
      "s_nop 0\n\t"
      "s_nop 0\n\t"
      "s_nop 0\n\t"
      "s_nop 0\n\t"
      "s_nop 0\n\t"
      "s_nop 0\n\t"
      "s_nop 0\n\t"
      "s_nop 0\n\t"
      "s_nop 0\n\t"
      "s_nop 0\n\t"
      "s_nop 0\n\t"
      "s_nop 0\n\t"
      "s_nop 0\n\t"
      "s_nop 0\n\t"
      "s_nop 0\n\t"
      "s_nop 0\n\t"
      "s_nop 0\n\t"
      "s_nop 0\n\t"
      "s_nop 0\n\t"
      "s_nop 0\n\t"
      "s_nop 0\n\t"
      "s_nop 0\n\t"
      "s_nop 0\n\t"
      "s_nop 0\n\t"
      "s_nop 0\n\t"
      "s_nop 0\n\t"
      "s_nop 0\n\t"
      "s_nop 0\n\t"
      "s_nop 0\n\t"
      "s_nop 0\n\t"
      "s_nop 0\n\t"
      "s_nop 0\n\t"
      "s_nop 0\n\t"
      "s_nop 0\n\t"
      "s_nop 0\n\t"
      "s_nop 0\n\t"
      "s_nop 0\n\t"
      "s_nop 0\n\t"
      "s_nop 0\n\t"
      "s_nop 0\n\t"
      "s_nop 0\n\t"
      "s_nop 0\n\t"
      "s_nop 0\n\t"
      "s_nop 0\n\t"
      "s_nop 0\n\t"
      "s_nop 0\n\t"
      "s_nop 0\n\t"
      "s_nop 0\n\t"
      "s_nop 0\n\t"
      "s_nop 0\n\t"
      "s_nop 0\n\t"
      "s_nop 0\n\t"
      "s_nop 0\n\t"
      "s_nop 0\n\t"
      "s_nop 0\n\t"
      "s_nop 0\n\t"
      "s_nop 0\n\t"
      "s_nop 0\n\t"
      "s_nop 0\n\t"
      "s_nop 0\n\t"
      "s_nop 0\n\t"
      "s_nop 0\n\t"
      "s_nop 0\n\t"
      "s_nop 0\n\t"
      "s_nop 0\n\t"
      "s_nop 0\n\t"
      "s_nop 0\n\t"
      "s_nop 0\n\t"
      "s_nop 0\n\t"
      "s_nop 0\n\t"
      "s_nop 0\n\t"
      "s_nop 0\n\t"
      "s_nop 0\n\t"
      "s_nop 0\n\t"
      "s_nop 0\n\t"
      "s_nop 0\n\t"
      "s_nop 0\n\t"
      "s_nop 0\n\t"
      "s_nop 0\n\t"
      "s_nop 0\n\t"
      "s_nop 0\n\t"
      "s_nop 0\n\t"
      "s_nop 0\n\t"
      "s_nop 0\n\t"
      "s_nop 0\n\t"
      "s_nop 0\n\t"
      "s_nop 0\n\t"
      "s_nop 0\n\t"
      "s_nop 0\n\t"
      "s_nop 0\n\t"
      "s_nop 0\n\t"
      "s_nop 0\n\t"
      "s_nop 0\n\t"
      "s_nop 0\n\t"
      "s_nop 0\n\t"
      "s_nop 0\n\t"
      "s_nop 0\n\t"
      "s_nop 0\n\t"
      "s_nop 0\n\t"
      "s_nop 0\n\t"
      "s_nop 0\n\t"
      "s_nop 0\n\t"
      "s_nop 0\n\t"
      "s_nop 0\n\t"
      "s_nop 0\n\t"
      "s_nop 0\n\t"
      "s_nop 0\n\t"
      "s_nop 0\n\t"
      "s_nop 0\n\t"
      "s_nop 0\n\t"
      "s_nop 0\n\t"
      "s_nop 0\n\t"
      "s_nop 0\n\t"
      "s_nop 0\n\t"
      "s_nop 0\n\t"
      "s_nop 0\n\t"
      "s_nop 0\n\t"
      "s_nop 0\n\t"
      "s_nop 0\n\t"
      "s_nop 0\n\t"
      "s_nop 0\n\t"
      "s_nop 0\n\t"
      "s_nop 0\n\t"
      "s_nop 0\n\t"
      "s_nop 0\n\t"
      "s_nop 0\n\t"
      "s_nop 0\n\t"
      "s_nop 0\n\t"
      "s_nop 0\n\t"
      "s_nop 0\n\t"
      "s_nop 0\n\t"
      "s_nop 0\n\t"
      "s_nop 0\n\t"
      "s_nop 0\n\t"
      "s_nop 0\n\t"
      "s_nop 0\n\t"
      "s_nop 0\n\t"
      "s_nop 0\n\t"
      "s_nop 0\n\t"
      "s_nop 0\n\t"
      "s_nop 0\n\t"
      "s_nop 0\n\t"
      "s_nop 0\n\t"
      "s_nop 0\n\t"
      "s_nop 0\n\t"
      "s_nop 0\n\t"
      "s_nop 0\n\t"
      "s_nop 0\n\t"
      "s_nop 0\n\t"
      "s_nop 0\n\t"
      "s_nop 0\n\t"
      "s_nop 0\n\t"
      "s_nop 0\n\t"
      "s_nop 0\n\t"
      "s_nop 0\n\t"
      "s_nop 0\n\t"
      "s_nop 0\n\t"
      "s_nop 0\n\t"
      "s_nop 0\n\t"
      "s_nop 0\n\t"
      "s_nop 0\n\t"
      "s_nop 0\n\t"
      "s_nop 0\n\t"
      "s_nop 0\n\t"
      "s_nop 0\n\t"
      "s_nop 0\n\t"
      "s_nop 0\n\t"
      "s_nop 0\n\t"
      "s_nop 0\n\t"
      "s_nop 0\n\t"
      "s_nop 0\n\t"
      "s_nop 0\n\t"
      "s_nop 0\n\t"
      "s_nop 0\n\t"
      "s_nop 0\n\t"
      "s_nop 0\n\t"
      "s_nop 0\n\t"
      "s_nop 0\n\t"
      "s_nop 0\n\t"
      "s_nop 0\n\t"
      "s_nop 0\n\t"
      "s_nop 0\n\t"
      "s_nop 0\n\t"
      "s_nop 0\n\t"
      "s_nop 0\n\t"
      "s_nop 0\n\t"
      "s_nop 0\n\t"
      "s_nop 0\n\t"
      "s_nop 0\n\t"
      "s_nop 0\n\t"
      "s_nop 0\n\t"
      "s_nop 0\n\t"
      "s_nop 0\n\t"
      "s_nop 0\n\t"
      "s_nop 0\n\t"
      "s_nop 0\n\t"
      "s_nop 0\n\t"
      "s_nop 0\n\t"
      "s_nop 0\n\t"
      "s_nop 0\n\t"
      "s_nop 0\n\t"
      "s_nop 0\n\t"
      "s_nop 0\n\t"
      "s_nop 0\n\t"
      "s_nop 0\n\t"
      "s_nop 0\n\t"
      "s_nop 0\n\t"
      "s_nop 0\n\t"
      "s_nop 0\n\t"
      "s_nop 0\n\t"
      "s_nop 0\n\t"
      "s_nop 0\n\t"
      "s_nop 0\n\t"
      "s_nop 0\n\t"
      "s_nop 0\n\t"
      "s_nop 0\n\t"
      "s_nop 0\n\t"
      "s_nop 0\n\t"
      "s_nop 0\n\t"
      "s_nop 0\n\t"
      "s_nop 0\n\t"
      "s_nop 0\n\t"
      "s_nop 0\n\t"
      "s_nop 0\n\t"
      "s_nop 0\n\t"
      "s_nop 0\n\t"
      "s_nop 0\n\t"
      "s_nop 0\n\t"
      "s_nop 0\n\t"
      "s_nop 0\n\t"
      "s_nop 0\n\t"
      "s_nop 0\n\t"
      "s_nop 0\n\t"
      "s_nop 0\n\t"
      "s_nop 0\n\t"
      "s_nop 0\n\t"
      "s_nop 0\n\t"
      "s_nop 0\n\t"
      "s_nop 0\n\t"
      "s_nop 0\n\t"
      "s_nop 0\n\t"
      "s_nop 0\n\t"
      "s_nop 0\n\t"
      "s_nop 0\n\t"
      "s_nop 0\n\t"
      "s_nop 0\n\t"
      "s_nop 0\n\t"
      "s_nop 0\n\t"
      "s_nop 0\n\t"
      "s_nop 0\n\t"
      "s_nop 0\n\t"
      "s_nop 0\n\t"
      "s_nop 0\n\t"
      "s_nop 0\n\t"
      "s_nop 0\n\t"
      "s_nop 0\n\t"
      "s_nop 0\n\t"
      "s_nop 0\n\t"
      "s_nop 0\n\t"
      "s_nop 0\n\t"
      "s_nop 0\n\t"
      "s_nop 0\n\t"
      "s_nop 0\n\t"
      "s_nop 0\n\t"
      "s_nop 0\n\t"
      "s_nop 0\n\t"
      "s_nop 0\n\t"
      "s_nop 0\n\t"
      "s_nop 0\n\t"
      "s_nop 0\n\t"
      "s_nop 0\n\t"
      "s_nop 0\n\t"
      "s_nop 0\n\t"
      "s_nop 0\n\t"
      "s_nop 0\n\t"
      "s_nop 0\n\t"
      "s_nop 0\n\t"
      "s_nop 0\n\t"
      "s_nop 0\n\t"
      "s_nop 0\n\t"
      "s_nop 0\n\t"
      "s_nop 0\n\t"
      "s_nop 0\n\t"
      "s_nop 0\n\t"
      "s_nop 0\n\t"
      "s_nop 0\n\t"
      "s_nop 0\n\t"
      "s_nop 0\n\t"
      "s_nop 0\n\t"
      "s_nop 0\n\t"
      "s_nop 0\n\t"
      "s_nop 0\n\t"
      "s_nop 0\n\t"
      "s_nop 0\n\t"
      "s_nop 0\n\t"
      "s_nop 0\n\t"
      "s_nop 0\n\t"
      "s_nop 0\n\t"
      "s_nop 0\n\t"
      "s_nop 0\n\t"
      "s_nop 0\n\t"
      "s_nop 0\n\t"
      "s_nop 0\n\t"
      "s_nop 0\n\t"
      "s_nop 0\n\t"
      "s_nop 0\n\t"
      "s_nop 0\n\t"
      "s_nop 0\n\t"
      "s_nop 0\n\t"
      "s_nop 0\n\t"
      "s_nop 0\n\t"
      "s_nop 0\n\t"
      "s_nop 0\n\t"
      "s_nop 0\n\t"
      "s_nop 0\n\t"
      "s_nop 0\n\t"
      "s_nop 0\n\t"
      : "+{s[4:19]}"(s), "+{s[20:21]}"(kr), "+{s22}"(hint), "+{v[240:247]}"(v)
      :
      : "memory", "vcc", "scc",
        "v0", "v1", "v2", "v3", "v4", "v5", "v6", "v7", "v8", "v9", "v10", "v11", "v12", "v13", "v14", "v15", "v16",
        "v17", "v18", "v19", "v20", "v21", "v22", "v23", "v24", "v25", "v26", "v27", "v28", "v29", "v30", "v31", "v32",
        "v33", "v34", "v35", "v36", "v37", "v38", "v39", "v40", "v41", "v42", "v43", "v44", "v45", "v46", "v47", "v48",
        "v49", "v50", "v51", "v52", "v53", "v54", "v55", "v56", "v57", "v58", "v59", "v60", "v61", "v62", "v63", "v64",
        "v65", "v66", "v67", "v68", "v69", "v70", "v71", "v72", "v73", "v74", "v75", "v76", "v77", "v78", "v79", "v80",
        "v81", "v82", "v83", "v84", "v85", "v86", "v87", "v88", "v89", "v90", "v91", "v92", "v93", "v94", "v95", "v96",
        "v97", "v98", "v99", "v100", "v101", "v102", "v103", "v104", "v105", "v106", "v107", "v108", "v109", "v110",
        "v111", "v112", "v113", "v114", "v115", "v116", "v117", "v118", "v119", "v120", "v121", "v122", "v123", "v124",
        "v125", "v126", "v127", "v128", "v129", "v130", "v131", "v132", "v133", "v134", "v135", "v136", "v137", "v138",
        "v139", "v140", "v141", "v142", "v143", "v144", "v145", "v146", "v147", "v148", "v149", "v150", "v151", "v152",
        "v153", "v154", "v155", "v156", "v157", "v158", "v159", "v160", "v161", "v162", "v163", "v164", "v165", "v166",
        "v167", "v168", "v169", "v170", "v171", "v172", "v173", "v174", "v175", "v176", "v177", "v178", "v179", "v180",
        "v181", "v182", "v183", "v184", "v185", "v186", "v187", "v188", "v189", "v190", "v191", "v192", "v193", "v194",
        "v195", "v196", "v197", "v198", "v199", "v200", "v201", "v202", "v203", "v204", "v205", "v206", "v207", "v208",
        "v209", "v210", "v211", "v212", "v213", "v214", "v215", "v216", "v217", "v218", "v219", "v220", "v221", "v222",
        "v223", "v224", "v225", "v226", "v227", "v228", "v229", "v230", "v231", "v232", "v233", "v234", "v235", "v236",
        "v237", "v238", "v239", "v248", "v249", "v250", "v251", "v252", "v253", "s23", "s44", "s45", "s46", "s47",
        "s48", "s49", "s50", "s51", "s52", "s53", "s54", "s55", "s56", "s57", "s58", "s59", "s60", "s61", "s62", "s63",
        "s64", "s65", "s66", "s67", "s68", "s69", "s70"
    );
    __builtin_amdgcn_endpgm();
  } else {
#define B_INDICES                                                                                            \
  const int ib = idx - 16, t = 16 * (4 * side + ((xp + XO) & 3)) + ib;                         \
  const int st = 8 * xp + (ib >> 1), rot = 4 * (ib & 1) + 2 * side - (ib & side & 1);                        \
  const int cb0 = 4 * wave - max(wave - 4, 0);               \
  const int k1 = (ib + 2 * xcd) & 15, k0 = k1 - (k1 >= 14 ? 7 : 0)
    u32x16 acc;
    u32x8 v1, s1;
    u32x4 s3;
    unsigned bits = (wave >> 2) | (wave == 0 ? 16 : 0) | ((idx >> 1) == 11 ? 4 : 0), id = blockIdx.x, wv = wave;
    {
      LANE_BITS(threadIdx.x & 63);
      B_INDICES;
      v1[0] = (8 * (wave & 3) + lr) * 896 + (wave >> 2) * 128 + ((lc ^ swz_tr(8 * wave + lr)) << 4) + 4096;
      v1[1] = B_X + (4 * g + q4) * 896 + cb0 * 32 + p4 * 8;
      v1[2] = B_PS + (4 * g + q4) * 64 + wave * 8;
      v1[3] = B_PS + (4 * g + p4) * 64 + q4 * 16;
      v1[4] = (t * 16 + 4 * g + p4) * 16;
      v1[5] = lr * 4096 + ((lc ^ swz_tr(lr)) << 4);
      v1[6] = lane * 16;
      v1[7] = WS_HN + (7 * (wave & 1) + 2 * (wave >> 2) + (g & 1)) * 131072 + (t >> 4) * 16384 + (wave & 2) * 4096 + (t & 15) * 512 + (g >> 1) * 256 + c * 16;
      put(s1, 0, (u64)(wo + k0 * 32 * 896 - 4096));
      put(s1, 2, (u64)(ws + WS_ST));
      put(s1, 4, 0);
      put(s1, 6, (u64)(w2 + st * 128 + (wave * 224 + rot * 32) * 4096));
      s3[0] = (wave & 3) * B_UNIT + (wave >> 2) * 1024;
      s3[1] = (wave >> 2) - 1;
      s3[2] = 13 - k0;
      s3[3] = wave * 4096;
    asm volatile(
      "s_waitcnt lgkmcnt(0)\n\ts_nop 4\n\t"
      ".p2align 3\n\t"
      "s_load_dwordx2 s[40:41], s[6:7], 0x20000\n\t"
      "v_mov_b32_e32 v248, -1\n\t"
      "v_mov_b32_e32 v249, -1\n\t"
      "v_mov_b32_e32 v250, -1\n\t"
      "v_mov_b32_e32 v251, -1\n\t"
      "v_add_u32_e32 v221, 4294965504, v224\n\t"
      "v_add_u32_e32 v222, 512, v224\n\t"
      "v_add_u32_e32 v223, 4294966016, v224\n\t"
      "s_bitcmp1_b32 s16, 0\n\t"
      "s_cselect_b64 s[46:47], -1, 0\n\t"
      "s_mov_b64 vcc, s[46:47]\n\t"
      "s_waitcnt vmcnt(3)\n\t"
      "s_barrier\n\t"
      "ds_read_b64_tr_b16 v[204:205], v225\n\t"
      "ds_read_b64_tr_b16 v[206:207], v225 offset:32\n\t"
      "ds_read_b64_tr_b16 v[208:209], v225 offset:64\n\t"
      "ds_read_b64_tr_b16 v[210:211], v225 offset:96\n\t"
      "s_waitcnt lgkmcnt(0)\n\t"
      "s_add_u32 s40, s40, 1\n\t"
      "s_addc_u32 s41, s41, 0\n\t"
      "s_mul_i32 s48, s40, 0x9e3779b9\n\t"
      "s_mul_hi_u32 s9, s40, 0x7f4a7c15\n\t"
      "s_add_u32 s9, s9, s48\n\t"
      "s_mul_i32 s48, s41, 0x7f4a7c15\n\t"
      "s_add_u32 s9, s9, s48\n\t"
      "s_mul_i32 s8, s40, 0x7f4a7c15\n\t"
      "s_add_u32 s8, s8, 0xd192ed03\n\t"
      "s_addc_u32 s9, s9, 0xd1b54a32\n\t"
      "v_mov_b32_e32 v188, s8\n\t"
      "v_mov_b32_e32 v189, s9\n\t"
      "v_mov_b32_e32 v190, s40\n\t"
      "v_mov_b32_e32 v191, s41\n\t"
      "v_mov_b32_e32 v192, 163600\n\t"
      "s_mov_b64 exec, 1\n\t"
      "ds_write_b128 v192, v[188:191]\n\t"
      "s_mov_b64 exec, -1\n\t"
      "v_and_b32_e32 v210, s13, v210\n\t"
      "v_and_b32_e32 v211, s13, v211\n\t"
      "v_lshlrev_b32_e32 v160, 16, v204\n\t"
      "v_and_b32_e32 v161, 0xffff0000, v204\n\t"
      "v_lshlrev_b32_e32 v162, 16, v205\n\t"
      "v_and_b32_e32 v163, 0xffff0000, v205\n\t"
      "v_lshlrev_b32_e32 v164, 16, v206\n\t"
      "v_and_b32_e32 v165, 0xffff0000, v206\n\t"
      "v_lshlrev_b32_e32 v166, 16, v207\n\t"
      "v_and_b32_e32 v167, 0xffff0000, v207\n\t"
      "v_lshlrev_b32_e32 v168, 16, v208\n\t"
      "v_and_b32_e32 v169, 0xffff0000, v208\n\t"
      "v_lshlrev_b32_e32 v170, 16, v209\n\t"
      "v_and_b32_e32 v171, 0xffff0000, v209\n\t"
      "v_lshlrev_b32_e32 v172, 16, v210\n\t"
      "v_and_b32_e32 v173, 0xffff0000, v210\n\t"
      "v_lshlrev_b32_e32 v174, 16, v211\n\t"
      "v_and_b32_e32 v175, 0xffff0000, v211\n\t"
      "v_pk_add_f32 v[188:189], v[160:161], v[164:165]\n\t"
      "v_pk_mul_f32 v[192:193], v[160:161], v[160:161]\n\t"
      "v_pk_add_f32 v[190:191], v[162:163], v[166:167]\n\t"
      "v_pk_mul_f32 v[194:195], v[162:163], v[162:163]\n\t"
      "v_pk_fma_f32 v[192:193], v[164:165], v[164:165], v[192:193]\n\t"
      "v_pk_fma_f32 v[194:195], v[166:167], v[166:167], v[194:195]\n\t"
      "v_pk_add_f32 v[188:189], v[188:189], v[168:169]\n\t"
      "v_pk_fma_f32 v[192:193], v[168:169], v[168:169], v[192:193]\n\t"
      "v_pk_add_f32 v[190:191], v[190:191], v[170:171]\n\t"
      "v_pk_fma_f32 v[194:195], v[170:171], v[170:171], v[194:195]\n\t"
      "s_cbranch_vccnz SB0_%=\n\t"
      "s_nop 0\n\t"
      "v_pk_add_f32 v[188:189], v[188:189], v[172:173]\n\t"
      "v_pk_fma_f32 v[192:193], v[172:173], v[172:173], v[192:193]\n\t"
      "v_pk_add_f32 v[190:191], v[190:191], v[174:175]\n\t"
      "v_pk_fma_f32 v[194:195], v[174:175], v[174:175], v[194:195]\n\t"
      "SB0_%=:\n\t"
      "v_add_f32_dpp v196, v188, v188 row_ror:8 row_mask:0xf bank_mask:0x3\n\t"
      "v_add_f32_dpp v197, v189, v189 row_ror:8 row_mask:0xf bank_mask:0x3\n\t"
      "v_add_f32_dpp v198, v192, v192 row_ror:8 row_mask:0xf bank_mask:0x3\n\t"
      "v_add_f32_dpp v199, v193, v193 row_ror:8 row_mask:0xf bank_mask:0x3\n\t"
      "v_add_f32_dpp v196, v190, v190 row_ror:8 row_mask:0xf bank_mask:0xc\n\t"
      "v_add_f32_dpp v197, v191, v191 row_ror:8 row_mask:0xf bank_mask:0xc\n\t"
      "v_add_f32_dpp v198, v194, v194 row_ror:8 row_mask:0xf bank_mask:0xc\n\t"
      "v_add_f32_dpp v199, v195, v195 row_ror:8 row_mask:0xf bank_mask:0xc\n\t"
      "v_add_f32_dpp v188, v196, v196 row_shl:4 row_mask:0xf bank_mask:0x5\n\t"
      "v_add_f32_dpp v189, v198, v198 row_shl:4 row_mask:0xf bank_mask:0x5\n\t"
      "s_nop 0\n\t"
      "v_add_f32_dpp v188, v197, v197 row_shr:4 row_mask:0xf bank_mask:0xa\n\t"
      "v_add_f32_dpp v189, v199, v199 row_shr:4 row_mask:0xf bank_mask:0xa\n\t"
      "s_nop 0\n\t"
      "v_add_f32_dpp v188, v188, v188 quad_perm:[1,0,3,2] row_mask:0xf bank_mask:0xf\n\t"
      "v_add_f32_dpp v189, v189, v189 quad_perm:[1,0,3,2] row_mask:0xf bank_mask:0xf\n\t"
      "s_nop 0\n\t"
      "v_add_f32_dpp v188, v188, v188 quad_perm:[2,3,0,1] row_mask:0xf bank_mask:0xf\n\t"
      "v_add_f32_dpp v189, v189, v189 quad_perm:[2,3,0,1] row_mask:0xf bank_mask:0xf\n\t"
      "ds_write_b64 v226, v[188:189]\n\t"
      "s_waitcnt lgkmcnt(0)\n\t"
      "s_barrier\n\t"
      "ds_read_b128 v[188:191], v227\n\t"
      "s_waitcnt lgkmcnt(0)\n\t"
      "v_pk_add_f32 v[188:189], v[188:189], v[190:191]\n\t"
      "s_nop 1\n\t"
      "v_add_f32_dpp v188, v188, v188 row_ror:4 row_mask:0xf bank_mask:0xf\n\t"
      "v_add_f32_dpp v189, v189, v189 row_ror:4 row_mask:0xf bank_mask:0xf\n\t"
      "s_nop 0\n\t"
      "v_add_f32_dpp v188, v188, v188 row_ror:8 row_mask:0xf bank_mask:0xf\n\t"
      "v_add_f32_dpp v189, v189, v189 row_ror:8 row_mask:0xf bank_mask:0xf\n\t"
      "v_mul_f32_e32 v188, 0x3b124925, v188\n\t"
      "v_mul_f32_e32 v189, 0x3b124925, v189\n\t"
      "v_fma_f32 v189, -v188, v188, v189\n\t"
      "v_max_f32_e32 v189, 0, v189\n\t"
      "s_bitcmp1_b32 s16, 4\n\t"
      "s_cbranch_scc0 GW_%=\n\t"
      "v_add_f32_e32 v189, 0x3727c5ac, v189\n\t"
      "v_rsq_f32_e32 v189, v189\n\t"
      "v_xor_b32_e32 v188, 0x80000000, v188\n\t"
      "v_xor_b32_e32 v190, s8, v188\n\t"
      "v_xor_b32_e32 v191, s9, v189\n\t"
      "s_mov_b32 s48, 0xf000f\n\t"
      "s_mov_b32 s49, 0xf000f\n\t"
      "s_mov_b64 exec, s[48:49]\n\t"
      "global_store_dwordx4 v228, v[188:191], s[6:7] sc1\n\t"
      "s_mov_b64 exec, -1\n\t"
      "GW_%=:\n\t"
      "s_add_u32 m0, s12, 0\n\t"
      "s_nop 0\n\t"
      "global_load_lds_dwordx4 v224, s[4:5]\n\t"
      "global_load_lds_dwordx4 v221, s[4:5] offset:2048\n\t"
      "s_add_u32 m0, s12, 4096\n\t"
      "s_nop 0\n\t"
      "global_load_lds_dwordx4 v222, s[4:5]\n\t"
      "s_cbranch_vccnz WR0_%=\n\t"
      "global_load_lds_dwordx4 v223, s[4:5] offset:2048\n\t"
      "WR0_%=:\n\t"
      "s_sub_u32 s14, s14, 1\n\t"
      "s_cselect_b32 s48, -13, 1\n\t"
      "s_cselect_b32 s49, -1, 0\n\t"
      "s_mul_i32 s48, s48, 28672\n\t"
      "s_add_u32 s4, s4, s48\n\t"
      "s_addc_u32 s5, s5, s49\n\t"
      "s_add_u32 m0, s12, 28672\n\t"
      "s_nop 0\n\t"
      "global_load_lds_dwordx4 v224, s[4:5]\n\t"
      "global_load_lds_dwordx4 v221, s[4:5] offset:2048\n\t"
      "s_add_u32 m0, s12, 32768\n\t"
      "s_nop 0\n\t"
      "global_load_lds_dwordx4 v222, s[4:5]\n\t"
      "s_cbranch_vccnz WR1_%=\n\t"
      "global_load_lds_dwordx4 v223, s[4:5] offset:2048\n\t"
      "WR1_%=:\n\t"
      "s_sub_u32 s14, s14, 1\n\t"
      "s_cselect_b32 s48, -13, 1\n\t"
      "s_cselect_b32 s49, -1, 0\n\t"
      "s_mul_i32 s48, s48, 28672\n\t"
      "s_add_u32 s4, s4, s48\n\t"
      "s_addc_u32 s5, s5, s49\n\t"
      "s_waitcnt lgkmcnt(0)\n\t"
      "s_nop 1\n\t"
      : "+{s[4:11]}"(s1), "+{s[12:15]}"(s3), "+{s16}"(bits), "+{s17}"(id), "+{s18}"(wv), "+{v[224:231]}"(v1), "={v[160:175]}"(acc)
      :
      : "memory", "vcc", "scc",
        "v176", "v177", "v178", "v179", "v180", "v181", "v182", "v183", "v184", "v185", "v186", "v187", "v188", "v189",
        "v190", "v191", "v192", "v193", "v194", "v195", "v196", "v197", "v198", "v199", "v200", "v201", "v202", "v203",
        "v204", "v205", "v206", "v207", "v208", "v209", "v210", "v211", "v212", "v213", "v214", "v215", "v216", "v217",
        "v218", "v219", "v220", "v221", "v222", "v223", "v248", "v249", "v250", "v251", "s40", "s41", "s42", "s43",
        "s44", "s45", "s46", "s47", "s48", "s49", "s50", "s51"
    );
    }
    PIN();
    {
    const int wave = wv & 0x3ffffff, xcd = id & 7, idx = id >> 3, side = xcd >> 2, xp = xcd & 3;
    B_INDICES;
    const int dg = 2 * side + (ib & 1), blk0 = 7 * dg, rn = (rot + W2E) % 7;
    bits |= (2 - ((wave >> 1) & 2)) | (((t + wave - 1) >> 26) & 32) | ((((wave - 7) & (t - 112)) >> 25) & 64) | (((t - 112) >> 24) & 128);
    u32x4 vo, v3;
    u64 par = ST_PW + xcd * 128;
    {
      LANE_BITS(v1[6] >> 4);
      u32x8 v;
      u32x4 s;
      const unsigned tv = t + (lane >> 6), wv = wave + (lane >> 6), sv = (tv >> 6) * 8192;
      const unsigned ab = g * B_UNIT + q4 * 128 + (p4 & 1) * 8, al = ((p4 >> 1) ^ swz_tr(8 * g + q4)) << 4;
      const unsigned oi = B_X + c * B_OP, b0 = 112 * wv + 8 * g - 64 * k0;
#pragma unroll
      for (int b = 0; b < 4; ++b) {
        const unsigned cb = 4 * wv - (wv >> 2) * (wv & 3) + b;
        v[b] = ab + (cb >> 2) * 1024 + (((cb & 3) << 5) ^ al);
        const unsigned by = b0 + 56 * (b >> 1) + 32 * (b & 1);
        vo[b] = oi + min(by, by + 896);
      }
      v[4] = oi + 16 * g;
      v[5] = WS_G + tv * 16384 + wv * 1024 + lane * 16;
      v[6] = v1[7] + 4 * 131072;
      v[7] = v[5] + 8192;
      v3[0] = (((wv & 3) + (lr & 2) * 2) * 224 + 32 + 64 * side + (tv & 15) * 2 + (lr & 1) + (lr >> 2) * 32) * 4096 + lc * 128 + xp * 1024;
      v3[1] = ST_H3 + (lc & 3) * 32768 + (lc >> 2) * 8192 + 2 * sv + (tv & 63) * 128;
      v3[2] = ST_H2 + sv + (tv & 32) * 512 + (tv & 24) * 256 + wv * 256 + (tv & 4) * 32 + (lane & 1) * 64;
      v3[3] = ST_CK + sv + (tv & 56) * 2048 + (tv & 4) * 1024 + wv * 512 + (g >> 1) * 256 + (tv & 3) * 64 + c * 4;
      put(s, 0, (u64)w2);
      put(s, 2, (u64)(ws + (unsigned)(WS_O + (t & 63) * 32768 + (wave >> 2) * 16384 + side * 8192 + (wave & 3) * 2048)));
    asm volatile(
      "s_waitcnt lgkmcnt(0)\n\ts_nop 4\n\t"
      ".p2align 3\n\t"
      "s_nop 0\n\t"
      "v_mov_b32_e32 v248, -1\n\t"
      "v_mov_b32_e32 v249, -1\n\t"
      "v_mov_b32_e32 v250, -1\n\t"
      "v_mov_b32_e64 v251, -1\n\t"
      "v_add_u32_e32 v221, 4294965504, v224\n\t"
      "v_add_u32_e32 v222, 512, v224\n\t"
      "v_add_u32_e32 v223, 4294966016, v224\n\t"
      "s_bitcmp1_b32 s16, 0\n\t"
      "s_cselect_b64 s[46:47], -1, 0\n\t"
      "s_mov_b64 vcc, s[46:47]\n\t"
      "s_mov_b32 s44, 0xffffffff\n\t"
      "s_mov_b32 s45, 0xffff\n\t"
      "s_or_b32 s42, s13, 0xffff\n\t"
      "s_or_b32 s43, s13, 0xffff\n\t"
      "v_xor_b32_e64 v176, 64, v229\n\t"
      "v_add_u32_e32 v177, 63488, v229\n\t"
      "v_add_u32_e32 v178, 95232, v176\n\t"
      "v_add_u32_e32 v176, 31744, v176\n\t"
      "s_add_u32 m0, s15, 130560\n\t"
      "s_nop 0\n\t"
      "global_load_lds_dwordx4 v229, s[10:11]\n\t"
      "global_load_lds_dwordx4 v176, s[10:11] offset:1024\n\t"
      "s_bitcmp1_b32 s16, 2\n\t"
      "s_cselect_b64 exec, 1, -1\n\t"
      "global_load_lds_dwordx4 v177, s[10:11] offset:2048\n\t"
      "s_mov_b64 exec, -1\n\t"
      "s_bitcmp1_b32 s16, 2\n\t"
      "s_cselect_b64 exec, 1, -1\n\t"
      "global_load_lds_dwordx4 v178, s[10:11] offset:3072\n\t"
      "s_mov_b64 exec, -1\n\t"
      "s_cbranch_vccnz SN3_%=\n\t"
      "global_store_dwordx4 v231, v[248:251], s[6:7] sc1\n\t"
      "SN3_%=:\n\t"
      "s_cbranch_vccnz WW0_%=\n\t"
      "s_waitcnt vmcnt(8)\n\t"
      "s_branch WX0_%=\n\t"
      "WW0_%=:\n\t"
      "s_waitcnt vmcnt(7)\n\t"
      "WX0_%=:\n\t"
      "s_waitcnt lgkmcnt(0)\n\t"
      "s_barrier\n\t"
      "ds_read_b64_tr_b16 v[0:1], v232\n\t"
      "ds_read_b64_tr_b16 v[2:3], v232 offset:512\n\t"
      "ds_read_b64_tr_b16 v[4:5], v233\n\t"
      "ds_read_b64_tr_b16 v[6:7], v233 offset:512\n\t"
      "ds_read_b64_tr_b16 v[8:9], v234\n\t"
      "ds_read_b64_tr_b16 v[10:11], v234 offset:512\n\t"
      "ds_read_b64_tr_b16 v[12:13], v235\n\t"
      "ds_read_b64_tr_b16 v[14:15], v235 offset:512\n\t"
      "s_add_u32 m0, s12, 57344\n\t"
      "s_nop 0\n\t"
      "global_load_lds_dwordx4 v224, s[4:5]\n\t"
      "global_load_lds_dwordx4 v221, s[4:5] offset:2048\n\t"
      "s_add_u32 m0, s12, 61440\n\t"
      "s_nop 0\n\t"
      "global_load_lds_dwordx4 v222, s[4:5]\n\t"
      "s_cbranch_vccnz WR2_%=\n\t"
      "global_load_lds_dwordx4 v223, s[4:5] offset:2048\n\t"
      "WR2_%=:\n\t"
      "s_sub_u32 s14, s14, 1\n\t"
      "s_cselect_b32 s48, -13, 1\n\t"
      "s_cselect_b32 s49, -1, 0\n\t"
      "s_mul_i32 s48, s48, 28672\n\t"
      "s_add_u32 s4, s4, s48\n\t"
      "s_addc_u32 s5, s5, s49\n\t"
      "s_mov_b64 exec, 0\n\t"
      "s_cmpk_eq_u32 s15, 0x0\n\t"
      "s_cbranch_scc1 .LhopB0\n\t"
      "s_cmpk_eq_u32 s15, 0x1000\n\t"
      "s_cbranch_scc1 .LhopB1\n\t"
      "s_cmpk_eq_u32 s15, 0x2000\n\t"
      "s_cbranch_scc1 .LhopB2\n\t"
      "s_cmpk_eq_u32 s15, 0x3000\n\t"
      "s_cbranch_scc1 .LhopB3\n\t"
      "s_cmpk_eq_u32 s15, 0x4000\n\t"
      "s_cbranch_scc1 .LbackB\n\t"
      "s_cmpk_eq_u32 s15, 0x5000\n\t"
      "s_cbranch_scc1 .LbackB\n\t"
      ".LbackB:\n\t"
      "s_mov_b64 exec, -1\n\t"
      "s_cbranch_vccnz WW1_%=\n\t"
      "s_waitcnt vmcnt(8)\n\t"
      "s_branch WX1_%=\n\t"
      "WW1_%=:\n\t"
      "s_waitcnt vmcnt(7)\n\t"
      "WX1_%=:\n\t"
      "s_waitcnt lgkmcnt(0)\n\t"
      "s_barrier\n\t"
      "ds_read_b64_tr_b16 v[16:17], v232 offset:28672\n\t"
      "ds_read_b64_tr_b16 v[18:19], v232 offset:29184\n\t"
      "ds_read_b64_tr_b16 v[20:21], v233 offset:28672\n\t"
      "ds_read_b64_tr_b16 v[22:23], v233 offset:29184\n\t"
      "ds_read_b64_tr_b16 v[24:25], v234 offset:28672\n\t"
      "ds_read_b64_tr_b16 v[26:27], v234 offset:29184\n\t"
      "ds_read_b64_tr_b16 v[28:29], v235 offset:28672\n\t"
      "ds_read_b64_tr_b16 v[30:31], v235 offset:29184\n\t"
      "s_add_u32 m0, s12, 86016\n\t"
      "s_nop 0\n\t"
      "global_load_lds_dwordx4 v224, s[4:5]\n\t"
      "global_load_lds_dwordx4 v221, s[4:5] offset:2048\n\t"
      "s_add_u32 m0, s12, 90112\n\t"
      "s_nop 0\n\t"
      "global_load_lds_dwordx4 v222, s[4:5]\n\t"
      "s_cbranch_vccnz WR3_%=\n\t"
      "global_load_lds_dwordx4 v223, s[4:5] offset:2048\n\t"
      "WR3_%=:\n\t"
      "s_sub_u32 s14, s14, 1\n\t"
      "s_cselect_b32 s48, -13, 1\n\t"
      "s_cselect_b32 s49, -1, 0\n\t"
      "s_mul_i32 s48, s48, 28672\n\t"
      "s_add_u32 s4, s4, s48\n\t"
      "s_addc_u32 s5, s5, s49\n\t"
      "v_add_u32_e32 v232, 57344, v232\n\t"
      "v_add_u32_e32 v233, 57344, v233\n\t"
      "v_add_u32_e32 v234, 57344, v234\n\t"
      "v_add_u32_e32 v235, 57344, v235\n\t"
      "s_cbranch_vccnz WW2_%=\n\t"
      "s_waitcnt vmcnt(4)\n\t"
      "s_branch WX2_%=\n\t"
      "WW2_%=:\n\t"
      "s_waitcnt vmcnt(3)\n\t"
      "WX2_%=:\n\t"
      "s_waitcnt lgkmcnt(0)\n\t"
      "s_barrier\n\t"
      "ds_read_b64_tr_b16 v[32:33], v232\n\t"
      "ds_read_b64_tr_b16 v[34:35], v232 offset:512\n\t"
      "ds_read_b64_tr_b16 v[36:37], v233\n\t"
      "ds_read_b64_tr_b16 v[38:39], v233 offset:512\n\t"
      "ds_read_b64_tr_b16 v[40:41], v234\n\t"
      "ds_read_b64_tr_b16 v[42:43], v234 offset:512\n\t"
      "ds_read_b64_tr_b16 v[44:45], v235\n\t"
      "ds_read_b64_tr_b16 v[46:47], v235 offset:512\n\t"
      "s_add_u32 m0, s12, 0\n\t"
      "s_nop 0\n\t"
      "global_load_lds_dwordx4 v224, s[4:5]\n\t"
      "global_load_lds_dwordx4 v221, s[4:5] offset:2048\n\t"
      "s_add_u32 m0, s12, 4096\n\t"
      "s_nop 0\n\t"
      "global_load_lds_dwordx4 v222, s[4:5]\n\t"
      "s_cbranch_vccnz WR4_%=\n\t"
      "global_load_lds_dwordx4 v223, s[4:5] offset:2048\n\t"
      "WR4_%=:\n\t"
      "s_sub_u32 s14, s14, 1\n\t"
      "s_cselect_b32 s48, -13, 1\n\t"
      "s_cselect_b32 s49, -1, 0\n\t"
      "s_mul_i32 s48, s48, 28672\n\t"
      "s_add_u32 s4, s4, s48\n\t"
      "s_addc_u32 s5, s5, s49\n\t"
      "s_cbranch_vccnz WW3_%=\n\t"
      "s_waitcnt vmcnt(4)\n\t"
      "s_branch WX3_%=\n\t"
      "WW3_%=:\n\t"
      "s_waitcnt vmcnt(3)\n\t"
      "WX3_%=:\n\t"
      "s_waitcnt lgkmcnt(0)\n\t"
      "s_barrier\n\t"
      "ds_read_b64_tr_b16 v[48:49], v232 offset:28672\n\t"
      "ds_read_b64_tr_b16 v[50:51], v232 offset:29184\n\t"
      "ds_read_b64_tr_b16 v[52:53], v233 offset:28672\n\t"
      "ds_read_b64_tr_b16 v[54:55], v233 offset:29184\n\t"
      "ds_read_b64_tr_b16 v[56:57], v234 offset:28672\n\t"
      "ds_read_b64_tr_b16 v[58:59], v234 offset:29184\n\t"
      "ds_read_b64_tr_b16 v[60:61], v235 offset:28672\n\t"
      "ds_read_b64_tr_b16 v[62:63], v235 offset:29184\n\t"
      "s_add_u32 m0, s12, 28672\n\t"
      "s_nop 0\n\t"
      "global_load_lds_dwordx4 v224, s[4:5]\n\t"
      "global_load_lds_dwordx4 v221, s[4:5] offset:2048\n\t"
      "s_add_u32 m0, s12, 32768\n\t"
      "s_nop 0\n\t"
      "global_load_lds_dwordx4 v222, s[4:5]\n\t"
      "s_cbranch_vccnz WR5_%=\n\t"
      "global_load_lds_dwordx4 v223, s[4:5] offset:2048\n\t"
      "WR5_%=:\n\t"
      "s_sub_u32 s14, s14, 1\n\t"
      "s_cselect_b32 s48, -13, 1\n\t"
      "s_cselect_b32 s49, -1, 0\n\t"
      "s_mul_i32 s48, s48, 28672\n\t"
      "s_add_u32 s4, s4, s48\n\t"
      "s_addc_u32 s5, s5, s49\n\t"
      "v_add_u32_e32 v232, 4294909952, v232\n\t"
      "v_add_u32_e32 v233, 4294909952, v233\n\t"
      "v_add_u32_e32 v234, 4294909952, v234\n\t"
      "v_add_u32_e32 v235, 4294909952, v235\n\t"
      "s_cbranch_vccnz WW4_%=\n\t"
      "s_waitcnt vmcnt(4)\n\t"
      "s_branch WX4_%=\n\t"
      "WW4_%=:\n\t"
      "s_waitcnt vmcnt(3)\n\t"
      "WX4_%=:\n\t"
      "s_waitcnt lgkmcnt(0)\n\t"
      "s_barrier\n\t"
      "ds_read_b64_tr_b16 v[64:65], v232\n\t"
      "ds_read_b64_tr_b16 v[66:67], v232 offset:512\n\t"
      "ds_read_b64_tr_b16 v[68:69], v233\n\t"
      "ds_read_b64_tr_b16 v[70:71], v233 offset:512\n\t"
      "ds_read_b64_tr_b16 v[72:73], v234\n\t"
      "ds_read_b64_tr_b16 v[74:75], v234 offset:512\n\t"
      "ds_read_b64_tr_b16 v[76:77], v235\n\t"
      "ds_read_b64_tr_b16 v[78:79], v235 offset:512\n\t"
      "s_add_u32 m0, s12, 57344\n\t"
      "s_nop 0\n\t"
      "global_load_lds_dwordx4 v224, s[4:5]\n\t"
      "global_load_lds_dwordx4 v221, s[4:5] offset:2048\n\t"
      "s_add_u32 m0, s12, 61440\n\t"
      "s_nop 0\n\t"
      "global_load_lds_dwordx4 v222, s[4:5]\n\t"
      "s_cbranch_vccnz WR6_%=\n\t"
      "global_load_lds_dwordx4 v223, s[4:5] offset:2048\n\t"
      "WR6_%=:\n\t"
      "s_sub_u32 s14, s14, 1\n\t"
      "s_cselect_b32 s48, -13, 1\n\t"
      "s_cselect_b32 s49, -1, 0\n\t"
      "s_mul_i32 s48, s48, 28672\n\t"
      "s_add_u32 s4, s4, s48\n\t"
      "s_addc_u32 s5, s5, s49\n\t"
      "s_cbranch_vccnz WW5_%=\n\t"
      "s_waitcnt vmcnt(4)\n\t"
      "s_branch WX5_%=\n\t"
      "WW5_%=:\n\t"
      "s_waitcnt vmcnt(3)\n\t"
      "WX5_%=:\n\t"
      "s_waitcnt lgkmcnt(0)\n\t"
      ".LhopB0:\n\t"
      "s_cbranch_execz .LbackB\n\t"
      "s_barrier\n\t"
      "ds_read_b64_tr_b16 v[80:81], v232 offset:28672\n\t"
      "ds_read_b64_tr_b16 v[82:83], v232 offset:29184\n\t"
      "ds_read_b64_tr_b16 v[84:85], v233 offset:28672\n\t"
      "ds_read_b64_tr_b16 v[86:87], v233 offset:29184\n\t"
      "ds_read_b64_tr_b16 v[88:89], v234 offset:28672\n\t"
      "ds_read_b64_tr_b16 v[90:91], v234 offset:29184\n\t"
      "ds_read_b64_tr_b16 v[92:93], v235 offset:28672\n\t"
      ".LhopB1:\n\t"
      "s_cbranch_execz .LbackB\n\t"
      "ds_read_b64_tr_b16 v[94:95], v235 offset:29184\n\t"
      "s_add_u32 m0, s12, 86016\n\t"
      "s_nop 0\n\t"
      "global_load_lds_dwordx4 v224, s[4:5]\n\t"
      "global_load_lds_dwordx4 v221, s[4:5] offset:2048\n\t"
      "s_add_u32 m0, s12, 90112\n\t"
      "s_nop 0\n\t"
      "global_load_lds_dwordx4 v222, s[4:5]\n\t"
      "s_cbranch_vccnz WR7_%=\n\t"
      "global_load_lds_dwordx4 v223, s[4:5] offset:2048\n\t"
      "WR7_%=:\n\t"
      "s_sub_u32 s14, s14, 1\n\t"
      "s_cselect_b32 s48, -13, 1\n\t"
      "s_cselect_b32 s49, -1, 0\n\t"
      "s_mul_i32 s48, s48, 28672\n\t"
      "s_add_u32 s4, s4, s48\n\t"
      "s_addc_u32 s5, s5, s49\n\t"
      "v_add_u32_e32 v232, 57344, v232\n\t"
      "v_add_u32_e32 v233, 57344, v233\n\t"
      "v_add_u32_e32 v234, 57344, v234\n\t"
      "v_add_u32_e32 v235, 57344, v235\n\t"
      "s_cbranch_vccnz WW6_%=\n\t"
      "s_waitcnt vmcnt(4)\n\t"
      "s_branch WX6_%=\n\t"
      "WW6_%=:\n\t"
      "s_waitcnt vmcnt(3)\n\t"
      "WX6_%=:\n\t"
      "s_waitcnt lgkmcnt(0)\n\t"
      "s_barrier\n\t"
      "ds_read_b64_tr_b16 v[96:97], v232\n\t"
      "ds_read_b64_tr_b16 v[98:99], v232 offset:512\n\t"
      "ds_read_b64_tr_b16 v[100:101], v233\n\t"
      "ds_read_b64_tr_b16 v[102:103], v233 offset:512\n\t"
      "ds_read_b64_tr_b16 v[104:105], v234\n\t"
      "ds_read_b64_tr_b16 v[106:107], v234 offset:512\n\t"
      "ds_read_b64_tr_b16 v[108:109], v235\n\t"
      "ds_read_b64_tr_b16 v[110:111], v235 offset:512\n\t"
      "s_add_u32 m0, s12, 0\n\t"
      "s_nop 0\n\t"
      "global_load_lds_dwordx4 v224, s[4:5]\n\t"
      "global_load_lds_dwordx4 v221, s[4:5] offset:2048\n\t"
      "s_add_u32 m0, s12, 4096\n\t"
      "s_nop 0\n\t"
      "global_load_lds_dwordx4 v222, s[4:5]\n\t"
      "s_cbranch_vccnz WR8_%=\n\t"
      "global_load_lds_dwordx4 v223, s[4:5] offset:2048\n\t"
      "WR8_%=:\n\t"
      "s_sub_u32 s14, s14, 1\n\t"
      "s_cselect_b32 s48, -13, 1\n\t"
      "s_cselect_b32 s49, -1, 0\n\t"
      "s_mul_i32 s48, s48, 28672\n\t"
      "s_add_u32 s4, s4, s48\n\t"
      "s_addc_u32 s5, s5, s49\n\t"
      "s_cbranch_vccnz WW7_%=\n\t"
      "s_waitcnt vmcnt(4)\n\t"
      "s_branch WX7_%=\n\t"
      "WW7_%=:\n\t"
      "s_waitcnt vmcnt(3)\n\t"
      "WX7_%=:\n\t"
      "s_waitcnt lgkmcnt(0)\n\t"
      "s_barrier\n\t"
      "ds_read_b64_tr_b16 v[112:113], v232 offset:28672\n\t"
      "ds_read_b64_tr_b16 v[114:115], v232 offset:29184\n\t"
      "ds_read_b64_tr_b16 v[116:117], v233 offset:28672\n\t"
      "ds_read_b64_tr_b16 v[118:119], v233 offset:29184\n\t"
      "ds_read_b64_tr_b16 v[120:121], v234 offset:28672\n\t"
      "ds_read_b64_tr_b16 v[122:123], v234 offset:29184\n\t"
      "ds_read_b64_tr_b16 v[124:125], v235 offset:28672\n\t"
      "ds_read_b64_tr_b16 v[126:127], v235 offset:29184\n\t"
      "s_add_u32 m0, s12, 28672\n\t"
      "s_nop 0\n\t"
      "global_load_lds_dwordx4 v224, s[4:5]\n\t"
      "global_load_lds_dwordx4 v221, s[4:5] offset:2048\n\t"
      "s_add_u32 m0, s12, 32768\n\t"
      "s_nop 0\n\t"
      "global_load_lds_dwordx4 v222, s[4:5]\n\t"
      "s_cbranch_vccnz WR9_%=\n\t"
      "global_load_lds_dwordx4 v223, s[4:5] offset:2048\n\t"
      "WR9_%=:\n\t"
      "s_sub_u32 s14, s14, 1\n\t"
      "s_cselect_b32 s48, -13, 1\n\t"
      "s_cselect_b32 s49, -1, 0\n\t"
      "s_mul_i32 s48, s48, 28672\n\t"
      "s_add_u32 s4, s4, s48\n\t"
      "s_addc_u32 s5, s5, s49\n\t"
      "v_add_u32_e32 v232, 4294909952, v232\n\t"
      "v_add_u32_e32 v233, 4294909952, v233\n\t"
      "v_add_u32_e32 v234, 4294909952, v234\n\t"
      "v_add_u32_e32 v235, 4294909952, v235\n\t"
      "s_cbranch_vccnz WW8_%=\n\t"
      "s_waitcnt vmcnt(4)\n\t"
      "s_branch WX8_%=\n\t"
      "WW8_%=:\n\t"
      "s_waitcnt vmcnt(3)\n\t"
      "WX8_%=:\n\t"
      "s_waitcnt lgkmcnt(0)\n\t"
      "s_barrier\n\t"
      "ds_read_b64_tr_b16 v[128:129], v232\n\t"
      "ds_read_b64_tr_b16 v[130:131], v232 offset:512\n\t"
      "ds_read_b64_tr_b16 v[132:133], v233\n\t"
      "ds_read_b64_tr_b16 v[134:135], v233 offset:512\n\t"
      "ds_read_b64_tr_b16 v[136:137], v234\n\t"
      "ds_read_b64_tr_b16 v[138:139], v234 offset:512\n\t"
      "ds_read_b64_tr_b16 v[140:141], v235\n\t"
      "ds_read_b64_tr_b16 v[142:143], v235 offset:512\n\t"
      "s_add_u32 m0, s12, 57344\n\t"
      "s_nop 0\n\t"
      "global_load_lds_dwordx4 v224, s[4:5]\n\t"
      "global_load_lds_dwordx4 v221, s[4:5] offset:2048\n\t"
      "s_add_u32 m0, s12, 61440\n\t"
      "s_nop 0\n\t"
      "global_load_lds_dwordx4 v222, s[4:5]\n\t"
      "s_cbranch_vccnz WR10_%=\n\t"
      "global_load_lds_dwordx4 v223, s[4:5] offset:2048\n\t"
      "WR10_%=:\n\t"
      "s_sub_u32 s14, s14, 1\n\t"
      "s_cselect_b32 s48, -13, 1\n\t"
      "s_cselect_b32 s49, -1, 0\n\t"
      "s_mul_i32 s48, s48, 28672\n\t"
      "s_add_u32 s4, s4, s48\n\t"
      "s_addc_u32 s5, s5, s49\n\t"
      "s_cbranch_vccnz WW9_%=\n\t"
      "s_waitcnt vmcnt(4)\n\t"
      "s_branch WX9_%=\n\t"
      "WW9_%=:\n\t"
      "s_waitcnt vmcnt(3)\n\t"
      "WX9_%=:\n\t"
      "s_waitcnt lgkmcnt(0)\n\t"
      "s_barrier\n\t"
      "ds_read_b64_tr_b16 v[144:145], v232 offset:28672\n\t"
      "ds_read_b64_tr_b16 v[146:147], v232 offset:29184\n\t"
      "ds_read_b64_tr_b16 v[148:149], v233 offset:28672\n\t"
      "ds_read_b64_tr_b16 v[150:151], v233 offset:29184\n\t"
      "ds_read_b64_tr_b16 v[152:153], v234 offset:28672\n\t"
      "ds_read_b64_tr_b16 v[154:155], v234 offset:29184\n\t"
      "ds_read_b64_tr_b16 v[156:157], v235 offset:28672\n\t"
      "ds_read_b64_tr_b16 v[158:159], v235 offset:29184\n\t"
      "s_add_u32 m0, s12, 86016\n\t"
      "s_nop 0\n\t"
      "global_load_lds_dwordx4 v224, s[4:5]\n\t"
      "global_load_lds_dwordx4 v221, s[4:5] offset:2048\n\t"
      "s_add_u32 m0, s12, 90112\n\t"
      "s_nop 0\n\t"
      "global_load_lds_dwordx4 v222, s[4:5]\n\t"
      "s_cbranch_vccnz WR11_%=\n\t"
      "global_load_lds_dwordx4 v223, s[4:5] offset:2048\n\t"
      "WR11_%=:\n\t"
      "s_sub_u32 s14, s14, 1\n\t"
      "s_cselect_b32 s48, -13, 1\n\t"
      "s_cselect_b32 s49, -1, 0\n\t"
      "s_mul_i32 s48, s48, 28672\n\t"
      "s_add_u32 s4, s4, s48\n\t"
      "s_addc_u32 s5, s5, s49\n\t"
      "v_add_u32_e32 v232, 57344, v232\n\t"
      "v_add_u32_e32 v233, 57344, v233\n\t"
      "v_add_u32_e32 v234, 57344, v234\n\t"
      "v_add_u32_e32 v235, 57344, v235\n\t"
      "v_min_u32_e32 v184, 32, v230\n\t"
      "v_add_u32_e32 v184, s28, v184\n\t"
      "global_load_dwordx4 v[180:183], v184, s[6:7] sc1\n\t"
      "s_cbranch_vccnz WW10_%=\n\t"
      "s_waitcnt vmcnt(5)\n\t"
      "s_branch WX10_%=\n\t"
      "WW10_%=:\n\t"
      "s_waitcnt vmcnt(4)\n\t"
      "WX10_%=:\n\t"
      "s_waitcnt lgkmcnt(0)\n\t"
      "s_barrier\n\t"
      "ds_read_b64_tr_b16 v[188:189], v232\n\t"
      "ds_read_b64_tr_b16 v[190:191], v232 offset:512\n\t"
      "ds_read_b64_tr_b16 v[192:193], v233\n\t"
      "ds_read_b64_tr_b16 v[194:195], v233 offset:512\n\t"
      "ds_read_b64_tr_b16 v[196:197], v234\n\t"
      "ds_read_b64_tr_b16 v[198:199], v234 offset:512\n\t"
      "ds_read_b64_tr_b16 v[200:201], v235\n\t"
      "ds_read_b64_tr_b16 v[202:203], v235 offset:512\n\t"
      "s_add_u32 m0, s12, 0\n\t"
      "s_nop 0\n\t"
      "global_load_lds_dwordx4 v224, s[4:5]\n\t"
      "global_load_lds_dwordx4 v221, s[4:5] offset:2048\n\t"
      "s_add_u32 m0, s12, 4096\n\t"
      "s_nop 0\n\t"
      "global_load_lds_dwordx4 v222, s[4:5]\n\t"
      "s_cbranch_vccnz WR12_%=\n\t"
      "global_load_lds_dwordx4 v223, s[4:5] offset:2048\n\t"
      "WR12_%=:\n\t"
      "s_sub_u32 s14, s14, 1\n\t"
      "s_cselect_b32 s48, -13, 1\n\t"
      "s_cselect_b32 s49, -1, 0\n\t"
      "s_mul_i32 s48, s48, 28672\n\t"
      "s_add_u32 s4, s4, s48\n\t"
      "s_addc_u32 s5, s5, s49\n\t"
      "s_cbranch_vccnz WW11_%=\n\t"
      "s_waitcnt vmcnt(5)\n\t"
      "s_branch WX11_%=\n\t"
      "WW11_%=:\n\t"
      "s_waitcnt vmcnt(4)\n\t"
      "WX11_%=:\n\t"
      "s_waitcnt lgkmcnt(0)\n\t"
      "s_barrier\n\t"
      "ds_read_b64_tr_b16 v[204:205], v232 offset:28672\n\t"
      "ds_read_b64_tr_b16 v[206:207], v232 offset:29184\n\t"
      "ds_read_b64_tr_b16 v[208:209], v233 offset:28672\n\t"
      "ds_read_b64_tr_b16 v[210:211], v233 offset:29184\n\t"
      "ds_read_b64_tr_b16 v[212:213], v234 offset:28672\n\t"
      "ds_read_b64_tr_b16 v[214:215], v234 offset:29184\n\t"
      "ds_read_b64_tr_b16 v[216:217], v235 offset:28672\n\t"
      "ds_read_b64_tr_b16 v[218:219], v235 offset:29184\n\t"
      "s_add_u32 m0, s12, 28672\n\t"
      "s_nop 0\n\t"
      "global_load_lds_dwordx4 v224, s[4:5]\n\t"
      "global_load_lds_dwordx4 v221, s[4:5] offset:2048\n\t"
      "s_add_u32 m0, s12, 32768\n\t"
      "s_nop 0\n\t"
      "global_load_lds_dwordx4 v222, s[4:5]\n\t"
      "s_cbranch_vccnz WR13_%=\n\t"
      "global_load_lds_dwordx4 v223, s[4:5] offset:2048\n\t"
      "WR13_%=:\n\t"
      "v_add_u32_e32 v232, 4294909952, v232\n\t"
      "v_add_u32_e32 v233, 4294909952, v233\n\t"
      "v_add_u32_e32 v234, 4294909952, v234\n\t"
      "v_add_u32_e32 v235, 4294909952, v235\n\t"
      "s_waitcnt vmcnt(6)\n\t"
      "v_xor_b32_e32 v182, v182, v180\n\t"
      "v_xor_b32_e64 v183, v183, v181\n\t"
      "v_cmp_ne_u64_e32 vcc, s[8:9], v[182:183]\n\t"
      "s_cbranch_vccnz PWR_%=\n\t"
      "PWB_%=:\n\t"
      "v_readlane_b32 s48, v180, 0\n\t"
      "v_readlane_b32 s28, v180, 1\n\t"
      "v_readlane_b32 s29, v180, 2\n\t"
      "v_mov_b32_e32 v252, s8\n\t"
      "v_mov_b32_e32 v253, s9\n\t"
      "s_xor_b32 s22, s22, s48\n\t"
      "v_xor_b32_e32 v231, s28, v231\n\t"
      "v_xor_b32_e32 v238, s28, v238\n\t"
      "v_xor_b32_e32 v246, s28, v246\n\t"
      "v_xor_b32_e32 v247, s28, v247\n\t"
      "v_xor_b32_e32 v245, s29, v245\n\t"
      "s_waitcnt lgkmcnt(0)\n\t"
      "s_waitcnt vmcnt(0)\n\t"
      "s_mov_b32 s51, 0\n\t"
      "s_branch QE_%=\n\t"
      ".p2align 6\n\t"
      "QL_%=:\n\t"
      "s_sleep 2\n\t"
      "s_add_u32 s51, s51, 1\n\t"
      "s_cmp_lt_u32 s51, 262144\n\t"
      "s_cbranch_scc0 QX_%=\n\t"
      "QE_%=:\n\t"
      "s_mov_b64 exec, 3\n\t"
      "v_mov_b64_e32 v[176:177], 0\n\t"
      "global_atomic_or_x2 v[176:177], v246, v[176:177], s[6:7] sc0 sc1\n\t"
      "s_waitcnt vmcnt(0)\n\t"
      "v_cmp_ne_u64_e32 vcc, s[8:9], v[176:177]\n\t"
      "s_mov_b64 exec, -1\n\t"
      "s_cbranch_vccnz QL_%=\n\t"
      "QX_%=:\n\t"
      "s_mov_b32 s51, 0\n\t"
      "s_branch OE_%=\n\t"
      "OL_%=:\n\t"
      "s_sleep 2\n\t"
      "s_add_u32 s51, s51, 1\n\t"
      "s_cmp_lt_u32 s51, 262144\n\t"
      "s_cbranch_scc0 OX_%=\n\t"
      "OE_%=:\n\t"
      "global_load_dwordx4 v[176:179], v230, s[22:23] sc1\n\t"
      "global_load_dwordx4 v[180:183], v230, s[22:23] offset:1024 sc1\n\t"
      "global_load_dword v186, v247, s[6:7] sc1\n\t"
      "s_waitcnt vmcnt(2)\n\t"
      "ds_write_b64 v240, v[176:177]\n\t"
      "s_mov_b64 exec, s[44:45]\n\t"
      "ds_write_b64 v241, v[178:179]\n\t"
      "s_mov_b64 exec, -1\n\t"
      "v_add3_u32 v184, v176, v177, v178\n\t"
      "v_add_u32_e32 v184, v184, v179\n\t"
      "s_waitcnt vmcnt(1)\n\t"
      "ds_write_b64 v242, v[180:181]\n\t"
      "s_mov_b64 exec, s[44:45]\n\t"
      "ds_write_b64 v243, v[182:183]\n\t"
      "s_mov_b64 exec, -1\n\t"
      "v_add3_u32 v185, v180, v181, v182\n\t"
      "v_add_u32_e32 v185, v185, v183\n\t"
      "s_nop 1\n\t"
      "v_permlane32_swap_b32_e32 v184, v185\n\t"
      "v_add_u32_e32 v184, v184, v185\n\t"
      "v_mov_b32_e32 v185, v184\n\t"
      "s_nop 1\n\t"
      "v_permlane16_swap_b32_e32 v184, v185\n\t"
      "v_add3_u32 v184, v184, v185, s8\n\t"
      "s_waitcnt vmcnt(0) lgkmcnt(0)\n\t"
      "v_cmp_ne_u32_e32 vcc, v184, v186\n\t"
      "s_cbranch_vccnz OL_%=\n\t"
      "OX_%=:\n\t"
      "s_mov_b64 vcc, s[46:47]\n\t"
      "s_waitcnt lgkmcnt(0)\n\t"
      "s_barrier\n\t"
      "s_cbranch_vccnz GY_%=\n\t"
      "ds_read_b128 v[176:179], v236\n\t"
      "ds_read_b128 v[180:183], v236 offset:64\n\t"
      "ds_read_b128 v[184:187], v236 offset:128\n\t"
      "ds_read_b128 v[240:243], v236 offset:192\n\t"
      "ds_read_b128 v[220:223], v236 offset:256\n\t"
      "s_waitcnt lgkmcnt(4)\n\t"
      "v_mfma_f32_16x16x32_bf16 v[160:163], v[176:179], v[0:3], v[160:163]\n\t"
      "s_waitcnt lgkmcnt(3)\n\t"
      "v_mfma_f32_16x16x32_bf16 v[160:163], v[180:183], v[16:19], v[160:163]\n\t"
      "v_mfma_f32_16x16x32_bf16 v[164:167], v[176:179], v[4:7], v[164:167]\n\t"
      "v_mfma_f32_16x16x32_bf16 v[164:167], v[180:183], v[20:23], v[164:167]\n\t"
      "v_mfma_f32_16x16x32_bf16 v[168:171], v[176:179], v[8:11], v[168:171]\n\t"
      "v_mfma_f32_16x16x32_bf16 v[168:171], v[180:183], v[24:27], v[168:171]\n\t"
      "v_mfma_f32_16x16x32_bf16 v[172:175], v[176:179], v[12:15], v[172:175]\n\t"
      "ds_read_b128 v[176:179], v236 offset:320\n\t"
      "v_mfma_f32_16x16x32_bf16 v[172:175], v[180:183], v[28:31], v[172:175]\n\t"
      "ds_read_b128 v[180:183], v236 offset:384\n\t"
      "s_waitcnt lgkmcnt(4)\n\t"
      "v_mfma_f32_16x16x32_bf16 v[160:163], v[184:187], v[32:35], v[160:163]\n\t"
      "s_waitcnt lgkmcnt(3)\n\t"
      "v_mfma_f32_16x16x32_bf16 v[160:163], v[240:243], v[48:51], v[160:163]\n\t"
      "ds_read_b64_tr_b16 v[0:1], v232\n\t"
      "ds_read_b64_tr_b16 v[2:3], v232 offset:512\n\t"
      "v_mfma_f32_16x16x32_bf16 v[164:167], v[184:187], v[36:39], v[164:167]\n\t"
      "v_mfma_f32_16x16x32_bf16 v[164:167], v[240:243], v[52:55], v[164:167]\n\t"
      "v_mfma_f32_16x16x32_bf16 v[168:171], v[184:187], v[40:43], v[168:171]\n\t"
      "v_mfma_f32_16x16x32_bf16 v[168:171], v[240:243], v[56:59], v[168:171]\n\t"
      "ds_read_b64_tr_b16 v[4:5], v233\n\t"
      "ds_read_b64_tr_b16 v[6:7], v233 offset:512\n\t"
      "v_mfma_f32_16x16x32_bf16 v[172:175], v[184:187], v[44:47], v[172:175]\n\t"
      "ds_read_b128 v[184:187], v236 offset:448\n\t"
      "v_mfma_f32_16x16x32_bf16 v[172:175], v[240:243], v[60:63], v[172:175]\n\t"
      "ds_read_b128 v[240:243], v236 offset:512\n\t"
      "s_waitcnt lgkmcnt(8)\n\t"
      "v_mfma_f32_16x16x32_bf16 v[160:163], v[220:223], v[64:67], v[160:163]\n\t"
      "s_waitcnt lgkmcnt(7)\n\t"
      "v_mfma_f32_16x16x32_bf16 v[160:163], v[176:179], v[80:83], v[160:163]\n\t"
      "ds_read_b64_tr_b16 v[8:9], v234\n\t"
      "ds_read_b64_tr_b16 v[10:11], v234 offset:512\n\t"
      "v_mfma_f32_16x16x32_bf16 v[164:167], v[220:223], v[68:71], v[164:167]\n\t"
      "v_mfma_f32_16x16x32_bf16 v[164:167], v[176:179], v[84:87], v[164:167]\n\t"
      "v_mfma_f32_16x16x32_bf16 v[168:171], v[220:223], v[72:75], v[168:171]\n\t"
      "v_mfma_f32_16x16x32_bf16 v[168:171], v[176:179], v[88:91], v[168:171]\n\t"
      "ds_read_b64_tr_b16 v[12:13], v235\n\t"
      "ds_read_b64_tr_b16 v[14:15], v235 offset:512\n\t"
      "v_mfma_f32_16x16x32_bf16 v[172:175], v[220:223], v[76:79], v[172:175]\n\t"
      "ds_read_b128 v[220:223], v236 offset:576\n\t"
      "v_mfma_f32_16x16x32_bf16 v[172:175], v[176:179], v[92:95], v[172:175]\n\t"
      "ds_read_b128 v[176:179], v236 offset:640\n\t"
      "s_waitcnt lgkmcnt(12)\n\t"
      "v_mfma_f32_16x16x32_bf16 v[160:163], v[180:183], v[96:99], v[160:163]\n\t"
      "s_waitcnt lgkmcnt(7)\n\t"
      "v_mfma_f32_16x16x32_bf16 v[160:163], v[184:187], v[112:115], v[160:163]\n\t"
      "ds_read_b64_tr_b16 v[16:17], v232 offset:28672\n\t"
      "ds_read_b64_tr_b16 v[18:19], v232 offset:29184\n\t"
      "v_mfma_f32_16x16x32_bf16 v[164:167], v[180:183], v[100:103], v[164:167]\n\t"
      "v_mfma_f32_16x16x32_bf16 v[164:167], v[184:187], v[116:119], v[164:167]\n\t"
      "v_mfma_f32_16x16x32_bf16 v[168:171], v[180:183], v[104:107], v[168:171]\n\t"
      "v_mfma_f32_16x16x32_bf16 v[168:171], v[184:187], v[120:123], v[168:171]\n\t"
      "ds_read_b64_tr_b16 v[20:21], v233 offset:28672\n\t"
      "ds_read_b64_tr_b16 v[22:23], v233 offset:29184\n\t"
      "v_mfma_f32_16x16x32_bf16 v[172:175], v[180:183], v[108:111], v[172:175]\n\t"
      "ds_read_b128 v[180:183], v236 offset:704\n\t"
      "v_mfma_f32_16x16x32_bf16 v[172:175], v[184:187], v[124:127], v[172:175]\n\t"
      "ds_read_b128 v[184:187], v236 offset:768\n\t"
      "s_waitcnt lgkmcnt(12)\n\t"
      "v_mfma_f32_16x16x32_bf16 v[160:163], v[240:243], v[128:131], v[160:163]\n\t"
      "s_waitcnt lgkmcnt(7)\n\t"
      "v_mfma_f32_16x16x32_bf16 v[160:163], v[220:223], v[144:147], v[160:163]\n\t"
      "ds_read_b64_tr_b16 v[24:25], v234 offset:28672\n\t"
      "ds_read_b64_tr_b16 v[26:27], v234 offset:29184\n\t"
      "v_mfma_f32_16x16x32_bf16 v[164:167], v[240:243], v[132:135], v[164:167]\n\t"
      "v_mfma_f32_16x16x32_bf16 v[164:167], v[220:223], v[148:151], v[164:167]\n\t"
      "v_mfma_f32_16x16x32_bf16 v[168:171], v[240:243], v[136:139], v[168:171]\n\t"
      "v_mfma_f32_16x16x32_bf16 v[168:171], v[220:223], v[152:155], v[168:171]\n\t"
      "ds_read_b64_tr_b16 v[28:29], v235 offset:28672\n\t"
      "ds_read_b64_tr_b16 v[30:31], v235 offset:29184\n\t"
      "v_mfma_f32_16x16x32_bf16 v[172:175], v[240:243], v[140:143], v[172:175]\n\t"
      "ds_read_b128 v[240:243], v236 offset:832\n\t"
      "v_mfma_f32_16x16x32_bf16 v[172:175], v[220:223], v[156:159], v[172:175]\n\t"
      "s_waitcnt lgkmcnt(11)\n\t"
      "v_mfma_f32_16x16x32_bf16 v[160:163], v[176:179], v[188:191], v[160:163]\n\t"
      "s_waitcnt lgkmcnt(6)\n\t"
      "v_mfma_f32_16x16x32_bf16 v[160:163], v[180:183], v[204:207], v[160:163]\n\t"
      "v_mfma_f32_16x16x32_bf16 v[164:167], v[176:179], v[192:195], v[164:167]\n\t"
      "v_mfma_f32_16x16x32_bf16 v[164:167], v[180:183], v[208:211], v[164:167]\n\t"
      "v_mfma_f32_16x16x32_bf16 v[168:171], v[176:179], v[196:199], v[168:171]\n\t"
      "v_mfma_f32_16x16x32_bf16 v[168:171], v[180:183], v[212:215], v[168:171]\n\t"
      "v_mfma_f32_16x16x32_bf16 v[172:175], v[176:179], v[200:203], v[172:175]\n\t"
      "v_mfma_f32_16x16x32_bf16 v[172:175], v[180:183], v[216:219], v[172:175]\n\t"
      "s_waitcnt lgkmcnt(5)\n\t"
      "v_mfma_f32_16x16x32_bf16 v[160:163], v[184:187], v[0:3], v[160:163]\n\t"
      "s_waitcnt lgkmcnt(0)\n\t"
      "v_mfma_f32_16x16x32_bf16 v[160:163], v[240:243], v[16:19], v[160:163]\n\t"
      "v_mfma_f32_16x16x32_bf16 v[164:167], v[184:187], v[4:7], v[164:167]\n\t"
      "s_bitcmp1_b32 s16, 4\n\t"
      "s_cbranch_scc0 HI_%=\n\t"
      "s_mov_b64 exec, 255\n\t"
      "global_store_dwordx2 v245, v[252:253], s[6:7] sc1\n\t"
      "s_mov_b64 exec, -1\n\t"
      "HI_%=:\n\t"
      "v_mfma_f32_16x16x32_bf16 v[164:167], v[240:243], v[20:23], v[164:167]\n\t"
      "v_mfma_f32_16x16x32_bf16 v[168:171], v[184:187], v[8:11], v[168:171]\n\t"
      "v_mfma_f32_16x16x32_bf16 v[168:171], v[240:243], v[24:27], v[168:171]\n\t"
      "v_mfma_f32_16x16x32_bf16 v[172:175], v[184:187], v[12:15], v[172:175]\n\t"
      "v_mfma_f32_16x16x32_bf16 v[172:175], v[240:243], v[28:31], v[172:175]\n\t"
      "v_mul_f32_e32 v192, v160, v160\n\t"
      "v_mul_f32_e32 v193, v161, v161\n\t"
      "v_mul_f32_e32 v194, v162, v162\n\t"
      "v_mul_f32_e32 v195, v163, v163\n\t"
      "v_add_f32_e32 v188, v160, v164\n\t"
      "v_add_f32_e32 v189, v161, v165\n\t"
      "v_add_f32_e32 v190, v162, v166\n\t"
      "v_add_f32_e32 v191, v163, v167\n\t"
      "v_fmac_f32_e32 v192, v164, v164\n\t"
      "v_fmac_f32_e32 v193, v165, v165\n\t"
      "v_fmac_f32_e32 v194, v166, v166\n\t"
      "v_fmac_f32_e32 v195, v167, v167\n\t"
      "v_add_f32_e32 v188, v188, v168\n\t"
      "v_add_f32_e32 v189, v189, v169\n\t"
      "v_add_f32_e32 v190, v190, v170\n\t"
      "v_add_f32_e32 v191, v191, v171\n\t"
      "v_fmac_f32_e32 v192, v168, v168\n\t"
      "v_fmac_f32_e32 v193, v169, v169\n\t"
      "v_fmac_f32_e32 v194, v170, v170\n\t"
      "v_fmac_f32_e32 v195, v171, v171\n\t"
      "v_add_f32_e32 v188, v188, v172\n\t"
      "v_add_f32_e32 v189, v189, v173\n\t"
      "v_add_f32_e32 v190, v190, v174\n\t"
      "v_add_f32_e32 v191, v191, v175\n\t"
      "v_fmac_f32_e32 v192, v172, v172\n\t"
      "v_fmac_f32_e32 v193, v173, v173\n\t"
      "v_fmac_f32_e32 v194, v174, v174\n\t"
      "v_fmac_f32_e32 v195, v175, v175\n\t"
      "s_nop 1\n\t"
      "s_branch GJ_%=\n\t"
      "GY_%=:\n\t"
      ".LhopB2:\n\t"
      "s_cbranch_execz .LbackB\n\t"
      "v_add_u32_e32 v240, 262144, v231\n\t"
      "global_store_dwordx4 v231, v[248:251], s[6:7] sc1\n\t"
      "global_store_dwordx4 v240, v[248:251], s[6:7] sc1\n\t"
      "s_mov_b64 exec, s[42:43]\n\t"
      "global_store_dwordx4 v238, v[248:251], s[6:7] sc1\n\t"
      "s_mov_b64 exec, -1\n\t"
      "ds_read_b128 v[176:179], v236\n\t"
      "ds_read_b128 v[180:183], v236 offset:64\n\t"
      "ds_read_b128 v[184:187], v236 offset:128\n\t"
      "ds_read_b128 v[12:15], v236 offset:192\n\t"
      "ds_read_b128 v[28:31], v236 offset:256\n\t"
      "ds_read_b128 v[44:47], v236 offset:320\n\t"
      "ds_read_b128 v[60:63], v236 offset:384\n\t"
      "ds_read_b128 v[76:79], v236 offset:448\n\t"
      "ds_read_b128 v[92:95], v236 offset:512\n\t"
      "ds_read_b128 v[108:111], v236 offset:576\n\t"
      "ds_read_b128 v[124:127], v236 offset:640\n\t"
      "ds_read_b128 v[140:143], v236 offset:704\n\t"
      "ds_read_b128 v[156:159], v236 offset:768\n\t"
      "ds_read_b128 v[200:203], v236 offset:832\n\t"
      "s_bitcmp1_b32 s16, 7\n\t"
      "s_cbranch_scc0 SY2_%=\n\t"
      "global_store_dwordx4 v237, v[248:251], s[6:7] offset:-4096 sc1\n\t"
      "global_store_dwordx4 v239, v[248:251], s[6:7] offset:-4096 sc1\n\t"
      "SY2_%=:\n\t"
      "s_bitcmp1_b32 s16, 6\n\t"
      "s_cbranch_scc0 SG2_%=\n\t"
      "global_store_dwordx4 v237, v[248:251], s[6:7] sc1\n\t"
      "global_store_dwordx4 v239, v[248:251], s[6:7] sc1\n\t"
      "SG2_%=:\n\t"
      "s_waitcnt lgkmcnt(13)\n\t"
      "v_mfma_f32_16x16x32_bf16 v[160:163], v[176:179], v[0:3], v[160:163]\n\t"
      "s_waitcnt lgkmcnt(12)\n\t"
      "v_mfma_f32_16x16x32_bf16 v[160:163], v[180:183], v[16:19], v[160:163]\n\t"
      "s_waitcnt lgkmcnt(11)\n\t"
      "v_mfma_f32_16x16x32_bf16 v[160:163], v[184:187], v[32:35], v[160:163]\n\t"
      "s_waitcnt lgkmcnt(10)\n\t"
      "v_mfma_f32_16x16x32_bf16 v[160:163], v[12:15], v[48:51], v[160:163]\n\t"
      "ds_read_b64_tr_b16 v[0:1], v232\n\t"
      "ds_read_b64_tr_b16 v[2:3], v232 offset:512\n\t"
      "s_waitcnt lgkmcnt(11)\n\t"
      "v_mfma_f32_16x16x32_bf16 v[160:163], v[28:31], v[64:67], v[160:163]\n\t"
      "ds_read_b64_tr_b16 v[16:17], v232 offset:28672\n\t"
      "ds_read_b64_tr_b16 v[18:19], v232 offset:29184\n\t"
      "s_waitcnt lgkmcnt(12)\n\t"
      "v_mfma_f32_16x16x32_bf16 v[160:163], v[44:47], v[80:83], v[160:163]\n\t"
      "s_waitcnt lgkmcnt(11)\n\t"
      "v_mfma_f32_16x16x32_bf16 v[160:163], v[60:63], v[96:99], v[160:163]\n\t"
      "s_waitcnt lgkmcnt(10)\n\t"
      "v_mfma_f32_16x16x32_bf16 v[160:163], v[76:79], v[112:115], v[160:163]\n\t"
      "s_waitcnt lgkmcnt(9)\n\t"
      "v_mfma_f32_16x16x32_bf16 v[160:163], v[92:95], v[128:131], v[160:163]\n\t"
      "s_waitcnt lgkmcnt(8)\n\t"
      "v_mfma_f32_16x16x32_bf16 v[160:163], v[108:111], v[144:147], v[160:163]\n\t"
      "s_waitcnt lgkmcnt(7)\n\t"
      "v_mfma_f32_16x16x32_bf16 v[160:163], v[124:127], v[188:191], v[160:163]\n\t"
      "s_waitcnt lgkmcnt(6)\n\t"
      "v_mfma_f32_16x16x32_bf16 v[160:163], v[140:143], v[204:207], v[160:163]\n\t"
      "s_waitcnt lgkmcnt(2)\n\t"
      "v_mfma_f32_16x16x32_bf16 v[160:163], v[156:159], v[0:3], v[160:163]\n\t"
      "s_waitcnt lgkmcnt(0)\n\t"
      "v_mfma_f32_16x16x32_bf16 v[160:163], v[200:203], v[16:19], v[160:163]\n\t"
      "v_mfma_f32_16x16x32_bf16 v[164:167], v[176:179], v[4:7], v[164:167]\n\t"
      "v_mfma_f32_16x16x32_bf16 v[164:167], v[180:183], v[20:23], v[164:167]\n\t"
      "v_mfma_f32_16x16x32_bf16 v[164:167], v[184:187], v[36:39], v[164:167]\n\t"
      "v_mfma_f32_16x16x32_bf16 v[164:167], v[12:15], v[52:55], v[164:167]\n\t"
      "ds_read_b64_tr_b16 v[4:5], v233\n\t"
      "ds_read_b64_tr_b16 v[6:7], v233 offset:512\n\t"
      "v_mfma_f32_16x16x32_bf16 v[164:167], v[28:31], v[68:71], v[164:167]\n\t"
      "ds_read_b64_tr_b16 v[20:21], v233 offset:28672\n\t"
      "ds_read_b64_tr_b16 v[22:23], v233 offset:29184\n\t"
      "v_mfma_f32_16x16x32_bf16 v[164:167], v[44:47], v[84:87], v[164:167]\n\t"
      "v_mfma_f32_16x16x32_bf16 v[164:167], v[60:63], v[100:103], v[164:167]\n\t"
      "v_mul_f32_e32 v80, v160, v160\n\t"
      "v_mul_f32_e32 v81, v161, v161\n\t"
      "v_mfma_f32_16x16x32_bf16 v[164:167], v[76:79], v[116:119], v[164:167]\n\t"
      "v_mul_f32_e32 v82, v162, v162\n\t"
      "v_mul_f32_e32 v83, v163, v163\n\t"
      "v_mfma_f32_16x16x32_bf16 v[164:167], v[92:95], v[132:135], v[164:167]\n\t"
      "v_mfma_f32_16x16x32_bf16 v[164:167], v[108:111], v[148:151], v[164:167]\n\t"
      "v_mfma_f32_16x16x32_bf16 v[164:167], v[124:127], v[192:195], v[164:167]\n\t"
      "v_mfma_f32_16x16x32_bf16 v[164:167], v[140:143], v[208:211], v[164:167]\n\t"
      "s_waitcnt lgkmcnt(2)\n\t"
      "v_mfma_f32_16x16x32_bf16 v[164:167], v[156:159], v[4:7], v[164:167]\n\t"
      "s_waitcnt lgkmcnt(0)\n\t"
      "v_mfma_f32_16x16x32_bf16 v[164:167], v[200:203], v[20:23], v[164:167]\n\t"
      "v_mfma_f32_16x16x32_bf16 v[168:171], v[176:179], v[8:11], v[168:171]\n\t"
      "v_mfma_f32_16x16x32_bf16 v[168:171], v[180:183], v[24:27], v[168:171]\n\t"
      "v_mfma_f32_16x16x32_bf16 v[168:171], v[184:187], v[40:43], v[168:171]\n\t"
      "v_mfma_f32_16x16x32_bf16 v[168:171], v[12:15], v[56:59], v[168:171]\n\t"
      "ds_read_b64_tr_b16 v[8:9], v234\n\t"
      "ds_read_b64_tr_b16 v[10:11], v234 offset:512\n\t"
      "v_mfma_f32_16x16x32_bf16 v[168:171], v[28:31], v[72:75], v[168:171]\n\t"
      "ds_read_b64_tr_b16 v[24:25], v234 offset:28672\n\t"
      "ds_read_b64_tr_b16 v[26:27], v234 offset:29184\n\t"
      "v_mfma_f32_16x16x32_bf16 v[168:171], v[44:47], v[88:91], v[168:171]\n\t"
      "v_mfma_f32_16x16x32_bf16 v[168:171], v[60:63], v[104:107], v[168:171]\n\t"
      "v_add_f32_e32 v64, v160, v164\n\t"
      "v_add_f32_e32 v65, v161, v165\n\t"
      "v_mfma_f32_16x16x32_bf16 v[168:171], v[76:79], v[120:123], v[168:171]\n\t"
      "v_add_f32_e32 v66, v162, v166\n\t"
      "v_add_f32_e32 v67, v163, v167\n\t"
      "v_mfma_f32_16x16x32_bf16 v[168:171], v[92:95], v[136:139], v[168:171]\n\t"
      "v_fmac_f32_e32 v80, v164, v164\n\t"
      "v_fmac_f32_e32 v81, v165, v165\n\t"
      "v_mfma_f32_16x16x32_bf16 v[168:171], v[108:111], v[152:155], v[168:171]\n\t"
      "v_fmac_f32_e64 v82, v166, v166\n\t"
      "v_fmac_f32_e32 v83, v167, v167\n\t"
      "v_mfma_f32_16x16x32_bf16 v[168:171], v[124:127], v[196:199], v[168:171]\n\t"
      "v_mfma_f32_16x16x32_bf16 v[168:171], v[140:143], v[212:215], v[168:171]\n\t"
      "s_waitcnt lgkmcnt(2)\n\t"
      "v_mfma_f32_16x16x32_bf16 v[168:171], v[156:159], v[8:11], v[168:171]\n\t"
      "s_waitcnt lgkmcnt(0)\n\t"
      "v_mfma_f32_16x16x32_bf16 v[168:171], v[200:203], v[24:27], v[168:171]\n\t"
      "s_nop 7\n\t"
      "v_pk_add_f32 v[188:189], v[64:65], v[168:169]\n\t"
      "v_pk_fma_f32 v[192:193], v[168:169], v[168:169], v[80:81]\n\t"
      "v_pk_add_f32 v[190:191], v[66:67], v[170:171]\n\t"
      "v_pk_fma_f32 v[194:195], v[170:171], v[170:171], v[82:83]\n\t"
      "GJ_%=:\n\t"
      "v_add_f32_dpp v196, v188, v188 row_ror:8 row_mask:0xf bank_mask:0x3\n\t"
      "v_add_f32_dpp v197, v189, v189 row_ror:8 row_mask:0xf bank_mask:0x3\n\t"
      "v_add_f32_dpp v198, v192, v192 row_ror:8 row_mask:0xf bank_mask:0x3\n\t"
      "v_add_f32_dpp v199, v193, v193 row_ror:8 row_mask:0xf bank_mask:0x3\n\t"
      "v_add_f32_dpp v196, v190, v190 row_ror:8 row_mask:0xf bank_mask:0xc\n\t"
      "v_add_f32_dpp v197, v191, v191 row_ror:8 row_mask:0xf bank_mask:0xc\n\t"
      "v_add_f32_dpp v198, v194, v194 row_ror:8 row_mask:0xf bank_mask:0xc\n\t"
      "v_add_f32_dpp v199, v195, v195 row_ror:8 row_mask:0xf bank_mask:0xc\n\t"
      "v_add_f32_dpp v188, v196, v196 row_shl:4 row_mask:0xf bank_mask:0x5\n\t"
      "v_add_f32_dpp v189, v198, v198 row_shl:4 row_mask:0xf bank_mask:0x5\n\t"
      "s_nop 0\n\t"
      "v_add_f32_dpp v188, v197, v197 row_shr:4 row_mask:0xf bank_mask:0xa\n\t"
      "v_add_f32_dpp v189, v199, v199 row_shr:4 row_mask:0xf bank_mask:0xa\n\t"
      "s_nop 0\n\t"
      "v_add_f32_dpp v188, v188, v188 quad_perm:[1,0,3,2] row_mask:0xf bank_mask:0xf\n\t"
      "v_add_f32_dpp v189, v189, v189 quad_perm:[1,0,3,2] row_mask:0xf bank_mask:0xf\n\t"
      "s_nop 0\n\t"
      "v_add_f32_dpp v188, v188, v188 quad_perm:[2,3,0,1] row_mask:0xf bank_mask:0xf\n\t"
      "v_add_f32_dpp v189, v189, v189 quad_perm:[2,3,0,1] row_mask:0xf bank_mask:0xf\n\t"
      "ds_write_b64 v226, v[188:189]\n\t"
      "s_cbranch_vccz DR_%=\n\t"
      "s_waitcnt vmcnt(0)\n\t"
      "DR_%=:\n\t"
      "s_waitcnt lgkmcnt(0)\n\t"
      "s_barrier\n\t"
      "ds_read_b128 v[188:191], v227\n\t"
      "s_waitcnt lgkmcnt(0)\n\t"
      "v_pk_add_f32 v[188:189], v[188:189], v[190:191]\n\t"
      "s_nop 1\n\t"
      "v_add_f32_dpp v188, v188, v188 row_ror:4 row_mask:0xf bank_mask:0xf\n\t"
      "v_add_f32_dpp v189, v189, v189 row_ror:4 row_mask:0xf bank_mask:0xf\n\t"
      "s_nop 0\n\t"
      "v_add_f32_dpp v188, v188, v188 row_ror:8 row_mask:0xf bank_mask:0xf\n\t"
      "v_add_f32_dpp v189, v189, v189 row_ror:8 row_mask:0xf bank_mask:0xf\n\t"
      "v_mul_f32_e32 v188, 0x3b124925, v188\n\t"
      "v_mul_f32_e32 v189, 0x3b124925, v189\n\t"
      "v_fma_f32 v189, -v188, v188, v189\n\t"
      "v_max_f32_e64 v189, 0, v189\n\t"
      "v_add_f32_e32 v190, 0x3727c5ac, v189\n\t"
      "v_fmac_f32_e32 v189, 0x3727c5ac, v190\n\t"
      "v_rsq_f32_e64 v191, v189\n\t"
      "s_nop 0\n\t"
      "v_mul_f32_e64 v190, -v188, v191\n\t"
      "v_mov_b32_dpp v192, v191 quad_perm:[0,0,0,0] row_mask:0xf bank_mask:0xf\n\t"
      "s_nop 0\n\t"
      "v_mov_b32_dpp v196, v190 quad_perm:[0,0,0,0] row_mask:0xf bank_mask:0xf\n\t"
      "v_mov_b32_dpp v193, v191 quad_perm:[1,1,1,1] row_mask:0xf bank_mask:0xf\n\t"
      "v_mov_b32_dpp v197, v190 quad_perm:[1,1,1,1] row_mask:0xf bank_mask:0xf\n\t"
      "v_mov_b32_dpp v194, v191 quad_perm:[2,2,2,2] row_mask:0xf bank_mask:0xf\n\t"
      "v_mov_b32_dpp v198, v190 quad_perm:[2,2,2,2] row_mask:0xf bank_mask:0xf\n\t"
      "v_mov_b32_dpp v195, v191 quad_perm:[3,3,3,3] row_mask:0xf bank_mask:0xf\n\t"
      "v_mov_b32_dpp v199, v190 quad_perm:[3,3,3,3] row_mask:0xf bank_mask:0xf\n\t"
      "v_pk_fma_f32 v[160:161], v[160:161], v[192:193], v[196:197]\n\t"
      "v_pk_fma_f32 v[162:163], v[162:163], v[194:195], v[198:199]\n\t"
      "v_pk_fma_f32 v[164:165], v[164:165], v[192:193], v[196:197]\n\t"
      "v_pk_fma_f32 v[166:167], v[166:167], v[194:195], v[198:199]\n\t"
      "v_cvt_pk_bf16_f32 v212, v160, v161\n\t"
      "v_cvt_pk_bf16_f32 v213, v162, v163\n\t"
      "v_cvt_pk_bf16_f32 v214, v164, v165\n\t"
      "v_cvt_pk_bf16_f32 v215, v166, v167\n\t"
      "v_min_u32_e32 v212, -2, v212\n\t"
      "v_min_u32_e32 v213, -2, v213\n\t"
      "v_min_u32_e64 v214, -2, v214\n\t"
      "v_min_u32_e32 v215, -2, v215\n\t"
      "s_nop 0\n\t"
      "v_permlane16_swap_b32_e32 v212, v214\n\t"
      "v_permlane16_swap_b32_e32 v213, v215\n\t"
      "global_store_dwordx4 v231, v[212:215], s[6:7] sc1\n\t"
      "v_pk_fma_f32 v[168:169], v[168:169], v[192:193], v[196:197]\n\t"
      "v_pk_fma_f32 v[170:171], v[170:171], v[194:195], v[198:199]\n\t"
      "v_pk_fma_f32 v[172:173], v[172:173], v[192:193], v[196:197]\n\t"
      "v_pk_fma_f32 v[174:175], v[174:175], v[194:195], v[198:199]\n\t"
      "v_cvt_pk_bf16_f32 v216, v168, v169\n\t"
      "v_cvt_pk_bf16_f32 v217, v170, v171\n\t"
      "v_cvt_pk_bf16_f32 v218, v172, v173\n\t"
      "v_cvt_pk_bf16_f32 v219, v174, v175\n\t"
      "v_min_u32_e32 v216, -2, v216\n\t"
      "v_min_u32_e32 v217, -2, v217\n\t"
      "v_min_u32_e32 v218, -2, v218\n\t"
      "v_min_u32_e32 v219, -2, v219\n\t"
      "s_nop 0\n\t"
      "v_permlane16_swap_b32_e32 v216, v218\n\t"
      "v_permlane16_swap_b32_e32 v217, v219\n\t"
      "s_mov_b64 exec, s[42:43]\n\t"
      "global_store_dwordx4 v238, v[216:219], s[6:7] sc1\n\t"
      "s_mov_b64 exec, -1\n\t"
      "v_xor_b32_e32 v0, 64, v229\n\t"
      "v_add_u32_e32 v1, 63488, v229\n\t"
      "v_add_u32_e32 v2, 95232, v0\n\t"
      "v_add_u32_e32 v0, 31744, v0\n\t"
      "s_add_u32 m0, s15, 130560\n\t"
      "s_bitcmp1_b32 s16, 2\n\t"
      "s_cselect_b64 exec, -1, 1\n\t"
      "global_load_lds_dwordx4 v1, s[10:11] offset:2048\n\t"
      "s_mov_b64 exec, -1\n\t"
      "s_bitcmp1_b32 s16, 2\n\t"
      "s_cselect_b64 exec, -1, 1\n\t"
      "global_load_lds_dwordx4 v2, s[10:11] offset:3072\n\t"
      "s_mov_b64 exec, -1\n\t"
      "s_nop 1\n\t"
      "v_mov_b32_e32 v240, v188\n\t"
      "v_mul_f32_e32 v241, v189, v191\n\t"
      "v_add_u32_e32 v246, 65536, v228\n\t"
      "v_xor_b32_e32 v242, s8, v188\n\t"
      "v_xor_b32_e32 v243, s9, v241\n\t"
      "s_nop 1\n\t"
      "s_branch BFX_%=\n\t"
      "PWR_%=:\n\t"
      "s_mov_b32 s51, 0\n\t"
      "PWL_%=:\n\t"
      "s_add_u32 s51, s51, 1\n\t"
      "s_cmp_lt_u32 s51, 262144\n\t"
      "s_cbranch_scc0 PWX_%=\n\t"
      "s_sleep 1\n\t"
      "v_min_u32_e32 v184, 32, v230\n\t"
      "v_add_u32_e32 v184, s28, v184\n\t"
      "global_load_dwordx4 v[180:183], v184, s[6:7] sc1\n\t"
      "s_waitcnt vmcnt(0)\n\t"
      "v_xor_b32_e32 v182, v182, v180\n\t"
      "v_xor_b32_e32 v183, v183, v181\n\t"
      "v_cmp_ne_u64_e32 vcc, s[8:9], v[182:183]\n\t"
      "s_cbranch_vccnz PWL_%=\n\t"
      "PWX_%=:\n\t"
      "v_and_b32_e32 v180, 0x2000, v180\n\t"
      "s_nop 1\n\t"
      "s_branch PWB_%=\n\t"
      "BFX_%=:\n\t"
      ".LhopB3:\n\t"
      "s_cbranch_execz .LbackB\n\t"
      : "+{s[4:11]}"(s1), "+{s[12:15]}"(s3), "+{s16}"(bits), "+{s[20:23]}"(s), "+{s[28:29]}"(par), "+{v[160:175]}"(acc), "+{v[224:231]}"(v1), "+{v[232:239]}"(v), "+{v[240:243]}"(vo), "+{v[244:247]}"(v3)
      :
      : "memory", "vcc", "scc",
        "v0", "v1", "v2", "v3", "v4", "v5", "v6", "v7", "v8", "v9", "v10", "v11", "v12", "v13", "v14", "v15", "v16",
        "v17", "v18", "v19", "v20", "v21", "v22", "v23", "v24", "v25", "v26", "v27", "v28", "v29", "v30", "v31", "v32",
        "v33", "v34", "v35", "v36", "v37", "v38", "v39", "v40", "v41", "v42", "v43", "v44", "v45", "v46", "v47", "v48",
        "v49", "v50", "v51", "v52", "v53", "v54", "v55", "v56", "v57", "v58", "v59", "v60", "v61", "v62", "v63", "v64",
        "v65", "v66", "v67", "v68", "v69", "v70", "v71", "v72", "v73", "v74", "v75", "v76", "v77", "v78", "v79", "v80",
        "v81", "v82", "v83", "v84", "v85", "v86", "v87", "v88", "v89", "v90", "v91", "v92", "v93", "v94", "v95", "v96",
        "v97", "v98", "v99", "v100", "v101", "v102", "v103", "v104", "v105", "v106", "v107", "v108", "v109", "v110",
        "v111", "v112", "v113", "v114", "v115", "v116", "v117", "v118", "v119", "v120", "v121", "v122", "v123", "v124",
        "v125", "v126", "v127", "v128", "v129", "v130", "v131", "v132", "v133", "v134", "v135", "v136", "v137", "v138",
        "v139", "v140", "v141", "v142", "v143", "v144", "v145", "v146", "v147", "v148", "v149", "v150", "v151", "v152",
        "v153", "v154", "v155", "v156", "v157", "v158", "v159", "v176", "v177", "v178", "v179", "v180", "v181", "v182",
        "v183", "v184", "v185", "v186", "v187", "v188", "v189", "v190", "v191", "v192", "v193", "v194", "v195", "v196",
        "v197", "v198", "v199", "v200", "v201", "v202", "v203", "v204", "v205", "v206", "v207", "v208", "v209", "v210",
        "v211", "v212", "v213", "v214", "v215", "v216", "v217", "v218", "v219", "v220", "v221", "v222", "v223", "v248",
        "v249", "v250", "v251", "v252", "v253", "s40", "s41", "s42", "s43", "s44", "s45", "s46", "s47", "s48", "s49",
        "s50", "s51"
    );
    }
    unsigned vq = v3[2];
    PIN();
    LANE_BITS(__builtin_amdgcn_mbcnt_hi(~0u, __builtin_amdgcn_mbcnt_lo(~0u, 0u)));
    u32x8 v, s2;
    u32x16 s;
    v[0] = lane * 16;
    u64 kr = (u64)(6 - rn) | ((u64)rot << 32);
    v[1] = lr * 4096 + ((lc ^ swz_tr(lr)) << 4);
    v[2] = wave * 4096 + g * 1024 + q4 * 128 + (((2 * (wave & 3) + (p4 >> 1)) ^ swz_tr(8 * g + q4)) << 4) + (p4 & 1) * 8;
    v[3] = wave * 4096 + (wave & 3) * 1024 + ((lane * 16) ^ (g << 6));
    v[4] = (wave & 3) * 1024 + (((p4 * 16 + (lane >> 2)) * 16) ^ (p4 << 6));
    v[5] = (lane >> 2) * 896 + p4 * 8;
    v[6] = c * 2;
    const unsigned mh = (unsigned)par, mg = (unsigned)(par >> 32);
    v[7] = ST_H4 + xp * 16384 + ((side * 8192) ^ mg) + ((ib & 1) * 28 + ((7 * wave) >> 1) + p4) * 128;
    put(s, 0, (u64)(w2 + st * 128 + (wave * 224 + rn * 32) * 4096));
    put(s, 2, (u64)(ws + WS_G + (unit(blk0, wave, 0) ^ mg)));
    put(s, 4, (u64)(out + (64 * st + 16 * (wave & 3)) * 896 + dg * 128));
    unsigned od = (16 - dg) * 32;
    put(s, 6, (u64)(b2 + (64 * st + 16 * (wave & 3)) * 2));
    put(s, 8, (u64)(ws + WS_ST));
    s[12] = bits;
    s[13] = wave * G_WV;
    put(s2, 0, (u64)(ws + WS_HN + (unit(blk0, st >> 2, 0) ^ mh) + (2 * (st & 3) + ((wave >> 1) & 1)) * 1024));
    s2[2] = ST_FL + (64 * st + 16 * (wave & 3)) * 16;
    s2[3] = UNI(wave & 1 ? ~0u : 0u);
    put(s2, 4, (u64)(ws + WS_EP));
    asm volatile(
      "s_waitcnt lgkmcnt(0)\n\ts_nop 4\n\t"
      ".p2align 3\n\t"
      "s_lshr_b32 s31, s17, 2\n\t"
      "v_mov_b32_e32 v112, 163600\n\t"
      "ds_read_b128 v[112:115], v112\n\t"
      "v_xor_b32_e32 v248, 64, v241\n\t"
      "v_add_u32_e32 v249, 63488, v241\n\t"
      "v_add_u32_e32 v250, 95232, v248\n\t"
      "v_add_u32_e32 v248, 31744, v248\n\t"
      "v_xor_b32_e64 v251, 32, v242\n\t"
      "v_xor_b32_e32 v252, 64, v242\n\t"
      "v_xor_b32_e32 v253, 96, v242\n\t"
      "s_bitcmp1_b32 s16, 4\n\t"
      "s_cbranch_scc0 GW2_%=\n\t"
      "s_mov_b32 s48, 0xf000f\n\t"
      "s_mov_b32 s49, 0xf000f\n\t"
      "s_mov_b64 exec, s[48:49]\n\t"
      "global_store_dwordx4 v140, v[136:139], s[12:13] sc1\n\t"
      "s_mov_b64 exec, -1\n\t"
      "GW2_%=:\n\t"
      "s_add_u32 m0, s31, 66048\n\t"
      "s_nop 0\n\t"
      "global_load_lds_dwordx4 v241, s[4:5]\n\t"
      "global_load_lds_dwordx4 v248, s[4:5] offset:1024\n\t"
      "global_load_lds_dwordx4 v249, s[4:5] offset:2048\n\t"
      "global_load_lds_dwordx4 v250, s[4:5] offset:3072\n\t"
      "s_sub_u32 s28, s28, 1\n\t"
      "s_cselect_b32 s48, -6, 1\n\t"
      "s_cselect_b32 s49, -1, 0\n\t"
      "s_mul_i32 s48, s48, 131072\n\t"
      "s_add_u32 s4, s4, s48\n\t"
      "s_addc_u32 s5, s5, s49\n\t"
      "global_load_ushort v236, v246, s[10:11]\n\t"
      "s_waitcnt lgkmcnt(0)\n\t"
      "v_readfirstlane_b32 s14, v112\n\t"
      "v_readfirstlane_b32 s15, v113\n\t"
      "v_readfirstlane_b32 s26, v114\n\t"
      "v_readfirstlane_b32 s27, v115\n\t"
      "s_add_u32 s52, s6, 0\n\t"
      "s_addc_u32 s53, s7, 0\n\t"
      "s_add_u32 s54, s6, 131072\n\t"
      "s_addc_u32 s55, s7, 0\n\t"
      "s_add_u32 s56, s6, 262144\n\t"
      "s_addc_u32 s57, s7, 0\n\t"
      "s_add_u32 s58, s6, 393216\n\t"
      "s_addc_u32 s59, s7, 0\n\t"
      "s_add_u32 s60, s6, 524288\n\t"
      "s_addc_u32 s61, s7, 0\n\t"
      "s_add_u32 s62, s6, 655360\n\t"
      "s_addc_u32 s63, s7, 0\n\t"
      "s_add_u32 s64, s6, 786432\n\t"
      "s_addc_u32 s65, s7, 0\n\t"
      "s_add_u32 s48, s29, 0\n\t"
      "s_cmp_ge_u32 s48, 7\n\t"
      "s_cselect_b32 s49, 7, 0\n\t"
      "s_sub_u32 s48, s48, s49\n\t"
      "v_lshl_add_u32 v224, s48, 10, v240\n\t"
      "s_add_u32 s48, s29, 1\n\t"
      "s_cmp_ge_u32 s48, 7\n\t"
      "s_cselect_b32 s49, 7, 0\n\t"
      "s_sub_u32 s48, s48, s49\n\t"
      "v_lshl_add_u32 v225, s48, 10, v240\n\t"
      "s_add_u32 s48, s29, 2\n\t"
      "s_cmp_ge_u32 s48, 7\n\t"
      "s_cselect_b32 s49, 7, 0\n\t"
      "s_sub_u32 s48, s48, s49\n\t"
      "v_lshl_add_u32 v226, s48, 10, v240\n\t"
      "s_add_u32 s48, s29, 3\n\t"
      "s_cmp_ge_u32 s48, 7\n\t"
      "s_cselect_b32 s49, 7, 0\n\t"
      "s_sub_u32 s48, s48, s49\n\t"
      "v_lshl_add_u32 v227, s48, 10, v240\n\t"
      "s_add_u32 s48, s29, 4\n\t"
      "s_cmp_ge_u32 s48, 7\n\t"
      "s_cselect_b32 s49, 7, 0\n\t"
      "s_sub_u32 s48, s48, s49\n\t"
      "v_lshl_add_u32 v228, s48, 10, v240\n\t"
      "s_add_u32 s48, s29, 5\n\t"
      "s_cmp_ge_u32 s48, 7\n\t"
      "s_cselect_b32 s49, 7, 0\n\t"
      "s_sub_u32 s48, s48, s49\n\t"
      "v_lshl_add_u32 v229, s48, 10, v240\n\t"
      "s_add_u32 s48, s29, 6\n\t"
      "s_cmp_ge_u32 s48, 7\n\t"
      "s_cselect_b32 s49, 7, 0\n\t"
      "s_sub_u32 s48, s48, s49\n\t"
      "v_lshl_add_u32 v230, s48, 10, v240\n\t"
      "v_xor_b32_e32 v220, 1024, v243\n\t"
      "v_xor_b32_e32 v221, 2048, v243\n\t"
      "v_xor_b32_e32 v222, 3072, v243\n\t"
      "s_add_u32 s66, s8, s30\n\t"
      "s_addc_u32 s67, s9, 0\n\t"
      "v_lshrrev_b32_e32 v112, 4, v240\n\t"
      "v_and_b32_e32 v113, 15, v112\n\t"
      "v_lshrrev_b32_e32 v112, 4, v112\n\t"
      "s_lshr_b32 s48, s17, 10\n\t"
      "s_and_b32 s48, s48, 16\n\t"
      "v_lshlrev_b32_e32 v112, 3, v112\n\t"
      "v_sub_u32_e32 v112, v113, v112\n\t"
      "v_add_u32_e32 v112, s48, v112\n\t"
      "v_mov_b32_e32 v114, 0xffff\n\t"
      "v_mov_b32_e32 v115, 0xffff0000\n\t"
      "v_cmp_eq_u32_e32 vcc, 0, v112\n\t"
      "s_nop 1\n\t"
      "v_cndmask_b32_e32 v216, 0, v114, vcc\n\t"
      "v_cmp_eq_u32_e32 vcc, 1, v112\n\t"
      "s_nop 1\n\t"
      "v_cndmask_b32_e32 v216, v216, v115, vcc\n\t"
      "v_cmp_eq_u32_e32 vcc, 2, v112\n\t"
      "s_nop 1\n\t"
      "v_cndmask_b32_e32 v217, 0, v114, vcc\n\t"
      "v_cmp_eq_u32_e32 vcc, 3, v112\n\t"
      "s_nop 1\n\t"
      "v_cndmask_b32_e32 v217, v217, v115, vcc\n\t"
      "v_cmp_eq_u32_e32 vcc, 4, v112\n\t"
      "s_nop 1\n\t"
      "v_cndmask_b32_e32 v218, 0, v114, vcc\n\t"
      "v_cmp_eq_u32_e32 vcc, 5, v112\n\t"
      "s_nop 1\n\t"
      "v_cndmask_b32_e32 v218, v218, v115, vcc\n\t"
      "v_cmp_eq_u32_e32 vcc, 6, v112\n\t"
      "s_nop 1\n\t"
      "v_cndmask_b32_e32 v219, 0, v114, vcc\n\t"
      "v_cmp_eq_u32_e32 vcc, 7, v112\n\t"
      "s_nop 1\n\t"
      "v_cndmask_b32_e32 v219, v219, v115, vcc\n\t"
      "v_and_b32_e32 v116, 0xf0, v240\n\t"
      "v_add_u32_e32 v116, s22, v116\n\t"
      "v_mov_b64_e32 v[184:185], 0\n\t"
      "v_mov_b64_e32 v[186:187], 0\n\t"
      "v_mov_b64_e32 v[188:189], 0\n\t"
      "v_mov_b64_e32 v[190:191], 0\n\t"
      "v_mov_b64_e32 v[192:193], 0\n\t"
      "v_mov_b64_e32 v[194:195], 0\n\t"
      "v_mov_b64_e32 v[196:197], 0\n\t"
      "v_mov_b64_e32 v[198:199], 0\n\t"
      "v_mov_b64_e32 v[200:201], 0\n\t"
      "v_mov_b64_e32 v[202:203], 0\n\t"
      "v_mov_b64_e32 v[204:205], 0\n\t"
      "v_mov_b64_e32 v[206:207], 0\n\t"
      "v_mov_b64_e32 v[208:209], 0\n\t"
      "v_mov_b64_e32 v[210:211], 0\n\t"
      "s_waitcnt vmcnt(5)\n\t"
      "v_add_u32_e32 v242, 66048, v242\n\t"
      "v_add_u32_e32 v251, 66048, v251\n\t"
      "v_add_u32_e32 v252, 66048, v252\n\t"
      "v_add_u32_e32 v253, 66048, v253\n\t"
      "ds_read_b64_tr_b16 v[0:1], v242 offset:64512\n\t"
      "ds_read_b64_tr_b16 v[2:3], v242 offset:65024\n\t"
      "ds_read_b64_tr_b16 v[4:5], v251 offset:64512\n\t"
      "ds_read_b64_tr_b16 v[6:7], v251 offset:65024\n\t"
      "ds_read_b64_tr_b16 v[8:9], v252 offset:64512\n\t"
      "ds_read_b64_tr_b16 v[10:11], v252 offset:65024\n\t"
      "ds_read_b64_tr_b16 v[12:13], v253 offset:64512\n\t"
      "ds_read_b64_tr_b16 v[14:15], v253 offset:65024\n\t"
      "s_waitcnt vmcnt(1)\n\t"
      "s_add_u32 m0, s31, 0\n\t"
      "s_nop 0\n\t"
      "global_load_lds_dwordx4 v241, s[4:5]\n\t"
      "global_load_lds_dwordx4 v248, s[4:5] offset:1024\n\t"
      "global_load_lds_dwordx4 v249, s[4:5] offset:2048\n\t"
      "global_load_lds_dwordx4 v250, s[4:5] offset:3072\n\t"
      "s_sub_u32 s28, s28, 1\n\t"
      "s_cselect_b32 s48, -6, 1\n\t"
      "s_cselect_b32 s49, -1, 0\n\t"
      "s_mul_i32 s48, s48, 131072\n\t"
      "s_add_u32 s4, s4, s48\n\t"
      "s_addc_u32 s5, s5, s49\n\t"
      "ds_read_b64_tr_b16 v[16:17], v242\n\t"
      "ds_read_b64_tr_b16 v[18:19], v242 offset:512\n\t"
      "ds_read_b64_tr_b16 v[20:21], v251\n\t"
      "ds_read_b64_tr_b16 v[22:23], v251 offset:512\n\t"
      "ds_read_b64_tr_b16 v[24:25], v252\n\t"
      "ds_read_b64_tr_b16 v[26:27], v252 offset:512\n\t"
      "ds_read_b64_tr_b16 v[28:29], v253\n\t"
      "ds_read_b64_tr_b16 v[30:31], v253 offset:512\n\t"
      "s_mov_b64 exec, 0\n\t"
      "s_cmpk_eq_u32 s31, 0x0\n\t"
      "s_cbranch_scc1 .LhopC0\n\t"
      "s_cmpk_eq_u32 s31, 0x1000\n\t"
      "s_cbranch_scc1 .LhopC1\n\t"
      "s_cmpk_eq_u32 s31, 0x2000\n\t"
      "s_cbranch_scc1 .LhopC2\n\t"
      "s_cmpk_eq_u32 s31, 0x3000\n\t"
      "s_cbranch_scc1 .LhopC3\n\t"
      "s_cmpk_eq_u32 s31, 0x4000\n\t"
      "s_cbranch_scc1 .LhopC4\n\t"
      "s_cmpk_eq_u32 s31, 0x5000\n\t"
      "s_cbranch_scc1 .LbackC\n\t"
      "s_cmpk_eq_u32 s31, 0x6000\n\t"
      "s_cbranch_scc1 .LbackC\n\t"
      "s_cmpk_eq_u32 s31, 0x7000\n\t"
      "s_cbranch_scc1 .LbackC\n\t"
      ".LbackC:\n\t"
      "s_mov_b64 exec, -1\n\t"
      "s_waitcnt vmcnt(0)\n\t"
      "s_add_u32 m0, s31, 32768\n\t"
      "s_nop 0\n\t"
      "global_load_lds_dwordx4 v241, s[4:5]\n\t"
      "global_load_lds_dwordx4 v248, s[4:5] offset:1024\n\t"
      "global_load_lds_dwordx4 v249, s[4:5] offset:2048\n\t"
      "global_load_lds_dwordx4 v250, s[4:5] offset:3072\n\t"
      "s_sub_u32 s28, s28, 1\n\t"
      "s_cselect_b32 s48, -6, 1\n\t"
      "s_cselect_b32 s49, -1, 0\n\t"
      "s_mul_i32 s48, s48, 131072\n\t"
      "s_add_u32 s4, s4, s48\n\t"
      "s_addc_u32 s5, s5, s49\n\t"
      "s_nop 0\n\t"
      "v_add_u32_e32 v242, 4294901248, v242\n\t"
      "v_add_u32_e32 v251, 4294901248, v251\n\t"
      "v_add_u32_e32 v252, 4294901248, v252\n\t"
      "v_add_u32_e32 v253, 4294901248, v253\n\t"
      "ds_read_b64_tr_b16 v[32:33], v242\n\t"
      "ds_read_b64_tr_b16 v[34:35], v242 offset:512\n\t"
      "ds_read_b64_tr_b16 v[36:37], v251\n\t"
      "ds_read_b64_tr_b16 v[38:39], v251 offset:512\n\t"
      "ds_read_b64_tr_b16 v[40:41], v252\n\t"
      "ds_read_b64_tr_b16 v[42:43], v252 offset:512\n\t"
      "ds_read_b64_tr_b16 v[44:45], v253\n\t"
      "ds_read_b64_tr_b16 v[46:47], v253 offset:512\n\t"
      "s_waitcnt vmcnt(0)\n\t"
      "s_waitcnt lgkmcnt(14)\n\t"
      "s_add_u32 m0, s31, 130560\n\t"
      "s_nop 0\n\t"
      "global_load_lds_dwordx4 v241, s[4:5]\n\t"
      "global_load_lds_dwordx4 v248, s[4:5] offset:1024\n\t"
      "global_load_lds_dwordx4 v249, s[4:5] offset:2048\n\t"
      "global_load_lds_dwordx4 v250, s[4:5] offset:3072\n\t"
      "s_sub_u32 s28, s28, 1\n\t"
      "s_cselect_b32 s48, -6, 1\n\t"
      "s_cselect_b32 s49, -1, 0\n\t"
      "s_mul_i32 s48, s48, 131072\n\t"
      "s_add_u32 s4, s4, s48\n\t"
      "s_addc_u32 s5, s5, s49\n\t"
      "ds_read_b64_tr_b16 v[48:49], v242 offset:32768\n\t"
      "ds_read_b64_tr_b16 v[50:51], v242 offset:33280\n\t"
      "ds_read_b64_tr_b16 v[52:53], v251 offset:32768\n\t"
      "ds_read_b64_tr_b16 v[54:55], v251 offset:33280\n\t"
      "ds_read_b64_tr_b16 v[56:57], v252 offset:32768\n\t"
      "ds_read_b64_tr_b16 v[58:59], v252 offset:33280\n\t"
      "ds_read_b64_tr_b16 v[60:61], v253 offset:32768\n\t"
      "ds_read_b64_tr_b16 v[62:63], v253 offset:33280\n\t"
      "s_bitcmp1_b32 s16, 1\n\t"
      "s_cbranch_scc0 HWQ_%=\n\t"
      "global_load_dwordx4 v[112:115], v116, s[12:13] sc1\n\t"
      "HWQ_%=:\n\t"
      "s_waitcnt vmcnt(0)\n\t"
      "s_waitcnt lgkmcnt(14)\n\t"
      "s_add_u32 m0, s31, 66048\n\t"
      "s_nop 0\n\t"
      "global_load_lds_dwordx4 v241, s[4:5]\n\t"
      "global_load_lds_dwordx4 v248, s[4:5] offset:1024\n\t"
      "global_load_lds_dwordx4 v249, s[4:5] offset:2048\n\t"
      "global_load_lds_dwordx4 v250, s[4:5] offset:3072\n\t"
      "s_sub_u32 s28, s28, 1\n\t"
      "s_cselect_b32 s48, -6, 1\n\t"
      "s_cselect_b32 s49, -1, 0\n\t"
      "s_mul_i32 s48, s48, 131072\n\t"
      "s_add_u32 s4, s4, s48\n\t"
      "s_addc_u32 s5, s5, s49\n\t"
      "v_add_u32_e32 v242, 66048, v242\n\t"
      "v_add_u32_e32 v251, 66048, v251\n\t"
      "v_add_u32_e32 v252, 66048, v252\n\t"
      "v_add_u32_e32 v253, 66048, v253\n\t"
      "ds_read_b64_tr_b16 v[64:65], v242 offset:64512\n\t"
      "ds_read_b64_tr_b16 v[66:67], v242 offset:65024\n\t"
      "ds_read_b64_tr_b16 v[68:69], v251 offset:64512\n\t"
      "ds_read_b64_tr_b16 v[70:71], v251 offset:65024\n\t"
      "ds_read_b64_tr_b16 v[72:73], v252 offset:64512\n\t"
      "ds_read_b64_tr_b16 v[74:75], v252 offset:65024\n\t"
      "ds_read_b64_tr_b16 v[76:77], v253 offset:64512\n\t"
      "ds_read_b64_tr_b16 v[78:79], v253 offset:65024\n\t"
      "s_waitcnt vmcnt(0)\n\t"
      "s_waitcnt lgkmcnt(14)\n\t"
      "s_add_u32 m0, s31, 0\n\t"
      "s_nop 0\n\t"
      "global_load_lds_dwordx4 v241, s[4:5]\n\t"
      "global_load_lds_dwordx4 v248, s[4:5] offset:1024\n\t"
      "global_load_lds_dwordx4 v249, s[4:5] offset:2048\n\t"
      "global_load_lds_dwordx4 v250, s[4:5] offset:3072\n\t"
      "ds_read_b64_tr_b16 v[80:81], v242\n\t"
      "ds_read_b64_tr_b16 v[82:83], v242 offset:512\n\t"
      "ds_read_b64_tr_b16 v[84:85], v251\n\t"
      "ds_read_b64_tr_b16 v[86:87], v251 offset:512\n\t"
      "ds_read_b64_tr_b16 v[88:89], v252\n\t"
      "ds_read_b64_tr_b16 v[90:91], v252 offset:512\n\t"
      "ds_read_b64_tr_b16 v[92:93], v253\n\t"
      "ds_read_b64_tr_b16 v[94:95], v253 offset:512\n\t"
      "s_bitcmp1_b32 s16, 1\n\t"
      "s_cbranch_scc0 HWH_%=\n\t"
      "v_xor_b32_e32 v114, v114, v112\n\t"
      "v_xor_b32_e32 v115, v115, v113\n\t"
      "v_cmp_ne_u64_e32 vcc, s[14:15], v[114:115]\n\t"
      "s_cbranch_vccnz HWR_%=\n\t"
      "HWB_%=:\n\t"
      "s_mov_b32 exec_hi, s23\n\t"
      "s_not_b32 exec_lo, s23\n\t"
      "s_add_u32 s68, s20, 0\n\t"
      "s_addc_u32 s69, s21, 0\n\t"
      "global_load_dwordx4 v[184:187], v240, s[68:69] sc1\n\t"
      "s_add_u32 s68, s20, 131072\n\t"
      "s_addc_u32 s69, s21, 0\n\t"
      "global_load_dwordx4 v[188:191], v240, s[68:69] sc1\n\t"
      "s_add_u32 s68, s20, 262144\n\t"
      "s_addc_u32 s69, s21, 0\n\t"
      "global_load_dwordx4 v[192:195], v240, s[68:69] sc1\n\t"
      "s_add_u32 s68, s20, 393216\n\t"
      "s_addc_u32 s69, s21, 0\n\t"
      "global_load_dwordx4 v[196:199], v240, s[68:69] sc1\n\t"
      "s_add_u32 s68, s20, 524288\n\t"
      "s_addc_u32 s69, s21, 0\n\t"
      "global_load_dwordx4 v[200:203], v240, s[68:69] sc1\n\t"
      "s_add_u32 s68, s20, 655360\n\t"
      "s_addc_u32 s69, s21, 0\n\t"
      "global_load_dwordx4 v[204:207], v240, s[68:69] sc1\n\t"
      "s_add_u32 s68, s20, 786432\n\t"
      "s_addc_u32 s69, s21, 0\n\t"
      "global_load_dwordx4 v[208:211], v240, s[68:69] sc1\n\t"
      "s_mov_b64 exec, -1\n\t"
      "v_lshlrev_b32_e32 v236, 16, v236\n\t"
      "v_add_f32_e32 v236, v236, v112\n\t"
      "v_cvt_pk_bf16_f32 v114, v113, v113\n\t"
      "v_lshlrev_b32_e32 v115, 16, v114\n\t"
      "v_sub_f32_e32 v115, v113, v115\n\t"
      "v_cvt_pk_bf16_f32 v115, v115, v115\n\t"
      "v_mov_b32_e32 v237, v236\n\t"
      "v_mov_b32_e32 v238, v236\n\t"
      "v_mov_b32_e32 v239, v236\n\t"
      "v_and_b32_e32 v120, v216, v115\n\t"
      "v_and_b32_e32 v216, v216, v114\n\t"
      "v_and_b32_e32 v121, v217, v115\n\t"
      "v_and_b32_e32 v217, v217, v114\n\t"
      "v_and_b32_e32 v122, v218, v115\n\t"
      "v_and_b32_e32 v218, v218, v114\n\t"
      "v_and_b32_e32 v123, v219, v115\n\t"
      "v_and_b32_e32 v219, v219, v114\n\t"
      "HWH_%=:\n\t"
      "s_waitcnt vmcnt(0)\n\t"
      "v_add_u32_e32 v242, 4294901248, v242\n\t"
      "v_add_u32_e32 v251, 4294901248, v251\n\t"
      "v_add_u32_e32 v252, 4294901248, v252\n\t"
      "v_add_u32_e32 v253, 4294901248, v253\n\t"
      "ds_read_b64_tr_b16 v[96:97], v242\n\t"
      "ds_read_b64_tr_b16 v[98:99], v242 offset:512\n\t"
      "ds_read_b64_tr_b16 v[100:101], v251\n\t"
      "ds_read_b64_tr_b16 v[102:103], v251 offset:512\n\t"
      "ds_read_b64_tr_b16 v[104:105], v252\n\t"
      "ds_read_b64_tr_b16 v[106:107], v252 offset:512\n\t"
      "ds_read_b64_tr_b16 v[108:109], v253\n\t"
      "ds_read_b64_tr_b16 v[110:111], v253 offset:512\n\t"
      "s_mov_b64 exec, 15\n\t"
      "v_mov_b64_e32 v[232:233], 0\n\t"
      "global_atomic_or_x2 v[232:233], v247, v[232:233], s[12:13] sc0 sc1\n\t"
      "s_mov_b64 exec, -1\n\t"
      "s_waitcnt lgkmcnt(0)\n\t"
      "s_barrier\n\t"
      "s_mov_b64 exec, 15\n\t"
      "s_waitcnt vmcnt(0)\n\t"
      "v_subrev_u32_e32 v232, s14, v232\n\t"
      "v_cmp_ne_u32_e64 s[48:49], s15, v233\n\t"
      "v_subrev_u32_e32 v232, 0, v232\n\t"
      "v_cmp_lt_u32_e32 vcc, 7, v232\n\t"
      "s_mov_b64 exec, -1\n\t"
      "s_or_b64 vcc, vcc, s[48:49]\n\t"
      "s_cbranch_vccz PRY_%=\n\t"
      "s_mov_b32 s51, 0\n\t"
      "s_branch PRE_%=\n\t"
      "PRL_%=:\n\t"
      "s_sleep 4\n\t"
      "s_add_u32 s51, s51, 1\n\t"
      "s_cmp_lt_u32 s51, 262144\n\t"
      "s_cbranch_scc0 PRX_%=\n\t"
      "PRE_%=:\n\t"
      "s_mov_b64 exec, 15\n\t"
      "v_mov_b64_e32 v[232:233], 0\n\t"
      "global_atomic_or_x2 v[232:233], v247, v[232:233], s[12:13] sc0 sc1\n\t"
      "s_waitcnt vmcnt(0)\n\t"
      "v_subrev_u32_e32 v232, s14, v232\n\t"
      "v_cmp_ne_u32_e64 s[48:49], s15, v233\n\t"
      "v_subrev_u32_e32 v232, 0, v232\n\t"
      "v_cmp_lt_u32_e32 vcc, 7, v232\n\t"
      "s_mov_b64 exec, -1\n\t"
      "s_or_b64 vcc, vcc, s[48:49]\n\t"
      "s_cbranch_vccnz PRL_%=\n\t"
      "PRX_%=:\n\t"
      "PRY_%=:\n\t"
      "global_load_dwordx4 v[128:131], v224, s[52:53] sc1\n\t"
      "global_load_dwordx4 v[132:135], v225, s[52:53] sc1\n\t"
      "global_load_dwordx4 v[136:139], v226, s[52:53] sc1\n\t"
      "global_load_dwordx4 v[140:143], v227, s[52:53] sc1\n\t"
      "global_load_dwordx4 v[144:147], v228, s[52:53] sc1\n\t"
      "global_load_dwordx4 v[148:151], v229, s[52:53] sc1\n\t"
      "global_load_dwordx4 v[152:155], v230, s[52:53] sc1\n\t"
      "s_bitcmp1_b32 s16, 1\n\t"
      "s_cbranch_scc0 HWE_%=\n\t"
      "v_max3_u32 v234, v184, v185, v186\n\t"
      "v_max3_u32 v234, v234, v187, v188\n\t"
      "v_max3_u32 v234, v234, v189, v190\n\t"
      "v_max3_u32 v234, v234, v191, v192\n\t"
      "v_max3_u32 v234, v234, v193, v194\n\t"
      "v_max3_u32 v234, v234, v195, v196\n\t"
      "v_max3_u32 v234, v234, v197, v198\n\t"
      "v_max3_u32 v234, v234, v199, v200\n\t"
      "v_max3_u32 v234, v234, v201, v202\n\t"
      "v_max3_u32 v234, v234, v203, v204\n\t"
      "v_max3_u32 v234, v234, v205, v206\n\t"
      "v_max3_u32 v234, v234, v207, v208\n\t"
      "v_max3_u32 v234, v234, v209, v210\n\t"
      "v_max_u32_e32 v234, v234, v211\n\t"
      "v_cmp_eq_u32_e32 vcc, -1, v234\n\t"
      "s_cbranch_vccnz HXR_%=\n\t"
      "HXB_%=:\n\t"
      "v_mfma_f32_16x16x32_bf16 v[156:159], v[184:187], v[120:123], v[236:239]\n\t"
      "v_mfma_f32_16x16x32_bf16 v[160:163], v[188:191], v[120:123], v[236:239]\n\t"
      "v_mfma_f32_16x16x32_bf16 v[164:167], v[192:195], v[120:123], v[236:239]\n\t"
      "v_mfma_f32_16x16x32_bf16 v[168:171], v[196:199], v[120:123], v[236:239]\n\t"
      "v_mfma_f32_16x16x32_bf16 v[172:175], v[200:203], v[120:123], v[236:239]\n\t"
      "v_mfma_f32_16x16x32_bf16 v[176:179], v[204:207], v[120:123], v[236:239]\n\t"
      "v_mfma_f32_16x16x32_bf16 v[180:183], v[208:211], v[120:123], v[236:239]\n\t"
      "v_mfma_f32_16x16x32_bf16 v[156:159], v[184:187], v[216:219], v[156:159]\n\t"
      "v_mfma_f32_16x16x32_bf16 v[160:163], v[188:191], v[216:219], v[160:163]\n\t"
      "v_mfma_f32_16x16x32_bf16 v[164:167], v[192:195], v[216:219], v[164:167]\n\t"
      "v_mfma_f32_16x16x32_bf16 v[168:171], v[196:199], v[216:219], v[168:171]\n\t"
      "v_mfma_f32_16x16x32_bf16 v[172:175], v[200:203], v[216:219], v[172:175]\n\t"
      "v_mfma_f32_16x16x32_bf16 v[176:179], v[204:207], v[216:219], v[176:179]\n\t"
      "v_mfma_f32_16x16x32_bf16 v[180:183], v[208:211], v[216:219], v[180:183]\n\t"
      "HWE_%=:\n\t"
      "s_nop 7\n\t"
      "s_cmp_ge_u32 s17, 65536\n\t"
      "s_cbranch_scc1 GQ_%=\n\t"
      ".p2align 6\n\t"
      "s_setprio 1\n\t"
      "s_waitcnt vmcnt(6)\n\t"
      "v_mfma_f32_16x16x32_bf16 v[184:187], v[128:131], v[0:3], v[156:159]\n\t"
      "v_mfma_f32_16x16x32_bf16 v[188:191], v[128:131], v[4:7], 0\n\t"
      "v_mfma_f32_16x16x32_bf16 v[192:195], v[128:131], v[8:11], 0\n\t"
      "v_mfma_f32_16x16x32_bf16 v[196:199], v[128:131], v[12:15], 0\n\t"
      "global_load_dwordx4 v[128:131], v224, s[54:55] sc1\n\t"
      "s_waitcnt vmcnt(6)\n\t"
      "v_mfma_f32_16x16x32_bf16 v[184:187], v[132:135], v[16:19], v[184:187]\n\t"
      "s_waitcnt vmcnt(5)\n\t"
      "v_mfma_f32_16x16x32_bf16 v[184:187], v[136:139], v[32:35], v[184:187]\n\t"
      "v_mfma_f32_16x16x32_bf16 v[188:191], v[132:135], v[20:23], v[188:191]\n\t"
      "v_mfma_f32_16x16x32_bf16 v[188:191], v[136:139], v[36:39], v[188:191]\n\t"
      "v_mfma_f32_16x16x32_bf16 v[192:195], v[132:135], v[24:27], v[192:195]\n\t"
      "v_mfma_f32_16x16x32_bf16 v[192:195], v[136:139], v[40:43], v[192:195]\n\t"
      "v_mfma_f32_16x16x32_bf16 v[196:199], v[132:135], v[28:31], v[196:199]\n\t"
      "global_load_dwordx4 v[132:135], v225, s[54:55] sc1\n\t"
      "v_mfma_f32_16x16x32_bf16 v[196:199], v[136:139], v[44:47], v[196:199]\n\t"
      "global_load_dwordx4 v[136:139], v226, s[54:55] sc1\n\t"
      "s_waitcnt vmcnt(6)\n\t"
      "v_mfma_f32_16x16x32_bf16 v[184:187], v[140:143], v[48:51], v[184:187]\n\t"
      "s_waitcnt vmcnt(5)\n\t"
      "v_mfma_f32_16x16x32_bf16 v[184:187], v[144:147], v[64:67], v[184:187]\n\t"
      "v_mfma_f32_16x16x32_bf16 v[188:191], v[140:143], v[52:55], v[188:191]\n\t"
      "v_mfma_f32_16x16x32_bf16 v[188:191], v[144:147], v[68:71], v[188:191]\n\t"
      "v_mfma_f32_16x16x32_bf16 v[192:195], v[140:143], v[56:59], v[192:195]\n\t"
      "v_mfma_f32_16x16x32_bf16 v[192:195], v[144:147], v[72:75], v[192:195]\n\t"
      "v_mfma_f32_16x16x32_bf16 v[196:199], v[140:143], v[60:63], v[196:199]\n\t"
      "global_load_dwordx4 v[140:143], v227, s[54:55] sc1\n\t"
      "v_mfma_f32_16x16x32_bf16 v[196:199], v[144:147], v[76:79], v[196:199]\n\t"
      "global_load_dwordx4 v[144:147], v228, s[54:55] sc1\n\t"
      "s_waitcnt vmcnt(6)\n\t"
      "v_mfma_f32_16x16x32_bf16 v[184:187], v[148:151], v[80:83], v[184:187]\n\t"
      "s_waitcnt vmcnt(5)\n\t"
      "v_mfma_f32_16x16x32_bf16 v[184:187], v[152:155], v[96:99], v[184:187]\n\t"
      "v_mfma_f32_16x16x32_bf16 v[188:191], v[148:151], v[84:87], v[188:191]\n\t"
      "v_mfma_f32_16x16x32_bf16 v[188:191], v[152:155], v[100:103], v[188:191]\n\t"
      "s_nop 5\n\t"
      "v_cmp_u_f32_e32 vcc, v184, v185\n\t"
      "v_cmp_u_f32_e64 s[48:49], v186, v187\n\t"
      "v_mfma_f32_16x16x32_bf16 v[192:195], v[148:151], v[88:91], v[192:195]\n\t"
      "v_mfma_f32_16x16x32_bf16 v[192:195], v[152:155], v[104:107], v[192:195]\n\t"
      "s_or_b64 vcc, vcc, s[48:49]\n\t"
      "s_cbranch_scc1 NP0R_%=\n\t"
      "NP0B_%=:\n\t"
      "ds_write_b128 v243, v[184:187]\n\t"
      "v_mfma_f32_16x16x32_bf16 v[196:199], v[148:151], v[92:95], v[196:199]\n\t"
      "global_load_dwordx4 v[148:151], v229, s[54:55] sc1\n\t"
      "v_mfma_f32_16x16x32_bf16 v[196:199], v[152:155], v[108:111], v[196:199]\n\t"
      "global_load_dwordx4 v[152:155], v230, s[54:55] sc1\n\t"
      "ds_write_b128 v220, v[188:191]\n\t"
      "ds_write_b128 v221, v[192:195]\n\t"
      "s_nop 4\n\t"
      "ds_write_b128 v222, v[196:199]\n\t"
      "s_waitcnt lgkmcnt(0)\n\t"
      "s_barrier\n\t"
      "ds_read_b128 v[112:115], v244\n\t"
      "ds_read_b128 v[116:119], v244 offset:4096\n\t"
      "ds_read_b128 v[120:123], v244 offset:8192\n\t"
      "ds_read_b128 v[124:127], v244 offset:12288\n\t"
      "ds_read_b128 v[184:187], v244 offset:16384\n\t"
      "ds_read_b128 v[188:191], v244 offset:20480\n\t"
      "ds_read_b128 v[192:195], v244 offset:24576\n\t"
      "ds_read_b128 v[196:199], v244 offset:28672\n\t"
      "s_waitcnt vmcnt(6)\n\t"
      "v_mfma_f32_16x16x32_bf16 v[200:203], v[128:131], v[0:3], v[160:163]\n\t"
      "v_mfma_f32_16x16x32_bf16 v[204:207], v[128:131], v[4:7], 0\n\t"
      "s_waitcnt lgkmcnt(6)\n\t"
      "v_add_f32_e32 v112, v112, v116\n\t"
      "v_add_f32_e32 v113, v113, v117\n\t"
      "v_mfma_f32_16x16x32_bf16 v[208:211], v[128:131], v[8:11], 0\n\t"
      "v_add_f32_e32 v114, v114, v118\n\t"
      "v_add_f32_e32 v115, v115, v119\n\t"
      "v_mfma_f32_16x16x32_bf16 v[212:215], v[128:131], v[12:15], 0\n\t"
      "global_load_dwordx4 v[128:131], v224, s[56:57] sc1\n\t"
      "s_waitcnt vmcnt(6)\n\t"
      "v_mfma_f32_16x16x32_bf16 v[200:203], v[132:135], v[16:19], v[200:203]\n\t"
      "s_waitcnt lgkmcnt(4)\n\t"
      "v_add_f32_e32 v120, v120, v124\n\t"
      "s_waitcnt vmcnt(5)\n\t"
      "v_mfma_f32_16x16x32_bf16 v[200:203], v[136:139], v[32:35], v[200:203]\n\t"
      "v_add_f32_e32 v121, v121, v125\n\t"
      "v_add_f32_e32 v122, v122, v126\n\t"
      "v_mfma_f32_16x16x32_bf16 v[204:207], v[132:135], v[20:23], v[204:207]\n\t"
      "v_add_f32_e32 v123, v123, v127\n\t"
      "s_waitcnt lgkmcnt(2)\n\t"
      "v_add_f32_e32 v184, v184, v188\n\t"
      "v_mfma_f32_16x16x32_bf16 v[204:207], v[136:139], v[36:39], v[204:207]\n\t"
      "v_add_f32_e32 v185, v185, v189\n\t"
      "v_add_f32_e32 v186, v186, v190\n\t"
      "v_mfma_f32_16x16x32_bf16 v[208:211], v[132:135], v[24:27], v[208:211]\n\t"
      "v_add_f32_e32 v187, v187, v191\n\t"
      "v_add_f32_e32 v112, v112, v120\n\t"
      "v_mfma_f32_16x16x32_bf16 v[208:211], v[136:139], v[40:43], v[208:211]\n\t"
      "v_add_f32_e32 v113, v113, v121\n\t"
      "v_add_f32_e32 v114, v114, v122\n\t"
      "v_mfma_f32_16x16x32_bf16 v[212:215], v[132:135], v[28:31], v[212:215]\n\t"
      "global_load_dwordx4 v[132:135], v225, s[56:57] sc1\n\t"
      "v_add_f32_e32 v115, v115, v123\n\t"
      "v_mfma_f32_16x16x32_bf16 v[212:215], v[136:139], v[44:47], v[212:215]\n\t"
      "global_load_dwordx4 v[136:139], v226, s[56:57] sc1\n\t"
      "s_waitcnt vmcnt(6)\n\t"
      "v_mfma_f32_16x16x32_bf16 v[200:203], v[140:143], v[48:51], v[200:203]\n\t"
      "s_waitcnt lgkmcnt(0)\n\t"
      "v_add_f32_e32 v192, v192, v196\n\t"
      "s_waitcnt vmcnt(5)\n\t"
      "v_mfma_f32_16x16x32_bf16 v[200:203], v[144:147], v[64:67], v[200:203]\n\t"
      "v_add_f32_e32 v193, v193, v197\n\t"
      "v_add_f32_e32 v194, v194, v198\n\t"
      "v_mfma_f32_16x16x32_bf16 v[204:207], v[140:143], v[52:55], v[204:207]\n\t"
      "v_add_f32_e32 v195, v195, v199\n\t"
      "v_add_f32_e32 v184, v184, v192\n\t"
      "v_mfma_f32_16x16x32_bf16 v[204:207], v[144:147], v[68:71], v[204:207]\n\t"
      "v_add_f32_e32 v185, v185, v193\n\t"
      "v_add_f32_e32 v186, v186, v194\n\t"
      "v_mfma_f32_16x16x32_bf16 v[208:211], v[140:143], v[56:59], v[208:211]\n\t"
      "v_add_f32_e32 v187, v187, v195\n\t"
      "v_add_f32_e32 v112, v112, v184\n\t"
      "v_mfma_f32_16x16x32_bf16 v[208:211], v[144:147], v[72:75], v[208:211]\n\t"
      "v_add_f32_e32 v113, v113, v185\n\t"
      "v_add_f32_e32 v114, v114, v186\n\t"
      "v_mfma_f32_16x16x32_bf16 v[212:215], v[140:143], v[60:63], v[212:215]\n\t"
      "global_load_dwordx4 v[140:143], v227, s[56:57] sc1\n\t"
      "v_add_f32_e32 v115, v115, v187\n\t"
      "v_mfma_f32_16x16x32_bf16 v[212:215], v[144:147], v[76:79], v[212:215]\n\t"
      "global_load_dwordx4 v[144:147], v228, s[56:57] sc1\n\t"
      "s_waitcnt vmcnt(6)\n\t"
      "v_mfma_f32_16x16x32_bf16 v[200:203], v[148:151], v[80:83], v[200:203]\n\t"
      "v_cvt_pk_bf16_f32 v116, v112, v113\n\t"
      "s_waitcnt vmcnt(5)\n\t"
      "v_mfma_f32_16x16x32_bf16 v[200:203], v[152:155], v[96:99], v[200:203]\n\t"
      "v_cvt_pk_bf16_f32 v117, v114, v115\n\t"
      "global_store_dwordx2 v245, v[116:117], s[8:9]\n\t"
      "v_mfma_f32_16x16x32_bf16 v[204:207], v[148:151], v[84:87], v[204:207]\n\t"
      "v_mfma_f32_16x16x32_bf16 v[204:207], v[152:155], v[100:103], v[204:207]\n\t"
      "s_nop 3\n\t"
      "v_cmp_u_f32_e32 vcc, v200, v201\n\t"
      "v_cmp_u_f32_e64 s[48:49], v202, v203\n\t"
      "v_mfma_f32_16x16x32_bf16 v[208:211], v[148:151], v[88:91], v[208:211]\n\t"
      "v_mfma_f32_16x16x32_bf16 v[208:211], v[152:155], v[104:107], v[208:211]\n\t"
      "s_or_b64 vcc, vcc, s[48:49]\n\t"
      "s_cbranch_scc1 NP1R_%=\n\t"
      "NP1B_%=:\n\t"
      ".LhopC0:\n\t"
      "s_cbranch_execz .LbackC\n\t"
      "ds_write_b128 v243, v[200:203] offset:32768\n\t"
      "v_mfma_f32_16x16x32_bf16 v[212:215], v[148:151], v[92:95], v[212:215]\n\t"
      "global_load_dwordx4 v[148:151], v229, s[56:57] sc1\n\t"
      "v_mfma_f32_16x16x32_bf16 v[212:215], v[152:155], v[108:111], v[212:215]\n\t"
      "global_load_dwordx4 v[152:155], v230, s[56:57] sc1\n\t"
      "ds_write_b128 v220, v[204:207] offset:32768\n\t"
      "ds_write_b128 v221, v[208:211] offset:32768\n\t"
      "s_nop 4\n\t"
      ".LhopC1:\n\t"
      "s_cbranch_execz .LbackC\n\t"
      "ds_write_b128 v222, v[212:215] offset:32768\n\t"
      "s_waitcnt lgkmcnt(0)\n\t"
      "s_barrier\n\t"
      "ds_read_b128 v[112:115], v244 offset:32768\n\t"
      "ds_read_b128 v[116:119], v244 offset:36864\n\t"
      "ds_read_b128 v[120:123], v244 offset:40960\n\t"
      "ds_read_b128 v[124:127], v244 offset:45056\n\t"
      "ds_read_b128 v[200:203], v244 offset:49152\n\t"
      "ds_read_b128 v[204:207], v244 offset:53248\n\t"
      "ds_read_b128 v[208:211], v244 offset:57344\n\t"
      "ds_read_b128 v[212:215], v244 offset:61440\n\t"
      "s_waitcnt vmcnt(7)\n\t"
      "v_mfma_f32_16x16x32_bf16 v[184:187], v[128:131], v[0:3], v[164:167]\n\t"
      "v_mfma_f32_16x16x32_bf16 v[188:191], v[128:131], v[4:7], 0\n\t"
      "s_waitcnt lgkmcnt(6)\n\t"
      "v_add_f32_e32 v112, v112, v116\n\t"
      "v_add_f32_e32 v113, v113, v117\n\t"
      "v_mfma_f32_16x16x32_bf16 v[192:195], v[128:131], v[8:11], 0\n\t"
      "v_add_f32_e32 v114, v114, v118\n\t"
      "v_add_f32_e32 v115, v115, v119\n\t"
      "v_mfma_f32_16x16x32_bf16 v[196:199], v[128:131], v[12:15], 0\n\t"
      "global_load_dwordx4 v[128:131], v224, s[58:59] sc1\n\t"
      "s_waitcnt vmcnt(7)\n\t"
      "v_mfma_f32_16x16x32_bf16 v[184:187], v[132:135], v[16:19], v[184:187]\n\t"
      "s_waitcnt lgkmcnt(4)\n\t"
      "v_add_f32_e32 v120, v120, v124\n\t"
      "s_waitcnt vmcnt(6)\n\t"
      "v_mfma_f32_16x16x32_bf16 v[184:187], v[136:139], v[32:35], v[184:187]\n\t"
      "v_add_f32_e32 v121, v121, v125\n\t"
      "v_add_f32_e32 v122, v122, v126\n\t"
      "v_mfma_f32_16x16x32_bf16 v[188:191], v[132:135], v[20:23], v[188:191]\n\t"
      "v_add_f32_e32 v123, v123, v127\n\t"
      "s_waitcnt lgkmcnt(2)\n\t"
      "v_add_f32_e32 v200, v200, v204\n\t"
      "v_mfma_f32_16x16x32_bf16 v[188:191], v[136:139], v[36:39], v[188:191]\n\t"
      "v_add_f32_e32 v201, v201, v205\n\t"
      "v_add_f32_e32 v202, v202, v206\n\t"
      "v_mfma_f32_16x16x32_bf16 v[192:195], v[132:135], v[24:27], v[192:195]\n\t"
      "v_add_f32_e32 v203, v203, v207\n\t"
      "v_add_f32_e32 v112, v112, v120\n\t"
      "v_mfma_f32_16x16x32_bf16 v[192:195], v[136:139], v[40:43], v[192:195]\n\t"
      "v_add_f32_e32 v113, v113, v121\n\t"
      "v_add_f32_e32 v114, v114, v122\n\t"
      "v_mfma_f32_16x16x32_bf16 v[196:199], v[132:135], v[28:31], v[196:199]\n\t"
      "global_load_dwordx4 v[132:135], v225, s[58:59] sc1\n\t"
      "v_add_f32_e32 v115, v115, v123\n\t"
      "v_mfma_f32_16x16x32_bf16 v[196:199], v[136:139], v[44:47], v[196:199]\n\t"
      "global_load_dwordx4 v[136:139], v226, s[58:59] sc1\n\t"
      "s_waitcnt vmcnt(7)\n\t"
      "v_mfma_f32_16x16x32_bf16 v[184:187], v[140:143], v[48:51], v[184:187]\n\t"
      "s_waitcnt lgkmcnt(0)\n\t"
      "v_add_f32_e32 v208, v208, v212\n\t"
      "s_waitcnt vmcnt(6)\n\t"
      "v_mfma_f32_16x16x32_bf16 v[184:187], v[144:147], v[64:67], v[184:187]\n\t"
      "v_add_f32_e32 v209, v209, v213\n\t"
      "v_add_f32_e32 v210, v210, v214\n\t"
      "v_mfma_f32_16x16x32_bf16 v[188:191], v[140:143], v[52:55], v[188:191]\n\t"
      "v_add_f32_e32 v211, v211, v215\n\t"
      "v_add_f32_e32 v200, v200, v208\n\t"
      "v_mfma_f32_16x16x32_bf16 v[188:191], v[144:147], v[68:71], v[188:191]\n\t"
      "v_add_f32_e32 v201, v201, v209\n\t"
      "v_add_f32_e32 v202, v202, v210\n\t"
      "v_mfma_f32_16x16x32_bf16 v[192:195], v[140:143], v[56:59], v[192:195]\n\t"
      "v_add_f32_e32 v203, v203, v211\n\t"
      "v_add_f32_e32 v112, v112, v200\n\t"
      "v_mfma_f32_16x16x32_bf16 v[192:195], v[144:147], v[72:75], v[192:195]\n\t"
      "v_add_f32_e32 v113, v113, v201\n\t"
      "v_add_f32_e32 v114, v114, v202\n\t"
      "v_mfma_f32_16x16x32_bf16 v[196:199], v[140:143], v[60:63], v[196:199]\n\t"
      "global_load_dwordx4 v[140:143], v227, s[58:59] sc1\n\t"
      "v_add_f32_e32 v115, v115, v203\n\t"
      "v_mfma_f32_16x16x32_bf16 v[196:199], v[144:147], v[76:79], v[196:199]\n\t"
      "global_load_dwordx4 v[144:147], v228, s[58:59] sc1\n\t"
      "s_waitcnt vmcnt(6)\n\t"
      "v_mfma_f32_16x16x32_bf16 v[184:187], v[148:151], v[80:83], v[184:187]\n\t"
      "v_cvt_pk_bf16_f32 v116, v112, v113\n\t"
      "s_waitcnt vmcnt(5)\n\t"
      "v_mfma_f32_16x16x32_bf16 v[184:187], v[152:155], v[96:99], v[184:187]\n\t"
      "v_cvt_pk_bf16_f32 v117, v114, v115\n\t"
      "global_store_dwordx2 v245, v[116:117], s[8:9] offset:32\n\t"
      "v_mfma_f32_16x16x32_bf16 v[188:191], v[148:151], v[84:87], v[188:191]\n\t"
      "v_mfma_f32_16x16x32_bf16 v[188:191], v[152:155], v[100:103], v[188:191]\n\t"
      "s_nop 3\n\t"
      "v_cmp_u_f32_e32 vcc, v184, v185\n\t"
      "v_cmp_u_f32_e64 s[48:49], v186, v187\n\t"
      "v_mfma_f32_16x16x32_bf16 v[192:195], v[148:151], v[88:91], v[192:195]\n\t"
      "v_mfma_f32_16x16x32_bf16 v[192:195], v[152:155], v[104:107], v[192:195]\n\t"
      "s_or_b64 vcc, vcc, s[48:49]\n\t"
      "s_cbranch_scc1 NP2R_%=\n\t"
      "NP2B_%=:\n\t"
      "ds_write_b128 v243, v[184:187]\n\t"
      "v_mfma_f32_16x16x32_bf16 v[196:199], v[148:151], v[92:95], v[196:199]\n\t"
      "global_load_dwordx4 v[148:151], v229, s[58:59] sc1\n\t"
      "v_mfma_f32_16x16x32_bf16 v[196:199], v[152:155], v[108:111], v[196:199]\n\t"
      "global_load_dwordx4 v[152:155], v230, s[58:59] sc1\n\t"
      "ds_write_b128 v220, v[188:191]\n\t"
      "ds_write_b128 v221, v[192:195]\n\t"
      "s_nop 4\n\t"
      "ds_write_b128 v222, v[196:199]\n\t"
      "s_waitcnt lgkmcnt(0)\n\t"
      "s_barrier\n\t"
      "ds_read_b128 v[112:115], v244\n\t"
      "ds_read_b128 v[116:119], v244 offset:4096\n\t"
      "ds_read_b128 v[120:123], v244 offset:8192\n\t"
      "ds_read_b128 v[124:127], v244 offset:12288\n\t"
      "ds_read_b128 v[184:187], v244 offset:16384\n\t"
      "ds_read_b128 v[188:191], v244 offset:20480\n\t"
      "ds_read_b128 v[192:195], v244 offset:24576\n\t"
      "ds_read_b128 v[196:199], v244 offset:28672\n\t"
      "s_waitcnt vmcnt(7)\n\t"
      "v_mfma_f32_16x16x32_bf16 v[200:203], v[128:131], v[0:3], v[168:171]\n\t"
      "v_mfma_f32_16x16x32_bf16 v[204:207], v[128:131], v[4:7], 0\n\t"
      "s_waitcnt lgkmcnt(6)\n\t"
      "v_add_f32_e32 v112, v112, v116\n\t"
      "v_add_f32_e32 v113, v113, v117\n\t"
      "v_mfma_f32_16x16x32_bf16 v[208:211], v[128:131], v[8:11], 0\n\t"
      "v_add_f32_e32 v114, v114, v118\n\t"
      "v_add_f32_e32 v115, v115, v119\n\t"
      "v_mfma_f32_16x16x32_bf16 v[212:215], v[128:131], v[12:15], 0\n\t"
      "global_load_dwordx4 v[128:131], v224, s[60:61] sc1\n\t"
      "s_waitcnt vmcnt(7)\n\t"
      "v_mfma_f32_16x16x32_bf16 v[200:203], v[132:135], v[16:19], v[200:203]\n\t"
      "s_waitcnt lgkmcnt(4)\n\t"
      "v_add_f32_e32 v120, v120, v124\n\t"
      "s_waitcnt vmcnt(6)\n\t"
      "v_mfma_f32_16x16x32_bf16 v[200:203], v[136:139], v[32:35], v[200:203]\n\t"
      "v_add_f32_e32 v121, v121, v125\n\t"
      "v_add_f32_e32 v122, v122, v126\n\t"
      "v_mfma_f32_16x16x32_bf16 v[204:207], v[132:135], v[20:23], v[204:207]\n\t"
      "v_add_f32_e32 v123, v123, v127\n\t"
      "s_waitcnt lgkmcnt(2)\n\t"
      "v_add_f32_e32 v184, v184, v188\n\t"
      "v_mfma_f32_16x16x32_bf16 v[204:207], v[136:139], v[36:39], v[204:207]\n\t"
      "v_add_f32_e32 v185, v185, v189\n\t"
      "v_add_f32_e32 v186, v186, v190\n\t"
      "v_mfma_f32_16x16x32_bf16 v[208:211], v[132:135], v[24:27], v[208:211]\n\t"
      "v_add_f32_e32 v187, v187, v191\n\t"
      "v_add_f32_e32 v112, v112, v120\n\t"
      "v_mfma_f32_16x16x32_bf16 v[208:211], v[136:139], v[40:43], v[208:211]\n\t"
      "v_add_f32_e32 v113, v113, v121\n\t"
      "v_add_f32_e32 v114, v114, v122\n\t"
      "v_mfma_f32_16x16x32_bf16 v[212:215], v[132:135], v[28:31], v[212:215]\n\t"
      "global_load_dwordx4 v[132:135], v225, s[60:61] sc1\n\t"
      "v_add_f32_e32 v115, v115, v123\n\t"
      "v_mfma_f32_16x16x32_bf16 v[212:215], v[136:139], v[44:47], v[212:215]\n\t"
      "global_load_dwordx4 v[136:139], v226, s[60:61] sc1\n\t"
      "s_waitcnt vmcnt(7)\n\t"
      "v_mfma_f32_16x16x32_bf16 v[200:203], v[140:143], v[48:51], v[200:203]\n\t"
      "s_waitcnt lgkmcnt(0)\n\t"
      "v_add_f32_e32 v192, v192, v196\n\t"
      "s_waitcnt vmcnt(6)\n\t"
      "v_mfma_f32_16x16x32_bf16 v[200:203], v[144:147], v[64:67], v[200:203]\n\t"
      "v_add_f32_e32 v193, v193, v197\n\t"
      "v_add_f32_e32 v194, v194, v198\n\t"
      "v_mfma_f32_16x16x32_bf16 v[204:207], v[140:143], v[52:55], v[204:207]\n\t"
      "v_add_f32_e32 v195, v195, v199\n\t"
      "v_add_f32_e32 v184, v184, v192\n\t"
      "v_mfma_f32_16x16x32_bf16 v[204:207], v[144:147], v[68:71], v[204:207]\n\t"
      "v_add_f32_e32 v185, v185, v193\n\t"
      "v_add_f32_e32 v186, v186, v194\n\t"
      "v_mfma_f32_16x16x32_bf16 v[208:211], v[140:143], v[56:59], v[208:211]\n\t"
      "v_add_f32_e32 v187, v187, v195\n\t"
      "v_add_f32_e32 v112, v112, v184\n\t"
      "v_mfma_f32_16x16x32_bf16 v[208:211], v[144:147], v[72:75], v[208:211]\n\t"
      "v_add_f32_e32 v113, v113, v185\n\t"
      "v_add_f32_e32 v114, v114, v186\n\t"
      "v_mfma_f32_16x16x32_bf16 v[212:215], v[140:143], v[60:63], v[212:215]\n\t"
      "global_load_dwordx4 v[140:143], v227, s[60:61] sc1\n\t"
      "v_add_f32_e32 v115, v115, v187\n\t"
      "v_mfma_f32_16x16x32_bf16 v[212:215], v[144:147], v[76:79], v[212:215]\n\t"
      "global_load_dwordx4 v[144:147], v228, s[60:61] sc1\n\t"
      "s_waitcnt vmcnt(6)\n\t"
      "v_mfma_f32_16x16x32_bf16 v[200:203], v[148:151], v[80:83], v[200:203]\n\t"
      "v_cvt_pk_bf16_f32 v116, v112, v113\n\t"
      "s_waitcnt vmcnt(5)\n\t"
      "v_mfma_f32_16x16x32_bf16 v[200:203], v[152:155], v[96:99], v[200:203]\n\t"
      "v_cvt_pk_bf16_f32 v117, v114, v115\n\t"
      "global_store_dwordx2 v245, v[116:117], s[66:67]\n\t"
      "v_mfma_f32_16x16x32_bf16 v[204:207], v[148:151], v[84:87], v[204:207]\n\t"
      "v_mfma_f32_16x16x32_bf16 v[204:207], v[152:155], v[100:103], v[204:207]\n\t"
      "s_nop 3\n\t"
      "v_cmp_u_f32_e32 vcc, v200, v201\n\t"
      "v_cmp_u_f32_e64 s[48:49], v202, v203\n\t"
      "v_mfma_f32_16x16x32_bf16 v[208:211], v[148:151], v[88:91], v[208:211]\n\t"
      "v_mfma_f32_16x16x32_bf16 v[208:211], v[152:155], v[104:107], v[208:211]\n\t"
      "s_or_b64 vcc, vcc, s[48:49]\n\t"
      "s_cbranch_scc1 NP3R_%=\n\t"
      "NP3B_%=:\n\t"
      "ds_write_b128 v243, v[200:203] offset:32768\n\t"
      "v_mfma_f32_16x16x32_bf16 v[212:215], v[148:151], v[92:95], v[212:215]\n\t"
      "global_load_dwordx4 v[148:151], v229, s[60:61] sc1\n\t"
      "v_mfma_f32_16x16x32_bf16 v[212:215], v[152:155], v[108:111], v[212:215]\n\t"
      "global_load_dwordx4 v[152:155], v230, s[60:61] sc1\n\t"
      "ds_write_b128 v220, v[204:207] offset:32768\n\t"
      "ds_write_b128 v221, v[208:211] offset:32768\n\t"
      "s_nop 4\n\t"
      "ds_write_b128 v222, v[212:215] offset:32768\n\t"
      "s_waitcnt lgkmcnt(0)\n\t"
      "s_barrier\n\t"
      "ds_read_b128 v[112:115], v244 offset:32768\n\t"
      "ds_read_b128 v[116:119], v244 offset:36864\n\t"
      "ds_read_b128 v[120:123], v244 offset:40960\n\t"
      "ds_read_b128 v[124:127], v244 offset:45056\n\t"
      "ds_read_b128 v[200:203], v244 offset:49152\n\t"
      "ds_read_b128 v[204:207], v244 offset:53248\n\t"
      "ds_read_b128 v[208:211], v244 offset:57344\n\t"
      "ds_read_b128 v[212:215], v244 offset:61440\n\t"
      "s_waitcnt vmcnt(7)\n\t"
      "v_mfma_f32_16x16x32_bf16 v[184:187], v[128:131], v[0:3], v[172:175]\n\t"
      "v_mfma_f32_16x16x32_bf16 v[188:191], v[128:131], v[4:7], 0\n\t"
      "s_waitcnt lgkmcnt(6)\n\t"
      "v_add_f32_e32 v112, v112, v116\n\t"
      "v_add_f32_e32 v113, v113, v117\n\t"
      "v_mfma_f32_16x16x32_bf16 v[192:195], v[128:131], v[8:11], 0\n\t"
      "v_add_f32_e32 v114, v114, v118\n\t"
      "v_add_f32_e32 v115, v115, v119\n\t"
      "v_mfma_f32_16x16x32_bf16 v[196:199], v[128:131], v[12:15], 0\n\t"
      "global_load_dwordx4 v[128:131], v224, s[62:63] sc1\n\t"
      "s_waitcnt vmcnt(7)\n\t"
      "v_mfma_f32_16x16x32_bf16 v[184:187], v[132:135], v[16:19], v[184:187]\n\t"
      "s_waitcnt lgkmcnt(4)\n\t"
      "v_add_f32_e32 v120, v120, v124\n\t"
      "s_waitcnt vmcnt(6)\n\t"
      "v_mfma_f32_16x16x32_bf16 v[184:187], v[136:139], v[32:35], v[184:187]\n\t"
      "v_add_f32_e32 v121, v121, v125\n\t"
      "v_add_f32_e32 v122, v122, v126\n\t"
      "v_mfma_f32_16x16x32_bf16 v[188:191], v[132:135], v[20:23], v[188:191]\n\t"
      "v_add_f32_e32 v123, v123, v127\n\t"
      "s_waitcnt lgkmcnt(2)\n\t"
      "v_add_f32_e32 v200, v200, v204\n\t"
      "v_mfma_f32_16x16x32_bf16 v[188:191], v[136:139], v[36:39], v[188:191]\n\t"
      "v_add_f32_e32 v201, v201, v205\n\t"
      "v_add_f32_e32 v202, v202, v206\n\t"
      "v_mfma_f32_16x16x32_bf16 v[192:195], v[132:135], v[24:27], v[192:195]\n\t"
      "v_add_f32_e32 v203, v203, v207\n\t"
      "v_add_f32_e32 v112, v112, v120\n\t"
      "v_mfma_f32_16x16x32_bf16 v[192:195], v[136:139], v[40:43], v[192:195]\n\t"
      "v_add_f32_e32 v113, v113, v121\n\t"
      "v_add_f32_e32 v114, v114, v122\n\t"
      "v_mfma_f32_16x16x32_bf16 v[196:199], v[132:135], v[28:31], v[196:199]\n\t"
      "global_load_dwordx4 v[132:135], v225, s[62:63] sc1\n\t"
      "v_add_f32_e32 v115, v115, v123\n\t"
      "v_mfma_f32_16x16x32_bf16 v[196:199], v[136:139], v[44:47], v[196:199]\n\t"
      "global_load_dwordx4 v[136:139], v226, s[62:63] sc1\n\t"
      "s_waitcnt vmcnt(7)\n\t"
      "v_mfma_f32_16x16x32_bf16 v[184:187], v[140:143], v[48:51], v[184:187]\n\t"
      "s_waitcnt lgkmcnt(0)\n\t"
      "v_add_f32_e32 v208, v208, v212\n\t"
      "s_waitcnt vmcnt(6)\n\t"
      "v_mfma_f32_16x16x32_bf16 v[184:187], v[144:147], v[64:67], v[184:187]\n\t"
      "v_add_f32_e32 v209, v209, v213\n\t"
      "v_add_f32_e32 v210, v210, v214\n\t"
      "v_mfma_f32_16x16x32_bf16 v[188:191], v[140:143], v[52:55], v[188:191]\n\t"
      "v_add_f32_e32 v211, v211, v215\n\t"
      "v_add_f32_e32 v200, v200, v208\n\t"
      "v_mfma_f32_16x16x32_bf16 v[188:191], v[144:147], v[68:71], v[188:191]\n\t"
      "v_add_f32_e32 v201, v201, v209\n\t"
      "v_add_f32_e32 v202, v202, v210\n\t"
      "v_mfma_f32_16x16x32_bf16 v[192:195], v[140:143], v[56:59], v[192:195]\n\t"
      "v_add_f32_e32 v203, v203, v211\n\t"
      "v_add_f32_e32 v112, v112, v200\n\t"
      "v_mfma_f32_16x16x32_bf16 v[192:195], v[144:147], v[72:75], v[192:195]\n\t"
      "v_add_f32_e32 v113, v113, v201\n\t"
      "v_add_f32_e32 v114, v114, v202\n\t"
      "v_mfma_f32_16x16x32_bf16 v[196:199], v[140:143], v[60:63], v[196:199]\n\t"
      "global_load_dwordx4 v[140:143], v227, s[62:63] sc1\n\t"
      "v_add_f32_e32 v115, v115, v203\n\t"
      "v_mfma_f32_16x16x32_bf16 v[196:199], v[144:147], v[76:79], v[196:199]\n\t"
      "global_load_dwordx4 v[144:147], v228, s[62:63] sc1\n\t"
      "s_waitcnt vmcnt(6)\n\t"
      "v_mfma_f32_16x16x32_bf16 v[184:187], v[148:151], v[80:83], v[184:187]\n\t"
      "v_cvt_pk_bf16_f32 v116, v112, v113\n\t"
      "s_waitcnt vmcnt(5)\n\t"
      "v_mfma_f32_16x16x32_bf16 v[184:187], v[152:155], v[96:99], v[184:187]\n\t"
      "v_cvt_pk_bf16_f32 v117, v114, v115\n\t"
      "global_store_dwordx2 v245, v[116:117], s[66:67] offset:32\n\t"
      "v_mfma_f32_16x16x32_bf16 v[188:191], v[148:151], v[84:87], v[188:191]\n\t"
      "v_mfma_f32_16x16x32_bf16 v[188:191], v[152:155], v[100:103], v[188:191]\n\t"
      "s_nop 3\n\t"
      "v_cmp_u_f32_e32 vcc, v184, v185\n\t"
      "v_cmp_u_f32_e64 s[48:49], v186, v187\n\t"
      "v_mfma_f32_16x16x32_bf16 v[192:195], v[148:151], v[88:91], v[192:195]\n\t"
      "v_mfma_f32_16x16x32_bf16 v[192:195], v[152:155], v[104:107], v[192:195]\n\t"
      "s_or_b64 vcc, vcc, s[48:49]\n\t"
      "s_cbranch_scc1 NP4R_%=\n\t"
      "NP4B_%=:\n\t"
      "ds_write_b128 v243, v[184:187]\n\t"
      "v_mfma_f32_16x16x32_bf16 v[196:199], v[148:151], v[92:95], v[196:199]\n\t"
      "global_load_dwordx4 v[148:151], v229, s[62:63] sc1\n\t"
      "v_mfma_f32_16x16x32_bf16 v[196:199], v[152:155], v[108:111], v[196:199]\n\t"
      "global_load_dwordx4 v[152:155], v230, s[62:63] sc1\n\t"
      "ds_write_b128 v220, v[188:191]\n\t"
      "ds_write_b128 v221, v[192:195]\n\t"
      "s_nop 4\n\t"
      "ds_write_b128 v222, v[196:199]\n\t"
      "s_waitcnt lgkmcnt(0)\n\t"
      "s_barrier\n\t"
      "ds_read_b128 v[112:115], v244\n\t"
      "ds_read_b128 v[116:119], v244 offset:4096\n\t"
      "ds_read_b128 v[120:123], v244 offset:8192\n\t"
      "ds_read_b128 v[124:127], v244 offset:12288\n\t"
      "ds_read_b128 v[184:187], v244 offset:16384\n\t"
      "ds_read_b128 v[188:191], v244 offset:20480\n\t"
      "ds_read_b128 v[192:195], v244 offset:24576\n\t"
      "ds_read_b128 v[196:199], v244 offset:28672\n\t"
      "s_waitcnt vmcnt(7)\n\t"
      "v_mfma_f32_16x16x32_bf16 v[200:203], v[128:131], v[0:3], v[176:179]\n\t"
      "v_mfma_f32_16x16x32_bf16 v[204:207], v[128:131], v[4:7], 0\n\t"
      "s_waitcnt lgkmcnt(6)\n\t"
      "v_add_f32_e32 v112, v112, v116\n\t"
      "v_add_f32_e32 v113, v113, v117\n\t"
      "v_mfma_f32_16x16x32_bf16 v[208:211], v[128:131], v[8:11], 0\n\t"
      "v_add_f32_e32 v114, v114, v118\n\t"
      "v_add_f32_e32 v115, v115, v119\n\t"
      "v_mfma_f32_16x16x32_bf16 v[212:215], v[128:131], v[12:15], 0\n\t"
      "global_load_dwordx4 v[128:131], v224, s[64:65] sc1\n\t"
      "s_waitcnt vmcnt(7)\n\t"
      "v_mfma_f32_16x16x32_bf16 v[200:203], v[132:135], v[16:19], v[200:203]\n\t"
      "s_waitcnt lgkmcnt(4)\n\t"
      "v_add_f32_e32 v120, v120, v124\n\t"
      "s_waitcnt vmcnt(6)\n\t"
      "v_mfma_f32_16x16x32_bf16 v[200:203], v[136:139], v[32:35], v[200:203]\n\t"
      "v_add_f32_e32 v121, v121, v125\n\t"
      "v_add_f32_e32 v122, v122, v126\n\t"
      "v_mfma_f32_16x16x32_bf16 v[204:207], v[132:135], v[20:23], v[204:207]\n\t"
      "v_add_f32_e32 v123, v123, v127\n\t"
      "s_waitcnt lgkmcnt(2)\n\t"
      "v_add_f32_e32 v184, v184, v188\n\t"
      "v_mfma_f32_16x16x32_bf16 v[204:207], v[136:139], v[36:39], v[204:207]\n\t"
      "v_add_f32_e32 v185, v185, v189\n\t"
      "v_add_f32_e32 v186, v186, v190\n\t"
      "v_mfma_f32_16x16x32_bf16 v[208:211], v[132:135], v[24:27], v[208:211]\n\t"
      "v_add_f32_e32 v187, v187, v191\n\t"
      "v_add_f32_e32 v112, v112, v120\n\t"
      "v_mfma_f32_16x16x32_bf16 v[208:211], v[136:139], v[40:43], v[208:211]\n\t"
      "v_add_f32_e32 v113, v113, v121\n\t"
      "v_add_f32_e32 v114, v114, v122\n\t"
      "v_mfma_f32_16x16x32_bf16 v[212:215], v[132:135], v[28:31], v[212:215]\n\t"
      "global_load_dwordx4 v[132:135], v225, s[64:65] sc1\n\t"
      "v_add_f32_e32 v115, v115, v123\n\t"
      "v_mfma_f32_16x16x32_bf16 v[212:215], v[136:139], v[44:47], v[212:215]\n\t"
      "global_load_dwordx4 v[136:139], v226, s[64:65] sc1\n\t"
      "s_waitcnt vmcnt(7)\n\t"
      "v_mfma_f32_16x16x32_bf16 v[200:203], v[140:143], v[48:51], v[200:203]\n\t"
      "s_waitcnt lgkmcnt(0)\n\t"
      "v_add_f32_e32 v192, v192, v196\n\t"
      "s_waitcnt vmcnt(6)\n\t"
      "v_mfma_f32_16x16x32_bf16 v[200:203], v[144:147], v[64:67], v[200:203]\n\t"
      "v_add_f32_e32 v193, v193, v197\n\t"
      "v_add_f32_e32 v194, v194, v198\n\t"
      "v_mfma_f32_16x16x32_bf16 v[204:207], v[140:143], v[52:55], v[204:207]\n\t"
      "v_add_f32_e32 v195, v195, v199\n\t"
      "v_add_f32_e32 v184, v184, v192\n\t"
      "v_mfma_f32_16x16x32_bf16 v[204:207], v[144:147], v[68:71], v[204:207]\n\t"
      "v_add_f32_e32 v185, v185, v193\n\t"
      "v_add_f32_e32 v186, v186, v194\n\t"
      "v_mfma_f32_16x16x32_bf16 v[208:211], v[140:143], v[56:59], v[208:211]\n\t"
      "v_add_f32_e32 v187, v187, v195\n\t"
      "v_add_f32_e32 v112, v112, v184\n\t"
      "v_mfma_f32_16x16x32_bf16 v[208:211], v[144:147], v[72:75], v[208:211]\n\t"
      "v_add_f32_e32 v113, v113, v185\n\t"
      "v_add_f32_e32 v114, v114, v186\n\t"
      "v_mfma_f32_16x16x32_bf16 v[212:215], v[140:143], v[60:63], v[212:215]\n\t"
      "global_load_dwordx4 v[140:143], v227, s[64:65] sc1\n\t"
      "v_add_f32_e32 v115, v115, v187\n\t"
      "v_mfma_f32_16x16x32_bf16 v[212:215], v[144:147], v[76:79], v[212:215]\n\t"
      "global_load_dwordx4 v[144:147], v228, s[64:65] sc1\n\t"
      "s_waitcnt vmcnt(6)\n\t"
      "v_mfma_f32_16x16x32_bf16 v[200:203], v[148:151], v[80:83], v[200:203]\n\t"
      "v_cvt_pk_bf16_f32 v116, v112, v113\n\t"
      "s_waitcnt vmcnt(5)\n\t"
      "v_mfma_f32_16x16x32_bf16 v[200:203], v[152:155], v[96:99], v[200:203]\n\t"
      "v_cvt_pk_bf16_f32 v117, v114, v115\n\t"
      "global_store_dwordx2 v245, v[116:117], s[8:9] offset:64\n\t"
      "v_mfma_f32_16x16x32_bf16 v[204:207], v[148:151], v[84:87], v[204:207]\n\t"
      "v_mfma_f32_16x16x32_bf16 v[204:207], v[152:155], v[100:103], v[204:207]\n\t"
      "s_nop 3\n\t"
      "v_cmp_u_f32_e32 vcc, v200, v201\n\t"
      "v_cmp_u_f32_e64 s[48:49], v202, v203\n\t"
      "v_mfma_f32_16x16x32_bf16 v[208:211], v[148:151], v[88:91], v[208:211]\n\t"
      "v_mfma_f32_16x16x32_bf16 v[208:211], v[152:155], v[104:107], v[208:211]\n\t"
      "s_or_b64 vcc, vcc, s[48:49]\n\t"
      "s_cbranch_scc1 NP5R_%=\n\t"
      "NP5B_%=:\n\t"
      "ds_write_b128 v243, v[200:203] offset:32768\n\t"
      "v_mfma_f32_16x16x32_bf16 v[212:215], v[148:151], v[92:95], v[212:215]\n\t"
      "global_load_dwordx4 v[148:151], v229, s[64:65] sc1\n\t"
      "v_mfma_f32_16x16x32_bf16 v[212:215], v[152:155], v[108:111], v[212:215]\n\t"
      "global_load_dwordx4 v[152:155], v230, s[64:65] sc1\n\t"
      "ds_write_b128 v220, v[204:207] offset:32768\n\t"
      "ds_write_b128 v221, v[208:211] offset:32768\n\t"
      "s_nop 4\n\t"
      "ds_write_b128 v222, v[212:215] offset:32768\n\t"
      "s_waitcnt lgkmcnt(0)\n\t"
      "s_barrier\n\t"
      "ds_read_b128 v[112:115], v244 offset:32768\n\t"
      "ds_read_b128 v[116:119], v244 offset:36864\n\t"
      "ds_read_b128 v[120:123], v244 offset:40960\n\t"
      "ds_read_b128 v[124:127], v244 offset:45056\n\t"
      "ds_read_b128 v[200:203], v244 offset:49152\n\t"
      "ds_read_b128 v[204:207], v244 offset:53248\n\t"
      "ds_read_b128 v[208:211], v244 offset:57344\n\t"
      "ds_read_b128 v[212:215], v244 offset:61440\n\t"
      "s_waitcnt vmcnt(7)\n\t"
      "v_mfma_f32_16x16x32_bf16 v[184:187], v[128:131], v[0:3], v[180:183]\n\t"
      "v_mfma_f32_16x16x32_bf16 v[188:191], v[128:131], v[4:7], 0\n\t"
      "v_mfma_f32_16x16x32_bf16 v[192:195], v[128:131], v[8:11], 0\n\t"
      "v_mfma_f32_16x16x32_bf16 v[196:199], v[128:131], v[12:15], 0\n\t"
      "s_waitcnt vmcnt(6)\n\t"
      "v_mfma_f32_16x16x32_bf16 v[184:187], v[132:135], v[16:19], v[184:187]\n\t"
      "s_waitcnt lgkmcnt(6)\n\t"
      "v_add_f32_e32 v112, v112, v116\n\t"
      "s_waitcnt vmcnt(5)\n\t"
      "v_mfma_f32_16x16x32_bf16 v[184:187], v[136:139], v[32:35], v[184:187]\n\t"
      "v_add_f32_e32 v113, v113, v117\n\t"
      "v_add_f32_e32 v114, v114, v118\n\t"
      "v_mfma_f32_16x16x32_bf16 v[188:191], v[132:135], v[20:23], v[188:191]\n\t"
      "v_add_f32_e32 v115, v115, v119\n\t"
      "s_waitcnt lgkmcnt(4)\n\t"
      "v_add_f32_e32 v120, v120, v124\n\t"
      "v_mfma_f32_16x16x32_bf16 v[188:191], v[136:139], v[36:39], v[188:191]\n\t"
      "v_add_f32_e32 v121, v121, v125\n\t"
      "v_add_f32_e32 v122, v122, v126\n\t"
      "v_mfma_f32_16x16x32_bf16 v[192:195], v[132:135], v[24:27], v[192:195]\n\t"
      "v_add_f32_e32 v123, v123, v127\n\t"
      "s_waitcnt lgkmcnt(2)\n\t"
      "v_add_f32_e32 v200, v200, v204\n\t"
      "v_mfma_f32_16x16x32_bf16 v[192:195], v[136:139], v[40:43], v[192:195]\n\t"
      "v_add_f32_e32 v201, v201, v205\n\t"
      "v_add_f32_e32 v202, v202, v206\n\t"
      "v_mfma_f32_16x16x32_bf16 v[196:199], v[132:135], v[28:31], v[196:199]\n\t"
      "v_add_f32_e32 v203, v203, v207\n\t"
      "v_add_f32_e32 v112, v112, v120\n\t"
      "v_mfma_f32_16x16x32_bf16 v[196:199], v[136:139], v[44:47], v[196:199]\n\t"
      "v_add_f32_e32 v113, v113, v121\n\t"
      "s_waitcnt vmcnt(4)\n\t"
      "v_mfma_f32_16x16x32_bf16 v[184:187], v[140:143], v[48:51], v[184:187]\n\t"
      "v_add_f32_e32 v114, v114, v122\n\t"
      "s_waitcnt vmcnt(3)\n\t"
      "v_mfma_f32_16x16x32_bf16 v[184:187], v[144:147], v[64:67], v[184:187]\n\t"
      "v_add_f32_e32 v115, v115, v123\n\t"
      "s_waitcnt lgkmcnt(0)\n\t"
      "v_add_f32_e32 v208, v208, v212\n\t"
      "v_mfma_f32_16x16x32_bf16 v[188:191], v[140:143], v[52:55], v[188:191]\n\t"
      "v_add_f32_e32 v209, v209, v213\n\t"
      "v_add_f32_e32 v210, v210, v214\n\t"
      "v_mfma_f32_16x16x32_bf16 v[188:191], v[144:147], v[68:71], v[188:191]\n\t"
      "v_add_f32_e32 v211, v211, v215\n\t"
      "v_add_f32_e32 v200, v200, v208\n\t"
      "v_mfma_f32_16x16x32_bf16 v[192:195], v[140:143], v[56:59], v[192:195]\n\t"
      "v_add_f32_e32 v201, v201, v209\n\t"
      "v_add_f32_e32 v202, v202, v210\n\t"
      "v_mfma_f32_16x16x32_bf16 v[192:195], v[144:147], v[72:75], v[192:195]\n\t"
      "v_add_f32_e32 v203, v203, v211\n\t"
      "v_add_f32_e32 v112, v112, v200\n\t"
      "v_mfma_f32_16x16x32_bf16 v[196:199], v[140:143], v[60:63], v[196:199]\n\t"
      "v_add_f32_e32 v113, v113, v201\n\t"
      "v_add_f32_e32 v114, v114, v202\n\t"
      "v_mfma_f32_16x16x32_bf16 v[196:199], v[144:147], v[76:79], v[196:199]\n\t"
      "v_add_f32_e32 v115, v115, v203\n\t"
      "s_waitcnt vmcnt(1)\n\t"
      "v_mfma_f32_16x16x32_bf16 v[184:187], v[148:151], v[80:83], v[184:187]\n\t"
      "v_cvt_pk_bf16_f32 v116, v112, v113\n\t"
      "s_waitcnt vmcnt(0)\n\t"
      "v_mfma_f32_16x16x32_bf16 v[184:187], v[152:155], v[96:99], v[184:187]\n\t"
      "v_cvt_pk_bf16_f32 v117, v114, v115\n\t"
      "global_store_dwordx2 v245, v[116:117], s[8:9] offset:96\n\t"
      "v_mfma_f32_16x16x32_bf16 v[188:191], v[148:151], v[84:87], v[188:191]\n\t"
      "v_mfma_f32_16x16x32_bf16 v[188:191], v[152:155], v[100:103], v[188:191]\n\t"
      "s_nop 3\n\t"
      "v_cmp_u_f32_e32 vcc, v184, v185\n\t"
      "v_cmp_u_f32_e64 s[48:49], v186, v187\n\t"
      "v_mfma_f32_16x16x32_bf16 v[192:195], v[148:151], v[88:91], v[192:195]\n\t"
      "v_mfma_f32_16x16x32_bf16 v[192:195], v[152:155], v[104:107], v[192:195]\n\t"
      "s_or_b64 vcc, vcc, s[48:49]\n\t"
      "s_cbranch_scc1 NP6R_%=\n\t"
      "NP6B_%=:\n\t"
      "ds_write_b128 v243, v[184:187]\n\t"
      "v_mfma_f32_16x16x32_bf16 v[196:199], v[148:151], v[92:95], v[196:199]\n\t"
      "v_mfma_f32_16x16x32_bf16 v[196:199], v[152:155], v[108:111], v[196:199]\n\t"
      "ds_write_b128 v220, v[188:191]\n\t"
      "s_nop 1\n\t"
      "ds_write_b128 v221, v[192:195]\n\t"
      "s_nop 3\n\t"
      "ds_write_b128 v222, v[196:199]\n\t"
      "s_waitcnt lgkmcnt(0)\n\t"
      "s_barrier\n\t"
      "ds_read_b128 v[112:115], v244\n\t"
      "ds_read_b128 v[116:119], v244 offset:4096\n\t"
      "ds_read_b128 v[120:123], v244 offset:8192\n\t"
      "ds_read_b128 v[124:127], v244 offset:12288\n\t"
      "ds_read_b128 v[184:187], v244 offset:16384\n\t"
      "ds_read_b128 v[188:191], v244 offset:20480\n\t"
      "ds_read_b128 v[192:195], v244 offset:24576\n\t"
      "ds_read_b128 v[196:199], v244 offset:28672\n\t"
      "s_waitcnt lgkmcnt(6)\n\t"
      "v_add_f32_e32 v112, v112, v116\n\t"
      "v_add_f32_e32 v113, v113, v117\n\t"
      "v_add_f32_e32 v114, v114, v118\n\t"
      "v_add_f32_e32 v115, v115, v119\n\t"
      "s_waitcnt lgkmcnt(4)\n\t"
      "v_add_f32_e32 v120, v120, v124\n\t"
      "v_add_f32_e32 v121, v121, v125\n\t"
      "v_add_f32_e32 v122, v122, v126\n\t"
      "v_add_f32_e32 v123, v123, v127\n\t"
      "s_waitcnt lgkmcnt(2)\n\t"
      "v_add_f32_e32 v184, v184, v188\n\t"
      "v_add_f32_e32 v185, v185, v189\n\t"
      "v_add_f32_e32 v186, v186, v190\n\t"
      "v_add_f32_e32 v187, v187, v191\n\t"
      "v_add_f32_e32 v112, v112, v120\n\t"
      "v_add_f32_e32 v113, v113, v121\n\t"
      "v_add_f32_e32 v114, v114, v122\n\t"
      "v_add_f32_e32 v115, v115, v123\n\t"
      "s_waitcnt lgkmcnt(0)\n\t"
      "v_add_f32_e32 v192, v192, v196\n\t"
      "v_add_f32_e32 v193, v193, v197\n\t"
      "v_add_f32_e32 v194, v194, v198\n\t"
      "v_add_f32_e32 v195, v195, v199\n\t"
      "v_add_f32_e32 v184, v184, v192\n\t"
      "v_add_f32_e32 v185, v185, v193\n\t"
      "v_add_f32_e32 v186, v186, v194\n\t"
      "v_add_f32_e32 v187, v187, v195\n\t"
      "v_add_f32_e32 v112, v112, v184\n\t"
      "v_add_f32_e32 v113, v113, v185\n\t"
      "v_add_f32_e32 v114, v114, v186\n\t"
      "v_add_f32_e32 v115, v115, v187\n\t"
      "v_cvt_pk_bf16_f32 v116, v112, v113\n\t"
      "v_cvt_pk_bf16_f32 v117, v114, v115\n\t"
      "global_store_dwordx2 v245, v[116:117], s[66:67] offset:64\n\t"
      "GX_%=:\n\t"
      "s_endpgm\n\t"
      ".p2align 6\n\t"
      "GQ_%=:\n\t"
      ".LhopC2:\n\t"
      "s_cbranch_execz .LbackC\n\t"
      "s_waitcnt vmcnt(6)\n\t"
      "v_mfma_f32_16x16x32_bf16 v[184:187], v[128:131], v[0:3], 0\n\t"
      "s_waitcnt vmcnt(5)\n\t"
      "v_cmp_eq_u32_e32 vcc, -1, v132\n\t"
      "v_mfma_f32_16x16x32_bf16 v[188:191], v[128:131], v[4:7], 0\n\t"
      "v_mfma_f32_16x16x32_bf16 v[192:195], v[128:131], v[8:11], 0\n\t"
      "s_cbranch_vccnz FQ1R_%=\n\t"
      "FQ1B_%=:\n\t"
      "v_mfma_f32_16x16x32_bf16 v[196:199], v[128:131], v[12:15], 0\n\t"
      "global_load_dwordx4 v[128:131], v224, s[54:55] sc1\n\t"
      "v_mfma_f32_16x16x32_bf16 v[184:187], v[132:135], v[16:19], v[184:187]\n\t"
      "s_waitcnt vmcnt(5)\n\t"
      "v_cmp_eq_u32_e32 vcc, -1, v136\n\t"
      "v_mfma_f32_16x16x32_bf16 v[188:191], v[132:135], v[20:23], v[188:191]\n\t"
      "v_mfma_f32_16x16x32_bf16 v[192:195], v[132:135], v[24:27], v[192:195]\n\t"
      "s_cbranch_vccnz FQ2R_%=\n\t"
      "FQ2B_%=:\n\t"
      "v_mfma_f32_16x16x32_bf16 v[196:199], v[132:135], v[28:31], v[196:199]\n\t"
      "global_load_dwordx4 v[132:135], v225, s[54:55] sc1\n\t"
      "v_mfma_f32_16x16x32_bf16 v[184:187], v[136:139], v[32:35], v[184:187]\n\t"
      "s_waitcnt vmcnt(5)\n\t"
      "v_cmp_eq_u32_e32 vcc, -1, v140\n\t"
      "v_mfma_f32_16x16x32_bf16 v[188:191], v[136:139], v[36:39], v[188:191]\n\t"
      "v_mfma_f32_16x16x32_bf16 v[192:195], v[136:139], v[40:43], v[192:195]\n\t"
      "s_cbranch_vccnz FQ3R_%=\n\t"
      "FQ3B_%=:\n\t"
      "v_mfma_f32_16x16x32_bf16 v[196:199], v[136:139], v[44:47], v[196:199]\n\t"
      "global_load_dwordx4 v[136:139], v226, s[54:55] sc1\n\t"
      "v_mfma_f32_16x16x32_bf16 v[184:187], v[140:143], v[48:51], v[184:187]\n\t"
      "s_waitcnt vmcnt(5)\n\t"
      "v_cmp_eq_u32_e32 vcc, -1, v144\n\t"
      "v_mfma_f32_16x16x32_bf16 v[188:191], v[140:143], v[52:55], v[188:191]\n\t"
      "v_mfma_f32_16x16x32_bf16 v[192:195], v[140:143], v[56:59], v[192:195]\n\t"
      "s_cbranch_vccnz FQ4R_%=\n\t"
      "FQ4B_%=:\n\t"
      "v_mfma_f32_16x16x32_bf16 v[196:199], v[140:143], v[60:63], v[196:199]\n\t"
      "global_load_dwordx4 v[140:143], v227, s[54:55] sc1\n\t"
      "v_mfma_f32_16x16x32_bf16 v[184:187], v[144:147], v[64:67], v[184:187]\n\t"
      "s_waitcnt vmcnt(5)\n\t"
      "v_cmp_eq_u32_e32 vcc, -1, v148\n\t"
      "v_mfma_f32_16x16x32_bf16 v[188:191], v[144:147], v[68:71], v[188:191]\n\t"
      "v_mfma_f32_16x16x32_bf16 v[192:195], v[144:147], v[72:75], v[192:195]\n\t"
      "s_cbranch_vccnz FQ5R_%=\n\t"
      "FQ5B_%=:\n\t"
      "v_mfma_f32_16x16x32_bf16 v[196:199], v[144:147], v[76:79], v[196:199]\n\t"
      "global_load_dwordx4 v[144:147], v228, s[54:55] sc1\n\t"
      "v_mfma_f32_16x16x32_bf16 v[184:187], v[148:151], v[80:83], v[184:187]\n\t"
      "s_waitcnt vmcnt(5)\n\t"
      "v_cmp_eq_u32_e32 vcc, -1, v152\n\t"
      "v_mfma_f32_16x16x32_bf16 v[188:191], v[148:151], v[84:87], v[188:191]\n\t"
      "v_mfma_f32_16x16x32_bf16 v[192:195], v[148:151], v[88:91], v[192:195]\n\t"
      "s_cbranch_vccnz FQ6R_%=\n\t"
      "FQ6B_%=:\n\t"
      "v_mfma_f32_16x16x32_bf16 v[196:199], v[148:151], v[92:95], v[196:199]\n\t"
      "global_load_dwordx4 v[148:151], v229, s[54:55] sc1\n\t"
      "v_mfma_f32_16x16x32_bf16 v[184:187], v[152:155], v[96:99], v[184:187]\n\t"
      "s_waitcnt vmcnt(5)\n\t"
      "v_cmp_eq_u32_e32 vcc, -1, v128\n\t"
      "v_mfma_f32_16x16x32_bf16 v[188:191], v[152:155], v[100:103], v[188:191]\n\t"
      "v_mfma_f32_16x16x32_bf16 v[192:195], v[152:155], v[104:107], v[192:195]\n\t"
      "s_cbranch_vccnz FQ7R_%=\n\t"
      "FQ7B_%=:\n\t"
      "v_mfma_f32_16x16x32_bf16 v[196:199], v[152:155], v[108:111], v[196:199]\n\t"
      "global_load_dwordx4 v[152:155], v230, s[54:55] sc1\n\t"
      "s_nop 0\n\t"
      "v_cmp_u_f32_e32 vcc, v184, v185\n\t"
      "v_cmp_u_f32_e64 s[48:49], v186, v187\n\t"
      "s_nop 0\n\t"
      "s_or_b64 vcc, vcc, s[48:49]\n\t"
      "s_cbranch_scc1 NQ0R_%=\n\t"
      "NQ0B_%=:\n\t"
      "ds_write_b128 v243, v[184:187]\n\t"
      "ds_write_b128 v220, v[188:191]\n\t"
      "ds_write_b128 v221, v[192:195]\n\t"
      "ds_write_b128 v222, v[196:199]\n\t"
      "v_mfma_f32_16x16x32_bf16 v[200:203], v[128:131], v[0:3], 0\n\t"
      "s_waitcnt vmcnt(5)\n\t"
      "v_cmp_eq_u32_e32 vcc, -1, v132\n\t"
      "v_mfma_f32_16x16x32_bf16 v[204:207], v[128:131], v[4:7], 0\n\t"
      "v_mfma_f32_16x16x32_bf16 v[208:211], v[128:131], v[8:11], 0\n\t"
      "s_cbranch_vccnz FQ8R_%=\n\t"
      "FQ8B_%=:\n\t"
      "v_mfma_f32_16x16x32_bf16 v[212:215], v[128:131], v[12:15], 0\n\t"
      "global_load_dwordx4 v[128:131], v224, s[56:57] sc1\n\t"
      "v_mfma_f32_16x16x32_bf16 v[200:203], v[132:135], v[16:19], v[200:203]\n\t"
      "s_waitcnt vmcnt(5)\n\t"
      "v_cmp_eq_u32_e32 vcc, -1, v136\n\t"
      "v_mfma_f32_16x16x32_bf16 v[204:207], v[132:135], v[20:23], v[204:207]\n\t"
      "v_mfma_f32_16x16x32_bf16 v[208:211], v[132:135], v[24:27], v[208:211]\n\t"
      "s_cbranch_vccnz FQ9R_%=\n\t"
      "FQ9B_%=:\n\t"
      "v_mfma_f32_16x16x32_bf16 v[212:215], v[132:135], v[28:31], v[212:215]\n\t"
      "global_load_dwordx4 v[132:135], v225, s[56:57] sc1\n\t"
      "v_mfma_f32_16x16x32_bf16 v[200:203], v[136:139], v[32:35], v[200:203]\n\t"
      "s_waitcnt vmcnt(5)\n\t"
      "v_cmp_eq_u32_e32 vcc, -1, v140\n\t"
      "v_mfma_f32_16x16x32_bf16 v[204:207], v[136:139], v[36:39], v[204:207]\n\t"
      "v_mfma_f32_16x16x32_bf16 v[208:211], v[136:139], v[40:43], v[208:211]\n\t"
      "s_cbranch_vccnz FQ10R_%=\n\t"
      "FQ10B_%=:\n\t"
      "v_mfma_f32_16x16x32_bf16 v[212:215], v[136:139], v[44:47], v[212:215]\n\t"
      "global_load_dwordx4 v[136:139], v226, s[56:57] sc1\n\t"
      "v_mfma_f32_16x16x32_bf16 v[200:203], v[140:143], v[48:51], v[200:203]\n\t"
      "s_waitcnt vmcnt(5)\n\t"
      "v_cmp_eq_u32_e32 vcc, -1, v144\n\t"
      "v_mfma_f32_16x16x32_bf16 v[204:207], v[140:143], v[52:55], v[204:207]\n\t"
      "v_mfma_f32_16x16x32_bf16 v[208:211], v[140:143], v[56:59], v[208:211]\n\t"
      "s_cbranch_vccnz FQ11R_%=\n\t"
      "FQ11B_%=:\n\t"
      "v_mfma_f32_16x16x32_bf16 v[212:215], v[140:143], v[60:63], v[212:215]\n\t"
      "global_load_dwordx4 v[140:143], v227, s[56:57] sc1\n\t"
      "v_mfma_f32_16x16x32_bf16 v[200:203], v[144:147], v[64:67], v[200:203]\n\t"
      "s_waitcnt vmcnt(5)\n\t"
      "v_cmp_eq_u32_e32 vcc, -1, v148\n\t"
      "v_mfma_f32_16x16x32_bf16 v[204:207], v[144:147], v[68:71], v[204:207]\n\t"
      "v_mfma_f32_16x16x32_bf16 v[208:211], v[144:147], v[72:75], v[208:211]\n\t"
      "s_cbranch_vccnz FQ12R_%=\n\t"
      "FQ12B_%=:\n\t"
      "v_mfma_f32_16x16x32_bf16 v[212:215], v[144:147], v[76:79], v[212:215]\n\t"
      "global_load_dwordx4 v[144:147], v228, s[56:57] sc1\n\t"
      "s_waitcnt lgkmcnt(0)\n\t"
      "s_barrier\n\t"
      "s_setprio 2\n\t"
      "v_mfma_f32_16x16x32_bf16 v[200:203], v[148:151], v[80:83], v[200:203]\n\t"
      "s_waitcnt vmcnt(5)\n\t"
      "v_cmp_eq_u32_e32 vcc, -1, v152\n\t"
      "v_mfma_f32_16x16x32_bf16 v[204:207], v[148:151], v[84:87], v[204:207]\n\t"
      "v_mfma_f32_16x16x32_bf16 v[208:211], v[148:151], v[88:91], v[208:211]\n\t"
      "s_cbranch_vccnz FQ13R_%=\n\t"
      "FQ13B_%=:\n\t"
      "v_mfma_f32_16x16x32_bf16 v[212:215], v[148:151], v[92:95], v[212:215]\n\t"
      "global_load_dwordx4 v[148:151], v229, s[56:57] sc1\n\t"
      "v_mfma_f32_16x16x32_bf16 v[200:203], v[152:155], v[96:99], v[200:203]\n\t"
      "s_waitcnt vmcnt(5)\n\t"
      "v_cmp_eq_u32_e32 vcc, -1, v128\n\t"
      "v_mfma_f32_16x16x32_bf16 v[204:207], v[152:155], v[100:103], v[204:207]\n\t"
      "v_mfma_f32_16x16x32_bf16 v[208:211], v[152:155], v[104:107], v[208:211]\n\t"
      "s_cbranch_vccnz FQ14R_%=\n\t"
      "FQ14B_%=:\n\t"
      "v_mfma_f32_16x16x32_bf16 v[212:215], v[152:155], v[108:111], v[212:215]\n\t"
      "global_load_dwordx4 v[152:155], v230, s[56:57] sc1\n\t"
      "s_setprio 0\n\t"
      "v_cmp_u_f32_e32 vcc, v200, v201\n\t"
      "v_cmp_u_f32_e64 s[48:49], v202, v203\n\t"
      "s_nop 0\n\t"
      "s_or_b64 vcc, vcc, s[48:49]\n\t"
      ".LhopC3:\n\t"
      "s_cbranch_execz .LbackC\n\t"
      "s_cbranch_scc1 NQ1R_%=\n\t"
      "NQ1B_%=:\n\t"
      "ds_write_b128 v243, v[200:203] offset:32768\n\t"
      "ds_write_b128 v220, v[204:207] offset:32768\n\t"
      "ds_write_b128 v221, v[208:211] offset:32768\n\t"
      "ds_write_b128 v222, v[212:215] offset:32768\n\t"
      "v_mfma_f32_16x16x32_bf16 v[184:187], v[128:131], v[0:3], 0\n\t"
      "s_waitcnt vmcnt(5)\n\t"
      "v_cmp_eq_u32_e32 vcc, -1, v132\n\t"
      "v_mfma_f32_16x16x32_bf16 v[188:191], v[128:131], v[4:7], 0\n\t"
      ".LhopC4:\n\t"
      "s_cbranch_execz .LbackC\n\t"
      "v_mfma_f32_16x16x32_bf16 v[192:195], v[128:131], v[8:11], 0\n\t"
      "s_cbranch_vccnz FQ15R_%=\n\t"
      "FQ15B_%=:\n\t"
      "v_mfma_f32_16x16x32_bf16 v[196:199], v[128:131], v[12:15], 0\n\t"
      "global_load_dwordx4 v[128:131], v224, s[58:59] sc1\n\t"
      "v_mfma_f32_16x16x32_bf16 v[184:187], v[132:135], v[16:19], v[184:187]\n\t"
      "s_waitcnt vmcnt(5)\n\t"
      "v_cmp_eq_u32_e32 vcc, -1, v136\n\t"
      "v_mfma_f32_16x16x32_bf16 v[188:191], v[132:135], v[20:23], v[188:191]\n\t"
      "v_mfma_f32_16x16x32_bf16 v[192:195], v[132:135], v[24:27], v[192:195]\n\t"
      "s_cbranch_vccnz FQ16R_%=\n\t"
      "FQ16B_%=:\n\t"
      "v_mfma_f32_16x16x32_bf16 v[196:199], v[132:135], v[28:31], v[196:199]\n\t"
      "global_load_dwordx4 v[132:135], v225, s[58:59] sc1\n\t"
      "v_mfma_f32_16x16x32_bf16 v[184:187], v[136:139], v[32:35], v[184:187]\n\t"
      "s_waitcnt vmcnt(5)\n\t"
      "v_cmp_eq_u32_e32 vcc, -1, v140\n\t"
      "v_mfma_f32_16x16x32_bf16 v[188:191], v[136:139], v[36:39], v[188:191]\n\t"
      "v_mfma_f32_16x16x32_bf16 v[192:195], v[136:139], v[40:43], v[192:195]\n\t"
      "s_cbranch_vccnz FQ17R_%=\n\t"
      "FQ17B_%=:\n\t"
      "v_mfma_f32_16x16x32_bf16 v[196:199], v[136:139], v[44:47], v[196:199]\n\t"
      "global_load_dwordx4 v[136:139], v226, s[58:59] sc1\n\t"
      "v_mfma_f32_16x16x32_bf16 v[184:187], v[140:143], v[48:51], v[184:187]\n\t"
      "s_waitcnt vmcnt(5)\n\t"
      "v_cmp_eq_u32_e32 vcc, -1, v144\n\t"
      "v_mfma_f32_16x16x32_bf16 v[188:191], v[140:143], v[52:55], v[188:191]\n\t"
      "v_mfma_f32_16x16x32_bf16 v[192:195], v[140:143], v[56:59], v[192:195]\n\t"
      "s_cbranch_vccnz FQ18R_%=\n\t"
      "FQ18B_%=:\n\t"
      "v_mfma_f32_16x16x32_bf16 v[196:199], v[140:143], v[60:63], v[196:199]\n\t"
      "global_load_dwordx4 v[140:143], v227, s[58:59] sc1\n\t"
      "v_mfma_f32_16x16x32_bf16 v[184:187], v[144:147], v[64:67], v[184:187]\n\t"
      "s_waitcnt vmcnt(5)\n\t"
      "v_cmp_eq_u32_e32 vcc, -1, v148\n\t"
      "v_mfma_f32_16x16x32_bf16 v[188:191], v[144:147], v[68:71], v[188:191]\n\t"
      "v_mfma_f32_16x16x32_bf16 v[192:195], v[144:147], v[72:75], v[192:195]\n\t"
      "s_cbranch_vccnz FQ19R_%=\n\t"
      "FQ19B_%=:\n\t"
      "v_mfma_f32_16x16x32_bf16 v[196:199], v[144:147], v[76:79], v[196:199]\n\t"
      "global_load_dwordx4 v[144:147], v228, s[58:59] sc1\n\t"
      "s_waitcnt lgkmcnt(0)\n\t"
      "s_barrier\n\t"
      "s_setprio 2\n\t"
      "v_mfma_f32_16x16x32_bf16 v[184:187], v[148:151], v[80:83], v[184:187]\n\t"
      "s_waitcnt vmcnt(5)\n\t"
      "v_cmp_eq_u32_e32 vcc, -1, v152\n\t"
      "v_mfma_f32_16x16x32_bf16 v[188:191], v[148:151], v[84:87], v[188:191]\n\t"
      "v_mfma_f32_16x16x32_bf16 v[192:195], v[148:151], v[88:91], v[192:195]\n\t"
      "s_cbranch_vccnz FQ20R_%=\n\t"
      "FQ20B_%=:\n\t"
      "v_mfma_f32_16x16x32_bf16 v[196:199], v[148:151], v[92:95], v[196:199]\n\t"
      "global_load_dwordx4 v[148:151], v229, s[58:59] sc1\n\t"
      "v_mfma_f32_16x16x32_bf16 v[184:187], v[152:155], v[96:99], v[184:187]\n\t"
      "s_waitcnt vmcnt(5)\n\t"
      "v_cmp_eq_u32_e32 vcc, -1, v128\n\t"
      "v_mfma_f32_16x16x32_bf16 v[188:191], v[152:155], v[100:103], v[188:191]\n\t"
      "v_mfma_f32_16x16x32_bf16 v[192:195], v[152:155], v[104:107], v[192:195]\n\t"
      "s_cbranch_vccnz FQ21R_%=\n\t"
      "FQ21B_%=:\n\t"
      "v_mfma_f32_16x16x32_bf16 v[196:199], v[152:155], v[108:111], v[196:199]\n\t"
      "global_load_dwordx4 v[152:155], v230, s[58:59] sc1\n\t"
      "s_setprio 0\n\t"
      "v_cmp_u_f32_e32 vcc, v184, v185\n\t"
      "v_cmp_u_f32_e64 s[48:49], v186, v187\n\t"
      "s_nop 0\n\t"
      "s_or_b64 vcc, vcc, s[48:49]\n\t"
      "s_cbranch_scc1 NQ2R_%=\n\t"
      "NQ2B_%=:\n\t"
      "ds_write_b128 v243, v[184:187]\n\t"
      "ds_write_b128 v220, v[188:191]\n\t"
      "ds_write_b128 v221, v[192:195]\n\t"
      "ds_write_b128 v222, v[196:199]\n\t"
      "v_mfma_f32_16x16x32_bf16 v[200:203], v[128:131], v[0:3], 0\n\t"
      "s_waitcnt vmcnt(5)\n\t"
      "v_cmp_eq_u32_e32 vcc, -1, v132\n\t"
      "v_mfma_f32_16x16x32_bf16 v[204:207], v[128:131], v[4:7], 0\n\t"
      "v_mfma_f32_16x16x32_bf16 v[208:211], v[128:131], v[8:11], 0\n\t"
      "s_cbranch_vccnz FQ22R_%=\n\t"
      "FQ22B_%=:\n\t"
      "v_mfma_f32_16x16x32_bf16 v[212:215], v[128:131], v[12:15], 0\n\t"
      "global_load_dwordx4 v[128:131], v224, s[60:61] sc1\n\t"
      "v_mfma_f32_16x16x32_bf16 v[200:203], v[132:135], v[16:19], v[200:203]\n\t"
      "s_waitcnt vmcnt(5)\n\t"
      "v_cmp_eq_u32_e32 vcc, -1, v136\n\t"
      "v_mfma_f32_16x16x32_bf16 v[204:207], v[132:135], v[20:23], v[204:207]\n\t"
      "v_mfma_f32_16x16x32_bf16 v[208:211], v[132:135], v[24:27], v[208:211]\n\t"
      "s_cbranch_vccnz FQ23R_%=\n\t"
      "FQ23B_%=:\n\t"
      "v_mfma_f32_16x16x32_bf16 v[212:215], v[132:135], v[28:31], v[212:215]\n\t"
      "global_load_dwordx4 v[132:135], v225, s[60:61] sc1\n\t"
      "v_mfma_f32_16x16x32_bf16 v[200:203], v[136:139], v[32:35], v[200:203]\n\t"
      "s_waitcnt vmcnt(5)\n\t"
      "v_cmp_eq_u32_e32 vcc, -1, v140\n\t"
      "v_mfma_f32_16x16x32_bf16 v[204:207], v[136:139], v[36:39], v[204:207]\n\t"
      "v_mfma_f32_16x16x32_bf16 v[208:211], v[136:139], v[40:43], v[208:211]\n\t"
      "s_cbranch_vccnz FQ24R_%=\n\t"
      "FQ24B_%=:\n\t"
      "v_mfma_f32_16x16x32_bf16 v[212:215], v[136:139], v[44:47], v[212:215]\n\t"
      "global_load_dwordx4 v[136:139], v226, s[60:61] sc1\n\t"
      "v_mfma_f32_16x16x32_bf16 v[200:203], v[140:143], v[48:51], v[200:203]\n\t"
      "s_waitcnt vmcnt(5)\n\t"
      "v_cmp_eq_u32_e32 vcc, -1, v144\n\t"
      "v_mfma_f32_16x16x32_bf16 v[204:207], v[140:143], v[52:55], v[204:207]\n\t"
      "v_mfma_f32_16x16x32_bf16 v[208:211], v[140:143], v[56:59], v[208:211]\n\t"
      "s_cbranch_vccnz FQ25R_%=\n\t"
      "FQ25B_%=:\n\t"
      "v_mfma_f32_16x16x32_bf16 v[212:215], v[140:143], v[60:63], v[212:215]\n\t"
      "global_load_dwordx4 v[140:143], v227, s[60:61] sc1\n\t"
      "v_mfma_f32_16x16x32_bf16 v[200:203], v[144:147], v[64:67], v[200:203]\n\t"
      "s_waitcnt vmcnt(5)\n\t"
      "v_cmp_eq_u32_e32 vcc, -1, v148\n\t"
      "v_mfma_f32_16x16x32_bf16 v[204:207], v[144:147], v[68:71], v[204:207]\n\t"
      "v_mfma_f32_16x16x32_bf16 v[208:211], v[144:147], v[72:75], v[208:211]\n\t"
      "s_cbranch_vccnz FQ26R_%=\n\t"
      "FQ26B_%=:\n\t"
      "v_mfma_f32_16x16x32_bf16 v[212:215], v[144:147], v[76:79], v[212:215]\n\t"
      "global_load_dwordx4 v[144:147], v228, s[60:61] sc1\n\t"
      "s_waitcnt lgkmcnt(0)\n\t"
      "s_barrier\n\t"
      "s_setprio 2\n\t"
      "v_mfma_f32_16x16x32_bf16 v[200:203], v[148:151], v[80:83], v[200:203]\n\t"
      "s_waitcnt vmcnt(5)\n\t"
      "v_cmp_eq_u32_e32 vcc, -1, v152\n\t"
      "v_mfma_f32_16x16x32_bf16 v[204:207], v[148:151], v[84:87], v[204:207]\n\t"
      "v_mfma_f32_16x16x32_bf16 v[208:211], v[148:151], v[88:91], v[208:211]\n\t"
      "s_cbranch_vccnz FQ27R_%=\n\t"
      "FQ27B_%=:\n\t"
      "v_mfma_f32_16x16x32_bf16 v[212:215], v[148:151], v[92:95], v[212:215]\n\t"
      "global_load_dwordx4 v[148:151], v229, s[60:61] sc1\n\t"
      "v_mfma_f32_16x16x32_bf16 v[200:203], v[152:155], v[96:99], v[200:203]\n\t"
      "s_waitcnt vmcnt(5)\n\t"
      "v_cmp_eq_u32_e32 vcc, -1, v128\n\t"
      "v_mfma_f32_16x16x32_bf16 v[204:207], v[152:155], v[100:103], v[204:207]\n\t"
      "v_mfma_f32_16x16x32_bf16 v[208:211], v[152:155], v[104:107], v[208:211]\n\t"
      "s_cbranch_vccnz FQ28R_%=\n\t"
      "FQ28B_%=:\n\t"
      "v_mfma_f32_16x16x32_bf16 v[212:215], v[152:155], v[108:111], v[212:215]\n\t"
      "global_load_dwordx4 v[152:155], v230, s[60:61] sc1\n\t"
      "s_setprio 0\n\t"
      "v_cmp_u_f32_e32 vcc, v200, v201\n\t"
      "v_cmp_u_f32_e64 s[48:49], v202, v203\n\t"
      "s_nop 0\n\t"
      "s_or_b64 vcc, vcc, s[48:49]\n\t"
      "s_cbranch_scc1 NQ3R_%=\n\t"
      "NQ3B_%=:\n\t"
      "ds_write_b128 v243, v[200:203] offset:32768\n\t"
      "ds_write_b128 v220, v[204:207] offset:32768\n\t"
      "ds_write_b128 v221, v[208:211] offset:32768\n\t"
      "ds_write_b128 v222, v[212:215] offset:32768\n\t"
      "v_mfma_f32_16x16x32_bf16 v[184:187], v[128:131], v[0:3], 0\n\t"
      "s_waitcnt vmcnt(5)\n\t"
      "v_cmp_eq_u32_e32 vcc, -1, v132\n\t"
      "v_mfma_f32_16x16x32_bf16 v[188:191], v[128:131], v[4:7], 0\n\t"
      "v_mfma_f32_16x16x32_bf16 v[192:195], v[128:131], v[8:11], 0\n\t"
      "s_cbranch_vccnz FQ29R_%=\n\t"
      "FQ29B_%=:\n\t"
      "v_mfma_f32_16x16x32_bf16 v[196:199], v[128:131], v[12:15], 0\n\t"
      "global_load_dwordx4 v[128:131], v224, s[62:63] sc1\n\t"
      "v_mfma_f32_16x16x32_bf16 v[184:187], v[132:135], v[16:19], v[184:187]\n\t"
      "s_waitcnt vmcnt(5)\n\t"
      "v_cmp_eq_u32_e32 vcc, -1, v136\n\t"
      "v_mfma_f32_16x16x32_bf16 v[188:191], v[132:135], v[20:23], v[188:191]\n\t"
      "v_mfma_f32_16x16x32_bf16 v[192:195], v[132:135], v[24:27], v[192:195]\n\t"
      "s_cbranch_vccnz FQ30R_%=\n\t"
      "FQ30B_%=:\n\t"
      "v_mfma_f32_16x16x32_bf16 v[196:199], v[132:135], v[28:31], v[196:199]\n\t"
      "global_load_dwordx4 v[132:135], v225, s[62:63] sc1\n\t"
      "v_mfma_f32_16x16x32_bf16 v[184:187], v[136:139], v[32:35], v[184:187]\n\t"
      "s_waitcnt vmcnt(5)\n\t"
      "v_cmp_eq_u32_e32 vcc, -1, v140\n\t"
      "v_mfma_f32_16x16x32_bf16 v[188:191], v[136:139], v[36:39], v[188:191]\n\t"
      "v_mfma_f32_16x16x32_bf16 v[192:195], v[136:139], v[40:43], v[192:195]\n\t"
      "s_cbranch_vccnz FQ31R_%=\n\t"
      "FQ31B_%=:\n\t"
      "v_mfma_f32_16x16x32_bf16 v[196:199], v[136:139], v[44:47], v[196:199]\n\t"
      "global_load_dwordx4 v[136:139], v226, s[62:63] sc1\n\t"
      "v_mfma_f32_16x16x32_bf16 v[184:187], v[140:143], v[48:51], v[184:187]\n\t"
      "s_waitcnt vmcnt(5)\n\t"
      "v_cmp_eq_u32_e32 vcc, -1, v144\n\t"
      "v_mfma_f32_16x16x32_bf16 v[188:191], v[140:143], v[52:55], v[188:191]\n\t"
      "v_mfma_f32_16x16x32_bf16 v[192:195], v[140:143], v[56:59], v[192:195]\n\t"
      "s_cbranch_vccnz FQ32R_%=\n\t"
      "FQ32B_%=:\n\t"
      "v_mfma_f32_16x16x32_bf16 v[196:199], v[140:143], v[60:63], v[196:199]\n\t"
      "global_load_dwordx4 v[140:143], v227, s[62:63] sc1\n\t"
      "v_mfma_f32_16x16x32_bf16 v[184:187], v[144:147], v[64:67], v[184:187]\n\t"
      "s_waitcnt vmcnt(5)\n\t"
      "v_cmp_eq_u32_e32 vcc, -1, v148\n\t"
      "v_mfma_f32_16x16x32_bf16 v[188:191], v[144:147], v[68:71], v[188:191]\n\t"
      "v_mfma_f32_16x16x32_bf16 v[192:195], v[144:147], v[72:75], v[192:195]\n\t"
      "s_cbranch_vccnz FQ33R_%=\n\t"
      "FQ33B_%=:\n\t"
      "v_mfma_f32_16x16x32_bf16 v[196:199], v[144:147], v[76:79], v[196:199]\n\t"
      "global_load_dwordx4 v[144:147], v228, s[62:63] sc1\n\t"
      "s_waitcnt lgkmcnt(0)\n\t"
      "s_barrier\n\t"
      "s_setprio 2\n\t"
      "v_mfma_f32_16x16x32_bf16 v[184:187], v[148:151], v[80:83], v[184:187]\n\t"
      "s_waitcnt vmcnt(5)\n\t"
      "v_cmp_eq_u32_e32 vcc, -1, v152\n\t"
      "v_mfma_f32_16x16x32_bf16 v[188:191], v[148:151], v[84:87], v[188:191]\n\t"
      "v_mfma_f32_16x16x32_bf16 v[192:195], v[148:151], v[88:91], v[192:195]\n\t"
      "s_cbranch_vccnz FQ34R_%=\n\t"
      "FQ34B_%=:\n\t"
      "v_mfma_f32_16x16x32_bf16 v[196:199], v[148:151], v[92:95], v[196:199]\n\t"
      "global_load_dwordx4 v[148:151], v229, s[62:63] sc1\n\t"
      "v_mfma_f32_16x16x32_bf16 v[184:187], v[152:155], v[96:99], v[184:187]\n\t"
      "s_waitcnt vmcnt(5)\n\t"
      "v_cmp_eq_u32_e32 vcc, -1, v128\n\t"
      "v_mfma_f32_16x16x32_bf16 v[188:191], v[152:155], v[100:103], v[188:191]\n\t"
      "v_mfma_f32_16x16x32_bf16 v[192:195], v[152:155], v[104:107], v[192:195]\n\t"
      "s_cbranch_vccnz FQ35R_%=\n\t"
      "FQ35B_%=:\n\t"
      "v_mfma_f32_16x16x32_bf16 v[196:199], v[152:155], v[108:111], v[196:199]\n\t"
      "global_load_dwordx4 v[152:155], v230, s[62:63] sc1\n\t"
      "s_setprio 0\n\t"
      "v_cmp_u_f32_e32 vcc, v184, v185\n\t"
      "v_cmp_u_f32_e64 s[48:49], v186, v187\n\t"
      "s_nop 0\n\t"
      "s_or_b64 vcc, vcc, s[48:49]\n\t"
      "s_cbranch_scc1 NQ4R_%=\n\t"
      "NQ4B_%=:\n\t"
      "ds_write_b128 v243, v[184:187]\n\t"
      "ds_write_b128 v220, v[188:191]\n\t"
      "ds_write_b128 v221, v[192:195]\n\t"
      "ds_write_b128 v222, v[196:199]\n\t"
      "v_mfma_f32_16x16x32_bf16 v[200:203], v[128:131], v[0:3], 0\n\t"
      "s_waitcnt vmcnt(5)\n\t"
      "v_cmp_eq_u32_e32 vcc, -1, v132\n\t"
      "v_mfma_f32_16x16x32_bf16 v[204:207], v[128:131], v[4:7], 0\n\t"
      "v_mfma_f32_16x16x32_bf16 v[208:211], v[128:131], v[8:11], 0\n\t"
      "s_cbranch_vccnz FQ36R_%=\n\t"
      "FQ36B_%=:\n\t"
      "v_mfma_f32_16x16x32_bf16 v[212:215], v[128:131], v[12:15], 0\n\t"
      "global_load_dwordx4 v[128:131], v224, s[64:65] sc1\n\t"
      "v_mfma_f32_16x16x32_bf16 v[200:203], v[132:135], v[16:19], v[200:203]\n\t"
      "s_waitcnt vmcnt(5)\n\t"
      "v_cmp_eq_u32_e32 vcc, -1, v136\n\t"
      "v_mfma_f32_16x16x32_bf16 v[204:207], v[132:135], v[20:23], v[204:207]\n\t"
      "v_mfma_f32_16x16x32_bf16 v[208:211], v[132:135], v[24:27], v[208:211]\n\t"
      "s_cbranch_vccnz FQ37R_%=\n\t"
      "FQ37B_%=:\n\t"
      "v_mfma_f32_16x16x32_bf16 v[212:215], v[132:135], v[28:31], v[212:215]\n\t"
      "global_load_dwordx4 v[132:135], v225, s[64:65] sc1\n\t"
      "v_mfma_f32_16x16x32_bf16 v[200:203], v[136:139], v[32:35], v[200:203]\n\t"
      "s_waitcnt vmcnt(5)\n\t"
      "v_cmp_eq_u32_e32 vcc, -1, v140\n\t"
      "v_mfma_f32_16x16x32_bf16 v[204:207], v[136:139], v[36:39], v[204:207]\n\t"
      "v_mfma_f32_16x16x32_bf16 v[208:211], v[136:139], v[40:43], v[208:211]\n\t"
      "s_cbranch_vccnz FQ38R_%=\n\t"
      "FQ38B_%=:\n\t"
      "v_mfma_f32_16x16x32_bf16 v[212:215], v[136:139], v[44:47], v[212:215]\n\t"
      "global_load_dwordx4 v[136:139], v226, s[64:65] sc1\n\t"
      "v_mfma_f32_16x16x32_bf16 v[200:203], v[140:143], v[48:51], v[200:203]\n\t"
      "s_waitcnt vmcnt(5)\n\t"
      "v_cmp_eq_u32_e32 vcc, -1, v144\n\t"
      "v_mfma_f32_16x16x32_bf16 v[204:207], v[140:143], v[52:55], v[204:207]\n\t"
      "v_mfma_f32_16x16x32_bf16 v[208:211], v[140:143], v[56:59], v[208:211]\n\t"
      "s_cbranch_vccnz FQ39R_%=\n\t"
      "FQ39B_%=:\n\t"
      "v_mfma_f32_16x16x32_bf16 v[212:215], v[140:143], v[60:63], v[212:215]\n\t"
      "global_load_dwordx4 v[140:143], v227, s[64:65] sc1\n\t"
      "v_mfma_f32_16x16x32_bf16 v[200:203], v[144:147], v[64:67], v[200:203]\n\t"
      "s_waitcnt vmcnt(5)\n\t"
      "v_cmp_eq_u32_e32 vcc, -1, v148\n\t"
      "v_mfma_f32_16x16x32_bf16 v[204:207], v[144:147], v[68:71], v[204:207]\n\t"
      "v_mfma_f32_16x16x32_bf16 v[208:211], v[144:147], v[72:75], v[208:211]\n\t"
      "s_cbranch_vccnz FQ40R_%=\n\t"
      "FQ40B_%=:\n\t"
      "v_mfma_f32_16x16x32_bf16 v[212:215], v[144:147], v[76:79], v[212:215]\n\t"
      "global_load_dwordx4 v[144:147], v228, s[64:65] sc1\n\t"
      "s_waitcnt lgkmcnt(0)\n\t"
      "s_barrier\n\t"
      "s_setprio 2\n\t"
      "v_mfma_f32_16x16x32_bf16 v[200:203], v[148:151], v[80:83], v[200:203]\n\t"
      "s_waitcnt vmcnt(5)\n\t"
      "v_cmp_eq_u32_e32 vcc, -1, v152\n\t"
      "v_mfma_f32_16x16x32_bf16 v[204:207], v[148:151], v[84:87], v[204:207]\n\t"
      "v_mfma_f32_16x16x32_bf16 v[208:211], v[148:151], v[88:91], v[208:211]\n\t"
      "s_cbranch_vccnz FQ41R_%=\n\t"
      "FQ41B_%=:\n\t"
      "v_mfma_f32_16x16x32_bf16 v[212:215], v[148:151], v[92:95], v[212:215]\n\t"
      "global_load_dwordx4 v[148:151], v229, s[64:65] sc1\n\t"
      "v_mfma_f32_16x16x32_bf16 v[200:203], v[152:155], v[96:99], v[200:203]\n\t"
      "s_waitcnt vmcnt(5)\n\t"
      "v_cmp_eq_u32_e32 vcc, -1, v128\n\t"
      "v_mfma_f32_16x16x32_bf16 v[204:207], v[152:155], v[100:103], v[204:207]\n\t"
      "v_mfma_f32_16x16x32_bf16 v[208:211], v[152:155], v[104:107], v[208:211]\n\t"
      "s_cbranch_vccnz FQ42R_%=\n\t"
      "FQ42B_%=:\n\t"
      "v_mfma_f32_16x16x32_bf16 v[212:215], v[152:155], v[108:111], v[212:215]\n\t"
      "global_load_dwordx4 v[152:155], v230, s[64:65] sc1\n\t"
      "s_setprio 0\n\t"
      "v_cmp_u_f32_e32 vcc, v200, v201\n\t"
      "v_cmp_u_f32_e64 s[48:49], v202, v203\n\t"
      "s_nop 0\n\t"
      "s_or_b64 vcc, vcc, s[48:49]\n\t"
      "s_cbranch_scc1 NQ5R_%=\n\t"
      "NQ5B_%=:\n\t"
      "ds_write_b128 v243, v[200:203] offset:32768\n\t"
      "ds_write_b128 v220, v[204:207] offset:32768\n\t"
      "ds_write_b128 v221, v[208:211] offset:32768\n\t"
      "ds_write_b128 v222, v[212:215] offset:32768\n\t"
      "v_mfma_f32_16x16x32_bf16 v[184:187], v[128:131], v[0:3], 0\n\t"
      "s_waitcnt vmcnt(5)\n\t"
      "v_cmp_eq_u32_e32 vcc, -1, v132\n\t"
      "v_mfma_f32_16x16x32_bf16 v[188:191], v[128:131], v[4:7], 0\n\t"
      "v_mfma_f32_16x16x32_bf16 v[192:195], v[128:131], v[8:11], 0\n\t"
      "s_cbranch_vccnz FQ43R_%=\n\t"
      "FQ43B_%=:\n\t"
      "v_mfma_f32_16x16x32_bf16 v[196:199], v[128:131], v[12:15], 0\n\t"
      "v_mfma_f32_16x16x32_bf16 v[184:187], v[132:135], v[16:19], v[184:187]\n\t"
      "s_waitcnt vmcnt(4)\n\t"
      "v_cmp_eq_u32_e32 vcc, -1, v136\n\t"
      "v_mfma_f32_16x16x32_bf16 v[188:191], v[132:135], v[20:23], v[188:191]\n\t"
      "v_mfma_f32_16x16x32_bf16 v[192:195], v[132:135], v[24:27], v[192:195]\n\t"
      "s_cbranch_vccnz FQ44R_%=\n\t"
      "FQ44B_%=:\n\t"
      "v_mfma_f32_16x16x32_bf16 v[196:199], v[132:135], v[28:31], v[196:199]\n\t"
      "v_mfma_f32_16x16x32_bf16 v[184:187], v[136:139], v[32:35], v[184:187]\n\t"
      "s_waitcnt vmcnt(3)\n\t"
      "v_cmp_eq_u32_e32 vcc, -1, v140\n\t"
      "v_mfma_f32_16x16x32_bf16 v[188:191], v[136:139], v[36:39], v[188:191]\n\t"
      "v_mfma_f32_16x16x32_bf16 v[192:195], v[136:139], v[40:43], v[192:195]\n\t"
      "s_cbranch_vccnz FQ45R_%=\n\t"
      "FQ45B_%=:\n\t"
      "v_mfma_f32_16x16x32_bf16 v[196:199], v[136:139], v[44:47], v[196:199]\n\t"
      "v_mfma_f32_16x16x32_bf16 v[184:187], v[140:143], v[48:51], v[184:187]\n\t"
      "s_waitcnt vmcnt(2)\n\t"
      "v_cmp_eq_u32_e32 vcc, -1, v144\n\t"
      "v_mfma_f32_16x16x32_bf16 v[188:191], v[140:143], v[52:55], v[188:191]\n\t"
      "v_mfma_f32_16x16x32_bf16 v[192:195], v[140:143], v[56:59], v[192:195]\n\t"
      "s_cbranch_vccnz FQ46R_%=\n\t"
      "FQ46B_%=:\n\t"
      "v_mfma_f32_16x16x32_bf16 v[196:199], v[140:143], v[60:63], v[196:199]\n\t"
      "v_mfma_f32_16x16x32_bf16 v[184:187], v[144:147], v[64:67], v[184:187]\n\t"
      "s_waitcnt vmcnt(1)\n\t"
      "v_cmp_eq_u32_e32 vcc, -1, v148\n\t"
      "v_mfma_f32_16x16x32_bf16 v[188:191], v[144:147], v[68:71], v[188:191]\n\t"
      "v_mfma_f32_16x16x32_bf16 v[192:195], v[144:147], v[72:75], v[192:195]\n\t"
      "s_cbranch_vccnz FQ47R_%=\n\t"
      "FQ47B_%=:\n\t"
      "v_mfma_f32_16x16x32_bf16 v[196:199], v[144:147], v[76:79], v[196:199]\n\t"
      "s_waitcnt lgkmcnt(0)\n\t"
      "s_barrier\n\t"
      "s_setprio 2\n\t"
      "v_mfma_f32_16x16x32_bf16 v[184:187], v[148:151], v[80:83], v[184:187]\n\t"
      "s_waitcnt vmcnt(0)\n\t"
      "v_cmp_eq_u32_e32 vcc, -1, v152\n\t"
      "v_mfma_f32_16x16x32_bf16 v[188:191], v[148:151], v[84:87], v[188:191]\n\t"
      "v_mfma_f32_16x16x32_bf16 v[192:195], v[148:151], v[88:91], v[192:195]\n\t"
      "s_cbranch_vccnz FQ48R_%=\n\t"
      "FQ48B_%=:\n\t"
      "v_mfma_f32_16x16x32_bf16 v[196:199], v[148:151], v[92:95], v[196:199]\n\t"
      "v_mfma_f32_16x16x32_bf16 v[184:187], v[152:155], v[96:99], v[184:187]\n\t"
      "v_mfma_f32_16x16x32_bf16 v[188:191], v[152:155], v[100:103], v[188:191]\n\t"
      "v_mfma_f32_16x16x32_bf16 v[192:195], v[152:155], v[104:107], v[192:195]\n\t"
      "v_mfma_f32_16x16x32_bf16 v[196:199], v[152:155], v[108:111], v[196:199]\n\t"
      "s_nop 4\n\t"
      "v_cmp_u_f32_e32 vcc, v184, v185\n\t"
      "v_cmp_u_f32_e64 s[48:49], v186, v187\n\t"
      "s_nop 0\n\t"
      "s_or_b64 vcc, vcc, s[48:49]\n\t"
      "s_cbranch_scc1 NQ6R_%=\n\t"
      "NQ6B_%=:\n\t"
      "ds_write_b128 v243, v[184:187]\n\t"
      "ds_write_b128 v220, v[188:191]\n\t"
      "ds_write_b128 v221, v[192:195]\n\t"
      "ds_write_b128 v222, v[196:199]\n\t"
      "s_waitcnt lgkmcnt(0)\n\t"
      "s_barrier\n\t"
      "s_branch GX_%=\n\t"
      "HWR_%=:\n\t"
      "s_mov_b32 s51, 0\n\t"
      "HWL_%=:\n\t"
      "s_add_u32 s51, s51, 1\n\t"
      "s_cmp_lt_u32 s51, 262144\n\t"
      "s_cbranch_scc0 HWX_%=\n\t"
      "s_sleep 2\n\t"
      "global_load_dwordx4 v[112:115], v116, s[12:13] sc1\n\t"
      "s_waitcnt vmcnt(0)\n\t"
      "v_xor_b32_e64 v114, v114, v112\n\t"
      "v_xor_b32_e32 v115, v115, v113\n\t"
      "v_cmp_ne_u64_e32 vcc, s[14:15], v[114:115]\n\t"
      "s_cbranch_vccnz HWL_%=\n\t"
      "HWX_%=:\n\t"
      "s_branch HWB_%=\n\t"
      "HXR_%=:\n\t"
      "s_mov_b32 s51, 0\n\t"
      "HXL_%=:\n\t"
      "s_add_u32 s51, s51, 1\n\t"
      "s_cmp_lt_u32 s51, 262144\n\t"
      "s_cbranch_scc0 HXX_%=\n\t"
      "s_sleep 2\n\t"
      "s_mov_b32 exec_hi, s23\n\t"
      "s_not_b32 exec_lo, s23\n\t"
      "s_add_u32 s68, s20, 0\n\t"
      "s_addc_u32 s69, s21, 0\n\t"
      "global_load_dwordx4 v[184:187], v240, s[68:69] sc1\n\t"
      "s_add_u32 s68, s20, 131072\n\t"
      "s_addc_u32 s69, s21, 0\n\t"
      "global_load_dwordx4 v[188:191], v240, s[68:69] sc1\n\t"
      "s_add_u32 s68, s20, 262144\n\t"
      "s_addc_u32 s69, s21, 0\n\t"
      "global_load_dwordx4 v[192:195], v240, s[68:69] sc1\n\t"
      "s_add_u32 s68, s20, 393216\n\t"
      "s_addc_u32 s69, s21, 0\n\t"
      "global_load_dwordx4 v[196:199], v240, s[68:69] sc1\n\t"
      "s_add_u32 s68, s20, 524288\n\t"
      "s_addc_u32 s69, s21, 0\n\t"
      "global_load_dwordx4 v[200:203], v240, s[68:69] sc1\n\t"
      "s_add_u32 s68, s20, 655360\n\t"
      "s_addc_u32 s69, s21, 0\n\t"
      "global_load_dwordx4 v[204:207], v240, s[68:69] sc1\n\t"
      "s_add_u32 s68, s20, 786432\n\t"
      "s_addc_u32 s69, s21, 0\n\t"
      "global_load_dwordx4 v[208:211], v240, s[68:69] sc1\n\t"
      "s_mov_b64 exec, -1\n\t"
      "s_waitcnt vmcnt(0)\n\t"
      "v_max3_u32 v234, v184, v185, v186\n\t"
      "v_max3_u32 v234, v234, v187, v188\n\t"
      "v_max3_u32 v234, v234, v189, v190\n\t"
      "v_max3_u32 v234, v234, v191, v192\n\t"
      "v_max3_u32 v234, v234, v193, v194\n\t"
      "v_max3_u32 v234, v234, v195, v196\n\t"
      "v_max3_u32 v234, v234, v197, v198\n\t"
      "v_max3_u32 v234, v234, v199, v200\n\t"
      "v_max3_u32 v234, v234, v201, v202\n\t"
      "v_max3_u32 v234, v234, v203, v204\n\t"
      "v_max3_u32 v234, v234, v205, v206\n\t"
      "v_max3_u32 v234, v234, v207, v208\n\t"
      "v_max3_u32 v234, v234, v209, v210\n\t"
      "v_max_u32_e32 v234, v234, v211\n\t"
      "v_cmp_eq_u32_e32 vcc, -1, v234\n\t"
      "s_cbranch_vccnz HXL_%=\n\t"
      "HXX_%=:\n\t"
      "s_nop 1\n\t"
      "s_branch HXB_%=\n\t"
      "FQ1R_%=:\n\t"
      "s_mov_b32 s51, 0\n\t"
      "FQ1L_%=:\n\t"
      "s_add_u32 s51, s51, 1\n\t"
      "s_cmp_lt_u32 s51, 262144\n\t"
      "s_cbranch_scc0 FQ1X_%=\n\t"
      "s_waitcnt vmcnt(0)\n\t"
      "s_sleep 1\n\t"
      "global_load_dwordx4 v[132:135], v225, s[52:53] sc1\n\t"
      "global_load_dwordx4 v[136:139], v226, s[52:53] sc1\n\t"
      "global_load_dwordx4 v[140:143], v227, s[52:53] sc1\n\t"
      "global_load_dwordx4 v[144:147], v228, s[52:53] sc1\n\t"
      "global_load_dwordx4 v[148:151], v229, s[52:53] sc1\n\t"
      "global_load_dwordx4 v[152:155], v230, s[52:53] sc1\n\t"
      "s_waitcnt vmcnt(5)\n\t"
      "v_max3_u32 v234, v132, v133, v134\n\t"
      "v_max_u32_e32 v234, v234, v135\n\t"
      "v_cmp_eq_u32_e32 vcc, -1, v234\n\t"
      "s_cbranch_vccnz FQ1L_%=\n\t"
      "FQ1X_%=:\n\t"
      "s_branch FQ1B_%=\n\t"
      "FQ2R_%=:\n\t"
      "s_mov_b32 s51, 0\n\t"
      "FQ2L_%=:\n\t"
      "s_add_u32 s51, s51, 1\n\t"
      "s_cmp_lt_u32 s51, 262144\n\t"
      "s_cbranch_scc0 FQ2X_%=\n\t"
      "s_waitcnt vmcnt(0)\n\t"
      "s_sleep 1\n\t"
      "global_load_dwordx4 v[136:139], v226, s[52:53] sc1\n\t"
      "global_load_dwordx4 v[140:143], v227, s[52:53] sc1\n\t"
      "global_load_dwordx4 v[144:147], v228, s[52:53] sc1\n\t"
      "global_load_dwordx4 v[148:151], v229, s[52:53] sc1\n\t"
      "global_load_dwordx4 v[152:155], v230, s[52:53] sc1\n\t"
      "global_load_dwordx4 v[128:131], v224, s[54:55] sc1\n\t"
      "s_waitcnt vmcnt(5)\n\t"
      "v_max3_u32 v234, v136, v137, v138\n\t"
      "v_max_u32_e32 v234, v234, v139\n\t"
      "v_cmp_eq_u32_e32 vcc, -1, v234\n\t"
      "s_cbranch_vccnz FQ2L_%=\n\t"
      "FQ2X_%=:\n\t"
      "s_branch FQ2B_%=\n\t"
      "FQ3R_%=:\n\t"
      "s_mov_b32 s51, 0\n\t"
      "FQ3L_%=:\n\t"
      "s_add_u32 s51, s51, 1\n\t"
      "s_cmp_lt_u32 s51, 262144\n\t"
      "s_cbranch_scc0 FQ3X_%=\n\t"
      "s_waitcnt vmcnt(0)\n\t"
      "s_sleep 1\n\t"
      "global_load_dwordx4 v[140:143], v227, s[52:53] sc1\n\t"
      "global_load_dwordx4 v[144:147], v228, s[52:53] sc1\n\t"
      "global_load_dwordx4 v[148:151], v229, s[52:53] sc1\n\t"
      "global_load_dwordx4 v[152:155], v230, s[52:53] sc1\n\t"
      "global_load_dwordx4 v[128:131], v224, s[54:55] sc1\n\t"
      "global_load_dwordx4 v[132:135], v225, s[54:55] sc1\n\t"
      "s_waitcnt vmcnt(5)\n\t"
      "v_max3_u32 v234, v140, v141, v142\n\t"
      "v_max_u32_e32 v234, v234, v143\n\t"
      "v_cmp_eq_u32_e32 vcc, -1, v234\n\t"
      "s_cbranch_vccnz FQ3L_%=\n\t"
      "FQ3X_%=:\n\t"
      "s_branch FQ3B_%=\n\t"
      "FQ4R_%=:\n\t"
      "s_mov_b32 s51, 0\n\t"
      "FQ4L_%=:\n\t"
      "s_add_u32 s51, s51, 1\n\t"
      "s_cmp_lt_u32 s51, 262144\n\t"
      "s_cbranch_scc0 FQ4X_%=\n\t"
      "s_waitcnt vmcnt(0)\n\t"
      "s_sleep 1\n\t"
      "global_load_dwordx4 v[144:147], v228, s[52:53] sc1\n\t"
      "global_load_dwordx4 v[148:151], v229, s[52:53] sc1\n\t"
      "global_load_dwordx4 v[152:155], v230, s[52:53] sc1\n\t"
      "global_load_dwordx4 v[128:131], v224, s[54:55] sc1\n\t"
      "global_load_dwordx4 v[132:135], v225, s[54:55] sc1\n\t"
      "global_load_dwordx4 v[136:139], v226, s[54:55] sc1\n\t"
      "s_waitcnt vmcnt(5)\n\t"
      "v_max3_u32 v234, v144, v145, v146\n\t"
      "v_max_u32_e32 v234, v234, v147\n\t"
      "v_cmp_eq_u32_e32 vcc, -1, v234\n\t"
      "s_cbranch_vccnz FQ4L_%=\n\t"
      "FQ4X_%=:\n\t"
      "s_branch FQ4B_%=\n\t"
      "FQ5R_%=:\n\t"
      "s_mov_b32 s51, 0\n\t"
      "FQ5L_%=:\n\t"
      "s_add_u32 s51, s51, 1\n\t"
      "s_cmp_lt_u32 s51, 262144\n\t"
      "s_cbranch_scc0 FQ5X_%=\n\t"
      "s_waitcnt vmcnt(0)\n\t"
      "s_sleep 1\n\t"
      "global_load_dwordx4 v[148:151], v229, s[52:53] sc1\n\t"
      "global_load_dwordx4 v[152:155], v230, s[52:53] sc1\n\t"
      "global_load_dwordx4 v[128:131], v224, s[54:55] sc1\n\t"
      "global_load_dwordx4 v[132:135], v225, s[54:55] sc1\n\t"
      "global_load_dwordx4 v[136:139], v226, s[54:55] sc1\n\t"
      "global_load_dwordx4 v[140:143], v227, s[54:55] sc1\n\t"
      "s_waitcnt vmcnt(5)\n\t"
      "v_max3_u32 v234, v148, v149, v150\n\t"
      "v_max_u32_e32 v234, v234, v151\n\t"
      "v_cmp_eq_u32_e32 vcc, -1, v234\n\t"
      "s_cbranch_vccnz FQ5L_%=\n\t"
      "FQ5X_%=:\n\t"
      "s_branch FQ5B_%=\n\t"
      "FQ6R_%=:\n\t"
      "s_mov_b32 s51, 0\n\t"
      "FQ6L_%=:\n\t"
      "s_add_u32 s51, s51, 1\n\t"
      "s_cmp_lt_u32 s51, 262144\n\t"
      "s_cbranch_scc0 FQ6X_%=\n\t"
      "s_waitcnt vmcnt(0)\n\t"
      "s_sleep 1\n\t"
      "global_load_dwordx4 v[152:155], v230, s[52:53] sc1\n\t"
      "global_load_dwordx4 v[128:131], v224, s[54:55] sc1\n\t"
      "global_load_dwordx4 v[132:135], v225, s[54:55] sc1\n\t"
      "global_load_dwordx4 v[136:139], v226, s[54:55] sc1\n\t"
      "global_load_dwordx4 v[140:143], v227, s[54:55] sc1\n\t"
      "global_load_dwordx4 v[144:147], v228, s[54:55] sc1\n\t"
      "s_waitcnt vmcnt(5)\n\t"
      "v_max3_u32 v234, v152, v153, v154\n\t"
      "v_max_u32_e32 v234, v234, v155\n\t"
      "v_cmp_eq_u32_e32 vcc, -1, v234\n\t"
      "s_cbranch_vccnz FQ6L_%=\n\t"
      "FQ6X_%=:\n\t"
      "s_branch FQ6B_%=\n\t"
      "FQ7R_%=:\n\t"
      "s_mov_b32 s51, 0\n\t"
      "FQ7L_%=:\n\t"
      "s_add_u32 s51, s51, 1\n\t"
      "s_cmp_lt_u32 s51, 262144\n\t"
      "s_cbranch_scc0 FQ7X_%=\n\t"
      "s_waitcnt vmcnt(0)\n\t"
      "s_sleep 4\n\t"
      "global_load_dwordx4 v[128:131], v224, s[54:55] sc1\n\t"
      "global_load_dwordx4 v[132:135], v225, s[54:55] sc1\n\t"
      "global_load_dwordx4 v[136:139], v226, s[54:55] sc1\n\t"
      "global_load_dwordx4 v[140:143], v227, s[54:55] sc1\n\t"
      "global_load_dwordx4 v[144:147], v228, s[54:55] sc1\n\t"
      "global_load_dwordx4 v[148:151], v229, s[54:55] sc1\n\t"
      "s_waitcnt vmcnt(5)\n\t"
      "v_max3_u32 v234, v128, v129, v130\n\t"
      "v_max_u32_e32 v234, v234, v131\n\t"
      "v_cmp_eq_u32_e32 vcc, -1, v234\n\t"
      "s_cbranch_vccnz FQ7L_%=\n\t"
      "FQ7X_%=:\n\t"
      "s_branch FQ7B_%=\n\t"
      "NQ0R_%=:\n\t"
      "s_mov_b32 s51, 0\n\t"
      "NQ0L_%=:\n\t"
      "s_add_u32 s51, s51, 1\n\t"
      "s_cmp_lt_u32 s51, 262144\n\t"
      "s_cbranch_scc0 NQ0X_%=\n\t"
      "s_waitcnt vmcnt(0)\n\t"
      "s_sleep 1\n\t"
      "global_load_dwordx4 v[128:131], v224, s[52:53] sc1\n\t"
      "global_load_dwordx4 v[132:135], v225, s[52:53] sc1\n\t"
      "global_load_dwordx4 v[136:139], v226, s[52:53] sc1\n\t"
      "global_load_dwordx4 v[140:143], v227, s[52:53] sc1\n\t"
      "global_load_dwordx4 v[144:147], v228, s[52:53] sc1\n\t"
      "global_load_dwordx4 v[148:151], v229, s[52:53] sc1\n\t"
      "global_load_dwordx4 v[152:155], v230, s[52:53] sc1\n\t"
      "s_waitcnt vmcnt(0)\n\t"
      "s_nop 0\n\t"
      "v_max3_u32 v234, v128, v129, v130\n\t"
      "v_max3_u32 v234, v234, v131, v132\n\t"
      "v_max3_u32 v234, v234, v133, v134\n\t"
      "v_max3_u32 v234, v234, v135, v136\n\t"
      "v_max3_u32 v234, v234, v137, v138\n\t"
      "v_max3_u32 v234, v234, v139, v140\n\t"
      "v_max3_u32 v234, v234, v141, v142\n\t"
      "v_max3_u32 v234, v234, v143, v144\n\t"
      "v_max3_u32 v234, v234, v145, v146\n\t"
      "v_max3_u32 v234, v234, v147, v148\n\t"
      "v_max3_u32 v234, v234, v149, v150\n\t"
      "v_max3_u32 v234, v234, v151, v152\n\t"
      "v_max3_u32 v234, v234, v153, v154\n\t"
      "v_max_u32_e32 v234, v234, v155\n\t"
      "v_cmp_eq_u32_e32 vcc, -1, v234\n\t"
      "s_cbranch_vccnz NQ0L_%=\n\t"
      "NQ0X_%=:\n\t"
      "v_mfma_f32_16x16x32_bf16 v[184:187], v[128:131], v[0:3], 0\n\t"
      "v_mfma_f32_16x16x32_bf16 v[188:191], v[128:131], v[4:7], 0\n\t"
      "v_mfma_f32_16x16x32_bf16 v[192:195], v[128:131], v[8:11], 0\n\t"
      "v_mfma_f32_16x16x32_bf16 v[196:199], v[128:131], v[12:15], 0\n\t"
      "v_mfma_f32_16x16x32_bf16 v[184:187], v[132:135], v[16:19], v[184:187]\n\t"
      "v_mfma_f32_16x16x32_bf16 v[188:191], v[132:135], v[20:23], v[188:191]\n\t"
      "v_mfma_f32_16x16x32_bf16 v[192:195], v[132:135], v[24:27], v[192:195]\n\t"
      "v_mfma_f32_16x16x32_bf16 v[196:199], v[132:135], v[28:31], v[196:199]\n\t"
      "v_mfma_f32_16x16x32_bf16 v[184:187], v[136:139], v[32:35], v[184:187]\n\t"
      "v_mfma_f32_16x16x32_bf16 v[188:191], v[136:139], v[36:39], v[188:191]\n\t"
      "v_mfma_f32_16x16x32_bf16 v[192:195], v[136:139], v[40:43], v[192:195]\n\t"
      "v_mfma_f32_16x16x32_bf16 v[196:199], v[136:139], v[44:47], v[196:199]\n\t"
      "v_mfma_f32_16x16x32_bf16 v[184:187], v[140:143], v[48:51], v[184:187]\n\t"
      "v_mfma_f32_16x16x32_bf16 v[188:191], v[140:143], v[52:55], v[188:191]\n\t"
      "v_mfma_f32_16x16x32_bf16 v[192:195], v[140:143], v[56:59], v[192:195]\n\t"
      "v_mfma_f32_16x16x32_bf16 v[196:199], v[140:143], v[60:63], v[196:199]\n\t"
      "v_mfma_f32_16x16x32_bf16 v[184:187], v[144:147], v[64:67], v[184:187]\n\t"
      "v_mfma_f32_16x16x32_bf16 v[188:191], v[144:147], v[68:71], v[188:191]\n\t"
      "v_mfma_f32_16x16x32_bf16 v[192:195], v[144:147], v[72:75], v[192:195]\n\t"
      "v_mfma_f32_16x16x32_bf16 v[196:199], v[144:147], v[76:79], v[196:199]\n\t"
      "v_mfma_f32_16x16x32_bf16 v[184:187], v[148:151], v[80:83], v[184:187]\n\t"
      "v_mfma_f32_16x16x32_bf16 v[188:191], v[148:151], v[84:87], v[188:191]\n\t"
      "v_mfma_f32_16x16x32_bf16 v[192:195], v[148:151], v[88:91], v[192:195]\n\t"
      "v_mfma_f32_16x16x32_bf16 v[196:199], v[148:151], v[92:95], v[196:199]\n\t"
      "v_mfma_f32_16x16x32_bf16 v[184:187], v[152:155], v[96:99], v[184:187]\n\t"
      "v_mfma_f32_16x16x32_bf16 v[188:191], v[152:155], v[100:103], v[188:191]\n\t"
      "v_mfma_f32_16x16x32_bf16 v[192:195], v[152:155], v[104:107], v[192:195]\n\t"
      "v_mfma_f32_16x16x32_bf16 v[196:199], v[152:155], v[108:111], v[196:199]\n\t"
      "global_load_dwordx4 v[128:131], v224, s[54:55] sc1\n\t"
      "global_load_dwordx4 v[132:135], v225, s[54:55] sc1\n\t"
      "global_load_dwordx4 v[136:139], v226, s[54:55] sc1\n\t"
      "global_load_dwordx4 v[140:143], v227, s[54:55] sc1\n\t"
      "global_load_dwordx4 v[144:147], v228, s[54:55] sc1\n\t"
      "global_load_dwordx4 v[148:151], v229, s[54:55] sc1\n\t"
      "global_load_dwordx4 v[152:155], v230, s[54:55] sc1\n\t"
      "s_waitcnt vmcnt(0)\n\t"
      "s_nop 7\n\t"
      "s_branch NQ0B_%=\n\t"
      "FQ8R_%=:\n\t"
      "s_mov_b32 s51, 0\n\t"
      "FQ8L_%=:\n\t"
      "s_add_u32 s51, s51, 1\n\t"
      "s_cmp_lt_u32 s51, 262144\n\t"
      "s_cbranch_scc0 FQ8X_%=\n\t"
      "s_waitcnt vmcnt(0)\n\t"
      "s_sleep 4\n\t"
      "global_load_dwordx4 v[132:135], v225, s[54:55] sc1\n\t"
      "global_load_dwordx4 v[136:139], v226, s[54:55] sc1\n\t"
      "global_load_dwordx4 v[140:143], v227, s[54:55] sc1\n\t"
      "global_load_dwordx4 v[144:147], v228, s[54:55] sc1\n\t"
      "global_load_dwordx4 v[148:151], v229, s[54:55] sc1\n\t"
      "global_load_dwordx4 v[152:155], v230, s[54:55] sc1\n\t"
      "s_waitcnt vmcnt(5)\n\t"
      "v_max3_u32 v234, v132, v133, v134\n\t"
      "v_max_u32_e32 v234, v234, v135\n\t"
      "v_cmp_eq_u32_e32 vcc, -1, v234\n\t"
      "s_cbranch_vccnz FQ8L_%=\n\t"
      "FQ8X_%=:\n\t"
      "s_branch FQ8B_%=\n\t"
      "FQ9R_%=:\n\t"
      "s_mov_b32 s51, 0\n\t"
      "FQ9L_%=:\n\t"
      "s_add_u32 s51, s51, 1\n\t"
      "s_cmp_lt_u32 s51, 262144\n\t"
      "s_cbranch_scc0 FQ9X_%=\n\t"
      "s_waitcnt vmcnt(0)\n\t"
      "s_sleep 4\n\t"
      "global_load_dwordx4 v[136:139], v226, s[54:55] sc1\n\t"
      "global_load_dwordx4 v[140:143], v227, s[54:55] sc1\n\t"
      "global_load_dwordx4 v[144:147], v228, s[54:55] sc1\n\t"
      "global_load_dwordx4 v[148:151], v229, s[54:55] sc1\n\t"
      "global_load_dwordx4 v[152:155], v230, s[54:55] sc1\n\t"
      "global_load_dwordx4 v[128:131], v224, s[56:57] sc1\n\t"
      "s_waitcnt vmcnt(5)\n\t"
      "v_max3_u32 v234, v136, v137, v138\n\t"
      "v_max_u32_e32 v234, v234, v139\n\t"
      "v_cmp_eq_u32_e32 vcc, -1, v234\n\t"
      "s_cbranch_vccnz FQ9L_%=\n\t"
      "FQ9X_%=:\n\t"
      "s_branch FQ9B_%=\n\t"
      "FQ10R_%=:\n\t"
      "s_mov_b32 s51, 0\n\t"
      "FQ10L_%=:\n\t"
      "s_add_u32 s51, s51, 1\n\t"
      "s_cmp_lt_u32 s51, 262144\n\t"
      "s_cbranch_scc0 FQ10X_%=\n\t"
      "s_waitcnt vmcnt(0)\n\t"
      "s_sleep 4\n\t"
      "global_load_dwordx4 v[140:143], v227, s[54:55] sc1\n\t"
      "global_load_dwordx4 v[144:147], v228, s[54:55] sc1\n\t"
      "global_load_dwordx4 v[148:151], v229, s[54:55] sc1\n\t"
      "global_load_dwordx4 v[152:155], v230, s[54:55] sc1\n\t"
      "global_load_dwordx4 v[128:131], v224, s[56:57] sc1\n\t"
      "global_load_dwordx4 v[132:135], v225, s[56:57] sc1\n\t"
      "s_waitcnt vmcnt(5)\n\t"
      "v_max3_u32 v234, v140, v141, v142\n\t"
      "v_max_u32_e32 v234, v234, v143\n\t"
      "v_cmp_eq_u32_e32 vcc, -1, v234\n\t"
      "s_cbranch_vccnz FQ10L_%=\n\t"
      "FQ10X_%=:\n\t"
      "s_branch FQ10B_%=\n\t"
      "FQ11R_%=:\n\t"
      "s_mov_b32 s51, 0\n\t"
      "FQ11L_%=:\n\t"
      "s_add_u32 s51, s51, 1\n\t"
      "s_cmp_lt_u32 s51, 262144\n\t"
      "s_cbranch_scc0 FQ11X_%=\n\t"
      "s_waitcnt vmcnt(0)\n\t"
      "s_sleep 4\n\t"
      "global_load_dwordx4 v[144:147], v228, s[54:55] sc1\n\t"
      "global_load_dwordx4 v[148:151], v229, s[54:55] sc1\n\t"
      "global_load_dwordx4 v[152:155], v230, s[54:55] sc1\n\t"
      "global_load_dwordx4 v[128:131], v224, s[56:57] sc1\n\t"
      "global_load_dwordx4 v[132:135], v225, s[56:57] sc1\n\t"
      "global_load_dwordx4 v[136:139], v226, s[56:57] sc1\n\t"
      "s_waitcnt vmcnt(5)\n\t"
      "v_max3_u32 v234, v144, v145, v146\n\t"
      "v_max_u32_e32 v234, v234, v147\n\t"
      "v_cmp_eq_u32_e32 vcc, -1, v234\n\t"
      "s_cbranch_vccnz FQ11L_%=\n\t"
      "FQ11X_%=:\n\t"
      "s_branch FQ11B_%=\n\t"
      "FQ12R_%=:\n\t"
      "s_mov_b32 s51, 0\n\t"
      "FQ12L_%=:\n\t"
      "s_add_u32 s51, s51, 1\n\t"
      "s_cmp_lt_u32 s51, 262144\n\t"
      "s_cbranch_scc0 FQ12X_%=\n\t"
      "s_waitcnt vmcnt(0)\n\t"
      "s_sleep 4\n\t"
      "global_load_dwordx4 v[148:151], v229, s[54:55] sc1\n\t"
      "global_load_dwordx4 v[152:155], v230, s[54:55] sc1\n\t"
      "global_load_dwordx4 v[128:131], v224, s[56:57] sc1\n\t"
      "global_load_dwordx4 v[132:135], v225, s[56:57] sc1\n\t"
      "global_load_dwordx4 v[136:139], v226, s[56:57] sc1\n\t"
      "global_load_dwordx4 v[140:143], v227, s[56:57] sc1\n\t"
      "s_waitcnt vmcnt(5)\n\t"
      "v_max3_u32 v234, v148, v149, v150\n\t"
      "v_max_u32_e32 v234, v234, v151\n\t"
      "v_cmp_eq_u32_e32 vcc, -1, v234\n\t"
      "s_cbranch_vccnz FQ12L_%=\n\t"
      "FQ12X_%=:\n\t"
      "s_branch FQ12B_%=\n\t"
      "FQ13R_%=:\n\t"
      "s_mov_b32 s51, 0\n\t"
      "FQ13L_%=:\n\t"
      "s_add_u32 s51, s51, 1\n\t"
      "s_cmp_lt_u32 s51, 262144\n\t"
      "s_cbranch_scc0 FQ13X_%=\n\t"
      "s_waitcnt vmcnt(0)\n\t"
      "s_sleep 4\n\t"
      "global_load_dwordx4 v[152:155], v230, s[54:55] sc1\n\t"
      "global_load_dwordx4 v[128:131], v224, s[56:57] sc1\n\t"
      "global_load_dwordx4 v[132:135], v225, s[56:57] sc1\n\t"
      "global_load_dwordx4 v[136:139], v226, s[56:57] sc1\n\t"
      "global_load_dwordx4 v[140:143], v227, s[56:57] sc1\n\t"
      "global_load_dwordx4 v[144:147], v228, s[56:57] sc1\n\t"
      "s_waitcnt vmcnt(5)\n\t"
      "v_max3_u32 v234, v152, v153, v154\n\t"
      "v_max_u32_e32 v234, v234, v155\n\t"
      "v_cmp_eq_u32_e32 vcc, -1, v234\n\t"
      "s_cbranch_vccnz FQ13L_%=\n\t"
      "FQ13X_%=:\n\t"
      "s_branch FQ13B_%=\n\t"
      "FQ14R_%=:\n\t"
      "s_mov_b32 s51, 0\n\t"
      "FQ14L_%=:\n\t"
      "s_add_u32 s51, s51, 1\n\t"
      "s_cmp_lt_u32 s51, 262144\n\t"
      "s_cbranch_scc0 FQ14X_%=\n\t"
      "s_waitcnt vmcnt(0)\n\t"
      "s_sleep 4\n\t"
      "global_load_dwordx4 v[128:131], v224, s[56:57] sc1\n\t"
      "global_load_dwordx4 v[132:135], v225, s[56:57] sc1\n\t"
      "global_load_dwordx4 v[136:139], v226, s[56:57] sc1\n\t"
      "global_load_dwordx4 v[140:143], v227, s[56:57] sc1\n\t"
      "global_load_dwordx4 v[144:147], v228, s[56:57] sc1\n\t"
      "global_load_dwordx4 v[148:151], v229, s[56:57] sc1\n\t"
      "s_waitcnt vmcnt(5)\n\t"
      "v_max3_u32 v234, v128, v129, v130\n\t"
      "v_max_u32_e32 v234, v234, v131\n\t"
      "v_cmp_eq_u32_e32 vcc, -1, v234\n\t"
      "s_cbranch_vccnz FQ14L_%=\n\t"
      "FQ14X_%=:\n\t"
      "s_branch FQ14B_%=\n\t"
      "NQ1R_%=:\n\t"
      "s_mov_b32 s51, 0\n\t"
      "NQ1L_%=:\n\t"
      "s_add_u32 s51, s51, 1\n\t"
      "s_cmp_lt_u32 s51, 262144\n\t"
      "s_cbranch_scc0 NQ1X_%=\n\t"
      "s_waitcnt vmcnt(0)\n\t"
      "s_sleep 1\n\t"
      "global_load_dwordx4 v[128:131], v224, s[54:55] sc1\n\t"
      "global_load_dwordx4 v[132:135], v225, s[54:55] sc1\n\t"
      "global_load_dwordx4 v[136:139], v226, s[54:55] sc1\n\t"
      "global_load_dwordx4 v[140:143], v227, s[54:55] sc1\n\t"
      "global_load_dwordx4 v[144:147], v228, s[54:55] sc1\n\t"
      "global_load_dwordx4 v[148:151], v229, s[54:55] sc1\n\t"
      "global_load_dwordx4 v[152:155], v230, s[54:55] sc1\n\t"
      "s_waitcnt vmcnt(0)\n\t"
      "v_max3_u32 v234, v128, v129, v130\n\t"
      "v_max3_u32 v234, v234, v131, v132\n\t"
      "v_max3_u32 v234, v234, v133, v134\n\t"
      "v_max3_u32 v234, v234, v135, v136\n\t"
      "v_max3_u32 v234, v234, v137, v138\n\t"
      "v_max3_u32 v234, v234, v139, v140\n\t"
      "v_max3_u32 v234, v234, v141, v142\n\t"
      "v_max3_u32 v234, v234, v143, v144\n\t"
      "v_max3_u32 v234, v234, v145, v146\n\t"
      "v_max3_u32 v234, v234, v147, v148\n\t"
      "v_max3_u32 v234, v234, v149, v150\n\t"
      "v_max3_u32 v234, v234, v151, v152\n\t"
      "v_max3_u32 v234, v234, v153, v154\n\t"
      "v_max_u32_e32 v234, v234, v155\n\t"
      "v_cmp_eq_u32_e32 vcc, -1, v234\n\t"
      "s_cbranch_vccnz NQ1L_%=\n\t"
      "NQ1X_%=:\n\t"
      "v_mfma_f32_16x16x32_bf16 v[200:203], v[128:131], v[0:3], 0\n\t"
      "v_mfma_f32_16x16x32_bf16 v[204:207], v[128:131], v[4:7], 0\n\t"
      "v_mfma_f32_16x16x32_bf16 v[208:211], v[128:131], v[8:11], 0\n\t"
      "v_mfma_f32_16x16x32_bf16 v[212:215], v[128:131], v[12:15], 0\n\t"
      "v_mfma_f32_16x16x32_bf16 v[200:203], v[132:135], v[16:19], v[200:203]\n\t"
      "v_mfma_f32_16x16x32_bf16 v[204:207], v[132:135], v[20:23], v[204:207]\n\t"
      "v_mfma_f32_16x16x32_bf16 v[208:211], v[132:135], v[24:27], v[208:211]\n\t"
      "v_mfma_f32_16x16x32_bf16 v[212:215], v[132:135], v[28:31], v[212:215]\n\t"
      "v_mfma_f32_16x16x32_bf16 v[200:203], v[136:139], v[32:35], v[200:203]\n\t"
      "v_mfma_f32_16x16x32_bf16 v[204:207], v[136:139], v[36:39], v[204:207]\n\t"
      "v_mfma_f32_16x16x32_bf16 v[208:211], v[136:139], v[40:43], v[208:211]\n\t"
      "v_mfma_f32_16x16x32_bf16 v[212:215], v[136:139], v[44:47], v[212:215]\n\t"
      "v_mfma_f32_16x16x32_bf16 v[200:203], v[140:143], v[48:51], v[200:203]\n\t"
      "v_mfma_f32_16x16x32_bf16 v[204:207], v[140:143], v[52:55], v[204:207]\n\t"
      "v_mfma_f32_16x16x32_bf16 v[208:211], v[140:143], v[56:59], v[208:211]\n\t"
      "v_mfma_f32_16x16x32_bf16 v[212:215], v[140:143], v[60:63], v[212:215]\n\t"
      "v_mfma_f32_16x16x32_bf16 v[200:203], v[144:147], v[64:67], v[200:203]\n\t"
      "v_mfma_f32_16x16x32_bf16 v[204:207], v[144:147], v[68:71], v[204:207]\n\t"
      "v_mfma_f32_16x16x32_bf16 v[208:211], v[144:147], v[72:75], v[208:211]\n\t"
      "v_mfma_f32_16x16x32_bf16 v[212:215], v[144:147], v[76:79], v[212:215]\n\t"
      "v_mfma_f32_16x16x32_bf16 v[200:203], v[148:151], v[80:83], v[200:203]\n\t"
      "v_mfma_f32_16x16x32_bf16 v[204:207], v[148:151], v[84:87], v[204:207]\n\t"
      "v_mfma_f32_16x16x32_bf16 v[208:211], v[148:151], v[88:91], v[208:211]\n\t"
      "v_mfma_f32_16x16x32_bf16 v[212:215], v[148:151], v[92:95], v[212:215]\n\t"
      "v_mfma_f32_16x16x32_bf16 v[200:203], v[152:155], v[96:99], v[200:203]\n\t"
      "v_mfma_f32_16x16x32_bf16 v[204:207], v[152:155], v[100:103], v[204:207]\n\t"
      "v_mfma_f32_16x16x32_bf16 v[208:211], v[152:155], v[104:107], v[208:211]\n\t"
      "v_mfma_f32_16x16x32_bf16 v[212:215], v[152:155], v[108:111], v[212:215]\n\t"
      "global_load_dwordx4 v[128:131], v224, s[56:57] sc1\n\t"
      "global_load_dwordx4 v[132:135], v225, s[56:57] sc1\n\t"
      "global_load_dwordx4 v[136:139], v226, s[56:57] sc1\n\t"
      "global_load_dwordx4 v[140:143], v227, s[56:57] sc1\n\t"
      "global_load_dwordx4 v[144:147], v228, s[56:57] sc1\n\t"
      "global_load_dwordx4 v[148:151], v229, s[56:57] sc1\n\t"
      "global_load_dwordx4 v[152:155], v230, s[56:57] sc1\n\t"
      "s_waitcnt vmcnt(0)\n\t"
      "s_nop 7\n\t"
      "s_branch NQ1B_%=\n\t"
      "FQ15R_%=:\n\t"
      "s_mov_b32 s51, 0\n\t"
      "FQ15L_%=:\n\t"
      "s_add_u32 s51, s51, 1\n\t"
      "s_cmp_lt_u32 s51, 262144\n\t"
      "s_cbranch_scc0 FQ15X_%=\n\t"
      "s_waitcnt vmcnt(0)\n\t"
      "s_sleep 4\n\t"
      "global_load_dwordx4 v[132:135], v225, s[56:57] sc1\n\t"
      "global_load_dwordx4 v[136:139], v226, s[56:57] sc1\n\t"
      "global_load_dwordx4 v[140:143], v227, s[56:57] sc1\n\t"
      "global_load_dwordx4 v[144:147], v228, s[56:57] sc1\n\t"
      "global_load_dwordx4 v[148:151], v229, s[56:57] sc1\n\t"
      "global_load_dwordx4 v[152:155], v230, s[56:57] sc1\n\t"
      "s_waitcnt vmcnt(5)\n\t"
      "v_max3_u32 v234, v132, v133, v134\n\t"
      "v_max_u32_e32 v234, v234, v135\n\t"
      "v_cmp_eq_u32_e32 vcc, -1, v234\n\t"
      "s_cbranch_vccnz FQ15L_%=\n\t"
      "FQ15X_%=:\n\t"
      "s_branch FQ15B_%=\n\t"
      "FQ16R_%=:\n\t"
      "s_mov_b32 s51, 0\n\t"
      "FQ16L_%=:\n\t"
      "s_add_u32 s51, s51, 1\n\t"
      "s_cmp_lt_u32 s51, 262144\n\t"
      "s_cbranch_scc0 FQ16X_%=\n\t"
      "s_waitcnt vmcnt(0)\n\t"
      "s_sleep 4\n\t"
      "global_load_dwordx4 v[136:139], v226, s[56:57] sc1\n\t"
      "global_load_dwordx4 v[140:143], v227, s[56:57] sc1\n\t"
      "global_load_dwordx4 v[144:147], v228, s[56:57] sc1\n\t"
      "global_load_dwordx4 v[148:151], v229, s[56:57] sc1\n\t"
      "global_load_dwordx4 v[152:155], v230, s[56:57] sc1\n\t"
      "global_load_dwordx4 v[128:131], v224, s[58:59] sc1\n\t"
      "s_waitcnt vmcnt(5)\n\t"
      "v_max3_u32 v234, v136, v137, v138\n\t"
      "v_max_u32_e32 v234, v234, v139\n\t"
      "v_cmp_eq_u32_e32 vcc, -1, v234\n\t"
      "s_cbranch_vccnz FQ16L_%=\n\t"
      "FQ16X_%=:\n\t"
      "s_branch FQ16B_%=\n\t"
      "FQ17R_%=:\n\t"
      "s_mov_b32 s51, 0\n\t"
      "FQ17L_%=:\n\t"
      "s_add_u32 s51, s51, 1\n\t"
      "s_cmp_lt_u32 s51, 262144\n\t"
      "s_cbranch_scc0 FQ17X_%=\n\t"
      "s_waitcnt vmcnt(0)\n\t"
      "s_sleep 4\n\t"
      "global_load_dwordx4 v[140:143], v227, s[56:57] sc1\n\t"
      "global_load_dwordx4 v[144:147], v228, s[56:57] sc1\n\t"
      "global_load_dwordx4 v[148:151], v229, s[56:57] sc1\n\t"
      "global_load_dwordx4 v[152:155], v230, s[56:57] sc1\n\t"
      "global_load_dwordx4 v[128:131], v224, s[58:59] sc1\n\t"
      "global_load_dwordx4 v[132:135], v225, s[58:59] sc1\n\t"
      "s_waitcnt vmcnt(5)\n\t"
      "v_max3_u32 v234, v140, v141, v142\n\t"
      "v_max_u32_e32 v234, v234, v143\n\t"
      "v_cmp_eq_u32_e32 vcc, -1, v234\n\t"
      "s_cbranch_vccnz FQ17L_%=\n\t"
      "FQ17X_%=:\n\t"
      "s_branch FQ17B_%=\n\t"
      "FQ18R_%=:\n\t"
      "s_mov_b32 s51, 0\n\t"
      "FQ18L_%=:\n\t"
      "s_add_u32 s51, s51, 1\n\t"
      "s_cmp_lt_u32 s51, 262144\n\t"
      "s_cbranch_scc0 FQ18X_%=\n\t"
      "s_waitcnt vmcnt(0)\n\t"
      "s_sleep 4\n\t"
      "global_load_dwordx4 v[144:147], v228, s[56:57] sc1\n\t"
      "global_load_dwordx4 v[148:151], v229, s[56:57] sc1\n\t"
      "global_load_dwordx4 v[152:155], v230, s[56:57] sc1\n\t"
      "global_load_dwordx4 v[128:131], v224, s[58:59] sc1\n\t"
      "global_load_dwordx4 v[132:135], v225, s[58:59] sc1\n\t"
      "global_load_dwordx4 v[136:139], v226, s[58:59] sc1\n\t"
      "s_waitcnt vmcnt(5)\n\t"
      "v_max3_u32 v234, v144, v145, v146\n\t"
      "v_max_u32_e32 v234, v234, v147\n\t"
      "v_cmp_eq_u32_e32 vcc, -1, v234\n\t"
      "s_cbranch_vccnz FQ18L_%=\n\t"
      "FQ18X_%=:\n\t"
      "s_branch FQ18B_%=\n\t"
      "FQ19R_%=:\n\t"
      "s_mov_b32 s51, 0\n\t"
      "FQ19L_%=:\n\t"
      "s_add_u32 s51, s51, 1\n\t"
      "s_cmp_lt_u32 s51, 262144\n\t"
      "s_cbranch_scc0 FQ19X_%=\n\t"
      "s_waitcnt vmcnt(0)\n\t"
      "s_sleep 4\n\t"
      "global_load_dwordx4 v[148:151], v229, s[56:57] sc1\n\t"
      "global_load_dwordx4 v[152:155], v230, s[56:57] sc1\n\t"
      "global_load_dwordx4 v[128:131], v224, s[58:59] sc1\n\t"
      "global_load_dwordx4 v[132:135], v225, s[58:59] sc1\n\t"
      "global_load_dwordx4 v[136:139], v226, s[58:59] sc1\n\t"
      "global_load_dwordx4 v[140:143], v227, s[58:59] sc1\n\t"
      "s_waitcnt vmcnt(5)\n\t"
      "v_max3_u32 v234, v148, v149, v150\n\t"
      "v_max_u32_e32 v234, v234, v151\n\t"
      "v_cmp_eq_u32_e32 vcc, -1, v234\n\t"
      "s_cbranch_vccnz FQ19L_%=\n\t"
      "FQ19X_%=:\n\t"
      "s_branch FQ19B_%=\n\t"
      "FQ20R_%=:\n\t"
      "s_mov_b32 s51, 0\n\t"
      "FQ20L_%=:\n\t"
      "s_add_u32 s51, s51, 1\n\t"
      "s_cmp_lt_u32 s51, 262144\n\t"
      "s_cbranch_scc0 FQ20X_%=\n\t"
      "s_waitcnt vmcnt(0)\n\t"
      "s_sleep 4\n\t"
      "global_load_dwordx4 v[152:155], v230, s[56:57] sc1\n\t"
      "global_load_dwordx4 v[128:131], v224, s[58:59] sc1\n\t"
      "global_load_dwordx4 v[132:135], v225, s[58:59] sc1\n\t"
      "global_load_dwordx4 v[136:139], v226, s[58:59] sc1\n\t"
      "global_load_dwordx4 v[140:143], v227, s[58:59] sc1\n\t"
      "global_load_dwordx4 v[144:147], v228, s[58:59] sc1\n\t"
      "s_waitcnt vmcnt(5)\n\t"
      "v_max3_u32 v234, v152, v153, v154\n\t"
      "v_max_u32_e32 v234, v234, v155\n\t"
      "v_cmp_eq_u32_e32 vcc, -1, v234\n\t"
      "s_cbranch_vccnz FQ20L_%=\n\t"
      "FQ20X_%=:\n\t"
      "s_branch FQ20B_%=\n\t"
      "FQ21R_%=:\n\t"
      "s_mov_b32 s51, 0\n\t"
      "FQ21L_%=:\n\t"
      "s_add_u32 s51, s51, 1\n\t"
      "s_cmp_lt_u32 s51, 262144\n\t"
      "s_cbranch_scc0 FQ21X_%=\n\t"
      "s_waitcnt vmcnt(0)\n\t"
      "s_sleep 4\n\t"
      "global_load_dwordx4 v[128:131], v224, s[58:59] sc1\n\t"
      "global_load_dwordx4 v[132:135], v225, s[58:59] sc1\n\t"
      "global_load_dwordx4 v[136:139], v226, s[58:59] sc1\n\t"
      "global_load_dwordx4 v[140:143], v227, s[58:59] sc1\n\t"
      "global_load_dwordx4 v[144:147], v228, s[58:59] sc1\n\t"
      "global_load_dwordx4 v[148:151], v229, s[58:59] sc1\n\t"
      "s_waitcnt vmcnt(5)\n\t"
      "v_max3_u32 v234, v128, v129, v130\n\t"
      "v_max_u32_e32 v234, v234, v131\n\t"
      "v_cmp_eq_u32_e32 vcc, -1, v234\n\t"
      "s_cbranch_vccnz FQ21L_%=\n\t"
      "FQ21X_%=:\n\t"
      "s_branch FQ21B_%=\n\t"
      "NQ2R_%=:\n\t"
      "s_mov_b32 s51, 0\n\t"
      "NQ2L_%=:\n\t"
      "s_add_u32 s51, s51, 1\n\t"
      "s_cmp_lt_u32 s51, 262144\n\t"
      "s_cbranch_scc0 NQ2X_%=\n\t"
      "s_waitcnt vmcnt(0)\n\t"
      "s_sleep 1\n\t"
      "global_load_dwordx4 v[128:131], v224, s[56:57] sc1\n\t"
      "global_load_dwordx4 v[132:135], v225, s[56:57] sc1\n\t"
      "global_load_dwordx4 v[136:139], v226, s[56:57] sc1\n\t"
      "global_load_dwordx4 v[140:143], v227, s[56:57] sc1\n\t"
      "global_load_dwordx4 v[144:147], v228, s[56:57] sc1\n\t"
      "global_load_dwordx4 v[148:151], v229, s[56:57] sc1\n\t"
      "global_load_dwordx4 v[152:155], v230, s[56:57] sc1\n\t"
      "s_waitcnt vmcnt(0)\n\t"
      "v_max3_u32 v234, v128, v129, v130\n\t"
      "v_max3_u32 v234, v234, v131, v132\n\t"
      "v_max3_u32 v234, v234, v133, v134\n\t"
      "v_max3_u32 v234, v234, v135, v136\n\t"
      "v_max3_u32 v234, v234, v137, v138\n\t"
      "v_max3_u32 v234, v234, v139, v140\n\t"
      "v_max3_u32 v234, v234, v141, v142\n\t"
      "v_max3_u32 v234, v234, v143, v144\n\t"
      "v_max3_u32 v234, v234, v145, v146\n\t"
      "v_max3_u32 v234, v234, v147, v148\n\t"
      "v_max3_u32 v234, v234, v149, v150\n\t"
      "v_max3_u32 v234, v234, v151, v152\n\t"
      "v_max3_u32 v234, v234, v153, v154\n\t"
      "v_max_u32_e32 v234, v234, v155\n\t"
      "v_cmp_eq_u32_e32 vcc, -1, v234\n\t"
      "s_cbranch_vccnz NQ2L_%=\n\t"
      "NQ2X_%=:\n\t"
      "v_mfma_f32_16x16x32_bf16 v[184:187], v[128:131], v[0:3], 0\n\t"
      "v_mfma_f32_16x16x32_bf16 v[188:191], v[128:131], v[4:7], 0\n\t"
      "v_mfma_f32_16x16x32_bf16 v[192:195], v[128:131], v[8:11], 0\n\t"
      "v_mfma_f32_16x16x32_bf16 v[196:199], v[128:131], v[12:15], 0\n\t"
      "v_mfma_f32_16x16x32_bf16 v[184:187], v[132:135], v[16:19], v[184:187]\n\t"
      "v_mfma_f32_16x16x32_bf16 v[188:191], v[132:135], v[20:23], v[188:191]\n\t"
      "v_mfma_f32_16x16x32_bf16 v[192:195], v[132:135], v[24:27], v[192:195]\n\t"
      "v_mfma_f32_16x16x32_bf16 v[196:199], v[132:135], v[28:31], v[196:199]\n\t"
      "v_mfma_f32_16x16x32_bf16 v[184:187], v[136:139], v[32:35], v[184:187]\n\t"
      "v_mfma_f32_16x16x32_bf16 v[188:191], v[136:139], v[36:39], v[188:191]\n\t"
      "v_mfma_f32_16x16x32_bf16 v[192:195], v[136:139], v[40:43], v[192:195]\n\t"
      "v_mfma_f32_16x16x32_bf16 v[196:199], v[136:139], v[44:47], v[196:199]\n\t"
      "v_mfma_f32_16x16x32_bf16 v[184:187], v[140:143], v[48:51], v[184:187]\n\t"
      "v_mfma_f32_16x16x32_bf16 v[188:191], v[140:143], v[52:55], v[188:191]\n\t"
      "v_mfma_f32_16x16x32_bf16 v[192:195], v[140:143], v[56:59], v[192:195]\n\t"
      "v_mfma_f32_16x16x32_bf16 v[196:199], v[140:143], v[60:63], v[196:199]\n\t"
      "v_mfma_f32_16x16x32_bf16 v[184:187], v[144:147], v[64:67], v[184:187]\n\t"
      "v_mfma_f32_16x16x32_bf16 v[188:191], v[144:147], v[68:71], v[188:191]\n\t"
      "v_mfma_f32_16x16x32_bf16 v[192:195], v[144:147], v[72:75], v[192:195]\n\t"
      "v_mfma_f32_16x16x32_bf16 v[196:199], v[144:147], v[76:79], v[196:199]\n\t"
      "v_mfma_f32_16x16x32_bf16 v[184:187], v[148:151], v[80:83], v[184:187]\n\t"
      "v_mfma_f32_16x16x32_bf16 v[188:191], v[148:151], v[84:87], v[188:191]\n\t"
      "v_mfma_f32_16x16x32_bf16 v[192:195], v[148:151], v[88:91], v[192:195]\n\t"
      "v_mfma_f32_16x16x32_bf16 v[196:199], v[148:151], v[92:95], v[196:199]\n\t"
      "v_mfma_f32_16x16x32_bf16 v[184:187], v[152:155], v[96:99], v[184:187]\n\t"
      "v_mfma_f32_16x16x32_bf16 v[188:191], v[152:155], v[100:103], v[188:191]\n\t"
      "v_mfma_f32_16x16x32_bf16 v[192:195], v[152:155], v[104:107], v[192:195]\n\t"
      "v_mfma_f32_16x16x32_bf16 v[196:199], v[152:155], v[108:111], v[196:199]\n\t"
      "global_load_dwordx4 v[128:131], v224, s[58:59] sc1\n\t"
      "global_load_dwordx4 v[132:135], v225, s[58:59] sc1\n\t"
      "global_load_dwordx4 v[136:139], v226, s[58:59] sc1\n\t"
      "global_load_dwordx4 v[140:143], v227, s[58:59] sc1\n\t"
      "global_load_dwordx4 v[144:147], v228, s[58:59] sc1\n\t"
      "global_load_dwordx4 v[148:151], v229, s[58:59] sc1\n\t"
      "global_load_dwordx4 v[152:155], v230, s[58:59] sc1\n\t"
      "s_waitcnt vmcnt(0)\n\t"
      "s_nop 7\n\t"
      "s_branch NQ2B_%=\n\t"
      "FQ22R_%=:\n\t"
      "s_mov_b32 s51, 0\n\t"
      "FQ22L_%=:\n\t"
      "s_add_u32 s51, s51, 1\n\t"
      "s_cmp_lt_u32 s51, 262144\n\t"
      "s_cbranch_scc0 FQ22X_%=\n\t"
      "s_waitcnt vmcnt(0)\n\t"
      "s_sleep 4\n\t"
      "global_load_dwordx4 v[132:135], v225, s[58:59] sc1\n\t"
      "global_load_dwordx4 v[136:139], v226, s[58:59] sc1\n\t"
      "global_load_dwordx4 v[140:143], v227, s[58:59] sc1\n\t"
      "global_load_dwordx4 v[144:147], v228, s[58:59] sc1\n\t"
      "global_load_dwordx4 v[148:151], v229, s[58:59] sc1\n\t"
      "global_load_dwordx4 v[152:155], v230, s[58:59] sc1\n\t"
      "s_waitcnt vmcnt(5)\n\t"
      "v_max3_u32 v234, v132, v133, v134\n\t"
      "v_max_u32_e32 v234, v234, v135\n\t"
      "v_cmp_eq_u32_e32 vcc, -1, v234\n\t"
      "s_cbranch_vccnz FQ22L_%=\n\t"
      "FQ22X_%=:\n\t"
      "s_branch FQ22B_%=\n\t"
      "FQ23R_%=:\n\t"
      "s_mov_b32 s51, 0\n\t"
      "FQ23L_%=:\n\t"
      "s_add_u32 s51, s51, 1\n\t"
      "s_cmp_lt_u32 s51, 262144\n\t"
      "s_cbranch_scc0 FQ23X_%=\n\t"
      "s_waitcnt vmcnt(0)\n\t"
      "s_sleep 4\n\t"
      "global_load_dwordx4 v[136:139], v226, s[58:59] sc1\n\t"
      "global_load_dwordx4 v[140:143], v227, s[58:59] sc1\n\t"
      "global_load_dwordx4 v[144:147], v228, s[58:59] sc1\n\t"
      "global_load_dwordx4 v[148:151], v229, s[58:59] sc1\n\t"
      "global_load_dwordx4 v[152:155], v230, s[58:59] sc1\n\t"
      "global_load_dwordx4 v[128:131], v224, s[60:61] sc1\n\t"
      "s_waitcnt vmcnt(5)\n\t"
      "v_max3_u32 v234, v136, v137, v138\n\t"
      "v_max_u32_e32 v234, v234, v139\n\t"
      "v_cmp_eq_u32_e32 vcc, -1, v234\n\t"
      "s_cbranch_vccnz FQ23L_%=\n\t"
      "FQ23X_%=:\n\t"
      "s_branch FQ23B_%=\n\t"
      "FQ24R_%=:\n\t"
      "s_mov_b32 s51, 0\n\t"
      "FQ24L_%=:\n\t"
      "s_add_u32 s51, s51, 1\n\t"
      "s_cmp_lt_u32 s51, 262144\n\t"
      "s_cbranch_scc0 FQ24X_%=\n\t"
      "s_waitcnt vmcnt(0)\n\t"
      "s_sleep 4\n\t"
      "global_load_dwordx4 v[140:143], v227, s[58:59] sc1\n\t"
      "global_load_dwordx4 v[144:147], v228, s[58:59] sc1\n\t"
      "global_load_dwordx4 v[148:151], v229, s[58:59] sc1\n\t"
      "global_load_dwordx4 v[152:155], v230, s[58:59] sc1\n\t"
      "global_load_dwordx4 v[128:131], v224, s[60:61] sc1\n\t"
      "global_load_dwordx4 v[132:135], v225, s[60:61] sc1\n\t"
      "s_waitcnt vmcnt(5)\n\t"
      "v_max3_u32 v234, v140, v141, v142\n\t"
      "v_max_u32_e32 v234, v234, v143\n\t"
      "v_cmp_eq_u32_e32 vcc, -1, v234\n\t"
      "s_cbranch_vccnz FQ24L_%=\n\t"
      "FQ24X_%=:\n\t"
      "s_branch FQ24B_%=\n\t"
      "FQ25R_%=:\n\t"
      "s_mov_b32 s51, 0\n\t"
      "FQ25L_%=:\n\t"
      "s_add_u32 s51, s51, 1\n\t"
      "s_cmp_lt_u32 s51, 262144\n\t"
      "s_cbranch_scc0 FQ25X_%=\n\t"
      "s_waitcnt vmcnt(0)\n\t"
      "s_sleep 4\n\t"
      "global_load_dwordx4 v[144:147], v228, s[58:59] sc1\n\t"
      "global_load_dwordx4 v[148:151], v229, s[58:59] sc1\n\t"
      "global_load_dwordx4 v[152:155], v230, s[58:59] sc1\n\t"
      "global_load_dwordx4 v[128:131], v224, s[60:61] sc1\n\t"
      "global_load_dwordx4 v[132:135], v225, s[60:61] sc1\n\t"
      "global_load_dwordx4 v[136:139], v226, s[60:61] sc1\n\t"
      "s_waitcnt vmcnt(5)\n\t"
      "v_max3_u32 v234, v144, v145, v146\n\t"
      "v_max_u32_e32 v234, v234, v147\n\t"
      "v_cmp_eq_u32_e32 vcc, -1, v234\n\t"
      "s_cbranch_vccnz FQ25L_%=\n\t"
      "FQ25X_%=:\n\t"
      "s_branch FQ25B_%=\n\t"
      "FQ26R_%=:\n\t"
      "s_mov_b32 s51, 0\n\t"
      "FQ26L_%=:\n\t"
      "s_add_u32 s51, s51, 1\n\t"
      "s_cmp_lt_u32 s51, 262144\n\t"
      "s_cbranch_scc0 FQ26X_%=\n\t"
      "s_waitcnt vmcnt(0)\n\t"
      "s_sleep 4\n\t"
      "global_load_dwordx4 v[148:151], v229, s[58:59] sc1\n\t"
      "global_load_dwordx4 v[152:155], v230, s[58:59] sc1\n\t"
      "global_load_dwordx4 v[128:131], v224, s[60:61] sc1\n\t"
      "global_load_dwordx4 v[132:135], v225, s[60:61] sc1\n\t"
      "global_load_dwordx4 v[136:139], v226, s[60:61] sc1\n\t"
      "global_load_dwordx4 v[140:143], v227, s[60:61] sc1\n\t"
      "s_waitcnt vmcnt(5)\n\t"
      "v_max3_u32 v234, v148, v149, v150\n\t"
      "v_max_u32_e32 v234, v234, v151\n\t"
      "v_cmp_eq_u32_e32 vcc, -1, v234\n\t"
      "s_cbranch_vccnz FQ26L_%=\n\t"
      "FQ26X_%=:\n\t"
      "s_branch FQ26B_%=\n\t"
      "FQ27R_%=:\n\t"
      "s_mov_b32 s51, 0\n\t"
      "FQ27L_%=:\n\t"
      "s_add_u32 s51, s51, 1\n\t"
      "s_cmp_lt_u32 s51, 262144\n\t"
      "s_cbranch_scc0 FQ27X_%=\n\t"
      "s_waitcnt vmcnt(0)\n\t"
      "s_sleep 4\n\t"
      "global_load_dwordx4 v[152:155], v230, s[58:59] sc1\n\t"
      "global_load_dwordx4 v[128:131], v224, s[60:61] sc1\n\t"
      "global_load_dwordx4 v[132:135], v225, s[60:61] sc1\n\t"
      "global_load_dwordx4 v[136:139], v226, s[60:61] sc1\n\t"
      "global_load_dwordx4 v[140:143], v227, s[60:61] sc1\n\t"
      "global_load_dwordx4 v[144:147], v228, s[60:61] sc1\n\t"
      "s_waitcnt vmcnt(5)\n\t"
      "v_max3_u32 v234, v152, v153, v154\n\t"
      "v_max_u32_e32 v234, v234, v155\n\t"
      "v_cmp_eq_u32_e32 vcc, -1, v234\n\t"
      "s_cbranch_vccnz FQ27L_%=\n\t"
      "FQ27X_%=:\n\t"
      "s_branch FQ27B_%=\n\t"
      "FQ28R_%=:\n\t"
      "s_mov_b32 s51, 0\n\t"
      "FQ28L_%=:\n\t"
      "s_add_u32 s51, s51, 1\n\t"
      "s_cmp_lt_u32 s51, 262144\n\t"
      "s_cbranch_scc0 FQ28X_%=\n\t"
      "s_waitcnt vmcnt(0)\n\t"
      "s_sleep 4\n\t"
      "global_load_dwordx4 v[128:131], v224, s[60:61] sc1\n\t"
      "global_load_dwordx4 v[132:135], v225, s[60:61] sc1\n\t"
      "global_load_dwordx4 v[136:139], v226, s[60:61] sc1\n\t"
      "global_load_dwordx4 v[140:143], v227, s[60:61] sc1\n\t"
      "global_load_dwordx4 v[144:147], v228, s[60:61] sc1\n\t"
      "global_load_dwordx4 v[148:151], v229, s[60:61] sc1\n\t"
      "s_waitcnt vmcnt(5)\n\t"
      "v_max3_u32 v234, v128, v129, v130\n\t"
      "v_max_u32_e32 v234, v234, v131\n\t"
      "v_cmp_eq_u32_e32 vcc, -1, v234\n\t"
      "s_cbranch_vccnz FQ28L_%=\n\t"
      "FQ28X_%=:\n\t"
      "s_branch FQ28B_%=\n\t"
      "NQ3R_%=:\n\t"
      "s_mov_b32 s51, 0\n\t"
      "NQ3L_%=:\n\t"
      "s_add_u32 s51, s51, 1\n\t"
      "s_cmp_lt_u32 s51, 262144\n\t"
      "s_cbranch_scc0 NQ3X_%=\n\t"
      "s_waitcnt vmcnt(0)\n\t"
      "s_sleep 1\n\t"
      "global_load_dwordx4 v[128:131], v224, s[58:59] sc1\n\t"
      "global_load_dwordx4 v[132:135], v225, s[58:59] sc1\n\t"
      "global_load_dwordx4 v[136:139], v226, s[58:59] sc1\n\t"
      "global_load_dwordx4 v[140:143], v227, s[58:59] sc1\n\t"
      "global_load_dwordx4 v[144:147], v228, s[58:59] sc1\n\t"
      "global_load_dwordx4 v[148:151], v229, s[58:59] sc1\n\t"
      "global_load_dwordx4 v[152:155], v230, s[58:59] sc1\n\t"
      "s_waitcnt vmcnt(0)\n\t"
      "v_max3_u32 v234, v128, v129, v130\n\t"
      "v_max3_u32 v234, v234, v131, v132\n\t"
      "v_max3_u32 v234, v234, v133, v134\n\t"
      "v_max3_u32 v234, v234, v135, v136\n\t"
      "v_max3_u32 v234, v234, v137, v138\n\t"
      "v_max3_u32 v234, v234, v139, v140\n\t"
      "v_max3_u32 v234, v234, v141, v142\n\t"
      "v_max3_u32 v234, v234, v143, v144\n\t"
      "v_max3_u32 v234, v234, v145, v146\n\t"
      "v_max3_u32 v234, v234, v147, v148\n\t"
      "v_max3_u32 v234, v234, v149, v150\n\t"
      "v_max3_u32 v234, v234, v151, v152\n\t"
      "v_max3_u32 v234, v234, v153, v154\n\t"
      "v_max_u32_e32 v234, v234, v155\n\t"
      "v_cmp_eq_u32_e32 vcc, -1, v234\n\t"
      "s_cbranch_vccnz NQ3L_%=\n\t"
      "NQ3X_%=:\n\t"
      "v_mfma_f32_16x16x32_bf16 v[200:203], v[128:131], v[0:3], 0\n\t"
      "v_mfma_f32_16x16x32_bf16 v[204:207], v[128:131], v[4:7], 0\n\t"
      "v_mfma_f32_16x16x32_bf16 v[208:211], v[128:131], v[8:11], 0\n\t"
      "v_mfma_f32_16x16x32_bf16 v[212:215], v[128:131], v[12:15], 0\n\t"
      "v_mfma_f32_16x16x32_bf16 v[200:203], v[132:135], v[16:19], v[200:203]\n\t"
      "v_mfma_f32_16x16x32_bf16 v[204:207], v[132:135], v[20:23], v[204:207]\n\t"
      "v_mfma_f32_16x16x32_bf16 v[208:211], v[132:135], v[24:27], v[208:211]\n\t"
      "v_mfma_f32_16x16x32_bf16 v[212:215], v[132:135], v[28:31], v[212:215]\n\t"
      "v_mfma_f32_16x16x32_bf16 v[200:203], v[136:139], v[32:35], v[200:203]\n\t"
      "v_mfma_f32_16x16x32_bf16 v[204:207], v[136:139], v[36:39], v[204:207]\n\t"
      "v_mfma_f32_16x16x32_bf16 v[208:211], v[136:139], v[40:43], v[208:211]\n\t"
      "v_mfma_f32_16x16x32_bf16 v[212:215], v[136:139], v[44:47], v[212:215]\n\t"
      "v_mfma_f32_16x16x32_bf16 v[200:203], v[140:143], v[48:51], v[200:203]\n\t"
      "v_mfma_f32_16x16x32_bf16 v[204:207], v[140:143], v[52:55], v[204:207]\n\t"
      "v_mfma_f32_16x16x32_bf16 v[208:211], v[140:143], v[56:59], v[208:211]\n\t"
      "v_mfma_f32_16x16x32_bf16 v[212:215], v[140:143], v[60:63], v[212:215]\n\t"
      "v_mfma_f32_16x16x32_bf16 v[200:203], v[144:147], v[64:67], v[200:203]\n\t"
      "v_mfma_f32_16x16x32_bf16 v[204:207], v[144:147], v[68:71], v[204:207]\n\t"
      "v_mfma_f32_16x16x32_bf16 v[208:211], v[144:147], v[72:75], v[208:211]\n\t"
      "v_mfma_f32_16x16x32_bf16 v[212:215], v[144:147], v[76:79], v[212:215]\n\t"
      "v_mfma_f32_16x16x32_bf16 v[200:203], v[148:151], v[80:83], v[200:203]\n\t"
      "v_mfma_f32_16x16x32_bf16 v[204:207], v[148:151], v[84:87], v[204:207]\n\t"
      "v_mfma_f32_16x16x32_bf16 v[208:211], v[148:151], v[88:91], v[208:211]\n\t"
      "v_mfma_f32_16x16x32_bf16 v[212:215], v[148:151], v[92:95], v[212:215]\n\t"
      "v_mfma_f32_16x16x32_bf16 v[200:203], v[152:155], v[96:99], v[200:203]\n\t"
      "v_mfma_f32_16x16x32_bf16 v[204:207], v[152:155], v[100:103], v[204:207]\n\t"
      "v_mfma_f32_16x16x32_bf16 v[208:211], v[152:155], v[104:107], v[208:211]\n\t"
      "v_mfma_f32_16x16x32_bf16 v[212:215], v[152:155], v[108:111], v[212:215]\n\t"
      "global_load_dwordx4 v[128:131], v224, s[60:61] sc1\n\t"
      "global_load_dwordx4 v[132:135], v225, s[60:61] sc1\n\t"
      "global_load_dwordx4 v[136:139], v226, s[60:61] sc1\n\t"
      "global_load_dwordx4 v[140:143], v227, s[60:61] sc1\n\t"
      "global_load_dwordx4 v[144:147], v228, s[60:61] sc1\n\t"
      "global_load_dwordx4 v[148:151], v229, s[60:61] sc1\n\t"
      "global_load_dwordx4 v[152:155], v230, s[60:61] sc1\n\t"
      "s_waitcnt vmcnt(0)\n\t"
      "s_nop 7\n\t"
      "s_branch NQ3B_%=\n\t"
      "FQ29R_%=:\n\t"
      "s_mov_b32 s51, 0\n\t"
      "FQ29L_%=:\n\t"
      "s_add_u32 s51, s51, 1\n\t"
      "s_cmp_lt_u32 s51, 262144\n\t"
      "s_cbranch_scc0 FQ29X_%=\n\t"
      "s_waitcnt vmcnt(0)\n\t"
      "s_sleep 4\n\t"
      "global_load_dwordx4 v[132:135], v225, s[60:61] sc1\n\t"
      "global_load_dwordx4 v[136:139], v226, s[60:61] sc1\n\t"
      "global_load_dwordx4 v[140:143], v227, s[60:61] sc1\n\t"
      "global_load_dwordx4 v[144:147], v228, s[60:61] sc1\n\t"
      "global_load_dwordx4 v[148:151], v229, s[60:61] sc1\n\t"
      "global_load_dwordx4 v[152:155], v230, s[60:61] sc1\n\t"
      "s_waitcnt vmcnt(5)\n\t"
      "v_max3_u32 v234, v132, v133, v134\n\t"
      "v_max_u32_e32 v234, v234, v135\n\t"
      "v_cmp_eq_u32_e32 vcc, -1, v234\n\t"
      "s_cbranch_vccnz FQ29L_%=\n\t"
      "FQ29X_%=:\n\t"
      "s_branch FQ29B_%=\n\t"
      "FQ30R_%=:\n\t"
      "s_mov_b32 s51, 0\n\t"
      "FQ30L_%=:\n\t"
      "s_add_u32 s51, s51, 1\n\t"
      "s_cmp_lt_u32 s51, 262144\n\t"
      "s_cbranch_scc0 FQ30X_%=\n\t"
      "s_waitcnt vmcnt(0)\n\t"
      "s_sleep 4\n\t"
      "global_load_dwordx4 v[136:139], v226, s[60:61] sc1\n\t"
      "global_load_dwordx4 v[140:143], v227, s[60:61] sc1\n\t"
      "global_load_dwordx4 v[144:147], v228, s[60:61] sc1\n\t"
      "global_load_dwordx4 v[148:151], v229, s[60:61] sc1\n\t"
      "global_load_dwordx4 v[152:155], v230, s[60:61] sc1\n\t"
      "global_load_dwordx4 v[128:131], v224, s[62:63] sc1\n\t"
      "s_waitcnt vmcnt(5)\n\t"
      "v_max3_u32 v234, v136, v137, v138\n\t"
      "v_max_u32_e32 v234, v234, v139\n\t"
      "v_cmp_eq_u32_e32 vcc, -1, v234\n\t"
      "s_cbranch_vccnz FQ30L_%=\n\t"
      "FQ30X_%=:\n\t"
      "s_branch FQ30B_%=\n\t"
      "FQ31R_%=:\n\t"
      "s_mov_b32 s51, 0\n\t"
      "FQ31L_%=:\n\t"
      "s_add_u32 s51, s51, 1\n\t"
      "s_cmp_lt_u32 s51, 262144\n\t"
      "s_cbranch_scc0 FQ31X_%=\n\t"
      "s_waitcnt vmcnt(0)\n\t"
      "s_sleep 4\n\t"
      "global_load_dwordx4 v[140:143], v227, s[60:61] sc1\n\t"
      "global_load_dwordx4 v[144:147], v228, s[60:61] sc1\n\t"
      "global_load_dwordx4 v[148:151], v229, s[60:61] sc1\n\t"
      "global_load_dwordx4 v[152:155], v230, s[60:61] sc1\n\t"
      "global_load_dwordx4 v[128:131], v224, s[62:63] sc1\n\t"
      "global_load_dwordx4 v[132:135], v225, s[62:63] sc1\n\t"
      "s_waitcnt vmcnt(5)\n\t"
      "v_max3_u32 v234, v140, v141, v142\n\t"
      "v_max_u32_e32 v234, v234, v143\n\t"
      "v_cmp_eq_u32_e32 vcc, -1, v234\n\t"
      "s_cbranch_vccnz FQ31L_%=\n\t"
      "FQ31X_%=:\n\t"
      "s_branch FQ31B_%=\n\t"
      "FQ32R_%=:\n\t"
      "s_mov_b32 s51, 0\n\t"
      "FQ32L_%=:\n\t"
      "s_add_u32 s51, s51, 1\n\t"
      "s_cmp_lt_u32 s51, 262144\n\t"
      "s_cbranch_scc0 FQ32X_%=\n\t"
      "s_waitcnt vmcnt(0)\n\t"
      "s_sleep 4\n\t"
      "global_load_dwordx4 v[144:147], v228, s[60:61] sc1\n\t"
      "global_load_dwordx4 v[148:151], v229, s[60:61] sc1\n\t"
      "global_load_dwordx4 v[152:155], v230, s[60:61] sc1\n\t"
      "global_load_dwordx4 v[128:131], v224, s[62:63] sc1\n\t"
      "global_load_dwordx4 v[132:135], v225, s[62:63] sc1\n\t"
      "global_load_dwordx4 v[136:139], v226, s[62:63] sc1\n\t"
      "s_waitcnt vmcnt(5)\n\t"
      "v_max3_u32 v234, v144, v145, v146\n\t"
      "v_max_u32_e32 v234, v234, v147\n\t"
      "v_cmp_eq_u32_e32 vcc, -1, v234\n\t"
      "s_cbranch_vccnz FQ32L_%=\n\t"
      "FQ32X_%=:\n\t"
      "s_branch FQ32B_%=\n\t"
      "FQ33R_%=:\n\t"
      "s_mov_b32 s51, 0\n\t"
      "FQ33L_%=:\n\t"
      "s_add_u32 s51, s51, 1\n\t"
      "s_cmp_lt_u32 s51, 262144\n\t"
      "s_cbranch_scc0 FQ33X_%=\n\t"
      "s_waitcnt vmcnt(0)\n\t"
      "s_sleep 4\n\t"
      "global_load_dwordx4 v[148:151], v229, s[60:61] sc1\n\t"
      "global_load_dwordx4 v[152:155], v230, s[60:61] sc1\n\t"
      "global_load_dwordx4 v[128:131], v224, s[62:63] sc1\n\t"
      "global_load_dwordx4 v[132:135], v225, s[62:63] sc1\n\t"
      "global_load_dwordx4 v[136:139], v226, s[62:63] sc1\n\t"
      "global_load_dwordx4 v[140:143], v227, s[62:63] sc1\n\t"
      "s_waitcnt vmcnt(5)\n\t"
      "v_max3_u32 v234, v148, v149, v150\n\t"
      "v_max_u32_e32 v234, v234, v151\n\t"
      "v_cmp_eq_u32_e32 vcc, -1, v234\n\t"
      "s_cbranch_vccnz FQ33L_%=\n\t"
      "FQ33X_%=:\n\t"
      "s_branch FQ33B_%=\n\t"
      "FQ34R_%=:\n\t"
      "s_mov_b32 s51, 0\n\t"
      "FQ34L_%=:\n\t"
      "s_add_u32 s51, s51, 1\n\t"
      "s_cmp_lt_u32 s51, 262144\n\t"
      "s_cbranch_scc0 FQ34X_%=\n\t"
      "s_waitcnt vmcnt(0)\n\t"
      "s_sleep 4\n\t"
      "global_load_dwordx4 v[152:155], v230, s[60:61] sc1\n\t"
      "global_load_dwordx4 v[128:131], v224, s[62:63] sc1\n\t"
      "global_load_dwordx4 v[132:135], v225, s[62:63] sc1\n\t"
      "global_load_dwordx4 v[136:139], v226, s[62:63] sc1\n\t"
      "global_load_dwordx4 v[140:143], v227, s[62:63] sc1\n\t"
      "global_load_dwordx4 v[144:147], v228, s[62:63] sc1\n\t"
      "s_waitcnt vmcnt(5)\n\t"
      "v_max3_u32 v234, v152, v153, v154\n\t"
      "v_max_u32_e32 v234, v234, v155\n\t"
      "v_cmp_eq_u32_e32 vcc, -1, v234\n\t"
      "s_cbranch_vccnz FQ34L_%=\n\t"
      "FQ34X_%=:\n\t"
      "s_branch FQ34B_%=\n\t"
      "FQ35R_%=:\n\t"
      "s_mov_b32 s51, 0\n\t"
      "FQ35L_%=:\n\t"
      "s_add_u32 s51, s51, 1\n\t"
      "s_cmp_lt_u32 s51, 262144\n\t"
      "s_cbranch_scc0 FQ35X_%=\n\t"
      "s_waitcnt vmcnt(0)\n\t"
      "s_sleep 4\n\t"
      "global_load_dwordx4 v[128:131], v224, s[62:63] sc1\n\t"
      "global_load_dwordx4 v[132:135], v225, s[62:63] sc1\n\t"
      "global_load_dwordx4 v[136:139], v226, s[62:63] sc1\n\t"
      "global_load_dwordx4 v[140:143], v227, s[62:63] sc1\n\t"
      "global_load_dwordx4 v[144:147], v228, s[62:63] sc1\n\t"
      "global_load_dwordx4 v[148:151], v229, s[62:63] sc1\n\t"
      "s_waitcnt vmcnt(5)\n\t"
      "v_max3_u32 v234, v128, v129, v130\n\t"
      "v_max_u32_e32 v234, v234, v131\n\t"
      "v_cmp_eq_u32_e32 vcc, -1, v234\n\t"
      "s_cbranch_vccnz FQ35L_%=\n\t"
      "FQ35X_%=:\n\t"
      "s_branch FQ35B_%=\n\t"
      "NQ4R_%=:\n\t"
      "s_mov_b32 s51, 0\n\t"
      "NQ4L_%=:\n\t"
      "s_add_u32 s51, s51, 1\n\t"
      "s_cmp_lt_u32 s51, 262144\n\t"
      "s_cbranch_scc0 NQ4X_%=\n\t"
      "s_waitcnt vmcnt(0)\n\t"
      "s_sleep 1\n\t"
      "global_load_dwordx4 v[128:131], v224, s[60:61] sc1\n\t"
      "global_load_dwordx4 v[132:135], v225, s[60:61] sc1\n\t"
      "global_load_dwordx4 v[136:139], v226, s[60:61] sc1\n\t"
      "global_load_dwordx4 v[140:143], v227, s[60:61] sc1\n\t"
      "global_load_dwordx4 v[144:147], v228, s[60:61] sc1\n\t"
      "global_load_dwordx4 v[148:151], v229, s[60:61] sc1\n\t"
      "global_load_dwordx4 v[152:155], v230, s[60:61] sc1\n\t"
      "s_waitcnt vmcnt(0)\n\t"
      "v_max3_u32 v234, v128, v129, v130\n\t"
      "v_max3_u32 v234, v234, v131, v132\n\t"
      "v_max3_u32 v234, v234, v133, v134\n\t"
      "v_max3_u32 v234, v234, v135, v136\n\t"
      "v_max3_u32 v234, v234, v137, v138\n\t"
      "v_max3_u32 v234, v234, v139, v140\n\t"
      "v_max3_u32 v234, v234, v141, v142\n\t"
      "v_max3_u32 v234, v234, v143, v144\n\t"
      "v_max3_u32 v234, v234, v145, v146\n\t"
      "v_max3_u32 v234, v234, v147, v148\n\t"
      "v_max3_u32 v234, v234, v149, v150\n\t"
      "v_max3_u32 v234, v234, v151, v152\n\t"
      "v_max3_u32 v234, v234, v153, v154\n\t"
      "v_max_u32_e32 v234, v234, v155\n\t"
      "v_cmp_eq_u32_e32 vcc, -1, v234\n\t"
      "s_cbranch_vccnz NQ4L_%=\n\t"
      "NQ4X_%=:\n\t"
      "v_mfma_f32_16x16x32_bf16 v[184:187], v[128:131], v[0:3], 0\n\t"
      "v_mfma_f32_16x16x32_bf16 v[188:191], v[128:131], v[4:7], 0\n\t"
      "v_mfma_f32_16x16x32_bf16 v[192:195], v[128:131], v[8:11], 0\n\t"
      "v_mfma_f32_16x16x32_bf16 v[196:199], v[128:131], v[12:15], 0\n\t"
      "v_mfma_f32_16x16x32_bf16 v[184:187], v[132:135], v[16:19], v[184:187]\n\t"
      "v_mfma_f32_16x16x32_bf16 v[188:191], v[132:135], v[20:23], v[188:191]\n\t"
      "v_mfma_f32_16x16x32_bf16 v[192:195], v[132:135], v[24:27], v[192:195]\n\t"
      "v_mfma_f32_16x16x32_bf16 v[196:199], v[132:135], v[28:31], v[196:199]\n\t"
      "v_mfma_f32_16x16x32_bf16 v[184:187], v[136:139], v[32:35], v[184:187]\n\t"
      "v_mfma_f32_16x16x32_bf16 v[188:191], v[136:139], v[36:39], v[188:191]\n\t"
      "v_mfma_f32_16x16x32_bf16 v[192:195], v[136:139], v[40:43], v[192:195]\n\t"
      "v_mfma_f32_16x16x32_bf16 v[196:199], v[136:139], v[44:47], v[196:199]\n\t"
      "v_mfma_f32_16x16x32_bf16 v[184:187], v[140:143], v[48:51], v[184:187]\n\t"
      "v_mfma_f32_16x16x32_bf16 v[188:191], v[140:143], v[52:55], v[188:191]\n\t"
      "v_mfma_f32_16x16x32_bf16 v[192:195], v[140:143], v[56:59], v[192:195]\n\t"
      "v_mfma_f32_16x16x32_bf16 v[196:199], v[140:143], v[60:63], v[196:199]\n\t"
      "v_mfma_f32_16x16x32_bf16 v[184:187], v[144:147], v[64:67], v[184:187]\n\t"
      "v_mfma_f32_16x16x32_bf16 v[188:191], v[144:147], v[68:71], v[188:191]\n\t"
      "v_mfma_f32_16x16x32_bf16 v[192:195], v[144:147], v[72:75], v[192:195]\n\t"
      "v_mfma_f32_16x16x32_bf16 v[196:199], v[144:147], v[76:79], v[196:199]\n\t"
      "v_mfma_f32_16x16x32_bf16 v[184:187], v[148:151], v[80:83], v[184:187]\n\t"
      "v_mfma_f32_16x16x32_bf16 v[188:191], v[148:151], v[84:87], v[188:191]\n\t"
      "v_mfma_f32_16x16x32_bf16 v[192:195], v[148:151], v[88:91], v[192:195]\n\t"
      "v_mfma_f32_16x16x32_bf16 v[196:199], v[148:151], v[92:95], v[196:199]\n\t"
      "v_mfma_f32_16x16x32_bf16 v[184:187], v[152:155], v[96:99], v[184:187]\n\t"
      "v_mfma_f32_16x16x32_bf16 v[188:191], v[152:155], v[100:103], v[188:191]\n\t"
      "v_mfma_f32_16x16x32_bf16 v[192:195], v[152:155], v[104:107], v[192:195]\n\t"
      "v_mfma_f32_16x16x32_bf16 v[196:199], v[152:155], v[108:111], v[196:199]\n\t"
      "global_load_dwordx4 v[128:131], v224, s[62:63] sc1\n\t"
      "global_load_dwordx4 v[132:135], v225, s[62:63] sc1\n\t"
      "global_load_dwordx4 v[136:139], v226, s[62:63] sc1\n\t"
      "global_load_dwordx4 v[140:143], v227, s[62:63] sc1\n\t"
      "global_load_dwordx4 v[144:147], v228, s[62:63] sc1\n\t"
      "global_load_dwordx4 v[148:151], v229, s[62:63] sc1\n\t"
      "global_load_dwordx4 v[152:155], v230, s[62:63] sc1\n\t"
      "s_waitcnt vmcnt(0)\n\t"
      "s_nop 7\n\t"
      "s_branch NQ4B_%=\n\t"
      "FQ36R_%=:\n\t"
      "s_mov_b32 s51, 0\n\t"
      "FQ36L_%=:\n\t"
      "s_add_u32 s51, s51, 1\n\t"
      "s_cmp_lt_u32 s51, 262144\n\t"
      "s_cbranch_scc0 FQ36X_%=\n\t"
      "s_waitcnt vmcnt(0)\n\t"
      "s_sleep 4\n\t"
      "global_load_dwordx4 v[132:135], v225, s[62:63] sc1\n\t"
      "global_load_dwordx4 v[136:139], v226, s[62:63] sc1\n\t"
      "global_load_dwordx4 v[140:143], v227, s[62:63] sc1\n\t"
      "global_load_dwordx4 v[144:147], v228, s[62:63] sc1\n\t"
      "global_load_dwordx4 v[148:151], v229, s[62:63] sc1\n\t"
      "global_load_dwordx4 v[152:155], v230, s[62:63] sc1\n\t"
      "s_waitcnt vmcnt(5)\n\t"
      "v_max3_u32 v234, v132, v133, v134\n\t"
      "v_max_u32_e32 v234, v234, v135\n\t"
      "v_cmp_eq_u32_e32 vcc, -1, v234\n\t"
      "s_cbranch_vccnz FQ36L_%=\n\t"
      "FQ36X_%=:\n\t"
      "s_branch FQ36B_%=\n\t"
      "FQ37R_%=:\n\t"
      "s_mov_b32 s51, 0\n\t"
      "FQ37L_%=:\n\t"
      "s_add_u32 s51, s51, 1\n\t"
      "s_cmp_lt_u32 s51, 262144\n\t"
      "s_cbranch_scc0 FQ37X_%=\n\t"
      "s_waitcnt vmcnt(0)\n\t"
      "s_sleep 4\n\t"
      "global_load_dwordx4 v[136:139], v226, s[62:63] sc1\n\t"
      "global_load_dwordx4 v[140:143], v227, s[62:63] sc1\n\t"
      "global_load_dwordx4 v[144:147], v228, s[62:63] sc1\n\t"
      "global_load_dwordx4 v[148:151], v229, s[62:63] sc1\n\t"
      "global_load_dwordx4 v[152:155], v230, s[62:63] sc1\n\t"
      "global_load_dwordx4 v[128:131], v224, s[64:65] sc1\n\t"
      "s_waitcnt vmcnt(5)\n\t"
      "v_max3_u32 v234, v136, v137, v138\n\t"
      "v_max_u32_e32 v234, v234, v139\n\t"
      "v_cmp_eq_u32_e32 vcc, -1, v234\n\t"
      "s_cbranch_vccnz FQ37L_%=\n\t"
      "FQ37X_%=:\n\t"
      "s_branch FQ37B_%=\n\t"
      "FQ38R_%=:\n\t"
      "s_mov_b32 s51, 0\n\t"
      "FQ38L_%=:\n\t"
      "s_add_u32 s51, s51, 1\n\t"
      "s_cmp_lt_u32 s51, 262144\n\t"
      "s_cbranch_scc0 FQ38X_%=\n\t"
      "s_waitcnt vmcnt(0)\n\t"
      "s_sleep 4\n\t"
      "global_load_dwordx4 v[140:143], v227, s[62:63] sc1\n\t"
      "global_load_dwordx4 v[144:147], v228, s[62:63] sc1\n\t"
      "global_load_dwordx4 v[148:151], v229, s[62:63] sc1\n\t"
      "global_load_dwordx4 v[152:155], v230, s[62:63] sc1\n\t"
      "global_load_dwordx4 v[128:131], v224, s[64:65] sc1\n\t"
      "global_load_dwordx4 v[132:135], v225, s[64:65] sc1\n\t"
      "s_waitcnt vmcnt(5)\n\t"
      "v_max3_u32 v234, v140, v141, v142\n\t"
      "v_max_u32_e32 v234, v234, v143\n\t"
      "v_cmp_eq_u32_e32 vcc, -1, v234\n\t"
      "s_cbranch_vccnz FQ38L_%=\n\t"
      "FQ38X_%=:\n\t"
      "s_branch FQ38B_%=\n\t"
      "FQ39R_%=:\n\t"
      "s_mov_b32 s51, 0\n\t"
      "FQ39L_%=:\n\t"
      "s_add_u32 s51, s51, 1\n\t"
      "s_cmp_lt_u32 s51, 262144\n\t"
      "s_cbranch_scc0 FQ39X_%=\n\t"
      "s_waitcnt vmcnt(0)\n\t"
      "s_sleep 4\n\t"
      "global_load_dwordx4 v[144:147], v228, s[62:63] sc1\n\t"
      "global_load_dwordx4 v[148:151], v229, s[62:63] sc1\n\t"
      "global_load_dwordx4 v[152:155], v230, s[62:63] sc1\n\t"
      "global_load_dwordx4 v[128:131], v224, s[64:65] sc1\n\t"
      "global_load_dwordx4 v[132:135], v225, s[64:65] sc1\n\t"
      "global_load_dwordx4 v[136:139], v226, s[64:65] sc1\n\t"
      "s_waitcnt vmcnt(5)\n\t"
      "v_max3_u32 v234, v144, v145, v146\n\t"
      "v_max_u32_e32 v234, v234, v147\n\t"
      "v_cmp_eq_u32_e32 vcc, -1, v234\n\t"
      "s_cbranch_vccnz FQ39L_%=\n\t"
      "FQ39X_%=:\n\t"
      "s_branch FQ39B_%=\n\t"
      "FQ40R_%=:\n\t"
      "s_mov_b32 s51, 0\n\t"
      "FQ40L_%=:\n\t"
      "s_add_u32 s51, s51, 1\n\t"
      "s_cmp_lt_u32 s51, 262144\n\t"
      "s_cbranch_scc0 FQ40X_%=\n\t"
      "s_waitcnt vmcnt(0)\n\t"
      "s_sleep 4\n\t"
      "global_load_dwordx4 v[148:151], v229, s[62:63] sc1\n\t"
      "global_load_dwordx4 v[152:155], v230, s[62:63] sc1\n\t"
      "global_load_dwordx4 v[128:131], v224, s[64:65] sc1\n\t"
      "global_load_dwordx4 v[132:135], v225, s[64:65] sc1\n\t"
      "global_load_dwordx4 v[136:139], v226, s[64:65] sc1\n\t"
      "global_load_dwordx4 v[140:143], v227, s[64:65] sc1\n\t"
      "s_waitcnt vmcnt(5)\n\t"
      "v_max3_u32 v234, v148, v149, v150\n\t"
      "v_max_u32_e32 v234, v234, v151\n\t"
      "v_cmp_eq_u32_e32 vcc, -1, v234\n\t"
      "s_cbranch_vccnz FQ40L_%=\n\t"
      "FQ40X_%=:\n\t"
      "s_branch FQ40B_%=\n\t"
      "FQ41R_%=:\n\t"
      "s_mov_b32 s51, 0\n\t"
      "FQ41L_%=:\n\t"
      "s_add_u32 s51, s51, 1\n\t"
      "s_cmp_lt_u32 s51, 262144\n\t"
      "s_cbranch_scc0 FQ41X_%=\n\t"
      "s_waitcnt vmcnt(0)\n\t"
      "s_sleep 4\n\t"
      "global_load_dwordx4 v[152:155], v230, s[62:63] sc1\n\t"
      "global_load_dwordx4 v[128:131], v224, s[64:65] sc1\n\t"
      "global_load_dwordx4 v[132:135], v225, s[64:65] sc1\n\t"
      "global_load_dwordx4 v[136:139], v226, s[64:65] sc1\n\t"
      "global_load_dwordx4 v[140:143], v227, s[64:65] sc1\n\t"
      "global_load_dwordx4 v[144:147], v228, s[64:65] sc1\n\t"
      "s_waitcnt vmcnt(5)\n\t"
      "v_max3_u32 v234, v152, v153, v154\n\t"
      "v_max_u32_e32 v234, v234, v155\n\t"
      "v_cmp_eq_u32_e32 vcc, -1, v234\n\t"
      "s_cbranch_vccnz FQ41L_%=\n\t"
      "FQ41X_%=:\n\t"
      "s_branch FQ41B_%=\n\t"
      "FQ42R_%=:\n\t"
      "s_mov_b32 s51, 0\n\t"
      "FQ42L_%=:\n\t"
      "s_add_u32 s51, s51, 1\n\t"
      "s_cmp_lt_u32 s51, 262144\n\t"
      "s_cbranch_scc0 FQ42X_%=\n\t"
      "s_sleep 1\n\t"
      "s_mov_b64 exec, 15\n\t"
      "v_mov_b64_e32 v[232:233], 0\n\t"
      "global_atomic_or_x2 v[232:233], v247, v[232:233], s[12:13] sc0 sc1\n\t"
      "s_waitcnt vmcnt(0)\n\t"
      "v_subrev_u32_e32 v232, s14, v232\n\t"
      "v_cmp_ne_u32_e64 s[48:49], s15, v233\n\t"
      "v_subrev_u32_e32 v232, 6, v232\n\t"
      "v_cmp_lt_u32_e32 vcc, 7, v232\n\t"
      "s_mov_b64 exec, -1\n\t"
      "s_or_b64 vcc, vcc, s[48:49]\n\t"
      "s_cbranch_vccnz FQ42L_%=\n\t"
      "FQ42X_%=:\n\t"
      "FQ42DR_%=:\n\t"
      "s_mov_b32 s51, 0\n\t"
      "FQ42DL_%=:\n\t"
      "s_add_u32 s51, s51, 1\n\t"
      "s_cmp_lt_u32 s51, 262144\n\t"
      "s_cbranch_scc0 FQ42DX_%=\n\t"
      "s_waitcnt vmcnt(0)\n\t"
      "s_sleep 1\n\t"
      "global_load_dwordx4 v[128:131], v224, s[64:65] sc1\n\t"
      "global_load_dwordx4 v[132:135], v225, s[64:65] sc1\n\t"
      "global_load_dwordx4 v[136:139], v226, s[64:65] sc1\n\t"
      "global_load_dwordx4 v[140:143], v227, s[64:65] sc1\n\t"
      "global_load_dwordx4 v[144:147], v228, s[64:65] sc1\n\t"
      "global_load_dwordx4 v[148:151], v229, s[64:65] sc1\n\t"
      "s_waitcnt vmcnt(5)\n\t"
      "v_max3_u32 v234, v128, v129, v130\n\t"
      "v_max_u32_e32 v234, v234, v131\n\t"
      "v_cmp_eq_u32_e32 vcc, -1, v234\n\t"
      "s_cbranch_vccnz FQ42DL_%=\n\t"
      "FQ42DX_%=:\n\t"
      "s_branch FQ42B_%=\n\t"
      "NQ5R_%=:\n\t"
      "s_mov_b32 s51, 0\n\t"
      "NQ5L_%=:\n\t"
      "s_add_u32 s51, s51, 1\n\t"
      "s_cmp_lt_u32 s51, 262144\n\t"
      "s_cbranch_scc0 NQ5X_%=\n\t"
      "s_waitcnt vmcnt(0)\n\t"
      "s_sleep 1\n\t"
      "global_load_dwordx4 v[128:131], v224, s[62:63] sc1\n\t"
      "global_load_dwordx4 v[132:135], v225, s[62:63] sc1\n\t"
      "global_load_dwordx4 v[136:139], v226, s[62:63] sc1\n\t"
      "global_load_dwordx4 v[140:143], v227, s[62:63] sc1\n\t"
      "global_load_dwordx4 v[144:147], v228, s[62:63] sc1\n\t"
      "global_load_dwordx4 v[148:151], v229, s[62:63] sc1\n\t"
      "global_load_dwordx4 v[152:155], v230, s[62:63] sc1\n\t"
      "s_waitcnt vmcnt(0)\n\t"
      "s_nop 0\n\t"
      "v_max3_u32 v234, v128, v129, v130\n\t"
      "v_max3_u32 v234, v234, v131, v132\n\t"
      "v_max3_u32 v234, v234, v133, v134\n\t"
      "v_max3_u32 v234, v234, v135, v136\n\t"
      "v_max3_u32 v234, v234, v137, v138\n\t"
      "v_max3_u32 v234, v234, v139, v140\n\t"
      "v_max3_u32 v234, v234, v141, v142\n\t"
      "v_max3_u32 v234, v234, v143, v144\n\t"
      "v_max3_u32 v234, v234, v145, v146\n\t"
      "v_max3_u32 v234, v234, v147, v148\n\t"
      "v_max3_u32 v234, v234, v149, v150\n\t"
      "v_max3_u32 v234, v234, v151, v152\n\t"
      "v_max3_u32 v234, v234, v153, v154\n\t"
      "v_max_u32_e32 v234, v234, v155\n\t"
      "v_cmp_eq_u32_e32 vcc, -1, v234\n\t"
      "s_cbranch_vccnz NQ5L_%=\n\t"
      "NQ5X_%=:\n\t"
      "v_mfma_f32_16x16x32_bf16 v[200:203], v[128:131], v[0:3], 0\n\t"
      "v_mfma_f32_16x16x32_bf16 v[204:207], v[128:131], v[4:7], 0\n\t"
      "v_mfma_f32_16x16x32_bf16 v[208:211], v[128:131], v[8:11], 0\n\t"
      "v_mfma_f32_16x16x32_bf16 v[212:215], v[128:131], v[12:15], 0\n\t"
      "v_mfma_f32_16x16x32_bf16 v[200:203], v[132:135], v[16:19], v[200:203]\n\t"
      "v_mfma_f32_16x16x32_bf16 v[204:207], v[132:135], v[20:23], v[204:207]\n\t"
      "v_mfma_f32_16x16x32_bf16 v[208:211], v[132:135], v[24:27], v[208:211]\n\t"
      "v_mfma_f32_16x16x32_bf16 v[212:215], v[132:135], v[28:31], v[212:215]\n\t"
      "v_mfma_f32_16x16x32_bf16 v[200:203], v[136:139], v[32:35], v[200:203]\n\t"
      "v_mfma_f32_16x16x32_bf16 v[204:207], v[136:139], v[36:39], v[204:207]\n\t"
      "v_mfma_f32_16x16x32_bf16 v[208:211], v[136:139], v[40:43], v[208:211]\n\t"
      "v_mfma_f32_16x16x32_bf16 v[212:215], v[136:139], v[44:47], v[212:215]\n\t"
      "v_mfma_f32_16x16x32_bf16 v[200:203], v[140:143], v[48:51], v[200:203]\n\t"
      "v_mfma_f32_16x16x32_bf16 v[204:207], v[140:143], v[52:55], v[204:207]\n\t"
      "v_mfma_f32_16x16x32_bf16 v[208:211], v[140:143], v[56:59], v[208:211]\n\t"
      "v_mfma_f32_16x16x32_bf16 v[212:215], v[140:143], v[60:63], v[212:215]\n\t"
      "v_mfma_f32_16x16x32_bf16 v[200:203], v[144:147], v[64:67], v[200:203]\n\t"
      "v_mfma_f32_16x16x32_bf16 v[204:207], v[144:147], v[68:71], v[204:207]\n\t"
      "v_mfma_f32_16x16x32_bf16 v[208:211], v[144:147], v[72:75], v[208:211]\n\t"
      "v_mfma_f32_16x16x32_bf16 v[212:215], v[144:147], v[76:79], v[212:215]\n\t"
      "v_mfma_f32_16x16x32_bf16 v[200:203], v[148:151], v[80:83], v[200:203]\n\t"
      "v_mfma_f32_16x16x32_bf16 v[204:207], v[148:151], v[84:87], v[204:207]\n\t"
      "v_mfma_f32_16x16x32_bf16 v[208:211], v[148:151], v[88:91], v[208:211]\n\t"
      "v_mfma_f32_16x16x32_bf16 v[212:215], v[148:151], v[92:95], v[212:215]\n\t"
      "v_mfma_f32_16x16x32_bf16 v[200:203], v[152:155], v[96:99], v[200:203]\n\t"
      "v_mfma_f32_16x16x32_bf16 v[204:207], v[152:155], v[100:103], v[204:207]\n\t"
      "v_mfma_f32_16x16x32_bf16 v[208:211], v[152:155], v[104:107], v[208:211]\n\t"
      "v_mfma_f32_16x16x32_bf16 v[212:215], v[152:155], v[108:111], v[212:215]\n\t"
      "global_load_dwordx4 v[128:131], v224, s[64:65] sc1\n\t"
      "global_load_dwordx4 v[132:135], v225, s[64:65] sc1\n\t"
      "global_load_dwordx4 v[136:139], v226, s[64:65] sc1\n\t"
      "global_load_dwordx4 v[140:143], v227, s[64:65] sc1\n\t"
      "global_load_dwordx4 v[144:147], v228, s[64:65] sc1\n\t"
      "global_load_dwordx4 v[148:151], v229, s[64:65] sc1\n\t"
      "global_load_dwordx4 v[152:155], v230, s[64:65] sc1\n\t"
      "s_waitcnt vmcnt(0)\n\t"
      "s_nop 7\n\t"
      "s_branch NQ5B_%=\n\t"
      "FQ43R_%=:\n\t"
      "s_mov_b32 s51, 0\n\t"
      "FQ43L_%=:\n\t"
      "s_add_u32 s51, s51, 1\n\t"
      "s_cmp_lt_u32 s51, 262144\n\t"
      "s_cbranch_scc0 FQ43X_%=\n\t"
      "s_sleep 1\n\t"
      "s_mov_b64 exec, 15\n\t"
      "v_mov_b64_e32 v[232:233], 0\n\t"
      "global_atomic_or_x2 v[232:233], v247, v[232:233], s[12:13] sc0 sc1\n\t"
      "s_waitcnt vmcnt(0)\n\t"
      "v_subrev_u32_e32 v232, s14, v232\n\t"
      "v_cmp_ne_u32_e64 s[48:49], s15, v233\n\t"
      "v_subrev_u32_e32 v232, 6, v232\n\t"
      "v_cmp_lt_u32_e32 vcc, 7, v232\n\t"
      "s_mov_b64 exec, -1\n\t"
      "s_or_b64 vcc, vcc, s[48:49]\n\t"
      "s_cbranch_vccnz FQ43L_%=\n\t"
      "FQ43X_%=:\n\t"
      "FQ43DR_%=:\n\t"
      "s_mov_b32 s51, 0\n\t"
      "FQ43DL_%=:\n\t"
      "s_add_u32 s51, s51, 1\n\t"
      "s_cmp_lt_u32 s51, 262144\n\t"
      "s_cbranch_scc0 FQ43DX_%=\n\t"
      "s_waitcnt vmcnt(0)\n\t"
      "s_sleep 1\n\t"
      "global_load_dwordx4 v[132:135], v225, s[64:65] sc1\n\t"
      "global_load_dwordx4 v[136:139], v226, s[64:65] sc1\n\t"
      "global_load_dwordx4 v[140:143], v227, s[64:65] sc1\n\t"
      "global_load_dwordx4 v[144:147], v228, s[64:65] sc1\n\t"
      "global_load_dwordx4 v[148:151], v229, s[64:65] sc1\n\t"
      "global_load_dwordx4 v[152:155], v230, s[64:65] sc1\n\t"
      "s_waitcnt vmcnt(5)\n\t"
      "v_max3_u32 v234, v132, v133, v134\n\t"
      "v_max_u32_e32 v234, v234, v135\n\t"
      "v_cmp_eq_u32_e32 vcc, -1, v234\n\t"
      "s_cbranch_vccnz FQ43DL_%=\n\t"
      "FQ43DX_%=:\n\t"
      "s_branch FQ43B_%=\n\t"
      "FQ44R_%=:\n\t"
      "s_mov_b32 s51, 0\n\t"
      "FQ44L_%=:\n\t"
      "s_add_u32 s51, s51, 1\n\t"
      "s_cmp_lt_u32 s51, 262144\n\t"
      "s_cbranch_scc0 FQ44X_%=\n\t"
      "s_sleep 1\n\t"
      "s_mov_b64 exec, 15\n\t"
      "v_mov_b64_e32 v[232:233], 0\n\t"
      "global_atomic_or_x2 v[232:233], v247, v[232:233], s[12:13] sc0 sc1\n\t"
      "s_waitcnt vmcnt(0)\n\t"
      "v_subrev_u32_e32 v232, s14, v232\n\t"
      "v_cmp_ne_u32_e64 s[48:49], s15, v233\n\t"
      "v_subrev_u32_e32 v232, 6, v232\n\t"
      "v_cmp_lt_u32_e32 vcc, 7, v232\n\t"
      "s_mov_b64 exec, -1\n\t"
      "s_or_b64 vcc, vcc, s[48:49]\n\t"
      "s_cbranch_vccnz FQ44L_%=\n\t"
      "FQ44X_%=:\n\t"
      "FQ44DR_%=:\n\t"
      "s_mov_b32 s51, 0\n\t"
      "FQ44DL_%=:\n\t"
      "s_add_u32 s51, s51, 1\n\t"
      "s_cmp_lt_u32 s51, 262144\n\t"
      "s_cbranch_scc0 FQ44DX_%=\n\t"
      "s_waitcnt vmcnt(0)\n\t"
      "s_sleep 1\n\t"
      "global_load_dwordx4 v[136:139], v226, s[64:65] sc1\n\t"
      "global_load_dwordx4 v[140:143], v227, s[64:65] sc1\n\t"
      "global_load_dwordx4 v[144:147], v228, s[64:65] sc1\n\t"
      "global_load_dwordx4 v[148:151], v229, s[64:65] sc1\n\t"
      "global_load_dwordx4 v[152:155], v230, s[64:65] sc1\n\t"
      "s_waitcnt vmcnt(4)\n\t"
      "v_max3_u32 v234, v136, v137, v138\n\t"
      "v_max_u32_e32 v234, v234, v139\n\t"
      "v_cmp_eq_u32_e32 vcc, -1, v234\n\t"
      "s_cbranch_vccnz FQ44DL_%=\n\t"
      "FQ44DX_%=:\n\t"
      "s_branch FQ44B_%=\n\t"
      "FQ45R_%=:\n\t"
      "s_mov_b32 s51, 0\n\t"
      "FQ45L_%=:\n\t"
      "s_add_u32 s51, s51, 1\n\t"
      "s_cmp_lt_u32 s51, 262144\n\t"
      "s_cbranch_scc0 FQ45X_%=\n\t"
      "s_sleep 1\n\t"
      "s_mov_b64 exec, 15\n\t"
      "v_mov_b64_e32 v[232:233], 0\n\t"
      "global_atomic_or_x2 v[232:233], v247, v[232:233], s[12:13] sc0 sc1\n\t"
      "s_waitcnt vmcnt(0)\n\t"
      "v_subrev_u32_e32 v232, s14, v232\n\t"
      "v_cmp_ne_u32_e64 s[48:49], s15, v233\n\t"
      "v_subrev_u32_e32 v232, 6, v232\n\t"
      "v_cmp_lt_u32_e32 vcc, 7, v232\n\t"
      "s_mov_b64 exec, -1\n\t"
      "s_or_b64 vcc, vcc, s[48:49]\n\t"
      "s_cbranch_vccnz FQ45L_%=\n\t"
      "FQ45X_%=:\n\t"
      "FQ45DR_%=:\n\t"
      "s_mov_b32 s51, 0\n\t"
      "FQ45DL_%=:\n\t"
      "s_add_u32 s51, s51, 1\n\t"
      "s_cmp_lt_u32 s51, 262144\n\t"
      "s_cbranch_scc0 FQ45DX_%=\n\t"
      "s_waitcnt vmcnt(0)\n\t"
      "s_sleep 1\n\t"
      "global_load_dwordx4 v[140:143], v227, s[64:65] sc1\n\t"
      "global_load_dwordx4 v[144:147], v228, s[64:65] sc1\n\t"
      "global_load_dwordx4 v[148:151], v229, s[64:65] sc1\n\t"
      "global_load_dwordx4 v[152:155], v230, s[64:65] sc1\n\t"
      "s_waitcnt vmcnt(3)\n\t"
      "v_max3_u32 v234, v140, v141, v142\n\t"
      "v_max_u32_e32 v234, v234, v143\n\t"
      "v_cmp_eq_u32_e32 vcc, -1, v234\n\t"
      "s_cbranch_vccnz FQ45DL_%=\n\t"
      "FQ45DX_%=:\n\t"
      "s_branch FQ45B_%=\n\t"
      "FQ46R_%=:\n\t"
      "s_mov_b32 s51, 0\n\t"
      "FQ46L_%=:\n\t"
      "s_add_u32 s51, s51, 1\n\t"
      "s_cmp_lt_u32 s51, 262144\n\t"
      "s_cbranch_scc0 FQ46X_%=\n\t"
      "s_sleep 1\n\t"
      "s_mov_b64 exec, 15\n\t"
      "v_mov_b64_e32 v[232:233], 0\n\t"
      "global_atomic_or_x2 v[232:233], v247, v[232:233], s[12:13] sc0 sc1\n\t"
      "s_waitcnt vmcnt(0)\n\t"
      "v_subrev_u32_e32 v232, s14, v232\n\t"
      "v_cmp_ne_u32_e64 s[48:49], s15, v233\n\t"
      "v_subrev_u32_e32 v232, 6, v232\n\t"
      "v_cmp_lt_u32_e32 vcc, 7, v232\n\t"
      "s_mov_b64 exec, -1\n\t"
      "s_or_b64 vcc, vcc, s[48:49]\n\t"
      "s_cbranch_vccnz FQ46L_%=\n\t"
      "FQ46X_%=:\n\t"
      "FQ46DR_%=:\n\t"
      "s_mov_b32 s51, 0\n\t"
      "FQ46DL_%=:\n\t"
      "s_add_u32 s51, s51, 1\n\t"
      "s_cmp_lt_u32 s51, 262144\n\t"
      "s_cbranch_scc0 FQ46DX_%=\n\t"
      "s_waitcnt vmcnt(0)\n\t"
      "s_sleep 1\n\t"
      "global_load_dwordx4 v[144:147], v228, s[64:65] sc1\n\t"
      "global_load_dwordx4 v[148:151], v229, s[64:65] sc1\n\t"
      "global_load_dwordx4 v[152:155], v230, s[64:65] sc1\n\t"
      "s_waitcnt vmcnt(2)\n\t"
      "v_max3_u32 v234, v144, v145, v146\n\t"
      "v_max_u32_e32 v234, v234, v147\n\t"
      "v_cmp_eq_u32_e32 vcc, -1, v234\n\t"
      "s_cbranch_vccnz FQ46DL_%=\n\t"
      "FQ46DX_%=:\n\t"
      "s_branch FQ46B_%=\n\t"
      "FQ47R_%=:\n\t"
      "s_mov_b32 s51, 0\n\t"
      "FQ47L_%=:\n\t"
      "s_add_u32 s51, s51, 1\n\t"
      "s_cmp_lt_u32 s51, 262144\n\t"
      "s_cbranch_scc0 FQ47X_%=\n\t"
      "s_sleep 1\n\t"
      "s_mov_b64 exec, 15\n\t"
      "v_mov_b64_e32 v[232:233], 0\n\t"
      "global_atomic_or_x2 v[232:233], v247, v[232:233], s[12:13] sc0 sc1\n\t"
      "s_waitcnt vmcnt(0)\n\t"
      "v_subrev_u32_e32 v232, s14, v232\n\t"
      "v_cmp_ne_u32_e64 s[48:49], s15, v233\n\t"
      "v_subrev_u32_e32 v232, 6, v232\n\t"
      "v_cmp_lt_u32_e32 vcc, 7, v232\n\t"
      "s_mov_b64 exec, -1\n\t"
      "s_or_b64 vcc, vcc, s[48:49]\n\t"
      "s_cbranch_vccnz FQ47L_%=\n\t"
      "FQ47X_%=:\n\t"
      "FQ47DR_%=:\n\t"
      "s_mov_b32 s51, 0\n\t"
      "FQ47DL_%=:\n\t"
      "s_add_u32 s51, s51, 1\n\t"
      "s_cmp_lt_u32 s51, 262144\n\t"
      "s_cbranch_scc0 FQ47DX_%=\n\t"
      "s_waitcnt vmcnt(0)\n\t"
      "s_sleep 1\n\t"
      "global_load_dwordx4 v[148:151], v229, s[64:65] sc1\n\t"
      "global_load_dwordx4 v[152:155], v230, s[64:65] sc1\n\t"
      "s_waitcnt vmcnt(1)\n\t"
      "v_max3_u32 v234, v148, v149, v150\n\t"
      "v_max_u32_e32 v234, v234, v151\n\t"
      "v_cmp_eq_u32_e32 vcc, -1, v234\n\t"
      "s_cbranch_vccnz FQ47DL_%=\n\t"
      "FQ47DX_%=:\n\t"
      "s_branch FQ47B_%=\n\t"
      "FQ48R_%=:\n\t"
      "s_mov_b32 s51, 0\n\t"
      "FQ48L_%=:\n\t"
      "s_add_u32 s51, s51, 1\n\t"
      "s_cmp_lt_u32 s51, 262144\n\t"
      "s_cbranch_scc0 FQ48X_%=\n\t"
      "s_sleep 1\n\t"
      "s_mov_b64 exec, 15\n\t"
      "v_mov_b64_e32 v[232:233], 0\n\t"
      "global_atomic_or_x2 v[232:233], v247, v[232:233], s[12:13] sc0 sc1\n\t"
      "s_waitcnt vmcnt(0)\n\t"
      "v_subrev_u32_e32 v232, s14, v232\n\t"
      "v_cmp_ne_u32_e64 s[48:49], s15, v233\n\t"
      "v_subrev_u32_e32 v232, 6, v232\n\t"
      "v_cmp_lt_u32_e32 vcc, 7, v232\n\t"
      "s_mov_b64 exec, -1\n\t"
      "s_or_b64 vcc, vcc, s[48:49]\n\t"
      "s_cbranch_vccnz FQ48L_%=\n\t"
      "FQ48X_%=:\n\t"
      "FQ48DR_%=:\n\t"
      "s_mov_b32 s51, 0\n\t"
      "FQ48DL_%=:\n\t"
      "s_add_u32 s51, s51, 1\n\t"
      "s_cmp_lt_u32 s51, 262144\n\t"
      "s_cbranch_scc0 FQ48DX_%=\n\t"
      "s_waitcnt vmcnt(0)\n\t"
      "s_sleep 1\n\t"
      "global_load_dwordx4 v[152:155], v230, s[64:65] sc1\n\t"
      "s_waitcnt vmcnt(0)\n\t"
      "v_max3_u32 v234, v152, v153, v154\n\t"
      "v_max_u32_e32 v234, v234, v155\n\t"
      "v_cmp_eq_u32_e32 vcc, -1, v234\n\t"
      "s_cbranch_vccnz FQ48DL_%=\n\t"
      "FQ48DX_%=:\n\t"
      "s_branch FQ48B_%=\n\t"
      "NQ6R_%=:\n\t"
      "s_mov_b32 s51, 0\n\t"
      "NQ6L_%=:\n\t"
      "s_add_u32 s51, s51, 1\n\t"
      "s_cmp_lt_u32 s51, 262144\n\t"
      "s_cbranch_scc0 NQ6X_%=\n\t"
      "s_waitcnt vmcnt(0)\n\t"
      "s_sleep 1\n\t"
      "global_load_dwordx4 v[128:131], v224, s[64:65] sc1\n\t"
      "global_load_dwordx4 v[132:135], v225, s[64:65] sc1\n\t"
      "global_load_dwordx4 v[136:139], v226, s[64:65] sc1\n\t"
      "global_load_dwordx4 v[140:143], v227, s[64:65] sc1\n\t"
      "global_load_dwordx4 v[144:147], v228, s[64:65] sc1\n\t"
      "global_load_dwordx4 v[148:151], v229, s[64:65] sc1\n\t"
      "global_load_dwordx4 v[152:155], v230, s[64:65] sc1\n\t"
      "s_waitcnt vmcnt(0)\n\t"
      "v_max3_u32 v234, v128, v129, v130\n\t"
      "v_max3_u32 v234, v234, v131, v132\n\t"
      "v_max3_u32 v234, v234, v133, v134\n\t"
      "v_max3_u32 v234, v234, v135, v136\n\t"
      "v_max3_u32 v234, v234, v137, v138\n\t"
      "v_max3_u32 v234, v234, v139, v140\n\t"
      "v_max3_u32 v234, v234, v141, v142\n\t"
      "v_max3_u32 v234, v234, v143, v144\n\t"
      "v_max3_u32 v234, v234, v145, v146\n\t"
      "v_max3_u32 v234, v234, v147, v148\n\t"
      "v_max3_u32 v234, v234, v149, v150\n\t"
      "v_max3_u32 v234, v234, v151, v152\n\t"
      "v_max3_u32 v234, v234, v153, v154\n\t"
      "v_max_u32_e32 v234, v234, v155\n\t"
      "v_cmp_eq_u32_e32 vcc, -1, v234\n\t"
      "s_cbranch_vccnz NQ6L_%=\n\t"
      "NQ6X_%=:\n\t"
      "v_mfma_f32_16x16x32_bf16 v[184:187], v[128:131], v[0:3], 0\n\t"
      "v_mfma_f32_16x16x32_bf16 v[188:191], v[128:131], v[4:7], 0\n\t"
      "v_mfma_f32_16x16x32_bf16 v[192:195], v[128:131], v[8:11], 0\n\t"
      "v_mfma_f32_16x16x32_bf16 v[196:199], v[128:131], v[12:15], 0\n\t"
      "v_mfma_f32_16x16x32_bf16 v[184:187], v[132:135], v[16:19], v[184:187]\n\t"
      "v_mfma_f32_16x16x32_bf16 v[188:191], v[132:135], v[20:23], v[188:191]\n\t"
      "v_mfma_f32_16x16x32_bf16 v[192:195], v[132:135], v[24:27], v[192:195]\n\t"
      "v_mfma_f32_16x16x32_bf16 v[196:199], v[132:135], v[28:31], v[196:199]\n\t"
      "v_mfma_f32_16x16x32_bf16 v[184:187], v[136:139], v[32:35], v[184:187]\n\t"
      "v_mfma_f32_16x16x32_bf16 v[188:191], v[136:139], v[36:39], v[188:191]\n\t"
      "v_mfma_f32_16x16x32_bf16 v[192:195], v[136:139], v[40:43], v[192:195]\n\t"
      "v_mfma_f32_16x16x32_bf16 v[196:199], v[136:139], v[44:47], v[196:199]\n\t"
      "v_mfma_f32_16x16x32_bf16 v[184:187], v[140:143], v[48:51], v[184:187]\n\t"
      "v_mfma_f32_16x16x32_bf16 v[188:191], v[140:143], v[52:55], v[188:191]\n\t"
      "v_mfma_f32_16x16x32_bf16 v[192:195], v[140:143], v[56:59], v[192:195]\n\t"
      "v_mfma_f32_16x16x32_bf16 v[196:199], v[140:143], v[60:63], v[196:199]\n\t"
      "v_mfma_f32_16x16x32_bf16 v[184:187], v[144:147], v[64:67], v[184:187]\n\t"
      "v_mfma_f32_16x16x32_bf16 v[188:191], v[144:147], v[68:71], v[188:191]\n\t"
      "v_mfma_f32_16x16x32_bf16 v[192:195], v[144:147], v[72:75], v[192:195]\n\t"
      "v_mfma_f32_16x16x32_bf16 v[196:199], v[144:147], v[76:79], v[196:199]\n\t"
      "v_mfma_f32_16x16x32_bf16 v[184:187], v[148:151], v[80:83], v[184:187]\n\t"
      "v_mfma_f32_16x16x32_bf16 v[188:191], v[148:151], v[84:87], v[188:191]\n\t"
      "v_mfma_f32_16x16x32_bf16 v[192:195], v[148:151], v[88:91], v[192:195]\n\t"
      "v_mfma_f32_16x16x32_bf16 v[196:199], v[148:151], v[92:95], v[196:199]\n\t"
      "v_mfma_f32_16x16x32_bf16 v[184:187], v[152:155], v[96:99], v[184:187]\n\t"
      "v_mfma_f32_16x16x32_bf16 v[188:191], v[152:155], v[100:103], v[188:191]\n\t"
      "v_mfma_f32_16x16x32_bf16 v[192:195], v[152:155], v[104:107], v[192:195]\n\t"
      "v_mfma_f32_16x16x32_bf16 v[196:199], v[152:155], v[108:111], v[196:199]\n\t"
      "s_waitcnt vmcnt(0)\n\t"
      "s_nop 7\n\t"
      "s_branch NQ6B_%=\n\t"
      "NP0R_%=:\n\t"
      "s_mov_b32 s51, 0\n\t"
      "NP0L_%=:\n\t"
      "s_add_u32 s51, s51, 1\n\t"
      "s_cmp_lt_u32 s51, 262144\n\t"
      "s_cbranch_scc0 NP0X_%=\n\t"
      "s_waitcnt vmcnt(0)\n\t"
      "s_sleep 1\n\t"
      "global_load_dwordx4 v[128:131], v224, s[52:53] sc1\n\t"
      "global_load_dwordx4 v[132:135], v225, s[52:53] sc1\n\t"
      "global_load_dwordx4 v[136:139], v226, s[52:53] sc1\n\t"
      "global_load_dwordx4 v[140:143], v227, s[52:53] sc1\n\t"
      "global_load_dwordx4 v[144:147], v228, s[52:53] sc1\n\t"
      "global_load_dwordx4 v[148:151], v229, s[52:53] sc1\n\t"
      "global_load_dwordx4 v[152:155], v230, s[52:53] sc1\n\t"
      "s_waitcnt vmcnt(0)\n\t"
      "v_max3_u32 v234, v128, v129, v130\n\t"
      "v_max3_u32 v234, v234, v131, v132\n\t"
      "v_max3_u32 v234, v234, v133, v134\n\t"
      "v_max3_u32 v234, v234, v135, v136\n\t"
      "v_max3_u32 v234, v234, v137, v138\n\t"
      "v_max3_u32 v234, v234, v139, v140\n\t"
      "v_max3_u32 v234, v234, v141, v142\n\t"
      "v_max3_u32 v234, v234, v143, v144\n\t"
      "v_max3_u32 v234, v234, v145, v146\n\t"
      "v_max3_u32 v234, v234, v147, v148\n\t"
      "v_max3_u32 v234, v234, v149, v150\n\t"
      "v_max3_u32 v234, v234, v151, v152\n\t"
      "v_max3_u32 v234, v234, v153, v154\n\t"
      "v_max_u32_e32 v234, v234, v155\n\t"
      "v_cmp_eq_u32_e32 vcc, -1, v234\n\t"
      "s_cbranch_vccnz NP0L_%=\n\t"
      "NP0X_%=:\n\t"
      "v_mfma_f32_16x16x32_bf16 v[184:187], v[128:131], v[0:3], v[156:159]\n\t"
      "v_mfma_f32_16x16x32_bf16 v[188:191], v[128:131], v[4:7], 0\n\t"
      "v_mfma_f32_16x16x32_bf16 v[192:195], v[128:131], v[8:11], 0\n\t"
      "v_mfma_f32_16x16x32_bf16 v[196:199], v[128:131], v[12:15], 0\n\t"
      "v_mfma_f32_16x16x32_bf16 v[184:187], v[132:135], v[16:19], v[184:187]\n\t"
      "v_mfma_f32_16x16x32_bf16 v[188:191], v[132:135], v[20:23], v[188:191]\n\t"
      "v_mfma_f32_16x16x32_bf16 v[192:195], v[132:135], v[24:27], v[192:195]\n\t"
      "v_mfma_f32_16x16x32_bf16 v[196:199], v[132:135], v[28:31], v[196:199]\n\t"
      "v_mfma_f32_16x16x32_bf16 v[184:187], v[136:139], v[32:35], v[184:187]\n\t"
      "v_mfma_f32_16x16x32_bf16 v[188:191], v[136:139], v[36:39], v[188:191]\n\t"
      "v_mfma_f32_16x16x32_bf16 v[192:195], v[136:139], v[40:43], v[192:195]\n\t"
      "v_mfma_f32_16x16x32_bf16 v[196:199], v[136:139], v[44:47], v[196:199]\n\t"
      "v_mfma_f32_16x16x32_bf16 v[184:187], v[140:143], v[48:51], v[184:187]\n\t"
      "v_mfma_f32_16x16x32_bf16 v[188:191], v[140:143], v[52:55], v[188:191]\n\t"
      "v_mfma_f32_16x16x32_bf16 v[192:195], v[140:143], v[56:59], v[192:195]\n\t"
      "v_mfma_f32_16x16x32_bf16 v[196:199], v[140:143], v[60:63], v[196:199]\n\t"
      "v_mfma_f32_16x16x32_bf16 v[184:187], v[144:147], v[64:67], v[184:187]\n\t"
      "v_mfma_f32_16x16x32_bf16 v[188:191], v[144:147], v[68:71], v[188:191]\n\t"
      "v_mfma_f32_16x16x32_bf16 v[192:195], v[144:147], v[72:75], v[192:195]\n\t"
      "v_mfma_f32_16x16x32_bf16 v[196:199], v[144:147], v[76:79], v[196:199]\n\t"
      "v_mfma_f32_16x16x32_bf16 v[184:187], v[148:151], v[80:83], v[184:187]\n\t"
      "v_mfma_f32_16x16x32_bf16 v[188:191], v[148:151], v[84:87], v[188:191]\n\t"
      "v_mfma_f32_16x16x32_bf16 v[192:195], v[148:151], v[88:91], v[192:195]\n\t"
      "v_mfma_f32_16x16x32_bf16 v[184:187], v[152:155], v[96:99], v[184:187]\n\t"
      "v_mfma_f32_16x16x32_bf16 v[188:191], v[152:155], v[100:103], v[188:191]\n\t"
      "v_mfma_f32_16x16x32_bf16 v[192:195], v[152:155], v[104:107], v[192:195]\n\t"
      "global_load_dwordx4 v[128:131], v224, s[54:55] sc1\n\t"
      "global_load_dwordx4 v[132:135], v225, s[54:55] sc1\n\t"
      "global_load_dwordx4 v[136:139], v226, s[54:55] sc1\n\t"
      "global_load_dwordx4 v[140:143], v227, s[54:55] sc1\n\t"
      "global_load_dwordx4 v[144:147], v228, s[54:55] sc1\n\t"
      "s_waitcnt vmcnt(0)\n\t"
      "s_nop 7\n\t"
      "s_branch NP0B_%=\n\t"
      "NP1R_%=:\n\t"
      "s_mov_b32 s51, 0\n\t"
      "NP1L_%=:\n\t"
      "s_add_u32 s51, s51, 1\n\t"
      "s_cmp_lt_u32 s51, 262144\n\t"
      "s_cbranch_scc0 NP1X_%=\n\t"
      "s_waitcnt vmcnt(0)\n\t"
      "s_sleep 1\n\t"
      "global_load_dwordx4 v[128:131], v224, s[54:55] sc1\n\t"
      "global_load_dwordx4 v[132:135], v225, s[54:55] sc1\n\t"
      "global_load_dwordx4 v[136:139], v226, s[54:55] sc1\n\t"
      "global_load_dwordx4 v[140:143], v227, s[54:55] sc1\n\t"
      "global_load_dwordx4 v[144:147], v228, s[54:55] sc1\n\t"
      "global_load_dwordx4 v[148:151], v229, s[54:55] sc1\n\t"
      "global_load_dwordx4 v[152:155], v230, s[54:55] sc1\n\t"
      "s_waitcnt vmcnt(0)\n\t"
      "v_max3_u32 v234, v128, v129, v130\n\t"
      "v_max3_u32 v234, v234, v131, v132\n\t"
      "v_max3_u32 v234, v234, v133, v134\n\t"
      "v_max3_u32 v234, v234, v135, v136\n\t"
      "v_max3_u32 v234, v234, v137, v138\n\t"
      "v_max3_u32 v234, v234, v139, v140\n\t"
      "v_max3_u32 v234, v234, v141, v142\n\t"
      "v_max3_u32 v234, v234, v143, v144\n\t"
      "v_max3_u32 v234, v234, v145, v146\n\t"
      "v_max3_u32 v234, v234, v147, v148\n\t"
      "v_max3_u32 v234, v234, v149, v150\n\t"
      "v_max3_u32 v234, v234, v151, v152\n\t"
      "v_max3_u32 v234, v234, v153, v154\n\t"
      "v_max_u32_e32 v234, v234, v155\n\t"
      "v_cmp_eq_u32_e32 vcc, -1, v234\n\t"
      "s_cbranch_vccnz NP1L_%=\n\t"
      "NP1X_%=:\n\t"
      "v_mfma_f32_16x16x32_bf16 v[200:203], v[128:131], v[0:3], v[160:163]\n\t"
      "v_mfma_f32_16x16x32_bf16 v[204:207], v[128:131], v[4:7], 0\n\t"
      "v_mfma_f32_16x16x32_bf16 v[208:211], v[128:131], v[8:11], 0\n\t"
      "v_mfma_f32_16x16x32_bf16 v[212:215], v[128:131], v[12:15], 0\n\t"
      "v_mfma_f32_16x16x32_bf16 v[200:203], v[132:135], v[16:19], v[200:203]\n\t"
      "v_mfma_f32_16x16x32_bf16 v[204:207], v[132:135], v[20:23], v[204:207]\n\t"
      "v_mfma_f32_16x16x32_bf16 v[208:211], v[132:135], v[24:27], v[208:211]\n\t"
      "v_mfma_f32_16x16x32_bf16 v[212:215], v[132:135], v[28:31], v[212:215]\n\t"
      "v_mfma_f32_16x16x32_bf16 v[200:203], v[136:139], v[32:35], v[200:203]\n\t"
      "v_mfma_f32_16x16x32_bf16 v[204:207], v[136:139], v[36:39], v[204:207]\n\t"
      "v_mfma_f32_16x16x32_bf16 v[208:211], v[136:139], v[40:43], v[208:211]\n\t"
      "v_mfma_f32_16x16x32_bf16 v[212:215], v[136:139], v[44:47], v[212:215]\n\t"
      "v_mfma_f32_16x16x32_bf16 v[200:203], v[140:143], v[48:51], v[200:203]\n\t"
      "v_mfma_f32_16x16x32_bf16 v[204:207], v[140:143], v[52:55], v[204:207]\n\t"
      "v_mfma_f32_16x16x32_bf16 v[208:211], v[140:143], v[56:59], v[208:211]\n\t"
      "v_mfma_f32_16x16x32_bf16 v[212:215], v[140:143], v[60:63], v[212:215]\n\t"
      "v_mfma_f32_16x16x32_bf16 v[200:203], v[144:147], v[64:67], v[200:203]\n\t"
      "v_mfma_f32_16x16x32_bf16 v[204:207], v[144:147], v[68:71], v[204:207]\n\t"
      "v_mfma_f32_16x16x32_bf16 v[208:211], v[144:147], v[72:75], v[208:211]\n\t"
      "v_mfma_f32_16x16x32_bf16 v[212:215], v[144:147], v[76:79], v[212:215]\n\t"
      "v_mfma_f32_16x16x32_bf16 v[200:203], v[148:151], v[80:83], v[200:203]\n\t"
      "v_mfma_f32_16x16x32_bf16 v[204:207], v[148:151], v[84:87], v[204:207]\n\t"
      "v_mfma_f32_16x16x32_bf16 v[208:211], v[148:151], v[88:91], v[208:211]\n\t"
      "v_mfma_f32_16x16x32_bf16 v[200:203], v[152:155], v[96:99], v[200:203]\n\t"
      "v_mfma_f32_16x16x32_bf16 v[204:207], v[152:155], v[100:103], v[204:207]\n\t"
      "v_mfma_f32_16x16x32_bf16 v[208:211], v[152:155], v[104:107], v[208:211]\n\t"
      "global_load_dwordx4 v[128:131], v224, s[56:57] sc1\n\t"
      "global_load_dwordx4 v[132:135], v225, s[56:57] sc1\n\t"
      "global_load_dwordx4 v[136:139], v226, s[56:57] sc1\n\t"
      "global_load_dwordx4 v[140:143], v227, s[56:57] sc1\n\t"
      "global_load_dwordx4 v[144:147], v228, s[56:57] sc1\n\t"
      "s_waitcnt vmcnt(0)\n\t"
      "s_nop 7\n\t"
      "s_branch NP1B_%=\n\t"
      "NP2R_%=:\n\t"
      "s_mov_b32 s51, 0\n\t"
      "NP2L_%=:\n\t"
      "s_add_u32 s51, s51, 1\n\t"
      "s_cmp_lt_u32 s51, 262144\n\t"
      "s_cbranch_scc0 NP2X_%=\n\t"
      "s_waitcnt vmcnt(0)\n\t"
      "s_sleep 1\n\t"
      "global_load_dwordx4 v[128:131], v224, s[56:57] sc1\n\t"
      "global_load_dwordx4 v[132:135], v225, s[56:57] sc1\n\t"
      "global_load_dwordx4 v[136:139], v226, s[56:57] sc1\n\t"
      "global_load_dwordx4 v[140:143], v227, s[56:57] sc1\n\t"
      "global_load_dwordx4 v[144:147], v228, s[56:57] sc1\n\t"
      "global_load_dwordx4 v[148:151], v229, s[56:57] sc1\n\t"
      "global_load_dwordx4 v[152:155], v230, s[56:57] sc1\n\t"
      "s_waitcnt vmcnt(0)\n\t"
      "v_max3_u32 v234, v128, v129, v130\n\t"
      "v_max3_u32 v234, v234, v131, v132\n\t"
      "v_max3_u32 v234, v234, v133, v134\n\t"
      "v_max3_u32 v234, v234, v135, v136\n\t"
      "v_max3_u32 v234, v234, v137, v138\n\t"
      "v_max3_u32 v234, v234, v139, v140\n\t"
      "v_max3_u32 v234, v234, v141, v142\n\t"
      "v_max3_u32 v234, v234, v143, v144\n\t"
      "v_max3_u32 v234, v234, v145, v146\n\t"
      "v_max3_u32 v234, v234, v147, v148\n\t"
      "v_max3_u32 v234, v234, v149, v150\n\t"
      "v_max3_u32 v234, v234, v151, v152\n\t"
      "v_max3_u32 v234, v234, v153, v154\n\t"
      "v_max_u32_e32 v234, v234, v155\n\t"
      "v_cmp_eq_u32_e32 vcc, -1, v234\n\t"
      "s_cbranch_vccnz NP2L_%=\n\t"
      "NP2X_%=:\n\t"
      "v_mfma_f32_16x16x32_bf16 v[184:187], v[128:131], v[0:3], v[164:167]\n\t"
      "v_mfma_f32_16x16x32_bf16 v[188:191], v[128:131], v[4:7], 0\n\t"
      "v_mfma_f32_16x16x32_bf16 v[192:195], v[128:131], v[8:11], 0\n\t"
      "v_mfma_f32_16x16x32_bf16 v[196:199], v[128:131], v[12:15], 0\n\t"
      "v_mfma_f32_16x16x32_bf16 v[184:187], v[132:135], v[16:19], v[184:187]\n\t"
      "v_mfma_f32_16x16x32_bf16 v[188:191], v[132:135], v[20:23], v[188:191]\n\t"
      "v_mfma_f32_16x16x32_bf16 v[192:195], v[132:135], v[24:27], v[192:195]\n\t"
      "v_mfma_f32_16x16x32_bf16 v[196:199], v[132:135], v[28:31], v[196:199]\n\t"
      "v_mfma_f32_16x16x32_bf16 v[184:187], v[136:139], v[32:35], v[184:187]\n\t"
      "v_mfma_f32_16x16x32_bf16 v[188:191], v[136:139], v[36:39], v[188:191]\n\t"
      "v_mfma_f32_16x16x32_bf16 v[192:195], v[136:139], v[40:43], v[192:195]\n\t"
      "v_mfma_f32_16x16x32_bf16 v[196:199], v[136:139], v[44:47], v[196:199]\n\t"
      "v_mfma_f32_16x16x32_bf16 v[184:187], v[140:143], v[48:51], v[184:187]\n\t"
      "v_mfma_f32_16x16x32_bf16 v[188:191], v[140:143], v[52:55], v[188:191]\n\t"
      "v_mfma_f32_16x16x32_bf16 v[192:195], v[140:143], v[56:59], v[192:195]\n\t"
      "v_mfma_f32_16x16x32_bf16 v[196:199], v[140:143], v[60:63], v[196:199]\n\t"
      "v_mfma_f32_16x16x32_bf16 v[184:187], v[144:147], v[64:67], v[184:187]\n\t"
      "v_mfma_f32_16x16x32_bf16 v[188:191], v[144:147], v[68:71], v[188:191]\n\t"
      "v_mfma_f32_16x16x32_bf16 v[192:195], v[144:147], v[72:75], v[192:195]\n\t"
      "v_mfma_f32_16x16x32_bf16 v[196:199], v[144:147], v[76:79], v[196:199]\n\t"
      "v_mfma_f32_16x16x32_bf16 v[184:187], v[148:151], v[80:83], v[184:187]\n\t"
      "v_mfma_f32_16x16x32_bf16 v[188:191], v[148:151], v[84:87], v[188:191]\n\t"
      "v_mfma_f32_16x16x32_bf16 v[192:195], v[148:151], v[88:91], v[192:195]\n\t"
      "v_mfma_f32_16x16x32_bf16 v[184:187], v[152:155], v[96:99], v[184:187]\n\t"
      "v_mfma_f32_16x16x32_bf16 v[188:191], v[152:155], v[100:103], v[188:191]\n\t"
      "v_mfma_f32_16x16x32_bf16 v[192:195], v[152:155], v[104:107], v[192:195]\n\t"
      "global_load_dwordx4 v[128:131], v224, s[58:59] sc1\n\t"
      "global_load_dwordx4 v[132:135], v225, s[58:59] sc1\n\t"
      "global_load_dwordx4 v[136:139], v226, s[58:59] sc1\n\t"
      "global_load_dwordx4 v[140:143], v227, s[58:59] sc1\n\t"
      "global_load_dwordx4 v[144:147], v228, s[58:59] sc1\n\t"
      "s_waitcnt vmcnt(0)\n\t"
      "s_nop 7\n\t"
      "s_branch NP2B_%=\n\t"
      "NP3R_%=:\n\t"
      "s_mov_b32 s51, 0\n\t"
      "NP3L_%=:\n\t"
      "s_add_u32 s51, s51, 1\n\t"
      "s_cmp_lt_u32 s51, 262144\n\t"
      "s_cbranch_scc0 NP3X_%=\n\t"
      "s_waitcnt vmcnt(0)\n\t"
      "s_sleep 1\n\t"
      "global_load_dwordx4 v[128:131], v224, s[58:59] sc1\n\t"
      "global_load_dwordx4 v[132:135], v225, s[58:59] sc1\n\t"
      "global_load_dwordx4 v[136:139], v226, s[58:59] sc1\n\t"
      "global_load_dwordx4 v[140:143], v227, s[58:59] sc1\n\t"
      "global_load_dwordx4 v[144:147], v228, s[58:59] sc1\n\t"
      "global_load_dwordx4 v[148:151], v229, s[58:59] sc1\n\t"
      "global_load_dwordx4 v[152:155], v230, s[58:59] sc1\n\t"
      "s_waitcnt vmcnt(0)\n\t"
      "v_max3_u32 v234, v128, v129, v130\n\t"
      "v_max3_u32 v234, v234, v131, v132\n\t"
      "v_max3_u32 v234, v234, v133, v134\n\t"
      "v_max3_u32 v234, v234, v135, v136\n\t"
      "v_max3_u32 v234, v234, v137, v138\n\t"
      "v_max3_u32 v234, v234, v139, v140\n\t"
      "v_max3_u32 v234, v234, v141, v142\n\t"
      "v_max3_u32 v234, v234, v143, v144\n\t"
      "v_max3_u32 v234, v234, v145, v146\n\t"
      "v_max3_u32 v234, v234, v147, v148\n\t"
      "v_max3_u32 v234, v234, v149, v150\n\t"
      "v_max3_u32 v234, v234, v151, v152\n\t"
      "v_max3_u32 v234, v234, v153, v154\n\t"
      "v_max_u32_e32 v234, v234, v155\n\t"
      "v_cmp_eq_u32_e32 vcc, -1, v234\n\t"
      "s_cbranch_vccnz NP3L_%=\n\t"
      "NP3X_%=:\n\t"
      "v_mfma_f32_16x16x32_bf16 v[200:203], v[128:131], v[0:3], v[168:171]\n\t"
      "v_mfma_f32_16x16x32_bf16 v[204:207], v[128:131], v[4:7], 0\n\t"
      "v_mfma_f32_16x16x32_bf16 v[208:211], v[128:131], v[8:11], 0\n\t"
      "v_mfma_f32_16x16x32_bf16 v[212:215], v[128:131], v[12:15], 0\n\t"
      "v_mfma_f32_16x16x32_bf16 v[200:203], v[132:135], v[16:19], v[200:203]\n\t"
      "v_mfma_f32_16x16x32_bf16 v[204:207], v[132:135], v[20:23], v[204:207]\n\t"
      "v_mfma_f32_16x16x32_bf16 v[208:211], v[132:135], v[24:27], v[208:211]\n\t"
      "v_mfma_f32_16x16x32_bf16 v[212:215], v[132:135], v[28:31], v[212:215]\n\t"
      "v_mfma_f32_16x16x32_bf16 v[200:203], v[136:139], v[32:35], v[200:203]\n\t"
      "v_mfma_f32_16x16x32_bf16 v[204:207], v[136:139], v[36:39], v[204:207]\n\t"
      "v_mfma_f32_16x16x32_bf16 v[208:211], v[136:139], v[40:43], v[208:211]\n\t"
      "v_mfma_f32_16x16x32_bf16 v[212:215], v[136:139], v[44:47], v[212:215]\n\t"
      "v_mfma_f32_16x16x32_bf16 v[200:203], v[140:143], v[48:51], v[200:203]\n\t"
      "v_mfma_f32_16x16x32_bf16 v[204:207], v[140:143], v[52:55], v[204:207]\n\t"
      "v_mfma_f32_16x16x32_bf16 v[208:211], v[140:143], v[56:59], v[208:211]\n\t"
      "v_mfma_f32_16x16x32_bf16 v[212:215], v[140:143], v[60:63], v[212:215]\n\t"
      "v_mfma_f32_16x16x32_bf16 v[200:203], v[144:147], v[64:67], v[200:203]\n\t"
      "v_mfma_f32_16x16x32_bf16 v[204:207], v[144:147], v[68:71], v[204:207]\n\t"
      "v_mfma_f32_16x16x32_bf16 v[208:211], v[144:147], v[72:75], v[208:211]\n\t"
      "v_mfma_f32_16x16x32_bf16 v[212:215], v[144:147], v[76:79], v[212:215]\n\t"
      "v_mfma_f32_16x16x32_bf16 v[200:203], v[148:151], v[80:83], v[200:203]\n\t"
      "v_mfma_f32_16x16x32_bf16 v[204:207], v[148:151], v[84:87], v[204:207]\n\t"
      "v_mfma_f32_16x16x32_bf16 v[208:211], v[148:151], v[88:91], v[208:211]\n\t"
      "v_mfma_f32_16x16x32_bf16 v[200:203], v[152:155], v[96:99], v[200:203]\n\t"
      "v_mfma_f32_16x16x32_bf16 v[204:207], v[152:155], v[100:103], v[204:207]\n\t"
      "v_mfma_f32_16x16x32_bf16 v[208:211], v[152:155], v[104:107], v[208:211]\n\t"
      "global_load_dwordx4 v[128:131], v224, s[60:61] sc1\n\t"
      "global_load_dwordx4 v[132:135], v225, s[60:61] sc1\n\t"
      "global_load_dwordx4 v[136:139], v226, s[60:61] sc1\n\t"
      "global_load_dwordx4 v[140:143], v227, s[60:61] sc1\n\t"
      "global_load_dwordx4 v[144:147], v228, s[60:61] sc1\n\t"
      "s_waitcnt vmcnt(0)\n\t"
      "s_nop 7\n\t"
      "s_branch NP3B_%=\n\t"
      "NP4R_%=:\n\t"
      "s_mov_b32 s51, 0\n\t"
      "NP4L_%=:\n\t"
      "s_add_u32 s51, s51, 1\n\t"
      "s_cmp_lt_u32 s51, 262144\n\t"
      "s_cbranch_scc0 NP4X_%=\n\t"
      "s_waitcnt vmcnt(0)\n\t"
      "s_sleep 1\n\t"
      "global_load_dwordx4 v[128:131], v224, s[60:61] sc1\n\t"
      "global_load_dwordx4 v[132:135], v225, s[60:61] sc1\n\t"
      "global_load_dwordx4 v[136:139], v226, s[60:61] sc1\n\t"
      "global_load_dwordx4 v[140:143], v227, s[60:61] sc1\n\t"
      "global_load_dwordx4 v[144:147], v228, s[60:61] sc1\n\t"
      "global_load_dwordx4 v[148:151], v229, s[60:61] sc1\n\t"
      "global_load_dwordx4 v[152:155], v230, s[60:61] sc1\n\t"
      "s_waitcnt vmcnt(0)\n\t"
      "v_max3_u32 v234, v128, v129, v130\n\t"
      "v_max3_u32 v234, v234, v131, v132\n\t"
      "v_max3_u32 v234, v234, v133, v134\n\t"
      "v_max3_u32 v234, v234, v135, v136\n\t"
      "v_max3_u32 v234, v234, v137, v138\n\t"
      "v_max3_u32 v234, v234, v139, v140\n\t"
      "v_max3_u32 v234, v234, v141, v142\n\t"
      "v_max3_u32 v234, v234, v143, v144\n\t"
      "v_max3_u32 v234, v234, v145, v146\n\t"
      "v_max3_u32 v234, v234, v147, v148\n\t"
      "v_max3_u32 v234, v234, v149, v150\n\t"
      "v_max3_u32 v234, v234, v151, v152\n\t"
      "v_max3_u32 v234, v234, v153, v154\n\t"
      "v_max_u32_e32 v234, v234, v155\n\t"
      "v_cmp_eq_u32_e32 vcc, -1, v234\n\t"
      "s_cbranch_vccnz NP4L_%=\n\t"
      "NP4X_%=:\n\t"
      "v_mfma_f32_16x16x32_bf16 v[184:187], v[128:131], v[0:3], v[172:175]\n\t"
      "v_mfma_f32_16x16x32_bf16 v[188:191], v[128:131], v[4:7], 0\n\t"
      "v_mfma_f32_16x16x32_bf16 v[192:195], v[128:131], v[8:11], 0\n\t"
      "v_mfma_f32_16x16x32_bf16 v[196:199], v[128:131], v[12:15], 0\n\t"
      "v_mfma_f32_16x16x32_bf16 v[184:187], v[132:135], v[16:19], v[184:187]\n\t"
      "v_mfma_f32_16x16x32_bf16 v[188:191], v[132:135], v[20:23], v[188:191]\n\t"
      "v_mfma_f32_16x16x32_bf16 v[192:195], v[132:135], v[24:27], v[192:195]\n\t"
      "v_mfma_f32_16x16x32_bf16 v[196:199], v[132:135], v[28:31], v[196:199]\n\t"
      "v_mfma_f32_16x16x32_bf16 v[184:187], v[136:139], v[32:35], v[184:187]\n\t"
      "v_mfma_f32_16x16x32_bf16 v[188:191], v[136:139], v[36:39], v[188:191]\n\t"
      "v_mfma_f32_16x16x32_bf16 v[192:195], v[136:139], v[40:43], v[192:195]\n\t"
      "v_mfma_f32_16x16x32_bf16 v[196:199], v[136:139], v[44:47], v[196:199]\n\t"
      "v_mfma_f32_16x16x32_bf16 v[184:187], v[140:143], v[48:51], v[184:187]\n\t"
      "v_mfma_f32_16x16x32_bf16 v[188:191], v[140:143], v[52:55], v[188:191]\n\t"
      "v_mfma_f32_16x16x32_bf16 v[192:195], v[140:143], v[56:59], v[192:195]\n\t"
      "v_mfma_f32_16x16x32_bf16 v[196:199], v[140:143], v[60:63], v[196:199]\n\t"
      "v_mfma_f32_16x16x32_bf16 v[184:187], v[144:147], v[64:67], v[184:187]\n\t"
      "v_mfma_f32_16x16x32_bf16 v[188:191], v[144:147], v[68:71], v[188:191]\n\t"
      "v_mfma_f32_16x16x32_bf16 v[192:195], v[144:147], v[72:75], v[192:195]\n\t"
      "v_mfma_f32_16x16x32_bf16 v[196:199], v[144:147], v[76:79], v[196:199]\n\t"
      "v_mfma_f32_16x16x32_bf16 v[184:187], v[148:151], v[80:83], v[184:187]\n\t"
      "v_mfma_f32_16x16x32_bf16 v[188:191], v[148:151], v[84:87], v[188:191]\n\t"
      "v_mfma_f32_16x16x32_bf16 v[192:195], v[148:151], v[88:91], v[192:195]\n\t"
      "v_mfma_f32_16x16x32_bf16 v[184:187], v[152:155], v[96:99], v[184:187]\n\t"
      "v_mfma_f32_16x16x32_bf16 v[188:191], v[152:155], v[100:103], v[188:191]\n\t"
      "v_mfma_f32_16x16x32_bf16 v[192:195], v[152:155], v[104:107], v[192:195]\n\t"
      "global_load_dwordx4 v[128:131], v224, s[62:63] sc1\n\t"
      "global_load_dwordx4 v[132:135], v225, s[62:63] sc1\n\t"
      "global_load_dwordx4 v[136:139], v226, s[62:63] sc1\n\t"
      "global_load_dwordx4 v[140:143], v227, s[62:63] sc1\n\t"
      "global_load_dwordx4 v[144:147], v228, s[62:63] sc1\n\t"
      "s_waitcnt vmcnt(0)\n\t"
      "s_nop 7\n\t"
      "s_branch NP4B_%=\n\t"
      "NP5R_%=:\n\t"
      "s_mov_b32 s51, 0\n\t"
      "NP5L_%=:\n\t"
      "s_add_u32 s51, s51, 1\n\t"
      "s_cmp_lt_u32 s51, 262144\n\t"
      "s_cbranch_scc0 NP5X_%=\n\t"
      "s_waitcnt vmcnt(0)\n\t"
      "s_sleep 1\n\t"
      "global_load_dwordx4 v[128:131], v224, s[62:63] sc1\n\t"
      "global_load_dwordx4 v[132:135], v225, s[62:63] sc1\n\t"
      "global_load_dwordx4 v[136:139], v226, s[62:63] sc1\n\t"
      "global_load_dwordx4 v[140:143], v227, s[62:63] sc1\n\t"
      "global_load_dwordx4 v[144:147], v228, s[62:63] sc1\n\t"
      "global_load_dwordx4 v[148:151], v229, s[62:63] sc1\n\t"
      "global_load_dwordx4 v[152:155], v230, s[62:63] sc1\n\t"
      "s_waitcnt vmcnt(0)\n\t"
      "v_max3_u32 v234, v128, v129, v130\n\t"
      "v_max3_u32 v234, v234, v131, v132\n\t"
      "v_max3_u32 v234, v234, v133, v134\n\t"
      "v_max3_u32 v234, v234, v135, v136\n\t"
      "v_max3_u32 v234, v234, v137, v138\n\t"
      "v_max3_u32 v234, v234, v139, v140\n\t"
      "v_max3_u32 v234, v234, v141, v142\n\t"
      "v_max3_u32 v234, v234, v143, v144\n\t"
      "v_max3_u32 v234, v234, v145, v146\n\t"
      "v_max3_u32 v234, v234, v147, v148\n\t"
      "v_max3_u32 v234, v234, v149, v150\n\t"
      "v_max3_u32 v234, v234, v151, v152\n\t"
      "v_max3_u32 v234, v234, v153, v154\n\t"
      "v_max_u32_e32 v234, v234, v155\n\t"
      "v_cmp_eq_u32_e32 vcc, -1, v234\n\t"
      "s_cbranch_vccnz NP5L_%=\n\t"
      "NP5X_%=:\n\t"
      "v_mfma_f32_16x16x32_bf16 v[200:203], v[128:131], v[0:3], v[176:179]\n\t"
      "v_mfma_f32_16x16x32_bf16 v[204:207], v[128:131], v[4:7], 0\n\t"
      "v_mfma_f32_16x16x32_bf16 v[208:211], v[128:131], v[8:11], 0\n\t"
      "v_mfma_f32_16x16x32_bf16 v[212:215], v[128:131], v[12:15], 0\n\t"
      "v_mfma_f32_16x16x32_bf16 v[200:203], v[132:135], v[16:19], v[200:203]\n\t"
      "v_mfma_f32_16x16x32_bf16 v[204:207], v[132:135], v[20:23], v[204:207]\n\t"
      "v_mfma_f32_16x16x32_bf16 v[208:211], v[132:135], v[24:27], v[208:211]\n\t"
      "v_mfma_f32_16x16x32_bf16 v[212:215], v[132:135], v[28:31], v[212:215]\n\t"
      "v_mfma_f32_16x16x32_bf16 v[200:203], v[136:139], v[32:35], v[200:203]\n\t"
      "v_mfma_f32_16x16x32_bf16 v[204:207], v[136:139], v[36:39], v[204:207]\n\t"
      "v_mfma_f32_16x16x32_bf16 v[208:211], v[136:139], v[40:43], v[208:211]\n\t"
      "v_mfma_f32_16x16x32_bf16 v[212:215], v[136:139], v[44:47], v[212:215]\n\t"
      "v_mfma_f32_16x16x32_bf16 v[200:203], v[140:143], v[48:51], v[200:203]\n\t"
      "v_mfma_f32_16x16x32_bf16 v[204:207], v[140:143], v[52:55], v[204:207]\n\t"
      "v_mfma_f32_16x16x32_bf16 v[208:211], v[140:143], v[56:59], v[208:211]\n\t"
      "v_mfma_f32_16x16x32_bf16 v[212:215], v[140:143], v[60:63], v[212:215]\n\t"
      "v_mfma_f32_16x16x32_bf16 v[200:203], v[144:147], v[64:67], v[200:203]\n\t"
      "v_mfma_f32_16x16x32_bf16 v[204:207], v[144:147], v[68:71], v[204:207]\n\t"
      "v_mfma_f32_16x16x32_bf16 v[208:211], v[144:147], v[72:75], v[208:211]\n\t"
      "v_mfma_f32_16x16x32_bf16 v[212:215], v[144:147], v[76:79], v[212:215]\n\t"
      "v_mfma_f32_16x16x32_bf16 v[200:203], v[148:151], v[80:83], v[200:203]\n\t"
      "v_mfma_f32_16x16x32_bf16 v[204:207], v[148:151], v[84:87], v[204:207]\n\t"
      "v_mfma_f32_16x16x32_bf16 v[208:211], v[148:151], v[88:91], v[208:211]\n\t"
      "v_mfma_f32_16x16x32_bf16 v[200:203], v[152:155], v[96:99], v[200:203]\n\t"
      "v_mfma_f32_16x16x32_bf16 v[204:207], v[152:155], v[100:103], v[204:207]\n\t"
      "v_mfma_f32_16x16x32_bf16 v[208:211], v[152:155], v[104:107], v[208:211]\n\t"
      "global_load_dwordx4 v[128:131], v224, s[64:65] sc1\n\t"
      "global_load_dwordx4 v[132:135], v225, s[64:65] sc1\n\t"
      "global_load_dwordx4 v[136:139], v226, s[64:65] sc1\n\t"
      "global_load_dwordx4 v[140:143], v227, s[64:65] sc1\n\t"
      "global_load_dwordx4 v[144:147], v228, s[64:65] sc1\n\t"
      "s_waitcnt vmcnt(0)\n\t"
      "s_nop 7\n\t"
      "s_branch NP5B_%=\n\t"
      "NP6R_%=:\n\t"
      "s_mov_b32 s51, 0\n\t"
      "NP6L_%=:\n\t"
      "s_add_u32 s51, s51, 1\n\t"
      "s_cmp_lt_u32 s51, 262144\n\t"
      "s_cbranch_scc0 NP6X_%=\n\t"
      "s_waitcnt vmcnt(0)\n\t"
      "s_sleep 1\n\t"
      "global_load_dwordx4 v[128:131], v224, s[64:65] sc1\n\t"
      "global_load_dwordx4 v[132:135], v225, s[64:65] sc1\n\t"
      "global_load_dwordx4 v[136:139], v226, s[64:65] sc1\n\t"
      "global_load_dwordx4 v[140:143], v227, s[64:65] sc1\n\t"
      "global_load_dwordx4 v[144:147], v228, s[64:65] sc1\n\t"
      "global_load_dwordx4 v[148:151], v229, s[64:65] sc1\n\t"
      "global_load_dwordx4 v[152:155], v230, s[64:65] sc1\n\t"
      "s_waitcnt vmcnt(0)\n\t"
      "v_max3_u32 v234, v128, v129, v130\n\t"
      "v_max3_u32 v234, v234, v131, v132\n\t"
      "v_max3_u32 v234, v234, v133, v134\n\t"
      "v_max3_u32 v234, v234, v135, v136\n\t"
      "v_max3_u32 v234, v234, v137, v138\n\t"
      "v_max3_u32 v234, v234, v139, v140\n\t"
      "v_max3_u32 v234, v234, v141, v142\n\t"
      "v_max3_u32 v234, v234, v143, v144\n\t"
      "v_max3_u32 v234, v234, v145, v146\n\t"
      "v_max3_u32 v234, v234, v147, v148\n\t"
      "v_max3_u32 v234, v234, v149, v150\n\t"
      "v_max3_u32 v234, v234, v151, v152\n\t"
      "v_max3_u32 v234, v234, v153, v154\n\t"
      "v_max_u32_e32 v234, v234, v155\n\t"
      "v_cmp_eq_u32_e32 vcc, -1, v234\n\t"
      "s_cbranch_vccnz NP6L_%=\n\t"
      "NP6X_%=:\n\t"
      "v_mfma_f32_16x16x32_bf16 v[184:187], v[128:131], v[0:3], v[180:183]\n\t"
      "v_mfma_f32_16x16x32_bf16 v[188:191], v[128:131], v[4:7], 0\n\t"
      "v_mfma_f32_16x16x32_bf16 v[192:195], v[128:131], v[8:11], 0\n\t"
      "v_mfma_f32_16x16x32_bf16 v[196:199], v[128:131], v[12:15], 0\n\t"
      "v_mfma_f32_16x16x32_bf16 v[184:187], v[132:135], v[16:19], v[184:187]\n\t"
      "v_mfma_f32_16x16x32_bf16 v[188:191], v[132:135], v[20:23], v[188:191]\n\t"
      "v_mfma_f32_16x16x32_bf16 v[192:195], v[132:135], v[24:27], v[192:195]\n\t"
      "v_mfma_f32_16x16x32_bf16 v[196:199], v[132:135], v[28:31], v[196:199]\n\t"
      "v_mfma_f32_16x16x32_bf16 v[184:187], v[136:139], v[32:35], v[184:187]\n\t"
      "v_mfma_f32_16x16x32_bf16 v[188:191], v[136:139], v[36:39], v[188:191]\n\t"
      "v_mfma_f32_16x16x32_bf16 v[192:195], v[136:139], v[40:43], v[192:195]\n\t"
      "v_mfma_f32_16x16x32_bf16 v[196:199], v[136:139], v[44:47], v[196:199]\n\t"
      "v_mfma_f32_16x16x32_bf16 v[184:187], v[140:143], v[48:51], v[184:187]\n\t"
      "v_mfma_f32_16x16x32_bf16 v[188:191], v[140:143], v[52:55], v[188:191]\n\t"
      "v_mfma_f32_16x16x32_bf16 v[192:195], v[140:143], v[56:59], v[192:195]\n\t"
      "v_mfma_f32_16x16x32_bf16 v[196:199], v[140:143], v[60:63], v[196:199]\n\t"
      "v_mfma_f32_16x16x32_bf16 v[184:187], v[144:147], v[64:67], v[184:187]\n\t"
      "v_mfma_f32_16x16x32_bf16 v[188:191], v[144:147], v[68:71], v[188:191]\n\t"
      "v_mfma_f32_16x16x32_bf16 v[192:195], v[144:147], v[72:75], v[192:195]\n\t"
      "v_mfma_f32_16x16x32_bf16 v[196:199], v[144:147], v[76:79], v[196:199]\n\t"
      "v_mfma_f32_16x16x32_bf16 v[184:187], v[148:151], v[80:83], v[184:187]\n\t"
      "v_mfma_f32_16x16x32_bf16 v[188:191], v[148:151], v[84:87], v[188:191]\n\t"
      "v_mfma_f32_16x16x32_bf16 v[192:195], v[148:151], v[88:91], v[192:195]\n\t"
      "v_mfma_f32_16x16x32_bf16 v[184:187], v[152:155], v[96:99], v[184:187]\n\t"
      "v_mfma_f32_16x16x32_bf16 v[188:191], v[152:155], v[100:103], v[188:191]\n\t"
      "v_mfma_f32_16x16x32_bf16 v[192:195], v[152:155], v[104:107], v[192:195]\n\t"
      "s_waitcnt vmcnt(0)\n\t"
      "s_nop 7\n\t"
      "s_branch NP6B_%=\n\t"
      : "+{s[4:19]}"(s), "+{s[20:27]}"(s2), "+{s[28:29]}"(kr), "+{s30}"(od), "+{v[240:247]}"(v), "+{v[136:139]}"(vo), "+{v140}"(vq)
      :
      : "memory", "vcc", "scc",
        "v0", "v1", "v2", "v3", "v4", "v5", "v6", "v7", "v8", "v9", "v10", "v11", "v12", "v13", "v14", "v15", "v16",
        "v17", "v18", "v19", "v20", "v21", "v22", "v23", "v24", "v25", "v26", "v27", "v28", "v29", "v30", "v31", "v32",
        "v33", "v34", "v35", "v36", "v37", "v38", "v39", "v40", "v41", "v42", "v43", "v44", "v45", "v46", "v47", "v48",
        "v49", "v50", "v51", "v52", "v53", "v54", "v55", "v56", "v57", "v58", "v59", "v60", "v61", "v62", "v63", "v64",
        "v65", "v66", "v67", "v68", "v69", "v70", "v71", "v72", "v73", "v74", "v75", "v76", "v77", "v78", "v79", "v80",
        "v81", "v82", "v83", "v84", "v85", "v86", "v87", "v88", "v89", "v90", "v91", "v92", "v93", "v94", "v95", "v96",
        "v97", "v98", "v99", "v100", "v101", "v102", "v103", "v104", "v105", "v106", "v107", "v108", "v109", "v110",
        "v111", "v112", "v113", "v114", "v115", "v116", "v117", "v118", "v119", "v120", "v121", "v122", "v123", "v124",
        "v125", "v126", "v127", "v128", "v129", "v130", "v131", "v132", "v133", "v134", "v135", "v141", "v142", "v143",
        "v144", "v145", "v146", "v147", "v148", "v149", "v150", "v151", "v152", "v153", "v154", "v155", "v156", "v157",
        "v158", "v159", "v160", "v161", "v162", "v163", "v164", "v165", "v166", "v167", "v168", "v169", "v170", "v171",
        "v172", "v173", "v174", "v175", "v176", "v177", "v178", "v179", "v180", "v181", "v182", "v183", "v184", "v185",
        "v186", "v187", "v188", "v189", "v190", "v191", "v192", "v193", "v194", "v195", "v196", "v197", "v198", "v199",
        "v200", "v201", "v202", "v203", "v204", "v205", "v206", "v207", "v208", "v209", "v210", "v211", "v212", "v213",
        "v214", "v215", "v216", "v217", "v218", "v219", "v220", "v221", "v222", "v223", "v224", "v225", "v226", "v227",
        "v228", "v229", "v230", "v231", "v232", "v233", "v234", "v235", "v236", "v237", "v238", "v239", "v248", "v249",
        "v250", "v251", "v252", "v253", "s31", "s44", "s45", "s46", "s47", "s48", "s49", "s50", "s51", "s52", "s53",
        "s54", "s55", "s56", "s57", "s58", "s59", "s60", "s61", "s62", "s63", "s64", "s65", "s66", "s67", "s68", "s69",
        "s70"
    );
    __builtin_amdgcn_endpgm();
    }
  }
}

extern "C" void kernel_launch(void* const* d_in, const int* in_sizes, int n_in, void* d_out, int out_size,
                              void* d_ws, size_t ws_size, hipStream_t stream) {
  (void)hipFuncSetAttribute((const void*)fused_block, hipFuncAttributeMaxDynamicSharedMemorySize, LDS_BYTES);
  fused_block<<<256, 512, LDS_BYTES, stream>>>((const char*)d_in[0], (const char*)d_in[1], (const char*)d_in[2],
                                               (const char*)d_in[4], (const char*)d_in[5], (const char*)d_in[6],
                                               (const char*)d_in[7], (char*)d_out, (char*)d_ws);
}
```

```cpp
#include <hip/hip_runtime.h>
#include <stdint.h>

#define WS_ST 0
#define WS_HN 2097152
#define WS_O 4194304
#define WS_G 6291456
#define ST_FL 65536
#define ST_PW 114688
#define WS_EP 131072
#define PROBE 1835008
#define ST_CK 3981312
#define ST_H2 4112384
#define ST_H3 8175616
#define ST_H4 8306688
#define A_UNIT 5120
#define A_HALO 40960
#define A_SLOT 43008
#define A_CS 131072
#define A_ST 132096
#define A_EX 142336
#define A_FL 154624
#define A_MK 154752
#define B_UNIT 7168
#define B_SLOT 57344
#define B_X 114688
#define B_OP 928
#define B_PS 129536
#define B_WX 130560
#define B_WY 66048
#define G_WV 16384
#define G_RED 0
#define G_RED2 32768
#define LDS_KEY 163600
#define LDS_SINK 163328
#define LDS_BYTES 163840
#define XO 0

typedef unsigned short u16;
typedef unsigned long long u64;
typedef __attribute__((ext_vector_type(4))) unsigned int u32x4;
typedef __attribute__((ext_vector_type(8))) unsigned int u32x8;
typedef __attribute__((ext_vector_type(16))) unsigned int u32x16;
#define UNI(x) (unsigned)__builtin_amdgcn_readfirstlane((int)(x))
#define PIN() __builtin_amdgcn_sched_barrier(0)

__device__ __forceinline__ int swz_tr(int row) { return (((row >> 1) & 1) | (((row >> 3) & 1) << 1)) << 1; }
__device__ __forceinline__ int swz_row(int row) { return (row >> 1) & 7; }
__device__ __forceinline__ int unit(int blk, int slice, int par) { return (((blk < 14 ? blk : blk - 14) * 8 + slice) * 2 + ((blk < 14 ? 0 : 1) ^ par)) * 8192; }
__device__ __forceinline__ int part(int n, int side) { return (n >> 13) * 16384 + side * 8192 + (n & 8191); }
template <class T>
__device__ __forceinline__ void put(T& s, int i, u64 p) { s[i] = (unsigned)p; s[i + 1] = (unsigned)(p >> 32); }
template <class T>
__device__ __forceinline__ void put2(T& s, int i, unsigned lo, unsigned hi) { s[i] = UNI(lo); s[i + 1] = UNI(hi); }

__global__ __launch_bounds__(512, 2) __attribute__((aligned(4096))) void fused_block(const char* __restrict__ x, const char* __restrict__ wqkv,
                                                   const char* __restrict__ wo, const char* __restrict__ w1,
                                                   const char* __restrict__ b1, const char* __restrict__ w2,
                                                   const char* __restrict__ b2, char* __restrict__ out,
                                                   char* __restrict__ ws0) {
  u32x8 f;
  asm volatile(
      "s_load_dwordx4 s[4:7], s[0:1], 0x0\n\t"
      "v_readfirstlane_b32 s3, v0\n\t"
      "s_getpc_b64 s[40:41]\n\t"
      "s_lshr_b32 s12, s2, 3\n\t"
      "s_or_b32 s13, s3, s12\n\t"
      "s_and_b32 s13, s13, 0x1d0\n\t"
      "s_cmp_lg_u32 s13, 16\n\t"
      "s_cbranch_scc1 ET2_%=\n\t"
      "s_and_b32 s12, s12, 7\n\t"
      "s_mov_b32 m0, 163328\n\t"
      "v_lshl_add_u32 v3, s12, 6, v0\n\t"
      "v_min_u32_e32 v3, 495, v3\n\t"
      "v_lshlrev_b32_e32 v3, 7, v3\n\t"
      "global_load_lds_dword v3, s[40:41]\n\t"
      "ET2_%=:\n\t"
      "v_lshrrev_b32_e32 v1, 3, v0\n\t"
      "v_and_b32_e32 v2, 7, v0\n\t"
      "s_and_b32 s17, s2, 7\n\t"
      "s_lshr_b32 s18, s2, 3\n\t"
      "s_lshr_b32 s3, s3, 6\n\t"
      "s_cmpk_gt_u32 s2, 0x7f\n\t"
      "s_cbranch_scc1 EB_%=\n\t"
      "v_and_b32_e32 v3, 2, v1\n\t"
      "v_lshrrev_b32_e32 v4, 1, v1\n\t"
      "s_lshr_b32 s19, s18, 2\n\t"
      "s_and_b32 s12, s17, 6\n\t"
      "s_lshl1_add_u32 s19, s12, s19\n\t"
      "v_and_or_b32 v3, v4, 4, v3\n\t"
      "v_xor_b32_e32 v3, v3, v2\n\t"
      "v_min_u32_e32 v3, 6, v3\n\t"
      "v_mul_u32_u24_e32 v5, 2688, v1\n\t"
      "v_lshl_add_u32 v3, v3, 4, v5\n\t"
      "v_add_u32_e32 v195, 2048, v3\n\t"
      "v_add_u32_e32 v196, 1920, v3\n\t"
      "v_add_u32_e32 v197, 5888, v3\n\t"
      "s_mul_i32 s12, s19, 114688\n\t"
      "v_and_b32_e32 v5, 0x38, v1\n\t"
      "v_add_u32_e32 v5, v5, v1\n\t"
      "v_mul_u32_u24_e32 v5, 896, v5\n\t"
      "s_and_b32 s14, s17, 1\n\t"
      "s_add_u32 s14, s14, 3\n\t"
      "s_and_b32 s13, s18, 3\n\t"
      "s_xor_b32 s13, s13, s14\n\t"
      "s_mul_i32 s13, s13, 112\n\t"
      "v_and_b32_e32 v6, 3, v4\n\t"
      "v_xor_b32_e32 v6, v6, v2\n\t"
      "v_lshl_add_u32 v5, v6, 4, v5\n\t"
      "s_sub_u32 s12, s12, 15360\n\t"
      "s_ashr_i32 s14, s12, 31\n\t"
      "s_sub_u32 s13, s13, 4096\n\t"
      "v_add_u32_e32 v192, 15360, v5\n\t"
      "v_xor_b32_e32 v5, 64, v5\n\t"
      "v_add_u32_e32 v193, 21504, v5\n\t"
      "s_mul_i32 s9, s3, 5120\n\t"
      "s_sub_u32 s3, s3, 4\n\t"
      "s_cbranch_scc0 EH_%=\n\t"
      "s_waitcnt lgkmcnt(0)\n\t"
      "s_add_u32 s4, s4, s12\n\t"
      "s_addc_u32 s5, s5, s14\n\t"
      "s_add_u32 s6, s6, s13\n\t"
      "s_addc_u32 s7, s7, -1\n\t"
      "s_add_u32 m0, s9, 0\n\t"
      "s_nop 0\n\t"
      "global_load_lds_dwordx4 v195, s[6:7] offset:2048\n\t"
      "global_load_lds_dwordx4 v192, s[4:5]\n\t"
      "global_load_lds_dwordx4 v193, s[4:5] offset:1024\n\t"
      "global_load_lds_dwordx4 v196, s[6:7] offset:3072\n\t"
      "s_add_u32 m0, s9, 4096\n\t"
      "s_nop 0\n\t"
      "global_load_lds_dwordx4 v197, s[6:7]\n\t"
      "s_branch EX_%=\n\t"
      "EH_%=:\n\t"
      "s_cmp_eq_u32 s19, 0\n\t"
      "s_cselect_b32 s13, 16, 0\n\t"
      "s_lshl2_add_u32 s13, s3, s13\n\t"
      "v_and_or_b32 v3, v1, 3, s13\n\t"
      "v_bfe_u32 v4, v3, 1, 3\n\t"
      "v_mul_u32_u24_e32 v3, 896, v3\n\t"
      "v_xor_b32_e32 v4, v4, v2\n\t"
      "v_lshl_add_u32 v3, v4, 4, v3\n\t"
      "v_add_u32_e32 v194, 1024, v3\n\t"
      "s_waitcnt lgkmcnt(0)\n\t"
      "s_branch EX_%=\n\t"
      "EB_%=:\n\t"
      "v_and_b32_e32 v3, 63, v0\n\t"
      "v_lshlrev_b32_e32 v3, 4, v3\n\t"
      "s_sub_u32 s18, s18, 16\n\t"
      "s_min_u32 s16, s3, 6\n\t"
      "s_xor_b32 s15, s17, 1\n\t"
      "s_lshl4_add_u32 s15, s15, s18\n\t"
      "s_mul_i32 s15, s15, 14336\n\t"
      "s_lshl_b32 s12, s16, 7\n\t"
      "s_add_u32 s15, s15, s12\n\t"
      "v_mul_u32_u24_e32 v6, 56, v3\n\t"
      "v_add_u32_e32 v6, s15, v6\n\t"
      "s_add_u32 s19, s17, 0\n\t"
      "s_and_b32 s19, s19, 3\n\t"
      "s_and_b32 s12, s17, 4\n\t"
      "s_or_b32 s19, s19, s12\n\t"
      "s_lshl4_add_u32 s19, s19, s18\n\t"
      "s_lshl3_add_u32 s12, s19, s3\n\t"
      "s_lshl_b32 s12, s12, 11\n\t"
      "s_mul_i32 s19, s19, 14336\n\t"
      "s_lshl_b32 s16, s16, 11\n\t"
      "s_add_u32 s19, s19, s16\n\t"
      "s_add_u32 s8, s16, 114688\n\t"
      "v_add_u32_e32 v1, s19, v3\n\t"
      "s_lshl4_add_u32 s16, s3, s18\n\t"
      "v_bfe_u32 v4, v0, 2, 2\n\t"
      "v_mul_u32_u24_e32 v4, 896, v4\n\t"
      "v_and_b32_e32 v5, 48, v3\n\t"
      "v_lshl_add_u32 v4, v5, 3, v4\n\t"
      "s_and_b32 s13, s17, 1\n\t"
      "s_mul_i32 s13, s13, 384\n\t"
      "v_add_u32_e32 v4, s13, v4\n\t"
      "v_lshrrev_b32_e32 v2, 8, v3\n\t"
      "s_mul_i32 s13, s16, 1\n\t"
      "s_mul_i32 s14, s16, 3\n\t"
      "s_add_u32 s14, s14, 128\n\t"
      "v_add_u32_e32 v5, s13, v2\n\t"
      "v_add_u32_e32 v2, s14, v2\n\t"
      "v_min_u32_e32 v2, 447, v2\n\t"
      "s_movk_i32 s13, 2688\n\t"
      "v_mad_u32_u24 v5, v5, s13, v4\n\t"
      "v_mad_u32_u24 v4, v2, s13, v4\n\t"
      "s_mov_b32 m0, 163328\n\t"
      "s_waitcnt lgkmcnt(0)\n\t"
      "s_mov_b32 m0, s8\n\t"
      "s_nop 0\n\t"
      "global_load_lds_dwordx4 v1, s[4:5]\n\t"
      "global_load_lds_dwordx4 v1, s[4:5] offset:1024\n\t"
      "s_mov_b32 m0, 163328\n\t"
      "s_mov_b32 exec_lo, 0xfff\n\t"
      "s_mov_b32 exec_hi, 0x0\n\t"
      "global_load_lds_dword v5, s[6:7]\n\t"
      "s_mov_b64 exec, -1\n\t"
      "s_barrier\n\t"
      "s_mov_b32 exec_lo, 0xfff0fff\n\t"
      "s_mov_b32 exec_hi, 0xfff\n\t"
      "global_load_lds_dword v4, s[6:7]\n\t"
      "s_mov_b64 exec, -1\n\t"
      "s_mov_b64 exec, 0xffff\n\t"
      "global_load_lds_dword v6, s[4:5]\n\t"
      "s_mov_b64 exec, -1\n\t"
      "EX_%=:\n\t"
      : "={v[192:199]}"(f)
      : "{s[0:1]}"(__builtin_amdgcn_kernarg_segment_ptr()), "{s2}"(blockIdx.x), "{v0}"(threadIdx.x)
      : "memory", "scc", "s3", "s4", "s5", "s6", "s7", "s8", "s9", "s10", "s11", "s12", "s13", "s14", "s15", "s16", "s17", "s18", "s19", "v1", "v2",
        "v3", "v4", "v5", "v6", "s40", "s41", "s42", "s43", "s48", "s49", "s50"
      );
  PIN();
  char* const ws = ws0 + (-(unsigned)(uintptr_t)ws0 & ((2u << 20) - 1));
  const int wave = __builtin_amdgcn_readfirstlane(threadIdx.x >> 6);
  const int xcd = blockIdx.x & 7, idx = blockIdx.x >> 3, side = xcd >> 2, xp = xcd & 3;
#define LANE_BITS(l) \
  const int lane = (l), g = lane >> 4, c = lane & 15, q4 = c >> 2, p4 = lane & 3, lr = lane >> 3, lc = lane & 7

  if (__builtin_expect(idx < 16, 1)) {
    const int rt = 2 * (xcd & 6) + (idx >> 2), hp = (idx & 3) ^ (3 + (xcd & 1)), hh = (wave >> 1) & 1, half = wave & 1;
    const unsigned sta = A_ST + half * 1024;
    if (wave < 4) {
      LANE_BITS(threadIdx.x & 63);
      u32x16 v;
      u32x8 s;
      v[0] = 4 * half * A_UNIT + c * 128 + ((g ^ swz_row(c)) << 4);
      v[1] = A_HALO + c * 128 + ((g ^ swz_row(c)) << 4);
#pragma unroll
      for (int j = 0; j < 2; ++j) {
        const int by = 56 * hh + 32 * j + 8 * p4;
        v[2 + j] = g * A_UNIT + 2048 + q4 * 128 + (((by >> 4) ^ swz_tr(8 * g + q4)) << 4) + (by & 8);
      }
      const unsigned csa = A_CS + hh * 384, exa = A_EX + hh * 3 * 2048;
      v[4] = csa + g * 16;
      v[5] = csa + c * 4;
      v[6] = v[4] + 64 * half;
      v[7] = v[5] + 64 * half;
      v[8] = A_FL + (wave ^ 1) * 8;
      v[9] = sta + g * 16;
      v[10] = sta + c * 4;
      const int key = 4 * g + (c & 1);
      v[11] = sta + 384 + (key <= c ? 128 : 64) + key * 4;
      unsigned vk1 = sta + 384 + (key + 2 <= c ? 128 : 64) + key * 4 + 8;
      v[12] = exa + half * 2048 + lane * 16;
      v[13] = exa + half * 2048 + lane * 8;
      v[14] = exa + lane * 16 + 8 * half;
      v[15] = lane * 16;
      put(s, 0, (u64)(ws + WS_O + part(((8 * (rt & 7) + 4 * half) * 16 + 2 * hp + hh) * 1024, 0)));
      s[2] = half;
      s[3] = UNI(rt == 0 && half == 0 ? 0xff800000u : 0u);
      s[4] = exa + 1024 + 512 * half;
      s[5] = A_FL + 32 + 4 * half;
      put(s, 6, (u64)(ws + WS_ST));
      unsigned hint = ST_H2 + part(((((rt & 7) * 8 + hp) * 2 + half) * 2 + hh) * 64, side);
      unsigned chk = ST_CK + part((((rt & 7) * 2 + half) * 16 + 2 * hp + hh) * 256, side);
      unsigned vn = (((wave ^ (4 * side)) * 256 + (idx & 1) * 128 + lr) * 28 + 7 * xp + min(idx >> 1, 6)) * 128 + ((lc ^ swz_tr(lr)) << 4);
      u64 tw = (u64)w1;
      unsigned lw = wave * 4096;
    asm volatile(
      "s_waitcnt lgkmcnt(0)\n\ts_nop 4\n\t"
      ".p2align 3\n\t"
      "v_xor_b32_e64 v248, 64, v224\n\t"
      "v_xor_b32_e32 v249, 64, v225\n\t"
      "s_mov_b32 s36, 0xaaaaaaaa\n\t"
      "s_mov_b32 s37, 0xaaaaaaaa\n\t"
      "s_mov_b32 s38, 0xfff0ffff\n\t"
      "s_mov_b32 s39, 0xf000ff00\n\t"
      "s_mov_b32 s40, 0xffc0fffc\n\t"
      "s_mov_b32 s41, 0xc000fc00\n\t"
      "s_mov_b32 s42, 0x0\n\t"
      "s_mov_b32 s43, 0xffff0000\n\t"
      "s_mov_b32 s44, 0x10001\n\t"
      "s_mov_b32 s45, 0x10001\n\t"
      "v_mov_b32_e32 v218, 0x0c0c0c0c\n\t"
      "v_mov_b32_e32 v216, 0xc0c0100\n\t"
      "v_mov_b32_e32 v217, 0x1000c0c\n\t"
      "v_cndmask_b32_e64 v216, v216, v217, s[36:37]\n\t"
      "v_cndmask_b32_e64 v240, v216, v218, s[38:39]\n\t"
      "v_cndmask_b32_e64 v242, v218, v216, s[38:39]\n\t"
      "v_mov_b32_e32 v216, 0xc0c0302\n\t"
      "v_mov_b32_e32 v217, 0x3020c0c\n\t"
      "v_cndmask_b32_e64 v216, v216, v217, s[36:37]\n\t"
      "v_cndmask_b32_e64 v241, v216, v218, s[40:41]\n\t"
      "v_cndmask_b32_e64 v243, v218, v216, s[40:41]\n\t"
      "v_xor_b32_e32 v213, 8, v232\n\t"
      "v_lshrrev_b32_e32 v214, 1, v239\n\t"
      "v_add_u32_e32 v214, s8, v214\n\t"
      "v_mov_b32_e32 v215, s9\n\t"
      "v_mov_b32_e32 v220, 154752\n\t"
      "v_lshrrev_b32_e32 v221, 2, v239\n\t"
      "s_add_u32 s56, s4, 32768\n\t"
      "s_addc_u32 s57, s5, 0\n\t"
      "s_add_u32 s58, s4, 65536\n\t"
      "s_addc_u32 s59, s5, 0\n\t"
      "s_add_u32 s60, s4, 98304\n\t"
      "s_addc_u32 s61, s5, 0\n\t"
      "s_and_b32 s52, s12, 0x2000\n\t"
      "v_mov_b32_e32 v244, s7\n\t"
      "v_mov_b32_e32 v245, s7\n\t"
      "v_mov_b32_e32 v246, s7\n\t"
      "v_mov_b32_e32 v247, s7\n\t"
      "v_mov_b64_e32 v[0:1], 0\n\t"
      "v_mov_b64_e32 v[2:3], 0\n\t"
      "v_mov_b64_e32 v[4:5], 0\n\t"
      "v_mov_b64_e32 v[6:7], 0\n\t"
      "v_mov_b64_e32 v[8:9], 0\n\t"
      "v_mov_b64_e32 v[10:11], 0\n\t"
      "v_mov_b64_e32 v[12:13], 0\n\t"
      "v_mov_b64_e32 v[14:15], 0\n\t"
      "v_mov_b64_e32 v[16:17], 0\n\t"
      "v_mov_b64_e32 v[18:19], 0\n\t"
      "v_mov_b64_e32 v[20:21], 0\n\t"
      "v_mov_b64_e32 v[22:23], 0\n\t"
      "v_mov_b64_e32 v[24:25], 0\n\t"
      "v_mov_b64_e32 v[26:27], 0\n\t"
      "v_mov_b64_e32 v[28:29], 0\n\t"
      "v_mov_b64_e32 v[30:31], 0\n\t"
      "v_mov_b64_e32 v[32:33], 0\n\t"
      "v_mov_b64_e32 v[34:35], 0\n\t"
      "v_mov_b64_e32 v[36:37], 0\n\t"
      "v_mov_b64_e32 v[38:39], 0\n\t"
      "v_mov_b64_e32 v[40:41], 0\n\t"
      "v_mov_b64_e32 v[42:43], 0\n\t"
      "v_mov_b64_e32 v[44:45], 0\n\t"
      "v_mov_b64_e32 v[46:47], 0\n\t"
      "v_mov_b64_e32 v[48:49], 0\n\t"
      "v_mov_b64_e32 v[50:51], 0\n\t"
      "v_mov_b64_e32 v[52:53], 0\n\t"
      "v_mov_b64_e32 v[54:55], 0\n\t"
      "v_mov_b64_e32 v[56:57], 0\n\t"
      "v_mov_b64_e32 v[58:59], 0\n\t"
      "v_mov_b64_e32 v[60:61], 0\n\t"
      "v_mov_b64_e32 v[62:63], 0\n\t"
      "v_mov_b64_e32 v[64:65], 0\n\t"
      "v_mov_b64_e32 v[66:67], 0\n\t"
      "v_mov_b64_e32 v[68:69], 0\n\t"
      "v_mov_b64_e32 v[70:71], 0\n\t"
      "v_mov_b64_e32 v[72:73], 0\n\t"
      "v_mov_b64_e32 v[74:75], 0\n\t"
      "v_mov_b64_e32 v[76:77], 0\n\t"
      "v_mov_b64_e32 v[78:79], 0\n\t"
      "v_mov_b64_e32 v[80:81], 0\n\t"
      "v_mov_b64_e32 v[82:83], 0\n\t"
      "v_mov_b64_e32 v[84:85], 0\n\t"
      "v_mov_b64_e32 v[86:87], 0\n\t"
      "v_mov_b64_e32 v[88:89], 0\n\t"
      "v_mov_b64_e32 v[90:91], 0\n\t"
      "v_mov_b64_e32 v[92:93], 0\n\t"
      "v_mov_b64_e32 v[94:95], 0\n\t"
      "v_mov_b64_e32 v[96:97], 0\n\t"
      "v_mov_b64_e32 v[98:99], 0\n\t"
      "v_mov_b64_e32 v[100:101], 0\n\t"
      "v_mov_b64_e32 v[102:103], 0\n\t"
      "v_mov_b64_e32 v[104:105], 0\n\t"
      "v_mov_b64_e32 v[106:107], 0\n\t"
      "s_mov_b32 s47, 6\n\t"
      "s_mov_b64 exec, 0\n\t"
      "s_cmpk_eq_u32 s16, 0x0\n\t"
      "s_cbranch_scc1 .LhopA0\n\t"
      "s_cmpk_eq_u32 s16, 0x1000\n\t"
      "s_cbranch_scc1 .LhopA1\n\t"
      "s_cmpk_eq_u32 s16, 0x2000\n\t"
      "s_cbranch_scc1 .LhopA2\n\t"
      "s_cmpk_eq_u32 s16, 0x3000\n\t"
      "s_cbranch_scc1 .LhopA3\n\t"
      ".LbackA:\n\t"
      "s_mov_b64 exec, -1\n\t"
      "s_cmp_eq_u32 s6, 0\n\t"
      "s_cbranch_scc0 LOOP1_%=\n\t"
      "s_waitcnt vmcnt(0)\n\t"
      "s_barrier\n\t"
      "ds_read_b64_tr_b16 v[108:109], v226\n\t"
      "ds_read_b64_tr_b16 v[110:111], v226 offset:512\n\t"
      "ds_read_b128 v[132:135], v224\n\t"
      "ds_read_b128 v[136:139], v224 offset:5120\n\t"
      "ds_read_b128 v[140:143], v224 offset:10240\n\t"
      "ds_read_b128 v[144:147], v224 offset:15360\n\t"
      "ds_read_b64_tr_b16 v[112:113], v227\n\t"
      "ds_read_b64_tr_b16 v[114:115], v227 offset:512\n\t"
      "ds_read_b128 v[148:151], v225\n\t"
      "ds_read_b64_tr_b16 v[116:117], v226 offset:1024\n\t"
      "ds_read_b64_tr_b16 v[118:119], v226 offset:1536\n\t"
      "ds_read_b64_tr_b16 v[120:121], v227 offset:1024\n\t"
      "ds_read_b64_tr_b16 v[122:123], v227 offset:1536\n\t"
      "ds_read_b64_tr_b16 v[124:125], v226 offset:2048\n\t"
      "ds_read_b64_tr_b16 v[126:127], v226 offset:2560\n\t"
      "ds_read_b64_tr_b16 v[128:129], v227 offset:2048\n\t"
      "ds_read_b64_tr_b16 v[130:131], v227 offset:2560\n\t"
      "s_branch EVEN0_%=\n\t"
      "s_waitcnt lgkmcnt(0)\n\t"
      ".p2align 6\n\t"
      ".LhopA1:\n\t"
      "s_cbranch_execz .LhopA5\n\t"
      "s_nop 0\n\t"
      "ODD0_%=:\n\t"
      "s_waitcnt lgkmcnt(0)\n\t"
      "v_mfma_f32_16x16x32_bf16 v[0:3], v[152:155], v[176:179], v[0:3]\n\t"
      "v_add_u32_e32 v224, s46, v224\n\t"
      "v_add_u32_e32 v226, s46, v226\n\t"
      "v_mfma_f32_16x16x32_bf16 v[24:27], v[152:155], v[180:183], v[24:27]\n\t"
      "v_add_u32_e32 v227, s46, v227\n\t"
      "v_add_u32_e32 v225, s46, v225\n\t"
      "v_mfma_f32_16x16x32_bf16 v[48:51], v[152:155], v[184:187], v[48:51]\n\t"
      "s_barrier\n\t"
      "ds_read_b64_tr_b16 v[108:109], v226\n\t"
      "v_mfma_f32_16x16x32_bf16 v[72:75], v[152:155], v[188:191], v[72:75]\n\t"
      "ds_read_b64_tr_b16 v[110:111], v226 offset:512\n\t"
      "v_mfma_f32_16x16x32_bf16 v[4:7], v[156:159], v[176:179], v[4:7]\n\t"
      "ds_read_b128 v[132:135], v224\n\t"
      "v_mfma_f32_16x16x32_bf16 v[28:31], v[156:159], v[180:183], v[28:31]\n\t"
      "ds_read_b128 v[136:139], v224 offset:5120\n\t"
      "v_mfma_f32_16x16x32_bf16 v[52:55], v[156:159], v[184:187], v[52:55]\n\t"
      "ds_read_b128 v[140:143], v224 offset:10240\n\t"
      "v_mfma_f32_16x16x32_bf16 v[76:79], v[156:159], v[188:191], v[76:79]\n\t"
      "ds_read_b128 v[144:147], v224 offset:15360\n\t"
      "v_mfma_f32_16x16x32_bf16 v[96:99], v[152:155], v[192:195], v[96:99]\n\t"
      "v_mfma_f32_16x16x32_bf16 v[8:11], v[160:163], v[176:179], v[8:11]\n\t"
      "ds_read_b64_tr_b16 v[112:113], v227\n\t"
      "v_mfma_f32_16x16x32_bf16 v[32:35], v[160:163], v[180:183], v[32:35]\n\t"
      "ds_read_b64_tr_b16 v[114:115], v227 offset:512\n\t"
      "v_mfma_f32_16x16x32_bf16 v[56:59], v[160:163], v[184:187], v[56:59]\n\t"
      "ds_read_b128 v[148:151], v225\n\t"
      "v_mfma_f32_16x16x32_bf16 v[80:83], v[160:163], v[188:191], v[80:83]\n\t"
      "ds_read_b64_tr_b16 v[116:117], v226 offset:1024\n\t"
      "v_mfma_f32_16x16x32_bf16 v[12:15], v[164:167], v[176:179], v[12:15]\n\t"
      "ds_read_b64_tr_b16 v[118:119], v226 offset:1536\n\t"
      "v_mfma_f32_16x16x32_bf16 v[36:39], v[164:167], v[180:183], v[36:39]\n\t"
      "ds_read_b64_tr_b16 v[120:121], v227 offset:1024\n\t"
      "v_mfma_f32_16x16x32_bf16 v[60:63], v[164:167], v[184:187], v[60:63]\n\t"
      "v_mfma_f32_16x16x32_bf16 v[84:87], v[164:167], v[188:191], v[84:87]\n\t"
      "ds_read_b64_tr_b16 v[122:123], v227 offset:1536\n\t"
      "v_mfma_f32_16x16x32_bf16 v[100:103], v[160:163], v[192:195], v[100:103]\n\t"
      "ds_read_b64_tr_b16 v[124:125], v226 offset:2048\n\t"
      "v_mfma_f32_16x16x32_bf16 v[16:19], v[176:179], v[168:171], v[16:19]\n\t"
      "ds_read_b64_tr_b16 v[126:127], v226 offset:2560\n\t"
      "v_mfma_f32_16x16x32_bf16 v[40:43], v[180:183], v[168:171], v[40:43]\n\t"
      "ds_read_b64_tr_b16 v[128:129], v227 offset:2048\n\t"
      "v_mfma_f32_16x16x32_bf16 v[64:67], v[184:187], v[168:171], v[64:67]\n\t"
      "ds_read_b64_tr_b16 v[130:131], v227 offset:2560\n\t"
      "v_mfma_f32_16x16x32_bf16 v[88:91], v[188:191], v[168:171], v[88:91]\n\t"
      "v_mfma_f32_16x16x32_bf16 v[20:23], v[176:179], v[172:175], v[20:23]\n\t"
      "v_mfma_f32_16x16x32_bf16 v[44:47], v[180:183], v[172:175], v[44:47]\n\t"
      "v_add_u32_e32 v248, s46, v248\n\t"
      "v_add_u32_e32 v249, s46, v249\n\t"
      "v_mfma_f32_16x16x32_bf16 v[68:71], v[184:187], v[172:175], v[68:71]\n\t"
      "v_mfma_f32_16x16x32_bf16 v[92:95], v[188:191], v[172:175], v[92:95]\n\t"
      "v_mfma_f32_16x16x32_bf16 v[104:107], v[192:195], v[168:171], v[104:107]\n\t"
      ".LhopA2:\n\t"
      "s_cbranch_execz .LhopA6\n\t"
      "EVEN0_%=:\n\t"
      "s_waitcnt lgkmcnt(0)\n\t"
      "v_mfma_f32_16x16x32_bf16 v[0:3], v[108:111], v[132:135], v[0:3]\n\t"
      "ds_read_b64_tr_b16 v[152:153], v226 offset:20480\n\t"
      "v_mfma_f32_16x16x32_bf16 v[24:27], v[108:111], v[136:139], v[24:27]\n\t"
      "ds_read_b64_tr_b16 v[154:155], v226 offset:20992\n\t"
      "v_mfma_f32_16x16x32_bf16 v[48:51], v[108:111], v[140:143], v[48:51]\n\t"
      "ds_read_b128 v[176:179], v248\n\t"
      "v_mfma_f32_16x16x32_bf16 v[72:75], v[108:111], v[144:147], v[72:75]\n\t"
      "ds_read_b128 v[180:183], v248 offset:5120\n\t"
      "v_mfma_f32_16x16x32_bf16 v[4:7], v[112:115], v[132:135], v[4:7]\n\t"
      "v_mfma_f32_16x16x32_bf16 v[28:31], v[112:115], v[136:139], v[28:31]\n\t"
      "ds_read_b128 v[184:187], v248 offset:10240\n\t"
      "v_mfma_f32_16x16x32_bf16 v[52:55], v[112:115], v[140:143], v[52:55]\n\t"
      "ds_read_b128 v[188:191], v248 offset:15360\n\t"
      "v_mfma_f32_16x16x32_bf16 v[76:79], v[112:115], v[144:147], v[76:79]\n\t"
      "ds_read_b64_tr_b16 v[156:157], v227 offset:20480\n\t"
      "v_mfma_f32_16x16x32_bf16 v[96:99], v[108:111], v[148:151], v[96:99]\n\t"
      "v_mfma_f32_16x16x32_bf16 v[8:11], v[116:119], v[132:135], v[8:11]\n\t"
      "ds_read_b64_tr_b16 v[158:159], v227 offset:20992\n\t"
      "v_mfma_f32_16x16x32_bf16 v[32:35], v[116:119], v[136:139], v[32:35]\n\t"
      "ds_read_b128 v[192:195], v249\n\t"
      "v_mfma_f32_16x16x32_bf16 v[56:59], v[116:119], v[140:143], v[56:59]\n\t"
      "ds_read_b64_tr_b16 v[160:161], v226 offset:21504\n\t"
      "v_mfma_f32_16x16x32_bf16 v[80:83], v[116:119], v[144:147], v[80:83]\n\t"
      "ds_read_b64_tr_b16 v[162:163], v226 offset:22016\n\t"
      "v_mfma_f32_16x16x32_bf16 v[12:15], v[120:123], v[132:135], v[12:15]\n\t"
      "v_mfma_f32_16x16x32_bf16 v[36:39], v[120:123], v[136:139], v[36:39]\n\t"
      "ds_read_b64_tr_b16 v[164:165], v227 offset:21504\n\t"
      "v_mfma_f32_16x16x32_bf16 v[60:63], v[120:123], v[140:143], v[60:63]\n\t"
      "ds_read_b64_tr_b16 v[166:167], v227 offset:22016\n\t"
      "v_mfma_f32_16x16x32_bf16 v[84:87], v[120:123], v[144:147], v[84:87]\n\t"
      "ds_read_b64_tr_b16 v[168:169], v226 offset:22528\n\t"
      "v_mfma_f32_16x16x32_bf16 v[100:103], v[116:119], v[148:151], v[100:103]\n\t"
      "v_mfma_f32_16x16x32_bf16 v[16:19], v[132:135], v[124:127], v[16:19]\n\t"
      "ds_read_b64_tr_b16 v[170:171], v226 offset:23040\n\t"
      "v_mfma_f32_16x16x32_bf16 v[40:43], v[136:139], v[124:127], v[40:43]\n\t"
      "ds_read_b64_tr_b16 v[172:173], v227 offset:22528\n\t"
      "v_mfma_f32_16x16x32_bf16 v[64:67], v[140:143], v[124:127], v[64:67]\n\t"
      "ds_read_b64_tr_b16 v[174:175], v227 offset:23040\n\t"
      "s_cmp_eq_u32 s47, 4\n\t"
      "s_cselect_b32 s46, -2, 1\n\t"
      "v_mfma_f32_16x16x32_bf16 v[88:91], v[144:147], v[124:127], v[88:91]\n\t"
      "s_cmp_eq_u32 s47, 1\n\t"
      "s_cselect_b32 s46, -2, s46\n\t"
      "v_mfma_f32_16x16x32_bf16 v[20:23], v[132:135], v[128:131], v[20:23]\n\t"
      "s_mul_i32 s46, s46, 43008\n\t"
      "v_mfma_f32_16x16x32_bf16 v[44:47], v[136:139], v[128:131], v[44:47]\n\t"
      "v_mfma_f32_16x16x32_bf16 v[68:71], v[140:143], v[128:131], v[68:71]\n\t"
      "v_mfma_f32_16x16x32_bf16 v[92:95], v[144:147], v[128:131], v[92:95]\n\t"
      "s_sub_u32 s47, s47, 1\n\t"
      "v_mfma_f32_16x16x32_bf16 v[104:107], v[148:151], v[124:127], v[104:107]\n\t"
      "s_cbranch_scc0 ODD0_%=\n\t"
      "s_waitcnt lgkmcnt(0)\n\t"
      "v_mfma_f32_16x16x32_bf16 v[96:99], v[152:155], v[192:195], v[96:99]\n\t"
      "ds_read_b32 v212, v215\n\t"
      "v_mfma_f32_16x16x32_bf16 v[100:103], v[160:163], v[192:195], v[100:103]\n\t"
      "ds_read2_b32 v[134:135], v234 offset0:80 offset1:80\n\t"
      "v_mfma_f32_16x16x32_bf16 v[104:107], v[192:195], v[168:171], v[104:107]\n\t"
      "ds_read_b128 v[148:151], v233 offset:320\n\t"
      "v_mfma_f32_16x16x32_bf16 v[80:83], v[160:163], v[188:191], v[80:83]\n\t"
      "ds_read2_b32 v[248:249], v234 offset0:0 offset1:0\n\t"
      "v_mfma_f32_16x16x32_bf16 v[84:87], v[164:167], v[188:191], v[84:87]\n\t"
      "ds_read_b128 v[196:199], v233\n\t"
      "v_mfma_f32_16x16x32_bf16 v[88:91], v[188:191], v[168:171], v[88:91]\n\t"
      "ds_read2_b32 v[132:133], v234 offset0:64 offset1:64\n\t"
      "v_mfma_f32_16x16x32_bf16 v[92:95], v[188:191], v[172:175], v[92:95]\n\t"
      "s_mov_b64 exec, s[44:45]\n\t"
      "ds_write_b128 v230, v[96:99]\n\t"
      "ds_write_b128 v230, v[100:103] offset:128\n\t"
      "s_mov_b64 exec, 0xffff\n\t"
      "ds_write_b32 v231, v104 offset:256\n\t"
      "ds_write_b32 v213, v213\n\t"
      "s_mov_b64 exec, -1\n\t"
      "v_mfma_f32_16x16x32_bf16 v[56:59], v[160:163], v[184:187], v[56:59]\n\t"
      "ds_read2_b32 v[130:131], v234 offset0:48 offset1:48\n\t"
      "v_mfma_f32_16x16x32_bf16 v[60:63], v[164:167], v[184:187], v[60:63]\n\t"
      "ds_read2_b32 v[128:129], v234 offset0:32 offset1:32\n\t"
      "v_mfma_f32_16x16x32_bf16 v[64:67], v[184:187], v[168:171], v[64:67]\n\t"
      "s_waitcnt lgkmcnt(0)\n\t"
      "v_mfma_f32_16x16x32_bf16 v[68:71], v[184:187], v[172:175], v[68:71]\n\t"
      "ds_read_b32 v226, v232\n\t"
      "ds_read_b128 v[116:119], v228 offset:128\n\t"
      "v_mfma_f32_16x16x32_bf16 v[32:35], v[160:163], v[180:183], v[32:35]\n\t"
      "ds_read_b128 v[120:123], v228 offset:192\n\t"
      "v_mfma_f32_16x16x32_bf16 v[36:39], v[164:167], v[180:183], v[36:39]\n\t"
      "ds_read2_b32 v[250:251], v229 offset0:64 offset1:64\n\t"
      "v_mfma_f32_16x16x32_bf16 v[40:43], v[180:183], v[168:171], v[40:43]\n\t"
      "ds_read2_b32 v[252:253], v229 offset0:80 offset1:80\n\t"
      "v_mfma_f32_16x16x32_bf16 v[44:47], v[180:183], v[172:175], v[44:47]\n\t"
      "ds_read_b128 v[124:127], v230 offset:128\n\t"
      "v_mfma_f32_16x16x32_bf16 v[8:11], v[160:163], v[176:179], v[8:11]\n\t"
      "ds_read2_b32 v[224:225], v231 offset0:64 offset1:64\n\t"
      "v_mfma_f32_16x16x32_bf16 v[12:15], v[164:167], v[176:179], v[12:15]\n\t"
      "v_mfma_f32_16x16x32_bf16 v[16:19], v[176:179], v[168:171], v[16:19]\n\t"
      "s_waitcnt lgkmcnt(6)\n\t"
      "v_min_u32_e32 v226, v226, v212\n\t"
      "v_cmp_eq_u32_e32 vcc, 0, v226\n\t"
      "v_mfma_f32_16x16x32_bf16 v[20:23], v[176:179], v[172:175], v[20:23]\n\t"
      "LOOPX_%=:\n\t"
      "s_cbranch_vccnz XAR_%=\n\t"
      "XAB_%=:\n\t"
      "v_mfma_f32_16x16x32_bf16 v[72:75], v[152:155], v[188:191], v[72:75]\n\t"
      "s_waitcnt lgkmcnt(4)\n\t"
      "v_fmac_f32_e32 v80, v134, v116\n\t"
      "v_fmac_f32_e32 v81, v134, v117\n\t"
      "v_mfma_f32_16x16x32_bf16 v[76:79], v[156:159], v[188:191], v[76:79]\n\t"
      "v_fmac_f32_e32 v82, v134, v118\n\t"
      "v_fmac_f32_e32 v83, v134, v119\n\t"
      "v_mfma_f32_16x16x32_bf16 v[48:51], v[152:155], v[184:187], v[48:51]\n\t"
      "v_fmac_f32_e32 v84, v134, v120\n\t"
      "v_fmac_f32_e32 v85, v134, v121\n\t"
      "v_mfma_f32_16x16x32_bf16 v[52:55], v[156:159], v[184:187], v[52:55]\n\t"
      "v_fmac_f32_e32 v86, v134, v122\n\t"
      "v_fmac_f32_e32 v87, v134, v123\n\t"
      "v_mfma_f32_16x16x32_bf16 v[24:27], v[152:155], v[180:183], v[24:27]\n\t"
      "v_cvt_pk_bf16_f32 v80, v80, v81\n\t"
      "v_cvt_pk_bf16_f32 v81, v82, v83\n\t"
      "v_mfma_f32_16x16x32_bf16 v[28:31], v[156:159], v[180:183], v[28:31]\n\t"
      "v_cvt_pk_bf16_f32 v82, v84, v85\n\t"
      "v_cvt_pk_bf16_f32 v83, v86, v87\n\t"
      "v_mfma_f32_16x16x32_bf16 v[0:3], v[152:155], v[176:179], v[0:3]\n\t"
      "s_waitcnt lgkmcnt(2)\n\t"
      "v_fmac_f32_e32 v88, v148, v250\n\t"
      "v_fmac_f32_e32 v89, v149, v250\n\t"
      "v_mfma_f32_16x16x32_bf16 v[4:7], v[156:159], v[176:179], v[4:7]\n\t"
      "v_fmac_f32_e32 v90, v150, v250\n\t"
      "v_fmac_f32_e32 v91, v151, v250\n\t"
      "ds_write_b128 v236, v[80:83] offset:2048\n\t"
      "v_fmac_f32_e32 v92, v148, v252\n\t"
      "v_fmac_f32_e32 v93, v149, v252\n\t"
      "v_fmac_f32_e32 v94, v150, v252\n\t"
      "v_fmac_f32_e32 v95, v151, v252\n\t"
      "v_cvt_pk_bf16_f32 v164, v88, v89\n\t"
      "v_cvt_pk_bf16_f32 v165, v90, v91\n\t"
      "v_cvt_pk_bf16_f32 v174, v92, v93\n\t"
      "v_cvt_pk_bf16_f32 v175, v94, v95\n\t"
      "ds_read_b128 v[108:111], v228\n\t"
      "ds_read_b128 v[112:115], v228 offset:64\n\t"
      "s_waitcnt lgkmcnt(3)\n\t"
      "s_nop 0\n\t"
      "v_pk_fma_f32 v[100:101], v[248:249], v[124:125], v[100:101]\n\t"
      "v_pk_fma_f32 v[104:105], v[196:197], v[224:225], v[104:105]\n\t"
      "v_pk_fma_f32 v[102:103], v[248:249], v[126:127], v[102:103]\n\t"
      "v_pk_fma_f32 v[106:107], v[198:199], v[224:225], v[106:107]\n\t"
      "ds_write_b64 v237, v[164:165] offset:3072\n\t"
      "ds_write_b64 v237, v[174:175] offset:3584\n\t"
      "v_cvt_pk_bf16_f32 v100, v100, v101\n\t"
      "v_cvt_pk_bf16_f32 v101, v102, v103\n\t"
      "v_cvt_pk_bf16_f32 v102, v104, v105\n\t"
      "v_cvt_pk_bf16_f32 v103, v106, v107\n\t"
      "v_pk_fma_f32 v[56:57], v[132:133], v[116:117], v[56:57]\n\t"
      "v_pk_fma_f32 v[58:59], v[132:133], v[118:119], v[58:59]\n\t"
      "v_pk_fma_f32 v[60:61], v[132:133], v[120:121], v[60:61]\n\t"
      "v_pk_fma_f32 v[62:63], v[132:133], v[122:123], v[62:63]\n\t"
      "ds_write_b64 v238, v[100:101]\n\t"
      "ds_write_b64 v214, v[102:103]\n\t"
      "ds_write_b32 v213, v213 offset:4\n\t"
      "v_cvt_pk_bf16_f32 v56, v56, v57\n\t"
      "v_cvt_pk_bf16_f32 v57, v58, v59\n\t"
      "v_cvt_pk_bf16_f32 v58, v60, v61\n\t"
      "v_cvt_pk_bf16_f32 v59, v62, v63\n\t"
      "ds_read_b128 v[144:147], v233 offset:256\n\t"
      "ds_read_b128 v[140:143], v233 offset:192\n\t"
      "ds_read_b128 v[136:139], v233 offset:128\n\t"
      "v_pk_fma_f32 v[32:33], v[130:131], v[116:117], v[32:33]\n\t"
      "v_pk_fma_f32 v[34:35], v[130:131], v[118:119], v[34:35]\n\t"
      "v_pk_fma_f32 v[36:37], v[130:131], v[120:121], v[36:37]\n\t"
      "v_pk_fma_f32 v[38:39], v[130:131], v[122:123], v[38:39]\n\t"
      "v_pk_fma_f32 v[8:9], v[128:129], v[116:117], v[8:9]\n\t"
      "v_pk_fma_f32 v[10:11], v[128:129], v[118:119], v[10:11]\n\t"
      "v_pk_fma_f32 v[12:13], v[128:129], v[120:121], v[12:13]\n\t"
      "v_pk_fma_f32 v[14:15], v[128:129], v[122:123], v[14:15]\n\t"
      "s_waitcnt lgkmcnt(8)\n\t"
      "s_nop 0\n\t"
      "v_pk_fma_f32 v[72:73], v[134:135], v[108:109], v[72:73]\n\t"
      "v_pk_fma_f32 v[74:75], v[134:135], v[110:111], v[74:75]\n\t"
      "v_pk_fma_f32 v[76:77], v[134:135], v[112:113], v[76:77]\n\t"
      "v_pk_fma_f32 v[78:79], v[134:135], v[114:115], v[78:79]\n\t"
      "v_pk_fma_f32 v[48:49], v[132:133], v[108:109], v[48:49]\n\t"
      "v_pk_fma_f32 v[50:51], v[132:133], v[110:111], v[50:51]\n\t"
      "v_pk_fma_f32 v[52:53], v[132:133], v[112:113], v[52:53]\n\t"
      "v_pk_fma_f32 v[54:55], v[132:133], v[114:115], v[54:55]\n\t"
      "s_waitcnt lgkmcnt(3)\n\t"
      "ds_read2_b32 v[204:205], v235 offset0:0 offset1:16\n\t"
      "ds_read2_b32 v[206:207], v235 offset0:32 offset1:48\n\t"
      "v_pk_fma_f32 v[24:25], v[130:131], v[108:109], v[24:25]\n\t"
      "v_pk_fma_f32 v[26:27], v[130:131], v[110:111], v[26:27]\n\t"
      "v_pk_fma_f32 v[28:29], v[130:131], v[112:113], v[28:29]\n\t"
      "v_pk_fma_f32 v[30:31], v[130:131], v[114:115], v[30:31]\n\t"
      "ds_read2_b32 v[208:209], v254 offset0:0 offset1:16\n\t"
      "ds_read2_b32 v[210:211], v254 offset0:32 offset1:48\n\t"
      "v_pk_fma_f32 v[0:1], v[128:129], v[108:109], v[0:1]\n\t"
      "v_pk_fma_f32 v[2:3], v[128:129], v[110:111], v[2:3]\n\t"
      "v_pk_fma_f32 v[4:5], v[128:129], v[112:113], v[4:5]\n\t"
      "v_pk_fma_f32 v[6:7], v[128:129], v[114:115], v[6:7]\n\t"
      "ds_read2_b32 v[200:201], v234 offset0:192 offset1:208\n\t"
      "ds_read2_b32 v[202:203], v234 offset0:224 offset1:240\n\t"
      "ds_read_b32 v226, v232 offset:4\n\t"
      "ds_read_b128 v[152:155], v236\n\t"
      "ds_read_b64 v[156:157], v237 offset:1024\n\t"
      "ds_read_b64 v[166:167], v237 offset:1536\n\t"
      "s_waitcnt lgkmcnt(12)\n\t"
      "v_pk_fma_f32 v[64:65], v[144:145], v[250:251], v[64:65]\n\t"
      "v_pk_fma_f32 v[66:67], v[146:147], v[250:251], v[66:67]\n\t"
      "v_pk_fma_f32 v[68:69], v[144:145], v[252:253], v[68:69]\n\t"
      "v_pk_fma_f32 v[70:71], v[146:147], v[252:253], v[70:71]\n\t"
      "s_waitcnt lgkmcnt(11)\n\t"
      "v_pk_fma_f32 v[40:41], v[140:141], v[250:251], v[40:41]\n\t"
      "v_pk_fma_f32 v[42:43], v[142:143], v[250:251], v[42:43]\n\t"
      "v_pk_fma_f32 v[44:45], v[140:141], v[252:253], v[44:45]\n\t"
      "v_pk_fma_f32 v[46:47], v[142:143], v[252:253], v[46:47]\n\t"
      "s_waitcnt lgkmcnt(10)\n\t"
      "v_pk_fma_f32 v[16:17], v[136:137], v[250:251], v[16:17]\n\t"
      "v_pk_fma_f32 v[18:19], v[138:139], v[250:251], v[18:19]\n\t"
      "v_pk_fma_f32 v[20:21], v[136:137], v[252:253], v[20:21]\n\t"
      "v_pk_fma_f32 v[22:23], v[138:139], v[252:253], v[22:23]\n\t"
      "ds_read_b64 v[250:251], v220 offset:8848\n\t"
      "ds_read_b64 v[196:197], v220\n\t"
      "v_cvt_pk_bf16_f32 v72, v72, v73\n\t"
      "v_cvt_pk_bf16_f32 v73, v74, v75\n\t"
      "v_cvt_pk_bf16_f32 v74, v76, v77\n\t"
      "v_cvt_pk_bf16_f32 v75, v78, v79\n\t"
      "v_cndmask_b32_e64 v74, v74, 0, s[42:43]\n\t"
      "v_cndmask_b32_e64 v75, v75, 0, s[42:43]\n\t"
      "v_cvt_pk_bf16_f32 v48, v48, v49\n\t"
      "v_cvt_pk_bf16_f32 v49, v50, v51\n\t"
      "v_cvt_pk_bf16_f32 v50, v52, v53\n\t"
      "v_cvt_pk_bf16_f32 v51, v54, v55\n\t"
      "v_cndmask_b32_e64 v50, v50, 0, s[42:43]\n\t"
      "v_cndmask_b32_e64 v51, v51, 0, s[42:43]\n\t"
      "v_cvt_pk_bf16_f32 v32, v32, v33\n\t"
      "v_cvt_pk_bf16_f32 v33, v34, v35\n\t"
      "v_cvt_pk_bf16_f32 v34, v36, v37\n\t"
      "v_cvt_pk_bf16_f32 v35, v38, v39\n\t"
      "v_cvt_pk_bf16_f32 v8, v8, v9\n\t"
      "v_cvt_pk_bf16_f32 v9, v10, v11\n\t"
      "v_cvt_pk_bf16_f32 v10, v12, v13\n\t"
      "v_cvt_pk_bf16_f32 v11, v14, v15\n\t"
      "v_mfma_f32_16x16x32_bf16 v[76:79], v[56:59], v[72:75], 0\n\t"
      "v_cvt_pk_bf16_f32 v24, v24, v25\n\t"
      "v_cvt_pk_bf16_f32 v25, v26, v27\n\t"
      "v_mfma_f32_16x16x32_bf16 v[84:87], v[80:83], v[72:75], 0\n\t"
      "v_cvt_pk_bf16_f32 v26, v28, v29\n\t"
      "v_cvt_pk_bf16_f32 v27, v30, v31\n\t"
      "v_mfma_f32_16x16x32_bf16 v[52:55], v[32:35], v[48:51], 0\n\t"
      "v_cndmask_b32_e64 v26, v26, 0, s[42:43]\n\t"
      "v_cndmask_b32_e64 v27, v27, 0, s[42:43]\n\t"
      "v_mfma_f32_16x16x32_bf16 v[60:63], v[56:59], v[48:51], 0\n\t"
      "s_waitcnt lgkmcnt(5)\n\t"
      "v_cmp_eq_u32_e32 vcc, 0, v226\n\t"
      "v_cvt_pk_bf16_f32 v0, v0, v1\n\t"
      "v_mfma_f32_16x16x32_bf16 v[28:31], v[8:11], v[24:27], 0\n\t"
      "v_cvt_pk_bf16_f32 v1, v2, v3\n\t"
      "v_cvt_pk_bf16_f32 v2, v4, v5\n\t"
      "v_mfma_f32_16x16x32_bf16 v[36:39], v[32:35], v[24:27], 0\n\t"
      "v_cvt_pk_bf16_f32 v3, v6, v7\n\t"
      "v_cndmask_b32_e64 v2, v2, 0, s[42:43]\n\t"
      "v_cndmask_b32_e64 v3, v3, 0, s[42:43]\n\t"
      "s_cbranch_vccnz XBR_%=\n\t"
      "XBB_%=:\n\t"
      "v_cvt_pk_bf16_f32 v162, v64, v65\n\t"
      "s_waitcnt lgkmcnt(4)\n\t"
      "v_mfma_f32_16x16x32_bf16 v[4:7], v[152:155], v[0:3], v[244:247]\n\t"
      "v_cvt_pk_bf16_f32 v163, v66, v67\n\t"
      "v_cvt_pk_bf16_f32 v172, v68, v69\n\t"
      "v_mfma_f32_16x16x32_bf16 v[12:15], v[8:11], v[0:3], 0\n\t"
      "v_cvt_pk_bf16_f32 v173, v70, v71\n\t"
      "v_cvt_pk_bf16_f32 v160, v40, v41\n\t"
      "v_cndmask_b32_e64 v76, v76, v77, s[36:37]\n\t"
      "v_cndmask_b32_e64 v84, v84, v85, s[36:37]\n\t"
      "v_cndmask_b32_e64 v78, v78, v79, s[36:37]\n\t"
      "v_cndmask_b32_e64 v86, v86, v87, s[36:37]\n\t"
      "v_cndmask_b32_e64 v179, v76, v84, s[38:39]\n\t"
      "v_cndmask_b32_e64 v183, v78, v86, s[40:41]\n\t"
      "v_cndmask_b32_e64 v52, v52, v53, s[36:37]\n\t"
      "v_cndmask_b32_e64 v60, v60, v61, s[36:37]\n\t"
      "v_cndmask_b32_e64 v54, v54, v55, s[36:37]\n\t"
      "v_cndmask_b32_e64 v62, v62, v63, s[36:37]\n\t"
      "v_cndmask_b32_e64 v178, v52, v60, s[38:39]\n\t"
      "v_cndmask_b32_e64 v182, v54, v62, s[40:41]\n\t"
      "v_cndmask_b32_e64 v28, v28, v29, s[36:37]\n\t"
      "v_cndmask_b32_e64 v36, v36, v37, s[36:37]\n\t"
      "v_cndmask_b32_e64 v30, v30, v31, s[36:37]\n\t"
      "v_cndmask_b32_e64 v38, v38, v39, s[36:37]\n\t"
      "v_cndmask_b32_e64 v177, v28, v36, s[38:39]\n\t"
      "v_cndmask_b32_e64 v181, v30, v38, s[40:41]\n\t"
      "v_cndmask_b32_e64 v4, v4, v5, s[36:37]\n\t"
      "v_cndmask_b32_e64 v12, v12, v13, s[36:37]\n\t"
      "v_cndmask_b32_e64 v6, v6, v7, s[36:37]\n\t"
      "v_cndmask_b32_e64 v14, v14, v15, s[36:37]\n\t"
      "v_cndmask_b32_e64 v176, v4, v12, s[38:39]\n\t"
      "v_cndmask_b32_e64 v180, v6, v14, s[40:41]\n\t"
      "v_pk_mul_f32 v[178:179], v[178:179], v[206:207]\n\t"
      "v_pk_mul_f32 v[182:183], v[182:183], v[210:211]\n\t"
      "v_pk_mul_f32 v[176:177], v[176:177], v[204:205]\n\t"
      "v_pk_mul_f32 v[180:181], v[180:181], v[208:209]\n\t"
      "v_max_f32_e32 v219, v179, v183\n\t"
      "v_max_f32_e32 v217, v177, v181\n\t"
      "v_max_f32_e32 v218, v178, v182\n\t"
      "v_max_f32_e32 v216, v176, v180\n\t"
      "v_cvt_pk_bf16_f32 v161, v42, v43\n\t"
      "v_permlane32_swap_b32_e32 v217, v219\n\t"
      "v_permlane32_swap_b32_e32 v216, v218\n\t"
      "v_max_f32_e32 v217, v217, v219\n\t"
      "v_max_f32_e32 v216, v216, v218\n\t"
      "v_cvt_pk_bf16_f32 v170, v44, v45\n\t"
      "v_cvt_pk_bf16_f32 v171, v46, v47\n\t"
      "v_permlane16_swap_b32_e32 v216, v217\n\t"
      "v_max_f32_e32 v216, v216, v217\n\t"
      "v_mov_b32_e32 v217, v216\n\t"
      "v_cvt_pk_bf16_f32 v158, v16, v17\n\t"
      "s_waitcnt lgkmcnt(0)\n\t"
      "v_xad_u32 v252, s13, v197, v221\n\t"
      "v_permlane16_swap_b32_e32 v216, v217\n\t"
      "v_mov_b32_e32 v218, v216\n\t"
      "v_mov_b32_e64 v219, v217\n\t"
      "v_xad_u32 v196, s52, v196, v239\n\t"
      "v_permlane32_swap_b32_e32 v216, v218\n\t"
      "v_permlane32_swap_b32_e32 v217, v219\n\t"
      "v_pk_add_f32 v[178:179], v[178:179], v[218:219] neg_lo:[0,1] neg_hi:[0,1]\n\t"
      "v_pk_add_f32 v[182:183], v[182:183], v[218:219] neg_lo:[0,1] neg_hi:[0,1]\n\t"
      "v_pk_add_f32 v[176:177], v[176:177], v[216:217] neg_lo:[0,1] neg_hi:[0,1]\n\t"
      "v_pk_add_f32 v[180:181], v[180:181], v[216:217] neg_lo:[0,1] neg_hi:[0,1]\n\t"
      "v_pk_mul_f32 v[178:179], v[178:179], v[202:203]\n\t"
      "v_pk_mul_f32 v[182:183], v[182:183], v[202:203]\n\t"
      "v_pk_mul_f32 v[176:177], v[176:177], v[200:201]\n\t"
      "v_pk_mul_f32 v[180:181], v[180:181], v[200:201]\n\t"
      "v_exp_f32_e32 v179, v179\n\t"
      "v_exp_f32_e32 v183, v183\n\t"
      "v_exp_f32_e32 v178, v178\n\t"
      "v_exp_f32_e32 v182, v182\n\t"
      "v_exp_f32_e32 v177, v177\n\t"
      "v_exp_f32_e32 v181, v181\n\t"
      "v_exp_f32_e32 v176, v176\n\t"
      "v_exp_f32_e32 v180, v180\n\t"
      "v_xor_b32_e64 v253, s12, v197\n\t"
      "s_mov_b64 exec, 1\n\t"
      "global_store_dwordx2 v253, v[250:251], s[10:11] sc1\n\t"
      "s_mov_b64 exec, -1\n\t"
      "v_pk_add_f32 v[218:219], v[178:179], v[182:183]\n\t"
      "v_pk_add_f32 v[216:217], v[176:177], v[180:181]\n\t"
      "v_cvt_pk_bf16_f32 v159, v18, v19\n\t"
      "v_cvt_pk_bf16_f32 v168, v20, v21\n\t"
      "v_permlane32_swap_b32_e32 v217, v219\n\t"
      "v_permlane32_swap_b32_e32 v216, v218\n\t"
      "v_add_f32_e32 v217, v217, v219\n\t"
      "v_add_f32_e32 v216, v216, v218\n\t"
      "v_pk_mul_f32 v[178:179], v[178:179], v[206:207]\n\t"
      "v_pk_mul_f32 v[182:183], v[182:183], v[210:211]\n\t"
      "v_permlane16_swap_b32_e32 v216, v217\n\t"
      "v_add_f32_e32 v216, v216, v217\n\t"
      "v_rcp_f32_e32 v216, v216\n\t"
      "v_pk_mul_f32 v[176:177], v[176:177], v[204:205]\n\t"
      "v_mov_b32_e32 v217, v216\n\t"
      "v_pk_mul_f32 v[180:181], v[180:181], v[208:209]\n\t"
      "v_cvt_pk_bf16_f32 v169, v22, v23\n\t"
      "v_permlane16_swap_b32_e32 v216, v217\n\t"
      "v_mov_b32_e32 v218, v216\n\t"
      "v_mov_b32_e32 v219, v217\n\t"
      "s_nop 0\n\t"
      "v_permlane32_swap_b32_e32 v216, v218\n\t"
      "v_permlane32_swap_b32_e32 v217, v219\n\t"
      "v_pk_mul_f32 v[178:179], v[178:179], v[218:219]\n\t"
      "v_pk_mul_f32 v[182:183], v[182:183], v[218:219]\n\t"
      ".LhopA3:\n\t"
      "s_cbranch_execz .LhopA7\n\t"
      "v_pk_mul_f32 v[176:177], v[176:177], v[216:217]\n\t"
      "v_pk_mul_f32 v[180:181], v[180:181], v[216:217]\n\t"
      "v_cvt_pk_bf16_f32 v179, v179, v183\n\t"
      "v_cvt_pk_bf16_f32 v178, v178, v182\n\t"
      "v_cvt_pk_bf16_f32 v177, v177, v181\n\t"
      "v_cvt_pk_bf16_f32 v176, v176, v180\n\t"
      "v_perm_b32 v72, v179, v179, v240\n\t"
      "v_perm_b32 v73, v179, v179, v241\n\t"
      ".LhopA4:\n\t"
      "s_cbranch_execz .LhopA8\n\t"
      "v_perm_b32 v74, v179, v179, v242\n\t"
      "v_perm_b32 v75, v179, v179, v243\n\t"
      "v_perm_b32 v48, v178, v178, v240\n\t"
      "v_perm_b32 v49, v178, v178, v241\n\t"
      "v_mfma_f32_16x16x32_bf16 v[88:91], v[162:165], v[72:75], 0\n\t"
      "v_perm_b32 v50, v178, v178, v242\n\t"
      "v_perm_b32 v51, v178, v178, v243\n\t"
      "v_mfma_f32_16x16x32_bf16 v[92:95], v[172:175], v[72:75], 0\n\t"
      "v_perm_b32 v24, v177, v177, v240\n\t"
      "v_perm_b32 v25, v177, v177, v241\n\t"
      "v_mfma_f32_16x16x32_bf16 v[64:67], v[160:163], v[48:51], 0\n\t"
      "v_perm_b32 v26, v177, v177, v242\n\t"
      "v_perm_b32 v27, v177, v177, v243\n\t"
      "v_mfma_f32_16x16x32_bf16 v[68:71], v[170:173], v[48:51], 0\n\t"
      "v_perm_b32 v0, v176, v176, v240\n\t"
      "v_perm_b32 v1, v176, v176, v241\n\t"
      "v_mfma_f32_16x16x32_bf16 v[40:43], v[158:161], v[24:27], 0\n\t"
      "v_perm_b32 v2, v176, v176, v242\n\t"
      "v_perm_b32 v3, v176, v176, v243\n\t"
      "v_mfma_f32_16x16x32_bf16 v[44:47], v[168:171], v[24:27], 0\n\t"
      "s_nop 0\n\t"
      "v_mfma_f32_16x16x32_bf16 v[16:19], v[156:159], v[0:3], 0\n\t"
      "v_cvt_pk_bf16_f32 v80, v88, v89\n\t"
      "v_cvt_pk_bf16_f32 v81, v90, v91\n\t"
      "v_mfma_f32_16x16x32_bf16 v[20:23], v[166:169], v[0:3], 0\n\t"
      "v_cvt_pk_bf16_f32 v82, v92, v93\n\t"
      "v_cvt_pk_bf16_f32 v83, v94, v95\n\t"
      "global_store_dwordx4 v196, v[80:83], s[60:61] sc1\n\t"
      "v_add3_u32 v219, v80, v81, v82\n\t"
      "v_add_u32_e32 v219, v219, v83\n\t"
      "v_cvt_pk_bf16_f32 v56, v64, v65\n\t"
      "v_cvt_pk_bf16_f32 v57, v66, v67\n\t"
      "v_cvt_pk_bf16_f32 v58, v68, v69\n\t"
      "v_cvt_pk_bf16_f32 v59, v70, v71\n\t"
      "global_store_dwordx4 v196, v[56:59], s[58:59] sc1\n\t"
      "v_add3_u32 v218, v56, v57, v58\n\t"
      "v_add_u32_e64 v218, v218, v59\n\t"
      "v_cvt_pk_bf16_f32 v32, v40, v41\n\t"
      "v_cvt_pk_bf16_f32 v33, v42, v43\n\t"
      "v_cvt_pk_bf16_f32 v34, v44, v45\n\t"
      "v_cvt_pk_bf16_f32 v35, v46, v47\n\t"
      "global_store_dwordx4 v196, v[32:35], s[56:57] sc1\n\t"
      "v_add3_u32 v217, v32, v33, v34\n\t"
      "v_add_u32_e64 v217, v217, v35\n\t"
      "v_cvt_pk_bf16_f32 v8, v16, v17\n\t"
      "v_cvt_pk_bf16_f32 v9, v18, v19\n\t"
      "v_cvt_pk_bf16_f32 v10, v20, v21\n\t"
      "v_cvt_pk_bf16_f32 v11, v22, v23\n\t"
      "global_store_dwordx4 v196, v[8:11], s[4:5] sc1\n\t"
      "v_add3_u32 v216, v8, v9, v10\n\t"
      "v_add_u32_e32 v216, v216, v11\n\t"
      "v_permlane32_swap_b32_e32 v217, v219\n\t"
      "v_add_u32_e32 v217, v217, v219\n\t"
      "v_permlane32_swap_b32_e32 v216, v218\n\t"
      "v_add_u32_e64 v216, v216, v218\n\t"
      "s_nop 1\n\t"
      "v_permlane16_swap_b32_e32 v216, v217\n\t"
      "v_add3_u32 v216, v216, v217, v250\n\t"
      "global_store_dword v252, v216, s[10:11] sc1\n\t"
      "v_xor_b32_e64 v120, 64, v222\n\t"
      "v_add_u32_e32 v121, 55296, v222\n\t"
      "v_add_u32_e32 v122, 82944, v120\n\t"
      "v_add_u32_e32 v120, 27648, v120\n\t"
      "s_barrier\n\t"
      "s_add_u32 m0, s16, 65536\n\t"
      "s_add_u32 s48, s14, 0\n\t"
      "s_addc_u32 s49, s15, 0\n\t"
      "global_load_lds_dwordx4 v222, s[48:49]\n\t"
      "global_load_lds_dwordx4 v120, s[48:49] offset:1024\n\t"
      "global_load_lds_dwordx4 v121, s[48:49] offset:2048\n\t"
      "global_load_lds_dwordx4 v122, s[48:49] offset:3072\n\t"
      "s_add_u32 m0, s16, 98304\n\t"
      "s_add_u32 s48, s14, 114688\n\t"
      "s_addc_u32 s49, s15, 0\n\t"
      "global_load_lds_dwordx4 v222, s[48:49]\n\t"
      "global_load_lds_dwordx4 v120, s[48:49] offset:1024\n\t"
      "global_load_lds_dwordx4 v121, s[48:49] offset:2048\n\t"
      "global_load_lds_dwordx4 v122, s[48:49] offset:3072\n\t"
      "s_nop 1\n\t"
      "s_branch CEND_%=\n\t"
      "LOOP1_%=:\n\t"
      "s_waitcnt vmcnt(0)\n\t"
      "s_barrier\n\t"
      "ds_read_b64_tr_b16 v[108:109], v226\n\t"
      "ds_read_b64_tr_b16 v[110:111], v226 offset:512\n\t"
      "ds_read_b128 v[132:135], v224\n\t"
      "ds_read_b128 v[136:139], v224 offset:5120\n\t"
      "ds_read_b128 v[140:143], v224 offset:10240\n\t"
      "ds_read_b128 v[144:147], v224 offset:15360\n\t"
      "ds_read_b64_tr_b16 v[112:113], v227\n\t"
      "ds_read_b64_tr_b16 v[114:115], v227 offset:512\n\t"
      "ds_read_b128 v[148:151], v225\n\t"
      "ds_read_b64_tr_b16 v[116:117], v226 offset:1024\n\t"
      "ds_read_b64_tr_b16 v[118:119], v226 offset:1536\n\t"
      "ds_read_b64_tr_b16 v[120:121], v227 offset:1024\n\t"
      "ds_read_b64_tr_b16 v[122:123], v227 offset:1536\n\t"
      "ds_read_b64_tr_b16 v[124:125], v226 offset:2048\n\t"
      "ds_read_b64_tr_b16 v[126:127], v226 offset:2560\n\t"
      "ds_read_b64_tr_b16 v[128:129], v227 offset:2048\n\t"
      "ds_read_b64_tr_b16 v[130:131], v227 offset:2560\n\t"
      "s_branch EVEN1_%=\n\t"
      "s_waitcnt lgkmcnt(0)\n\t"
      ".p2align 6\n\t"
      ".LhopA5:\n\t"
      "s_cbranch_execz .LhopA9\n\t"
      "s_nop 0\n\t"
      "ODD1_%=:\n\t"
      "s_waitcnt lgkmcnt(0)\n\t"
      "v_mfma_f32_16x16x32_bf16 v[0:3], v[152:155], v[176:179], v[0:3]\n\t"
      "v_add_u32_e32 v224, s46, v224\n\t"
      "v_add_u32_e32 v226, s46, v226\n\t"
      "v_mfma_f32_16x16x32_bf16 v[24:27], v[152:155], v[180:183], v[24:27]\n\t"
      "v_add_u32_e32 v227, s46, v227\n\t"
      "v_add_u32_e32 v225, s46, v225\n\t"
      "v_mfma_f32_16x16x32_bf16 v[48:51], v[152:155], v[184:187], v[48:51]\n\t"
      "s_barrier\n\t"
      "ds_read_b64_tr_b16 v[108:109], v226\n\t"
      "v_mfma_f32_16x16x32_bf16 v[72:75], v[152:155], v[188:191], v[72:75]\n\t"
      "ds_read_b64_tr_b16 v[110:111], v226 offset:512\n\t"
      "v_mfma_f32_16x16x32_bf16 v[4:7], v[156:159], v[176:179], v[4:7]\n\t"
      "ds_read_b128 v[132:135], v224\n\t"
      "v_mfma_f32_16x16x32_bf16 v[28:31], v[156:159], v[180:183], v[28:31]\n\t"
      "ds_read_b128 v[136:139], v224 offset:5120\n\t"
      "v_mfma_f32_16x16x32_bf16 v[52:55], v[156:159], v[184:187], v[52:55]\n\t"
      "ds_read_b128 v[140:143], v224 offset:10240\n\t"
      "v_mfma_f32_16x16x32_bf16 v[76:79], v[156:159], v[188:191], v[76:79]\n\t"
      "ds_read_b128 v[144:147], v224 offset:15360\n\t"
      "v_mfma_f32_16x16x32_bf16 v[96:99], v[156:159], v[192:195], v[96:99]\n\t"
      "v_mfma_f32_16x16x32_bf16 v[8:11], v[160:163], v[176:179], v[8:11]\n\t"
      "ds_read_b64_tr_b16 v[112:113], v227\n\t"
      "v_mfma_f32_16x16x32_bf16 v[32:35], v[160:163], v[180:183], v[32:35]\n\t"
      "ds_read_b64_tr_b16 v[114:115], v227 offset:512\n\t"
      "v_mfma_f32_16x16x32_bf16 v[56:59], v[160:163], v[184:187], v[56:59]\n\t"
      "ds_read_b128 v[148:151], v225\n\t"
      "v_mfma_f32_16x16x32_bf16 v[80:83], v[160:163], v[188:191], v[80:83]\n\t"
      "ds_read_b64_tr_b16 v[116:117], v226 offset:1024\n\t"
      "v_mfma_f32_16x16x32_bf16 v[12:15], v[164:167], v[176:179], v[12:15]\n\t"
      "ds_read_b64_tr_b16 v[118:119], v226 offset:1536\n\t"
      "v_mfma_f32_16x16x32_bf16 v[36:39], v[164:167], v[180:183], v[36:39]\n\t"
      "ds_read_b64_tr_b16 v[120:121], v227 offset:1024\n\t"
      "v_mfma_f32_16x16x32_bf16 v[60:63], v[164:167], v[184:187], v[60:63]\n\t"
      "v_mfma_f32_16x16x32_bf16 v[84:87], v[164:167], v[188:191], v[84:87]\n\t"
      "ds_read_b64_tr_b16 v[122:123], v227 offset:1536\n\t"
      "v_mfma_f32_16x16x32_bf16 v[100:103], v[164:167], v[192:195], v[100:103]\n\t"
      "ds_read_b64_tr_b16 v[124:125], v226 offset:2048\n\t"
      "v_mfma_f32_16x16x32_bf16 v[16:19], v[176:179], v[168:171], v[16:19]\n\t"
      "ds_read_b64_tr_b16 v[126:127], v226 offset:2560\n\t"
      "v_mfma_f32_16x16x32_bf16 v[40:43], v[180:183], v[168:171], v[40:43]\n\t"
      "ds_read_b64_tr_b16 v[128:129], v227 offset:2048\n\t"
      "v_mfma_f32_16x16x32_bf16 v[64:67], v[184:187], v[168:171], v[64:67]\n\t"
      "ds_read_b64_tr_b16 v[130:131], v227 offset:2560\n\t"
      "v_mfma_f32_16x16x32_bf16 v[88:91], v[188:191], v[168:171], v[88:91]\n\t"
      "v_mfma_f32_16x16x32_bf16 v[20:23], v[176:179], v[172:175], v[20:23]\n\t"
      "v_mfma_f32_16x16x32_bf16 v[44:47], v[180:183], v[172:175], v[44:47]\n\t"
      "v_add_u32_e32 v248, s46, v248\n\t"
      "v_add_u32_e32 v249, s46, v249\n\t"
      "v_mfma_f32_16x16x32_bf16 v[68:71], v[184:187], v[172:175], v[68:71]\n\t"
      "v_mfma_f32_16x16x32_bf16 v[92:95], v[188:191], v[172:175], v[92:95]\n\t"
      "v_mfma_f32_16x16x32_bf16 v[104:107], v[192:195], v[172:175], v[104:107]\n\t"
      ".LhopA6:\n\t"
      "s_cbranch_execz .LbackA\n\t"
      "EVEN1_%=:\n\t"
      "s_waitcnt lgkmcnt(0)\n\t"
      "v_mfma_f32_16x16x32_bf16 v[0:3], v[108:111], v[132:135], v[0:3]\n\t"
      "ds_read_b64_tr_b16 v[152:153], v226 offset:20480\n\t"
      "v_mfma_f32_16x16x32_bf16 v[24:27], v[108:111], v[136:139], v[24:27]\n\t"
      "ds_read_b64_tr_b16 v[154:155], v226 offset:20992\n\t"
      "v_mfma_f32_16x16x32_bf16 v[48:51], v[108:111], v[140:143], v[48:51]\n\t"
      "ds_read_b128 v[176:179], v248\n\t"
      "v_mfma_f32_16x16x32_bf16 v[72:75], v[108:111], v[144:147], v[72:75]\n\t"
      "ds_read_b128 v[180:183], v248 offset:5120\n\t"
      "v_mfma_f32_16x16x32_bf16 v[4:7], v[112:115], v[132:135], v[4:7]\n\t"
      "v_mfma_f32_16x16x32_bf16 v[28:31], v[112:115], v[136:139], v[28:31]\n\t"
      "ds_read_b128 v[184:187], v248 offset:10240\n\t"
      "v_mfma_f32_16x16x32_bf16 v[52:55], v[112:115], v[140:143], v[52:55]\n\t"
      "ds_read_b128 v[188:191], v248 offset:15360\n\t"
      "v_mfma_f32_16x16x32_bf16 v[76:79], v[112:115], v[144:147], v[76:79]\n\t"
      "ds_read_b64_tr_b16 v[156:157], v227 offset:20480\n\t"
      "v_mfma_f32_16x16x32_bf16 v[96:99], v[112:115], v[148:151], v[96:99]\n\t"
      "v_mfma_f32_16x16x32_bf16 v[8:11], v[116:119], v[132:135], v[8:11]\n\t"
      "ds_read_b64_tr_b16 v[158:159], v227 offset:20992\n\t"
      "v_mfma_f32_16x16x32_bf16 v[32:35], v[116:119], v[136:139], v[32:35]\n\t"
      "ds_read_b128 v[192:195], v249\n\t"
      "v_mfma_f32_16x16x32_bf16 v[56:59], v[116:119], v[140:143], v[56:59]\n\t"
      "ds_read_b64_tr_b16 v[160:161], v226 offset:21504\n\t"
      "v_mfma_f32_16x16x32_bf16 v[80:83], v[116:119], v[144:147], v[80:83]\n\t"
      "ds_read_b64_tr_b16 v[162:163], v226 offset:22016\n\t"
      "v_mfma_f32_16x16x32_bf16 v[12:15], v[120:123], v[132:135], v[12:15]\n\t"
      "v_mfma_f32_16x16x32_bf16 v[36:39], v[120:123], v[136:139], v[36:39]\n\t"
      "ds_read_b64_tr_b16 v[164:165], v227 offset:21504\n\t"
      "v_mfma_f32_16x16x32_bf16 v[60:63], v[120:123], v[140:143], v[60:63]\n\t"
      "ds_read_b64_tr_b16 v[166:167], v227 offset:22016\n\t"
      "v_mfma_f32_16x16x32_bf16 v[84:87], v[120:123], v[144:147], v[84:87]\n\t"
      "ds_read_b64_tr_b16 v[168:169], v226 offset:22528\n\t"
      "v_mfma_f32_16x16x32_bf16 v[100:103], v[120:123], v[148:151], v[100:103]\n\t"
      "v_mfma_f32_16x16x32_bf16 v[16:19], v[132:135], v[124:127], v[16:19]\n\t"
      "ds_read_b64_tr_b16 v[170:171], v226 offset:23040\n\t"
      "v_mfma_f32_16x16x32_bf16 v[40:43], v[136:139], v[124:127], v[40:43]\n\t"
      "ds_read_b64_tr_b16 v[172:173], v227 offset:22528\n\t"
      "v_mfma_f32_16x16x32_bf16 v[64:67], v[140:143], v[124:127], v[64:67]\n\t"
      "ds_read_b64_tr_b16 v[174:175], v227 offset:23040\n\t"
      "s_cmp_eq_u32 s47, 4\n\t"
      "s_cselect_b32 s46, -2, 1\n\t"
      "v_mfma_f32_16x16x32_bf16 v[88:91], v[144:147], v[124:127], v[88:91]\n\t"
      "s_cmp_eq_u32 s47, 1\n\t"
      "s_cselect_b32 s46, -2, s46\n\t"
      "v_mfma_f32_16x16x32_bf16 v[20:23], v[132:135], v[128:131], v[20:23]\n\t"
      "s_mul_i32 s46, s46, 43008\n\t"
      "v_mfma_f32_16x16x32_bf16 v[44:47], v[136:139], v[128:131], v[44:47]\n\t"
      "v_mfma_f32_16x16x32_bf16 v[68:71], v[140:143], v[128:131], v[68:71]\n\t"
      "v_mfma_f32_16x16x32_bf16 v[92:95], v[144:147], v[128:131], v[92:95]\n\t"
      "s_sub_u32 s47, s47, 1\n\t"
      "v_mfma_f32_16x16x32_bf16 v[104:107], v[148:151], v[128:131], v[104:107]\n\t"
      "s_cbranch_scc0 ODD1_%=\n\t"
      "s_waitcnt lgkmcnt(0)\n\t"
      "v_mfma_f32_16x16x32_bf16 v[96:99], v[156:159], v[192:195], v[96:99]\n\t"
      "ds_read_b32 v212, v215\n\t"
      "v_mfma_f32_16x16x32_bf16 v[100:103], v[164:167], v[192:195], v[100:103]\n\t"
      "ds_read2_b32 v[134:135], v234 offset0:80 offset1:80\n\t"
      "v_mfma_f32_16x16x32_bf16 v[104:107], v[192:195], v[172:175], v[104:107]\n\t"
      "ds_read_b128 v[148:151], v233 offset:320\n\t"
      "v_mfma_f32_16x16x32_bf16 v[80:83], v[160:163], v[188:191], v[80:83]\n\t"
      "ds_read2_b32 v[248:249], v234 offset0:0 offset1:0\n\t"
      "v_mfma_f32_16x16x32_bf16 v[84:87], v[164:167], v[188:191], v[84:87]\n\t"
      "ds_read_b128 v[196:199], v233\n\t"
      "v_mfma_f32_16x16x32_bf16 v[88:91], v[188:191], v[168:171], v[88:91]\n\t"
      "ds_read2_b32 v[132:133], v234 offset0:64 offset1:64\n\t"
      "v_mfma_f32_16x16x32_bf16 v[92:95], v[188:191], v[172:175], v[92:95]\n\t"
      "s_mov_b64 exec, s[44:45]\n\t"
      "ds_write_b128 v230, v[96:99]\n\t"
      "ds_write_b128 v230, v[100:103] offset:128\n\t"
      "s_mov_b64 exec, 0xffff\n\t"
      "ds_write_b32 v231, v104 offset:256\n\t"
      "ds_write_b32 v213, v213\n\t"
      "s_mov_b64 exec, -1\n\t"
      "v_mfma_f32_16x16x32_bf16 v[56:59], v[160:163], v[184:187], v[56:59]\n\t"
      "ds_read2_b32 v[130:131], v234 offset0:48 offset1:48\n\t"
      "v_mfma_f32_16x16x32_bf16 v[60:63], v[164:167], v[184:187], v[60:63]\n\t"
      "ds_read2_b32 v[128:129], v234 offset0:32 offset1:32\n\t"
      "v_mfma_f32_16x16x32_bf16 v[64:67], v[184:187], v[168:171], v[64:67]\n\t"
      "s_waitcnt lgkmcnt(0)\n\t"
      "v_mfma_f32_16x16x32_bf16 v[68:71], v[184:187], v[172:175], v[68:71]\n\t"
      "ds_read_b32 v226, v232\n\t"
      "ds_read_b128 v[116:119], v228 offset:128\n\t"
      "v_mfma_f32_16x16x32_bf16 v[32:35], v[160:163], v[180:183], v[32:35]\n\t"
      "ds_read_b128 v[120:123], v228 offset:192\n\t"
      "v_mfma_f32_16x16x32_bf16 v[36:39], v[164:167], v[180:183], v[36:39]\n\t"
      "ds_read2_b32 v[250:251], v229 offset0:64 offset1:64\n\t"
      "v_mfma_f32_16x16x32_bf16 v[40:43], v[180:183], v[168:171], v[40:43]\n\t"
      "ds_read2_b32 v[252:253], v229 offset0:80 offset1:80\n\t"
      "v_mfma_f32_16x16x32_bf16 v[44:47], v[180:183], v[172:175], v[44:47]\n\t"
      "ds_read_b128 v[124:127], v230 offset:128\n\t"
      "v_mfma_f32_16x16x32_bf16 v[8:11], v[160:163], v[176:179], v[8:11]\n\t"
      "ds_read2_b32 v[224:225], v231 offset0:64 offset1:64\n\t"
      "v_mfma_f32_16x16x32_bf16 v[12:15], v[164:167], v[176:179], v[12:15]\n\t"
      "v_mfma_f32_16x16x32_bf16 v[16:19], v[176:179], v[168:171], v[16:19]\n\t"
      "s_waitcnt lgkmcnt(6)\n\t"
      "v_min_u32_e32 v226, v226, v212\n\t"
      "v_cmp_eq_u32_e32 vcc, 0, v226\n\t"
      "v_mfma_f32_16x16x32_bf16 v[20:23], v[176:179], v[172:175], v[20:23]\n\t"
      "s_branch LOOPX_%=\n\t"
      "XAR_%=:\n\t"
      "s_mov_b32 s51, 0\n\t"
      "XAL_%=:\n\t"
      "s_add_u32 s51, s51, 1\n\t"
      "s_cmp_lt_u32 s51, 262144\n\t"
      "s_cbranch_scc0 XAX_%=\n\t"
      "s_waitcnt lgkmcnt(0)\n\t"
      "ds_read_b32 v212, v215\n\t"
      "ds_read2_b32 v[134:135], v234 offset0:80 offset1:80\n\t"
      "ds_read_b128 v[148:151], v233 offset:320\n\t"
      "ds_read2_b32 v[248:249], v234 offset0:0 offset1:0\n\t"
      "ds_read_b128 v[196:199], v233\n\t"
      "ds_read2_b32 v[132:133], v234 offset0:64 offset1:64\n\t"
      "ds_read2_b32 v[130:131], v234 offset0:48 offset1:48\n\t"
      "ds_read2_b32 v[128:129], v234 offset0:32 offset1:32\n\t"
      "s_waitcnt lgkmcnt(0)\n\t"
      "ds_read_b32 v226, v232\n\t"
      "ds_read_b128 v[116:119], v228 offset:128\n\t"
      "ds_read_b128 v[120:123], v228 offset:192\n\t"
      "ds_read2_b32 v[250:251], v229 offset0:64 offset1:64\n\t"
      "ds_read2_b32 v[252:253], v229 offset0:80 offset1:80\n\t"
      "ds_read_b128 v[124:127], v230 offset:128\n\t"
      "ds_read2_b32 v[224:225], v231 offset0:64 offset1:64\n\t"
      "s_waitcnt lgkmcnt(0)\n\t"
      "v_min_u32_e32 v226, v226, v212\n\t"
      "v_cmp_eq_u32_e32 vcc, 0, v226\n\t"
      "s_cbranch_vccnz XAL_%=\n\t"
      "XAX_%=:\n\t"
      "s_branch XAB_%=\n\t"
      "XBR_%=:\n\t"
      "s_mov_b32 s51, 0\n\t"
      "XBL_%=:\n\t"
      "s_add_u32 s51, s51, 1\n\t"
      "s_cmp_lt_u32 s51, 262144\n\t"
      "s_cbranch_scc0 XBX_%=\n\t"
      "s_waitcnt lgkmcnt(0)\n\t"
      "ds_read_b32 v226, v232 offset:4\n\t"
      "ds_read_b128 v[152:155], v236\n\t"
      "ds_read_b64 v[156:157], v237 offset:1024\n\t"
      "ds_read_b64 v[166:167], v237 offset:1536\n\t"
      "s_waitcnt lgkmcnt(0)\n\t"
      "v_cmp_eq_u32_e32 vcc, 0, v226\n\t"
      "s_cbranch_vccnz XBL_%=\n\t"
      "XBX_%=:\n\t"
      "s_branch XBB_%=\n\t"
      "CEND_%=:\n\t"
      ".LhopA7:\n\t"
      "s_cbranch_execz .LbackA\n\t"
      : "+{s[4:11]}"(s), "+{s12}"(hint), "+{s13}"(chk), "+{s[14:15]}"(tw), "+{s16}"(lw), "+{v222}"(vn), "+{v[224:239]}"(v), "+{v254}"(vk1)
      :
      : "memory", "vcc", "scc",
        "v0", "v1", "v2", "v3", "v4", "v5", "v6", "v7", "v8", "v9", "v10", "v11", "v12", "v13", "v14", "v15", "v16",
        "v17", "v18", "v19", "v20", "v21", "v22", "v23", "v24", "v25", "v26", "v27", "v28", "v29", "v30", "v31", "v32",
        "v33", "v34", "v35", "v36", "v37", "v38", "v39", "v40", "v41", "v42", "v43", "v44", "v45", "v46", "v47", "v48",
        "v49", "v50", "v51", "v52", "v53", "v54", "v55", "v56", "v57", "v58", "v59", "v60", "v61", "v62", "v63", "v64",
        "v65", "v66", "v67", "v68", "v69", "v70", "v71", "v72", "v73", "v74", "v75", "v76", "v77", "v78", "v79", "v80",
        "v81", "v82", "v83", "v84", "v85", "v86", "v87", "v88", "v89", "v90", "v91", "v92", "v93", "v94", "v95", "v96",
        "v97", "v98", "v99", "v100", "v101", "v102", "v103", "v104", "v105", "v106", "v107", "v108", "v109", "v110",
        "v111", "v112", "v113", "v114", "v115", "v116", "v117", "v118", "v119", "v120", "v121", "v122", "v123", "v124",
        "v125", "v126", "v127", "v128", "v129", "v130", "v131", "v132", "v133", "v134", "v135", "v136", "v137", "v138",
        "v139", "v140", "v141", "v142", "v143", "v144", "v145", "v146", "v147", "v148", "v149", "v150", "v151", "v152",
        "v153", "v154", "v155", "v156", "v157", "v158", "v159", "v160", "v161", "v162", "v163", "v164", "v165", "v166",
        "v167", "v168", "v169", "v170", "v171", "v172", "v173", "v174", "v175", "v176", "v177", "v178", "v179", "v180",
        "v181", "v182", "v183", "v184", "v185", "v186", "v187", "v188", "v189", "v190", "v191", "v192", "v193", "v194",
        "v195", "v196", "v197", "v198", "v199", "v200", "v201", "v202", "v203", "v204", "v205", "v206", "v207", "v208",
        "v209", "v210", "v211", "v212", "v213", "v214", "v215", "v216", "v217", "v218", "v219", "v220", "v221", "v223",
        "v240", "v241", "v242", "v243", "v244", "v245", "v246", "v247", "v248", "v249", "v250", "v251", "v252", "v253",
        "s36", "s37", "s38", "s39", "s40", "s41", "s42", "s43", "s44", "s45", "s46", "s47", "s48", "s49", "s50", "s51",
        "s52", "s53", "s54", "s55", "s56", "s57", "s58", "s59", "s60", "s61"
    );
    } else {
      LANE_BITS(threadIdx.x & 63);
      u32x8 v;
      u32x16 s;
      v[0] = ((8 * rt + 4 * half + g) * 16 + c) * 16;
      const int hi = -(lane >> 5), m3 = min(c, 2);
      v[1] = (((max(8 * rt + ((g & 1) ? 0 : 4 * half) - 1, 0) * 16 + c) * 16) & ~hi) | ((ST_PW + xcd * 128 + m3 * 16) & hi);
      v[2] = sta + 128 + lane * 4;
      v[3] = ((sta + ((g & 1) ? 0 : 64) + c * 4) & ~hi) | ((A_MK + m3 * 4) & hi);
      v[4] = lr * A_SLOT + A_HALO + lc * 16;
      v[6] = (side * 1024 + (idx * 4 + wave - 4) * 8 + lr) * 3584 + (7 * xp + min(lc, 6)) * 128;
      v[7] = 0;
      u64 tw = (u64)w1;
      unsigned vn = (((wave ^ (4 * side)) * 256 + (idx & 1) * 128 + lr) * 28 + 7 * xp + min(idx >> 1, 6)) * 128 + ((lc ^ swz_tr(lr)) << 4);
      s[4] = UNI(idx == 15 && xcd < 3 && wave == 4 ? (xcd == 0 ? WS_O : xcd == 1 ? WS_HN + 16 : WS_G + 32) + PROBE : 0);
      v[5] = lane * (8192 + 640);
      put(s, 0, (u64)(x + (rt * 128 * 896 + 128) - (16 * 896 + 1024)));
      put(s, 2, (u64)(wqkv + (hp * 112 + 64 * 2688) - 4096));
      s[5] = wave * A_UNIT;
      put2(s, 6, wave > 4 ? 0xffffffffu : 0xffffff00u, 0u);
      s[8] = A_HALO + (wave - 4) * 512;
      s[9] = A_FL + 32 + 4 * half;
      put(s, 10, (u64)(ws + WS_ST));
    asm volatile(
      "s_waitcnt lgkmcnt(0)\n\ts_nop 4\n\t"
      ".p2align 3\n\t"
      "s_load_dwordx2 s[18:19], s[14:15], 0x20000\n\t"
      "s_sub_u32 s40, s4, 57344\n\t"
      "s_subb_u32 s41, s5, 0\n\t"
      "s_sub_u32 s42, s6, 86016\n\t"
      "s_subb_u32 s43, s7, 0\n\t"
      "v_mov_b32_e32 v0, 0x3f803f80\n\t"
      "v_mov_b32_e32 v1, 0x3f803f80\n\t"
      "v_mov_b32_e32 v2, 0x3f803f80\n\t"
      "v_mov_b32_e32 v3, 0x3f803f80\n\t"
      "s_mov_b64 exec, 0xffffff\n\t"
      "ds_write_b128 v204, v[0:3]\n\t"
      "s_mov_b64 exec, -1\n\t"
      "v_mov_b32_e32 v32, 154624\n\t"
      "v_mov_b32_e32 v33, s13\n\t"
      "v_mov_b32_e32 v36, 0\n\t"
      "v_mov_b32_e32 v37, 0\n\t"
      "v_mov_b32_e32 v38, 0\n\t"
      "v_mov_b32_e32 v39, 0\n\t"
      "ds_write_b128 v32, v[36:39]\n\t"
      "ds_write_b128 v32, v[36:39] offset:16\n\t"
      "ds_write_b128 v32, v[36:39] offset:32\n\t"
      "v_xor_b32_e64 v21, 64, v20\n\t"
      "v_add_u32_e32 v22, 55296, v20\n\t"
      "v_add_u32_e32 v23, 82944, v21\n\t"
      "v_add_u32_e32 v21, 27648, v21\n\t"
      "s_sub_u32 s4, s4, 128\n\t"
      "s_subb_u32 s5, s5, 0\n\t"
      "s_sub_u32 s6, s6, 172032\n\t"
      "s_subb_u32 s7, s7, 0\n\t"
      "s_add_u32 m0, s9, 0\n\t"
      "s_nop 0\n\t"
      "global_load_lds_dwordx4 v195, s[6:7] offset:2048\n\t"
      "global_load_lds_dwordx4 v192, s[4:5]\n\t"
      "global_load_lds_dwordx4 v193, s[4:5] offset:1024\n\t"
      "global_load_lds_dwordx4 v196, s[6:7] offset:3072\n\t"
      "s_add_u32 m0, s9, 4096\n\t"
      "s_nop 0\n\t"
      "global_load_lds_dwordx4 v197, s[6:7]\n\t"
      "s_add_u32 m0, s12, 0\n\t"
      "s_mov_b64 exec, s[10:11]\n\t"
      "global_load_lds_dwordx4 v194, s[4:5]\n\t"
      "s_mov_b64 exec, -1\n\t"
      "s_add_u32 s4, s4, 128\n\t"
      "s_addc_u32 s5, s5, 0\n\t"
      "s_add_u32 s6, s6, 172032\n\t"
      "s_addc_u32 s7, s7, 0\n\t"
      "s_waitcnt lgkmcnt(0)\n\t"
      "s_add_u32 s18, s18, 1\n\t"
      "s_addc_u32 s19, s19, 0\n\t"
      "s_mul_i32 s48, s18, 0x9e3779b9\n\t"
      "s_mul_hi_u32 s17, s18, 0x7f4a7c15\n\t"
      "s_add_u32 s17, s17, s48\n\t"
      "s_mul_i32 s48, s19, 0x7f4a7c15\n\t"
      "s_add_u32 s17, s17, s48\n\t"
      "s_mul_i32 s16, s18, 0x7f4a7c15\n\t"
      "s_add_u32 s16, s16, 0xd192ed03\n\t"
      "s_addc_u32 s17, s17, 0xd1b54a32\n\t"
      "v_mov_b32_e32 v12, s16\n\t"
      "v_mov_b32_e32 v13, s17\n\t"
      "v_mov_b32_e32 v14, s18\n\t"
      "v_mov_b32_e32 v15, s19\n\t"
      "v_mov_b32_e32 v16, 163600\n\t"
      "s_mov_b64 exec, 1\n\t"
      "ds_write_b128 v16, v[12:15]\n\t"
      "s_mov_b64 exec, -1\n\t"
      "s_and_b32 s47, s8, 48\n\t"
      "s_add_u32 s47, s47, 114688\n\t"
      "v_mbcnt_lo_u32_b32 v28, -1, 0\n\t"
      "v_mov_b32_e32 v25, 0\n\t"
      "v_mov_b32_e32 v26, s16\n\t"
      "v_mov_b32_e32 v27, s17\n\t"
      "v_lshl_add_u32 v28, v28, 7, s47\n\t"
      "s_cmp_eq_u32 s8, 0\n\t"
      "s_cbranch_scc1 PRt_%=\n\t"
      "s_add_u32 s52, s14, s8\n\t"
      "s_addc_u32 s53, s15, 0\n\t"
      "s_mov_b32 s54, 0\n\t"
      "s_mov_b64 s[56:57], -1\n\t"
      "s_load_dword s50, s[52:53], 0x1f80\n\t"
      "PRt_%=:\n\t"
      "s_cmp_eq_u32 s8, 0\n\t"
      "s_cbranch_scc1 PRu_%=\n\t"
      "s_mov_b64 exec, 3\n\t"
      "global_store_dword v205, v0, s[52:53] sc1\n\t"
      "s_mov_b64 exec, -1\n\t"
      "PRu_%=:\n\t"
      "s_add_u32 m0, s9, 22528\n\t"
      "s_nop 0\n\t"
      "global_load_lds_dwordx4 v195, s[42:43] offset:2048\n\t"
      "global_load_lds_dwordx4 v192, s[40:41]\n\t"
      "global_load_lds_dwordx4 v193, s[40:41] offset:1024\n\t"
      "global_load_lds_dwordx4 v196, s[42:43] offset:3072\n\t"
      "s_add_u32 m0, s9, 26624\n\t"
      "s_nop 0\n\t"
      "global_load_lds_dwordx4 v197, s[42:43]\n\t"
      "s_add_u32 m0, s9, 43008\n\t"
      "s_nop 0\n\t"
      "global_load_lds_dwordx4 v195, s[6:7] offset:2048\n\t"
      "global_load_lds_dwordx4 v192, s[4:5]\n\t"
      "global_load_lds_dwordx4 v193, s[4:5] offset:1024\n\t"
      "global_load_lds_dwordx4 v196, s[6:7] offset:3072\n\t"
      "s_add_u32 m0, s9, 47104\n\t"
      "s_nop 0\n\t"
      "global_load_lds_dwordx4 v197, s[6:7]\n\t"
      "s_add_u32 m0, s12, 43008\n\t"
      "s_mov_b64 exec, s[10:11]\n\t"
      "global_load_lds_dwordx4 v194, s[4:5]\n\t"
      "s_mov_b64 exec, -1\n\t"
      "s_waitcnt vmcnt(11) lgkmcnt(0)\n\t"
      "s_barrier\n\t"
      "s_cmp_eq_u32 s8, 0\n\t"
      "s_cbranch_scc1 PRC0_%=\n\t"
      "s_mov_b32 s55, 8832\n\t"
      "s_call_b64 s[44:45], PRB_%=\n\t"
      "PRC0_%=:\n\t"
      "s_add_u32 s4, s4, 128\n\t"
      "s_addc_u32 s5, s5, 0\n\t"
      "s_add_u32 s6, s6, 172032\n\t"
      "s_addc_u32 s7, s7, 0\n\t"
      "s_add_u32 s40, s40, 128\n\t"
      "s_addc_u32 s41, s41, 0\n\t"
      "s_add_u32 s42, s42, 172032\n\t"
      "s_addc_u32 s43, s43, 0\n\t"
      "s_add_u32 m0, s9, 65536\n\t"
      "s_nop 0\n\t"
      "global_load_lds_dwordx4 v195, s[42:43] offset:2048\n\t"
      "global_load_lds_dwordx4 v192, s[40:41]\n\t"
      "global_load_lds_dwordx4 v193, s[40:41] offset:1024\n\t"
      "global_load_lds_dwordx4 v196, s[42:43] offset:3072\n\t"
      "s_add_u32 m0, s9, 69632\n\t"
      "s_nop 0\n\t"
      "global_load_lds_dwordx4 v197, s[42:43]\n\t"
      "s_add_u32 m0, s9, 86016\n\t"
      "s_nop 0\n\t"
      "global_load_lds_dwordx4 v195, s[6:7] offset:2048\n\t"
      "global_load_lds_dwordx4 v192, s[4:5]\n\t"
      "global_load_lds_dwordx4 v193, s[4:5] offset:1024\n\t"
      "global_load_lds_dwordx4 v196, s[6:7] offset:3072\n\t"
      "s_add_u32 m0, s9, 90112\n\t"
      "s_nop 0\n\t"
      "global_load_lds_dwordx4 v197, s[6:7]\n\t"
      "s_add_u32 m0, s12, 86016\n\t"
      "s_mov_b64 exec, s[10:11]\n\t"
      "global_load_lds_dwordx4 v194, s[4:5]\n\t"
      "s_mov_b64 exec, -1\n\t"
      "s_waitcnt vmcnt(11)\n\t"
      "s_barrier\n\t"
      "s_cmp_eq_u32 s8, 0\n\t"
      "s_cbranch_scc1 PRC1_%=\n\t"
      "s_mov_b32 s55, 17664\n\t"
      "s_call_b64 s[44:45], PRB_%=\n\t"
      "PRC1_%=:\n\t"
      "s_add_u32 s4, s4, 128\n\t"
      "s_addc_u32 s5, s5, 0\n\t"
      "s_add_u32 s6, s6, 172032\n\t"
      "s_addc_u32 s7, s7, 0\n\t"
      "s_add_u32 s40, s40, 128\n\t"
      "s_addc_u32 s41, s41, 0\n\t"
      "s_add_u32 s42, s42, 172032\n\t"
      "s_addc_u32 s43, s43, 0\n\t"
      "s_add_u32 m0, s9, 4294946816\n\t"
      "s_nop 0\n\t"
      "global_load_lds_dwordx4 v195, s[42:43] offset:2048\n\t"
      "global_load_lds_dwordx4 v192, s[40:41]\n\t"
      "global_load_lds_dwordx4 v193, s[40:41] offset:1024\n\t"
      "global_load_lds_dwordx4 v196, s[42:43] offset:3072\n\t"
      "s_add_u32 m0, s9, 4294950912\n\t"
      "s_nop 0\n\t"
      "global_load_lds_dwordx4 v197, s[42:43]\n\t"
      "s_add_u32 m0, s9, 0\n\t"
      "s_nop 0\n\t"
      "global_load_lds_dwordx4 v195, s[6:7] offset:2048\n\t"
      "global_load_lds_dwordx4 v192, s[4:5]\n\t"
      "global_load_lds_dwordx4 v193, s[4:5] offset:1024\n\t"
      "global_load_lds_dwordx4 v196, s[6:7] offset:3072\n\t"
      "s_add_u32 m0, s9, 4096\n\t"
      "s_nop 0\n\t"
      "global_load_lds_dwordx4 v197, s[6:7]\n\t"
      "s_add_u32 m0, s12, 0\n\t"
      "s_mov_b64 exec, s[10:11]\n\t"
      "global_load_lds_dwordx4 v194, s[4:5]\n\t"
      "s_mov_b64 exec, -1\n\t"
      "s_cmp_eq_u32 s8, 0\n\t"
      "s_cbranch_scc1 PRS_%=\n\t"
      "s_cmp_lt_u32 s57, s56\n\t"
      "s_cselect_b32 s46, 0x2000, 0\n\t"
      "v_mov_b32_e32 v24, s46\n\t"
      "v_xor_b32_e32 v26, s46, v26\n\t"
      "s_mov_b64 exec, 0xff\n\t"
      "global_store_dwordx4 v28, v[24:27], s[14:15] sc1\n\t"
      "s_mov_b64 exec, -1\n\t"
      "PRS_%=:\n\t"
      "s_waitcnt vmcnt(11)\n\t"
      "s_barrier\n\t"
      "s_add_u32 s4, s4, 128\n\t"
      "s_addc_u32 s5, s5, 0\n\t"
      "s_add_u32 s6, s6, 172032\n\t"
      "s_addc_u32 s7, s7, 0\n\t"
      "s_add_u32 s40, s40, 128\n\t"
      "s_addc_u32 s41, s41, 0\n\t"
      "s_add_u32 s42, s42, 172032\n\t"
      "s_addc_u32 s43, s43, 0\n\t"
      "s_add_u32 m0, s9, 22528\n\t"
      "s_nop 0\n\t"
      "global_load_lds_dwordx4 v195, s[42:43] offset:2048\n\t"
      "global_load_lds_dwordx4 v192, s[40:41]\n\t"
      "global_load_lds_dwordx4 v193, s[40:41] offset:1024\n\t"
      "global_load_lds_dwordx4 v196, s[42:43] offset:3072\n\t"
      "s_add_u32 m0, s9, 26624\n\t"
      "s_nop 0\n\t"
      "global_load_lds_dwordx4 v197, s[42:43]\n\t"
      "s_add_u32 m0, s9, 43008\n\t"
      "s_nop 0\n\t"
      "global_load_lds_dwordx4 v195, s[6:7] offset:2048\n\t"
      "global_load_lds_dwordx4 v192, s[4:5]\n\t"
      "global_load_lds_dwordx4 v193, s[4:5] offset:1024\n\t"
      "global_load_lds_dwordx4 v196, s[6:7] offset:3072\n\t"
      "s_add_u32 m0, s9, 47104\n\t"
      "s_nop 0\n\t"
      "global_load_lds_dwordx4 v197, s[6:7]\n\t"
      "s_add_u32 m0, s12, 43008\n\t"
      "s_mov_b64 exec, s[10:11]\n\t"
      "global_load_lds_dwordx4 v194, s[4:5]\n\t"
      "s_mov_b64 exec, -1\n\t"
      "s_waitcnt vmcnt(11)\n\t"
      "s_barrier\n\t"
      "s_add_u32 s4, s4, 128\n\t"
      "s_addc_u32 s5, s5, 0\n\t"
      "s_add_u32 s6, s6, 172032\n\t"
      "s_addc_u32 s7, s7, 0\n\t"
      "s_add_u32 s40, s40, 128\n\t"
      "s_addc_u32 s41, s41, 0\n\t"
      "s_add_u32 s42, s42, 172032\n\t"
      "s_addc_u32 s43, s43, 0\n\t"
      "s_add_u32 m0, s9, 65536\n\t"
      "s_nop 0\n\t"
      "global_load_lds_dwordx4 v195, s[42:43] offset:2048\n\t"
      "global_load_lds_dwordx4 v192, s[40:41]\n\t"
      "global_load_lds_dwordx4 v193, s[40:41] offset:1024\n\t"
      "global_load_lds_dwordx4 v196, s[42:43] offset:3072\n\t"
      "s_add_u32 m0, s9, 69632\n\t"
      "s_nop 0\n\t"
      "global_load_lds_dwordx4 v197, s[42:43]\n\t"
      "s_add_u32 m0, s9, 86016\n\t"
      "s_nop 0\n\t"
      "global_load_lds_dwordx4 v195, s[6:7] offset:2048\n\t"
      "global_load_lds_dwordx4 v192, s[4:5]\n\t"
      "global_load_lds_dwordx4 v193, s[4:5] offset:1024\n\t"
      "global_load_lds_dwordx4 v196, s[6:7] offset:3072\n\t"
      "s_add_u32 m0, s9, 90112\n\t"
      "s_nop 0\n\t"
      "global_load_lds_dwordx4 v197, s[6:7]\n\t"
      "s_add_u32 m0, s12, 86016\n\t"
      "s_mov_b64 exec, s[10:11]\n\t"
      "global_load_lds_dwordx4 v194, s[4:5]\n\t"
      "s_mov_b64 exec, -1\n\t"
      "global_load_dwordx4 v[4:7], v200, s[14:15] sc1\n\t"
      "global_load_dwordx4 v[8:11], v201, s[14:15] sc1\n\t"
      "s_waitcnt vmcnt(13)\n\t"
      "s_barrier\n\t"
      "s_add_u32 s4, s4, 128\n\t"
      "s_addc_u32 s5, s5, 0\n\t"
      "s_add_u32 s6, s6, 172032\n\t"
      "s_addc_u32 s7, s7, 0\n\t"
      "s_add_u32 s40, s40, 128\n\t"
      "s_addc_u32 s41, s41, 0\n\t"
      "s_add_u32 s42, s42, 172032\n\t"
      "s_addc_u32 s43, s43, 0\n\t"
      "s_add_u32 m0, s9, 4294946816\n\t"
      "s_nop 0\n\t"
      "global_load_lds_dwordx4 v195, s[42:43] offset:2048\n\t"
      "global_load_lds_dwordx4 v192, s[40:41]\n\t"
      "global_load_lds_dwordx4 v193, s[40:41] offset:1024\n\t"
      "global_load_lds_dwordx4 v196, s[42:43] offset:3072\n\t"
      "s_add_u32 m0, s9, 4294950912\n\t"
      "s_nop 0\n\t"
      "global_load_lds_dwordx4 v197, s[42:43]\n\t"
      "s_add_u32 m0, s9, 0\n\t"
      "s_nop 0\n\t"
      "global_load_lds_dwordx4 v195, s[6:7] offset:2048\n\t"
      "global_load_lds_dwordx4 v192, s[4:5]\n\t"
      "global_load_lds_dwordx4 v193, s[4:5] offset:1024\n\t"
      "global_load_lds_dwordx4 v196, s[6:7] offset:3072\n\t"
      "s_add_u32 m0, s9, 4096\n\t"
      "s_nop 0\n\t"
      "global_load_lds_dwordx4 v197, s[6:7]\n\t"
      "s_add_u32 m0, s12, 0\n\t"
      "s_mov_b64 exec, s[10:11]\n\t"
      "global_load_lds_dwordx4 v194, s[4:5]\n\t"
      "s_mov_b64 exec, -1\n\t"
      "s_waitcnt vmcnt(13)\n\t"
      "s_barrier\n\t"
      "s_mov_b32 m0, 163328\n\t"
      "s_add_u32 s44, s20, 0\n\t"
      "s_addc_u32 s45, s21, 0\n\t"
      "global_load_lds_dword v206, s[44:45]\n\t"
      "s_mov_b32 m0, 163328\n\t"
      "s_add_u32 s44, s20, 1835008\n\t"
      "s_addc_u32 s45, s21, 0\n\t"
      "global_load_lds_dword v206, s[44:45]\n\t"
      "s_waitcnt vmcnt(2)\n\t"
      "s_barrier\n\t"
      "v_xor_b32_e32 v6, v6, v4\n\t"
      "v_xor_b32_e32 v7, v7, v5\n\t"
      "v_xor_b32_e32 v10, v10, v8\n\t"
      "v_xor_b32_e32 v11, v11, v9\n\t"
      "v_cmp_ne_u64_e64 s[48:49], s[16:17], v[6:7]\n\t"
      "v_cmp_ne_u64_e32 vcc, s[16:17], v[10:11]\n\t"
      "s_nop 0\n\t"
      "s_or_b64 vcc, vcc, s[48:49]\n\t"
      "s_cbranch_scc1 GRR_%=\n\t"
      "GRB_%=:\n\t"
      "v_mul_f32_e32 v6, 0x3e8b97f6, v5\n\t"
      "ds_write2_b32 v202, v4, v5 offset0:0 offset1:96\n\t"
      "ds_write2_b32 v203, v8, v9 offset0:0 offset1:96\n\t"
      "ds_write_b32 v202, v6 offset:640\n\t"
      "s_mov_b64 exec, 1\n\t"
      "ds_write_b32 v33, v33\n\t"
      "s_mov_b64 exec, -1\n\t"
      "s_sub_u32 s46, s12, 38912\n\t"
      "s_lshl_b32 s46, s46, 3\n\t"
      "s_add_u32 m0, s46, 65536\n\t"
      "s_add_u32 s44, s20, 0\n\t"
      "s_addc_u32 s45, s21, 0\n\t"
      "global_load_lds_dwordx4 v20, s[44:45]\n\t"
      "global_load_lds_dwordx4 v21, s[44:45] offset:1024\n\t"
      "global_load_lds_dwordx4 v22, s[44:45] offset:2048\n\t"
      "global_load_lds_dwordx4 v23, s[44:45] offset:3072\n\t"
      "s_add_u32 m0, s46, 98304\n\t"
      "s_add_u32 s44, s20, 114688\n\t"
      "s_addc_u32 s45, s21, 0\n\t"
      "global_load_lds_dwordx4 v20, s[44:45]\n\t"
      "global_load_lds_dwordx4 v21, s[44:45] offset:1024\n\t"
      "global_load_lds_dwordx4 v22, s[44:45] offset:2048\n\t"
      "global_load_lds_dwordx4 v23, s[44:45] offset:3072\n\t"
      "s_waitcnt lgkmcnt(0)\n\t"
      "s_branch HEND_%=\n\t"
      ".p2align 6\n\t"
      "PRB_%=:\n\t"
      "s_waitcnt lgkmcnt(0)\n\t"
      "s_memtime s[58:59]\n\t"
      "s_waitcnt lgkmcnt(0)\n\t"
      "s_load_dword s50, s[52:53], s54\n\t"
      "s_waitcnt lgkmcnt(0)\n\t"
      "s_memtime s[46:47]\n\t"
      "s_add_u32 s54, s54, 8832\n\t"
      "s_waitcnt lgkmcnt(0)\n\t"
      "s_sub_u32 s46, s46, s58\n\t"
      "s_min_u32 s46, s56, s46\n\t"
      "s_mov_b32 s56, s57\n\t"
      "s_mov_b32 s57, s46\n\t"
      "s_cmp_lt_u32 s54, s55\n\t"
      "s_cbranch_scc1 PRB_%=\n\t"
      "s_setpc_b64 s[44:45]\n\t"
      "GRR_%=:\n\t"
      "s_mov_b32 s51, 0\n\t"
      "GRL_%=:\n\t"
      "s_add_u32 s51, s51, 1\n\t"
      "s_cmp_lt_u32 s51, 262144\n\t"
      "s_cbranch_scc0 GRX_%=\n\t"
      "s_sleep 2\n\t"
      "global_load_dwordx4 v[4:7], v200, s[14:15] sc1\n\t"
      "global_load_dwordx4 v[8:11], v201, s[14:15] sc1\n\t"
      "s_waitcnt vmcnt(0)\n\t"
      "v_xor_b32_e32 v6, v6, v4\n\t"
      "v_xor_b32_e32 v7, v7, v5\n\t"
      "v_xor_b32_e32 v10, v10, v8\n\t"
      "v_xor_b32_e32 v11, v11, v9\n\t"
      "v_cmp_ne_u64_e64 s[48:49], s[16:17], v[6:7]\n\t"
      "v_cmp_ne_u64_e32 vcc, s[16:17], v[10:11]\n\t"
      "s_nop 0\n\t"
      "s_or_b64 vcc, vcc, s[48:49]\n\t"
      "s_cbranch_scc1 GRL_%=\n\t"
      "GRX_%=:\n\t"
      "s_mov_b32 exec_lo, 0\n\t"
      "v_and_b32_e32 v8, 0x2000, v8\n\t"
      "s_mov_b32 exec_lo, -1\n\t"
      "s_branch GRB_%=\n\t"
      "HEND_%=:\n\t"
      ".LhopA0:\n\t"
      "s_cbranch_execz .LhopA4\n\t"
      : "+{s[4:19]}"(s), "+{s[20:21]}"(tw), "+{v[192:199]}"(f), "+{v[200:207]}"(v), "+{v20}"(vn)
      :
      : "memory", "vcc", "scc",
        "v0", "v1", "v2", "v3", "v4", "v5", "v6", "v7", "v8", "v9", "v10", "v11", "v12", "v13", "v14", "v15", "v16",
        "v17", "v18", "v19", "v21", "v22", "v23", "v24", "v25", "v26", "v27", "v28", "v29", "v30", "v31", "v32", "v33",
        "v34", "v35", "v36", "v37", "v38", "v39", "s40", "s41", "s42", "s43", "s44", "s45", "s46", "s47", "s48", "s49",
        "s50", "s51", "s52", "s53", "s54", "s55", "s56", "s57", "s58", "s59"
    );
    }
    PIN();
    LANE_BITS(__builtin_amdgcn_mbcnt_hi(~0u, __builtin_amdgcn_mbcnt_lo(~0u, 0u)));
    const bool has3 = idx < 14;
    const int ft = has3 ? 7 * xp + (idx >> 1) : 0, blk0 = 7 * (2 * side + (idx & 1));
    u32x8 v;
    u32x16 s;
    v[0] = lane * 16;
    const int rot = 4 * (idx & 1), kg = 2 * ft + ((wave >> 1) & 1), ks = wave ^ (4 * side), rn = rot + 2;
    u64 kr = (u64)(7 - rn) | ((u64)rot << 32);
    v[1] = has3 ? lr * 3584 + ((lc ^ swz_tr(lr)) << 4) : (min(lane, 55) + 96 * (idx & 1)) * 4096 + 384 + xp * 1024;
    v[2] = wave * 4096 + g * 1024 + q4 * 128 + (((2 * (wave & 3) + (p4 >> 1)) ^ swz_tr(8 * g + q4)) << 4) + (p4 & 1) * 8;
    v[3] = wave * 4096 + (wave & 3) * 1024 + ((lane * 16) ^ ((g & 1) << 7));
    const int gg = 2 * (lane >> 5) + (lane & 1);
    v[4] = (wave & 3) * 1024 + (((gg * 16 + ((lane >> 1) & 15)) * 16) ^ ((gg & 1) << 7));
    v[5] = (wave & 1) * 512 + lane * 8;
    v[6] = g * 8;
    v[7] = ST_H3 + part(xp * 16384 + (ks * 16 + c) * 128, side);
    put(s, 0, has3 ? (u64)(w1 + ft * 128 + (ks * 256 + rn * 32) * 3584) : (u64)(w2 + (wave * 224 + (idx & 1) * 16) * 4096));
    put(s, 2, (u64)(ws + WS_HN + unit(blk0, ks, 0)));
    put(s, 4, (u64)(ws + WS_G + unit(blk0, kg / 7, 0) + kg % 7 * 1024));
    put(s, 6, (u64)(b1 + (64 * ft + 16 * (wave & 3)) * 2));
    put(s, 8, (u64)(ws + WS_ST));
    unsigned hint = ST_H4 + side * 8192 + ((idx & 1) * 28 + ft) * 128;
    s[12] = UNI((has3 ? 4 : 0) | (blockIdx.x == 0 && wave == 0 ? 32 : 0) |  (ks == 4 * side + ((xp + XO) & 3) ? 16 : 0));
    s[13] = wave * G_WV;
    asm volatile(
      "s_waitcnt lgkmcnt(0)\n\ts_nop 4\n\t"
      ".p2align 3\n\t"
      "s_bitcmp0_b32 s16, 2\n\t"
      "s_cbranch_scc1 ASP_%=\n\t"
      "s_lshr_b32 s23, s17, 2\n\t"
      "v_mov_b32_e32 v216, 163600\n\t"
      "ds_read_b128 v[216:219], v216\n\t"
      "v_mov_b32_e32 v220, 154756\n\t"
      "ds_read2_b32 v[220:221], v220 offset0:0 offset1:1\n\t"
      "v_xor_b32_e32 v248, 64, v241\n\t"
      "v_add_u32_e32 v249, 55296, v241\n\t"
      "v_add_u32_e32 v250, 82944, v248\n\t"
      "v_add_u32_e32 v248, 27648, v248\n\t"
      "v_xor_b32_e32 v251, 32, v242\n\t"
      "v_xor_b32_e32 v252, 64, v242\n\t"
      "v_xor_b32_e32 v253, 96, v242\n\t"
      "global_load_dwordx2 v[238:239], v246, s[10:11]\n\t"
      "s_waitcnt lgkmcnt(0)\n\t"
      "v_readfirstlane_b32 s14, v216\n\t"
      "v_readfirstlane_b32 s15, v217\n\t"
      "v_readfirstlane_b32 s46, v218\n\t"
      "v_readfirstlane_b32 s47, v219\n\t"
      "v_readfirstlane_b32 s48, v220\n\t"
      "v_readfirstlane_b32 s49, v221\n\t"
      "v_xor_b32_e32 v247, v247, v221\n\t"
      "s_nop 0\n\t"
      "s_xor_b32 s6, s6, s48\n\t"
      "s_xor_b32 s8, s8, s49\n\t"
      "s_xor_b32 s22, s22, s49\n\t"
      "s_add_u32 s52, s6, 0\n\t"
      "s_addc_u32 s53, s7, 0\n\t"
      "s_add_u32 s54, s6, 131072\n\t"
      "s_addc_u32 s55, s7, 0\n\t"
      "s_add_u32 s56, s6, 262144\n\t"
      "s_addc_u32 s57, s7, 0\n\t"
      "s_add_u32 s58, s6, 393216\n\t"
      "s_addc_u32 s59, s7, 0\n\t"
      "s_add_u32 s60, s6, 524288\n\t"
      "s_addc_u32 s61, s7, 0\n\t"
      "s_add_u32 s62, s6, 655360\n\t"
      "s_addc_u32 s63, s7, 0\n\t"
      "s_add_u32 s64, s6, 786432\n\t"
      "s_addc_u32 s65, s7, 0\n\t"
      "s_add_u32 s48, s21, 0\n\t"
      "s_cmp_ge_u32 s48, 8\n\t"
      "s_cselect_b32 s49, 8, 0\n\t"
      "s_sub_u32 s48, s48, s49\n\t"
      "v_lshl_add_u32 v176, s48, 10, v240\n\t"
      "s_add_u32 s48, s21, 1\n\t"
      "s_cmp_ge_u32 s48, 8\n\t"
      "s_cselect_b32 s49, 8, 0\n\t"
      "s_sub_u32 s48, s48, s49\n\t"
      "v_lshl_add_u32 v177, s48, 10, v240\n\t"
      "s_add_u32 s48, s21, 2\n\t"
      "s_cmp_ge_u32 s48, 8\n\t"
      "s_cselect_b32 s49, 8, 0\n\t"
      "s_sub_u32 s48, s48, s49\n\t"
      "v_lshl_add_u32 v178, s48, 10, v240\n\t"
      "s_add_u32 s48, s21, 3\n\t"
      "s_cmp_ge_u32 s48, 8\n\t"
      "s_cselect_b32 s49, 8, 0\n\t"
      "s_sub_u32 s48, s48, s49\n\t"
      "v_lshl_add_u32 v179, s48, 10, v240\n\t"
      "s_add_u32 s48, s21, 4\n\t"
      "s_cmp_ge_u32 s48, 8\n\t"
      "s_cselect_b32 s49, 8, 0\n\t"
      "s_sub_u32 s48, s48, s49\n\t"
      "v_lshl_add_u32 v180, s48, 10, v240\n\t"
      "s_add_u32 s48, s21, 5\n\t"
      "s_cmp_ge_u32 s48, 8\n\t"
      "s_cselect_b32 s49, 8, 0\n\t"
      "s_sub_u32 s48, s48, s49\n\t"
      "v_lshl_add_u32 v181, s48, 10, v240\n\t"
      "s_add_u32 s48, s21, 6\n\t"
      "s_cmp_ge_u32 s48, 8\n\t"
      "s_cselect_b32 s49, 8, 0\n\t"
      "s_sub_u32 s48, s48, s49\n\t"
      "v_lshl_add_u32 v182, s48, 10, v240\n\t"
      "s_add_u32 s48, s21, 7\n\t"
      "s_cmp_ge_u32 s48, 8\n\t"
      "s_cselect_b32 s49, 8, 0\n\t"
      "s_sub_u32 s48, s48, s49\n\t"
      "v_lshl_add_u32 v183, s48, 10, v240\n\t"
      "s_cmp_ge_u32 s17, 65536\n\t"
      "s_cbranch_scc0 BOH_%=\n\t"
      "s_barrier\n\t"
      "BOH_%=:\n\t"
      "s_waitcnt vmcnt(5)\n\t"
      "s_add_u32 m0, s23, 0\n\t"
      "s_nop 0\n\t"
      "global_load_lds_dwordx4 v241, s[4:5]\n\t"
      "global_load_lds_dwordx4 v248, s[4:5] offset:1024\n\t"
      "global_load_lds_dwordx4 v249, s[4:5] offset:2048\n\t"
      "global_load_lds_dwordx4 v250, s[4:5] offset:3072\n\t"
      "s_sub_u32 s20, s20, 1\n\t"
      "s_cselect_b32 s48, -7, 1\n\t"
      "s_cselect_b32 s49, -1, 0\n\t"
      "s_mul_i32 s48, s48, 114688\n\t"
      "s_add_u32 s4, s4, s48\n\t"
      "s_addc_u32 s5, s5, s49\n\t"
      "v_add_u32_e32 v242, 65536, v242\n\t"
      "v_add_u32_e32 v251, 65536, v251\n\t"
      "v_add_u32_e32 v252, 65536, v252\n\t"
      "v_add_u32_e32 v253, 65536, v253\n\t"
      "ds_read_b64_tr_b16 v[0:1], v242\n\t"
      "ds_read_b64_tr_b16 v[2:3], v242 offset:512\n\t"
      "ds_read_b64_tr_b16 v[4:5], v251\n\t"
      "ds_read_b64_tr_b16 v[6:7], v251 offset:512\n\t"
      "ds_read_b64_tr_b16 v[8:9], v252\n\t"
      "ds_read_b64_tr_b16 v[10:11], v252 offset:512\n\t"
      "ds_read_b64_tr_b16 v[12:13], v253\n\t"
      "ds_read_b64_tr_b16 v[14:15], v253 offset:512\n\t"
      "s_waitcnt vmcnt(5)\n\t"
      "s_add_u32 m0, s23, 32768\n\t"
      "s_nop 0\n\t"
      "global_load_lds_dwordx4 v241, s[4:5]\n\t"
      "global_load_lds_dwordx4 v248, s[4:5] offset:1024\n\t"
      "global_load_lds_dwordx4 v249, s[4:5] offset:2048\n\t"
      "global_load_lds_dwordx4 v250, s[4:5] offset:3072\n\t"
      "s_sub_u32 s20, s20, 1\n\t"
      "s_cselect_b32 s48, -7, 1\n\t"
      "s_cselect_b32 s49, -1, 0\n\t"
      "s_mul_i32 s48, s48, 114688\n\t"
      "s_add_u32 s4, s4, s48\n\t"
      "s_addc_u32 s5, s5, s49\n\t"
      "ds_read_b64_tr_b16 v[16:17], v242 offset:32768\n\t"
      "ds_read_b64_tr_b16 v[18:19], v242 offset:33280\n\t"
      "ds_read_b64_tr_b16 v[20:21], v251 offset:32768\n\t"
      "ds_read_b64_tr_b16 v[22:23], v251 offset:33280\n\t"
      "ds_read_b64_tr_b16 v[24:25], v252 offset:32768\n\t"
      "ds_read_b64_tr_b16 v[26:27], v252 offset:33280\n\t"
      "ds_read_b64_tr_b16 v[28:29], v253 offset:32768\n\t"
      "ds_read_b64_tr_b16 v[30:31], v253 offset:33280\n\t"
      "s_waitcnt vmcnt(8)\n\t"
      "v_xor_b32_e32 v164, 1024, v243\n\t"
      "v_xor_b32_e32 v165, 2048, v243\n\t"
      "v_xor_b32_e32 v166, 3072, v243\n\t"
      "v_lshlrev_b32_e32 v236, 16, v238\n\t"
      "v_and_b32_e32 v237, 0xffff0000, v238\n\t"
      "v_lshlrev_b32_e32 v238, 16, v239\n\t"
      "v_and_b32_e32 v239, 0xffff0000, v239\n\t"
      "v_mov_b32_e32 v235, 0xbdd2d3e8\n\t"
      "s_waitcnt vmcnt(4)\n\t"
      "s_waitcnt lgkmcnt(8)\n\t"
      "s_add_u32 m0, s23, 65536\n\t"
      "s_nop 0\n\t"
      "global_load_lds_dwordx4 v241, s[4:5]\n\t"
      "global_load_lds_dwordx4 v248, s[4:5] offset:1024\n\t"
      "global_load_lds_dwordx4 v249, s[4:5] offset:2048\n\t"
      "global_load_lds_dwordx4 v250, s[4:5] offset:3072\n\t"
      "s_sub_u32 s20, s20, 1\n\t"
      "s_cselect_b32 s48, -7, 1\n\t"
      "s_cselect_b32 s49, -1, 0\n\t"
      "s_mul_i32 s48, s48, 114688\n\t"
      "s_add_u32 s4, s4, s48\n\t"
      "s_addc_u32 s5, s5, s49\n\t"
      "v_add_u32_e32 v242, 4294901760, v242\n\t"
      "v_add_u32_e32 v251, 4294901760, v251\n\t"
      "v_add_u32_e32 v252, 4294901760, v252\n\t"
      "v_add_u32_e32 v253, 4294901760, v253\n\t"
      "ds_read_b64_tr_b16 v[32:33], v242\n\t"
      "ds_read_b64_tr_b16 v[34:35], v242 offset:512\n\t"
      "ds_read_b64_tr_b16 v[36:37], v251\n\t"
      "ds_read_b64_tr_b16 v[38:39], v251 offset:512\n\t"
      "ds_read_b64_tr_b16 v[40:41], v252\n\t"
      "ds_read_b64_tr_b16 v[42:43], v252 offset:512\n\t"
      "ds_read_b64_tr_b16 v[44:45], v253\n\t"
      "ds_read_b64_tr_b16 v[46:47], v253 offset:512\n\t"
      "s_waitcnt vmcnt(4)\n\t"
      "s_waitcnt lgkmcnt(8)\n\t"
      "s_add_u32 m0, s23, 98304\n\t"
      "s_nop 0\n\t"
      "global_load_lds_dwordx4 v241, s[4:5]\n\t"
      "global_load_lds_dwordx4 v248, s[4:5] offset:1024\n\t"
      "global_load_lds_dwordx4 v249, s[4:5] offset:2048\n\t"
      "global_load_lds_dwordx4 v250, s[4:5] offset:3072\n\t"
      "s_sub_u32 s20, s20, 1\n\t"
      "s_cselect_b32 s48, -7, 1\n\t"
      "s_cselect_b32 s49, -1, 0\n\t"
      "s_mul_i32 s48, s48, 114688\n\t"
      "s_add_u32 s4, s4, s48\n\t"
      "s_addc_u32 s5, s5, s49\n\t"
      "ds_read_b64_tr_b16 v[48:49], v242 offset:32768\n\t"
      "ds_read_b64_tr_b16 v[50:51], v242 offset:33280\n\t"
      ".LhopA8:\n\t"
      "s_cbranch_execz .LbackA\n\t"
      "ds_read_b64_tr_b16 v[52:53], v251 offset:32768\n\t"
      "ds_read_b64_tr_b16 v[54:55], v251 offset:33280\n\t"
      "ds_read_b64_tr_b16 v[56:57], v252 offset:32768\n\t"
      "ds_read_b64_tr_b16 v[58:59], v252 offset:33280\n\t"
      "ds_read_b64_tr_b16 v[60:61], v253 offset:32768\n\t"
      "ds_read_b64_tr_b16 v[62:63], v253 offset:33280\n\t"
      "s_waitcnt vmcnt(4)\n\t"
      "s_waitcnt lgkmcnt(8)\n\t"
      "s_add_u32 m0, s23, 0\n\t"
      ".LhopA9:\n\t"
      "s_cbranch_execz .LbackA\n\t"
      "s_nop 0\n\t"
      "global_load_lds_dwordx4 v241, s[4:5]\n\t"
      "global_load_lds_dwordx4 v248, s[4:5] offset:1024\n\t"
      "global_load_lds_dwordx4 v249, s[4:5] offset:2048\n\t"
      "global_load_lds_dwordx4 v250, s[4:5] offset:3072\n\t"
      "s_sub_u32 s20, s20, 1\n\t"
      "s_cselect_b32 s48, -7, 1\n\t"
      "s_cselect_b32 s49, -1, 0\n\t"
      "s_mul_i32 s48, s48, 114688\n\t"
      "s_add_u32 s4, s4, s48\n\t"
      "s_addc_u32 s5, s5, s49\n\t"
      "s_nop 0\n\t"
      "v_add_u32_e32 v242, 65536, v242\n\t"
      "v_add_u32_e32 v251, 65536, v251\n\t"
      "v_add_u32_e32 v252, 65536, v252\n\t"
      "v_add_u32_e32 v253, 65536, v253\n\t"
      "ds_read_b64_tr_b16 v[64:65], v242\n\t"
      "ds_read_b64_tr_b16 v[66:67], v242 offset:512\n\t"
      "ds_read_b64_tr_b16 v[68:69], v251\n\t"
      "ds_read_b64_tr_b16 v[70:71], v251 offset:512\n\t"
      "ds_read_b64_tr_b16 v[72:73], v252\n\t"
      "ds_read_b64_tr_b16 v[74:75], v252 offset:512\n\t"
      "ds_read_b64_tr_b16 v[76:77], v253\n\t"
      "ds_read_b64_tr_b16 v[78:79], v253 offset:512\n\t"
      "s_waitcnt vmcnt(4)\n\t"
      "s_waitcnt lgkmcnt(8)\n\t"
      "s_add_u32 m0, s23, 32768\n\t"
      "s_nop 0\n\t"
      "global_load_lds_dwordx4 v241, s[4:5]\n\t"
      "global_load_lds_dwordx4 v248, s[4:5] offset:1024\n\t"
      "global_load_lds_dwordx4 v249, s[4:5] offset:2048\n\t"
      "global_load_lds_dwordx4 v250, s[4:5] offset:3072\n\t"
      "ds_read_b64_tr_b16 v[80:81], v242 offset:32768\n\t"
      "ds_read_b64_tr_b16 v[82:83], v242 offset:33280\n\t"
      "ds_read_b64_tr_b16 v[84:85], v251 offset:32768\n\t"
      "ds_read_b64_tr_b16 v[86:87], v251 offset:33280\n\t"
      "ds_read_b64_tr_b16 v[88:89], v252 offset:32768\n\t"
      "ds_read_b64_tr_b16 v[90:91], v252 offset:33280\n\t"
      "ds_read_b64_tr_b16 v[92:93], v253 offset:32768\n\t"
      "ds_read_b64_tr_b16 v[94:95], v253 offset:33280\n\t"
      "s_waitcnt vmcnt(4)\n\t"
      "v_add_u32_e32 v242, 4294901760, v242\n\t"
      "v_add_u32_e32 v251, 4294901760, v251\n\t"
      "v_add_u32_e32 v252, 4294901760, v252\n\t"
      "v_add_u32_e32 v253, 4294901760, v253\n\t"
      "ds_read_b64_tr_b16 v[96:97], v242\n\t"
      "ds_read_b64_tr_b16 v[98:99], v242 offset:512\n\t"
      "ds_read_b64_tr_b16 v[100:101], v251\n\t"
      "ds_read_b64_tr_b16 v[102:103], v251 offset:512\n\t"
      "ds_read_b64_tr_b16 v[104:105], v252\n\t"
      "ds_read_b64_tr_b16 v[106:107], v252 offset:512\n\t"
      "ds_read_b64_tr_b16 v[108:109], v253\n\t"
      "ds_read_b64_tr_b16 v[110:111], v253 offset:512\n\t"
      "s_waitcnt vmcnt(0)\n\t"
      "ds_read_b64_tr_b16 v[112:113], v242 offset:32768\n\t"
      "ds_read_b64_tr_b16 v[114:115], v242 offset:33280\n\t"
      "ds_read_b64_tr_b16 v[116:117], v251 offset:32768\n\t"
      "ds_read_b64_tr_b16 v[118:119], v251 offset:33280\n\t"
      "ds_read_b64_tr_b16 v[120:121], v252 offset:32768\n\t"
      "ds_read_b64_tr_b16 v[122:123], v252 offset:33280\n\t"
      "ds_read_b64_tr_b16 v[124:125], v253 offset:32768\n\t"
      "ds_read_b64_tr_b16 v[126:127], v253 offset:33280\n\t"
      "s_mov_b64 exec, 0\n\t"
      "s_cmpk_eq_u32 s23, 0x0\n\t"
      "s_cbranch_scc1 .LhopD0\n\t"
      "s_cmpk_eq_u32 s23, 0x1000\n\t"
      "s_cbranch_scc1 .LhopD1\n\t"
      "s_cmpk_eq_u32 s23, 0x2000\n\t"
      "s_cbranch_scc1 .LhopD2\n\t"
      "s_cmpk_eq_u32 s23, 0x3000\n\t"
      "s_cbranch_scc1 .LhopD3\n\t"
      "s_cmpk_eq_u32 s23, 0x4000\n\t"
      "s_cbranch_scc1 .LhopD4\n\t"
      "s_cmpk_eq_u32 s23, 0x5000\n\t"
      "s_cbranch_scc1 .LbackD\n\t"
      "s_cmpk_eq_u32 s23, 0x6000\n\t"
      "s_cbranch_scc1 .LbackD\n\t"
      "s_cmpk_eq_u32 s23, 0x7000\n\t"
      "s_cbranch_scc1 .LbackD\n\t"
      ".LbackD:\n\t"
      "s_mov_b64 exec, -1\n\t"
      "s_waitcnt lgkmcnt(0)\n\t"
      "s_barrier\n\t"
      "s_mov_b32 s51, 0\n\t"
      "s_branch PRE_%=\n\t"
      "PRL_%=:\n\t"
      "s_sleep 4\n\t"
      "s_add_u32 s51, s51, 1\n\t"
      "s_cmp_lt_u32 s51, 262144\n\t"
      "s_cbranch_scc0 PRX_%=\n\t"
      "PRE_%=:\n\t"
      "s_mov_b64 exec, 65535\n\t"
      "s_bitcmp1_b32 s16, 4\n\t"
      "s_cbranch_scc1 PM_%=\n\t"
      "global_load_dwordx2 v[232:233], v247, s[12:13] sc1\n\t"
      "s_waitcnt vmcnt(0)\n\t"
      "PM_%=:\n\t"
      "s_cbranch_scc0 PN_%=\n\t"
      "v_mov_b64_e32 v[232:233], 0\n\t"
      "global_atomic_or_x2 v[232:233], v247, v[232:233], s[12:13] sc0 sc1\n\t"
      "s_waitcnt vmcnt(0)\n\t"
      "PN_%=:\n\t"
      "v_subrev_u32_e32 v232, s14, v232\n\t"
      "v_cmp_ne_u32_e64 s[48:49], s15, v233\n\t"
      "v_subrev_u32_e32 v232, 0, v232\n\t"
      "v_cmp_lt_u32_e32 vcc, 7, v232\n\t"
      "s_mov_b64 exec, -1\n\t"
      "s_or_b64 vcc, vcc, s[48:49]\n\t"
      "s_cbranch_vccnz PRL_%=\n\t"
      "PRX_%=:\n\t"
      "PRY_%=:\n\t"
      "global_load_dwordx4 v[128:131], v176, s[52:53] sc1\n\t"
      "global_load_dwordx4 v[132:135], v177, s[52:53] sc1\n\t"
      "global_load_dwordx4 v[136:139], v178, s[52:53] sc1\n\t"
      "global_load_dwordx4 v[140:143], v179, s[52:53] sc1\n\t"
      "global_load_dwordx4 v[144:147], v180, s[52:53] sc1\n\t"
      "global_load_dwordx4 v[148:151], v181, s[52:53] sc1\n\t"
      "global_load_dwordx4 v[152:155], v182, s[52:53] sc1\n\t"
      "global_load_dwordx4 v[156:159], v183, s[52:53] sc1\n\t"
      "s_nop 7\n\t"
      "s_cmp_ge_u32 s17, 65536\n\t"
      "s_cbranch_scc1 GQ_%=\n\t"
      ".p2align 6\n\t"
      "s_setprio 1\n\t"
      "v_mov_b32_e32 v232, s14\n\t"
      "v_mov_b32_e32 v233, s15\n\t"
      "v_lshl_add_u32 v247, v240, 10, s22\n\t"
      "s_waitcnt vmcnt(7)\n\t"
      "v_max3_u32 v234, v128, v129, v130\n\t"
      "v_max_u32_e32 v234, v234, v131\n\t"
      "v_cmp_eq_u32_e32 vcc, -1, v234\n\t"
      "s_cbranch_vccnz FP0R_%=\n\t"
      "FP0B_%=:\n\t"
      "v_mfma_f32_16x16x32_bf16 v[184:187], v[0:3], v[128:131], 0\n\t"
      "s_waitcnt vmcnt(6)\n\t"
      "v_max3_u32 v234, v132, v133, v134\n\t"
      "v_max_u32_e32 v234, v234, v135\n\t"
      "v_cmp_eq_u32_e32 vcc, -1, v234\n\t"
      "s_cbranch_vccnz FP1R_%=\n\t"
      "FP1B_%=:\n\t"
      "v_mfma_f32_16x16x32_bf16 v[184:187], v[16:19], v[132:135], v[184:187]\n\t"
      "v_mfma_f32_16x16x32_bf16 v[188:191], v[4:7], v[128:131], 0\n\t"
      "v_mfma_f32_16x16x32_bf16 v[188:191], v[20:23], v[132:135], v[188:191]\n\t"
      "v_mfma_f32_16x16x32_bf16 v[192:195], v[8:11], v[128:131], 0\n\t"
      "v_mfma_f32_16x16x32_bf16 v[192:195], v[24:27], v[132:135], v[192:195]\n\t"
      "v_mfma_f32_16x16x32_bf16 v[196:199], v[12:15], v[128:131], 0\n\t"
      "global_load_dwordx4 v[128:131], v176, s[54:55] sc1\n\t"
      "v_mfma_f32_16x16x32_bf16 v[196:199], v[28:31], v[132:135], v[196:199]\n\t"
      "global_load_dwordx4 v[132:135], v177, s[54:55] sc1\n\t"
      "s_waitcnt vmcnt(7)\n\t"
      "v_max3_u32 v234, v136, v137, v138\n\t"
      "v_max_u32_e32 v234, v234, v139\n\t"
      "v_cmp_eq_u32_e32 vcc, -1, v234\n\t"
      "s_cbranch_vccnz FP2R_%=\n\t"
      "FP2B_%=:\n\t"
      "v_mfma_f32_16x16x32_bf16 v[184:187], v[32:35], v[136:139], v[184:187]\n\t"
      "s_waitcnt vmcnt(6)\n\t"
      "v_max3_u32 v234, v140, v141, v142\n\t"
      "v_max_u32_e32 v234, v234, v143\n\t"
      "v_cmp_eq_u32_e32 vcc, -1, v234\n\t"
      "s_cbranch_vccnz FP3R_%=\n\t"
      "FP3B_%=:\n\t"
      "v_mfma_f32_16x16x32_bf16 v[184:187], v[48:51], v[140:143], v[184:187]\n\t"
      "v_mfma_f32_16x16x32_bf16 v[188:191], v[36:39], v[136:139], v[188:191]\n\t"
      "v_mfma_f32_16x16x32_bf16 v[188:191], v[52:55], v[140:143], v[188:191]\n\t"
      "v_mfma_f32_16x16x32_bf16 v[192:195], v[40:43], v[136:139], v[192:195]\n\t"
      "v_mfma_f32_16x16x32_bf16 v[192:195], v[56:59], v[140:143], v[192:195]\n\t"
      "v_mfma_f32_16x16x32_bf16 v[196:199], v[44:47], v[136:139], v[196:199]\n\t"
      "global_load_dwordx4 v[136:139], v178, s[54:55] sc1\n\t"
      "v_mfma_f32_16x16x32_bf16 v[196:199], v[60:63], v[140:143], v[196:199]\n\t"
      "global_load_dwordx4 v[140:143], v179, s[54:55] sc1\n\t"
      "s_waitcnt vmcnt(7)\n\t"
      "v_max3_u32 v234, v144, v145, v146\n\t"
      "v_max_u32_e32 v234, v234, v147\n\t"
      "v_cmp_eq_u32_e32 vcc, -1, v234\n\t"
      "s_cbranch_vccnz FP4R_%=\n\t"
      "FP4B_%=:\n\t"
      "v_mfma_f32_16x16x32_bf16 v[184:187], v[64:67], v[144:147], v[184:187]\n\t"
      "s_waitcnt vmcnt(6)\n\t"
      "v_max3_u32 v234, v148, v149, v150\n\t"
      "v_max_u32_e32 v234, v234, v151\n\t"
      "v_cmp_eq_u32_e32 vcc, -1, v234\n\t"
      "s_cbranch_vccnz FP5R_%=\n\t"
      "FP5B_%=:\n\t"
      "v_mfma_f32_16x16x32_bf16 v[184:187], v[80:83], v[148:151], v[184:187]\n\t"
      "v_mfma_f32_16x16x32_bf16 v[188:191], v[68:71], v[144:147], v[188:191]\n\t"
      "v_mfma_f32_16x16x32_bf16 v[188:191], v[84:87], v[148:151], v[188:191]\n\t"
      "v_mfma_f32_16x16x32_bf16 v[192:195], v[72:75], v[144:147], v[192:195]\n\t"
      "v_mfma_f32_16x16x32_bf16 v[192:195], v[88:91], v[148:151], v[192:195]\n\t"
      "v_mfma_f32_16x16x32_bf16 v[196:199], v[76:79], v[144:147], v[196:199]\n\t"
      "global_load_dwordx4 v[144:147], v180, s[54:55] sc1\n\t"
      "v_mfma_f32_16x16x32_bf16 v[196:199], v[92:95], v[148:151], v[196:199]\n\t"
      "global_load_dwordx4 v[148:151], v181, s[54:55] sc1\n\t"
      "s_waitcnt vmcnt(7)\n\t"
      "v_max3_u32 v234, v152, v153, v154\n\t"
      "v_max_u32_e32 v234, v234, v155\n\t"
      "v_cmp_eq_u32_e32 vcc, -1, v234\n\t"
      "s_cbranch_vccnz FP6R_%=\n\t"
      "FP6B_%=:\n\t"
      "v_mfma_f32_16x16x32_bf16 v[184:187], v[96:99], v[152:155], v[184:187]\n\t"
      "s_waitcnt vmcnt(6)\n\t"
      "v_max3_u32 v234, v156, v157, v158\n\t"
      "v_max_u32_e32 v234, v234, v159\n\t"
      "v_cmp_eq_u32_e32 vcc, -1, v234\n\t"
      "s_cbranch_vccnz FP7R_%=\n\t"
      "FP7B_%=:\n\t"
      "v_mfma_f32_16x16x32_bf16 v[184:187], v[112:115], v[156:159], v[184:187]\n\t"
      "v_mfma_f32_16x16x32_bf16 v[188:191], v[100:103], v[152:155], v[188:191]\n\t"
      "v_mfma_f32_16x16x32_bf16 v[188:191], v[116:119], v[156:159], v[188:191]\n\t"
      "v_mfma_f32_16x16x32_bf16 v[192:195], v[104:107], v[152:155], v[192:195]\n\t"
      "v_mfma_f32_16x16x32_bf16 v[192:195], v[120:123], v[156:159], v[192:195]\n\t"
      "s_nop 3\n\t"
      "ds_write_b128 v243, v[184:187]\n\t"
      "v_mfma_f32_16x16x32_bf16 v[196:199], v[108:111], v[152:155], v[196:199]\n\t"
      "global_load_dwordx4 v[152:155], v182, s[54:55] sc1\n\t"
      "v_mfma_f32_16x16x32_bf16 v[196:199], v[124:127], v[156:159], v[196:199]\n\t"
      "global_load_dwordx4 v[156:159], v183, s[54:55] sc1\n\t"
      "ds_write_b128 v164, v[188:191]\n\t"
      "ds_write_b128 v165, v[192:195]\n\t"
      "s_nop 4\n\t"
      "ds_write_b128 v166, v[196:199]\n\t"
      "s_waitcnt lgkmcnt(0)\n\t"
      "s_barrier\n\t"
      "ds_read_b128 v[216:219], v244\n\t"
      "ds_read_b128 v[220:223], v244 offset:4096\n\t"
      "ds_read_b128 v[224:227], v244 offset:8192\n\t"
      "ds_read_b128 v[228:231], v244 offset:12288\n\t"
      "ds_read_b128 v[184:187], v244 offset:16384\n\t"
      "ds_read_b128 v[188:191], v244 offset:20480\n\t"
      "ds_read_b128 v[192:195], v244 offset:24576\n\t"
      "ds_read_b128 v[196:199], v244 offset:28672\n\t"
      "s_waitcnt lgkmcnt(6)\n\t"
      "v_add_f32_e32 v216, v216, v220\n\t"
      "v_add_f32_e32 v217, v217, v221\n\t"
      "v_add_f32_e32 v218, v218, v222\n\t"
      "v_add_f32_e32 v219, v219, v223\n\t"
      "s_waitcnt lgkmcnt(4)\n\t"
      "v_add_f32_e32 v224, v224, v228\n\t"
      "v_add_f32_e32 v225, v225, v229\n\t"
      "v_add_f32_e32 v226, v226, v230\n\t"
      "v_add_f32_e32 v227, v227, v231\n\t"
      "s_waitcnt lgkmcnt(2)\n\t"
      "v_add_f32_e32 v184, v184, v188\n\t"
      "v_add_f32_e32 v185, v185, v189\n\t"
      "v_add_f32_e32 v186, v186, v190\n\t"
      "v_add_f32_e32 v187, v187, v191\n\t"
      "v_add_f32_e32 v216, v216, v224\n\t"
      "v_add_f32_e32 v217, v217, v225\n\t"
      "v_add_f32_e32 v218, v218, v226\n\t"
      "v_add_f32_e32 v219, v219, v227\n\t"
      "s_waitcnt lgkmcnt(0)\n\t"
      "v_add_f32_e32 v192, v192, v196\n\t"
      "v_add_f32_e32 v193, v193, v197\n\t"
      "v_add_f32_e32 v194, v194, v198\n\t"
      "v_add_f32_e32 v195, v195, v199\n\t"
      "v_add_f32_e32 v184, v184, v192\n\t"
      "v_add_f32_e32 v185, v185, v193\n\t"
      "v_add_f32_e32 v186, v186, v194\n\t"
      "v_add_f32_e32 v187, v187, v195\n\t"
      "v_add_f32_e32 v216, v216, v184\n\t"
      "s_waitcnt vmcnt(7)\n\t"
      "v_mfma_f32_16x16x32_bf16 v[200:203], v[0:3], v[128:131], 0\n\t"
      "v_add_f32_e32 v217, v217, v185\n\t"
      "s_waitcnt vmcnt(6)\n\t"
      "v_mfma_f32_16x16x32_bf16 v[200:203], v[16:19], v[132:135], v[200:203]\n\t"
      "v_add_f32_e32 v218, v218, v186\n\t"
      "v_add_f32_e32 v219, v219, v187\n\t"
      "v_mfma_f32_16x16x32_bf16 v[204:207], v[4:7], v[128:131], 0\n\t"
      "v_mul_f32_e32 v220, v216, v216\n\t"
      "v_mul_f32_e32 v221, v217, v217\n\t"
      "v_mfma_f32_16x16x32_bf16 v[204:207], v[20:23], v[132:135], v[204:207]\n\t"
      "v_mul_f32_e32 v222, v218, v218\n\t"
      "v_mul_f32_e32 v223, v219, v219\n\t"
      "v_mfma_f32_16x16x32_bf16 v[208:211], v[8:11], v[128:131], 0\n\t"
      "v_fmaak_f32 v220, v235, v220, 0xc0135761\n\t"
      "v_fmaak_f32 v221, v235, v221, 0xc0135761\n\t"
      "v_mfma_f32_16x16x32_bf16 v[208:211], v[24:27], v[132:135], v[208:211]\n\t"
      "v_fmaak_f32 v222, v235, v222, 0xc0135761\n\t"
      "v_fmaak_f32 v223, v235, v223, 0xc0135761\n\t"
      "v_mfma_f32_16x16x32_bf16 v[212:215], v[12:15], v[128:131], 0\n\t"
      "global_load_dwordx4 v[128:131], v176, s[56:57] sc1\n\t"
      "v_mul_f32_e32 v220, v216, v220\n\t"
      "v_mfma_f32_16x16x32_bf16 v[212:215], v[28:31], v[132:135], v[212:215]\n\t"
      "global_load_dwordx4 v[132:135], v177, s[56:57] sc1\n\t"
      "s_waitcnt vmcnt(7)\n\t"
      "v_mfma_f32_16x16x32_bf16 v[200:203], v[32:35], v[136:139], v[200:203]\n\t"
      "v_mul_f32_e32 v221, v217, v221\n\t"
      "s_waitcnt vmcnt(6)\n\t"
      "v_mfma_f32_16x16x32_bf16 v[200:203], v[48:51], v[140:143], v[200:203]\n\t"
      "v_mul_f32_e32 v222, v218, v222\n\t"
      "v_mul_f32_e32 v223, v219, v223\n\t"
      "v_mfma_f32_16x16x32_bf16 v[204:207], v[36:39], v[136:139], v[204:207]\n\t"
      "v_exp_f32_e32 v220, v220\n\t"
      "v_mfma_f32_16x16x32_bf16 v[204:207], v[52:55], v[140:143], v[204:207]\n\t"
      "v_exp_f32_e32 v221, v221\n\t"
      "v_mfma_f32_16x16x32_bf16 v[208:211], v[40:43], v[136:139], v[208:211]\n\t"
      "v_exp_f32_e32 v222, v222\n\t"
      "v_mfma_f32_16x16x32_bf16 v[208:211], v[56:59], v[140:143], v[208:211]\n\t"
      "v_exp_f32_e32 v223, v223\n\t"
      "v_mfma_f32_16x16x32_bf16 v[212:215], v[44:47], v[136:139], v[212:215]\n\t"
      "global_load_dwordx4 v[136:139], v178, s[56:57] sc1\n\t"
      "v_add_f32_e32 v220, 1.0, v220\n\t"
      "v_mfma_f32_16x16x32_bf16 v[212:215], v[60:63], v[140:143], v[212:215]\n\t"
      "global_load_dwordx4 v[140:143], v179, s[56:57] sc1\n\t"
      "s_waitcnt vmcnt(7)\n\t"
      "v_mfma_f32_16x16x32_bf16 v[200:203], v[64:67], v[144:147], v[200:203]\n\t"
      "v_add_f32_e32 v221, 1.0, v221\n\t"
      "s_waitcnt vmcnt(6)\n\t"
      "v_mfma_f32_16x16x32_bf16 v[200:203], v[80:83], v[148:151], v[200:203]\n\t"
      "v_add_f32_e32 v222, 1.0, v222\n\t"
      "v_add_f32_e32 v223, 1.0, v223\n\t"
      "v_mfma_f32_16x16x32_bf16 v[204:207], v[68:71], v[144:147], v[204:207]\n\t"
      "v_rcp_f32_e32 v220, v220\n\t"
      "v_mfma_f32_16x16x32_bf16 v[204:207], v[84:87], v[148:151], v[204:207]\n\t"
      "v_rcp_f32_e32 v221, v221\n\t"
      "v_mfma_f32_16x16x32_bf16 v[208:211], v[72:75], v[144:147], v[208:211]\n\t"
      "v_rcp_f32_e32 v222, v222\n\t"
      "v_mfma_f32_16x16x32_bf16 v[208:211], v[88:91], v[148:151], v[208:211]\n\t"
      "v_rcp_f32_e32 v223, v223\n\t"
      "v_mfma_f32_16x16x32_bf16 v[212:215], v[76:79], v[144:147], v[212:215]\n\t"
      "global_load_dwordx4 v[144:147], v180, s[56:57] sc1\n\t"
      "v_mul_f32_e32 v216, v216, v220\n\t"
      "v_mfma_f32_16x16x32_bf16 v[212:215], v[92:95], v[148:151], v[212:215]\n\t"
      "global_load_dwordx4 v[148:151], v181, s[56:57] sc1\n\t"
      "s_waitcnt vmcnt(7)\n\t"
      "v_mfma_f32_16x16x32_bf16 v[200:203], v[96:99], v[152:155], v[200:203]\n\t"
      "v_mul_f32_e32 v217, v217, v221\n\t"
      "s_waitcnt vmcnt(6)\n\t"
      "v_mfma_f32_16x16x32_bf16 v[200:203], v[112:115], v[156:159], v[200:203]\n\t"
      "v_mul_f32_e32 v218, v218, v222\n\t"
      "v_mul_f32_e32 v219, v219, v223\n\t"
      "v_mfma_f32_16x16x32_bf16 v[204:207], v[100:103], v[152:155], v[204:207]\n\t"
      "v_cvt_pk_bf16_f32 v220, v216, v217\n\t"
      "v_cvt_pk_bf16_f32 v221, v218, v219\n\t"
      "v_mfma_f32_16x16x32_bf16 v[204:207], v[116:119], v[156:159], v[204:207]\n\t"
      "s_nop 1\n\t"
      "v_cmp_u_f32_e32 vcc, v200, v200\n\t"
      "v_min_u32_e32 v220, -2, v220\n\t"
      "v_mfma_f32_16x16x32_bf16 v[208:211], v[104:107], v[152:155], v[208:211]\n\t"
      "v_min_u32_e32 v221, -2, v221\n\t"
      "global_store_dwordx2 v245, v[220:221], s[8:9] sc1\n\t"
      "s_add_u32 s8, s8, 131072\n\t"
      "s_addc_u32 s9, s9, 0\n\t"
      "v_mfma_f32_16x16x32_bf16 v[208:211], v[120:123], v[156:159], v[208:211]\n\t"
      "s_cbranch_vccnz NP1R_%=\n\t"
      "NP1B_%=:\n\t"
      "ds_write_b128 v243, v[200:203] offset:32768\n\t"
      "v_mfma_f32_16x16x32_bf16 v[212:215], v[108:111], v[152:155], v[212:215]\n\t"
      "global_load_dwordx4 v[152:155], v182, s[56:57] sc1\n\t"
      "v_mfma_f32_16x16x32_bf16 v[212:215], v[124:127], v[156:159], v[212:215]\n\t"
      "global_load_dwordx4 v[156:159], v183, s[56:57] sc1\n\t"
      "ds_write_b128 v164, v[204:207] offset:32768\n\t"
      "s_nop 0\n\t"
      "ds_write_b128 v165, v[208:211] offset:32768\n\t"
      "s_nop 3\n\t"
      "ds_write_b128 v166, v[212:215] offset:32768\n\t"
      "s_waitcnt lgkmcnt(0)\n\t"
      "s_barrier\n\t"
      "ds_read_b128 v[216:219], v244 offset:32768\n\t"
      "ds_read_b128 v[220:223], v244 offset:36864\n\t"
      "ds_read_b128 v[224:227], v244 offset:40960\n\t"
      "ds_read_b128 v[228:231], v244 offset:45056\n\t"
      "ds_read_b128 v[200:203], v244 offset:49152\n\t"
      "ds_read_b128 v[204:207], v244 offset:53248\n\t"
      "ds_read_b128 v[208:211], v244 offset:57344\n\t"
      "ds_read_b128 v[212:215], v244 offset:61440\n\t"
      "s_waitcnt lgkmcnt(6)\n\t"
      "v_add_f32_e32 v216, v216, v220\n\t"
      "v_add_f32_e32 v217, v217, v221\n\t"
      "v_add_f32_e32 v218, v218, v222\n\t"
      "v_add_f32_e32 v219, v219, v223\n\t"
      "s_waitcnt lgkmcnt(4)\n\t"
      "v_add_f32_e32 v224, v224, v228\n\t"
      "v_add_f32_e32 v225, v225, v229\n\t"
      "v_add_f32_e32 v226, v226, v230\n\t"
      "v_add_f32_e32 v227, v227, v231\n\t"
      "s_waitcnt lgkmcnt(2)\n\t"
      "v_add_f32_e32 v200, v200, v204\n\t"
      "v_add_f32_e32 v201, v201, v205\n\t"
      "v_add_f32_e32 v202, v202, v206\n\t"
      "v_add_f32_e32 v203, v203, v207\n\t"
      "v_add_f32_e32 v216, v216, v224\n\t"
      "v_add_f32_e32 v217, v217, v225\n\t"
      "v_add_f32_e32 v218, v218, v226\n\t"
      "v_add_f32_e32 v219, v219, v227\n\t"
      "s_waitcnt lgkmcnt(0)\n\t"
      "v_add_f32_e32 v208, v208, v212\n\t"
      "v_add_f32_e32 v209, v209, v213\n\t"
      "v_add_f32_e32 v210, v210, v214\n\t"
      "v_add_f32_e32 v211, v211, v215\n\t"
      "v_add_f32_e32 v200, v200, v208\n\t"
      "v_add_f32_e32 v201, v201, v209\n\t"
      "v_add_f32_e32 v202, v202, v210\n\t"
      "v_add_f32_e32 v203, v203, v211\n\t"
      "v_add_f32_e32 v216, v216, v200\n\t"
      "s_waitcnt vmcnt(8)\n\t"
      "v_mfma_f32_16x16x32_bf16 v[184:187], v[0:3], v[128:131], 0\n\t"
      "v_add_f32_e32 v217, v217, v201\n\t"
      "s_cmp_eq_u32 s17, 0\n\t"
      "s_cbranch_scc0 HI0_%=\n\t"
      "s_mov_b64 exec, 0xf\n\t"
      "global_store_dwordx2 v247, v[232:233], s[12:13] sc1\n\t"
      "s_mov_b64 exec, -1\n\t"
      "s_nop 0\n\t"
      "v_add_u32_e32 v232, 6, v232\n\t"
      "HI0_%=:\n\t"
      "s_waitcnt vmcnt(7)\n\t"
      "v_mfma_f32_16x16x32_bf16 v[184:187], v[16:19], v[132:135], v[184:187]\n\t"
      "v_add_f32_e32 v218, v218, v202\n\t"
      "v_add_f32_e32 v219, v219, v203\n\t"
      "v_mfma_f32_16x16x32_bf16 v[188:191], v[4:7], v[128:131], 0\n\t"
      "v_mul_f32_e32 v220, v216, v216\n\t"
      "v_mul_f32_e32 v221, v217, v217\n\t"
      "v_mfma_f32_16x16x32_bf16 v[188:191], v[20:23], v[132:135], v[188:191]\n\t"
      "v_mul_f32_e32 v222, v218, v218\n\t"
      "v_mul_f32_e32 v223, v219, v219\n\t"
      "v_mfma_f32_16x16x32_bf16 v[192:195], v[8:11], v[128:131], 0\n\t"
      "v_fmaak_f32 v220, v235, v220, 0xc0135761\n\t"
      "v_fmaak_f32 v221, v235, v221, 0xc0135761\n\t"
      "v_mfma_f32_16x16x32_bf16 v[192:195], v[24:27], v[132:135], v[192:195]\n\t"
      "v_fmaak_f32 v222, v235, v222, 0xc0135761\n\t"
      "v_fmaak_f32 v223, v235, v223, 0xc0135761\n\t"
      "v_mfma_f32_16x16x32_bf16 v[196:199], v[12:15], v[128:131], 0\n\t"
      "global_load_dwordx4 v[128:131], v176, s[58:59] sc1\n\t"
      "v_mul_f32_e32 v220, v216, v220\n\t"
      "v_mfma_f32_16x16x32_bf16 v[196:199], v[28:31], v[132:135], v[196:199]\n\t"
      "global_load_dwordx4 v[132:135], v177, s[58:59] sc1\n\t"
      "s_waitcnt vmcnt(8)\n\t"
      "v_mfma_f32_16x16x32_bf16 v[184:187], v[32:35], v[136:139], v[184:187]\n\t"
      "v_mul_f32_e32 v221, v217, v221\n\t"
      "s_waitcnt vmcnt(7)\n\t"
      "v_mfma_f32_16x16x32_bf16 v[184:187], v[48:51], v[140:143], v[184:187]\n\t"
      "v_mul_f32_e32 v222, v218, v222\n\t"
      "v_mul_f32_e32 v223, v219, v223\n\t"
      "v_mfma_f32_16x16x32_bf16 v[188:191], v[36:39], v[136:139], v[188:191]\n\t"
      "v_exp_f32_e32 v220, v220\n\t"
      "v_mfma_f32_16x16x32_bf16 v[188:191], v[52:55], v[140:143], v[188:191]\n\t"
      "v_exp_f32_e32 v221, v221\n\t"
      "v_mfma_f32_16x16x32_bf16 v[192:195], v[40:43], v[136:139], v[192:195]\n\t"
      "v_exp_f32_e32 v222, v222\n\t"
      "v_mfma_f32_16x16x32_bf16 v[192:195], v[56:59], v[140:143], v[192:195]\n\t"
      "v_exp_f32_e32 v223, v223\n\t"
      "v_mfma_f32_16x16x32_bf16 v[196:199], v[44:47], v[136:139], v[196:199]\n\t"
      "global_load_dwordx4 v[136:139], v178, s[58:59] sc1\n\t"
      "v_add_f32_e32 v220, 1.0, v220\n\t"
      "v_mfma_f32_16x16x32_bf16 v[196:199], v[60:63], v[140:143], v[196:199]\n\t"
      "global_load_dwordx4 v[140:143], v179, s[58:59] sc1\n\t"
      "s_waitcnt vmcnt(8)\n\t"
      "v_mfma_f32_16x16x32_bf16 v[184:187], v[64:67], v[144:147], v[184:187]\n\t"
      "v_add_f32_e32 v221, 1.0, v221\n\t"
      "s_waitcnt vmcnt(7)\n\t"
      "v_mfma_f32_16x16x32_bf16 v[184:187], v[80:83], v[148:151], v[184:187]\n\t"
      "v_add_f32_e32 v222, 1.0, v222\n\t"
      "v_add_f32_e32 v223, 1.0, v223\n\t"
      "v_mfma_f32_16x16x32_bf16 v[188:191], v[68:71], v[144:147], v[188:191]\n\t"
      "v_rcp_f32_e32 v220, v220\n\t"
      "v_mfma_f32_16x16x32_bf16 v[188:191], v[84:87], v[148:151], v[188:191]\n\t"
      "v_rcp_f32_e32 v221, v221\n\t"
      "v_mfma_f32_16x16x32_bf16 v[192:195], v[72:75], v[144:147], v[192:195]\n\t"
      "v_rcp_f32_e32 v222, v222\n\t"
      "v_mfma_f32_16x16x32_bf16 v[192:195], v[88:91], v[148:151], v[192:195]\n\t"
      "v_rcp_f32_e32 v223, v223\n\t"
      "v_mfma_f32_16x16x32_bf16 v[196:199], v[76:79], v[144:147], v[196:199]\n\t"
      "global_load_dwordx4 v[144:147], v180, s[58:59] sc1\n\t"
      "v_mul_f32_e32 v216, v216, v220\n\t"
      "v_mfma_f32_16x16x32_bf16 v[196:199], v[92:95], v[148:151], v[196:199]\n\t"
      "global_load_dwordx4 v[148:151], v181, s[58:59] sc1\n\t"
      "s_waitcnt vmcnt(7)\n\t"
      "v_mfma_f32_16x16x32_bf16 v[184:187], v[96:99], v[152:155], v[184:187]\n\t"
      "v_mul_f32_e32 v217, v217, v221\n\t"
      "s_waitcnt vmcnt(6)\n\t"
      "v_mfma_f32_16x16x32_bf16 v[184:187], v[112:115], v[156:159], v[184:187]\n\t"
      "v_mul_f32_e32 v218, v218, v222\n\t"
      "v_mul_f32_e32 v219, v219, v223\n\t"
      "v_mfma_f32_16x16x32_bf16 v[188:191], v[100:103], v[152:155], v[188:191]\n\t"
      "v_cvt_pk_bf16_f32 v220, v216, v217\n\t"
      "v_cvt_pk_bf16_f32 v221, v218, v219\n\t"
      "v_mfma_f32_16x16x32_bf16 v[188:191], v[116:119], v[156:159], v[188:191]\n\t"
      "s_nop 1\n\t"
      "v_cmp_u_f32_e32 vcc, v184, v184\n\t"
      "v_min_u32_e32 v220, -2, v220\n\t"
      "v_mfma_f32_16x16x32_bf16 v[192:195], v[104:107], v[152:155], v[192:195]\n\t"
      "v_min_u32_e32 v221, -2, v221\n\t"
      "global_store_dwordx2 v245, v[220:221], s[8:9] sc1\n\t"
      "s_add_u32 s8, s8, 131072\n\t"
      "s_addc_u32 s9, s9, 0\n\t"
      "v_mfma_f32_16x16x32_bf16 v[192:195], v[120:123], v[156:159], v[192:195]\n\t"
      "s_cbranch_vccnz NP2R_%=\n\t"
      "NP2B_%=:\n\t"
      "ds_write_b128 v243, v[184:187]\n\t"
      "v_mfma_f32_16x16x32_bf16 v[196:199], v[108:111], v[152:155], v[196:199]\n\t"
      "global_load_dwordx4 v[152:155], v182, s[58:59] sc1\n\t"
      "v_mfma_f32_16x16x32_bf16 v[196:199], v[124:127], v[156:159], v[196:199]\n\t"
      "global_load_dwordx4 v[156:159], v183, s[58:59] sc1\n\t"
      "ds_write_b128 v164, v[188:191]\n\t"
      "s_nop 0\n\t"
      "ds_write_b128 v165, v[192:195]\n\t"
      "s_nop 3\n\t"
      "ds_write_b128 v166, v[196:199]\n\t"
      "s_waitcnt lgkmcnt(0)\n\t"
      "s_barrier\n\t"
      "ds_read_b128 v[216:219], v244\n\t"
      "ds_read_b128 v[220:223], v244 offset:4096\n\t"
      "ds_read_b128 v[224:227], v244 offset:8192\n\t"
      "ds_read_b128 v[228:231], v244 offset:12288\n\t"
      "ds_read_b128 v[184:187], v244 offset:16384\n\t"
      "ds_read_b128 v[188:191], v244 offset:20480\n\t"
      "ds_read_b128 v[192:195], v244 offset:24576\n\t"
      "ds_read_b128 v[196:199], v244 offset:28672\n\t"
      "s_waitcnt lgkmcnt(6)\n\t"
      "v_add_f32_e32 v216, v216, v220\n\t"
      "v_add_f32_e32 v217, v217, v221\n\t"
      "v_add_f32_e32 v218, v218, v222\n\t"
      "v_add_f32_e32 v219, v219, v223\n\t"
      "s_waitcnt lgkmcnt(4)\n\t"
      "v_add_f32_e32 v224, v224, v228\n\t"
      "v_add_f32_e32 v225, v225, v229\n\t"
      "v_add_f32_e32 v226, v226, v230\n\t"
      "v_add_f32_e32 v227, v227, v231\n\t"
      "s_waitcnt lgkmcnt(2)\n\t"
      "v_add_f32_e32 v184, v184, v188\n\t"
      "v_add_f32_e32 v185, v185, v189\n\t"
      "v_add_f32_e32 v186, v186, v190\n\t"
      "v_add_f32_e32 v187, v187, v191\n\t"
      "v_add_f32_e32 v216, v216, v224\n\t"
      "v_add_f32_e32 v217, v217, v225\n\t"
      "v_add_f32_e32 v218, v218, v226\n\t"
      "v_add_f32_e32 v219, v219, v227\n\t"
      "s_waitcnt lgkmcnt(0)\n\t"
      "v_add_f32_e32 v192, v192, v196\n\t"
      "v_add_f32_e32 v193, v193, v197\n\t"
      "v_add_f32_e32 v194, v194, v198\n\t"
      "v_add_f32_e32 v195, v195, v199\n\t"
      "v_add_f32_e32 v184, v184, v192\n\t"
      "v_add_f32_e32 v185, v185, v193\n\t"
      "v_add_f32_e32 v186, v186, v194\n\t"
      "v_add_f32_e32 v187, v187, v195\n\t"
      "v_add_f32_e32 v216, v216, v184\n\t"
      "s_waitcnt vmcnt(8)\n\t"
      "v_mfma_f32_16x16x32_bf16 v[200:203], v[0:3], v[128:131], 0\n\t"
      "v_add_f32_e32 v217, v217, v185\n\t"
      "s_waitcnt vmcnt(7)\n\t"
      "v_mfma_f32_16x16x32_bf16 v[200:203], v[16:19], v[132:135], v[200:203]\n\t"
      "v_add_f32_e32 v218, v218, v186\n\t"
      "v_add_f32_e32 v219, v219, v187\n\t"
      "v_mfma_f32_16x16x32_bf16 v[204:207], v[4:7], v[128:131], 0\n\t"
      "v_mul_f32_e32 v220, v216, v216\n\t"
      "v_mul_f32_e32 v221, v217, v217\n\t"
      "v_mfma_f32_16x16x32_bf16 v[204:207], v[20:23], v[132:135], v[204:207]\n\t"
      "v_mul_f32_e32 v222, v218, v218\n\t"
      "v_mul_f32_e32 v223, v219, v219\n\t"
      "v_mfma_f32_16x16x32_bf16 v[208:211], v[8:11], v[128:131], 0\n\t"
      "v_fmaak_f32 v220, v235, v220, 0xc0135761\n\t"
      "v_fmaak_f32 v221, v235, v221, 0xc0135761\n\t"
      "v_mfma_f32_16x16x32_bf16 v[208:211], v[24:27], v[132:135], v[208:211]\n\t"
      "v_fmaak_f32 v222, v235, v222, 0xc0135761\n\t"
      "v_fmaak_f32 v223, v235, v223, 0xc0135761\n\t"
      "v_mfma_f32_16x16x32_bf16 v[212:215], v[12:15], v[128:131], 0\n\t"
      "global_load_dwordx4 v[128:131], v176, s[60:61] sc1\n\t"
      "v_mul_f32_e32 v220, v216, v220\n\t"
      "v_mfma_f32_16x16x32_bf16 v[212:215], v[28:31], v[132:135], v[212:215]\n\t"
      "global_load_dwordx4 v[132:135], v177, s[60:61] sc1\n\t"
      "s_waitcnt vmcnt(8)\n\t"
      "v_mfma_f32_16x16x32_bf16 v[200:203], v[32:35], v[136:139], v[200:203]\n\t"
      "v_mul_f32_e32 v221, v217, v221\n\t"
      "s_waitcnt vmcnt(7)\n\t"
      "v_mfma_f32_16x16x32_bf16 v[200:203], v[48:51], v[140:143], v[200:203]\n\t"
      "v_mul_f32_e32 v222, v218, v222\n\t"
      "v_mul_f32_e32 v223, v219, v223\n\t"
      "v_mfma_f32_16x16x32_bf16 v[204:207], v[36:39], v[136:139], v[204:207]\n\t"
      "v_exp_f32_e32 v220, v220\n\t"
      "v_mfma_f32_16x16x32_bf16 v[204:207], v[52:55], v[140:143], v[204:207]\n\t"
      "v_exp_f32_e32 v221, v221\n\t"
      "v_mfma_f32_16x16x32_bf16 v[208:211], v[40:43], v[136:139], v[208:211]\n\t"
      "v_exp_f32_e32 v222, v222\n\t"
      "v_mfma_f32_16x16x32_bf16 v[208:211], v[56:59], v[140:143], v[208:211]\n\t"
      "v_exp_f32_e32 v223, v223\n\t"
      "v_mfma_f32_16x16x32_bf16 v[212:215], v[44:47], v[136:139], v[212:215]\n\t"
      "global_load_dwordx4 v[136:139], v178, s[60:61] sc1\n\t"
      "v_add_f32_e32 v220, 1.0, v220\n\t"
      "v_mfma_f32_16x16x32_bf16 v[212:215], v[60:63], v[140:143], v[212:215]\n\t"
      "global_load_dwordx4 v[140:143], v179, s[60:61] sc1\n\t"
      "s_waitcnt vmcnt(8)\n\t"
      "v_mfma_f32_16x16x32_bf16 v[200:203], v[64:67], v[144:147], v[200:203]\n\t"
      "v_add_f32_e32 v221, 1.0, v221\n\t"
      "s_waitcnt vmcnt(7)\n\t"
      "v_mfma_f32_16x16x32_bf16 v[200:203], v[80:83], v[148:151], v[200:203]\n\t"
      "v_add_f32_e32 v222, 1.0, v222\n\t"
      "v_add_f32_e32 v223, 1.0, v223\n\t"
      "v_mfma_f32_16x16x32_bf16 v[204:207], v[68:71], v[144:147], v[204:207]\n\t"
      "v_rcp_f32_e32 v220, v220\n\t"
      "v_mfma_f32_16x16x32_bf16 v[204:207], v[84:87], v[148:151], v[204:207]\n\t"
      "v_rcp_f32_e32 v221, v221\n\t"
      "v_mfma_f32_16x16x32_bf16 v[208:211], v[72:75], v[144:147], v[208:211]\n\t"
      "v_rcp_f32_e32 v222, v222\n\t"
      "v_mfma_f32_16x16x32_bf16 v[208:211], v[88:91], v[148:151], v[208:211]\n\t"
      "v_rcp_f32_e32 v223, v223\n\t"
      "v_mfma_f32_16x16x32_bf16 v[212:215], v[76:79], v[144:147], v[212:215]\n\t"
      "global_load_dwordx4 v[144:147], v180, s[60:61] sc1\n\t"
      "v_mul_f32_e32 v216, v216, v220\n\t"
      "v_mfma_f32_16x16x32_bf16 v[212:215], v[92:95], v[148:151], v[212:215]\n\t"
      "global_load_dwordx4 v[148:151], v181, s[60:61] sc1\n\t"
      "s_waitcnt vmcnt(7)\n\t"
      "v_mfma_f32_16x16x32_bf16 v[200:203], v[96:99], v[152:155], v[200:203]\n\t"
      "v_mul_f32_e32 v217, v217, v221\n\t"
      "s_waitcnt vmcnt(6)\n\t"
      "v_mfma_f32_16x16x32_bf16 v[200:203], v[112:115], v[156:159], v[200:203]\n\t"
      "v_mul_f32_e32 v218, v218, v222\n\t"
      "v_mul_f32_e32 v219, v219, v223\n\t"
      "v_mfma_f32_16x16x32_bf16 v[204:207], v[100:103], v[152:155], v[204:207]\n\t"
      "v_cvt_pk_bf16_f32 v220, v216, v217\n\t"
      "v_cvt_pk_bf16_f32 v221, v218, v219\n\t"
      "v_mfma_f32_16x16x32_bf16 v[204:207], v[116:119], v[156:159], v[204:207]\n\t"
      "s_nop 1\n\t"
      "v_cmp_u_f32_e32 vcc, v200, v200\n\t"
      "v_min_u32_e32 v220, -2, v220\n\t"
      "v_mfma_f32_16x16x32_bf16 v[208:211], v[104:107], v[152:155], v[208:211]\n\t"
      "v_min_u32_e32 v221, -2, v221\n\t"
      "global_store_dwordx2 v245, v[220:221], s[8:9] sc1\n\t"
      "s_add_u32 s8, s8, 131072\n\t"
      "s_addc_u32 s9, s9, 0\n\t"
      "v_mfma_f32_16x16x32_bf16 v[208:211], v[120:123], v[156:159], v[208:211]\n\t"
      "s_cbranch_vccnz NP3R_%=\n\t"
      "NP3B_%=:\n\t"
      "ds_write_b128 v243, v[200:203] offset:32768\n\t"
      "v_mfma_f32_16x16x32_bf16 v[212:215], v[108:111], v[152:155], v[212:215]\n\t"
      "global_load_dwordx4 v[152:155], v182, s[60:61] sc1\n\t"
      "v_mfma_f32_16x16x32_bf16 v[212:215], v[124:127], v[156:159], v[212:215]\n\t"
      "global_load_dwordx4 v[156:159], v183, s[60:61] sc1\n\t"
      "ds_write_b128 v164, v[204:207] offset:32768\n\t"
      "s_nop 0\n\t"
      "ds_write_b128 v165, v[208:211] offset:32768\n\t"
      "s_nop 3\n\t"
      "ds_write_b128 v166, v[212:215] offset:32768\n\t"
      "s_waitcnt lgkmcnt(0)\n\t"
      "s_barrier\n\t"
      "ds_read_b128 v[216:219], v244 offset:32768\n\t"
      "ds_read_b128 v[220:223], v244 offset:36864\n\t"
      "ds_read_b128 v[224:227], v244 offset:40960\n\t"
      "ds_read_b128 v[228:231], v244 offset:45056\n\t"
      "ds_read_b128 v[200:203], v244 offset:49152\n\t"
      "ds_read_b128 v[204:207], v244 offset:53248\n\t"
      "ds_read_b128 v[208:211], v244 offset:57344\n\t"
      "ds_read_b128 v[212:215], v244 offset:61440\n\t"
      "s_waitcnt lgkmcnt(6)\n\t"
      "v_add_f32_e32 v216, v216, v220\n\t"
      "v_add_f32_e32 v217, v217, v221\n\t"
      "v_add_f32_e32 v218, v218, v222\n\t"
      "v_add_f32_e32 v219, v219, v223\n\t"
      "s_waitcnt lgkmcnt(4)\n\t"
      "v_add_f32_e32 v224, v224, v228\n\t"
      "v_add_f32_e32 v225, v225, v229\n\t"
      "v_add_f32_e32 v226, v226, v230\n\t"
      "v_add_f32_e32 v227, v227, v231\n\t"
      "s_waitcnt lgkmcnt(2)\n\t"
      "v_add_f32_e32 v200, v200, v204\n\t"
      "v_add_f32_e32 v201, v201, v205\n\t"
      "v_add_f32_e32 v202, v202, v206\n\t"
      "v_add_f32_e32 v203, v203, v207\n\t"
      "v_add_f32_e32 v216, v216, v224\n\t"
      "v_add_f32_e32 v217, v217, v225\n\t"
      "v_add_f32_e32 v218, v218, v226\n\t"
      "v_add_f32_e32 v219, v219, v227\n\t"
      "s_waitcnt lgkmcnt(0)\n\t"
      "v_add_f32_e32 v208, v208, v212\n\t"
      "v_add_f32_e32 v209, v209, v213\n\t"
      "v_add_f32_e32 v210, v210, v214\n\t"
      "v_add_f32_e32 v211, v211, v215\n\t"
      "v_add_f32_e32 v200, v200, v208\n\t"
      "v_add_f32_e32 v201, v201, v209\n\t"
      "v_add_f32_e32 v202, v202, v210\n\t"
      "v_add_f32_e32 v203, v203, v211\n\t"
      "v_add_f32_e32 v216, v216, v200\n\t"
      "s_waitcnt vmcnt(8)\n\t"
      "v_mfma_f32_16x16x32_bf16 v[184:187], v[0:3], v[128:131], 0\n\t"
      "v_add_f32_e32 v217, v217, v201\n\t"
      "s_waitcnt vmcnt(7)\n\t"
      "v_mfma_f32_16x16x32_bf16 v[184:187], v[16:19], v[132:135], v[184:187]\n\t"
      "v_add_f32_e32 v218, v218, v202\n\t"
      "v_add_f32_e32 v219, v219, v203\n\t"
      "v_mfma_f32_16x16x32_bf16 v[188:191], v[4:7], v[128:131], 0\n\t"
      "v_mul_f32_e32 v220, v216, v216\n\t"
      "v_mul_f32_e32 v221, v217, v217\n\t"
      "v_mfma_f32_16x16x32_bf16 v[188:191], v[20:23], v[132:135], v[188:191]\n\t"
      "v_mul_f32_e32 v222, v218, v218\n\t"
      "v_mul_f32_e32 v223, v219, v219\n\t"
      "v_mfma_f32_16x16x32_bf16 v[192:195], v[8:11], v[128:131], 0\n\t"
      "v_fmaak_f32 v220, v235, v220, 0xc0135761\n\t"
      "v_fmaak_f32 v221, v235, v221, 0xc0135761\n\t"
      "v_mfma_f32_16x16x32_bf16 v[192:195], v[24:27], v[132:135], v[192:195]\n\t"
      "v_fmaak_f32 v222, v235, v222, 0xc0135761\n\t"
      "v_fmaak_f32 v223, v235, v223, 0xc0135761\n\t"
      "v_mfma_f32_16x16x32_bf16 v[196:199], v[12:15], v[128:131], 0\n\t"
      "global_load_dwordx4 v[128:131], v176, s[62:63] sc1\n\t"
      "v_mul_f32_e32 v220, v216, v220\n\t"
      "v_mfma_f32_16x16x32_bf16 v[196:199], v[28:31], v[132:135], v[196:199]\n\t"
      ".LhopD0:\n\t"
      "s_cbranch_execz .LbackD\n\t"
      "global_load_dwordx4 v[132:135], v177, s[62:63] sc1\n\t"
      "s_waitcnt vmcnt(8)\n\t"
      "v_mfma_f32_16x16x32_bf16 v[184:187], v[32:35], v[136:139], v[184:187]\n\t"
      "v_mul_f32_e32 v221, v217, v221\n\t"
      "s_waitcnt vmcnt(7)\n\t"
      "v_mfma_f32_16x16x32_bf16 v[184:187], v[48:51], v[140:143], v[184:187]\n\t"
      "v_mul_f32_e32 v222, v218, v222\n\t"
      "v_mul_f32_e32 v223, v219, v223\n\t"
      "v_mfma_f32_16x16x32_bf16 v[188:191], v[36:39], v[136:139], v[188:191]\n\t"
      "v_exp_f32_e32 v220, v220\n\t"
      "v_mfma_f32_16x16x32_bf16 v[188:191], v[52:55], v[140:143], v[188:191]\n\t"
      ".LhopD1:\n\t"
      "s_cbranch_execz .LbackD\n\t"
      "v_exp_f32_e32 v221, v221\n\t"
      "v_mfma_f32_16x16x32_bf16 v[192:195], v[40:43], v[136:139], v[192:195]\n\t"
      "v_exp_f32_e32 v222, v222\n\t"
      "v_mfma_f32_16x16x32_bf16 v[192:195], v[56:59], v[140:143], v[192:195]\n\t"
      "v_exp_f32_e32 v223, v223\n\t"
      "v_mfma_f32_16x16x32_bf16 v[196:199], v[44:47], v[136:139], v[196:199]\n\t"
      "global_load_dwordx4 v[136:139], v178, s[62:63] sc1\n\t"
      "v_add_f32_e32 v220, 1.0, v220\n\t"
      "v_mfma_f32_16x16x32_bf16 v[196:199], v[60:63], v[140:143], v[196:199]\n\t"
      "global_load_dwordx4 v[140:143], v179, s[62:63] sc1\n\t"
      "s_waitcnt vmcnt(8)\n\t"
      "v_mfma_f32_16x16x32_bf16 v[184:187], v[64:67], v[144:147], v[184:187]\n\t"
      "v_add_f32_e32 v221, 1.0, v221\n\t"
      "s_waitcnt vmcnt(7)\n\t"
      "v_mfma_f32_16x16x32_bf16 v[184:187], v[80:83], v[148:151], v[184:187]\n\t"
      "v_add_f32_e32 v222, 1.0, v222\n\t"
      "v_add_f32_e32 v223, 1.0, v223\n\t"
      "v_mfma_f32_16x16x32_bf16 v[188:191], v[68:71], v[144:147], v[188:191]\n\t"
      "v_rcp_f32_e32 v220, v220\n\t"
      "v_mfma_f32_16x16x32_bf16 v[188:191], v[84:87], v[148:151], v[188:191]\n\t"
      "v_rcp_f32_e32 v221, v221\n\t"
      "v_mfma_f32_16x16x32_bf16 v[192:195], v[72:75], v[144:147], v[192:195]\n\t"
      "v_rcp_f32_e32 v222, v222\n\t"
      "v_mfma_f32_16x16x32_bf16 v[192:195], v[88:91], v[148:151], v[192:195]\n\t"
      "v_rcp_f32_e32 v223, v223\n\t"
      "v_mfma_f32_16x16x32_bf16 v[196:199], v[76:79], v[144:147], v[196:199]\n\t"
      "global_load_dwordx4 v[144:147], v180, s[62:63] sc1\n\t"
      "v_mul_f32_e32 v216, v216, v220\n\t"
      "v_mfma_f32_16x16x32_bf16 v[196:199], v[92:95], v[148:151], v[196:199]\n\t"
      "global_load_dwordx4 v[148:151], v181, s[62:63] sc1\n\t"
      "s_waitcnt vmcnt(7)\n\t"
      "v_mfma_f32_16x16x32_bf16 v[184:187], v[96:99], v[152:155], v[184:187]\n\t"
      "v_mul_f32_e32 v217, v217, v221\n\t"
      "s_waitcnt vmcnt(6)\n\t"
      "v_mfma_f32_16x16x32_bf16 v[184:187], v[112:115], v[156:159], v[184:187]\n\t"
      "v_mul_f32_e32 v218, v218, v222\n\t"
      "v_mul_f32_e32 v219, v219, v223\n\t"
      "v_mfma_f32_16x16x32_bf16 v[188:191], v[100:103], v[152:155], v[188:191]\n\t"
      "v_cvt_pk_bf16_f32 v220, v216, v217\n\t"
      "v_cvt_pk_bf16_f32 v221, v218, v219\n\t"
      "v_mfma_f32_16x16x32_bf16 v[188:191], v[116:119], v[156:159], v[188:191]\n\t"
      "s_nop 1\n\t"
      "v_cmp_u_f32_e32 vcc, v184, v184\n\t"
      "v_min_u32_e32 v220, -2, v220\n\t"
      "v_mfma_f32_16x16x32_bf16 v[192:195], v[104:107], v[152:155], v[192:195]\n\t"
      "v_min_u32_e32 v221, -2, v221\n\t"
      "global_store_dwordx2 v245, v[220:221], s[8:9] sc1\n\t"
      "s_add_u32 s8, s8, 131072\n\t"
      "s_addc_u32 s9, s9, 0\n\t"
      "v_mfma_f32_16x16x32_bf16 v[192:195], v[120:123], v[156:159], v[192:195]\n\t"
      "s_cbranch_vccnz NP4R_%=\n\t"
      "NP4B_%=:\n\t"
      "ds_write_b128 v243, v[184:187]\n\t"
      "v_mfma_f32_16x16x32_bf16 v[196:199], v[108:111], v[152:155], v[196:199]\n\t"
      "global_load_dwordx4 v[152:155], v182, s[62:63] sc1\n\t"
      "v_mfma_f32_16x16x32_bf16 v[196:199], v[124:127], v[156:159], v[196:199]\n\t"
      "global_load_dwordx4 v[156:159], v183, s[62:63] sc1\n\t"
      "ds_write_b128 v164, v[188:191]\n\t"
      "s_nop 0\n\t"
      "ds_write_b128 v165, v[192:195]\n\t"
      "s_nop 3\n\t"
      "ds_write_b128 v166, v[196:199]\n\t"
      "s_waitcnt lgkmcnt(0)\n\t"
      "s_barrier\n\t"
      "ds_read_b128 v[216:219], v244\n\t"
      "ds_read_b128 v[220:223], v244 offset:4096\n\t"
      "ds_read_b128 v[224:227], v244 offset:8192\n\t"
      "ds_read_b128 v[228:231], v244 offset:12288\n\t"
      "ds_read_b128 v[184:187], v244 offset:16384\n\t"
      "ds_read_b128 v[188:191], v244 offset:20480\n\t"
      "ds_read_b128 v[192:195], v244 offset:24576\n\t"
      "ds_read_b128 v[196:199], v244 offset:28672\n\t"
      "s_waitcnt lgkmcnt(6)\n\t"
      "v_add_f32_e32 v216, v216, v220\n\t"
      "v_add_f32_e32 v217, v217, v221\n\t"
      "v_add_f32_e32 v218, v218, v222\n\t"
      "v_add_f32_e32 v219, v219, v223\n\t"
      "s_waitcnt lgkmcnt(4)\n\t"
      "v_add_f32_e32 v224, v224, v228\n\t"
      "v_add_f32_e32 v225, v225, v229\n\t"
      "v_add_f32_e32 v226, v226, v230\n\t"
      "v_add_f32_e32 v227, v227, v231\n\t"
      "s_waitcnt lgkmcnt(2)\n\t"
      "v_add_f32_e32 v184, v184, v188\n\t"
      "v_add_f32_e32 v185, v185, v189\n\t"
      "v_add_f32_e32 v186, v186, v190\n\t"
      "v_add_f32_e32 v187, v187, v191\n\t"
      "v_add_f32_e32 v216, v216, v224\n\t"
      "v_add_f32_e32 v217, v217, v225\n\t"
      "v_add_f32_e32 v218, v218, v226\n\t"
      "v_add_f32_e32 v219, v219, v227\n\t"
      "s_waitcnt lgkmcnt(0)\n\t"
      "v_add_f32_e32 v192, v192, v196\n\t"
      "v_add_f32_e32 v193, v193, v197\n\t"
      "v_add_f32_e32 v194, v194, v198\n\t"
      "v_add_f32_e32 v195, v195, v199\n\t"
      "v_add_f32_e32 v184, v184, v192\n\t"
      "v_add_f32_e32 v185, v185, v193\n\t"
      "v_add_f32_e32 v186, v186, v194\n\t"
      "v_add_f32_e32 v187, v187, v195\n\t"
      "v_add_f32_e32 v216, v216, v184\n\t"
      "s_waitcnt vmcnt(8)\n\t"
      "v_mfma_f32_16x16x32_bf16 v[200:203], v[0:3], v[128:131], 0\n\t"
      "v_add_f32_e32 v217, v217, v185\n\t"
      "s_waitcnt vmcnt(7)\n\t"
      "v_mfma_f32_16x16x32_bf16 v[200:203], v[16:19], v[132:135], v[200:203]\n\t"
      "v_add_f32_e32 v218, v218, v186\n\t"
      "v_add_f32_e32 v219, v219, v187\n\t"
      "v_mfma_f32_16x16x32_bf16 v[204:207], v[4:7], v[128:131], 0\n\t"
      "v_mul_f32_e32 v220, v216, v216\n\t"
      "v_mul_f32_e32 v221, v217, v217\n\t"
      "v_mfma_f32_16x16x32_bf16 v[204:207], v[20:23], v[132:135], v[204:207]\n\t"
      "v_mul_f32_e32 v222, v218, v218\n\t"
      "v_mul_f32_e32 v223, v219, v219\n\t"
      "v_mfma_f32_16x16x32_bf16 v[208:211], v[8:11], v[128:131], 0\n\t"
      "v_fmaak_f32 v220, v235, v220, 0xc0135761\n\t"
      "v_fmaak_f32 v221, v235, v221, 0xc0135761\n\t"
      "v_mfma_f32_16x16x32_bf16 v[208:211], v[24:27], v[132:135], v[208:211]\n\t"
      "v_fmaak_f32 v222, v235, v222, 0xc0135761\n\t"
      "v_fmaak_f32 v223, v235, v223, 0xc0135761\n\t"
      "v_mfma_f32_16x16x32_bf16 v[212:215], v[12:15], v[128:131], 0\n\t"
      "global_load_dwordx4 v[128:131], v176, s[64:65] sc1\n\t"
      "v_mul_f32_e32 v220, v216, v220\n\t"
      "v_mfma_f32_16x16x32_bf16 v[212:215], v[28:31], v[132:135], v[212:215]\n\t"
      "global_load_dwordx4 v[132:135], v177, s[64:65] sc1\n\t"
      "s_waitcnt vmcnt(8)\n\t"
      "v_mfma_f32_16x16x32_bf16 v[200:203], v[32:35], v[136:139], v[200:203]\n\t"
      "v_mul_f32_e32 v221, v217, v221\n\t"
      "s_waitcnt vmcnt(7)\n\t"
      "v_mfma_f32_16x16x32_bf16 v[200:203], v[48:51], v[140:143], v[200:203]\n\t"
      "v_mul_f32_e32 v222, v218, v222\n\t"
      "v_mul_f32_e32 v223, v219, v223\n\t"
      "v_mfma_f32_16x16x32_bf16 v[204:207], v[36:39], v[136:139], v[204:207]\n\t"
      "v_exp_f32_e32 v220, v220\n\t"
      "v_mfma_f32_16x16x32_bf16 v[204:207], v[52:55], v[140:143], v[204:207]\n\t"
      "v_exp_f32_e32 v221, v221\n\t"
      "v_mfma_f32_16x16x32_bf16 v[208:211], v[40:43], v[136:139], v[208:211]\n\t"
      "v_exp_f32_e32 v222, v222\n\t"
      "v_mfma_f32_16x16x32_bf16 v[208:211], v[56:59], v[140:143], v[208:211]\n\t"
      "v_exp_f32_e32 v223, v223\n\t"
      "v_mfma_f32_16x16x32_bf16 v[212:215], v[44:47], v[136:139], v[212:215]\n\t"
      "global_load_dwordx4 v[136:139], v178, s[64:65] sc1\n\t"
      "v_add_f32_e32 v220, 1.0, v220\n\t"
      "v_mfma_f32_16x16x32_bf16 v[212:215], v[60:63], v[140:143], v[212:215]\n\t"
      "global_load_dwordx4 v[140:143], v179, s[64:65] sc1\n\t"
      "s_waitcnt vmcnt(8)\n\t"
      "v_mfma_f32_16x16x32_bf16 v[200:203], v[64:67], v[144:147], v[200:203]\n\t"
      "v_add_f32_e32 v221, 1.0, v221\n\t"
      "s_waitcnt vmcnt(7)\n\t"
      "v_mfma_f32_16x16x32_bf16 v[200:203], v[80:83], v[148:151], v[200:203]\n\t"
      "v_add_f32_e32 v222, 1.0, v222\n\t"
      "v_add_f32_e32 v223, 1.0, v223\n\t"
      "v_mfma_f32_16x16x32_bf16 v[204:207], v[68:71], v[144:147], v[204:207]\n\t"
      "v_rcp_f32_e32 v220, v220\n\t"
      "v_mfma_f32_16x16x32_bf16 v[204:207], v[84:87], v[148:151], v[204:207]\n\t"
      "v_rcp_f32_e32 v221, v221\n\t"
      "v_mfma_f32_16x16x32_bf16 v[208:211], v[72:75], v[144:147], v[208:211]\n\t"
      "v_rcp_f32_e32 v222, v222\n\t"
      "v_mfma_f32_16x16x32_bf16 v[208:211], v[88:91], v[148:151], v[208:211]\n\t"
      "v_rcp_f32_e32 v223, v223\n\t"
      "v_mfma_f32_16x16x32_bf16 v[212:215], v[76:79], v[144:147], v[212:215]\n\t"
      "global_load_dwordx4 v[144:147], v180, s[64:65] sc1\n\t"
      "v_mul_f32_e32 v216, v216, v220\n\t"
      "v_mfma_f32_16x16x32_bf16 v[212:215], v[92:95], v[148:151], v[212:215]\n\t"
      "global_load_dwordx4 v[148:151], v181, s[64:65] sc1\n\t"
      "s_waitcnt vmcnt(7)\n\t"
      "v_mfma_f32_16x16x32_bf16 v[200:203], v[96:99], v[152:155], v[200:203]\n\t"
      "v_mul_f32_e32 v217, v217, v221\n\t"
      "s_waitcnt vmcnt(6)\n\t"
      "v_mfma_f32_16x16x32_bf16 v[200:203], v[112:115], v[156:159], v[200:203]\n\t"
      "v_mul_f32_e32 v218, v218, v222\n\t"
      "v_mul_f32_e32 v219, v219, v223\n\t"
      "v_mfma_f32_16x16x32_bf16 v[204:207], v[100:103], v[152:155], v[204:207]\n\t"
      "v_cvt_pk_bf16_f32 v220, v216, v217\n\t"
      "v_cvt_pk_bf16_f32 v221, v218, v219\n\t"
      "v_mfma_f32_16x16x32_bf16 v[204:207], v[116:119], v[156:159], v[204:207]\n\t"
      "s_nop 1\n\t"
      "v_cmp_u_f32_e32 vcc, v200, v200\n\t"
      "v_min_u32_e32 v220, -2, v220\n\t"
      "v_mfma_f32_16x16x32_bf16 v[208:211], v[104:107], v[152:155], v[208:211]\n\t"
      "v_min_u32_e32 v221, -2, v221\n\t"
      "global_store_dwordx2 v245, v[220:221], s[8:9] sc1\n\t"
      "s_add_u32 s8, s8, 131072\n\t"
      "s_addc_u32 s9, s9, 0\n\t"
      "v_mfma_f32_16x16x32_bf16 v[208:211], v[120:123], v[156:159], v[208:211]\n\t"
      "s_cbranch_vccnz NP5R_%=\n\t"
      "NP5B_%=:\n\t"
      "ds_write_b128 v243, v[200:203] offset:32768\n\t"
      "v_mfma_f32_16x16x32_bf16 v[212:215], v[108:111], v[152:155], v[212:215]\n\t"
      "global_load_dwordx4 v[152:155], v182, s[64:65] sc1\n\t"
      "v_mfma_f32_16x16x32_bf16 v[212:215], v[124:127], v[156:159], v[212:215]\n\t"
      "global_load_dwordx4 v[156:159], v183, s[64:65] sc1\n\t"
      "ds_write_b128 v164, v[204:207] offset:32768\n\t"
      "s_nop 0\n\t"
      "ds_write_b128 v165, v[208:211] offset:32768\n\t"
      "s_nop 3\n\t"
      "ds_write_b128 v166, v[212:215] offset:32768\n\t"
      "s_waitcnt lgkmcnt(0)\n\t"
      "s_barrier\n\t"
      "ds_read_b128 v[216:219], v244 offset:32768\n\t"
      "ds_read_b128 v[220:223], v244 offset:36864\n\t"
      "ds_read_b128 v[224:227], v244 offset:40960\n\t"
      "ds_read_b128 v[228:231], v244 offset:45056\n\t"
      "ds_read_b128 v[200:203], v244 offset:49152\n\t"
      "ds_read_b128 v[204:207], v244 offset:53248\n\t"
      "ds_read_b128 v[208:211], v244 offset:57344\n\t"
      "ds_read_b128 v[212:215], v244 offset:61440\n\t"
      "s_waitcnt lgkmcnt(6)\n\t"
      "v_add_f32_e32 v216, v216, v220\n\t"
      "v_add_f32_e32 v217, v217, v221\n\t"
      "v_add_f32_e32 v218, v218, v222\n\t"
      "v_add_f32_e32 v219, v219, v223\n\t"
      "s_waitcnt lgkmcnt(4)\n\t"
      "v_add_f32_e32 v224, v224, v228\n\t"
      "v_add_f32_e32 v225, v225, v229\n\t"
      "v_add_f32_e32 v226, v226, v230\n\t"
      "v_add_f32_e32 v227, v227, v231\n\t"
      "s_waitcnt lgkmcnt(2)\n\t"
      "v_add_f32_e32 v200, v200, v204\n\t"
      "v_add_f32_e32 v201, v201, v205\n\t"
      "v_add_f32_e32 v202, v202, v206\n\t"
      "v_add_f32_e32 v203, v203, v207\n\t"
      "v_add_f32_e32 v216, v216, v224\n\t"
      "v_add_f32_e32 v217, v217, v225\n\t"
      "v_add_f32_e32 v218, v218, v226\n\t"
      "v_add_f32_e32 v219, v219, v227\n\t"
      "s_waitcnt lgkmcnt(0)\n\t"
      "v_add_f32_e32 v208, v208, v212\n\t"
      "v_add_f32_e32 v209, v209, v213\n\t"
      "v_add_f32_e32 v210, v210, v214\n\t"
      "v_add_f32_e32 v211, v211, v215\n\t"
      "v_add_f32_e32 v200, v200, v208\n\t"
      "s_waitcnt vmcnt(8)\n\t"
      "v_mfma_f32_16x16x32_bf16 v[184:187], v[0:3], v[128:131], 0\n\t"
      "v_add_f32_e32 v201, v201, v209\n\t"
      "s_waitcnt vmcnt(7)\n\t"
      "v_mfma_f32_16x16x32_bf16 v[184:187], v[16:19], v[132:135], v[184:187]\n\t"
      "v_add_f32_e32 v202, v202, v210\n\t"
      "v_add_f32_e32 v203, v203, v211\n\t"
      "v_mfma_f32_16x16x32_bf16 v[188:191], v[4:7], v[128:131], 0\n\t"
      "v_add_f32_e32 v216, v216, v200\n\t"
      "v_add_f32_e32 v217, v217, v201\n\t"
      "v_mfma_f32_16x16x32_bf16 v[188:191], v[20:23], v[132:135], v[188:191]\n\t"
      "v_add_f32_e32 v218, v218, v202\n\t"
      "v_add_f32_e32 v219, v219, v203\n\t"
      "v_mfma_f32_16x16x32_bf16 v[192:195], v[8:11], v[128:131], 0\n\t"
      "v_mul_f32_e32 v220, v216, v216\n\t"
      "v_mul_f32_e32 v221, v217, v217\n\t"
      "v_mfma_f32_16x16x32_bf16 v[192:195], v[24:27], v[132:135], v[192:195]\n\t"
      "v_mul_f32_e32 v222, v218, v218\n\t"
      "v_mul_f32_e32 v223, v219, v219\n\t"
      "v_mfma_f32_16x16x32_bf16 v[196:199], v[12:15], v[128:131], 0\n\t"
      "v_fmaak_f32 v220, v235, v220, 0xc0135761\n\t"
      "v_fmaak_f32 v221, v235, v221, 0xc0135761\n\t"
      "v_mfma_f32_16x16x32_bf16 v[196:199], v[28:31], v[132:135], v[196:199]\n\t"
      "v_fmaak_f32 v222, v235, v222, 0xc0135761\n\t"
      "s_waitcnt vmcnt(6)\n\t"
      "v_mfma_f32_16x16x32_bf16 v[184:187], v[32:35], v[136:139], v[184:187]\n\t"
      "v_fmaak_f32 v223, v235, v223, 0xc0135761\n\t"
      "s_waitcnt vmcnt(5)\n\t"
      "v_mfma_f32_16x16x32_bf16 v[184:187], v[48:51], v[140:143], v[184:187]\n\t"
      "v_mul_f32_e32 v220, v216, v220\n\t"
      "v_mul_f32_e32 v221, v217, v221\n\t"
      "v_mfma_f32_16x16x32_bf16 v[188:191], v[36:39], v[136:139], v[188:191]\n\t"
      "v_mul_f32_e32 v222, v218, v222\n\t"
      "v_mul_f32_e32 v223, v219, v223\n\t"
      "v_mfma_f32_16x16x32_bf16 v[188:191], v[52:55], v[140:143], v[188:191]\n\t"
      "v_exp_f32_e32 v220, v220\n\t"
      "v_mfma_f32_16x16x32_bf16 v[192:195], v[40:43], v[136:139], v[192:195]\n\t"
      "v_exp_f32_e32 v221, v221\n\t"
      "v_mfma_f32_16x16x32_bf16 v[192:195], v[56:59], v[140:143], v[192:195]\n\t"
      "v_exp_f32_e32 v222, v222\n\t"
      "v_mfma_f32_16x16x32_bf16 v[196:199], v[44:47], v[136:139], v[196:199]\n\t"
      "v_exp_f32_e32 v223, v223\n\t"
      "v_mfma_f32_16x16x32_bf16 v[196:199], v[60:63], v[140:143], v[196:199]\n\t"
      "s_waitcnt vmcnt(4)\n\t"
      "v_mfma_f32_16x16x32_bf16 v[184:187], v[64:67], v[144:147], v[184:187]\n\t"
      "s_waitcnt vmcnt(3)\n\t"
      "v_mfma_f32_16x16x32_bf16 v[184:187], v[80:83], v[148:151], v[184:187]\n\t"
      "v_add_f32_e32 v220, 1.0, v220\n\t"
      "v_add_f32_e32 v221, 1.0, v221\n\t"
      "v_mfma_f32_16x16x32_bf16 v[188:191], v[68:71], v[144:147], v[188:191]\n\t"
      "v_add_f32_e32 v222, 1.0, v222\n\t"
      "v_add_f32_e32 v223, 1.0, v223\n\t"
      "v_mfma_f32_16x16x32_bf16 v[188:191], v[84:87], v[148:151], v[188:191]\n\t"
      "v_rcp_f32_e32 v220, v220\n\t"
      "v_mfma_f32_16x16x32_bf16 v[192:195], v[72:75], v[144:147], v[192:195]\n\t"
      "v_rcp_f32_e32 v221, v221\n\t"
      "v_mfma_f32_16x16x32_bf16 v[192:195], v[88:91], v[148:151], v[192:195]\n\t"
      "v_rcp_f32_e32 v222, v222\n\t"
      "v_mfma_f32_16x16x32_bf16 v[196:199], v[76:79], v[144:147], v[196:199]\n\t"
      "v_rcp_f32_e32 v223, v223\n\t"
      "v_mfma_f32_16x16x32_bf16 v[196:199], v[92:95], v[148:151], v[196:199]\n\t"
      "v_mul_f32_e32 v216, v216, v220\n\t"
      "s_waitcnt vmcnt(1)\n\t"
      "v_mfma_f32_16x16x32_bf16 v[184:187], v[96:99], v[152:155], v[184:187]\n\t"
      "v_mul_f32_e32 v217, v217, v221\n\t"
      "s_waitcnt vmcnt(0)\n\t"
      "v_mfma_f32_16x16x32_bf16 v[184:187], v[112:115], v[156:159], v[184:187]\n\t"
      "v_mul_f32_e32 v218, v218, v222\n\t"
      "v_mul_f32_e32 v219, v219, v223\n\t"
      "v_mfma_f32_16x16x32_bf16 v[188:191], v[100:103], v[152:155], v[188:191]\n\t"
      "v_cvt_pk_bf16_f32 v220, v216, v217\n\t"
      "v_cvt_pk_bf16_f32 v221, v218, v219\n\t"
      "v_mfma_f32_16x16x32_bf16 v[188:191], v[116:119], v[156:159], v[188:191]\n\t"
      "s_nop 1\n\t"
      "v_cmp_u_f32_e32 vcc, v184, v184\n\t"
      "v_min_u32_e32 v220, -2, v220\n\t"
      "v_mfma_f32_16x16x32_bf16 v[192:195], v[104:107], v[152:155], v[192:195]\n\t"
      "v_min_u32_e32 v221, -2, v221\n\t"
      "global_store_dwordx2 v245, v[220:221], s[8:9] sc1\n\t"
      "s_add_u32 s8, s8, 131072\n\t"
      "s_addc_u32 s9, s9, 0\n\t"
      "v_mfma_f32_16x16x32_bf16 v[192:195], v[120:123], v[156:159], v[192:195]\n\t"
      "s_cbranch_vccnz NP6R_%=\n\t"
      "NP6B_%=:\n\t"
      "ds_write_b128 v243, v[184:187]\n\t"
      "v_mfma_f32_16x16x32_bf16 v[196:199], v[108:111], v[152:155], v[196:199]\n\t"
      "v_mfma_f32_16x16x32_bf16 v[196:199], v[124:127], v[156:159], v[196:199]\n\t"
      "ds_write_b128 v164, v[188:191]\n\t"
      "s_nop 2\n\t"
      "ds_write_b128 v165, v[192:195]\n\t"
      "s_nop 2\n\t"
      "ds_write_b128 v166, v[196:199]\n\t"
      "s_waitcnt lgkmcnt(0)\n\t"
      "s_barrier\n\t"
      "ds_read_b128 v[216:219], v244\n\t"
      "ds_read_b128 v[220:223], v244 offset:4096\n\t"
      "ds_read_b128 v[224:227], v244 offset:8192\n\t"
      "ds_read_b128 v[228:231], v244 offset:12288\n\t"
      "ds_read_b128 v[184:187], v244 offset:16384\n\t"
      "ds_read_b128 v[188:191], v244 offset:20480\n\t"
      "ds_read_b128 v[192:195], v244 offset:24576\n\t"
      "ds_read_b128 v[196:199], v244 offset:28672\n\t"
      "s_waitcnt lgkmcnt(6)\n\t"
      "v_add_f32_e32 v216, v216, v220\n\t"
      "v_add_f32_e32 v217, v217, v221\n\t"
      "v_add_f32_e32 v218, v218, v222\n\t"
      "v_add_f32_e32 v219, v219, v223\n\t"
      "s_waitcnt lgkmcnt(4)\n\t"
      "v_add_f32_e32 v224, v224, v228\n\t"
      "v_add_f32_e32 v225, v225, v229\n\t"
      "v_add_f32_e32 v226, v226, v230\n\t"
      "v_add_f32_e32 v227, v227, v231\n\t"
      "s_waitcnt lgkmcnt(2)\n\t"
      "v_add_f32_e32 v184, v184, v188\n\t"
      "v_add_f32_e32 v185, v185, v189\n\t"
      "v_add_f32_e32 v186, v186, v190\n\t"
      "v_add_f32_e32 v187, v187, v191\n\t"
      "v_add_f32_e32 v216, v216, v224\n\t"
      "v_add_f32_e32 v217, v217, v225\n\t"
      "v_add_f32_e32 v218, v218, v226\n\t"
      "v_add_f32_e32 v219, v219, v227\n\t"
      "s_waitcnt lgkmcnt(0)\n\t"
      "v_add_f32_e32 v192, v192, v196\n\t"
      "v_add_f32_e32 v193, v193, v197\n\t"
      "v_add_f32_e32 v194, v194, v198\n\t"
      "v_add_f32_e32 v195, v195, v199\n\t"
      "v_add_f32_e32 v184, v184, v192\n\t"
      "v_add_f32_e32 v185, v185, v193\n\t"
      "v_add_f32_e32 v186, v186, v194\n\t"
      "v_add_f32_e32 v187, v187, v195\n\t"
      "v_add_f32_e32 v216, v216, v184\n\t"
      "v_add_f32_e32 v217, v217, v185\n\t"
      "v_add_f32_e32 v218, v218, v186\n\t"
      "v_add_f32_e32 v219, v219, v187\n\t"
      "v_mul_f32_e32 v220, v216, v216\n\t"
      "v_mul_f32_e32 v221, v217, v217\n\t"
      "v_mul_f32_e32 v222, v218, v218\n\t"
      "v_mul_f32_e64 v223, v219, v219\n\t"
      "v_fmaak_f32 v220, v235, v220, 0xc0135761\n\t"
      "v_fmaak_f32 v221, v235, v221, 0xc0135761\n\t"
      "v_fmaak_f32 v222, v235, v222, 0xc0135761\n\t"
      "v_fmaak_f32 v223, v235, v223, 0xc0135761\n\t"
      "v_mul_f32_e32 v220, v216, v220\n\t"
      "v_mul_f32_e32 v221, v217, v221\n\t"
      "v_mul_f32_e32 v222, v218, v222\n\t"
      "v_mul_f32_e32 v223, v219, v223\n\t"
      "v_exp_f32_e32 v220, v220\n\t"
      "v_exp_f32_e32 v221, v221\n\t"
      "v_exp_f32_e32 v222, v222\n\t"
      "v_exp_f32_e32 v223, v223\n\t"
      "v_add_f32_e32 v220, 1.0, v220\n\t"
      "v_add_f32_e32 v221, 1.0, v221\n\t"
      "v_add_f32_e32 v222, 1.0, v222\n\t"
      "v_add_f32_e32 v223, 1.0, v223\n\t"
      "v_rcp_f32_e32 v220, v220\n\t"
      "v_rcp_f32_e32 v221, v221\n\t"
      "v_rcp_f32_e32 v222, v222\n\t"
      "v_rcp_f32_e32 v223, v223\n\t"
      "v_mul_f32_e32 v216, v216, v220\n\t"
      "v_mul_f32_e32 v217, v217, v221\n\t"
      "v_mul_f32_e32 v218, v218, v222\n\t"
      "v_mul_f32_e32 v219, v219, v223\n\t"
      "v_cvt_pk_bf16_f32 v220, v216, v217\n\t"
      "v_cvt_pk_bf16_f32 v221, v218, v219\n\t"
      "v_min_u32_e32 v220, -2, v220\n\t"
      "v_min_u32_e32 v221, -2, v221\n\t"
      "s_cmp_eq_u32 s17, 0\n\t"
      "s_cbranch_scc0 HI6_%=\n\t"
      "s_mov_b64 exec, 0xf\n\t"
      "global_store_dwordx2 v247, v[232:233], s[12:13] sc1\n\t"
      "s_mov_b64 exec, -1\n\t"
      "HI6_%=:\n\t"
      "global_store_dwordx2 v245, v[220:221], s[8:9] sc1\n\t"
      "GX_%=:\n\t"
      "s_bitcmp1_b32 s16, 5\n\t"
      "s_cbranch_scc0 EP_%=\n\t"
      "v_mov_b32_e32 v0, s46\n\t"
      "v_mov_b32_e32 v1, s47\n\t"
      "v_mov_b32_e32 v2, 131072\n\t"
      "s_mov_b64 exec, 1\n\t"
      "global_store_dwordx2 v2, v[0:1], s[12:13] sc1\n\t"
      "s_mov_b64 exec, -1\n\t"
      "EP_%=:\n\t"
      "AEND_%=:\n\t"
      "s_endpgm\n\t"
      ".p2align 6\n\t"
      "GQ_%=:\n\t"
      ".LhopD2:\n\t"
      "s_cbranch_execz .LbackD\n\t"
      "s_waitcnt vmcnt(7)\n\t"
      "v_max3_u32 v234, v128, v129, v130\n\t"
      "v_max_u32_e32 v234, v234, v131\n\t"
      "v_cmp_eq_u32_e32 vcc, -1, v234\n\t"
      "s_cbranch_vccnz FQ0R_%=\n\t"
      "FQ0B_%=:\n\t"
      "v_mfma_f32_16x16x32_bf16 v[184:187], v[0:3], v[128:131], v[236:239]\n\t"
      "s_waitcnt vmcnt(6)\n\t"
      "v_max3_u32 v234, v132, v133, v134\n\t"
      "v_max_u32_e32 v234, v234, v135\n\t"
      "v_cmp_eq_u32_e32 vcc, -1, v234\n\t"
      "s_cbranch_vccnz FQ1R_%=\n\t"
      "FQ1B_%=:\n\t"
      "v_mfma_f32_16x16x32_bf16 v[184:187], v[16:19], v[132:135], v[184:187]\n\t"
      "v_mfma_f32_16x16x32_bf16 v[188:191], v[4:7], v[128:131], 0\n\t"
      "v_mfma_f32_16x16x32_bf16 v[188:191], v[20:23], v[132:135], v[188:191]\n\t"
      "v_mfma_f32_16x16x32_bf16 v[192:195], v[8:11], v[128:131], 0\n\t"
      "v_mfma_f32_16x16x32_bf16 v[192:195], v[24:27], v[132:135], v[192:195]\n\t"
      "v_mfma_f32_16x16x32_bf16 v[196:199], v[12:15], v[128:131], 0\n\t"
      "global_load_dwordx4 v[128:131], v176, s[54:55] sc1\n\t"
      "v_mfma_f32_16x16x32_bf16 v[196:199], v[28:31], v[132:135], v[196:199]\n\t"
      "global_load_dwordx4 v[132:135], v177, s[54:55] sc1\n\t"
      "s_waitcnt vmcnt(7)\n\t"
      "v_max3_u32 v234, v136, v137, v138\n\t"
      "v_max_u32_e32 v234, v234, v139\n\t"
      "v_cmp_eq_u32_e32 vcc, -1, v234\n\t"
      "s_cbranch_vccnz FQ2R_%=\n\t"
      "FQ2B_%=:\n\t"
      "v_mfma_f32_16x16x32_bf16 v[184:187], v[32:35], v[136:139], v[184:187]\n\t"
      "s_waitcnt vmcnt(6)\n\t"
      "v_max3_u32 v234, v140, v141, v142\n\t"
      "v_max_u32_e32 v234, v234, v143\n\t"
      "v_cmp_eq_u32_e32 vcc, -1, v234\n\t"
      "s_cbranch_vccnz FQ3R_%=\n\t"
      "FQ3B_%=:\n\t"
      "v_mfma_f32_16x16x32_bf16 v[184:187], v[48:51], v[140:143], v[184:187]\n\t"
      "v_mfma_f32_16x16x32_bf16 v[188:191], v[36:39], v[136:139], v[188:191]\n\t"
      "v_mfma_f32_16x16x32_bf16 v[188:191], v[52:55], v[140:143], v[188:191]\n\t"
      "v_mfma_f32_16x16x32_bf16 v[192:195], v[40:43], v[136:139], v[192:195]\n\t"
      "v_mfma_f32_16x16x32_bf16 v[192:195], v[56:59], v[140:143], v[192:195]\n\t"
      "v_mfma_f32_16x16x32_bf16 v[196:199], v[44:47], v[136:139], v[196:199]\n\t"
      "global_load_dwordx4 v[136:139], v178, s[54:55] sc1\n\t"
      "v_mfma_f32_16x16x32_bf16 v[196:199], v[60:63], v[140:143], v[196:199]\n\t"
      "global_load_dwordx4 v[140:143], v179, s[54:55] sc1\n\t"
      "s_waitcnt vmcnt(7)\n\t"
      "v_max3_u32 v234, v144, v145, v146\n\t"
      "v_max_u32_e32 v234, v234, v147\n\t"
      "v_cmp_eq_u32_e32 vcc, -1, v234\n\t"
      "s_cbranch_vccnz FQ4R_%=\n\t"
      "FQ4B_%=:\n\t"
      "v_mfma_f32_16x16x32_bf16 v[184:187], v[64:67], v[144:147], v[184:187]\n\t"
      "s_waitcnt vmcnt(6)\n\t"
      "v_max3_u32 v234, v148, v149, v150\n\t"
      "v_max_u32_e32 v234, v234, v151\n\t"
      "v_cmp_eq_u32_e32 vcc, -1, v234\n\t"
      "s_cbranch_vccnz FQ5R_%=\n\t"
      "FQ5B_%=:\n\t"
      "v_mfma_f32_16x16x32_bf16 v[184:187], v[80:83], v[148:151], v[184:187]\n\t"
      "v_mfma_f32_16x16x32_bf16 v[188:191], v[68:71], v[144:147], v[188:191]\n\t"
      "v_mfma_f32_16x16x32_bf16 v[188:191], v[84:87], v[148:151], v[188:191]\n\t"
      "v_mfma_f32_16x16x32_bf16 v[192:195], v[72:75], v[144:147], v[192:195]\n\t"
      "v_mfma_f32_16x16x32_bf16 v[192:195], v[88:91], v[148:151], v[192:195]\n\t"
      "v_mfma_f32_16x16x32_bf16 v[196:199], v[76:79], v[144:147], v[196:199]\n\t"
      "global_load_dwordx4 v[144:147], v180, s[54:55] sc1\n\t"
      "v_mfma_f32_16x16x32_bf16 v[196:199], v[92:95], v[148:151], v[196:199]\n\t"
      "global_load_dwordx4 v[148:151], v181, s[54:55] sc1\n\t"
      "s_waitcnt vmcnt(7)\n\t"
      "v_max3_u32 v234, v152, v153, v154\n\t"
      "v_max_u32_e32 v234, v234, v155\n\t"
      "v_cmp_eq_u32_e32 vcc, -1, v234\n\t"
      "s_cbranch_vccnz FQ6R_%=\n\t"
      "FQ6B_%=:\n\t"
      "v_mfma_f32_16x16x32_bf16 v[184:187], v[96:99], v[152:155], v[184:187]\n\t"
      "s_waitcnt vmcnt(6)\n\t"
      "v_max3_u32 v234, v156, v157, v158\n\t"
      "v_max_u32_e32 v234, v234, v159\n\t"
      "v_cmp_eq_u32_e32 vcc, -1, v234\n\t"
      "s_cbranch_vccnz FQ7R_%=\n\t"
      "FQ7B_%=:\n\t"
      "v_mfma_f32_16x16x32_bf16 v[184:187], v[112:115], v[156:159], v[184:187]\n\t"
      "v_mfma_f32_16x16x32_bf16 v[188:191], v[100:103], v[152:155], v[188:191]\n\t"
      "v_mfma_f32_16x16x32_bf16 v[188:191], v[116:119], v[156:159], v[188:191]\n\t"
      "v_mfma_f32_16x16x32_bf16 v[192:195], v[104:107], v[152:155], v[192:195]\n\t"
      "v_mfma_f32_16x16x32_bf16 v[192:195], v[120:123], v[156:159], v[192:195]\n\t"
      "v_mfma_f32_16x16x32_bf16 v[196:199], v[108:111], v[152:155], v[196:199]\n\t"
      "global_load_dwordx4 v[152:155], v182, s[54:55] sc1\n\t"
      "v_mfma_f32_16x16x32_bf16 v[196:199], v[124:127], v[156:159], v[196:199]\n\t"
      "global_load_dwordx4 v[156:159], v183, s[54:55] sc1\n\t"
      "ds_write_b128 v243, v[184:187]\n\t"
      "s_nop 0\n\t"
      "ds_write_b128 v164, v[188:191]\n\t"
      "s_nop 0\n\t"
      "ds_write_b128 v165, v[192:195]\n\t"
      "s_nop 1\n\t"
      "ds_write_b128 v166, v[196:199]\n\t"
      "s_waitcnt vmcnt(7)\n\t"
      "v_mfma_f32_16x16x32_bf16 v[200:203], v[0:3], v[128:131], v[236:239]\n\t"
      "s_waitcnt vmcnt(6)\n\t"
      "v_mfma_f32_16x16x32_bf16 v[200:203], v[16:19], v[132:135], v[200:203]\n\t"
      "v_mfma_f32_16x16x32_bf16 v[204:207], v[4:7], v[128:131], 0\n\t"
      "v_mfma_f32_16x16x32_bf16 v[204:207], v[20:23], v[132:135], v[204:207]\n\t"
      "v_mfma_f32_16x16x32_bf16 v[208:211], v[8:11], v[128:131], 0\n\t"
      "v_mfma_f32_16x16x32_bf16 v[208:211], v[24:27], v[132:135], v[208:211]\n\t"
      "v_mfma_f32_16x16x32_bf16 v[212:215], v[12:15], v[128:131], 0\n\t"
      "global_load_dwordx4 v[128:131], v176, s[56:57] sc1\n\t"
      "v_mfma_f32_16x16x32_bf16 v[212:215], v[28:31], v[132:135], v[212:215]\n\t"
      "global_load_dwordx4 v[132:135], v177, s[56:57] sc1\n\t"
      "s_waitcnt vmcnt(7)\n\t"
      "v_mfma_f32_16x16x32_bf16 v[200:203], v[32:35], v[136:139], v[200:203]\n\t"
      "s_waitcnt vmcnt(6)\n\t"
      "v_mfma_f32_16x16x32_bf16 v[200:203], v[48:51], v[140:143], v[200:203]\n\t"
      "v_mfma_f32_16x16x32_bf16 v[204:207], v[36:39], v[136:139], v[204:207]\n\t"
      "v_mfma_f32_16x16x32_bf16 v[204:207], v[52:55], v[140:143], v[204:207]\n\t"
      "v_mfma_f32_16x16x32_bf16 v[208:211], v[40:43], v[136:139], v[208:211]\n\t"
      "v_mfma_f32_16x16x32_bf16 v[208:211], v[56:59], v[140:143], v[208:211]\n\t"
      "v_mfma_f32_16x16x32_bf16 v[212:215], v[44:47], v[136:139], v[212:215]\n\t"
      "global_load_dwordx4 v[136:139], v178, s[56:57] sc1\n\t"
      "v_mfma_f32_16x16x32_bf16 v[212:215], v[60:63], v[140:143], v[212:215]\n\t"
      "global_load_dwordx4 v[140:143], v179, s[56:57] sc1\n\t"
      "s_waitcnt vmcnt(7)\n\t"
      "v_mfma_f32_16x16x32_bf16 v[200:203], v[64:67], v[144:147], v[200:203]\n\t"
      "s_waitcnt vmcnt(6)\n\t"
      "v_mfma_f32_16x16x32_bf16 v[200:203], v[80:83], v[148:151], v[200:203]\n\t"
      "v_mfma_f32_16x16x32_bf16 v[204:207], v[68:71], v[144:147], v[204:207]\n\t"
      "v_mfma_f32_16x16x32_bf16 v[204:207], v[84:87], v[148:151], v[204:207]\n\t"
      "v_mfma_f32_16x16x32_bf16 v[208:211], v[72:75], v[144:147], v[208:211]\n\t"
      "v_mfma_f32_16x16x32_bf16 v[208:211], v[88:91], v[148:151], v[208:211]\n\t"
      "s_waitcnt lgkmcnt(0)\n\t"
      "s_barrier\n\t"
      "s_setprio 2\n\t"
      "v_mfma_f32_16x16x32_bf16 v[212:215], v[76:79], v[144:147], v[212:215]\n\t"
      "global_load_dwordx4 v[144:147], v180, s[56:57] sc1\n\t"
      "v_mfma_f32_16x16x32_bf16 v[212:215], v[92:95], v[148:151], v[212:215]\n\t"
      "global_load_dwordx4 v[148:151], v181, s[56:57] sc1\n\t"
      "s_waitcnt vmcnt(7)\n\t"
      "v_mfma_f32_16x16x32_bf16 v[200:203], v[96:99], v[152:155], v[200:203]\n\t"
      "s_waitcnt vmcnt(6)\n\t"
      "v_mfma_f32_16x16x32_bf16 v[200:203], v[112:115], v[156:159], v[200:203]\n\t"
      "v_mfma_f32_16x16x32_bf16 v[204:207], v[100:103], v[152:155], v[204:207]\n\t"
      "v_mfma_f32_16x16x32_bf16 v[204:207], v[116:119], v[156:159], v[204:207]\n\t"
      "v_mfma_f32_16x16x32_bf16 v[208:211], v[104:107], v[152:155], v[208:211]\n\t"
      "v_mfma_f32_16x16x32_bf16 v[208:211], v[120:123], v[156:159], v[208:211]\n\t"
      "v_mfma_f32_16x16x32_bf16 v[212:215], v[108:111], v[152:155], v[212:215]\n\t"
      "global_load_dwordx4 v[152:155], v182, s[56:57] sc1\n\t"
      "v_mfma_f32_16x16x32_bf16 v[212:215], v[124:127], v[156:159], v[212:215]\n\t"
      "global_load_dwordx4 v[156:159], v183, s[56:57] sc1\n\t"
      "s_setprio 0\n\t"
      "v_cmp_u_f32_e32 vcc, v200, v200\n\t"
      "s_cbranch_vccnz NQ1R_%=\n\t"
      "NQ1B_%=:\n\t"
      "ds_write_b128 v243, v[200:203] offset:32768\n\t"
      "ds_write_b128 v164, v[204:207] offset:32768\n\t"
      "ds_write_b128 v165, v[208:211] offset:32768\n\t"
      "s_nop 0\n\t"
      "ds_write_b128 v166, v[212:215] offset:32768\n\t"
      "s_waitcnt vmcnt(7)\n\t"
      "v_mfma_f32_16x16x32_bf16 v[184:187], v[0:3], v[128:131], v[236:239]\n\t"
      "s_waitcnt vmcnt(6)\n\t"
      "v_mfma_f32_16x16x32_bf16 v[184:187], v[16:19], v[132:135], v[184:187]\n\t"
      "v_mfma_f32_16x16x32_bf16 v[188:191], v[4:7], v[128:131], 0\n\t"
      "v_mfma_f32_16x16x32_bf16 v[188:191], v[20:23], v[132:135], v[188:191]\n\t"
      "v_mfma_f32_16x16x32_bf16 v[192:195], v[8:11], v[128:131], 0\n\t"
      "v_mfma_f32_16x16x32_bf16 v[192:195], v[24:27], v[132:135], v[192:195]\n\t"
      "v_mfma_f32_16x16x32_bf16 v[196:199], v[12:15], v[128:131], 0\n\t"
      "global_load_dwordx4 v[128:131], v176, s[58:59] sc1\n\t"
      "v_mfma_f32_16x16x32_bf16 v[196:199], v[28:31], v[132:135], v[196:199]\n\t"
      "global_load_dwordx4 v[132:135], v177, s[58:59] sc1\n\t"
      "s_waitcnt vmcnt(7)\n\t"
      "v_mfma_f32_16x16x32_bf16 v[184:187], v[32:35], v[136:139], v[184:187]\n\t"
      "s_waitcnt vmcnt(6)\n\t"
      "v_mfma_f32_16x16x32_bf16 v[184:187], v[48:51], v[140:143], v[184:187]\n\t"
      "v_mfma_f32_16x16x32_bf16 v[188:191], v[36:39], v[136:139], v[188:191]\n\t"
      "v_mfma_f32_16x16x32_bf16 v[188:191], v[52:55], v[140:143], v[188:191]\n\t"
      "v_mfma_f32_16x16x32_bf16 v[192:195], v[40:43], v[136:139], v[192:195]\n\t"
      "v_mfma_f32_16x16x32_bf16 v[192:195], v[56:59], v[140:143], v[192:195]\n\t"
      "v_mfma_f32_16x16x32_bf16 v[196:199], v[44:47], v[136:139], v[196:199]\n\t"
      "global_load_dwordx4 v[136:139], v178, s[58:59] sc1\n\t"
      "v_mfma_f32_16x16x32_bf16 v[196:199], v[60:63], v[140:143], v[196:199]\n\t"
      "global_load_dwordx4 v[140:143], v179, s[58:59] sc1\n\t"
      "s_waitcnt vmcnt(7)\n\t"
      "v_mfma_f32_16x16x32_bf16 v[184:187], v[64:67], v[144:147], v[184:187]\n\t"
      "s_waitcnt vmcnt(6)\n\t"
      "v_mfma_f32_16x16x32_bf16 v[184:187], v[80:83], v[148:151], v[184:187]\n\t"
      "v_mfma_f32_16x16x32_bf16 v[188:191], v[68:71], v[144:147], v[188:191]\n\t"
      "v_mfma_f32_16x16x32_bf16 v[188:191], v[84:87], v[148:151], v[188:191]\n\t"
      "v_mfma_f32_16x16x32_bf16 v[192:195], v[72:75], v[144:147], v[192:195]\n\t"
      "v_mfma_f32_16x16x32_bf16 v[192:195], v[88:91], v[148:151], v[192:195]\n\t"
      "s_waitcnt lgkmcnt(0)\n\t"
      "s_barrier\n\t"
      "s_setprio 2\n\t"
      "v_mfma_f32_16x16x32_bf16 v[196:199], v[76:79], v[144:147], v[196:199]\n\t"
      "global_load_dwordx4 v[144:147], v180, s[58:59] sc1\n\t"
      "v_mfma_f32_16x16x32_bf16 v[196:199], v[92:95], v[148:151], v[196:199]\n\t"
      "global_load_dwordx4 v[148:151], v181, s[58:59] sc1\n\t"
      "s_waitcnt vmcnt(7)\n\t"
      "v_mfma_f32_16x16x32_bf16 v[184:187], v[96:99], v[152:155], v[184:187]\n\t"
      "s_waitcnt vmcnt(6)\n\t"
      "v_mfma_f32_16x16x32_bf16 v[184:187], v[112:115], v[156:159], v[184:187]\n\t"
      "v_mfma_f32_16x16x32_bf16 v[188:191], v[100:103], v[152:155], v[188:191]\n\t"
      "v_mfma_f32_16x16x32_bf16 v[188:191], v[116:119], v[156:159], v[188:191]\n\t"
      "v_mfma_f32_16x16x32_bf16 v[192:195], v[104:107], v[152:155], v[192:195]\n\t"
      "v_mfma_f32_16x16x32_bf16 v[192:195], v[120:123], v[156:159], v[192:195]\n\t"
      "v_mfma_f32_16x16x32_bf16 v[196:199], v[108:111], v[152:155], v[196:199]\n\t"
      "global_load_dwordx4 v[152:155], v182, s[58:59] sc1\n\t"
      "v_mfma_f32_16x16x32_bf16 v[196:199], v[124:127], v[156:159], v[196:199]\n\t"
      "global_load_dwordx4 v[156:159], v183, s[58:59] sc1\n\t"
      "s_setprio 0\n\t"
      "v_cmp_u_f32_e32 vcc, v184, v184\n\t"
      "s_cbranch_vccnz NQ2R_%=\n\t"
      "NQ2B_%=:\n\t"
      "ds_write_b128 v243, v[184:187]\n\t"
      "ds_write_b128 v164, v[188:191]\n\t"
      "ds_write_b128 v165, v[192:195]\n\t"
      "s_nop 0\n\t"
      "ds_write_b128 v166, v[196:199]\n\t"
      "s_waitcnt vmcnt(7)\n\t"
      "v_mfma_f32_16x16x32_bf16 v[200:203], v[0:3], v[128:131], v[236:239]\n\t"
      "s_waitcnt vmcnt(6)\n\t"
      "v_mfma_f32_16x16x32_bf16 v[200:203], v[16:19], v[132:135], v[200:203]\n\t"
      "v_mfma_f32_16x16x32_bf16 v[204:207], v[4:7], v[128:131], 0\n\t"
      "v_mfma_f32_16x16x32_bf16 v[204:207], v[20:23], v[132:135], v[204:207]\n\t"
      "v_mfma_f32_16x16x32_bf16 v[208:211], v[8:11], v[128:131], 0\n\t"
      "v_mfma_f32_16x16x32_bf16 v[208:211], v[24:27], v[132:135], v[208:211]\n\t"
      "v_mfma_f32_16x16x32_bf16 v[212:215], v[12:15], v[128:131], 0\n\t"
      "global_load_dwordx4 v[128:131], v176, s[60:61] sc1\n\t"
      "v_mfma_f32_16x16x32_bf16 v[212:215], v[28:31], v[132:135], v[212:215]\n\t"
      "global_load_dwordx4 v[132:135], v177, s[60:61] sc1\n\t"
      "s_waitcnt vmcnt(7)\n\t"
      "v_mfma_f32_16x16x32_bf16 v[200:203], v[32:35], v[136:139], v[200:203]\n\t"
      "s_waitcnt vmcnt(6)\n\t"
      "v_mfma_f32_16x16x32_bf16 v[200:203], v[48:51], v[140:143], v[200:203]\n\t"
      "v_mfma_f32_16x16x32_bf16 v[204:207], v[36:39], v[136:139], v[204:207]\n\t"
      "v_mfma_f32_16x16x32_bf16 v[204:207], v[52:55], v[140:143], v[204:207]\n\t"
      "v_mfma_f32_16x16x32_bf16 v[208:211], v[40:43], v[136:139], v[208:211]\n\t"
      "v_mfma_f32_16x16x32_bf16 v[208:211], v[56:59], v[140:143], v[208:211]\n\t"
      "v_mfma_f32_16x16x32_bf16 v[212:215], v[44:47], v[136:139], v[212:215]\n\t"
      "global_load_dwordx4 v[136:139], v178, s[60:61] sc1\n\t"
      "v_mfma_f32_16x16x32_bf16 v[212:215], v[60:63], v[140:143], v[212:215]\n\t"
      "global_load_dwordx4 v[140:143], v179, s[60:61] sc1\n\t"
      "s_waitcnt vmcnt(7)\n\t"
      "v_mfma_f32_16x16x32_bf16 v[200:203], v[64:67], v[144:147], v[200:203]\n\t"
      "s_waitcnt vmcnt(6)\n\t"
      "v_mfma_f32_16x16x32_bf16 v[200:203], v[80:83], v[148:151], v[200:203]\n\t"
      "v_mfma_f32_16x16x32_bf16 v[204:207], v[68:71], v[144:147], v[204:207]\n\t"
      "v_mfma_f32_16x16x32_bf16 v[204:207], v[84:87], v[148:151], v[204:207]\n\t"
      "v_mfma_f32_16x16x32_bf16 v[208:211], v[72:75], v[144:147], v[208:211]\n\t"
      "v_mfma_f32_16x16x32_bf16 v[208:211], v[88:91], v[148:151], v[208:211]\n\t"
      "s_waitcnt lgkmcnt(0)\n\t"
      "s_barrier\n\t"
      "s_setprio 2\n\t"
      "v_mfma_f32_16x16x32_bf16 v[212:215], v[76:79], v[144:147], v[212:215]\n\t"
      "global_load_dwordx4 v[144:147], v180, s[60:61] sc1\n\t"
      "v_mfma_f32_16x16x32_bf16 v[212:215], v[92:95], v[148:151], v[212:215]\n\t"
      "global_load_dwordx4 v[148:151], v181, s[60:61] sc1\n\t"
      "s_waitcnt vmcnt(7)\n\t"
      ".LhopD3:\n\t"
      "s_cbranch_execz .LbackD\n\t"
      "v_mfma_f32_16x16x32_bf16 v[200:203], v[96:99], v[152:155], v[200:203]\n\t"
      "s_waitcnt vmcnt(6)\n\t"
      "v_mfma_f32_16x16x32_bf16 v[200:203], v[112:115], v[156:159], v[200:203]\n\t"
      "v_mfma_f32_16x16x32_bf16 v[204:207], v[100:103], v[152:155], v[204:207]\n\t"
      "v_mfma_f32_16x16x32_bf16 v[204:207], v[116:119], v[156:159], v[204:207]\n\t"
      "v_mfma_f32_16x16x32_bf16 v[208:211], v[104:107], v[152:155], v[208:211]\n\t"
      "v_mfma_f32_16x16x32_bf16 v[208:211], v[120:123], v[156:159], v[208:211]\n\t"
      "v_mfma_f32_16x16x32_bf16 v[212:215], v[108:111], v[152:155], v[212:215]\n\t"
      ".LhopD4:\n\t"
      "s_cbranch_execz .LbackD\n\t"
      "global_load_dwordx4 v[152:155], v182, s[60:61] sc1\n\t"
      "v_mfma_f32_16x16x32_bf16 v[212:215], v[124:127], v[156:159], v[212:215]\n\t"
      "global_load_dwordx4 v[156:159], v183, s[60:61] sc1\n\t"
      "s_setprio 0\n\t"
      "v_cmp_u_f32_e32 vcc, v200, v200\n\t"
      "s_cbranch_vccnz NQ3R_%=\n\t"
      "NQ3B_%=:\n\t"
      "ds_write_b128 v243, v[200:203] offset:32768\n\t"
      "ds_write_b128 v164, v[204:207] offset:32768\n\t"
      "ds_write_b128 v165, v[208:211] offset:32768\n\t"
      "s_nop 0\n\t"
      "ds_write_b128 v166, v[212:215] offset:32768\n\t"
      "s_waitcnt vmcnt(7)\n\t"
      "v_mfma_f32_16x16x32_bf16 v[184:187], v[0:3], v[128:131], v[236:239]\n\t"
      "s_waitcnt vmcnt(6)\n\t"
      "v_mfma_f32_16x16x32_bf16 v[184:187], v[16:19], v[132:135], v[184:187]\n\t"
      "v_mfma_f32_16x16x32_bf16 v[188:191], v[4:7], v[128:131], 0\n\t"
      "v_mfma_f32_16x16x32_bf16 v[188:191], v[20:23], v[132:135], v[188:191]\n\t"
      "v_mfma_f32_16x16x32_bf16 v[192:195], v[8:11], v[128:131], 0\n\t"
      "v_mfma_f32_16x16x32_bf16 v[192:195], v[24:27], v[132:135], v[192:195]\n\t"
      "v_mfma_f32_16x16x32_bf16 v[196:199], v[12:15], v[128:131], 0\n\t"
      "global_load_dwordx4 v[128:131], v176, s[62:63] sc1\n\t"
      "v_mfma_f32_16x16x32_bf16 v[196:199], v[28:31], v[132:135], v[196:199]\n\t"
      "global_load_dwordx4 v[132:135], v177, s[62:63] sc1\n\t"
      "s_waitcnt vmcnt(7)\n\t"
      "v_mfma_f32_16x16x32_bf16 v[184:187], v[32:35], v[136:139], v[184:187]\n\t"
      "s_waitcnt vmcnt(6)\n\t"
      "v_mfma_f32_16x16x32_bf16 v[184:187], v[48:51], v[140:143], v[184:187]\n\t"
      "v_mfma_f32_16x16x32_bf16 v[188:191], v[36:39], v[136:139], v[188:191]\n\t"
      "v_mfma_f32_16x16x32_bf16 v[188:191], v[52:55], v[140:143], v[188:191]\n\t"
      "v_mfma_f32_16x16x32_bf16 v[192:195], v[40:43], v[136:139], v[192:195]\n\t"
      "v_mfma_f32_16x16x32_bf16 v[192:195], v[56:59], v[140:143], v[192:195]\n\t"
      "v_mfma_f32_16x16x32_bf16 v[196:199], v[44:47], v[136:139], v[196:199]\n\t"
      "global_load_dwordx4 v[136:139], v178, s[62:63] sc1\n\t"
      "v_mfma_f32_16x16x32_bf16 v[196:199], v[60:63], v[140:143], v[196:199]\n\t"
      "global_load_dwordx4 v[140:143], v179, s[62:63] sc1\n\t"
      "s_waitcnt vmcnt(7)\n\t"
      "v_mfma_f32_16x16x32_bf16 v[184:187], v[64:67], v[144:147], v[184:187]\n\t"
      "s_waitcnt vmcnt(6)\n\t"
      "v_mfma_f32_16x16x32_bf16 v[184:187], v[80:83], v[148:151], v[184:187]\n\t"
      "v_mfma_f32_16x16x32_bf16 v[188:191], v[68:71], v[144:147], v[188:191]\n\t"
      "v_mfma_f32_16x16x32_bf16 v[188:191], v[84:87], v[148:151], v[188:191]\n\t"
      "v_mfma_f32_16x16x32_bf16 v[192:195], v[72:75], v[144:147], v[192:195]\n\t"
      "v_mfma_f32_16x16x32_bf16 v[192:195], v[88:91], v[148:151], v[192:195]\n\t"
      "s_waitcnt lgkmcnt(0)\n\t"
      "s_barrier\n\t"
      "s_setprio 2\n\t"
      "v_mfma_f32_16x16x32_bf16 v[196:199], v[76:79], v[144:147], v[196:199]\n\t"
      "global_load_dwordx4 v[144:147], v180, s[62:63] sc1\n\t"
      "v_mfma_f32_16x16x32_bf16 v[196:199], v[92:95], v[148:151], v[196:199]\n\t"
      "global_load_dwordx4 v[148:151], v181, s[62:63] sc1\n\t"
      "s_waitcnt vmcnt(7)\n\t"
      "v_mfma_f32_16x16x32_bf16 v[184:187], v[96:99], v[152:155], v[184:187]\n\t"
      "s_waitcnt vmcnt(6)\n\t"
      "v_mfma_f32_16x16x32_bf16 v[184:187], v[112:115], v[156:159], v[184:187]\n\t"
      "v_mfma_f32_16x16x32_bf16 v[188:191], v[100:103], v[152:155], v[188:191]\n\t"
      "v_mfma_f32_16x16x32_bf16 v[188:191], v[116:119], v[156:159], v[188:191]\n\t"
      "v_mfma_f32_16x16x32_bf16 v[192:195], v[104:107], v[152:155], v[192:195]\n\t"
      "v_mfma_f32_16x16x32_bf16 v[192:195], v[120:123], v[156:159], v[192:195]\n\t"
      "v_mfma_f32_16x16x32_bf16 v[196:199], v[108:111], v[152:155], v[196:199]\n\t"
      "global_load_dwordx4 v[152:155], v182, s[62:63] sc1\n\t"
      "v_mfma_f32_16x16x32_bf16 v[196:199], v[124:127], v[156:159], v[196:199]\n\t"
      "global_load_dwordx4 v[156:159], v183, s[62:63] sc1\n\t"
      "s_setprio 0\n\t"
      "v_cmp_u_f32_e32 vcc, v184, v184\n\t"
      "s_cbranch_vccnz NQ4R_%=\n\t"
      "NQ4B_%=:\n\t"
      "ds_write_b128 v243, v[184:187]\n\t"
      "ds_write_b128 v164, v[188:191]\n\t"
      "ds_write_b128 v165, v[192:195]\n\t"
      "s_nop 0\n\t"
      "ds_write_b128 v166, v[196:199]\n\t"
      "s_waitcnt vmcnt(7)\n\t"
      "v_mfma_f32_16x16x32_bf16 v[200:203], v[0:3], v[128:131], v[236:239]\n\t"
      "s_waitcnt vmcnt(6)\n\t"
      "v_mfma_f32_16x16x32_bf16 v[200:203], v[16:19], v[132:135], v[200:203]\n\t"
      "v_mfma_f32_16x16x32_bf16 v[204:207], v[4:7], v[128:131], 0\n\t"
      "v_mfma_f32_16x16x32_bf16 v[204:207], v[20:23], v[132:135], v[204:207]\n\t"
      "v_mfma_f32_16x16x32_bf16 v[208:211], v[8:11], v[128:131], 0\n\t"
      "v_mfma_f32_16x16x32_bf16 v[208:211], v[24:27], v[132:135], v[208:211]\n\t"
      "v_mfma_f32_16x16x32_bf16 v[212:215], v[12:15], v[128:131], 0\n\t"
      "global_load_dwordx4 v[128:131], v176, s[64:65] sc1\n\t"
      "v_mfma_f32_16x16x32_bf16 v[212:215], v[28:31], v[132:135], v[212:215]\n\t"
      "global_load_dwordx4 v[132:135], v177, s[64:65] sc1\n\t"
      "s_waitcnt vmcnt(7)\n\t"
      "v_mfma_f32_16x16x32_bf16 v[200:203], v[32:35], v[136:139], v[200:203]\n\t"
      "s_waitcnt vmcnt(6)\n\t"
      "v_mfma_f32_16x16x32_bf16 v[200:203], v[48:51], v[140:143], v[200:203]\n\t"
      "v_mfma_f32_16x16x32_bf16 v[204:207], v[36:39], v[136:139], v[204:207]\n\t"
      "v_mfma_f32_16x16x32_bf16 v[204:207], v[52:55], v[140:143], v[204:207]\n\t"
      "v_mfma_f32_16x16x32_bf16 v[208:211], v[40:43], v[136:139], v[208:211]\n\t"
      "v_mfma_f32_16x16x32_bf16 v[208:211], v[56:59], v[140:143], v[208:211]\n\t"
      "v_mfma_f32_16x16x32_bf16 v[212:215], v[44:47], v[136:139], v[212:215]\n\t"
      "global_load_dwordx4 v[136:139], v178, s[64:65] sc1\n\t"
      "v_mfma_f32_16x16x32_bf16 v[212:215], v[60:63], v[140:143], v[212:215]\n\t"
      "global_load_dwordx4 v[140:143], v179, s[64:65] sc1\n\t"
      "s_waitcnt vmcnt(7)\n\t"
      "v_mfma_f32_16x16x32_bf16 v[200:203], v[64:67], v[144:147], v[200:203]\n\t"
      "s_waitcnt vmcnt(6)\n\t"
      "v_mfma_f32_16x16x32_bf16 v[200:203], v[80:83], v[148:151], v[200:203]\n\t"
      "v_mfma_f32_16x16x32_bf16 v[204:207], v[68:71], v[144:147], v[204:207]\n\t"
      "v_mfma_f32_16x16x32_bf16 v[204:207], v[84:87], v[148:151], v[204:207]\n\t"
      "v_mfma_f32_16x16x32_bf16 v[208:211], v[72:75], v[144:147], v[208:211]\n\t"
      "v_mfma_f32_16x16x32_bf16 v[208:211], v[88:91], v[148:151], v[208:211]\n\t"
      "s_waitcnt lgkmcnt(0)\n\t"
      "s_barrier\n\t"
      "s_setprio 2\n\t"
      "v_mfma_f32_16x16x32_bf16 v[212:215], v[76:79], v[144:147], v[212:215]\n\t"
      "global_load_dwordx4 v[144:147], v180, s[64:65] sc1\n\t"
      "v_mfma_f32_16x16x32_bf16 v[212:215], v[92:95], v[148:151], v[212:215]\n\t"
      "global_load_dwordx4 v[148:151], v181, s[64:65] sc1\n\t"
      "s_waitcnt vmcnt(7)\n\t"
      "v_mfma_f32_16x16x32_bf16 v[200:203], v[96:99], v[152:155], v[200:203]\n\t"
      "s_waitcnt vmcnt(6)\n\t"
      "v_mfma_f32_16x16x32_bf16 v[200:203], v[112:115], v[156:159], v[200:203]\n\t"
      "v_mfma_f32_16x16x32_bf16 v[204:207], v[100:103], v[152:155], v[204:207]\n\t"
      "v_mfma_f32_16x16x32_bf16 v[204:207], v[116:119], v[156:159], v[204:207]\n\t"
      "v_mfma_f32_16x16x32_bf16 v[208:211], v[104:107], v[152:155], v[208:211]\n\t"
      "v_mfma_f32_16x16x32_bf16 v[208:211], v[120:123], v[156:159], v[208:211]\n\t"
      "v_mfma_f32_16x16x32_bf16 v[212:215], v[108:111], v[152:155], v[212:215]\n\t"
      "global_load_dwordx4 v[152:155], v182, s[64:65] sc1\n\t"
      "v_mfma_f32_16x16x32_bf16 v[212:215], v[124:127], v[156:159], v[212:215]\n\t"
      "global_load_dwordx4 v[156:159], v183, s[64:65] sc1\n\t"
      "s_setprio 0\n\t"
      "v_cmp_u_f32_e32 vcc, v200, v200\n\t"
      "s_cbranch_vccnz NQ5R_%=\n\t"
      "NQ5B_%=:\n\t"
      "ds_write_b128 v243, v[200:203] offset:32768\n\t"
      "ds_write_b128 v164, v[204:207] offset:32768\n\t"
      "ds_write_b128 v165, v[208:211] offset:32768\n\t"
      "s_nop 0\n\t"
      "ds_write_b128 v166, v[212:215] offset:32768\n\t"
      "s_waitcnt vmcnt(7)\n\t"
      "v_mfma_f32_16x16x32_bf16 v[184:187], v[0:3], v[128:131], v[236:239]\n\t"
      "s_waitcnt vmcnt(6)\n\t"
      "v_mfma_f32_16x16x32_bf16 v[184:187], v[16:19], v[132:135], v[184:187]\n\t"
      "v_mfma_f32_16x16x32_bf16 v[188:191], v[4:7], v[128:131], 0\n\t"
      "v_mfma_f32_16x16x32_bf16 v[188:191], v[20:23], v[132:135], v[188:191]\n\t"
      "v_mfma_f32_16x16x32_bf16 v[192:195], v[8:11], v[128:131], 0\n\t"
      "v_mfma_f32_16x16x32_bf16 v[192:195], v[24:27], v[132:135], v[192:195]\n\t"
      "v_mfma_f32_16x16x32_bf16 v[196:199], v[12:15], v[128:131], 0\n\t"
      "v_mfma_f32_16x16x32_bf16 v[196:199], v[28:31], v[132:135], v[196:199]\n\t"
      "s_waitcnt vmcnt(5)\n\t"
      "v_mfma_f32_16x16x32_bf16 v[184:187], v[32:35], v[136:139], v[184:187]\n\t"
      "s_waitcnt vmcnt(4)\n\t"
      "v_mfma_f32_16x16x32_bf16 v[184:187], v[48:51], v[140:143], v[184:187]\n\t"
      "v_mfma_f32_16x16x32_bf16 v[188:191], v[36:39], v[136:139], v[188:191]\n\t"
      "v_mfma_f32_16x16x32_bf16 v[188:191], v[52:55], v[140:143], v[188:191]\n\t"
      "v_mfma_f32_16x16x32_bf16 v[192:195], v[40:43], v[136:139], v[192:195]\n\t"
      "v_mfma_f32_16x16x32_bf16 v[192:195], v[56:59], v[140:143], v[192:195]\n\t"
      "v_mfma_f32_16x16x32_bf16 v[196:199], v[44:47], v[136:139], v[196:199]\n\t"
      "v_mfma_f32_16x16x32_bf16 v[196:199], v[60:63], v[140:143], v[196:199]\n\t"
      "s_waitcnt vmcnt(3)\n\t"
      "v_mfma_f32_16x16x32_bf16 v[184:187], v[64:67], v[144:147], v[184:187]\n\t"
      "s_waitcnt vmcnt(2)\n\t"
      "v_mfma_f32_16x16x32_bf16 v[184:187], v[80:83], v[148:151], v[184:187]\n\t"
      "v_mfma_f32_16x16x32_bf16 v[188:191], v[68:71], v[144:147], v[188:191]\n\t"
      "v_mfma_f32_16x16x32_bf16 v[188:191], v[84:87], v[148:151], v[188:191]\n\t"
      "v_mfma_f32_16x16x32_bf16 v[192:195], v[72:75], v[144:147], v[192:195]\n\t"
      "v_mfma_f32_16x16x32_bf16 v[192:195], v[88:91], v[148:151], v[192:195]\n\t"
      "s_waitcnt lgkmcnt(0)\n\t"
      "s_barrier\n\t"
      "s_setprio 2\n\t"
      "v_mfma_f32_16x16x32_bf16 v[196:199], v[76:79], v[144:147], v[196:199]\n\t"
      "v_mfma_f32_16x16x32_bf16 v[196:199], v[92:95], v[148:151], v[196:199]\n\t"
      "s_waitcnt vmcnt(1)\n\t"
      "v_mfma_f32_16x16x32_bf16 v[184:187], v[96:99], v[152:155], v[184:187]\n\t"
      "s_waitcnt vmcnt(0)\n\t"
      "v_mfma_f32_16x16x32_bf16 v[184:187], v[112:115], v[156:159], v[184:187]\n\t"
      "v_mfma_f32_16x16x32_bf16 v[188:191], v[100:103], v[152:155], v[188:191]\n\t"
      "v_mfma_f32_16x16x32_bf16 v[188:191], v[116:119], v[156:159], v[188:191]\n\t"
      "v_mfma_f32_16x16x32_bf16 v[192:195], v[104:107], v[152:155], v[192:195]\n\t"
      "v_mfma_f32_16x16x32_bf16 v[192:195], v[120:123], v[156:159], v[192:195]\n\t"
      "v_mfma_f32_16x16x32_bf16 v[196:199], v[108:111], v[152:155], v[196:199]\n\t"
      "v_mfma_f32_16x16x32_bf16 v[196:199], v[124:127], v[156:159], v[196:199]\n\t"
      "s_nop 1\n\t"
      "v_cmp_u_f32_e32 vcc, v184, v184\n\t"
      "s_cbranch_vccnz NQ6R_%=\n\t"
      "NQ6B_%=:\n\t"
      "ds_write_b128 v243, v[184:187]\n\t"
      "ds_write_b128 v164, v[188:191]\n\t"
      "ds_write_b128 v165, v[192:195]\n\t"
      "s_nop 0\n\t"
      "ds_write_b128 v166, v[196:199]\n\t"
      "s_waitcnt lgkmcnt(0)\n\t"
      "s_barrier\n\t"
      "s_branch GX_%=\n\t"
      "ASP_%=:\n\t"
      "s_mov_b32 m0, 163328\n\t"
      "s_cmp_ge_u32 s17, 65536\n\t"
      "s_cbranch_scc0 BOT_%=\n\t"
      "s_barrier\n\t"
      "BOT_%=:\n\t"
      "v_add_u32_e32 v1, 229376, v241\n\t"
      "global_load_lds_dword v241, s[4:5]\n\t"
      "global_load_lds_dword v1, s[4:5]\n\t"
      "s_waitcnt vmcnt(0)\n\t"
      "s_sleep 1\n\t"
      "s_getpc_b64 s[44:45]\n\t"
      "s_add_u32 s44, s44, _Z11fused_blockPKcS0_S0_S0_S0_S0_S0_PcS1_@rel32@lo+4\n\t"
      "s_addc_u32 s45, s45, _Z11fused_blockPKcS0_S0_S0_S0_S0_S0_PcS1_@rel32@hi+12\n\t"
      "s_lshr_b32 s46, s17, 8\n\t"
      "v_lshrrev_b32_e32 v0, 4, v240\n\t"
      "v_add_u32_e32 v0, s46, v0\n\t"
      "v_min_u32_e32 v0, 495, v0\n\t"
      "v_lshlrev_b32_e32 v0, 7, v0\n\t"
      "global_load_lds_dword v0, s[44:45] sc1\n\t"
      "s_branch AEND_%=\n\t"
      "FQ0R_%=:\n\t"
      "s_mov_b32 s51, 0\n\t"
      "FQ0L_%=:\n\t"
      "s_add_u32 s51, s51, 1\n\t"
      "s_cmp_lt_u32 s51, 262144\n\t"
      "s_cbranch_scc0 FQ0X_%=\n\t"
      "s_waitcnt vmcnt(0)\n\t"
      "s_sleep 1\n\t"
      "global_load_dwordx4 v[128:131], v176, s[52:53] sc1\n\t"
      "global_load_dwordx4 v[132:135], v177, s[52:53] sc1\n\t"
      "global_load_dwordx4 v[136:139], v178, s[52:53] sc1\n\t"
      "global_load_dwordx4 v[140:143], v179, s[52:53] sc1\n\t"
      "global_load_dwordx4 v[144:147], v180, s[52:53] sc1\n\t"
      "global_load_dwordx4 v[148:151], v181, s[52:53] sc1\n\t"
      "global_load_dwordx4 v[152:155], v182, s[52:53] sc1\n\t"
      "global_load_dwordx4 v[156:159], v183, s[52:53] sc1\n\t"
      "s_waitcnt vmcnt(7)\n\t"
      "v_max3_u32 v234, v128, v129, v130\n\t"
      "v_max_u32_e32 v234, v234, v131\n\t"
      "v_cmp_eq_u32_e32 vcc, -1, v234\n\t"
      "s_cbranch_vccnz FQ0L_%=\n\t"
      "FQ0X_%=:\n\t"
      "s_branch FQ0B_%=\n\t"
      "FQ1R_%=:\n\t"
      "s_mov_b32 s51, 0\n\t"
      "FQ1L_%=:\n\t"
      "s_add_u32 s51, s51, 1\n\t"
      "s_cmp_lt_u32 s51, 262144\n\t"
      "s_cbranch_scc0 FQ1X_%=\n\t"
      "s_waitcnt vmcnt(0)\n\t"
      "s_sleep 1\n\t"
      "global_load_dwordx4 v[132:135], v177, s[52:53] sc1\n\t"
      "global_load_dwordx4 v[136:139], v178, s[52:53] sc1\n\t"
      "global_load_dwordx4 v[140:143], v179, s[52:53] sc1\n\t"
      "global_load_dwordx4 v[144:147], v180, s[52:53] sc1\n\t"
      "global_load_dwordx4 v[148:151], v181, s[52:53] sc1\n\t"
      "global_load_dwordx4 v[152:155], v182, s[52:53] sc1\n\t"
      "global_load_dwordx4 v[156:159], v183, s[52:53] sc1\n\t"
      "s_waitcnt vmcnt(6)\n\t"
      "v_max3_u32 v234, v132, v133, v134\n\t"
      "v_max_u32_e32 v234, v234, v135\n\t"
      "v_cmp_eq_u32_e32 vcc, -1, v234\n\t"
      "s_cbranch_vccnz FQ1L_%=\n\t"
      "FQ1X_%=:\n\t"
      "s_branch FQ1B_%=\n\t"
      "FQ2R_%=:\n\t"
      "s_mov_b32 s51, 0\n\t"
      "FQ2L_%=:\n\t"
      "s_add_u32 s51, s51, 1\n\t"
      "s_cmp_lt_u32 s51, 262144\n\t"
      "s_cbranch_scc0 FQ2X_%=\n\t"
      "s_waitcnt vmcnt(0)\n\t"
      "s_sleep 1\n\t"
      "global_load_dwordx4 v[136:139], v178, s[52:53] sc1\n\t"
      "global_load_dwordx4 v[140:143], v179, s[52:53] sc1\n\t"
      "global_load_dwordx4 v[144:147], v180, s[52:53] sc1\n\t"
      "global_load_dwordx4 v[148:151], v181, s[52:53] sc1\n\t"
      "global_load_dwordx4 v[152:155], v182, s[52:53] sc1\n\t"
      "global_load_dwordx4 v[156:159], v183, s[52:53] sc1\n\t"
      "global_load_dwordx4 v[128:131], v176, s[54:55] sc1\n\t"
      "global_load_dwordx4 v[132:135], v177, s[54:55] sc1\n\t"
      "s_waitcnt vmcnt(7)\n\t"
      "v_max3_u32 v234, v136, v137, v138\n\t"
      "v_max_u32_e32 v234, v234, v139\n\t"
      "v_cmp_eq_u32_e32 vcc, -1, v234\n\t"
      "s_cbranch_vccnz FQ2L_%=\n\t"
      "FQ2X_%=:\n\t"
      "s_branch FQ2B_%=\n\t"
      "FQ3R_%=:\n\t"
      "s_mov_b32 s51, 0\n\t"
      "FQ3L_%=:\n\t"
      "s_add_u32 s51, s51, 1\n\t"
      "s_cmp_lt_u32 s51, 262144\n\t"
      "s_cbranch_scc0 FQ3X_%=\n\t"
      "s_waitcnt vmcnt(0)\n\t"
      "s_sleep 1\n\t"
      "global_load_dwordx4 v[140:143], v179, s[52:53] sc1\n\t"
      "global_load_dwordx4 v[144:147], v180, s[52:53] sc1\n\t"
      "global_load_dwordx4 v[148:151], v181, s[52:53] sc1\n\t"
      "global_load_dwordx4 v[152:155], v182, s[52:53] sc1\n\t"
      "global_load_dwordx4 v[156:159], v183, s[52:53] sc1\n\t"
      "global_load_dwordx4 v[128:131], v176, s[54:55] sc1\n\t"
      "global_load_dwordx4 v[132:135], v177, s[54:55] sc1\n\t"
      "s_waitcnt vmcnt(6)\n\t"
      "v_max3_u32 v234, v140, v141, v142\n\t"
      "v_max_u32_e32 v234, v234, v143\n\t"
      "v_cmp_eq_u32_e32 vcc, -1, v234\n\t"
      "s_cbranch_vccnz FQ3L_%=\n\t"
      "FQ3X_%=:\n\t"
      "s_branch FQ3B_%=\n\t"
      "FQ4R_%=:\n\t"
      "s_mov_b32 s51, 0\n\t"
      "FQ4L_%=:\n\t"
      "s_add_u32 s51, s51, 1\n\t"
      "s_cmp_lt_u32 s51, 262144\n\t"
      "s_cbranch_scc0 FQ4X_%=\n\t"
      "s_waitcnt vmcnt(0)\n\t"
      "s_sleep 1\n\t"
      "global_load_dwordx4 v[144:147], v180, s[52:53] sc1\n\t"
      "global_load_dwordx4 v[148:151], v181, s[52:53] sc1\n\t"
      "global_load_dwordx4 v[152:155], v182, s[52:53] sc1\n\t"
      "global_load_dwordx4 v[156:159], v183, s[52:53] sc1\n\t"
      "global_load_dwordx4 v[128:131], v176, s[54:55] sc1\n\t"
      "global_load_dwordx4 v[132:135], v177, s[54:55] sc1\n\t"
      "global_load_dwordx4 v[136:139], v178, s[54:55] sc1\n\t"
      "global_load_dwordx4 v[140:143], v179, s[54:55] sc1\n\t"
      "s_waitcnt vmcnt(7)\n\t"
      "v_max3_u32 v234, v144, v145, v146\n\t"
      "v_max_u32_e32 v234, v234, v147\n\t"
      "v_cmp_eq_u32_e32 vcc, -1, v234\n\t"
      "s_cbranch_vccnz FQ4L_%=\n\t"
      "FQ4X_%=:\n\t"
      "s_branch FQ4B_%=\n\t"
      "FQ5R_%=:\n\t"
      "s_mov_b32 s51, 0\n\t"
      "FQ5L_%=:\n\t"
      "s_add_u32 s51, s51, 1\n\t"
      "s_cmp_lt_u32 s51, 262144\n\t"
      "s_cbranch_scc0 FQ5X_%=\n\t"
      "s_waitcnt vmcnt(0)\n\t"
      "s_sleep 1\n\t"
      "global_load_dwordx4 v[148:151], v181, s[52:53] sc1\n\t"
      "global_load_dwordx4 v[152:155], v182, s[52:53] sc1\n\t"
      "global_load_dwordx4 v[156:159], v183, s[52:53] sc1\n\t"
      "global_load_dwordx4 v[128:131], v176, s[54:55] sc1\n\t"
      "global_load_dwordx4 v[132:135], v177, s[54:55] sc1\n\t"
      "global_load_dwordx4 v[136:139], v178, s[54:55] sc1\n\t"
      "global_load_dwordx4 v[140:143], v179, s[54:55] sc1\n\t"
      "s_waitcnt vmcnt(6)\n\t"
      "v_max3_u32 v234, v148, v149, v150\n\t"
      "v_max_u32_e32 v234, v234, v151\n\t"
      "v_cmp_eq_u32_e32 vcc, -1, v234\n\t"
      "s_cbranch_vccnz FQ5L_%=\n\t"
      "FQ5X_%=:\n\t"
      "s_branch FQ5B_%=\n\t"
      "FQ6R_%=:\n\t"
      "s_mov_b32 s51, 0\n\t"
      "FQ6L_%=:\n\t"
      "s_add_u32 s51, s51, 1\n\t"
      "s_cmp_lt_u32 s51, 262144\n\t"
      "s_cbranch_scc0 FQ6X_%=\n\t"
      "s_waitcnt vmcnt(0)\n\t"
      "s_sleep 1\n\t"
      "global_load_dwordx4 v[152:155], v182, s[52:53] sc1\n\t"
      "global_load_dwordx4 v[156:159], v183, s[52:53] sc1\n\t"
      "global_load_dwordx4 v[128:131], v176, s[54:55] sc1\n\t"
      "global_load_dwordx4 v[132:135], v177, s[54:55] sc1\n\t"
      "global_load_dwordx4 v[136:139], v178, s[54:55] sc1\n\t"
      "global_load_dwordx4 v[140:143], v179, s[54:55] sc1\n\t"
      "global_load_dwordx4 v[144:147], v180, s[54:55] sc1\n\t"
      "global_load_dwordx4 v[148:151], v181, s[54:55] sc1\n\t"
      "s_waitcnt vmcnt(7)\n\t"
      "v_max3_u32 v234, v152, v153, v154\n\t"
      "v_max_u32_e32 v234, v234, v155\n\t"
      "v_cmp_eq_u32_e32 vcc, -1, v234\n\t"
      "s_cbranch_vccnz FQ6L_%=\n\t"
      "FQ6X_%=:\n\t"
      "s_branch FQ6B_%=\n\t"
      "FQ7R_%=:\n\t"
      "s_mov_b32 s51, 0\n\t"
      "FQ7L_%=:\n\t"
      "s_add_u32 s51, s51, 1\n\t"
      "s_cmp_lt_u32 s51, 262144\n\t"
      "s_cbranch_scc0 FQ7X_%=\n\t"
      "s_waitcnt vmcnt(0)\n\t"
      "s_sleep 1\n\t"
      "global_load_dwordx4 v[156:159], v183, s[52:53] sc1\n\t"
      "global_load_dwordx4 v[128:131], v176, s[54:55] sc1\n\t"
      "global_load_dwordx4 v[132:135], v177, s[54:55] sc1\n\t"
      "global_load_dwordx4 v[136:139], v178, s[54:55] sc1\n\t"
      "global_load_dwordx4 v[140:143], v179, s[54:55] sc1\n\t"
      "global_load_dwordx4 v[144:147], v180, s[54:55] sc1\n\t"
      "global_load_dwordx4 v[148:151], v181, s[54:55] sc1\n\t"
      "s_waitcnt vmcnt(6)\n\t"
      "v_max3_u32 v234, v156, v157, v158\n\t"
      "v_max_u32_e32 v234, v234, v159\n\t"
      "v_cmp_eq_u32_e32 vcc, -1, v234\n\t"
      "s_cbranch_vccnz FQ7L_%=\n\t"
      "FQ7X_%=:\n\t"
      "s_branch FQ7B_%=\n\t"
      "NQ1R_%=:\n\t"
      "s_mov_b32 s51, 0\n\t"
      "NQ1L_%=:\n\t"
      "s_add_u32 s51, s51, 1\n\t"
      "s_cmp_lt_u32 s51, 262144\n\t"
      "s_cbranch_scc0 NQ1X_%=\n\t"
      "s_waitcnt vmcnt(0)\n\t"
      "s_sleep 1\n\t"
      "global_load_dwordx4 v[128:131], v176, s[54:55] sc1\n\t"
      "global_load_dwordx4 v[132:135], v177, s[54:55] sc1\n\t"
      "global_load_dwordx4 v[136:139], v178, s[54:55] sc1\n\t"
      "global_load_dwordx4 v[140:143], v179, s[54:55] sc1\n\t"
      "global_load_dwordx4 v[144:147], v180, s[54:55] sc1\n\t"
      "global_load_dwordx4 v[148:151], v181, s[54:55] sc1\n\t"
      "global_load_dwordx4 v[152:155], v182, s[54:55] sc1\n\t"
      "global_load_dwordx4 v[156:159], v183, s[54:55] sc1\n\t"
      "s_waitcnt vmcnt(0)\n\t"
      "v_max3_u32 v234, v128, v129, v130\n\t"
      "v_max3_u32 v234, v234, v131, v132\n\t"
      "v_max3_u32 v234, v234, v133, v134\n\t"
      "v_max3_u32 v234, v234, v135, v136\n\t"
      "v_max3_u32 v234, v234, v137, v138\n\t"
      "v_max3_u32 v234, v234, v139, v140\n\t"
      "v_max3_u32 v234, v234, v141, v142\n\t"
      "v_max3_u32 v234, v234, v143, v144\n\t"
      "v_max3_u32 v234, v234, v145, v146\n\t"
      "v_max3_u32 v234, v234, v147, v148\n\t"
      "v_max3_u32 v234, v234, v149, v150\n\t"
      "v_max3_u32 v234, v234, v151, v152\n\t"
      "v_max3_u32 v234, v234, v153, v154\n\t"
      "v_max3_u32 v234, v234, v155, v156\n\t"
      "v_max3_u32 v234, v234, v157, v158\n\t"
      "v_max_u32_e32 v234, v234, v159\n\t"
      "v_cmp_eq_u32_e32 vcc, -1, v234\n\t"
      "s_cbranch_vccnz NQ1L_%=\n\t"
      "NQ1X_%=:\n\t"
      "v_mfma_f32_16x16x32_bf16 v[200:203], v[0:3], v[128:131], v[236:239]\n\t"
      "v_mfma_f32_16x16x32_bf16 v[204:207], v[4:7], v[128:131], 0\n\t"
      "v_mfma_f32_16x16x32_bf16 v[208:211], v[8:11], v[128:131], 0\n\t"
      "v_mfma_f32_16x16x32_bf16 v[212:215], v[12:15], v[128:131], 0\n\t"
      "v_mfma_f32_16x16x32_bf16 v[200:203], v[16:19], v[132:135], v[200:203]\n\t"
      "v_mfma_f32_16x16x32_bf16 v[204:207], v[20:23], v[132:135], v[204:207]\n\t"
      "v_mfma_f32_16x16x32_bf16 v[208:211], v[24:27], v[132:135], v[208:211]\n\t"
      "v_mfma_f32_16x16x32_bf16 v[212:215], v[28:31], v[132:135], v[212:215]\n\t"
      "v_mfma_f32_16x16x32_bf16 v[200:203], v[32:35], v[136:139], v[200:203]\n\t"
      "v_mfma_f32_16x16x32_bf16 v[204:207], v[36:39], v[136:139], v[204:207]\n\t"
      "v_mfma_f32_16x16x32_bf16 v[208:211], v[40:43], v[136:139], v[208:211]\n\t"
      "v_mfma_f32_16x16x32_bf16 v[212:215], v[44:47], v[136:139], v[212:215]\n\t"
      "v_mfma_f32_16x16x32_bf16 v[200:203], v[48:51], v[140:143], v[200:203]\n\t"
      "v_mfma_f32_16x16x32_bf16 v[204:207], v[52:55], v[140:143], v[204:207]\n\t"
      "v_mfma_f32_16x16x32_bf16 v[208:211], v[56:59], v[140:143], v[208:211]\n\t"
      "v_mfma_f32_16x16x32_bf16 v[212:215], v[60:63], v[140:143], v[212:215]\n\t"
      "v_mfma_f32_16x16x32_bf16 v[200:203], v[64:67], v[144:147], v[200:203]\n\t"
      "v_mfma_f32_16x16x32_bf16 v[204:207], v[68:71], v[144:147], v[204:207]\n\t"
      "v_mfma_f32_16x16x32_bf16 v[208:211], v[72:75], v[144:147], v[208:211]\n\t"
      "v_mfma_f32_16x16x32_bf16 v[212:215], v[76:79], v[144:147], v[212:215]\n\t"
      "v_mfma_f32_16x16x32_bf16 v[200:203], v[80:83], v[148:151], v[200:203]\n\t"
      "v_mfma_f32_16x16x32_bf16 v[204:207], v[84:87], v[148:151], v[204:207]\n\t"
      "v_mfma_f32_16x16x32_bf16 v[208:211], v[88:91], v[148:151], v[208:211]\n\t"
      "v_mfma_f32_16x16x32_bf16 v[212:215], v[92:95], v[148:151], v[212:215]\n\t"
      "v_mfma_f32_16x16x32_bf16 v[200:203], v[96:99], v[152:155], v[200:203]\n\t"
      "v_mfma_f32_16x16x32_bf16 v[204:207], v[100:103], v[152:155], v[204:207]\n\t"
      "v_mfma_f32_16x16x32_bf16 v[208:211], v[104:107], v[152:155], v[208:211]\n\t"
      "v_mfma_f32_16x16x32_bf16 v[212:215], v[108:111], v[152:155], v[212:215]\n\t"
      "v_mfma_f32_16x16x32_bf16 v[200:203], v[112:115], v[156:159], v[200:203]\n\t"
      "v_mfma_f32_16x16x32_bf16 v[204:207], v[116:119], v[156:159], v[204:207]\n\t"
      "v_mfma_f32_16x16x32_bf16 v[208:211], v[120:123], v[156:159], v[208:211]\n\t"
      "v_mfma_f32_16x16x32_bf16 v[212:215], v[124:127], v[156:159], v[212:215]\n\t"
      "global_load_dwordx4 v[128:131], v176, s[56:57] sc1\n\t"
      "global_load_dwordx4 v[132:135], v177, s[56:57] sc1\n\t"
      "global_load_dwordx4 v[136:139], v178, s[56:57] sc1\n\t"
      "global_load_dwordx4 v[140:143], v179, s[56:57] sc1\n\t"
      "global_load_dwordx4 v[144:147], v180, s[56:57] sc1\n\t"
      "global_load_dwordx4 v[148:151], v181, s[56:57] sc1\n\t"
      "global_load_dwordx4 v[152:155], v182, s[56:57] sc1\n\t"
      "global_load_dwordx4 v[156:159], v183, s[56:57] sc1\n\t"
      "s_waitcnt vmcnt(0)\n\t"
      "s_nop 7\n\t"
      "s_branch NQ1B_%=\n\t"
      "NQ2R_%=:\n\t"
      "s_mov_b32 s51, 0\n\t"
      "NQ2L_%=:\n\t"
      "s_add_u32 s51, s51, 1\n\t"
      "s_cmp_lt_u32 s51, 262144\n\t"
      "s_cbranch_scc0 NQ2X_%=\n\t"
      "s_waitcnt vmcnt(0)\n\t"
      "s_sleep 1\n\t"
      "global_load_dwordx4 v[128:131], v176, s[56:57] sc1\n\t"
      "global_load_dwordx4 v[132:135], v177, s[56:57] sc1\n\t"
      "global_load_dwordx4 v[136:139], v178, s[56:57] sc1\n\t"
      "global_load_dwordx4 v[140:143], v179, s[56:57] sc1\n\t"
      "global_load_dwordx4 v[144:147], v180, s[56:57] sc1\n\t"
      "global_load_dwordx4 v[148:151], v181, s[56:57] sc1\n\t"
      "global_load_dwordx4 v[152:155], v182, s[56:57] sc1\n\t"
      "global_load_dwordx4 v[156:159], v183, s[56:57] sc1\n\t"
      "s_waitcnt vmcnt(0)\n\t"
      "v_max3_u32 v234, v128, v129, v130\n\t"
      "v_max3_u32 v234, v234, v131, v132\n\t"
      "v_max3_u32 v234, v234, v133, v134\n\t"
      "v_max3_u32 v234, v234, v135, v136\n\t"
      "v_max3_u32 v234, v234, v137, v138\n\t"
      "v_max3_u32 v234, v234, v139, v140\n\t"
      "v_max3_u32 v234, v234, v141, v142\n\t"
      "v_max3_u32 v234, v234, v143, v144\n\t"
      "v_max3_u32 v234, v234, v145, v146\n\t"
      "v_max3_u32 v234, v234, v147, v148\n\t"
      "v_max3_u32 v234, v234, v149, v150\n\t"
      "v_max3_u32 v234, v234, v151, v152\n\t"
      "v_max3_u32 v234, v234, v153, v154\n\t"
      "v_max3_u32 v234, v234, v155, v156\n\t"
      "v_max3_u32 v234, v234, v157, v158\n\t"
      "v_max_u32_e32 v234, v234, v159\n\t"
      "v_cmp_eq_u32_e32 vcc, -1, v234\n\t"
      "s_cbranch_vccnz NQ2L_%=\n\t"
      "NQ2X_%=:\n\t"
      "v_mfma_f32_16x16x32_bf16 v[184:187], v[0:3], v[128:131], v[236:239]\n\t"
      "v_mfma_f32_16x16x32_bf16 v[188:191], v[4:7], v[128:131], 0\n\t"
      "v_mfma_f32_16x16x32_bf16 v[192:195], v[8:11], v[128:131], 0\n\t"
      "v_mfma_f32_16x16x32_bf16 v[196:199], v[12:15], v[128:131], 0\n\t"
      "v_mfma_f32_16x16x32_bf16 v[184:187], v[16:19], v[132:135], v[184:187]\n\t"
      "v_mfma_f32_16x16x32_bf16 v[188:191], v[20:23], v[132:135], v[188:191]\n\t"
      "v_mfma_f32_16x16x32_bf16 v[192:195], v[24:27], v[132:135], v[192:195]\n\t"
      "v_mfma_f32_16x16x32_bf16 v[196:199], v[28:31], v[132:135], v[196:199]\n\t"
      "v_mfma_f32_16x16x32_bf16 v[184:187], v[32:35], v[136:139], v[184:187]\n\t"
      "v_mfma_f32_16x16x32_bf16 v[188:191], v[36:39], v[136:139], v[188:191]\n\t"
      "v_mfma_f32_16x16x32_bf16 v[192:195], v[40:43], v[136:139], v[192:195]\n\t"
      "v_mfma_f32_16x16x32_bf16 v[196:199], v[44:47], v[136:139], v[196:199]\n\t"
      "v_mfma_f32_16x16x32_bf16 v[184:187], v[48:51], v[140:143], v[184:187]\n\t"
      "v_mfma_f32_16x16x32_bf16 v[188:191], v[52:55], v[140:143], v[188:191]\n\t"
      "v_mfma_f32_16x16x32_bf16 v[192:195], v[56:59], v[140:143], v[192:195]\n\t"
      "v_mfma_f32_16x16x32_bf16 v[196:199], v[60:63], v[140:143], v[196:199]\n\t"
      "v_mfma_f32_16x16x32_bf16 v[184:187], v[64:67], v[144:147], v[184:187]\n\t"
      "v_mfma_f32_16x16x32_bf16 v[188:191], v[68:71], v[144:147], v[188:191]\n\t"
      "v_mfma_f32_16x16x32_bf16 v[192:195], v[72:75], v[144:147], v[192:195]\n\t"
      "v_mfma_f32_16x16x32_bf16 v[196:199], v[76:79], v[144:147], v[196:199]\n\t"
      "v_mfma_f32_16x16x32_bf16 v[184:187], v[80:83], v[148:151], v[184:187]\n\t"
      "v_mfma_f32_16x16x32_bf16 v[188:191], v[84:87], v[148:151], v[188:191]\n\t"
      "v_mfma_f32_16x16x32_bf16 v[192:195], v[88:91], v[148:151], v[192:195]\n\t"
      "v_mfma_f32_16x16x32_bf16 v[196:199], v[92:95], v[148:151], v[196:199]\n\t"
      "v_mfma_f32_16x16x32_bf16 v[184:187], v[96:99], v[152:155], v[184:187]\n\t"
      "v_mfma_f32_16x16x32_bf16 v[188:191], v[100:103], v[152:155], v[188:191]\n\t"
      "v_mfma_f32_16x16x32_bf16 v[192:195], v[104:107], v[152:155], v[192:195]\n\t"
      "v_mfma_f32_16x16x32_bf16 v[196:199], v[108:111], v[152:155], v[196:199]\n\t"
      "v_mfma_f32_16x16x32_bf16 v[184:187], v[112:115], v[156:159], v[184:187]\n\t"
      "v_mfma_f32_16x16x32_bf16 v[188:191], v[116:119], v[156:159], v[188:191]\n\t"
      "v_mfma_f32_16x16x32_bf16 v[192:195], v[120:123], v[156:159], v[192:195]\n\t"
      "v_mfma_f32_16x16x32_bf16 v[196:199], v[124:127], v[156:159], v[196:199]\n\t"
      "global_load_dwordx4 v[128:131], v176, s[58:59] sc1\n\t"
      "global_load_dwordx4 v[132:135], v177, s[58:59] sc1\n\t"
      "global_load_dwordx4 v[136:139], v178, s[58:59] sc1\n\t"
      "global_load_dwordx4 v[140:143], v179, s[58:59] sc1\n\t"
      "global_load_dwordx4 v[144:147], v180, s[58:59] sc1\n\t"
      "global_load_dwordx4 v[148:151], v181, s[58:59] sc1\n\t"
      "global_load_dwordx4 v[152:155], v182, s[58:59] sc1\n\t"
      "global_load_dwordx4 v[156:159], v183, s[58:59] sc1\n\t"
      "s_waitcnt vmcnt(0)\n\t"
      "s_nop 7\n\t"
      "s_branch NQ2B_%=\n\t"
      "NQ3R_%=:\n\t"
      "s_mov_b32 s51, 0\n\t"
      "NQ3L_%=:\n\t"
      "s_add_u32 s51, s51, 1\n\t"
      "s_cmp_lt_u32 s51, 262144\n\t"
      "s_cbranch_scc0 NQ3X_%=\n\t"
      "s_waitcnt vmcnt(0)\n\t"
      "s_sleep 1\n\t"
      "global_load_dwordx4 v[128:131], v176, s[58:59] sc1\n\t"
      "global_load_dwordx4 v[132:135], v177, s[58:59] sc1\n\t"
      "global_load_dwordx4 v[136:139], v178, s[58:59] sc1\n\t"
      "global_load_dwordx4 v[140:143], v179, s[58:59] sc1\n\t"
      "global_load_dwordx4 v[144:147], v180, s[58:59] sc1\n\t"
      "global_load_dwordx4 v[148:151], v181, s[58:59] sc1\n\t"
      "global_load_dwordx4 v[152:155], v182, s[58:59] sc1\n\t"
      "global_load_dwordx4 v[156:159], v183, s[58:59] sc1\n\t"
      "s_waitcnt vmcnt(0)\n\t"
      "v_max3_u32 v234, v128, v129, v130\n\t"
      "v_max3_u32 v234, v234, v131, v132\n\t"
      "v_max3_u32 v234, v234, v133, v134\n\t"
      "v_max3_u32 v234, v234, v135, v136\n\t"
      "v_max3_u32 v234, v234, v137, v138\n\t"
      "v_max3_u32 v234, v234, v139, v140\n\t"
      "v_max3_u32 v234, v234, v141, v142\n\t"
      "v_max3_u32 v234, v234, v143, v144\n\t"
      "v_max3_u32 v234, v234, v145, v146\n\t"
      "v_max3_u32 v234, v234, v147, v148\n\t"
      "v_max3_u32 v234, v234, v149, v150\n\t"
      "v_max3_u32 v234, v234, v151, v152\n\t"
      "v_max3_u32 v234, v234, v153, v154\n\t"
      "v_max3_u32 v234, v234, v155, v156\n\t"
      "v_max3_u32 v234, v234, v157, v158\n\t"
      "v_max_u32_e32 v234, v234, v159\n\t"
      "v_cmp_eq_u32_e32 vcc, -1, v234\n\t"
      "s_cbranch_vccnz NQ3L_%=\n\t"
      "NQ3X_%=:\n\t"
      "v_mfma_f32_16x16x32_bf16 v[200:203], v[0:3], v[128:131], v[236:239]\n\t"
      "v_mfma_f32_16x16x32_bf16 v[204:207], v[4:7], v[128:131], 0\n\t"
      "v_mfma_f32_16x16x32_bf16 v[208:211], v[8:11], v[128:131], 0\n\t"
      "v_mfma_f32_16x16x32_bf16 v[212:215], v[12:15], v[128:131], 0\n\t"
      "v_mfma_f32_16x16x32_bf16 v[200:203], v[16:19], v[132:135], v[200:203]\n\t"
      "v_mfma_f32_16x16x32_bf16 v[204:207], v[20:23], v[132:135], v[204:207]\n\t"
      "v_mfma_f32_16x16x32_bf16 v[208:211], v[24:27], v[132:135], v[208:211]\n\t"
      "v_mfma_f32_16x16x32_bf16 v[212:215], v[28:31], v[132:135], v[212:215]\n\t"
      "v_mfma_f32_16x16x32_bf16 v[200:203], v[32:35], v[136:139], v[200:203]\n\t"
      "v_mfma_f32_16x16x32_bf16 v[204:207], v[36:39], v[136:139], v[204:207]\n\t"
      "v_mfma_f32_16x16x32_bf16 v[208:211], v[40:43], v[136:139], v[208:211]\n\t"
      "v_mfma_f32_16x16x32_bf16 v[212:215], v[44:47], v[136:139], v[212:215]\n\t"
      "v_mfma_f32_16x16x32_bf16 v[200:203], v[48:51], v[140:143], v[200:203]\n\t"
      "v_mfma_f32_16x16x32_bf16 v[204:207], v[52:55], v[140:143], v[204:207]\n\t"
      "v_mfma_f32_16x16x32_bf16 v[208:211], v[56:59], v[140:143], v[208:211]\n\t"
      "v_mfma_f32_16x16x32_bf16 v[212:215], v[60:63], v[140:143], v[212:215]\n\t"
      "v_mfma_f32_16x16x32_bf16 v[200:203], v[64:67], v[144:147], v[200:203]\n\t"
      "v_mfma_f32_16x16x32_bf16 v[204:207], v[68:71], v[144:147], v[204:207]\n\t"
      "v_mfma_f32_16x16x32_bf16 v[208:211], v[72:75], v[144:147], v[208:211]\n\t"
      "v_mfma_f32_16x16x32_bf16 v[212:215], v[76:79], v[144:147], v[212:215]\n\t"
      "v_mfma_f32_16x16x32_bf16 v[200:203], v[80:83], v[148:151], v[200:203]\n\t"
      "v_mfma_f32_16x16x32_bf16 v[204:207], v[84:87], v[148:151], v[204:207]\n\t"
      "v_mfma_f32_16x16x32_bf16 v[208:211], v[88:91], v[148:151], v[208:211]\n\t"
      "v_mfma_f32_16x16x32_bf16 v[212:215], v[92:95], v[148:151], v[212:215]\n\t"
      "v_mfma_f32_16x16x32_bf16 v[200:203], v[96:99], v[152:155], v[200:203]\n\t"
      "v_mfma_f32_16x16x32_bf16 v[204:207], v[100:103], v[152:155], v[204:207]\n\t"
      "v_mfma_f32_16x16x32_bf16 v[208:211], v[104:107], v[152:155], v[208:211]\n\t"
      "v_mfma_f32_16x16x32_bf16 v[212:215], v[108:111], v[152:155], v[212:215]\n\t"
      "v_mfma_f32_16x16x32_bf16 v[200:203], v[112:115], v[156:159], v[200:203]\n\t"
      "v_mfma_f32_16x16x32_bf16 v[204:207], v[116:119], v[156:159], v[204:207]\n\t"
      "v_mfma_f32_16x16x32_bf16 v[208:211], v[120:123], v[156:159], v[208:211]\n\t"
      "v_mfma_f32_16x16x32_bf16 v[212:215], v[124:127], v[156:159], v[212:215]\n\t"
      "global_load_dwordx4 v[128:131], v176, s[60:61] sc1\n\t"
      "global_load_dwordx4 v[132:135], v177, s[60:61] sc1\n\t"
      "global_load_dwordx4 v[136:139], v178, s[60:61] sc1\n\t"
      "global_load_dwordx4 v[140:143], v179, s[60:61] sc1\n\t"
      "global_load_dwordx4 v[144:147], v180, s[60:61] sc1\n\t"
      "global_load_dwordx4 v[148:151], v181, s[60:61] sc1\n\t"
      "global_load_dwordx4 v[152:155], v182, s[60:61] sc1\n\t"
      "global_load_dwordx4 v[156:159], v183, s[60:61] sc1\n\t"
      "s_waitcnt vmcnt(0)\n\t"
      "s_nop 7\n\t"
      "s_branch NQ3B_%=\n\t"
      "NQ4R_%=:\n\t"
      "s_mov_b32 s51, 0\n\t"
      "NQ4L_%=:\n\t"
      "s_add_u32 s51, s51, 1\n\t"
      "s_cmp_lt_u32 s51, 262144\n\t"
      "s_cbranch_scc0 NQ4X_%=\n\t"
      "s_waitcnt vmcnt(0)\n\t"
      "s_sleep 1\n\t"
      "global_load_dwordx4 v[128:131], v176, s[60:61] sc1\n\t"
      "global_load_dwordx4 v[132:135], v177, s[60:61] sc1\n\t"
      "global_load_dwordx4 v[136:139], v178, s[60:61] sc1\n\t"
      "global_load_dwordx4 v[140:143], v179, s[60:61] sc1\n\t"
      "global_load_dwordx4 v[144:147], v180, s[60:61] sc1\n\t"
      "global_load_dwordx4 v[148:151], v181, s[60:61] sc1\n\t"
      "global_load_dwordx4 v[152:155], v182, s[60:61] sc1\n\t"
      "global_load_dwordx4 v[156:159], v183, s[60:61] sc1\n\t"
      "s_waitcnt vmcnt(0)\n\t"
      "v_max3_u32 v234, v128, v129, v130\n\t"
      "v_max3_u32 v234, v234, v131, v132\n\t"
      "v_max3_u32 v234, v234, v133, v134\n\t"
      "v_max3_u32 v234, v234, v135, v136\n\t"
      "v_max3_u32 v234, v234, v137, v138\n\t"
      "v_max3_u32 v234, v234, v139, v140\n\t"
      "v_max3_u32 v234, v234, v141, v142\n\t"
      "v_max3_u32 v234, v234, v143, v144\n\t"
      "v_max3_u32 v234, v234, v145, v146\n\t"
      "v_max3_u32 v234, v234, v147, v148\n\t"
      "v_max3_u32 v234, v234, v149, v150\n\t"
      "v_max3_u32 v234, v234, v151, v152\n\t"
      "v_max3_u32 v234, v234, v153, v154\n\t"
      "v_max3_u32 v234, v234, v155, v156\n\t"
      "v_max3_u32 v234, v234, v157, v158\n\t"
      "v_max_u32_e32 v234, v234, v159\n\t"
      "v_cmp_eq_u32_e32 vcc, -1, v234\n\t"
      "s_cbranch_vccnz NQ4L_%=\n\t"
      "NQ4X_%=:\n\t"
      "v_mfma_f32_16x16x32_bf16 v[184:187], v[0:3], v[128:131], v[236:239]\n\t"
      "v_mfma_f32_16x16x32_bf16 v[188:191], v[4:7], v[128:131], 0\n\t"
      "v_mfma_f32_16x16x32_bf16 v[192:195], v[8:11], v[128:131], 0\n\t"
      "v_mfma_f32_16x16x32_bf16 v[196:199], v[12:15], v[128:131], 0\n\t"
      "v_mfma_f32_16x16x32_bf16 v[184:187], v[16:19], v[132:135], v[184:187]\n\t"
      "v_mfma_f32_16x16x32_bf16 v[188:191], v[20:23], v[132:135], v[188:191]\n\t"
      "v_mfma_f32_16x16x32_bf16 v[192:195], v[24:27], v[132:135], v[192:195]\n\t"
      "v_mfma_f32_16x16x32_bf16 v[196:199], v[28:31], v[132:135], v[196:199]\n\t"
      "v_mfma_f32_16x16x32_bf16 v[184:187], v[32:35], v[136:139], v[184:187]\n\t"
      "v_mfma_f32_16x16x32_bf16 v[188:191], v[36:39], v[136:139], v[188:191]\n\t"
      "v_mfma_f32_16x16x32_bf16 v[192:195], v[40:43], v[136:139], v[192:195]\n\t"
      "v_mfma_f32_16x16x32_bf16 v[196:199], v[44:47], v[136:139], v[196:199]\n\t"
      "v_mfma_f32_16x16x32_bf16 v[184:187], v[48:51], v[140:143], v[184:187]\n\t"
      "v_mfma_f32_16x16x32_bf16 v[188:191], v[52:55], v[140:143], v[188:191]\n\t"
      "v_mfma_f32_16x16x32_bf16 v[192:195], v[56:59], v[140:143], v[192:195]\n\t"
      "v_mfma_f32_16x16x32_bf16 v[196:199], v[60:63], v[140:143], v[196:199]\n\t"
      "v_mfma_f32_16x16x32_bf16 v[184:187], v[64:67], v[144:147], v[184:187]\n\t"
      "v_mfma_f32_16x16x32_bf16 v[188:191], v[68:71], v[144:147], v[188:191]\n\t"
      "v_mfma_f32_16x16x32_bf16 v[192:195], v[72:75], v[144:147], v[192:195]\n\t"
      "v_mfma_f32_16x16x32_bf16 v[196:199], v[76:79], v[144:147], v[196:199]\n\t"
      "v_mfma_f32_16x16x32_bf16 v[184:187], v[80:83], v[148:151], v[184:187]\n\t"
      "v_mfma_f32_16x16x32_bf16 v[188:191], v[84:87], v[148:151], v[188:191]\n\t"
      "v_mfma_f32_16x16x32_bf16 v[192:195], v[88:91], v[148:151], v[192:195]\n\t"
      "v_mfma_f32_16x16x32_bf16 v[196:199], v[92:95], v[148:151], v[196:199]\n\t"
      "v_mfma_f32_16x16x32_bf16 v[184:187], v[96:99], v[152:155], v[184:187]\n\t"
      "v_mfma_f32_16x16x32_bf16 v[188:191], v[100:103], v[152:155], v[188:191]\n\t"
      "v_mfma_f32_16x16x32_bf16 v[192:195], v[104:107], v[152:155], v[192:195]\n\t"
      "v_mfma_f32_16x16x32_bf16 v[196:199], v[108:111], v[152:155], v[196:199]\n\t"
      "v_mfma_f32_16x16x32_bf16 v[184:187], v[112:115], v[156:159], v[184:187]\n\t"
      "v_mfma_f32_16x16x32_bf16 v[188:191], v[116:119], v[156:159], v[188:191]\n\t"
      "v_mfma_f32_16x16x32_bf16 v[192:195], v[120:123], v[156:159], v[192:195]\n\t"
      "v_mfma_f32_16x16x32_bf16 v[196:199], v[124:127], v[156:159], v[196:199]\n\t"
      "global_load_dwordx4 v[128:131], v176, s[62:63] sc1\n\t"
      "global_load_dwordx4 v[132:135], v177, s[62:63] sc1\n\t"
      "global_load_dwordx4 v[136:139], v178, s[62:63] sc1\n\t"
      "global_load_dwordx4 v[140:143], v179, s[62:63] sc1\n\t"
      "global_load_dwordx4 v[144:147], v180, s[62:63] sc1\n\t"
      "global_load_dwordx4 v[148:151], v181, s[62:63] sc1\n\t"
      "global_load_dwordx4 v[152:155], v182, s[62:63] sc1\n\t"
      "global_load_dwordx4 v[156:159], v183, s[62:63] sc1\n\t"
      "s_waitcnt vmcnt(0)\n\t"
      "s_nop 7\n\t"
      "s_branch NQ4B_%=\n\t"
      "NQ5R_%=:\n\t"
      "s_mov_b32 s51, 0\n\t"
      "NQ5L_%=:\n\t"
      "s_add_u32 s51, s51, 1\n\t"
      "s_cmp_lt_u32 s51, 262144\n\t"
      "s_cbranch_scc0 NQ5X_%=\n\t"
      "s_waitcnt vmcnt(0)\n\t"
      "s_sleep 1\n\t"
      "global_load_dwordx4 v[128:131], v176, s[62:63] sc1\n\t"
      "global_load_dwordx4 v[132:135], v177, s[62:63] sc1\n\t"
      "global_load_dwordx4 v[136:139], v178, s[62:63] sc1\n\t"
      "global_load_dwordx4 v[140:143], v179, s[62:63] sc1\n\t"
      "global_load_dwordx4 v[144:147], v180, s[62:63] sc1\n\t"
      "global_load_dwordx4 v[148:151], v181, s[62:63] sc1\n\t"
      "global_load_dwordx4 v[152:155], v182, s[62:63] sc1\n\t"
      "global_load_dwordx4 v[156:159], v183, s[62:63] sc1\n\t"
      "s_waitcnt vmcnt(0)\n\t"
      "v_max3_u32 v234, v128, v129, v130\n\t"
      "v_max3_u32 v234, v234, v131, v132\n\t"
      "v_max3_u32 v234, v234, v133, v134\n\t"
      "v_max3_u32 v234, v234, v135, v136\n\t"
      "v_max3_u32 v234, v234, v137, v138\n\t"
      "v_max3_u32 v234, v234, v139, v140\n\t"
      "v_max3_u32 v234, v234, v141, v142\n\t"
      "v_max3_u32 v234, v234, v143, v144\n\t"
      "v_max3_u32 v234, v234, v145, v146\n\t"
      "v_max3_u32 v234, v234, v147, v148\n\t"
      "v_max3_u32 v234, v234, v149, v150\n\t"
      "v_max3_u32 v234, v234, v151, v152\n\t"
      "v_max3_u32 v234, v234, v153, v154\n\t"
      "v_max3_u32 v234, v234, v155, v156\n\t"
      "v_max3_u32 v234, v234, v157, v158\n\t"
      "v_max_u32_e32 v234, v234, v159\n\t"
      "v_cmp_eq_u32_e32 vcc, -1, v234\n\t"
      "s_cbranch_vccnz NQ5L_%=\n\t"
      "NQ5X_%=:\n\t"
      "v_mfma_f32_16x16x32_bf16 v[200:203], v[0:3], v[128:131], v[236:239]\n\t"
      "v_mfma_f32_16x16x32_bf16 v[204:207], v[4:7], v[128:131], 0\n\t"
      "v_mfma_f32_16x16x32_bf16 v[208:211], v[8:11], v[128:131], 0\n\t"
      "v_mfma_f32_16x16x32_bf16 v[212:215], v[12:15], v[128:131], 0\n\t"
      "v_mfma_f32_16x16x32_bf16 v[200:203], v[16:19], v[132:135], v[200:203]\n\t"
      "v_mfma_f32_16x16x32_bf16 v[204:207], v[20:23], v[132:135], v[204:207]\n\t"
      "v_mfma_f32_16x16x32_bf16 v[208:211], v[24:27], v[132:135], v[208:211]\n\t"
      "v_mfma_f32_16x16x32_bf16 v[212:215], v[28:31], v[132:135], v[212:215]\n\t"
      "v_mfma_f32_16x16x32_bf16 v[200:203], v[32:35], v[136:139], v[200:203]\n\t"
      "v_mfma_f32_16x16x32_bf16 v[204:207], v[36:39], v[136:139], v[204:207]\n\t"
      "v_mfma_f32_16x16x32_bf16 v[208:211], v[40:43], v[136:139], v[208:211]\n\t"
      "v_mfma_f32_16x16x32_bf16 v[212:215], v[44:47], v[136:139], v[212:215]\n\t"
      "v_mfma_f32_16x16x32_bf16 v[200:203], v[48:51], v[140:143], v[200:203]\n\t"
      "v_mfma_f32_16x16x32_bf16 v[204:207], v[52:55], v[140:143], v[204:207]\n\t"
      "v_mfma_f32_16x16x32_bf16 v[208:211], v[56:59], v[140:143], v[208:211]\n\t"
      "v_mfma_f32_16x16x32_bf16 v[212:215], v[60:63], v[140:143], v[212:215]\n\t"
      "v_mfma_f32_16x16x32_bf16 v[200:203], v[64:67], v[144:147], v[200:203]\n\t"
      "v_mfma_f32_16x16x32_bf16 v[204:207], v[68:71], v[144:147], v[204:207]\n\t"
      "v_mfma_f32_16x16x32_bf16 v[208:211], v[72:75], v[144:147], v[208:211]\n\t"
      "v_mfma_f32_16x16x32_bf16 v[212:215], v[76:79], v[144:147], v[212:215]\n\t"
      "v_mfma_f32_16x16x32_bf16 v[200:203], v[80:83], v[148:151], v[200:203]\n\t"
      "v_mfma_f32_16x16x32_bf16 v[204:207], v[84:87], v[148:151], v[204:207]\n\t"
      "v_mfma_f32_16x16x32_bf16 v[208:211], v[88:91], v[148:151], v[208:211]\n\t"
      "v_mfma_f32_16x16x32_bf16 v[212:215], v[92:95], v[148:151], v[212:215]\n\t"
      "v_mfma_f32_16x16x32_bf16 v[200:203], v[96:99], v[152:155], v[200:203]\n\t"
      "v_mfma_f32_16x16x32_bf16 v[204:207], v[100:103], v[152:155], v[204:207]\n\t"
      "v_mfma_f32_16x16x32_bf16 v[208:211], v[104:107], v[152:155], v[208:211]\n\t"
      "v_mfma_f32_16x16x32_bf16 v[212:215], v[108:111], v[152:155], v[212:215]\n\t"
      "v_mfma_f32_16x16x32_bf16 v[200:203], v[112:115], v[156:159], v[200:203]\n\t"
      "v_mfma_f32_16x16x32_bf16 v[204:207], v[116:119], v[156:159], v[204:207]\n\t"
      "v_mfma_f32_16x16x32_bf16 v[208:211], v[120:123], v[156:159], v[208:211]\n\t"
      "v_mfma_f32_16x16x32_bf16 v[212:215], v[124:127], v[156:159], v[212:215]\n\t"
      "global_load_dwordx4 v[128:131], v176, s[64:65] sc1\n\t"
      "global_load_dwordx4 v[132:135], v177, s[64:65] sc1\n\t"
      "global_load_dwordx4 v[136:139], v178, s[64:65] sc1\n\t"
      "global_load_dwordx4 v[140:143], v179, s[64:65] sc1\n\t"
      "global_load_dwordx4 v[144:147], v180, s[64:65] sc1\n\t"
      "global_load_dwordx4 v[148:151], v181, s[64:65] sc1\n\t"
      "global_load_dwordx4 v[152:155], v182, s[64:65] sc1\n\t"
      "global_load_dwordx4 v[156:159], v183, s[64:65] sc1\n\t"
      "s_waitcnt vmcnt(0)\n\t"
      "s_nop 7\n\t"
      "s_branch NQ5B_%=\n\t"
      "NQ6R_%=:\n\t"
      "s_mov_b32 s51, 0\n\t"
      "NQ6L_%=:\n\t"
      "s_add_u32 s51, s51, 1\n\t"
      "s_cmp_lt_u32 s51, 262144\n\t"
      "s_cbranch_scc0 NQ6X_%=\n\t"
      "s_waitcnt vmcnt(0)\n\t"
      "s_sleep 1\n\t"
      "global_load_dwordx4 v[128:131], v176, s[64:65] sc1\n\t"
      "global_load_dwordx4 v[132:135], v177, s[64:65] sc1\n\t"
      "global_load_dwordx4 v[136:139], v178, s[64:65] sc1\n\t"
      "global_load_dwordx4 v[140:143], v179, s[64:65] sc1\n\t"
      "global_load_dwordx4 v[144:147], v180, s[64:65] sc1\n\t"
      "global_load_dwordx4 v[148:151], v181, s[64:65] sc1\n\t"
      "global_load_dwordx4 v[152:155], v182, s[64:65] sc1\n\t"
      "global_load_dwordx4 v[156:159], v183, s[64:65] sc1\n\t"
      "s_waitcnt vmcnt(0)\n\t"
      "v_max3_u32 v234, v128, v129, v130\n\t"
      "v_max3_u32 v234, v234, v131, v132\n\t"
      "v_max3_u32 v234, v234, v133, v134\n\t"
      "v_max3_u32 v234, v234, v135, v136\n\t"
      "v_max3_u32 v234, v234, v137, v138\n\t"
      "v_max3_u32 v234, v234, v139, v140\n\t"
      "v_max3_u32 v234, v234, v141, v142\n\t"
      "v_max3_u32 v234, v234, v143, v144\n\t"
      "v_max3_u32 v234, v234, v145, v146\n\t"
      "v_max3_u32 v234, v234, v147, v148\n\t"
      "v_max3_u32 v234, v234, v149, v150\n\t"
      "v_max3_u32 v234, v234, v151, v152\n\t"
      "v_max3_u32 v234, v234, v153, v154\n\t"
      "v_max3_u32 v234, v234, v155, v156\n\t"
      "v_max3_u32 v234, v234, v157, v158\n\t"
      "v_max_u32_e32 v234, v234, v159\n\t"
      "v_cmp_eq_u32_e32 vcc, -1, v234\n\t"
      "s_cbranch_vccnz NQ6L_%=\n\t"
      "NQ6X_%=:\n\t"
      "v_mfma_f32_16x16x32_bf16 v[184:187], v[0:3], v[128:131], v[236:239]\n\t"
      "v_mfma_f32_16x16x32_bf16 v[188:191], v[4:7], v[128:131], 0\n\t"
      "v_mfma_f32_16x16x32_bf16 v[192:195], v[8:11], v[128:131], 0\n\t"
      "v_mfma_f32_16x16x32_bf16 v[196:199], v[12:15], v[128:131], 0\n\t"
      "v_mfma_f32_16x16x32_bf16 v[184:187], v[16:19], v[132:135], v[184:187]\n\t"
      "v_mfma_f32_16x16x32_bf16 v[188:191], v[20:23], v[132:135], v[188:191]\n\t"
      "v_mfma_f32_16x16x32_bf16 v[192:195], v[24:27], v[132:135], v[192:195]\n\t"
      "v_mfma_f32_16x16x32_bf16 v[196:199], v[28:31], v[132:135], v[196:199]\n\t"
      "v_mfma_f32_16x16x32_bf16 v[184:187], v[32:35], v[136:139], v[184:187]\n\t"
      "v_mfma_f32_16x16x32_bf16 v[188:191], v[36:39], v[136:139], v[188:191]\n\t"
      "v_mfma_f32_16x16x32_bf16 v[192:195], v[40:43], v[136:139], v[192:195]\n\t"
      "v_mfma_f32_16x16x32_bf16 v[196:199], v[44:47], v[136:139], v[196:199]\n\t"
      "v_mfma_f32_16x16x32_bf16 v[184:187], v[48:51], v[140:143], v[184:187]\n\t"
      "v_mfma_f32_16x16x32_bf16 v[188:191], v[52:55], v[140:143], v[188:191]\n\t"
      "v_mfma_f32_16x16x32_bf16 v[192:195], v[56:59], v[140:143], v[192:195]\n\t"
      "v_mfma_f32_16x16x32_bf16 v[196:199], v[60:63], v[140:143], v[196:199]\n\t"
      "v_mfma_f32_16x16x32_bf16 v[184:187], v[64:67], v[144:147], v[184:187]\n\t"
      "v_mfma_f32_16x16x32_bf16 v[188:191], v[68:71], v[144:147], v[188:191]\n\t"
      "v_mfma_f32_16x16x32_bf16 v[192:195], v[72:75], v[144:147], v[192:195]\n\t"
      "v_mfma_f32_16x16x32_bf16 v[196:199], v[76:79], v[144:147], v[196:199]\n\t"
      "v_mfma_f32_16x16x32_bf16 v[184:187], v[80:83], v[148:151], v[184:187]\n\t"
      "v_mfma_f32_16x16x32_bf16 v[188:191], v[84:87], v[148:151], v[188:191]\n\t"
      "v_mfma_f32_16x16x32_bf16 v[192:195], v[88:91], v[148:151], v[192:195]\n\t"
      "v_mfma_f32_16x16x32_bf16 v[196:199], v[92:95], v[148:151], v[196:199]\n\t"
      "v_mfma_f32_16x16x32_bf16 v[184:187], v[96:99], v[152:155], v[184:187]\n\t"
      "v_mfma_f32_16x16x32_bf16 v[188:191], v[100:103], v[152:155], v[188:191]\n\t"
      "v_mfma_f32_16x16x32_bf16 v[192:195], v[104:107], v[152:155], v[192:195]\n\t"
      "v_mfma_f32_16x16x32_bf16 v[196:199], v[108:111], v[152:155], v[196:199]\n\t"
      "v_mfma_f32_16x16x32_bf16 v[184:187], v[112:115], v[156:159], v[184:187]\n\t"
      "v_mfma_f32_16x16x32_bf16 v[188:191], v[116:119], v[156:159], v[188:191]\n\t"
      "v_mfma_f32_16x16x32_bf16 v[192:195], v[120:123], v[156:159], v[192:195]\n\t"
      "v_mfma_f32_16x16x32_bf16 v[196:199], v[124:127], v[156:159], v[196:199]\n\t"
      "s_waitcnt vmcnt(0)\n\t"
      "s_nop 7\n\t"
      "s_branch NQ6B_%=\n\t"
      "FP0R_%=:\n\t"
      "s_mov_b32 s51, 0\n\t"
      "FP0L_%=:\n\t"
      "s_add_u32 s51, s51, 1\n\t"
      "s_cmp_lt_u32 s51, 262144\n\t"
      "s_cbranch_scc0 FP0X_%=\n\t"
      "s_waitcnt vmcnt(0)\n\t"
      "s_sleep 1\n\t"
      "global_load_dwordx4 v[128:131], v176, s[52:53] sc1\n\t"
      "global_load_dwordx4 v[132:135], v177, s[52:53] sc1\n\t"
      "global_load_dwordx4 v[136:139], v178, s[52:53] sc1\n\t"
      "global_load_dwordx4 v[140:143], v179, s[52:53] sc1\n\t"
      "global_load_dwordx4 v[144:147], v180, s[52:53] sc1\n\t"
      "global_load_dwordx4 v[148:151], v181, s[52:53] sc1\n\t"
      "global_load_dwordx4 v[152:155], v182, s[52:53] sc1\n\t"
      "global_load_dwordx4 v[156:159], v183, s[52:53] sc1\n\t"
      "s_waitcnt vmcnt(7)\n\t"
      "v_max3_u32 v234, v128, v129, v130\n\t"
      "v_max_u32_e32 v234, v234, v131\n\t"
      "v_cmp_eq_u32_e32 vcc, -1, v234\n\t"
      "s_cbranch_vccnz FP0L_%=\n\t"
      "FP0X_%=:\n\t"
      "s_branch FP0B_%=\n\t"
      "FP1R_%=:\n\t"
      "s_mov_b32 s51, 0\n\t"
      "FP1L_%=:\n\t"
      "s_add_u32 s51, s51, 1\n\t"
      "s_cmp_lt_u32 s51, 262144\n\t"
      "s_cbranch_scc0 FP1X_%=\n\t"
      "s_waitcnt vmcnt(0)\n\t"
      "s_sleep 1\n\t"
      "global_load_dwordx4 v[132:135], v177, s[52:53] sc1\n\t"
      "global_load_dwordx4 v[136:139], v178, s[52:53] sc1\n\t"
      "global_load_dwordx4 v[140:143], v179, s[52:53] sc1\n\t"
      "global_load_dwordx4 v[144:147], v180, s[52:53] sc1\n\t"
      "global_load_dwordx4 v[148:151], v181, s[52:53] sc1\n\t"
      "global_load_dwordx4 v[152:155], v182, s[52:53] sc1\n\t"
      "global_load_dwordx4 v[156:159], v183, s[52:53] sc1\n\t"
      "s_waitcnt vmcnt(6)\n\t"
      "v_max3_u32 v234, v132, v133, v134\n\t"
      "v_max_u32_e32 v234, v234, v135\n\t"
      "v_cmp_eq_u32_e32 vcc, -1, v234\n\t"
      "s_cbranch_vccnz FP1L_%=\n\t"
      "FP1X_%=:\n\t"
      "s_branch FP1B_%=\n\t"
      "FP2R_%=:\n\t"
      "s_mov_b32 s51, 0\n\t"
      "FP2L_%=:\n\t"
      "s_add_u32 s51, s51, 1\n\t"
      "s_cmp_lt_u32 s51, 262144\n\t"
      "s_cbranch_scc0 FP2X_%=\n\t"
      "s_waitcnt vmcnt(0)\n\t"
      "s_sleep 1\n\t"
      "global_load_dwordx4 v[136:139], v178, s[52:53] sc1\n\t"
      "global_load_dwordx4 v[140:143], v179, s[52:53] sc1\n\t"
      "global_load_dwordx4 v[144:147], v180, s[52:53] sc1\n\t"
      "global_load_dwordx4 v[148:151], v181, s[52:53] sc1\n\t"
      "global_load_dwordx4 v[152:155], v182, s[52:53] sc1\n\t"
      "global_load_dwordx4 v[156:159], v183, s[52:53] sc1\n\t"
      "global_load_dwordx4 v[128:131], v176, s[54:55] sc1\n\t"
      "global_load_dwordx4 v[132:135], v177, s[54:55] sc1\n\t"
      "s_waitcnt vmcnt(7)\n\t"
      "v_max3_u32 v234, v136, v137, v138\n\t"
      "v_max_u32_e32 v234, v234, v139\n\t"
      "v_cmp_eq_u32_e32 vcc, -1, v234\n\t"
      "s_cbranch_vccnz FP2L_%=\n\t"
      "FP2X_%=:\n\t"
      "s_branch FP2B_%=\n\t"
      "FP3R_%=:\n\t"
      "s_mov_b32 s51, 0\n\t"
      "FP3L_%=:\n\t"
      "s_add_u32 s51, s51, 1\n\t"
      "s_cmp_lt_u32 s51, 262144\n\t"
      "s_cbranch_scc0 FP3X_%=\n\t"
      "s_waitcnt vmcnt(0)\n\t"
      "s_sleep 1\n\t"
      "global_load_dwordx4 v[140:143], v179, s[52:53] sc1\n\t"
      "global_load_dwordx4 v[144:147], v180, s[52:53] sc1\n\t"
      "global_load_dwordx4 v[148:151], v181, s[52:53] sc1\n\t"
      "global_load_dwordx4 v[152:155], v182, s[52:53] sc1\n\t"
      "global_load_dwordx4 v[156:159], v183, s[52:53] sc1\n\t"
      "global_load_dwordx4 v[128:131], v176, s[54:55] sc1\n\t"
      "global_load_dwordx4 v[132:135], v177, s[54:55] sc1\n\t"
      "s_waitcnt vmcnt(6)\n\t"
      "v_max3_u32 v234, v140, v141, v142\n\t"
      "v_max_u32_e32 v234, v234, v143\n\t"
      "v_cmp_eq_u32_e32 vcc, -1, v234\n\t"
      "s_cbranch_vccnz FP3L_%=\n\t"
      "FP3X_%=:\n\t"
      "s_branch FP3B_%=\n\t"
      "FP4R_%=:\n\t"
      "s_mov_b32 s51, 0\n\t"
      "FP4L_%=:\n\t"
      "s_add_u32 s51, s51, 1\n\t"
      "s_cmp_lt_u32 s51, 262144\n\t"
      "s_cbranch_scc0 FP4X_%=\n\t"
      "s_waitcnt vmcnt(0)\n\t"
      "s_sleep 1\n\t"
      "global_load_dwordx4 v[144:147], v180, s[52:53] sc1\n\t"
      "global_load_dwordx4 v[148:151], v181, s[52:53] sc1\n\t"
      "global_load_dwordx4 v[152:155], v182, s[52:53] sc1\n\t"
      "global_load_dwordx4 v[156:159], v183, s[52:53] sc1\n\t"
      "global_load_dwordx4 v[128:131], v176, s[54:55] sc1\n\t"
      "global_load_dwordx4 v[132:135], v177, s[54:55] sc1\n\t"
      "global_load_dwordx4 v[136:139], v178, s[54:55] sc1\n\t"
      "global_load_dwordx4 v[140:143], v179, s[54:55] sc1\n\t"
      "s_waitcnt vmcnt(7)\n\t"
      "v_max3_u32 v234, v144, v145, v146\n\t"
      "v_max_u32_e32 v234, v234, v147\n\t"
      "v_cmp_eq_u32_e32 vcc, -1, v234\n\t"
      "s_cbranch_vccnz FP4L_%=\n\t"
      "FP4X_%=:\n\t"
      "s_branch FP4B_%=\n\t"
      "FP5R_%=:\n\t"
      "s_mov_b32 s51, 0\n\t"
      "FP5L_%=:\n\t"
      "s_add_u32 s51, s51, 1\n\t"
      "s_cmp_lt_u32 s51, 262144\n\t"
      "s_cbranch_scc0 FP5X_%=\n\t"
      "s_waitcnt vmcnt(0)\n\t"
      "s_sleep 1\n\t"
      "global_load_dwordx4 v[148:151], v181, s[52:53] sc1\n\t"
      "global_load_dwordx4 v[152:155], v182, s[52:53] sc1\n\t"
      "global_load_dwordx4 v[156:159], v183, s[52:53] sc1\n\t"
      "global_load_dwordx4 v[128:131], v176, s[54:55] sc1\n\t"
      "global_load_dwordx4 v[132:135], v177, s[54:55] sc1\n\t"
      "global_load_dwordx4 v[136:139], v178, s[54:55] sc1\n\t"
      "global_load_dwordx4 v[140:143], v179, s[54:55] sc1\n\t"
      "s_waitcnt vmcnt(6)\n\t"
      "v_max3_u32 v234, v148, v149, v150\n\t"
      "v_max_u32_e32 v234, v234, v151\n\t"
      "v_cmp_eq_u32_e32 vcc, -1, v234\n\t"
      "s_cbranch_vccnz FP5L_%=\n\t"
      "FP5X_%=:\n\t"
      "s_branch FP5B_%=\n\t"
      "FP6R_%=:\n\t"
      "s_mov_b32 s51, 0\n\t"
      "FP6L_%=:\n\t"
      "s_add_u32 s51, s51, 1\n\t"
      "s_cmp_lt_u32 s51, 262144\n\t"
      "s_cbranch_scc0 FP6X_%=\n\t"
      "s_waitcnt vmcnt(0)\n\t"
      "s_sleep 1\n\t"
      "global_load_dwordx4 v[152:155], v182, s[52:53] sc1\n\t"
      "global_load_dwordx4 v[156:159], v183, s[52:53] sc1\n\t"
      "global_load_dwordx4 v[128:131], v176, s[54:55] sc1\n\t"
      "global_load_dwordx4 v[132:135], v177, s[54:55] sc1\n\t"
      "global_load_dwordx4 v[136:139], v178, s[54:55] sc1\n\t"
      "global_load_dwordx4 v[140:143], v179, s[54:55] sc1\n\t"
      "global_load_dwordx4 v[144:147], v180, s[54:55] sc1\n\t"
      "global_load_dwordx4 v[148:151], v181, s[54:55] sc1\n\t"
      "s_waitcnt vmcnt(7)\n\t"
      "v_max3_u32 v234, v152, v153, v154\n\t"
      "v_max_u32_e32 v234, v234, v155\n\t"
      "v_cmp_eq_u32_e32 vcc, -1, v234\n\t"
      "s_cbranch_vccnz FP6L_%=\n\t"
      "FP6X_%=:\n\t"
      "s_branch FP6B_%=\n\t"
      "FP7R_%=:\n\t"
      "s_mov_b32 s51, 0\n\t"
      "FP7L_%=:\n\t"
      "s_add_u32 s51, s51, 1\n\t"
      "s_cmp_lt_u32 s51, 262144\n\t"
      "s_cbranch_scc0 FP7X_%=\n\t"
      "s_waitcnt vmcnt(0)\n\t"
      "s_sleep 1\n\t"
      "global_load_dwordx4 v[156:159], v183, s[52:53] sc1\n\t"
      "global_load_dwordx4 v[128:131], v176, s[54:55] sc1\n\t"
      "global_load_dwordx4 v[132:135], v177, s[54:55] sc1\n\t"
      "global_load_dwordx4 v[136:139], v178, s[54:55] sc1\n\t"
      "global_load_dwordx4 v[140:143], v179, s[54:55] sc1\n\t"
      "global_load_dwordx4 v[144:147], v180, s[54:55] sc1\n\t"
      "global_load_dwordx4 v[148:151], v181, s[54:55] sc1\n\t"
      "s_waitcnt vmcnt(6)\n\t"
      "v_max3_u32 v234, v156, v157, v158\n\t"
      "v_max_u32_e32 v234, v234, v159\n\t"
      "v_cmp_eq_u32_e32 vcc, -1, v234\n\t"
      "s_cbranch_vccnz FP7L_%=\n\t"
      "FP7X_%=:\n\t"
      "s_branch FP7B_%=\n\t"
      "NP1R_%=:\n\t"
      "s_mov_b32 s51, 0\n\t"
      "NP1L_%=:\n\t"
      "s_add_u32 s51, s51, 1\n\t"
      "s_cmp_lt_u32 s51, 262144\n\t"
      "s_cbranch_scc0 NP1X_%=\n\t"
      "s_waitcnt vmcnt(0)\n\t"
      "s_sleep 1\n\t"
      "global_load_dwordx4 v[128:131], v176, s[54:55] sc1\n\t"
      "global_load_dwordx4 v[132:135], v177, s[54:55] sc1\n\t"
      "global_load_dwordx4 v[136:139], v178, s[54:55] sc1\n\t"
      "global_load_dwordx4 v[140:143], v179, s[54:55] sc1\n\t"
      "global_load_dwordx4 v[144:147], v180, s[54:55] sc1\n\t"
      "global_load_dwordx4 v[148:151], v181, s[54:55] sc1\n\t"
      "global_load_dwordx4 v[152:155], v182, s[54:55] sc1\n\t"
      "global_load_dwordx4 v[156:159], v183, s[54:55] sc1\n\t"
      "s_waitcnt vmcnt(0)\n\t"
      "v_max3_u32 v234, v128, v129, v130\n\t"
      "v_max3_u32 v234, v234, v131, v132\n\t"
      "v_max3_u32 v234, v234, v133, v134\n\t"
      "v_max3_u32 v234, v234, v135, v136\n\t"
      "v_max3_u32 v234, v234, v137, v138\n\t"
      "v_max3_u32 v234, v234, v139, v140\n\t"
      "v_max3_u32 v234, v234, v141, v142\n\t"
      "v_max3_u32 v234, v234, v143, v144\n\t"
      "v_max3_u32 v234, v234, v145, v146\n\t"
      "v_max3_u32 v234, v234, v147, v148\n\t"
      "v_max3_u32 v234, v234, v149, v150\n\t"
      "v_max3_u32 v234, v234, v151, v152\n\t"
      "v_max3_u32 v234, v234, v153, v154\n\t"
      "v_max3_u32 v234, v234, v155, v156\n\t"
      "v_max3_u32 v234, v234, v157, v158\n\t"
      "v_max_u32_e32 v234, v234, v159\n\t"
      "v_cmp_eq_u32_e32 vcc, -1, v234\n\t"
      "s_cbranch_vccnz NP1L_%=\n\t"
      "NP1X_%=:\n\t"
      "v_mfma_f32_16x16x32_bf16 v[200:203], v[0:3], v[128:131], 0\n\t"
      "v_mfma_f32_16x16x32_bf16 v[204:207], v[4:7], v[128:131], 0\n\t"
      "v_mfma_f32_16x16x32_bf16 v[208:211], v[8:11], v[128:131], 0\n\t"
      "v_mfma_f32_16x16x32_bf16 v[212:215], v[12:15], v[128:131], 0\n\t"
      "v_mfma_f32_16x16x32_bf16 v[200:203], v[16:19], v[132:135], v[200:203]\n\t"
      "v_mfma_f32_16x16x32_bf16 v[204:207], v[20:23], v[132:135], v[204:207]\n\t"
      "v_mfma_f32_16x16x32_bf16 v[208:211], v[24:27], v[132:135], v[208:211]\n\t"
      "v_mfma_f32_16x16x32_bf16 v[212:215], v[28:31], v[132:135], v[212:215]\n\t"
      "v_mfma_f32_16x16x32_bf16 v[200:203], v[32:35], v[136:139], v[200:203]\n\t"
      "v_mfma_f32_16x16x32_bf16 v[204:207], v[36:39], v[136:139], v[204:207]\n\t"
      "v_mfma_f32_16x16x32_bf16 v[208:211], v[40:43], v[136:139], v[208:211]\n\t"
      "v_mfma_f32_16x16x32_bf16 v[212:215], v[44:47], v[136:139], v[212:215]\n\t"
      "v_mfma_f32_16x16x32_bf16 v[200:203], v[48:51], v[140:143], v[200:203]\n\t"
      "v_mfma_f32_16x16x32_bf16 v[204:207], v[52:55], v[140:143], v[204:207]\n\t"
      "v_mfma_f32_16x16x32_bf16 v[208:211], v[56:59], v[140:143], v[208:211]\n\t"
      "v_mfma_f32_16x16x32_bf16 v[212:215], v[60:63], v[140:143], v[212:215]\n\t"
      "v_mfma_f32_16x16x32_bf16 v[200:203], v[64:67], v[144:147], v[200:203]\n\t"
      "v_mfma_f32_16x16x32_bf16 v[204:207], v[68:71], v[144:147], v[204:207]\n\t"
      "v_mfma_f32_16x16x32_bf16 v[208:211], v[72:75], v[144:147], v[208:211]\n\t"
      "v_mfma_f32_16x16x32_bf16 v[212:215], v[76:79], v[144:147], v[212:215]\n\t"
      "v_mfma_f32_16x16x32_bf16 v[200:203], v[80:83], v[148:151], v[200:203]\n\t"
      "v_mfma_f32_16x16x32_bf16 v[204:207], v[84:87], v[148:151], v[204:207]\n\t"
      "v_mfma_f32_16x16x32_bf16 v[208:211], v[88:91], v[148:151], v[208:211]\n\t"
      "v_mfma_f32_16x16x32_bf16 v[212:215], v[92:95], v[148:151], v[212:215]\n\t"
      "v_mfma_f32_16x16x32_bf16 v[200:203], v[96:99], v[152:155], v[200:203]\n\t"
      "v_mfma_f32_16x16x32_bf16 v[204:207], v[100:103], v[152:155], v[204:207]\n\t"
      "v_mfma_f32_16x16x32_bf16 v[208:211], v[104:107], v[152:155], v[208:211]\n\t"
      "v_mfma_f32_16x16x32_bf16 v[200:203], v[112:115], v[156:159], v[200:203]\n\t"
      "v_mfma_f32_16x16x32_bf16 v[204:207], v[116:119], v[156:159], v[204:207]\n\t"
      "v_mfma_f32_16x16x32_bf16 v[208:211], v[120:123], v[156:159], v[208:211]\n\t"
      "global_load_dwordx4 v[128:131], v176, s[56:57] sc1\n\t"
      "global_load_dwordx4 v[132:135], v177, s[56:57] sc1\n\t"
      "global_load_dwordx4 v[136:139], v178, s[56:57] sc1\n\t"
      "global_load_dwordx4 v[140:143], v179, s[56:57] sc1\n\t"
      "global_load_dwordx4 v[144:147], v180, s[56:57] sc1\n\t"
      "global_load_dwordx4 v[148:151], v181, s[56:57] sc1\n\t"
      "s_waitcnt vmcnt(0)\n\t"
      "s_nop 7\n\t"
      "s_branch NP1B_%=\n\t"
      "NP2R_%=:\n\t"
      "s_mov_b32 s51, 0\n\t"
      "NP2L_%=:\n\t"
      "s_add_u32 s51, s51, 1\n\t"
      "s_cmp_lt_u32 s51, 262144\n\t"
      "s_cbranch_scc0 NP2X_%=\n\t"
      "s_waitcnt vmcnt(0)\n\t"
      "s_sleep 1\n\t"
      "global_load_dwordx4 v[128:131], v176, s[56:57] sc1\n\t"
      "global_load_dwordx4 v[132:135], v177, s[56:57] sc1\n\t"
      "global_load_dwordx4 v[136:139], v178, s[56:57] sc1\n\t"
      "global_load_dwordx4 v[140:143], v179, s[56:57] sc1\n\t"
      "global_load_dwordx4 v[144:147], v180, s[56:57] sc1\n\t"
      "global_load_dwordx4 v[148:151], v181, s[56:57] sc1\n\t"
      "global_load_dwordx4 v[152:155], v182, s[56:57] sc1\n\t"
      "global_load_dwordx4 v[156:159], v183, s[56:57] sc1\n\t"
      "s_waitcnt vmcnt(0)\n\t"
      "v_max3_u32 v234, v128, v129, v130\n\t"
      "v_max3_u32 v234, v234, v131, v132\n\t"
      "v_max3_u32 v234, v234, v133, v134\n\t"
      "v_max3_u32 v234, v234, v135, v136\n\t"
      "v_max3_u32 v234, v234, v137, v138\n\t"
      "v_max3_u32 v234, v234, v139, v140\n\t"
      "v_max3_u32 v234, v234, v141, v142\n\t"
      "v_max3_u32 v234, v234, v143, v144\n\t"
      "v_max3_u32 v234, v234, v145, v146\n\t"
      "v_max3_u32 v234, v234, v147, v148\n\t"
      "v_max3_u32 v234, v234, v149, v150\n\t"
      "v_max3_u32 v234, v234, v151, v152\n\t"
      "v_max3_u32 v234, v234, v153, v154\n\t"
      "v_max3_u32 v234, v234, v155, v156\n\t"
      "v_max3_u32 v234, v234, v157, v158\n\t"
      "v_max_u32_e32 v234, v234, v159\n\t"
      "v_cmp_eq_u32_e32 vcc, -1, v234\n\t"
      "s_cbranch_vccnz NP2L_%=\n\t"
      "NP2X_%=:\n\t"
      "v_mfma_f32_16x16x32_bf16 v[184:187], v[0:3], v[128:131], 0\n\t"
      "v_mfma_f32_16x16x32_bf16 v[188:191], v[4:7], v[128:131], 0\n\t"
      "v_mfma_f32_16x16x32_bf16 v[192:195], v[8:11], v[128:131], 0\n\t"
      "v_mfma_f32_16x16x32_bf16 v[196:199], v[12:15], v[128:131], 0\n\t"
      "v_mfma_f32_16x16x32_bf16 v[184:187], v[16:19], v[132:135], v[184:187]\n\t"
      "v_mfma_f32_16x16x32_bf16 v[188:191], v[20:23], v[132:135], v[188:191]\n\t"
      "v_mfma_f32_16x16x32_bf16 v[192:195], v[24:27], v[132:135], v[192:195]\n\t"
      "v_mfma_f32_16x16x32_bf16 v[196:199], v[28:31], v[132:135], v[196:199]\n\t"
      "v_mfma_f32_16x16x32_bf16 v[184:187], v[32:35], v[136:139], v[184:187]\n\t"
      "v_mfma_f32_16x16x32_bf16 v[188:191], v[36:39], v[136:139], v[188:191]\n\t"
      "v_mfma_f32_16x16x32_bf16 v[192:195], v[40:43], v[136:139], v[192:195]\n\t"
      "v_mfma_f32_16x16x32_bf16 v[196:199], v[44:47], v[136:139], v[196:199]\n\t"
      "v_mfma_f32_16x16x32_bf16 v[184:187], v[48:51], v[140:143], v[184:187]\n\t"
      "v_mfma_f32_16x16x32_bf16 v[188:191], v[52:55], v[140:143], v[188:191]\n\t"
      "v_mfma_f32_16x16x32_bf16 v[192:195], v[56:59], v[140:143], v[192:195]\n\t"
      "v_mfma_f32_16x16x32_bf16 v[196:199], v[60:63], v[140:143], v[196:199]\n\t"
      "v_mfma_f32_16x16x32_bf16 v[184:187], v[64:67], v[144:147], v[184:187]\n\t"
      "v_mfma_f32_16x16x32_bf16 v[188:191], v[68:71], v[144:147], v[188:191]\n\t"
      "v_mfma_f32_16x16x32_bf16 v[192:195], v[72:75], v[144:147], v[192:195]\n\t"
      "v_mfma_f32_16x16x32_bf16 v[196:199], v[76:79], v[144:147], v[196:199]\n\t"
      "v_mfma_f32_16x16x32_bf16 v[184:187], v[80:83], v[148:151], v[184:187]\n\t"
      "v_mfma_f32_16x16x32_bf16 v[188:191], v[84:87], v[148:151], v[188:191]\n\t"
      "v_mfma_f32_16x16x32_bf16 v[192:195], v[88:91], v[148:151], v[192:195]\n\t"
      "v_mfma_f32_16x16x32_bf16 v[196:199], v[92:95], v[148:151], v[196:199]\n\t"
      "v_mfma_f32_16x16x32_bf16 v[184:187], v[96:99], v[152:155], v[184:187]\n\t"
      "v_mfma_f32_16x16x32_bf16 v[188:191], v[100:103], v[152:155], v[188:191]\n\t"
      "v_mfma_f32_16x16x32_bf16 v[192:195], v[104:107], v[152:155], v[192:195]\n\t"
      "v_mfma_f32_16x16x32_bf16 v[184:187], v[112:115], v[156:159], v[184:187]\n\t"
      "v_mfma_f32_16x16x32_bf16 v[188:191], v[116:119], v[156:159], v[188:191]\n\t"
      "v_mfma_f32_16x16x32_bf16 v[192:195], v[120:123], v[156:159], v[192:195]\n\t"
      "global_load_dwordx4 v[128:131], v176, s[58:59] sc1\n\t"
      "global_load_dwordx4 v[132:135], v177, s[58:59] sc1\n\t"
      "global_load_dwordx4 v[136:139], v178, s[58:59] sc1\n\t"
      "global_load_dwordx4 v[140:143], v179, s[58:59] sc1\n\t"
      "global_load_dwordx4 v[144:147], v180, s[58:59] sc1\n\t"
      "global_load_dwordx4 v[148:151], v181, s[58:59] sc1\n\t"
      "s_waitcnt vmcnt(0)\n\t"
      "s_nop 7\n\t"
      "s_branch NP2B_%=\n\t"
      "NP3R_%=:\n\t"
      "s_mov_b32 s51, 0\n\t"
      "NP3L_%=:\n\t"
      "s_add_u32 s51, s51, 1\n\t"
      "s_cmp_lt_u32 s51, 262144\n\t"
      "s_cbranch_scc0 NP3X_%=\n\t"
      "s_waitcnt vmcnt(0)\n\t"
      "s_sleep 1\n\t"
      "global_load_dwordx4 v[128:131], v176, s[58:59] sc1\n\t"
      "global_load_dwordx4 v[132:135], v177, s[58:59] sc1\n\t"
      "global_load_dwordx4 v[136:139], v178, s[58:59] sc1\n\t"
      "global_load_dwordx4 v[140:143], v179, s[58:59] sc1\n\t"
      "global_load_dwordx4 v[144:147], v180, s[58:59] sc1\n\t"
      "global_load_dwordx4 v[148:151], v181, s[58:59] sc1\n\t"
      "global_load_dwordx4 v[152:155], v182, s[58:59] sc1\n\t"
      "global_load_dwordx4 v[156:159], v183, s[58:59] sc1\n\t"
      "s_waitcnt vmcnt(0)\n\t"
      "v_max3_u32 v234, v128, v129, v130\n\t"
      "v_max3_u32 v234, v234, v131, v132\n\t"
      "v_max3_u32 v234, v234, v133, v134\n\t"
      "v_max3_u32 v234, v234, v135, v136\n\t"
      "v_max3_u32 v234, v234, v137, v138\n\t"
      "v_max3_u32 v234, v234, v139, v140\n\t"
      "v_max3_u32 v234, v234, v141, v142\n\t"
      "v_max3_u32 v234, v234, v143, v144\n\t"
      "v_max3_u32 v234, v234, v145, v146\n\t"
      "v_max3_u32 v234, v234, v147, v148\n\t"
      "v_max3_u32 v234, v234, v149, v150\n\t"
      "v_max3_u32 v234, v234, v151, v152\n\t"
      "v_max3_u32 v234, v234, v153, v154\n\t"
      "v_max3_u32 v234, v234, v155, v156\n\t"
      "v_max3_u32 v234, v234, v157, v158\n\t"
      "v_max_u32_e32 v234, v234, v159\n\t"
      "v_cmp_eq_u32_e32 vcc, -1, v234\n\t"
      "s_cbranch_vccnz NP3L_%=\n\t"
      "NP3X_%=:\n\t"
      "v_mfma_f32_16x16x32_bf16 v[200:203], v[0:3], v[128:131], 0\n\t"
      "v_mfma_f32_16x16x32_bf16 v[204:207], v[4:7], v[128:131], 0\n\t"
      "v_mfma_f32_16x16x32_bf16 v[208:211], v[8:11], v[128:131], 0\n\t"
      "v_mfma_f32_16x16x32_bf16 v[212:215], v[12:15], v[128:131], 0\n\t"
      "v_mfma_f32_16x16x32_bf16 v[200:203], v[16:19], v[132:135], v[200:203]\n\t"
      "v_mfma_f32_16x16x32_bf16 v[204:207], v[20:23], v[132:135], v[204:207]\n\t"
      "v_mfma_f32_16x16x32_bf16 v[208:211], v[24:27], v[132:135], v[208:211]\n\t"
      "v_mfma_f32_16x16x32_bf16 v[212:215], v[28:31], v[132:135], v[212:215]\n\t"
      "v_mfma_f32_16x16x32_bf16 v[200:203], v[32:35], v[136:139], v[200:203]\n\t"
      "v_mfma_f32_16x16x32_bf16 v[204:207], v[36:39], v[136:139], v[204:207]\n\t"
      "v_mfma_f32_16x16x32_bf16 v[208:211], v[40:43], v[136:139], v[208:211]\n\t"
      "v_mfma_f32_16x16x32_bf16 v[212:215], v[44:47], v[136:139], v[212:215]\n\t"
      "v_mfma_f32_16x16x32_bf16 v[200:203], v[48:51], v[140:143], v[200:203]\n\t"
      "v_mfma_f32_16x16x32_bf16 v[204:207], v[52:55], v[140:143], v[204:207]\n\t"
      "v_mfma_f32_16x16x32_bf16 v[208:211], v[56:59], v[140:143], v[208:211]\n\t"
      "v_mfma_f32_16x16x32_bf16 v[212:215], v[60:63], v[140:143], v[212:215]\n\t"
      "v_mfma_f32_16x16x32_bf16 v[200:203], v[64:67], v[144:147], v[200:203]\n\t"
      "v_mfma_f32_16x16x32_bf16 v[204:207], v[68:71], v[144:147], v[204:207]\n\t"
      "v_mfma_f32_16x16x32_bf16 v[208:211], v[72:75], v[144:147], v[208:211]\n\t"
      "v_mfma_f32_16x16x32_bf16 v[212:215], v[76:79], v[144:147], v[212:215]\n\t"
      "v_mfma_f32_16x16x32_bf16 v[200:203], v[80:83], v[148:151], v[200:203]\n\t"
      "v_mfma_f32_16x16x32_bf16 v[204:207], v[84:87], v[148:151], v[204:207]\n\t"
      "v_mfma_f32_16x16x32_bf16 v[208:211], v[88:91], v[148:151], v[208:211]\n\t"
      "v_mfma_f32_16x16x32_bf16 v[212:215], v[92:95], v[148:151], v[212:215]\n\t"
      "v_mfma_f32_16x16x32_bf16 v[200:203], v[96:99], v[152:155], v[200:203]\n\t"
      "v_mfma_f32_16x16x32_bf16 v[204:207], v[100:103], v[152:155], v[204:207]\n\t"
      "v_mfma_f32_16x16x32_bf16 v[208:211], v[104:107], v[152:155], v[208:211]\n\t"
      "v_mfma_f32_16x16x32_bf16 v[200:203], v[112:115], v[156:159], v[200:203]\n\t"
      "v_mfma_f32_16x16x32_bf16 v[204:207], v[116:119], v[156:159], v[204:207]\n\t"
      "v_mfma_f32_16x16x32_bf16 v[208:211], v[120:123], v[156:159], v[208:211]\n\t"
      "global_load_dwordx4 v[128:131], v176, s[60:61] sc1\n\t"
      "global_load_dwordx4 v[132:135], v177, s[60:61] sc1\n\t"
      "global_load_dwordx4 v[136:139], v178, s[60:61] sc1\n\t"
      "global_load_dwordx4 v[140:143], v179, s[60:61] sc1\n\t"
      "global_load_dwordx4 v[144:147], v180, s[60:61] sc1\n\t"
      "global_load_dwordx4 v[148:151], v181, s[60:61] sc1\n\t"
      "s_waitcnt vmcnt(0)\n\t"
      "s_nop 7\n\t"
      "s_branch NP3B_%=\n\t"
      "NP4R_%=:\n\t"
      "s_mov_b32 s51, 0\n\t"
      "NP4L_%=:\n\t"
      "s_add_u32 s51, s51, 1\n\t"
      "s_cmp_lt_u32 s51, 262144\n\t"
      "s_cbranch_scc0 NP4X_%=\n\t"
      "s_waitcnt vmcnt(0)\n\t"
      "s_sleep 1\n\t"
      "global_load_dwordx4 v[128:131], v176, s[60:61] sc1\n\t"
      "global_load_dwordx4 v[132:135], v177, s[60:61] sc1\n\t"
      "global_load_dwordx4 v[136:139], v178, s[60:61] sc1\n\t"
      "global_load_dwordx4 v[140:143], v179, s[60:61] sc1\n\t"
      "global_load_dwordx4 v[144:147], v180, s[60:61] sc1\n\t"
      "global_load_dwordx4 v[148:151], v181, s[60:61] sc1\n\t"
      "global_load_dwordx4 v[152:155], v182, s[60:61] sc1\n\t"
      "global_load_dwordx4 v[156:159], v183, s[60:61] sc1\n\t"
      "s_waitcnt vmcnt(0)\n\t"
      "v_max3_u32 v234, v128, v129, v130\n\t"
      "v_max3_u32 v234, v234, v131, v132\n\t"
      "v_max3_u32 v234, v234, v133, v134\n\t"
      "v_max3_u32 v234, v234, v135, v136\n\t"
      "v_max3_u32 v234, v234, v137, v138\n\t"
      "v_max3_u32 v234, v234, v139, v140\n\t"
      "v_max3_u32 v234, v234, v141, v142\n\t"
      "v_max3_u32 v234, v234, v143, v144\n\t"
      "v_max3_u32 v234, v234, v145, v146\n\t"
      "v_max3_u32 v234, v234, v147, v148\n\t"
      "v_max3_u32 v234, v234, v149, v150\n\t"
      "v_max3_u32 v234, v234, v151, v152\n\t"
      "v_max3_u32 v234, v234, v153, v154\n\t"
      "v_max3_u32 v234, v234, v155, v156\n\t"
      "v_max3_u32 v234, v234, v157, v158\n\t"
      "v_max_u32_e32 v234, v234, v159\n\t"
      "v_cmp_eq_u32_e32 vcc, -1, v234\n\t"
      "s_cbranch_vccnz NP4L_%=\n\t"
      "NP4X_%=:\n\t"
      "v_mfma_f32_16x16x32_bf16 v[184:187], v[0:3], v[128:131], 0\n\t"
      "v_mfma_f32_16x16x32_bf16 v[188:191], v[4:7], v[128:131], 0\n\t"
      "v_mfma_f32_16x16x32_bf16 v[192:195], v[8:11], v[128:131], 0\n\t"
      "v_mfma_f32_16x16x32_bf16 v[196:199], v[12:15], v[128:131], 0\n\t"
      "v_mfma_f32_16x16x32_bf16 v[184:187], v[16:19], v[132:135], v[184:187]\n\t"
      "v_mfma_f32_16x16x32_bf16 v[188:191], v[20:23], v[132:135], v[188:191]\n\t"
      "v_mfma_f32_16x16x32_bf16 v[192:195], v[24:27], v[132:135], v[192:195]\n\t"
      "v_mfma_f32_16x16x32_bf16 v[196:199], v[28:31], v[132:135], v[196:199]\n\t"
      "v_mfma_f32_16x16x32_bf16 v[184:187], v[32:35], v[136:139], v[184:187]\n\t"
      "v_mfma_f32_16x16x32_bf16 v[188:191], v[36:39], v[136:139], v[188:191]\n\t"
      "v_mfma_f32_16x16x32_bf16 v[192:195], v[40:43], v[136:139], v[192:195]\n\t"
      "v_mfma_f32_16x16x32_bf16 v[196:199], v[44:47], v[136:139], v[196:199]\n\t"
      "v_mfma_f32_16x16x32_bf16 v[184:187], v[48:51], v[140:143], v[184:187]\n\t"
      "v_mfma_f32_16x16x32_bf16 v[188:191], v[52:55], v[140:143], v[188:191]\n\t"
      "v_mfma_f32_16x16x32_bf16 v[192:195], v[56:59], v[140:143], v[192:195]\n\t"
      "v_mfma_f32_16x16x32_bf16 v[196:199], v[60:63], v[140:143], v[196:199]\n\t"
      "v_mfma_f32_16x16x32_bf16 v[184:187], v[64:67], v[144:147], v[184:187]\n\t"
      "v_mfma_f32_16x16x32_bf16 v[188:191], v[68:71], v[144:147], v[188:191]\n\t"
      "v_mfma_f32_16x16x32_bf16 v[192:195], v[72:75], v[144:147], v[192:195]\n\t"
      "v_mfma_f32_16x16x32_bf16 v[196:199], v[76:79], v[144:147], v[196:199]\n\t"
      "v_mfma_f32_16x16x32_bf16 v[184:187], v[80:83], v[148:151], v[184:187]\n\t"
      "v_mfma_f32_16x16x32_bf16 v[188:191], v[84:87], v[148:151], v[188:191]\n\t"
      "v_mfma_f32_16x16x32_bf16 v[192:195], v[88:91], v[148:151], v[192:195]\n\t"
      "v_mfma_f32_16x16x32_bf16 v[196:199], v[92:95], v[148:151], v[196:199]\n\t"
      "v_mfma_f32_16x16x32_bf16 v[184:187], v[96:99], v[152:155], v[184:187]\n\t"
      "v_mfma_f32_16x16x32_bf16 v[188:191], v[100:103], v[152:155], v[188:191]\n\t"
      "v_mfma_f32_16x16x32_bf16 v[192:195], v[104:107], v[152:155], v[192:195]\n\t"
      "v_mfma_f32_16x16x32_bf16 v[184:187], v[112:115], v[156:159], v[184:187]\n\t"
      "v_mfma_f32_16x16x32_bf16 v[188:191], v[116:119], v[156:159], v[188:191]\n\t"
      "v_mfma_f32_16x16x32_bf16 v[192:195], v[120:123], v[156:159], v[192:195]\n\t"
      "global_load_dwordx4 v[128:131], v176, s[62:63] sc1\n\t"
      "global_load_dwordx4 v[132:135], v177, s[62:63] sc1\n\t"
      "global_load_dwordx4 v[136:139], v178, s[62:63] sc1\n\t"
      "global_load_dwordx4 v[140:143], v179, s[62:63] sc1\n\t"
      "global_load_dwordx4 v[144:147], v180, s[62:63] sc1\n\t"
      "global_load_dwordx4 v[148:151], v181, s[62:63] sc1\n\t"
      "s_waitcnt vmcnt(0)\n\t"
      "s_nop 7\n\t"
      "s_branch NP4B_%=\n\t"
      "NP5R_%=:\n\t"
      "s_mov_b32 s51, 0\n\t"
      "NP5L_%=:\n\t"
      "s_add_u32 s51, s51, 1\n\t"
      "s_cmp_lt_u32 s51, 262144\n\t"
      "s_cbranch_scc0 NP5X_%=\n\t"
      "s_waitcnt vmcnt(0)\n\t"
      "s_sleep 1\n\t"
      "global_load_dwordx4 v[128:131], v176, s[62:63] sc1\n\t"
      "global_load_dwordx4 v[132:135], v177, s[62:63] sc1\n\t"
      "global_load_dwordx4 v[136:139], v178, s[62:63] sc1\n\t"
      "global_load_dwordx4 v[140:143], v179, s[62:63] sc1\n\t"
      "global_load_dwordx4 v[144:147], v180, s[62:63] sc1\n\t"
      "global_load_dwordx4 v[148:151], v181, s[62:63] sc1\n\t"
      "global_load_dwordx4 v[152:155], v182, s[62:63] sc1\n\t"
      "global_load_dwordx4 v[156:159], v183, s[62:63] sc1\n\t"
      "s_waitcnt vmcnt(0)\n\t"
      "v_max3_u32 v234, v128, v129, v130\n\t"
      "v_max3_u32 v234, v234, v131, v132\n\t"
      "v_max3_u32 v234, v234, v133, v134\n\t"
      "v_max3_u32 v234, v234, v135, v136\n\t"
      "v_max3_u32 v234, v234, v137, v138\n\t"
      "v_max3_u32 v234, v234, v139, v140\n\t"
      "v_max3_u32 v234, v234, v141, v142\n\t"
      "v_max3_u32 v234, v234, v143, v144\n\t"
      "v_max3_u32 v234, v234, v145, v146\n\t"
      "v_max3_u32 v234, v234, v147, v148\n\t"
      "v_max3_u32 v234, v234, v149, v150\n\t"
      "v_max3_u32 v234, v234, v151, v152\n\t"
      "v_max3_u32 v234, v234, v153, v154\n\t"
      "v_max3_u32 v234, v234, v155, v156\n\t"
      "v_max3_u32 v234, v234, v157, v158\n\t"
      "v_max_u32_e32 v234, v234, v159\n\t"
      "v_cmp_eq_u32_e32 vcc, -1, v234\n\t"
      "s_cbranch_vccnz NP5L_%=\n\t"
      "NP5X_%=:\n\t"
      "v_mfma_f32_16x16x32_bf16 v[200:203], v[0:3], v[128:131], 0\n\t"
      "v_mfma_f32_16x16x32_bf16 v[204:207], v[4:7], v[128:131], 0\n\t"
      "v_mfma_f32_16x16x32_bf16 v[208:211], v[8:11], v[128:131], 0\n\t"
      "v_mfma_f32_16x16x32_bf16 v[212:215], v[12:15], v[128:131], 0\n\t"
      "v_mfma_f32_16x16x32_bf16 v[200:203], v[16:19], v[132:135], v[200:203]\n\t"
      "v_mfma_f32_16x16x32_bf16 v[204:207], v[20:23], v[132:135], v[204:207]\n\t"
      "v_mfma_f32_16x16x32_bf16 v[208:211], v[24:27], v[132:135], v[208:211]\n\t"
      "v_mfma_f32_16x16x32_bf16 v[212:215], v[28:31], v[132:135], v[212:215]\n\t"
      "v_mfma_f32_16x16x32_bf16 v[200:203], v[32:35], v[136:139], v[200:203]\n\t"
      "v_mfma_f32_16x16x32_bf16 v[204:207], v[36:39], v[136:139], v[204:207]\n\t"
      "v_mfma_f32_16x16x32_bf16 v[208:211], v[40:43], v[136:139], v[208:211]\n\t"
      "v_mfma_f32_16x16x32_bf16 v[212:215], v[44:47], v[136:139], v[212:215]\n\t"
      "v_mfma_f32_16x16x32_bf16 v[200:203], v[48:51], v[140:143], v[200:203]\n\t"
      "v_mfma_f32_16x16x32_bf16 v[204:207], v[52:55], v[140:143], v[204:207]\n\t"
      "v_mfma_f32_16x16x32_bf16 v[208:211], v[56:59], v[140:143], v[208:211]\n\t"
      "v_mfma_f32_16x16x32_bf16 v[212:215], v[60:63], v[140:143], v[212:215]\n\t"
      "v_mfma_f32_16x16x32_bf16 v[200:203], v[64:67], v[144:147], v[200:203]\n\t"
      "v_mfma_f32_16x16x32_bf16 v[204:207], v[68:71], v[144:147], v[204:207]\n\t"
      "v_mfma_f32_16x16x32_bf16 v[208:211], v[72:75], v[144:147], v[208:211]\n\t"
      "v_mfma_f32_16x16x32_bf16 v[212:215], v[76:79], v[144:147], v[212:215]\n\t"
      "v_mfma_f32_16x16x32_bf16 v[200:203], v[80:83], v[148:151], v[200:203]\n\t"
      "v_mfma_f32_16x16x32_bf16 v[204:207], v[84:87], v[148:151], v[204:207]\n\t"
      "v_mfma_f32_16x16x32_bf16 v[208:211], v[88:91], v[148:151], v[208:211]\n\t"
      "v_mfma_f32_16x16x32_bf16 v[212:215], v[92:95], v[148:151], v[212:215]\n\t"
      "v_mfma_f32_16x16x32_bf16 v[200:203], v[96:99], v[152:155], v[200:203]\n\t"
      "v_mfma_f32_16x16x32_bf16 v[204:207], v[100:103], v[152:155], v[204:207]\n\t"
      "v_mfma_f32_16x16x32_bf16 v[208:211], v[104:107], v[152:155], v[208:211]\n\t"
      "v_mfma_f32_16x16x32_bf16 v[200:203], v[112:115], v[156:159], v[200:203]\n\t"
      "v_mfma_f32_16x16x32_bf16 v[204:207], v[116:119], v[156:159], v[204:207]\n\t"
      "v_mfma_f32_16x16x32_bf16 v[208:211], v[120:123], v[156:159], v[208:211]\n\t"
      "global_load_dwordx4 v[128:131], v176, s[64:65] sc1\n\t"
      "global_load_dwordx4 v[132:135], v177, s[64:65] sc1\n\t"
      "global_load_dwordx4 v[136:139], v178, s[64:65] sc1\n\t"
      "global_load_dwordx4 v[140:143], v179, s[64:65] sc1\n\t"
      "global_load_dwordx4 v[144:147], v180, s[64:65] sc1\n\t"
      "global_load_dwordx4 v[148:151], v181, s[64:65] sc1\n\t"
      "s_waitcnt vmcnt(0)\n\t"
      "s_nop 7\n\t"
      "s_branch NP5B_%=\n\t"
      "NP6R_%=:\n\t"
      "s_mov_b32 s51, 0\n\t"
      "NP6L_%=:\n\t"
      "s_add_u32 s51, s51, 1\n\t"
      "s_cmp_lt_u32 s51, 262144\n\t"
      "s_cbranch_scc0 NP6X_%=\n\t"
      "s_waitcnt vmcnt(0)\n\t"
      "s_sleep 1\n\t"
      "global_load_dwordx4 v[128:131], v176, s[64:65] sc1\n\t"
      "global_load_dwordx4 v[132:135], v177, s[64:65] sc1\n\t"
      "global_load_dwordx4 v[136:139], v178, s[64:65] sc1\n\t"
      "global_load_dwordx4 v[140:143], v179, s[64:65] sc1\n\t"
      "global_load_dwordx4 v[144:147], v180, s[64:65] sc1\n\t"
      "global_load_dwordx4 v[148:151], v181, s[64:65] sc1\n\t"
      "global_load_dwordx4 v[152:155], v182, s[64:65] sc1\n\t"
      "global_load_dwordx4 v[156:159], v183, s[64:65] sc1\n\t"
      "s_waitcnt vmcnt(0)\n\t"
      "v_max3_u32 v234, v128, v129, v130\n\t"
      "v_max3_u32 v234, v234, v131, v132\n\t"
      "v_max3_u32 v234, v234, v133, v134\n\t"
      "v_max3_u32 v234, v234, v135, v136\n\t"
      "v_max3_u32 v234, v234, v137, v138\n\t"
      "v_max3_u32 v234, v234, v139, v140\n\t"
      "v_max3_u32 v234, v234, v141, v142\n\t"
      "v_max3_u32 v234, v234, v143, v144\n\t"
      "v_max3_u32 v234, v234, v145, v146\n\t"
      "v_max3_u32 v234, v234, v147, v148\n\t"
      "v_max3_u32 v234, v234, v149, v150\n\t"
      "v_max3_u32 v234, v234, v151, v152\n\t"
      "v_max3_u32 v234, v234, v153, v154\n\t"
      "v_max3_u32 v234, v234, v155, v156\n\t"
      "v_max3_u32 v234, v234, v157, v158\n\t"
      "v_max_u32_e32 v234, v234, v159\n\t"
      "v_cmp_eq_u32_e32 vcc, -1, v234\n\t"
      "s_cbranch_vccnz NP6L_%=\n\t"
      "NP6X_%=:\n\t"
      "v_mfma_f32_16x16x32_bf16 v[184:187], v[0:3], v[128:131], 0\n\t"
      "v_mfma_f32_16x16x32_bf16 v[188:191], v[4:7], v[128:131], 0\n\t"
      "v_mfma_f32_16x16x32_bf16 v[192:195], v[8:11], v[128:131], 0\n\t"
      "v_mfma_f32_16x16x32_bf16 v[196:199], v[12:15], v[128:131], 0\n\t"
      "v_mfma_f32_16x16x32_bf16 v[184:187], v[16:19], v[132:135], v[184:187]\n\t"
      "v_mfma_f32_16x16x32_bf16 v[188:191], v[20:23], v[132:135], v[188:191]\n\t"
      "v_mfma_f32_16x16x32_bf16 v[192:195], v[24:27], v[132:135], v[192:195]\n\t"
      "v_mfma_f32_16x16x32_bf16 v[196:199], v[28:31], v[132:135], v[196:199]\n\t"
      "v_mfma_f32_16x16x32_bf16 v[184:187], v[32:35], v[136:139], v[184:187]\n\t"
      "v_mfma_f32_16x16x32_bf16 v[188:191], v[36:39], v[136:139], v[188:191]\n\t"
      "v_mfma_f32_16x16x32_bf16 v[192:195], v[40:43], v[136:139], v[192:195]\n\t"
      "v_mfma_f32_16x16x32_bf16 v[196:199], v[44:47], v[136:139], v[196:199]\n\t"
      "v_mfma_f32_16x16x32_bf16 v[184:187], v[48:51], v[140:143], v[184:187]\n\t"
      "v_mfma_f32_16x16x32_bf16 v[188:191], v[52:55], v[140:143], v[188:191]\n\t"
      "v_mfma_f32_16x16x32_bf16 v[192:195], v[56:59], v[140:143], v[192:195]\n\t"
      "v_mfma_f32_16x16x32_bf16 v[196:199], v[60:63], v[140:143], v[196:199]\n\t"
      "v_mfma_f32_16x16x32_bf16 v[184:187], v[64:67], v[144:147], v[184:187]\n\t"
      "v_mfma_f32_16x16x32_bf16 v[188:191], v[68:71], v[144:147], v[188:191]\n\t"
      "v_mfma_f32_16x16x32_bf16 v[192:195], v[72:75], v[144:147], v[192:195]\n\t"
      "v_mfma_f32_16x16x32_bf16 v[196:199], v[76:79], v[144:147], v[196:199]\n\t"
      "v_mfma_f32_16x16x32_bf16 v[184:187], v[80:83], v[148:151], v[184:187]\n\t"
      "v_mfma_f32_16x16x32_bf16 v[188:191], v[84:87], v[148:151], v[188:191]\n\t"
      "v_mfma_f32_16x16x32_bf16 v[192:195], v[88:91], v[148:151], v[192:195]\n\t"
      "v_mfma_f32_16x16x32_bf16 v[196:199], v[92:95], v[148:151], v[196:199]\n\t"
      "v_mfma_f32_16x16x32_bf16 v[184:187], v[96:99], v[152:155], v[184:187]\n\t"
      "v_mfma_f32_16x16x32_bf16 v[188:191], v[100:103], v[152:155], v[188:191]\n\t"
      "v_mfma_f32_16x16x32_bf16 v[192:195], v[104:107], v[152:155], v[192:195]\n\t"
      "v_mfma_f32_16x16x32_bf16 v[184:187], v[112:115], v[156:159], v[184:187]\n\t"
      "v_mfma_f32_16x16x32_bf16 v[188:191], v[116:119], v[156:159], v[188:191]\n\t"
      "v_mfma_f32_16x16x32_bf16 v[192:195], v[120:123], v[156:159], v[192:195]\n\t"
      "s_waitcnt vmcnt(0)\n\t"
      "s_nop 7\n\t"
      "s_branch NP6B_%=\n\t"
      "s_nop 0\n\t"
      "s_nop 0\n\t"
      "s_nop 0\n\t"
      "s_nop 0\n\t"
      "s_nop 0\n\t"
      "s_nop 0\n\t"
      "s_nop 0\n\t"
      "s_nop 0\n\t"
      "s_nop 0\n\t"
      "s_nop 0\n\t"
      "s_nop 0\n\t"
      "s_nop 0\n\t"
      "s_nop 0\n\t"
      "s_nop 0\n\t"
      "s_nop 0\n\t"
      "s_nop 0\n\t"
      "s_nop 0\n\t"
      "s_nop 0\n\t"
      "s_nop 0\n\t"
      "s_nop 0\n\t"
      "s_nop 0\n\t"
      "s_nop 0\n\t"
      "s_nop 0\n\t"
      "s_nop 0\n\t"
      "s_nop 0\n\t"
      "s_nop 0\n\t"
      "s_nop 0\n\t"
      "s_nop 0\n\t"
      "s_nop 0\n\t"
      "s_nop 0\n\t"
      "s_nop 0\n\t"
      "s_nop 0\n\t"
      "s_nop 0\n\t"
      "s_nop 0\n\t"
      "s_nop 0\n\t"
      "s_nop 0\n\t"
      "s_nop 0\n\t"
      "s_nop 0\n\t"
      "s_nop 0\n\t"
      "s_nop 0\n\t"
      "s_nop 0\n\t"
      "s_nop 0\n\t"
      "s_nop 0\n\t"
      "s_nop 0\n\t"
      "s_nop 0\n\t"
      "s_nop 0\n\t"
      "s_nop 0\n\t"
      "s_nop 0\n\t"
      "s_nop 0\n\t"
      "s_nop 0\n\t"
      "s_nop 0\n\t"
      "s_nop 0\n\t"
      "s_nop 0\n\t"
      "s_nop 0\n\t"
      "s_nop 0\n\t"
      "s_nop 0\n\t"
      "s_nop 0\n\t"
      "s_nop 0\n\t"
      "s_nop 0\n\t"
      "s_nop 0\n\t"
      "s_nop 0\n\t"
      "s_nop 0\n\t"
      "s_nop 0\n\t"
      "s_nop 0\n\t"
      "s_nop 0\n\t"
      "s_nop 0\n\t"
      "s_nop 0\n\t"
      "s_nop 0\n\t"
      "s_nop 0\n\t"
      "s_nop 0\n\t"
      "s_nop 0\n\t"
      "s_nop 0\n\t"
      "s_nop 0\n\t"
      "s_nop 0\n\t"
      "s_nop 0\n\t"
      "s_nop 0\n\t"
      "s_nop 0\n\t"
      "s_nop 0\n\t"
      "s_nop 0\n\t"
      "s_nop 0\n\t"
      "s_nop 0\n\t"
      "s_nop 0\n\t"
      "s_nop 0\n\t"
      "s_nop 0\n\t"
      "s_nop 0\n\t"
      "s_nop 0\n\t"
      "s_nop 0\n\t"
      "s_nop 0\n\t"
      "s_nop 0\n\t"
      "s_nop 0\n\t"
      "s_nop 0\n\t"
      "s_nop 0\n\t"
      "s_nop 0\n\t"
      "s_nop 0\n\t"
      "s_nop 0\n\t"
      "s_nop 0\n\t"
      "s_nop 0\n\t"
      "s_nop 0\n\t"
      "s_nop 0\n\t"
      "s_nop 0\n\t"
      "s_nop 0\n\t"
      "s_nop 0\n\t"
      "s_nop 0\n\t"
      "s_nop 0\n\t"
      "s_nop 0\n\t"
      "s_nop 0\n\t"
      "s_nop 0\n\t"
      "s_nop 0\n\t"
      "s_nop 0\n\t"
      "s_nop 0\n\t"
      "s_nop 0\n\t"
      "s_nop 0\n\t"
      "s_nop 0\n\t"
      "s_nop 0\n\t"
      "s_nop 0\n\t"
      "s_nop 0\n\t"
      "s_nop 0\n\t"
      "s_nop 0\n\t"
      "s_nop 0\n\t"
      "s_nop 0\n\t"
      "s_nop 0\n\t"
      "s_nop 0\n\t"
      "s_nop 0\n\t"
      "s_nop 0\n\t"
      "s_nop 0\n\t"
      "s_nop 0\n\t"
      "s_nop 0\n\t"
      "s_nop 0\n\t"
      "s_nop 0\n\t"
      "s_nop 0\n\t"
      "s_nop 0\n\t"
      "s_nop 0\n\t"
      "s_nop 0\n\t"
      "s_nop 0\n\t"
      "s_nop 0\n\t"
      "s_nop 0\n\t"
      "s_nop 0\n\t"
      "s_nop 0\n\t"
      "s_nop 0\n\t"
      "s_nop 0\n\t"
      "s_nop 0\n\t"
      "s_nop 0\n\t"
      "s_nop 0\n\t"
      "s_nop 0\n\t"
      "s_nop 0\n\t"
      "s_nop 0\n\t"
      "s_nop 0\n\t"
      "s_nop 0\n\t"
      "s_nop 0\n\t"
      "s_nop 0\n\t"
      "s_nop 0\n\t"
      "s_nop 0\n\t"
      "s_nop 0\n\t"
      "s_nop 0\n\t"
      "s_nop 0\n\t"
      "s_nop 0\n\t"
      "s_nop 0\n\t"
      "s_nop 0\n\t"
      "s_nop 0\n\t"
      "s_nop 0\n\t"
      "s_nop 0\n\t"
      "s_nop 0\n\t"
      "s_nop 0\n\t"
      "s_nop 0\n\t"
      "s_nop 0\n\t"
      "s_nop 0\n\t"
      "s_nop 0\n\t"
      "s_nop 0\n\t"
      "s_nop 0\n\t"
      "s_nop 0\n\t"
      "s_nop 0\n\t"
      "s_nop 0\n\t"
      "s_nop 0\n\t"
      "s_nop 0\n\t"
      "s_nop 0\n\t"
      "s_nop 0\n\t"
      "s_nop 0\n\t"
      "s_nop 0\n\t"
      "s_nop 0\n\t"
      "s_nop 0\n\t"
      "s_nop 0\n\t"
      "s_nop 0\n\t"
      "s_nop 0\n\t"
      "s_nop 0\n\t"
      "s_nop 0\n\t"
      "s_nop 0\n\t"
      "s_nop 0\n\t"
      "s_nop 0\n\t"
      "s_nop 0\n\t"
      "s_nop 0\n\t"
      "s_nop 0\n\t"
      "s_nop 0\n\t"
      "s_nop 0\n\t"
      "s_nop 0\n\t"
      "s_nop 0\n\t"
      "s_nop 0\n\t"
      "s_nop 0\n\t"
      "s_nop 0\n\t"
      "s_nop 0\n\t"
      "s_nop 0\n\t"
      "s_nop 0\n\t"
      "s_nop 0\n\t"
      "s_nop 0\n\t"
      "s_nop 0\n\t"
      "s_nop 0\n\t"
      "s_nop 0\n\t"
      "s_nop 0\n\t"
      "s_nop 0\n\t"
      "s_nop 0\n\t"
      "s_nop 0\n\t"
      "s_nop 0\n\t"
      "s_nop 0\n\t"
      "s_nop 0\n\t"
      "s_nop 0\n\t"
      "s_nop 0\n\t"
      "s_nop 0\n\t"
      "s_nop 0\n\t"
      "s_nop 0\n\t"
      "s_nop 0\n\t"
      "s_nop 0\n\t"
      "s_nop 0\n\t"
      "s_nop 0\n\t"
      "s_nop 0\n\t"
      "s_nop 0\n\t"
      "s_nop 0\n\t"
      "s_nop 0\n\t"
      "s_nop 0\n\t"
      "s_nop 0\n\t"
      "s_nop 0\n\t"
      "s_nop 0\n\t"
      "s_nop 0\n\t"
      "s_nop 0\n\t"
      "s_nop 0\n\t"
      "s_nop 0\n\t"
      "s_nop 0\n\t"
      "s_nop 0\n\t"
      "s_nop 0\n\t"
      "s_nop 0\n\t"
      "s_nop 0\n\t"
      "s_nop 0\n\t"
      "s_nop 0\n\t"
      "s_nop 0\n\t"
      "s_nop 0\n\t"
      "s_nop 0\n\t"
      "s_nop 0\n\t"
      "s_nop 0\n\t"
      "s_nop 0\n\t"
      "s_nop 0\n\t"
      "s_nop 0\n\t"
      "s_nop 0\n\t"
      "s_nop 0\n\t"
      "s_nop 0\n\t"
      "s_nop 0\n\t"
      "s_nop 0\n\t"
      "s_nop 0\n\t"
      "s_nop 0\n\t"
      "s_nop 0\n\t"
      "s_nop 0\n\t"
      "s_nop 0\n\t"
      "s_nop 0\n\t"
      "s_nop 0\n\t"
      "s_nop 0\n\t"
      "s_nop 0\n\t"
      "s_nop 0\n\t"
      "s_nop 0\n\t"
      "s_nop 0\n\t"
      "s_nop 0\n\t"
      "s_nop 0\n\t"
      "s_nop 0\n\t"
      "s_nop 0\n\t"
      "s_nop 0\n\t"
      "s_nop 0\n\t"
      "s_nop 0\n\t"
      "s_nop 0\n\t"
      "s_nop 0\n\t"
      "s_nop 0\n\t"
      "s_nop 0\n\t"
      "s_nop 0\n\t"
      "s_nop 0\n\t"
      "s_nop 0\n\t"
      "s_nop 0\n\t"
      "s_nop 0\n\t"
      "s_nop 0\n\t"
      "s_nop 0\n\t"
      "s_nop 0\n\t"
      "s_nop 0\n\t"
      "s_nop 0\n\t"
      "s_nop 0\n\t"
      "s_nop 0\n\t"
      "s_nop 0\n\t"
      "s_nop 0\n\t"
      "s_nop 0\n\t"
      "s_nop 0\n\t"
      "s_nop 0\n\t"
      "s_nop 0\n\t"
      "s_nop 0\n\t"
      "s_nop 0\n\t"
      "s_nop 0\n\t"
      "s_nop 0\n\t"
      "s_nop 0\n\t"
      "s_nop 0\n\t"
      "s_nop 0\n\t"
      "s_nop 0\n\t"
      "s_nop 0\n\t"
      "s_nop 0\n\t"
      "s_nop 0\n\t"
      "s_nop 0\n\t"
      "s_nop 0\n\t"
      "s_nop 0\n\t"
      "s_nop 0\n\t"
      "s_nop 0\n\t"
      "s_nop 0\n\t"
      "s_nop 0\n\t"
      "s_nop 0\n\t"
      "s_nop 0\n\t"
      "s_nop 0\n\t"
      "s_nop 0\n\t"
      "s_nop 0\n\t"
      "s_nop 0\n\t"
      "s_nop 0\n\t"
      "s_nop 0\n\t"
      "s_nop 0\n\t"
      "s_nop 0\n\t"
      "s_nop 0\n\t"
      "s_nop 0\n\t"
      "s_nop 0\n\t"
      "s_nop 0\n\t"
      "s_nop 0\n\t"
      "s_nop 0\n\t"
      "s_nop 0\n\t"
      "s_nop 0\n\t"
      "s_nop 0\n\t"
      "s_nop 0\n\t"
      "s_nop 0\n\t"
      "s_nop 0\n\t"
      "s_nop 0\n\t"
      "s_nop 0\n\t"
      "s_nop 0\n\t"
      "s_nop 0\n\t"
      "s_nop 0\n\t"
      "s_nop 0\n\t"
      "s_nop 0\n\t"
      "s_nop 0\n\t"
      "s_nop 0\n\t"
      "s_nop 0\n\t"
      "s_nop 0\n\t"
      "s_nop 0\n\t"
      "s_nop 0\n\t"
      "s_nop 0\n\t"
      "s_nop 0\n\t"
      "s_nop 0\n\t"
      "s_nop 0\n\t"
      "s_nop 0\n\t"
      "s_nop 0\n\t"
      "s_nop 0\n\t"
      "s_nop 0\n\t"
      "s_nop 0\n\t"
      "s_nop 0\n\t"
      "s_nop 0\n\t"
      "s_nop 0\n\t"
      "s_nop 0\n\t"
      "s_nop 0\n\t"
      "s_nop 0\n\t"
      "s_nop 0\n\t"
      "s_nop 0\n\t"
      "s_nop 0\n\t"
      "s_nop 0\n\t"
      "s_nop 0\n\t"
      "s_nop 0\n\t"
      "s_nop 0\n\t"
      "s_nop 0\n\t"
      "s_nop 0\n\t"
      "s_nop 0\n\t"
      "s_nop 0\n\t"
      "s_nop 0\n\t"
      "s_nop 0\n\t"
      "s_nop 0\n\t"
      "s_nop 0\n\t"
      "s_nop 0\n\t"
      "s_nop 0\n\t"
      "s_nop 0\n\t"
      "s_nop 0\n\t"
      "s_nop 0\n\t"
      "s_nop 0\n\t"
      "s_nop 0\n\t"
      "s_nop 0\n\t"
      "s_nop 0\n\t"
      "s_nop 0\n\t"
      "s_nop 0\n\t"
      "s_nop 0\n\t"
      "s_nop 0\n\t"
      "s_nop 0\n\t"
      "s_nop 0\n\t"
      "s_nop 0\n\t"
      "s_nop 0\n\t"
      "s_nop 0\n\t"
      "s_nop 0\n\t"
      "s_nop 0\n\t"
      "s_nop 0\n\t"
      "s_nop 0\n\t"
      "s_nop 0\n\t"
      "s_nop 0\n\t"
      "s_nop 0\n\t"
      "s_nop 0\n\t"
      "s_nop 0\n\t"
      "s_nop 0\n\t"
      "s_nop 0\n\t"
      "s_nop 0\n\t"
      "s_nop 0\n\t"
      "s_nop 0\n\t"
      "s_nop 0\n\t"
      "s_nop 0\n\t"
      "s_nop 0\n\t"
      "s_nop 0\n\t"
      "s_nop 0\n\t"
      "s_nop 0\n\t"
      "s_nop 0\n\t"
      "s_nop 0\n\t"
      "s_nop 0\n\t"
      "s_nop 0\n\t"
      "s_nop 0\n\t"
      "s_nop 0\n\t"
      "s_nop 0\n\t"
      "s_nop 0\n\t"
      "s_nop 0\n\t"
      "s_nop 0\n\t"
      "s_nop 0\n\t"
      "s_nop 0\n\t"
      "s_nop 0\n\t"
      "s_nop 0\n\t"
      "s_nop 0\n\t"
      "s_nop 0\n\t"
      "s_nop 0\n\t"
      "s_nop 0\n\t"
      "s_nop 0\n\t"
      "s_nop 0\n\t"
      "s_nop 0\n\t"
      "s_nop 0\n\t"
      "s_nop 0\n\t"
      "s_nop 0\n\t"
      "s_nop 0\n\t"
      "s_nop 0\n\t"
      "s_nop 0\n\t"
      "s_nop 0\n\t"
      "s_nop 0\n\t"
      "s_nop 0\n\t"
      "s_nop 0\n\t"
      "s_nop 0\n\t"
      "s_nop 0\n\t"
      "s_nop 0\n\t"
      "s_nop 0\n\t"
      "s_nop 0\n\t"
      "s_nop 0\n\t"
      "s_nop 0\n\t"
      "s_nop 0\n\t"
      "s_nop 0\n\t"
      "s_nop 0\n\t"
      "s_nop 0\n\t"
      "s_nop 0\n\t"
      "s_nop 0\n\t"
      "s_nop 0\n\t"
      "s_nop 0\n\t"
      "s_nop 0\n\t"
      "s_nop 0\n\t"
      "s_nop 0\n\t"
      "s_nop 0\n\t"
      "s_nop 0\n\t"
      "s_nop 0\n\t"
      "s_nop 0\n\t"
      "s_nop 0\n\t"
      "s_nop 0\n\t"
      "s_nop 0\n\t"
      "s_nop 0\n\t"
      "s_nop 0\n\t"
      "s_nop 0\n\t"
      "s_nop 0\n\t"
      "s_nop 0\n\t"
      "s_nop 0\n\t"
      "s_nop 0\n\t"
      "s_nop 0\n\t"
      "s_nop 0\n\t"
      "s_nop 0\n\t"
      "s_nop 0\n\t"
      "s_nop 0\n\t"
      "s_nop 0\n\t"
      "s_nop 0\n\t"
      "s_nop 0\n\t"
      "s_nop 0\n\t"
      "s_nop 0\n\t"
      "s_nop 0\n\t"
      "s_nop 0\n\t"
      "s_nop 0\n\t"
      "s_nop 0\n\t"
      "s_nop 0\n\t"
      "s_nop 0\n\t"
      "s_nop 0\n\t"
      "s_nop 0\n\t"
      "s_nop 0\n\t"
      "s_nop 0\n\t"
      "s_nop 0\n\t"
      "s_nop 0\n\t"
      "s_nop 0\n\t"
      "s_nop 0\n\t"
      "s_nop 0\n\t"
      "s_nop 0\n\t"
      "s_nop 0\n\t"
      "s_nop 0\n\t"
      "s_nop 0\n\t"
      "s_nop 0\n\t"
      "s_nop 0\n\t"
      "s_nop 0\n\t"
      "s_nop 0\n\t"
      "s_nop 0\n\t"
      "s_nop 0\n\t"
      "s_nop 0\n\t"
      "s_nop 0\n\t"
      "s_nop 0\n\t"
      "s_nop 0\n\t"
      "s_nop 0\n\t"
      "s_nop 0\n\t"
      "s_nop 0\n\t"
      "s_nop 0\n\t"
      "s_nop 0\n\t"
      "s_nop 0\n\t"
      "s_nop 0\n\t"
      "s_nop 0\n\t"
      "s_nop 0\n\t"
      "s_nop 0\n\t"
      "s_nop 0\n\t"
      "s_nop 0\n\t"
      "s_nop 0\n\t"
      "s_nop 0\n\t"
      "s_nop 0\n\t"
      "s_nop 0\n\t"
      "s_nop 0\n\t"
      "s_nop 0\n\t"
      "s_nop 0\n\t"
      "s_nop 0\n\t"
      "s_nop 0\n\t"
      "s_nop 0\n\t"
      "s_nop 0\n\t"
      "s_nop 0\n\t"
      "s_nop 0\n\t"
      "s_nop 0\n\t"
      "s_nop 0\n\t"
      "s_nop 0\n\t"
      "s_nop 0\n\t"
      "s_nop 0\n\t"
      "s_nop 0\n\t"
      "s_nop 0\n\t"
      "s_nop 0\n\t"
      "s_nop 0\n\t"
      "s_nop 0\n\t"
      "s_nop 0\n\t"
      "s_nop 0\n\t"
      "s_nop 0\n\t"
      "s_nop 0\n\t"
      "s_nop 0\n\t"
      "s_nop 0\n\t"
      "s_nop 0\n\t"
      "s_nop 0\n\t"
      "s_nop 0\n\t"
      "s_nop 0\n\t"
      "s_nop 0\n\t"
      "s_nop 0\n\t"
      "s_nop 0\n\t"
      "s_nop 0\n\t"
      "s_nop 0\n\t"
      "s_nop 0\n\t"
      "s_nop 0\n\t"
      "s_nop 0\n\t"
      "s_nop 0\n\t"
      "s_nop 0\n\t"
      "s_nop 0\n\t"
      "s_nop 0\n\t"
      "s_nop 0\n\t"
      "s_nop 0\n\t"
      "s_nop 0\n\t"
      "s_nop 0\n\t"
      "s_nop 0\n\t"
      "s_nop 0\n\t"
      "s_nop 0\n\t"
      "s_nop 0\n\t"
      "s_nop 0\n\t"
      "s_nop 0\n\t"
      "s_nop 0\n\t"
      "s_nop 0\n\t"
      "s_nop 0\n\t"
      "s_nop 0\n\t"
      "s_nop 0\n\t"
      "s_nop 0\n\t"
      "s_nop 0\n\t"
      "s_nop 0\n\t"
      "s_nop 0\n\t"
      "s_nop 0\n\t"
      "s_nop 0\n\t"
      "s_nop 0\n\t"
      "s_nop 0\n\t"
      "s_nop 0\n\t"
      "s_nop 0\n\t"
      "s_nop 0\n\t"
      "s_nop 0\n\t"
      "s_nop 0\n\t"
      "s_nop 0\n\t"
      "s_nop 0\n\t"
      "s_nop 0\n\t"
      "s_nop 0\n\t"
      "s_nop 0\n\t"
      "s_nop 0\n\t"
      "s_nop 0\n\t"
      "s_nop 0\n\t"
      "s_nop 0\n\t"
      "s_nop 0\n\t"
      "s_nop 0\n\t"
      "s_nop 0\n\t"
      "s_nop 0\n\t"
      "s_nop 0\n\t"
      "s_nop 0\n\t"
      "s_nop 0\n\t"
      "s_nop 0\n\t"
      "s_nop 0\n\t"
      "s_nop 0\n\t"
      "s_nop 0\n\t"
      "s_nop 0\n\t"
      "s_nop 0\n\t"
      "s_nop 0\n\t"
      "s_nop 0\n\t"
      "s_nop 0\n\t"
      "s_nop 0\n\t"
      "s_nop 0\n\t"
      "s_nop 0\n\t"
      "s_nop 0\n\t"
      "s_nop 0\n\t"
      "s_nop 0\n\t"
      "s_nop 0\n\t"
      "s_nop 0\n\t"
      "s_nop 0\n\t"
      "s_nop 0\n\t"
      "s_nop 0\n\t"
      "s_nop 0\n\t"
      "s_nop 0\n\t"
      "s_nop 0\n\t"
      "s_nop 0\n\t"
      "s_nop 0\n\t"
      "s_nop 0\n\t"
      "s_nop 0\n\t"
      "s_nop 0\n\t"
      "s_nop 0\n\t"
      "s_nop 0\n\t"
      "s_nop 0\n\t"
      "s_nop 0\n\t"
      "s_nop 0\n\t"
      "s_nop 0\n\t"
      "s_nop 0\n\t"
      "s_nop 0\n\t"
      "s_nop 0\n\t"
      "s_nop 0\n\t"
      "s_nop 0\n\t"
      "s_nop 0\n\t"
      "s_nop 0\n\t"
      "s_nop 0\n\t"
      "s_nop 0\n\t"
      "s_nop 0\n\t"
      "s_nop 0\n\t"
      "s_nop 0\n\t"
      "s_nop 0\n\t"
      "s_nop 0\n\t"
      "s_nop 0\n\t"
      "s_nop 0\n\t"
      "s_nop 0\n\t"
      "s_nop 0\n\t"
      "s_nop 0\n\t"
      "s_nop 0\n\t"
      "s_nop 0\n\t"
      "s_nop 0\n\t"
      "s_nop 0\n\t"
      "s_nop 0\n\t"
      "s_nop 0\n\t"
      "s_nop 0\n\t"
      "s_nop 0\n\t"
      "s_nop 0\n\t"
      "s_nop 0\n\t"
      "s_nop 0\n\t"
      "s_nop 0\n\t"
      "s_nop 0\n\t"
      "s_nop 0\n\t"
      "s_nop 0\n\t"
      "s_nop 0\n\t"
      "s_nop 0\n\t"
      "s_nop 0\n\t"
      "s_nop 0\n\t"
      "s_nop 0\n\t"
      "s_nop 0\n\t"
      "s_nop 0\n\t"
      "s_nop 0\n\t"
      "s_nop 0\n\t"
      "s_nop 0\n\t"
      "s_nop 0\n\t"
      "s_nop 0\n\t"
      "s_nop 0\n\t"
      "s_nop 0\n\t"
      "s_nop 0\n\t"
      "s_nop 0\n\t"
      "s_nop 0\n\t"
      "s_nop 0\n\t"
      "s_nop 0\n\t"
      "s_nop 0\n\t"
      "s_nop 0\n\t"
      "s_nop 0\n\t"
      "s_nop 0\n\t"
      "s_nop 0\n\t"
      "s_nop 0\n\t"
      "s_nop 0\n\t"
      "s_nop 0\n\t"
      "s_nop 0\n\t"
      "s_nop 0\n\t"
      "s_nop 0\n\t"
      "s_nop 0\n\t"
      "s_nop 0\n\t"
      "s_nop 0\n\t"
      "s_nop 0\n\t"
      "s_nop 0\n\t"
      "s_nop 0\n\t"
      "s_nop 0\n\t"
      "s_nop 0\n\t"
      "s_nop 0\n\t"
      "s_nop 0\n\t"
      "s_nop 0\n\t"
      "s_nop 0\n\t"
      "s_nop 0\n\t"
      "s_nop 0\n\t"
      "s_nop 0\n\t"
      "s_nop 0\n\t"
      "s_nop 0\n\t"
      "s_nop 0\n\t"
      "s_nop 0\n\t"
      : "+{s[4:19]}"(s), "+{s[20:21]}"(kr), "+{s22}"(hint), "+{v[240:247]}"(v)
      :
      : "memory", "vcc", "scc",
        "v0", "v1", "v2", "v3", "v4", "v5", "v6", "v7", "v8", "v9", "v10", "v11", "v12", "v13", "v14", "v15", "v16",
        "v17", "v18", "v19", "v20", "v21", "v22", "v23", "v24", "v25", "v26", "v27", "v28", "v29", "v30", "v31", "v32",
        "v33", "v34", "v35", "v36", "v37", "v38", "v39", "v40", "v41", "v42", "v43", "v44", "v45", "v46", "v47", "v48",
        "v49", "v50", "v51", "v52", "v53", "v54", "v55", "v56", "v57", "v58", "v59", "v60", "v61", "v62", "v63", "v64",
        "v65", "v66", "v67", "v68", "v69", "v70", "v71", "v72", "v73", "v74", "v75", "v76", "v77", "v78", "v79", "v80",
        "v81", "v82", "v83", "v84", "v85", "v86", "v87", "v88", "v89", "v90", "v91", "v92", "v93", "v94", "v95", "v96",
        "v97", "v98", "v99", "v100", "v101", "v102", "v103", "v104", "v105", "v106", "v107", "v108", "v109", "v110",
        "v111", "v112", "v113", "v114", "v115", "v116", "v117", "v118", "v119", "v120", "v121", "v122", "v123", "v124",
        "v125", "v126", "v127", "v128", "v129", "v130", "v131", "v132", "v133", "v134", "v135", "v136", "v137", "v138",
        "v139", "v140", "v141", "v142", "v143", "v144", "v145", "v146", "v147", "v148", "v149", "v150", "v151", "v152",
        "v153", "v154", "v155", "v156", "v157", "v158", "v159", "v160", "v161", "v162", "v163", "v164", "v165", "v166",
        "v167", "v168", "v169", "v170", "v171", "v172", "v173", "v174", "v175", "v176", "v177", "v178", "v179", "v180",
        "v181", "v182", "v183", "v184", "v185", "v186", "v187", "v188", "v189", "v190", "v191", "v192", "v193", "v194",
        "v195", "v196", "v197", "v198", "v199", "v200", "v201", "v202", "v203", "v204", "v205", "v206", "v207", "v208",
        "v209", "v210", "v211", "v212", "v213", "v214", "v215", "v216", "v217", "v218", "v219", "v220", "v221", "v222",
        "v223", "v224", "v225", "v226", "v227", "v228", "v229", "v230", "v231", "v232", "v233", "v234", "v235", "v236",
        "v237", "v238", "v239", "v248", "v249", "v250", "v251", "v252", "v253", "s23", "s44", "s45", "s46", "s47",
        "s48", "s49", "s50", "s51", "s52", "s53", "s54", "s55", "s56", "s57", "s58", "s59", "s60", "s61", "s62", "s63",
        "s64", "s65", "s66", "s67", "s68", "s69", "s70"
    );
    __builtin_amdgcn_endpgm();
  } else {
#define B_INDICES                                                                                            \
  const int ib = idx - 16, t = 16 * (4 * side + ((xp + XO) & 3)) + ib;                         \
  const int st = 8 * xp + (ib >> 1), rot = 4 * (ib & 1) + 2 * side - (ib & side & 1);                        \
  const int cb0 = 4 * wave - max(wave - 4, 0);               \
  const int k1 = (ib + 2 * xcd) & 15, k0 = k1 - (k1 >= 14 ? 7 : 0)
    u32x16 acc;
    u32x8 v1, s1;
    u32x4 s3;
    unsigned bits = (wave >> 2) | (wave == 0 ? 16 : 0) | ((idx >> 1) == 11 ? 4 : 0), id = blockIdx.x, wv = wave;
    {
      LANE_BITS(threadIdx.x & 63);
      B_INDICES;
      v1[0] = (8 * (wave & 3) + lr) * 896 + (wave >> 2) * 128 + ((lc ^ swz_tr(8 * wave + lr)) << 4) + 4096;
      v1[1] = B_X + (4 * g + q4) * 896 + cb0 * 32 + p4 * 8;
      v1[2] = B_PS + (4 * g + q4) * 64 + wave * 8;
      v1[3] = B_PS + (4 * g + p4) * 64 + q4 * 16;
      v1[4] = (t * 16 + 4 * g + p4) * 16;
      v1[5] = lr * 4096 + ((lc ^ swz_tr(lr)) << 4);
      v1[6] = lane * 16;
      v1[7] = WS_HN + (7 * (wave & 1) + 2 * (wave >> 2) + (g & 1)) * 131072 + (t >> 4) * 16384 + (wave & 2) * 4096 + (t & 15) * 512 + (g >> 1) * 256 + c * 16;
      put(s1, 0, (u64)(wo + k0 * 32 * 896 - 4096));
      put(s1, 2, (u64)(ws + WS_ST));
      put(s1, 4, 0);
      put(s1, 6, (u64)(w2 + st * 128 + (wave * 224 + rot * 32) * 4096));
      s3[0] = (wave & 3) * B_UNIT + (wave >> 2) * 1024;
      s3[1] = (wave >> 2) - 1;
      s3[2] = 13 - k0;
      s3[3] = wave * 4096;
    asm volatile(
      "s_waitcnt lgkmcnt(0)\n\ts_nop 4\n\t"
      ".p2align 3\n\t"
      "s_load_dwordx2 s[40:41], s[6:7], 0x20000\n\t"
      "v_mov_b32_e32 v248, -1\n\t"
      "v_mov_b32_e32 v249, -1\n\t"
      "v_mov_b32_e32 v250, -1\n\t"
      "v_mov_b32_e32 v251, -1\n\t"
      "v_add_u32_e32 v221, 4294965504, v224\n\t"
      "v_add_u32_e32 v222, 512, v224\n\t"
      "v_add_u32_e32 v223, 4294966016, v224\n\t"
      "s_bitcmp1_b32 s16, 0\n\t"
      "s_cselect_b64 s[46:47], -1, 0\n\t"
      "s_mov_b64 vcc, s[46:47]\n\t"
      "s_waitcnt vmcnt(3)\n\t"
      "s_barrier\n\t"
      "ds_read_b64_tr_b16 v[204:205], v225\n\t"
      "ds_read_b64_tr_b16 v[206:207], v225 offset:32\n\t"
      "ds_read_b64_tr_b16 v[208:209], v225 offset:64\n\t"
      "ds_read_b64_tr_b16 v[210:211], v225 offset:96\n\t"
      "s_waitcnt lgkmcnt(0)\n\t"
      "s_add_u32 s40, s40, 1\n\t"
      "s_addc_u32 s41, s41, 0\n\t"
      "s_mul_i32 s48, s40, 0x9e3779b9\n\t"
      "s_mul_hi_u32 s9, s40, 0x7f4a7c15\n\t"
      "s_add_u32 s9, s9, s48\n\t"
      "s_mul_i32 s48, s41, 0x7f4a7c15\n\t"
      "s_add_u32 s9, s9, s48\n\t"
      "s_mul_i32 s8, s40, 0x7f4a7c15\n\t"
      "s_add_u32 s8, s8, 0xd192ed03\n\t"
      "s_addc_u32 s9, s9, 0xd1b54a32\n\t"
      "v_mov_b32_e32 v188, s8\n\t"
      "v_mov_b32_e32 v189, s9\n\t"
      "v_mov_b32_e32 v190, s40\n\t"
      "v_mov_b32_e32 v191, s41\n\t"
      "v_mov_b32_e32 v192, 163600\n\t"
      "s_mov_b64 exec, 1\n\t"
      "ds_write_b128 v192, v[188:191]\n\t"
      "s_mov_b64 exec, -1\n\t"
      "v_and_b32_e32 v210, s13, v210\n\t"
      "v_and_b32_e32 v211, s13, v211\n\t"
      "v_lshlrev_b32_e32 v160, 16, v204\n\t"
      "v_and_b32_e32 v161, 0xffff0000, v204\n\t"
      "v_lshlrev_b32_e32 v162, 16, v205\n\t"
      "v_and_b32_e32 v163, 0xffff0000, v205\n\t"
      "v_lshlrev_b32_e32 v164, 16, v206\n\t"
      "v_and_b32_e32 v165, 0xffff0000, v206\n\t"
      "v_lshlrev_b32_e32 v166, 16, v207\n\t"
      "v_and_b32_e32 v167, 0xffff0000, v207\n\t"
      "v_lshlrev_b32_e32 v168, 16, v208\n\t"
      "v_and_b32_e32 v169, 0xffff0000, v208\n\t"
      "v_lshlrev_b32_e32 v170, 16, v209\n\t"
      "v_and_b32_e32 v171, 0xffff0000, v209\n\t"
      "v_lshlrev_b32_e32 v172, 16, v210\n\t"
      "v_and_b32_e32 v173, 0xffff0000, v210\n\t"
      "v_lshlrev_b32_e32 v174, 16, v211\n\t"
      "v_and_b32_e32 v175, 0xffff0000, v211\n\t"
      "v_pk_add_f32 v[188:189], v[160:161], v[164:165]\n\t"
      "v_pk_mul_f32 v[192:193], v[160:161], v[160:161]\n\t"
      "v_pk_add_f32 v[190:191], v[162:163], v[166:167]\n\t"
      "v_pk_mul_f32 v[194:195], v[162:163], v[162:163]\n\t"
      "v_pk_fma_f32 v[192:193], v[164:165], v[164:165], v[192:193]\n\t"
      "v_pk_fma_f32 v[194:195], v[166:167], v[166:167], v[194:195]\n\t"
      "v_pk_add_f32 v[188:189], v[188:189], v[168:169]\n\t"
      "v_pk_fma_f32 v[192:193], v[168:169], v[168:169], v[192:193]\n\t"
      "v_pk_add_f32 v[190:191], v[190:191], v[170:171]\n\t"
      "v_pk_fma_f32 v[194:195], v[170:171], v[170:171], v[194:195]\n\t"
      "s_cbranch_vccnz SB0_%=\n\t"
      "s_nop 0\n\t"
      "v_pk_add_f32 v[188:189], v[188:189], v[172:173]\n\t"
      "v_pk_fma_f32 v[192:193], v[172:173], v[172:173], v[192:193]\n\t"
      "v_pk_add_f32 v[190:191], v[190:191], v[174:175]\n\t"
      "v_pk_fma_f32 v[194:195], v[174:175], v[174:175], v[194:195]\n\t"
      "SB0_%=:\n\t"
      "v_add_f32_dpp v196, v188, v188 row_ror:8 row_mask:0xf bank_mask:0x3\n\t"
      "v_add_f32_dpp v197, v189, v189 row_ror:8 row_mask:0xf bank_mask:0x3\n\t"
      "v_add_f32_dpp v198, v192, v192 row_ror:8 row_mask:0xf bank_mask:0x3\n\t"
      "v_add_f32_dpp v199, v193, v193 row_ror:8 row_mask:0xf bank_mask:0x3\n\t"
      "v_add_f32_dpp v196, v190, v190 row_ror:8 row_mask:0xf bank_mask:0xc\n\t"
      "v_add_f32_dpp v197, v191, v191 row_ror:8 row_mask:0xf bank_mask:0xc\n\t"
      "v_add_f32_dpp v198, v194, v194 row_ror:8 row_mask:0xf bank_mask:0xc\n\t"
      "v_add_f32_dpp v199, v195, v195 row_ror:8 row_mask:0xf bank_mask:0xc\n\t"
      "v_add_f32_dpp v188, v196, v196 row_shl:4 row_mask:0xf bank_mask:0x5\n\t"
      "v_add_f32_dpp v189, v198, v198 row_shl:4 row_mask:0xf bank_mask:0x5\n\t"
      "s_nop 0\n\t"
      "v_add_f32_dpp v188, v197, v197 row_shr:4 row_mask:0xf bank_mask:0xa\n\t"
      "v_add_f32_dpp v189, v199, v199 row_shr:4 row_mask:0xf bank_mask:0xa\n\t"
      "s_nop 0\n\t"
      "v_add_f32_dpp v188, v188, v188 quad_perm:[1,0,3,2] row_mask:0xf bank_mask:0xf\n\t"
      "v_add_f32_dpp v189, v189, v189 quad_perm:[1,0,3,2] row_mask:0xf bank_mask:0xf\n\t"
      "s_nop 0\n\t"
      "v_add_f32_dpp v188, v188, v188 quad_perm:[2,3,0,1] row_mask:0xf bank_mask:0xf\n\t"
      "v_add_f32_dpp v189, v189, v189 quad_perm:[2,3,0,1] row_mask:0xf bank_mask:0xf\n\t"
      "ds_write_b64 v226, v[188:189]\n\t"
      "s_waitcnt lgkmcnt(0)\n\t"
      "s_barrier\n\t"
      "ds_read_b128 v[188:191], v227\n\t"
      "s_waitcnt lgkmcnt(0)\n\t"
      "v_pk_add_f32 v[188:189], v[188:189], v[190:191]\n\t"
      "s_nop 1\n\t"
      "v_add_f32_dpp v188, v188, v188 row_ror:4 row_mask:0xf bank_mask:0xf\n\t"
      "v_add_f32_dpp v189, v189, v189 row_ror:4 row_mask:0xf bank_mask:0xf\n\t"
      "s_nop 0\n\t"
      "v_add_f32_dpp v188, v188, v188 row_ror:8 row_mask:0xf bank_mask:0xf\n\t"
      "v_add_f32_dpp v189, v189, v189 row_ror:8 row_mask:0xf bank_mask:0xf\n\t"
      "v_mul_f32_e32 v188, 0x3b124925, v188\n\t"
      "v_mul_f32_e32 v189, 0x3b124925, v189\n\t"
      "v_fma_f32 v189, -v188, v188, v189\n\t"
      "v_max_f32_e32 v189, 0, v189\n\t"
      "s_bitcmp1_b32 s16, 4\n\t"
      "s_cbranch_scc0 GW_%=\n\t"
      "v_add_f32_e32 v189, 0x3727c5ac, v189\n\t"
      "v_rsq_f32_e32 v189, v189\n\t"
      "v_xor_b32_e32 v188, 0x80000000, v188\n\t"
      "v_xor_b32_e32 v190, s8, v188\n\t"
      "v_xor_b32_e32 v191, s9, v189\n\t"
      "s_mov_b32 s48, 0xf000f\n\t"
      "s_mov_b32 s49, 0xf000f\n\t"
      "s_mov_b64 exec, s[48:49]\n\t"
      "global_store_dwordx4 v228, v[188:191], s[6:7] sc1\n\t"
      "s_mov_b64 exec, -1\n\t"
      "GW_%=:\n\t"
      "s_add_u32 m0, s12, 0\n\t"
      "s_nop 0\n\t"
      "global_load_lds_dwordx4 v224, s[4:5]\n\t"
      "global_load_lds_dwordx4 v221, s[4:5] offset:2048\n\t"
      "s_add_u32 m0, s12, 4096\n\t"
      "s_nop 0\n\t"
      "global_load_lds_dwordx4 v222, s[4:5]\n\t"
      "s_cbranch_vccnz WR0_%=\n\t"
      "global_load_lds_dwordx4 v223, s[4:5] offset:2048\n\t"
      "WR0_%=:\n\t"
      "s_sub_u32 s14, s14, 1\n\t"
      "s_cselect_b32 s48, -13, 1\n\t"
      "s_cselect_b32 s49, -1, 0\n\t"
      "s_mul_i32 s48, s48, 28672\n\t"
      "s_add_u32 s4, s4, s48\n\t"
      "s_addc_u32 s5, s5, s49\n\t"
      "s_add_u32 m0, s12, 28672\n\t"
      "s_nop 0\n\t"
      "global_load_lds_dwordx4 v224, s[4:5]\n\t"
      "global_load_lds_dwordx4 v221, s[4:5] offset:2048\n\t"
      "s_add_u32 m0, s12, 32768\n\t"
      "s_nop 0\n\t"
      "global_load_lds_dwordx4 v222, s[4:5]\n\t"
      "s_cbranch_vccnz WR1_%=\n\t"
      "global_load_lds_dwordx4 v223, s[4:5] offset:2048\n\t"
      "WR1_%=:\n\t"
      "s_sub_u32 s14, s14, 1\n\t"
      "s_cselect_b32 s48, -13, 1\n\t"
      "s_cselect_b32 s49, -1, 0\n\t"
      "s_mul_i32 s48, s48, 28672\n\t"
      "s_add_u32 s4, s4, s48\n\t"
      "s_addc_u32 s5, s5, s49\n\t"
      "s_waitcnt lgkmcnt(0)\n\t"
      "s_nop 1\n\t"
      : "+{s[4:11]}"(s1), "+{s[12:15]}"(s3), "+{s16}"(bits), "+{s17}"(id), "+{s18}"(wv), "+{v[224:231]}"(v1), "={v[160:175]}"(acc)
      :
      : "memory", "vcc", "scc",
        "v176", "v177", "v178", "v179", "v180", "v181", "v182", "v183", "v184", "v185", "v186", "v187", "v188", "v189",
        "v190", "v191", "v192", "v193", "v194", "v195", "v196", "v197", "v198", "v199", "v200", "v201", "v202", "v203",
        "v204", "v205", "v206", "v207", "v208", "v209", "v210", "v211", "v212", "v213", "v214", "v215", "v216", "v217",
        "v218", "v219", "v220", "v221", "v222", "v223", "v248", "v249", "v250", "v251", "s40", "s41", "s42", "s43",
        "s44", "s45", "s46", "s47", "s48", "s49", "s50", "s51"
    );
    }
    PIN();
    {
    const int wave = wv & 0x3ffffff, xcd = id & 7, idx = id >> 3, side = xcd >> 2, xp = xcd & 3;
    B_INDICES;
    const int dg = 2 * side + (ib & 1), blk0 = 7 * dg, rn = rot + 1;
    bits |= (2 - ((wave >> 1) & 2)) | ((((wave - 7) & (t - 112)) >> 25) & 64) | (((t - 112) >> 24) & 128);
    u32x4 vo, v3;
    u64 par = ST_PW + xcd * 128;
    {
      LANE_BITS(v1[6] >> 4);
      u32x8 v;
      const unsigned tv = t + (lane >> 6), wv = wave + (lane >> 6), sv = (tv >> 6) * 8192;
      const unsigned ab = g * B_UNIT + q4 * 128 + (p4 & 1) * 8, al = ((p4 >> 1) ^ swz_tr(8 * g + q4)) << 4;
      const unsigned oi = B_X + c * B_OP, b0 = 112 * wv + 8 * g - 64 * k0;
#pragma unroll
      for (int b = 0; b < 4; ++b) {
        const unsigned cb = 4 * wv - (wv >> 2) * (wv & 3) + b;
        v[b] = ab + (cb >> 2) * 1024 + (((cb & 3) << 5) ^ al);
        const unsigned by = b0 + 56 * (b >> 1) + 32 * (b & 1);
        vo[b] = oi + min(by, by + 896);
      }
      v[4] = oi + 16 * g;
      v[5] = WS_G + tv * 16384 + wv * 1024 + lane * 16;
      v[6] = v1[7] + 4 * 131072;
      v[7] = v[5] + 8192;
      v3[0] = 0;
      v3[1] = ST_H3 + (lc & 3) * 32768 + (lc >> 2) * 8192 + 2 * sv + (tv & 63) * 128;
      v3[2] = ST_H2 + sv + (tv & 32) * 512 + (tv & 24) * 256 + wv * 256 + (tv & 4) * 32 + (lane & 1) * 64;
      v3[3] = ST_CK + sv + (tv & 56) * 2048 + (tv & 4) * 1024 + wv * 512 + (g >> 1) * 256 + (tv & 3) * 64 + c * 4;
      u64 po = (u64)(ws + (unsigned)(WS_O + (t & 63) * 32768 + (wave >> 2) * 16384 + side * 8192 + (wave & 3) * 2048));
    asm volatile(
      "s_waitcnt lgkmcnt(0)\n\ts_nop 4\n\t"
      ".p2align 3\n\t"
      "s_nop 0\n\t"
      "v_mov_b32_e32 v248, -1\n\t"
      "v_mov_b32_e32 v249, -1\n\t"
      "v_mov_b32_e32 v250, -1\n\t"
      "v_mov_b32_e64 v251, -1\n\t"
      "v_add_u32_e32 v221, 4294965504, v224\n\t"
      "v_add_u32_e32 v222, 512, v224\n\t"
      "v_add_u32_e32 v223, 4294966016, v224\n\t"
      "s_bitcmp1_b32 s16, 0\n\t"
      "s_cselect_b64 s[46:47], -1, 0\n\t"
      "s_mov_b64 vcc, s[46:47]\n\t"
      "s_mov_b32 s44, 0xffffffff\n\t"
      "s_mov_b32 s45, 0xffff\n\t"
      "s_or_b32 s42, s13, 0xffff\n\t"
      "s_or_b32 s43, s13, 0xffff\n\t"
      "v_xor_b32_e64 v176, 64, v229\n\t"
      "v_add_u32_e32 v177, 63488, v229\n\t"
      "v_add_u32_e32 v178, 95232, v176\n\t"
      "v_add_u32_e32 v176, 31744, v176\n\t"
      "s_add_u32 m0, s15, 130560\n\t"
      "s_nop 0\n\t"
      "global_load_lds_dwordx4 v229, s[10:11]\n\t"
      "global_load_lds_dwordx4 v176, s[10:11] offset:1024\n\t"
      "s_bitcmp1_b32 s16, 2\n\t"
      "s_cselect_b64 exec, 1, -1\n\t"
      "global_load_lds_dwordx4 v177, s[10:11] offset:2048\n\t"
      "s_mov_b64 exec, -1\n\t"
      "s_bitcmp1_b32 s16, 2\n\t"
      "s_cselect_b64 exec, 1, -1\n\t"
      "global_load_lds_dwordx4 v178, s[10:11] offset:3072\n\t"
      "s_mov_b64 exec, -1\n\t"
      "s_cbranch_vccnz SN3_%=\n\t"
      "global_store_dwordx4 v231, v[248:251], s[6:7] sc1\n\t"
      "SN3_%=:\n\t"
      "s_cbranch_vccnz WW0_%=\n\t"
      "s_waitcnt vmcnt(8)\n\t"
      "s_branch WX0_%=\n\t"
      "WW0_%=:\n\t"
      "s_waitcnt vmcnt(7)\n\t"
      "WX0_%=:\n\t"
      "s_waitcnt lgkmcnt(0)\n\t"
      "s_barrier\n\t"
      "ds_read_b64_tr_b16 v[0:1], v232\n\t"
      "ds_read_b64_tr_b16 v[2:3], v232 offset:512\n\t"
      "ds_read_b64_tr_b16 v[4:5], v233\n\t"
      "ds_read_b64_tr_b16 v[6:7], v233 offset:512\n\t"
      "ds_read_b64_tr_b16 v[8:9], v234\n\t"
      "ds_read_b64_tr_b16 v[10:11], v234 offset:512\n\t"
      "ds_read_b64_tr_b16 v[12:13], v235\n\t"
      "ds_read_b64_tr_b16 v[14:15], v235 offset:512\n\t"
      "s_add_u32 m0, s12, 57344\n\t"
      "s_nop 0\n\t"
      "global_load_lds_dwordx4 v224, s[4:5]\n\t"
      "global_load_lds_dwordx4 v221, s[4:5] offset:2048\n\t"
      "s_add_u32 m0, s12, 61440\n\t"
      "s_nop 0\n\t"
      "global_load_lds_dwordx4 v222, s[4:5]\n\t"
      "s_cbranch_vccnz WR2_%=\n\t"
      "global_load_lds_dwordx4 v223, s[4:5] offset:2048\n\t"
      "WR2_%=:\n\t"
      "s_sub_u32 s14, s14, 1\n\t"
      "s_cselect_b32 s48, -13, 1\n\t"
      "s_cselect_b32 s49, -1, 0\n\t"
      "s_mul_i32 s48, s48, 28672\n\t"
      "s_add_u32 s4, s4, s48\n\t"
      "s_addc_u32 s5, s5, s49\n\t"
      "s_mov_b64 exec, 0\n\t"
      "s_cmpk_eq_u32 s15, 0x0\n\t"
      "s_cbranch_scc1 .LhopB0\n\t"
      "s_cmpk_eq_u32 s15, 0x1000\n\t"
      "s_cbranch_scc1 .LhopB1\n\t"
      "s_cmpk_eq_u32 s15, 0x2000\n\t"
      "s_cbranch_scc1 .LhopB2\n\t"
      "s_cmpk_eq_u32 s15, 0x3000\n\t"
      "s_cbranch_scc1 .LhopB3\n\t"
      "s_cmpk_eq_u32 s15, 0x4000\n\t"
      "s_cbranch_scc1 .LbackB\n\t"
      "s_cmpk_eq_u32 s15, 0x5000\n\t"
      "s_cbranch_scc1 .LbackB\n\t"
      ".LbackB:\n\t"
      "s_mov_b64 exec, -1\n\t"
      "s_cbranch_vccnz WW1_%=\n\t"
      "s_waitcnt vmcnt(8)\n\t"
      "s_branch WX1_%=\n\t"
      "WW1_%=:\n\t"
      "s_waitcnt vmcnt(7)\n\t"
      "WX1_%=:\n\t"
      "s_waitcnt lgkmcnt(0)\n\t"
      "s_barrier\n\t"
      "ds_read_b64_tr_b16 v[16:17], v232 offset:28672\n\t"
      "ds_read_b64_tr_b16 v[18:19], v232 offset:29184\n\t"
      "ds_read_b64_tr_b16 v[20:21], v233 offset:28672\n\t"
      "ds_read_b64_tr_b16 v[22:23], v233 offset:29184\n\t"
      "ds_read_b64_tr_b16 v[24:25], v234 offset:28672\n\t"
      "ds_read_b64_tr_b16 v[26:27], v234 offset:29184\n\t"
      "ds_read_b64_tr_b16 v[28:29], v235 offset:28672\n\t"
      "ds_read_b64_tr_b16 v[30:31], v235 offset:29184\n\t"
      "s_add_u32 m0, s12, 86016\n\t"
      "s_nop 0\n\t"
      "global_load_lds_dwordx4 v224, s[4:5]\n\t"
      "global_load_lds_dwordx4 v221, s[4:5] offset:2048\n\t"
      "s_add_u32 m0, s12, 90112\n\t"
      "s_nop 0\n\t"
      "global_load_lds_dwordx4 v222, s[4:5]\n\t"
      "s_cbranch_vccnz WR3_%=\n\t"
      "global_load_lds_dwordx4 v223, s[4:5] offset:2048\n\t"
      "WR3_%=:\n\t"
      "s_sub_u32 s14, s14, 1\n\t"
      "s_cselect_b32 s48, -13, 1\n\t"
      "s_cselect_b32 s49, -1, 0\n\t"
      "s_mul_i32 s48, s48, 28672\n\t"
      "s_add_u32 s4, s4, s48\n\t"
      "s_addc_u32 s5, s5, s49\n\t"
      "v_add_u32_e32 v232, 57344, v232\n\t"
      "v_add_u32_e32 v233, 57344, v233\n\t"
      "v_add_u32_e32 v234, 57344, v234\n\t"
      "v_add_u32_e32 v235, 57344, v235\n\t"
      "s_cbranch_vccnz WW2_%=\n\t"
      "s_waitcnt vmcnt(4)\n\t"
      "s_branch WX2_%=\n\t"
      "WW2_%=:\n\t"
      "s_waitcnt vmcnt(3)\n\t"
      "WX2_%=:\n\t"
      "s_waitcnt lgkmcnt(0)\n\t"
      "s_barrier\n\t"
      "ds_read_b64_tr_b16 v[32:33], v232\n\t"
      "ds_read_b64_tr_b16 v[34:35], v232 offset:512\n\t"
      "ds_read_b64_tr_b16 v[36:37], v233\n\t"
      "ds_read_b64_tr_b16 v[38:39], v233 offset:512\n\t"
      "ds_read_b64_tr_b16 v[40:41], v234\n\t"
      "ds_read_b64_tr_b16 v[42:43], v234 offset:512\n\t"
      "ds_read_b64_tr_b16 v[44:45], v235\n\t"
      "ds_read_b64_tr_b16 v[46:47], v235 offset:512\n\t"
      "s_add_u32 m0, s12, 0\n\t"
      "s_nop 0\n\t"
      "global_load_lds_dwordx4 v224, s[4:5]\n\t"
      "global_load_lds_dwordx4 v221, s[4:5] offset:2048\n\t"
      "s_add_u32 m0, s12, 4096\n\t"
      "s_nop 0\n\t"
      "global_load_lds_dwordx4 v222, s[4:5]\n\t"
      "s_cbranch_vccnz WR4_%=\n\t"
      "global_load_lds_dwordx4 v223, s[4:5] offset:2048\n\t"
      "WR4_%=:\n\t"
      "s_sub_u32 s14, s14, 1\n\t"
      "s_cselect_b32 s48, -13, 1\n\t"
      "s_cselect_b32 s49, -1, 0\n\t"
      "s_mul_i32 s48, s48, 28672\n\t"
      "s_add_u32 s4, s4, s48\n\t"
      "s_addc_u32 s5, s5, s49\n\t"
      "s_cbranch_vccnz WW3_%=\n\t"
      "s_waitcnt vmcnt(4)\n\t"
      "s_branch WX3_%=\n\t"
      "WW3_%=:\n\t"
      "s_waitcnt vmcnt(3)\n\t"
      "WX3_%=:\n\t"
      "s_waitcnt lgkmcnt(0)\n\t"
      "s_barrier\n\t"
      "ds_read_b64_tr_b16 v[48:49], v232 offset:28672\n\t"
      "ds_read_b64_tr_b16 v[50:51], v232 offset:29184\n\t"
      "ds_read_b64_tr_b16 v[52:53], v233 offset:28672\n\t"
      "ds_read_b64_tr_b16 v[54:55], v233 offset:29184\n\t"
      "ds_read_b64_tr_b16 v[56:57], v234 offset:28672\n\t"
      "ds_read_b64_tr_b16 v[58:59], v234 offset:29184\n\t"
      "ds_read_b64_tr_b16 v[60:61], v235 offset:28672\n\t"
      "ds_read_b64_tr_b16 v[62:63], v235 offset:29184\n\t"
      "s_add_u32 m0, s12, 28672\n\t"
      "s_nop 0\n\t"
      "global_load_lds_dwordx4 v224, s[4:5]\n\t"
      "global_load_lds_dwordx4 v221, s[4:5] offset:2048\n\t"
      "s_add_u32 m0, s12, 32768\n\t"
      "s_nop 0\n\t"
      "global_load_lds_dwordx4 v222, s[4:5]\n\t"
      "s_cbranch_vccnz WR5_%=\n\t"
      "global_load_lds_dwordx4 v223, s[4:5] offset:2048\n\t"
      "WR5_%=:\n\t"
      "s_sub_u32 s14, s14, 1\n\t"
      "s_cselect_b32 s48, -13, 1\n\t"
      "s_cselect_b32 s49, -1, 0\n\t"
      "s_mul_i32 s48, s48, 28672\n\t"
      "s_add_u32 s4, s4, s48\n\t"
      "s_addc_u32 s5, s5, s49\n\t"
      "v_add_u32_e32 v232, 4294909952, v232\n\t"
      "v_add_u32_e32 v233, 4294909952, v233\n\t"
      "v_add_u32_e32 v234, 4294909952, v234\n\t"
      "v_add_u32_e32 v235, 4294909952, v235\n\t"
      "s_cbranch_vccnz WW4_%=\n\t"
      "s_waitcnt vmcnt(4)\n\t"
      "s_branch WX4_%=\n\t"
      "WW4_%=:\n\t"
      "s_waitcnt vmcnt(3)\n\t"
      "WX4_%=:\n\t"
      "s_waitcnt lgkmcnt(0)\n\t"
      "s_barrier\n\t"
      "ds_read_b64_tr_b16 v[64:65], v232\n\t"
      "ds_read_b64_tr_b16 v[66:67], v232 offset:512\n\t"
      "ds_read_b64_tr_b16 v[68:69], v233\n\t"
      "ds_read_b64_tr_b16 v[70:71], v233 offset:512\n\t"
      "ds_read_b64_tr_b16 v[72:73], v234\n\t"
      "ds_read_b64_tr_b16 v[74:75], v234 offset:512\n\t"
      "ds_read_b64_tr_b16 v[76:77], v235\n\t"
      "ds_read_b64_tr_b16 v[78:79], v235 offset:512\n\t"
      "s_add_u32 m0, s12, 57344\n\t"
      "s_nop 0\n\t"
      "global_load_lds_dwordx4 v224, s[4:5]\n\t"
      "global_load_lds_dwordx4 v221, s[4:5] offset:2048\n\t"
      "s_add_u32 m0, s12, 61440\n\t"
      "s_nop 0\n\t"
      "global_load_lds_dwordx4 v222, s[4:5]\n\t"
      "s_cbranch_vccnz WR6_%=\n\t"
      "global_load_lds_dwordx4 v223, s[4:5] offset:2048\n\t"
      "WR6_%=:\n\t"
      "s_sub_u32 s14, s14, 1\n\t"
      "s_cselect_b32 s48, -13, 1\n\t"
      "s_cselect_b32 s49, -1, 0\n\t"
      "s_mul_i32 s48, s48, 28672\n\t"
      "s_add_u32 s4, s4, s48\n\t"
      "s_addc_u32 s5, s5, s49\n\t"
      "s_cbranch_vccnz WW5_%=\n\t"
      "s_waitcnt vmcnt(4)\n\t"
      "s_branch WX5_%=\n\t"
      "WW5_%=:\n\t"
      "s_waitcnt vmcnt(3)\n\t"
      "WX5_%=:\n\t"
      "s_waitcnt lgkmcnt(0)\n\t"
      "s_barrier\n\t"
      "ds_read_b64_tr_b16 v[80:81], v232 offset:28672\n\t"
      ".LhopB0:\n\t"
      "s_cbranch_execz .LbackB\n\t"
      "ds_read_b64_tr_b16 v[82:83], v232 offset:29184\n\t"
      "ds_read_b64_tr_b16 v[84:85], v233 offset:28672\n\t"
      "ds_read_b64_tr_b16 v[86:87], v233 offset:29184\n\t"
      "ds_read_b64_tr_b16 v[88:89], v234 offset:28672\n\t"
      "ds_read_b64_tr_b16 v[90:91], v234 offset:29184\n\t"
      "ds_read_b64_tr_b16 v[92:93], v235 offset:28672\n\t"
      "ds_read_b64_tr_b16 v[94:95], v235 offset:29184\n\t"
      ".LhopB1:\n\t"
      "s_cbranch_execz .LbackB\n\t"
      "s_add_u32 m0, s12, 86016\n\t"
      "s_nop 0\n\t"
      "global_load_lds_dwordx4 v224, s[4:5]\n\t"
      "global_load_lds_dwordx4 v221, s[4:5] offset:2048\n\t"
      "s_add_u32 m0, s12, 90112\n\t"
      "s_nop 0\n\t"
      "global_load_lds_dwordx4 v222, s[4:5]\n\t"
      "s_cbranch_vccnz WR7_%=\n\t"
      "global_load_lds_dwordx4 v223, s[4:5] offset:2048\n\t"
      "WR7_%=:\n\t"
      "s_sub_u32 s14, s14, 1\n\t"
      "s_cselect_b32 s48, -13, 1\n\t"
      "s_cselect_b32 s49, -1, 0\n\t"
      "s_mul_i32 s48, s48, 28672\n\t"
      "s_add_u32 s4, s4, s48\n\t"
      "s_addc_u32 s5, s5, s49\n\t"
      "v_add_u32_e32 v232, 57344, v232\n\t"
      "v_add_u32_e32 v233, 57344, v233\n\t"
      "v_add_u32_e32 v234, 57344, v234\n\t"
      "v_add_u32_e32 v235, 57344, v235\n\t"
      "s_cbranch_vccnz WW6_%=\n\t"
      "s_waitcnt vmcnt(4)\n\t"
      "s_branch WX6_%=\n\t"
      "WW6_%=:\n\t"
      "s_waitcnt vmcnt(3)\n\t"
      "WX6_%=:\n\t"
      "s_waitcnt lgkmcnt(0)\n\t"
      "s_barrier\n\t"
      "ds_read_b64_tr_b16 v[96:97], v232\n\t"
      "ds_read_b64_tr_b16 v[98:99], v232 offset:512\n\t"
      "ds_read_b64_tr_b16 v[100:101], v233\n\t"
      "ds_read_b64_tr_b16 v[102:103], v233 offset:512\n\t"
      "ds_read_b64_tr_b16 v[104:105], v234\n\t"
      "ds_read_b64_tr_b16 v[106:107], v234 offset:512\n\t"
      "ds_read_b64_tr_b16 v[108:109], v235\n\t"
      "ds_read_b64_tr_b16 v[110:111], v235 offset:512\n\t"
      "s_add_u32 m0, s12, 0\n\t"
      "s_nop 0\n\t"
      "global_load_lds_dwordx4 v224, s[4:5]\n\t"
      "global_load_lds_dwordx4 v221, s[4:5] offset:2048\n\t"
      "s_add_u32 m0, s12, 4096\n\t"
      "s_nop 0\n\t"
      "global_load_lds_dwordx4 v222, s[4:5]\n\t"
      "s_cbranch_vccnz WR8_%=\n\t"
      "global_load_lds_dwordx4 v223, s[4:5] offset:2048\n\t"
      "WR8_%=:\n\t"
      "s_sub_u32 s14, s14, 1\n\t"
      "s_cselect_b32 s48, -13, 1\n\t"
      "s_cselect_b32 s49, -1, 0\n\t"
      "s_mul_i32 s48, s48, 28672\n\t"
      "s_add_u32 s4, s4, s48\n\t"
      "s_addc_u32 s5, s5, s49\n\t"
      "s_cbranch_vccnz WW7_%=\n\t"
      "s_waitcnt vmcnt(4)\n\t"
      "s_branch WX7_%=\n\t"
      "WW7_%=:\n\t"
      "s_waitcnt vmcnt(3)\n\t"
      "WX7_%=:\n\t"
      "s_waitcnt lgkmcnt(0)\n\t"
      "s_barrier\n\t"
      "ds_read_b64_tr_b16 v[112:113], v232 offset:28672\n\t"
      "ds_read_b64_tr_b16 v[114:115], v232 offset:29184\n\t"
      "ds_read_b64_tr_b16 v[116:117], v233 offset:28672\n\t"
      "ds_read_b64_tr_b16 v[118:119], v233 offset:29184\n\t"
      "ds_read_b64_tr_b16 v[120:121], v234 offset:28672\n\t"
      "ds_read_b64_tr_b16 v[122:123], v234 offset:29184\n\t"
      "ds_read_b64_tr_b16 v[124:125], v235 offset:28672\n\t"
      "ds_read_b64_tr_b16 v[126:127], v235 offset:29184\n\t"
      "s_add_u32 m0, s12, 28672\n\t"
      "s_nop 0\n\t"
      "global_load_lds_dwordx4 v224, s[4:5]\n\t"
      "global_load_lds_dwordx4 v221, s[4:5] offset:2048\n\t"
      "s_add_u32 m0, s12, 32768\n\t"
      "s_nop 0\n\t"
      "global_load_lds_dwordx4 v222, s[4:5]\n\t"
      "s_cbranch_vccnz WR9_%=\n\t"
      "global_load_lds_dwordx4 v223, s[4:5] offset:2048\n\t"
      "WR9_%=:\n\t"
      "s_sub_u32 s14, s14, 1\n\t"
      "s_cselect_b32 s48, -13, 1\n\t"
      "s_cselect_b32 s49, -1, 0\n\t"
      "s_mul_i32 s48, s48, 28672\n\t"
      "s_add_u32 s4, s4, s48\n\t"
      "s_addc_u32 s5, s5, s49\n\t"
      "v_add_u32_e32 v232, 4294909952, v232\n\t"
      "v_add_u32_e32 v233, 4294909952, v233\n\t"
      "v_add_u32_e32 v234, 4294909952, v234\n\t"
      "v_add_u32_e32 v235, 4294909952, v235\n\t"
      "s_cbranch_vccnz WW8_%=\n\t"
      "s_waitcnt vmcnt(4)\n\t"
      "s_branch WX8_%=\n\t"
      "WW8_%=:\n\t"
      "s_waitcnt vmcnt(3)\n\t"
      "WX8_%=:\n\t"
      "s_waitcnt lgkmcnt(0)\n\t"
      "s_barrier\n\t"
      "ds_read_b64_tr_b16 v[128:129], v232\n\t"
      "ds_read_b64_tr_b16 v[130:131], v232 offset:512\n\t"
      "ds_read_b64_tr_b16 v[132:133], v233\n\t"
      "ds_read_b64_tr_b16 v[134:135], v233 offset:512\n\t"
      "ds_read_b64_tr_b16 v[136:137], v234\n\t"
      "ds_read_b64_tr_b16 v[138:139], v234 offset:512\n\t"
      "ds_read_b64_tr_b16 v[140:141], v235\n\t"
      "ds_read_b64_tr_b16 v[142:143], v235 offset:512\n\t"
      "s_add_u32 m0, s12, 57344\n\t"
      "s_nop 0\n\t"
      "global_load_lds_dwordx4 v224, s[4:5]\n\t"
      "global_load_lds_dwordx4 v221, s[4:5] offset:2048\n\t"
      "s_add_u32 m0, s12, 61440\n\t"
      "s_nop 0\n\t"
      "global_load_lds_dwordx4 v222, s[4:5]\n\t"
      "s_cbranch_vccnz WR10_%=\n\t"
      "global_load_lds_dwordx4 v223, s[4:5] offset:2048\n\t"
      "WR10_%=:\n\t"
      "s_sub_u32 s14, s14, 1\n\t"
      "s_cselect_b32 s48, -13, 1\n\t"
      "s_cselect_b32 s49, -1, 0\n\t"
      "s_mul_i32 s48, s48, 28672\n\t"
      "s_add_u32 s4, s4, s48\n\t"
      "s_addc_u32 s5, s5, s49\n\t"
      "s_cbranch_vccnz WW9_%=\n\t"
      "s_waitcnt vmcnt(4)\n\t"
      "s_branch WX9_%=\n\t"
      "WW9_%=:\n\t"
      "s_waitcnt vmcnt(3)\n\t"
      "WX9_%=:\n\t"
      "s_waitcnt lgkmcnt(0)\n\t"
      "s_barrier\n\t"
      "ds_read_b64_tr_b16 v[144:145], v232 offset:28672\n\t"
      "ds_read_b64_tr_b16 v[146:147], v232 offset:29184\n\t"
      "ds_read_b64_tr_b16 v[148:149], v233 offset:28672\n\t"
      "ds_read_b64_tr_b16 v[150:151], v233 offset:29184\n\t"
      "ds_read_b64_tr_b16 v[152:153], v234 offset:28672\n\t"
      "ds_read_b64_tr_b16 v[154:155], v234 offset:29184\n\t"
      "ds_read_b64_tr_b16 v[156:157], v235 offset:28672\n\t"
      "ds_read_b64_tr_b16 v[158:159], v235 offset:29184\n\t"
      "s_add_u32 m0, s12, 86016\n\t"
      "s_nop 0\n\t"
      "global_load_lds_dwordx4 v224, s[4:5]\n\t"
      "global_load_lds_dwordx4 v221, s[4:5] offset:2048\n\t"
      "s_add_u32 m0, s12, 90112\n\t"
      "s_nop 0\n\t"
      "global_load_lds_dwordx4 v222, s[4:5]\n\t"
      "s_cbranch_vccnz WR11_%=\n\t"
      "global_load_lds_dwordx4 v223, s[4:5] offset:2048\n\t"
      "WR11_%=:\n\t"
      "s_sub_u32 s14, s14, 1\n\t"
      "s_cselect_b32 s48, -13, 1\n\t"
      "s_cselect_b32 s49, -1, 0\n\t"
      "s_mul_i32 s48, s48, 28672\n\t"
      "s_add_u32 s4, s4, s48\n\t"
      "s_addc_u32 s5, s5, s49\n\t"
      "v_add_u32_e32 v232, 57344, v232\n\t"
      "v_add_u32_e32 v233, 57344, v233\n\t"
      "v_add_u32_e32 v234, 57344, v234\n\t"
      "v_add_u32_e32 v235, 57344, v235\n\t"
      "v_min_u32_e32 v184, 32, v230\n\t"
      "v_add_u32_e32 v184, s28, v184\n\t"
      "global_load_dwordx4 v[180:183], v184, s[6:7] sc1\n\t"
      "s_cbranch_vccnz WW10_%=\n\t"
      "s_waitcnt vmcnt(5)\n\t"
      "s_branch WX10_%=\n\t"
      "WW10_%=:\n\t"
      "s_waitcnt vmcnt(4)\n\t"
      "WX10_%=:\n\t"
      "s_waitcnt lgkmcnt(0)\n\t"
      "s_barrier\n\t"
      "ds_read_b64_tr_b16 v[188:189], v232\n\t"
      "ds_read_b64_tr_b16 v[190:191], v232 offset:512\n\t"
      "ds_read_b64_tr_b16 v[192:193], v233\n\t"
      "ds_read_b64_tr_b16 v[194:195], v233 offset:512\n\t"
      "ds_read_b64_tr_b16 v[196:197], v234\n\t"
      "ds_read_b64_tr_b16 v[198:199], v234 offset:512\n\t"
      "ds_read_b64_tr_b16 v[200:201], v235\n\t"
      "ds_read_b64_tr_b16 v[202:203], v235 offset:512\n\t"
      "s_add_u32 m0, s12, 0\n\t"
      "s_nop 0\n\t"
      "global_load_lds_dwordx4 v224, s[4:5]\n\t"
      "global_load_lds_dwordx4 v221, s[4:5] offset:2048\n\t"
      "s_add_u32 m0, s12, 4096\n\t"
      "s_nop 0\n\t"
      "global_load_lds_dwordx4 v222, s[4:5]\n\t"
      "s_cbranch_vccnz WR12_%=\n\t"
      "global_load_lds_dwordx4 v223, s[4:5] offset:2048\n\t"
      "WR12_%=:\n\t"
      "s_sub_u32 s14, s14, 1\n\t"
      "s_cselect_b32 s48, -13, 1\n\t"
      "s_cselect_b32 s49, -1, 0\n\t"
      "s_mul_i32 s48, s48, 28672\n\t"
      "s_add_u32 s4, s4, s48\n\t"
      "s_addc_u32 s5, s5, s49\n\t"
      "s_cbranch_vccnz WW11_%=\n\t"
      "s_waitcnt vmcnt(5)\n\t"
      "s_branch WX11_%=\n\t"
      "WW11_%=:\n\t"
      "s_waitcnt vmcnt(4)\n\t"
      "WX11_%=:\n\t"
      "s_waitcnt lgkmcnt(0)\n\t"
      "s_barrier\n\t"
      "ds_read_b64_tr_b16 v[204:205], v232 offset:28672\n\t"
      "ds_read_b64_tr_b16 v[206:207], v232 offset:29184\n\t"
      "ds_read_b64_tr_b16 v[208:209], v233 offset:28672\n\t"
      "ds_read_b64_tr_b16 v[210:211], v233 offset:29184\n\t"
      "ds_read_b64_tr_b16 v[212:213], v234 offset:28672\n\t"
      "ds_read_b64_tr_b16 v[214:215], v234 offset:29184\n\t"
      "ds_read_b64_tr_b16 v[216:217], v235 offset:28672\n\t"
      "ds_read_b64_tr_b16 v[218:219], v235 offset:29184\n\t"
      "s_add_u32 m0, s12, 28672\n\t"
      "s_nop 0\n\t"
      "global_load_lds_dwordx4 v224, s[4:5]\n\t"
      "global_load_lds_dwordx4 v221, s[4:5] offset:2048\n\t"
      "s_add_u32 m0, s12, 32768\n\t"
      "s_nop 0\n\t"
      "global_load_lds_dwordx4 v222, s[4:5]\n\t"
      "s_cbranch_vccnz WR13_%=\n\t"
      "global_load_lds_dwordx4 v223, s[4:5] offset:2048\n\t"
      "WR13_%=:\n\t"
      "v_add_u32_e32 v232, 4294909952, v232\n\t"
      "v_add_u32_e32 v233, 4294909952, v233\n\t"
      "v_add_u32_e32 v234, 4294909952, v234\n\t"
      "v_add_u32_e32 v235, 4294909952, v235\n\t"
      "s_waitcnt vmcnt(6)\n\t"
      "v_xor_b32_e32 v182, v182, v180\n\t"
      "v_xor_b32_e64 v183, v183, v181\n\t"
      "v_cmp_ne_u64_e32 vcc, s[8:9], v[182:183]\n\t"
      "s_cbranch_vccnz PWR_%=\n\t"
      "PWB_%=:\n\t"
      "v_readlane_b32 s48, v180, 0\n\t"
      "v_readlane_b32 s28, v180, 1\n\t"
      "v_readlane_b32 s29, v180, 2\n\t"
      "v_mov_b32_e32 v252, s8\n\t"
      "v_mov_b32_e32 v253, s9\n\t"
      "s_xor_b32 s22, s22, s48\n\t"
      "v_xor_b32_e32 v231, s28, v231\n\t"
      "v_xor_b32_e32 v238, s28, v238\n\t"
      "v_xor_b32_e32 v246, s28, v246\n\t"
      "v_xor_b32_e32 v247, s28, v247\n\t"
      "v_xor_b32_e32 v245, s29, v245\n\t"
      "s_waitcnt lgkmcnt(0)\n\t"
      "s_waitcnt vmcnt(0)\n\t"
      "s_mov_b32 s51, 0\n\t"
      "s_branch QE_%=\n\t"
      ".p2align 6\n\t"
      "QL_%=:\n\t"
      "s_sleep 2\n\t"
      "s_add_u32 s51, s51, 1\n\t"
      "s_cmp_lt_u32 s51, 262144\n\t"
      "s_cbranch_scc0 QX_%=\n\t"
      "QE_%=:\n\t"
      "s_mov_b64 exec, 3\n\t"
      "v_mov_b64_e32 v[176:177], 0\n\t"
      "global_atomic_or_x2 v[176:177], v246, v[176:177], s[6:7] sc0 sc1\n\t"
      "s_waitcnt vmcnt(0)\n\t"
      "v_cmp_ne_u64_e32 vcc, s[8:9], v[176:177]\n\t"
      "s_mov_b64 exec, -1\n\t"
      "s_cbranch_vccnz QL_%=\n\t"
      "QX_%=:\n\t"
      "s_mov_b32 s51, 0\n\t"
      "s_branch OE_%=\n\t"
      "OL_%=:\n\t"
      "s_sleep 2\n\t"
      "s_add_u32 s51, s51, 1\n\t"
      "s_cmp_lt_u32 s51, 262144\n\t"
      "s_cbranch_scc0 OX_%=\n\t"
      "OE_%=:\n\t"
      "global_load_dwordx4 v[176:179], v230, s[22:23] sc1\n\t"
      "global_load_dwordx4 v[180:183], v230, s[22:23] offset:1024 sc1\n\t"
      "global_load_dword v186, v247, s[6:7] sc1\n\t"
      "s_waitcnt vmcnt(2)\n\t"
      "ds_write_b64 v240, v[176:177]\n\t"
      "s_mov_b64 exec, s[44:45]\n\t"
      "ds_write_b64 v241, v[178:179]\n\t"
      "s_mov_b64 exec, -1\n\t"
      "v_add3_u32 v184, v176, v177, v178\n\t"
      "v_add_u32_e32 v184, v184, v179\n\t"
      "s_waitcnt vmcnt(1)\n\t"
      "ds_write_b64 v242, v[180:181]\n\t"
      "s_mov_b64 exec, s[44:45]\n\t"
      "ds_write_b64 v243, v[182:183]\n\t"
      "s_mov_b64 exec, -1\n\t"
      "v_add3_u32 v185, v180, v181, v182\n\t"
      "v_add_u32_e32 v185, v185, v183\n\t"
      "s_nop 1\n\t"
      "v_permlane32_swap_b32_e32 v184, v185\n\t"
      "v_add_u32_e32 v184, v184, v185\n\t"
      "v_mov_b32_e32 v185, v184\n\t"
      "s_nop 1\n\t"
      "v_permlane16_swap_b32_e32 v184, v185\n\t"
      "v_add3_u32 v184, v184, v185, s8\n\t"
      "s_waitcnt vmcnt(0) lgkmcnt(0)\n\t"
      "v_cmp_ne_u32_e32 vcc, v184, v186\n\t"
      "s_cbranch_vccnz OL_%=\n\t"
      "OX_%=:\n\t"
      "s_mov_b64 vcc, s[46:47]\n\t"
      "s_waitcnt lgkmcnt(0)\n\t"
      "s_barrier\n\t"
      "s_cbranch_vccnz GY_%=\n\t"
      "ds_read_b128 v[176:179], v236\n\t"
      "ds_read_b128 v[180:183], v236 offset:64\n\t"
      "ds_read_b128 v[184:187], v236 offset:128\n\t"
      "ds_read_b128 v[240:243], v236 offset:192\n\t"
      "ds_read_b128 v[220:223], v236 offset:256\n\t"
      "s_waitcnt lgkmcnt(4)\n\t"
      "v_mfma_f32_16x16x32_bf16 v[160:163], v[176:179], v[0:3], v[160:163]\n\t"
      "s_waitcnt lgkmcnt(3)\n\t"
      "v_mfma_f32_16x16x32_bf16 v[160:163], v[180:183], v[16:19], v[160:163]\n\t"
      "v_mfma_f32_16x16x32_bf16 v[164:167], v[176:179], v[4:7], v[164:167]\n\t"
      "v_mfma_f32_16x16x32_bf16 v[164:167], v[180:183], v[20:23], v[164:167]\n\t"
      "v_mfma_f32_16x16x32_bf16 v[168:171], v[176:179], v[8:11], v[168:171]\n\t"
      "v_mfma_f32_16x16x32_bf16 v[168:171], v[180:183], v[24:27], v[168:171]\n\t"
      "v_mfma_f32_16x16x32_bf16 v[172:175], v[176:179], v[12:15], v[172:175]\n\t"
      "ds_read_b128 v[176:179], v236 offset:320\n\t"
      "v_mfma_f32_16x16x32_bf16 v[172:175], v[180:183], v[28:31], v[172:175]\n\t"
      "ds_read_b128 v[180:183], v236 offset:384\n\t"
      "s_waitcnt lgkmcnt(4)\n\t"
      "v_mfma_f32_16x16x32_bf16 v[160:163], v[184:187], v[32:35], v[160:163]\n\t"
      "s_waitcnt lgkmcnt(3)\n\t"
      "v_mfma_f32_16x16x32_bf16 v[160:163], v[240:243], v[48:51], v[160:163]\n\t"
      "ds_read_b64_tr_b16 v[0:1], v232\n\t"
      "ds_read_b64_tr_b16 v[2:3], v232 offset:512\n\t"
      "v_mfma_f32_16x16x32_bf16 v[164:167], v[184:187], v[36:39], v[164:167]\n\t"
      "v_mfma_f32_16x16x32_bf16 v[164:167], v[240:243], v[52:55], v[164:167]\n\t"
      "v_mfma_f32_16x16x32_bf16 v[168:171], v[184:187], v[40:43], v[168:171]\n\t"
      "v_mfma_f32_16x16x32_bf16 v[168:171], v[240:243], v[56:59], v[168:171]\n\t"
      "ds_read_b64_tr_b16 v[4:5], v233\n\t"
      "ds_read_b64_tr_b16 v[6:7], v233 offset:512\n\t"
      "v_mfma_f32_16x16x32_bf16 v[172:175], v[184:187], v[44:47], v[172:175]\n\t"
      "ds_read_b128 v[184:187], v236 offset:448\n\t"
      "v_mfma_f32_16x16x32_bf16 v[172:175], v[240:243], v[60:63], v[172:175]\n\t"
      "ds_read_b128 v[240:243], v236 offset:512\n\t"
      "s_waitcnt lgkmcnt(8)\n\t"
      "v_mfma_f32_16x16x32_bf16 v[160:163], v[220:223], v[64:67], v[160:163]\n\t"
      "s_waitcnt lgkmcnt(7)\n\t"
      "v_mfma_f32_16x16x32_bf16 v[160:163], v[176:179], v[80:83], v[160:163]\n\t"
      "ds_read_b64_tr_b16 v[8:9], v234\n\t"
      "ds_read_b64_tr_b16 v[10:11], v234 offset:512\n\t"
      "v_mfma_f32_16x16x32_bf16 v[164:167], v[220:223], v[68:71], v[164:167]\n\t"
      "v_mfma_f32_16x16x32_bf16 v[164:167], v[176:179], v[84:87], v[164:167]\n\t"
      "v_mfma_f32_16x16x32_bf16 v[168:171], v[220:223], v[72:75], v[168:171]\n\t"
      "v_mfma_f32_16x16x32_bf16 v[168:171], v[176:179], v[88:91], v[168:171]\n\t"
      "ds_read_b64_tr_b16 v[12:13], v235\n\t"
      "ds_read_b64_tr_b16 v[14:15], v235 offset:512\n\t"
      "v_mfma_f32_16x16x32_bf16 v[172:175], v[220:223], v[76:79], v[172:175]\n\t"
      "ds_read_b128 v[220:223], v236 offset:576\n\t"
      "v_mfma_f32_16x16x32_bf16 v[172:175], v[176:179], v[92:95], v[172:175]\n\t"
      "ds_read_b128 v[176:179], v236 offset:640\n\t"
      "s_waitcnt lgkmcnt(12)\n\t"
      "v_mfma_f32_16x16x32_bf16 v[160:163], v[180:183], v[96:99], v[160:163]\n\t"
      "s_waitcnt lgkmcnt(7)\n\t"
      "v_mfma_f32_16x16x32_bf16 v[160:163], v[184:187], v[112:115], v[160:163]\n\t"
      "ds_read_b64_tr_b16 v[16:17], v232 offset:28672\n\t"
      "ds_read_b64_tr_b16 v[18:19], v232 offset:29184\n\t"
      "v_mfma_f32_16x16x32_bf16 v[164:167], v[180:183], v[100:103], v[164:167]\n\t"
      "v_mfma_f32_16x16x32_bf16 v[164:167], v[184:187], v[116:119], v[164:167]\n\t"
      "v_mfma_f32_16x16x32_bf16 v[168:171], v[180:183], v[104:107], v[168:171]\n\t"
      "v_mfma_f32_16x16x32_bf16 v[168:171], v[184:187], v[120:123], v[168:171]\n\t"
      "ds_read_b64_tr_b16 v[20:21], v233 offset:28672\n\t"
      "ds_read_b64_tr_b16 v[22:23], v233 offset:29184\n\t"
      "v_mfma_f32_16x16x32_bf16 v[172:175], v[180:183], v[108:111], v[172:175]\n\t"
      "ds_read_b128 v[180:183], v236 offset:704\n\t"
      "v_mfma_f32_16x16x32_bf16 v[172:175], v[184:187], v[124:127], v[172:175]\n\t"
      "ds_read_b128 v[184:187], v236 offset:768\n\t"
      "s_waitcnt lgkmcnt(12)\n\t"
      "v_mfma_f32_16x16x32_bf16 v[160:163], v[240:243], v[128:131], v[160:163]\n\t"
      "s_waitcnt lgkmcnt(7)\n\t"
      "v_mfma_f32_16x16x32_bf16 v[160:163], v[220:223], v[144:147], v[160:163]\n\t"
      "ds_read_b64_tr_b16 v[24:25], v234 offset:28672\n\t"
      "ds_read_b64_tr_b16 v[26:27], v234 offset:29184\n\t"
      "v_mfma_f32_16x16x32_bf16 v[164:167], v[240:243], v[132:135], v[164:167]\n\t"
      "v_mfma_f32_16x16x32_bf16 v[164:167], v[220:223], v[148:151], v[164:167]\n\t"
      "v_mfma_f32_16x16x32_bf16 v[168:171], v[240:243], v[136:139], v[168:171]\n\t"
      "v_mfma_f32_16x16x32_bf16 v[168:171], v[220:223], v[152:155], v[168:171]\n\t"
      "ds_read_b64_tr_b16 v[28:29], v235 offset:28672\n\t"
      "ds_read_b64_tr_b16 v[30:31], v235 offset:29184\n\t"
      "v_mfma_f32_16x16x32_bf16 v[172:175], v[240:243], v[140:143], v[172:175]\n\t"
      "ds_read_b128 v[240:243], v236 offset:832\n\t"
      "v_mfma_f32_16x16x32_bf16 v[172:175], v[220:223], v[156:159], v[172:175]\n\t"
      "s_waitcnt lgkmcnt(11)\n\t"
      "v_mfma_f32_16x16x32_bf16 v[160:163], v[176:179], v[188:191], v[160:163]\n\t"
      "s_waitcnt lgkmcnt(6)\n\t"
      "v_mfma_f32_16x16x32_bf16 v[160:163], v[180:183], v[204:207], v[160:163]\n\t"
      "v_mfma_f32_16x16x32_bf16 v[164:167], v[176:179], v[192:195], v[164:167]\n\t"
      "v_mfma_f32_16x16x32_bf16 v[164:167], v[180:183], v[208:211], v[164:167]\n\t"
      "v_mfma_f32_16x16x32_bf16 v[168:171], v[176:179], v[196:199], v[168:171]\n\t"
      "v_mfma_f32_16x16x32_bf16 v[168:171], v[180:183], v[212:215], v[168:171]\n\t"
      "v_mfma_f32_16x16x32_bf16 v[172:175], v[176:179], v[200:203], v[172:175]\n\t"
      "v_mfma_f32_16x16x32_bf16 v[172:175], v[180:183], v[216:219], v[172:175]\n\t"
      "s_waitcnt lgkmcnt(5)\n\t"
      "v_mfma_f32_16x16x32_bf16 v[160:163], v[184:187], v[0:3], v[160:163]\n\t"
      "s_waitcnt lgkmcnt(0)\n\t"
      "v_mfma_f32_16x16x32_bf16 v[160:163], v[240:243], v[16:19], v[160:163]\n\t"
      "v_mfma_f32_16x16x32_bf16 v[164:167], v[184:187], v[4:7], v[164:167]\n\t"
      "s_bitcmp1_b32 s16, 4\n\t"
      "s_cbranch_scc0 HI_%=\n\t"
      "s_mov_b64 exec, 255\n\t"
      "global_store_dwordx2 v245, v[252:253], s[6:7] sc1\n\t"
      "s_mov_b64 exec, -1\n\t"
      "HI_%=:\n\t"
      "v_mfma_f32_16x16x32_bf16 v[164:167], v[240:243], v[20:23], v[164:167]\n\t"
      "v_mfma_f32_16x16x32_bf16 v[168:171], v[184:187], v[8:11], v[168:171]\n\t"
      "v_mfma_f32_16x16x32_bf16 v[168:171], v[240:243], v[24:27], v[168:171]\n\t"
      "v_mfma_f32_16x16x32_bf16 v[172:175], v[184:187], v[12:15], v[172:175]\n\t"
      "v_mfma_f32_16x16x32_bf16 v[172:175], v[240:243], v[28:31], v[172:175]\n\t"
      "v_mul_f32_e32 v192, v160, v160\n\t"
      "v_mul_f32_e32 v193, v161, v161\n\t"
      "v_mul_f32_e32 v194, v162, v162\n\t"
      "v_mul_f32_e32 v195, v163, v163\n\t"
      "v_add_f32_e32 v188, v160, v164\n\t"
      "v_add_f32_e32 v189, v161, v165\n\t"
      "v_add_f32_e32 v190, v162, v166\n\t"
      "v_add_f32_e32 v191, v163, v167\n\t"
      "v_fmac_f32_e32 v192, v164, v164\n\t"
      "v_fmac_f32_e32 v193, v165, v165\n\t"
      "v_fmac_f32_e32 v194, v166, v166\n\t"
      "v_fmac_f32_e32 v195, v167, v167\n\t"
      "v_add_f32_e32 v188, v188, v168\n\t"
      "v_add_f32_e32 v189, v189, v169\n\t"
      "v_add_f32_e32 v190, v190, v170\n\t"
      "v_add_f32_e32 v191, v191, v171\n\t"
      "v_fmac_f32_e32 v192, v168, v168\n\t"
      "v_fmac_f32_e32 v193, v169, v169\n\t"
      "v_fmac_f32_e32 v194, v170, v170\n\t"
      "v_fmac_f32_e32 v195, v171, v171\n\t"
      "v_add_f32_e32 v188, v188, v172\n\t"
      "v_add_f32_e32 v189, v189, v173\n\t"
      "v_add_f32_e32 v190, v190, v174\n\t"
      "v_add_f32_e32 v191, v191, v175\n\t"
      "v_fmac_f32_e32 v192, v172, v172\n\t"
      "v_fmac_f32_e32 v193, v173, v173\n\t"
      "v_fmac_f32_e32 v194, v174, v174\n\t"
      "v_fmac_f32_e32 v195, v175, v175\n\t"
      "s_nop 1\n\t"
      "s_branch GJ_%=\n\t"
      "GY_%=:\n\t"
      ".LhopB2:\n\t"
      "s_cbranch_execz .LbackB\n\t"
      "v_add_u32_e32 v240, 262144, v231\n\t"
      "global_store_dwordx4 v231, v[248:251], s[6:7] sc1\n\t"
      "global_store_dwordx4 v240, v[248:251], s[6:7] sc1\n\t"
      "s_mov_b64 exec, s[42:43]\n\t"
      "global_store_dwordx4 v238, v[248:251], s[6:7] sc1\n\t"
      "s_mov_b64 exec, -1\n\t"
      "ds_read_b128 v[176:179], v236\n\t"
      "ds_read_b128 v[180:183], v236 offset:64\n\t"
      "ds_read_b128 v[184:187], v236 offset:128\n\t"
      "ds_read_b128 v[12:15], v236 offset:192\n\t"
      "ds_read_b128 v[28:31], v236 offset:256\n\t"
      "ds_read_b128 v[44:47], v236 offset:320\n\t"
      "ds_read_b128 v[60:63], v236 offset:384\n\t"
      "ds_read_b128 v[76:79], v236 offset:448\n\t"
      "ds_read_b128 v[92:95], v236 offset:512\n\t"
      "ds_read_b128 v[108:111], v236 offset:576\n\t"
      "ds_read_b128 v[124:127], v236 offset:640\n\t"
      "ds_read_b128 v[140:143], v236 offset:704\n\t"
      "ds_read_b128 v[156:159], v236 offset:768\n\t"
      "ds_read_b128 v[200:203], v236 offset:832\n\t"
      "s_bitcmp1_b32 s16, 7\n\t"
      "s_cbranch_scc0 SY2_%=\n\t"
      "global_store_dwordx4 v237, v[248:251], s[6:7] offset:-4096 sc1\n\t"
      "global_store_dwordx4 v239, v[248:251], s[6:7] offset:-4096 sc1\n\t"
      "SY2_%=:\n\t"
      "s_bitcmp1_b32 s16, 6\n\t"
      "s_cbranch_scc0 SG2_%=\n\t"
      "global_store_dwordx4 v237, v[248:251], s[6:7] sc1\n\t"
      "global_store_dwordx4 v239, v[248:251], s[6:7] sc1\n\t"
      "SG2_%=:\n\t"
      "s_waitcnt lgkmcnt(13)\n\t"
      "v_mfma_f32_16x16x32_bf16 v[160:163], v[176:179], v[0:3], v[160:163]\n\t"
      "s_waitcnt lgkmcnt(12)\n\t"
      "v_mfma_f32_16x16x32_bf16 v[160:163], v[180:183], v[16:19], v[160:163]\n\t"
      "s_waitcnt lgkmcnt(11)\n\t"
      "v_mfma_f32_16x16x32_bf16 v[160:163], v[184:187], v[32:35], v[160:163]\n\t"
      "s_waitcnt lgkmcnt(10)\n\t"
      "v_mfma_f32_16x16x32_bf16 v[160:163], v[12:15], v[48:51], v[160:163]\n\t"
      "ds_read_b64_tr_b16 v[0:1], v232\n\t"
      "ds_read_b64_tr_b16 v[2:3], v232 offset:512\n\t"
      "s_waitcnt lgkmcnt(11)\n\t"
      "v_mfma_f32_16x16x32_bf16 v[160:163], v[28:31], v[64:67], v[160:163]\n\t"
      "ds_read_b64_tr_b16 v[16:17], v232 offset:28672\n\t"
      "ds_read_b64_tr_b16 v[18:19], v232 offset:29184\n\t"
      "s_waitcnt lgkmcnt(12)\n\t"
      "v_mfma_f32_16x16x32_bf16 v[160:163], v[44:47], v[80:83], v[160:163]\n\t"
      "s_waitcnt lgkmcnt(11)\n\t"
      "v_mfma_f32_16x16x32_bf16 v[160:163], v[60:63], v[96:99], v[160:163]\n\t"
      "s_waitcnt lgkmcnt(10)\n\t"
      "v_mfma_f32_16x16x32_bf16 v[160:163], v[76:79], v[112:115], v[160:163]\n\t"
      "s_waitcnt lgkmcnt(9)\n\t"
      "v_mfma_f32_16x16x32_bf16 v[160:163], v[92:95], v[128:131], v[160:163]\n\t"
      "s_waitcnt lgkmcnt(8)\n\t"
      "v_mfma_f32_16x16x32_bf16 v[160:163], v[108:111], v[144:147], v[160:163]\n\t"
      "s_waitcnt lgkmcnt(7)\n\t"
      "v_mfma_f32_16x16x32_bf16 v[160:163], v[124:127], v[188:191], v[160:163]\n\t"
      "s_waitcnt lgkmcnt(6)\n\t"
      "v_mfma_f32_16x16x32_bf16 v[160:163], v[140:143], v[204:207], v[160:163]\n\t"
      "s_waitcnt lgkmcnt(2)\n\t"
      "v_mfma_f32_16x16x32_bf16 v[160:163], v[156:159], v[0:3], v[160:163]\n\t"
      "s_waitcnt lgkmcnt(0)\n\t"
      "v_mfma_f32_16x16x32_bf16 v[160:163], v[200:203], v[16:19], v[160:163]\n\t"
      "v_mfma_f32_16x16x32_bf16 v[164:167], v[176:179], v[4:7], v[164:167]\n\t"
      "v_mfma_f32_16x16x32_bf16 v[164:167], v[180:183], v[20:23], v[164:167]\n\t"
      "v_mfma_f32_16x16x32_bf16 v[164:167], v[184:187], v[36:39], v[164:167]\n\t"
      "v_mfma_f32_16x16x32_bf16 v[164:167], v[12:15], v[52:55], v[164:167]\n\t"
      "ds_read_b64_tr_b16 v[4:5], v233\n\t"
      "ds_read_b64_tr_b16 v[6:7], v233 offset:512\n\t"
      "v_mfma_f32_16x16x32_bf16 v[164:167], v[28:31], v[68:71], v[164:167]\n\t"
      "ds_read_b64_tr_b16 v[20:21], v233 offset:28672\n\t"
      "ds_read_b64_tr_b16 v[22:23], v233 offset:29184\n\t"
      "v_mfma_f32_16x16x32_bf16 v[164:167], v[44:47], v[84:87], v[164:167]\n\t"
      "v_mfma_f32_16x16x32_bf16 v[164:167], v[60:63], v[100:103], v[164:167]\n\t"
      "v_mul_f32_e32 v80, v160, v160\n\t"
      "v_mul_f32_e32 v81, v161, v161\n\t"
      "v_mfma_f32_16x16x32_bf16 v[164:167], v[76:79], v[116:119], v[164:167]\n\t"
      "v_mul_f32_e32 v82, v162, v162\n\t"
      "v_mul_f32_e32 v83, v163, v163\n\t"
      "v_mfma_f32_16x16x32_bf16 v[164:167], v[92:95], v[132:135], v[164:167]\n\t"
      "v_mfma_f32_16x16x32_bf16 v[164:167], v[108:111], v[148:151], v[164:167]\n\t"
      "v_mfma_f32_16x16x32_bf16 v[164:167], v[124:127], v[192:195], v[164:167]\n\t"
      "v_mfma_f32_16x16x32_bf16 v[164:167], v[140:143], v[208:211], v[164:167]\n\t"
      "s_waitcnt lgkmcnt(2)\n\t"
      "v_mfma_f32_16x16x32_bf16 v[164:167], v[156:159], v[4:7], v[164:167]\n\t"
      "s_waitcnt lgkmcnt(0)\n\t"
      "v_mfma_f32_16x16x32_bf16 v[164:167], v[200:203], v[20:23], v[164:167]\n\t"
      "v_mfma_f32_16x16x32_bf16 v[168:171], v[176:179], v[8:11], v[168:171]\n\t"
      "v_mfma_f32_16x16x32_bf16 v[168:171], v[180:183], v[24:27], v[168:171]\n\t"
      "v_mfma_f32_16x16x32_bf16 v[168:171], v[184:187], v[40:43], v[168:171]\n\t"
      "v_mfma_f32_16x16x32_bf16 v[168:171], v[12:15], v[56:59], v[168:171]\n\t"
      "ds_read_b64_tr_b16 v[8:9], v234\n\t"
      "ds_read_b64_tr_b16 v[10:11], v234 offset:512\n\t"
      "v_mfma_f32_16x16x32_bf16 v[168:171], v[28:31], v[72:75], v[168:171]\n\t"
      "ds_read_b64_tr_b16 v[24:25], v234 offset:28672\n\t"
      "ds_read_b64_tr_b16 v[26:27], v234 offset:29184\n\t"
      "v_mfma_f32_16x16x32_bf16 v[168:171], v[44:47], v[88:91], v[168:171]\n\t"
      "v_mfma_f32_16x16x32_bf16 v[168:171], v[60:63], v[104:107], v[168:171]\n\t"
      "v_add_f32_e32 v64, v160, v164\n\t"
      "v_add_f32_e32 v65, v161, v165\n\t"
      "v_mfma_f32_16x16x32_bf16 v[168:171], v[76:79], v[120:123], v[168:171]\n\t"
      "v_add_f32_e32 v66, v162, v166\n\t"
      "v_add_f32_e32 v67, v163, v167\n\t"
      "v_mfma_f32_16x16x32_bf16 v[168:171], v[92:95], v[136:139], v[168:171]\n\t"
      "v_fmac_f32_e32 v80, v164, v164\n\t"
      "v_fmac_f32_e32 v81, v165, v165\n\t"
      "v_mfma_f32_16x16x32_bf16 v[168:171], v[108:111], v[152:155], v[168:171]\n\t"
      "v_fmac_f32_e64 v82, v166, v166\n\t"
      "v_fmac_f32_e32 v83, v167, v167\n\t"
      "v_mfma_f32_16x16x32_bf16 v[168:171], v[124:127], v[196:199], v[168:171]\n\t"
      "v_mfma_f32_16x16x32_bf16 v[168:171], v[140:143], v[212:215], v[168:171]\n\t"
      "s_waitcnt lgkmcnt(2)\n\t"
      "v_mfma_f32_16x16x32_bf16 v[168:171], v[156:159], v[8:11], v[168:171]\n\t"
      "s_waitcnt lgkmcnt(0)\n\t"
      "v_mfma_f32_16x16x32_bf16 v[168:171], v[200:203], v[24:27], v[168:171]\n\t"
      "s_nop 7\n\t"
      "v_pk_add_f32 v[188:189], v[64:65], v[168:169]\n\t"
      "v_pk_fma_f32 v[192:193], v[168:169], v[168:169], v[80:81]\n\t"
      "v_pk_add_f32 v[190:191], v[66:67], v[170:171]\n\t"
      "v_pk_fma_f32 v[194:195], v[170:171], v[170:171], v[82:83]\n\t"
      "GJ_%=:\n\t"
      "v_add_f32_dpp v196, v188, v188 row_ror:8 row_mask:0xf bank_mask:0x3\n\t"
      "v_add_f32_dpp v197, v189, v189 row_ror:8 row_mask:0xf bank_mask:0x3\n\t"
      "v_add_f32_dpp v198, v192, v192 row_ror:8 row_mask:0xf bank_mask:0x3\n\t"
      "v_add_f32_dpp v199, v193, v193 row_ror:8 row_mask:0xf bank_mask:0x3\n\t"
      "v_add_f32_dpp v196, v190, v190 row_ror:8 row_mask:0xf bank_mask:0xc\n\t"
      "v_add_f32_dpp v197, v191, v191 row_ror:8 row_mask:0xf bank_mask:0xc\n\t"
      "v_add_f32_dpp v198, v194, v194 row_ror:8 row_mask:0xf bank_mask:0xc\n\t"
      "v_add_f32_dpp v199, v195, v195 row_ror:8 row_mask:0xf bank_mask:0xc\n\t"
      "v_add_f32_dpp v188, v196, v196 row_shl:4 row_mask:0xf bank_mask:0x5\n\t"
      "v_add_f32_dpp v189, v198, v198 row_shl:4 row_mask:0xf bank_mask:0x5\n\t"
      "s_nop 0\n\t"
      "v_add_f32_dpp v188, v197, v197 row_shr:4 row_mask:0xf bank_mask:0xa\n\t"
      "v_add_f32_dpp v189, v199, v199 row_shr:4 row_mask:0xf bank_mask:0xa\n\t"
      "s_nop 0\n\t"
      "v_add_f32_dpp v188, v188, v188 quad_perm:[1,0,3,2] row_mask:0xf bank_mask:0xf\n\t"
      "v_add_f32_dpp v189, v189, v189 quad_perm:[1,0,3,2] row_mask:0xf bank_mask:0xf\n\t"
      "s_nop 0\n\t"
      "v_add_f32_dpp v188, v188, v188 quad_perm:[2,3,0,1] row_mask:0xf bank_mask:0xf\n\t"
      "v_add_f32_dpp v189, v189, v189 quad_perm:[2,3,0,1] row_mask:0xf bank_mask:0xf\n\t"
      "ds_write_b64 v226, v[188:189]\n\t"
      "s_cbranch_vccz DR_%=\n\t"
      "s_waitcnt vmcnt(0)\n\t"
      "DR_%=:\n\t"
      "s_waitcnt lgkmcnt(0)\n\t"
      "s_barrier\n\t"
      "ds_read_b128 v[188:191], v227\n\t"
      "s_waitcnt lgkmcnt(0)\n\t"
      "v_pk_add_f32 v[188:189], v[188:189], v[190:191]\n\t"
      "s_nop 1\n\t"
      "v_add_f32_dpp v188, v188, v188 row_ror:4 row_mask:0xf bank_mask:0xf\n\t"
      "v_add_f32_dpp v189, v189, v189 row_ror:4 row_mask:0xf bank_mask:0xf\n\t"
      "s_nop 0\n\t"
      "v_add_f32_dpp v188, v188, v188 row_ror:8 row_mask:0xf bank_mask:0xf\n\t"
      "v_add_f32_dpp v189, v189, v189 row_ror:8 row_mask:0xf bank_mask:0xf\n\t"
      "v_mul_f32_e32 v188, 0x3b124925, v188\n\t"
      "v_mul_f32_e32 v189, 0x3b124925, v189\n\t"
      "v_fma_f32 v189, -v188, v188, v189\n\t"
      "v_max_f32_e64 v189, 0, v189\n\t"
      "v_add_f32_e32 v190, 0x3727c5ac, v189\n\t"
      "v_fmac_f32_e32 v189, 0x3727c5ac, v190\n\t"
      "v_rsq_f32_e64 v191, v189\n\t"
      "s_nop 0\n\t"
      "v_mul_f32_e64 v190, -v188, v191\n\t"
      "v_mov_b32_dpp v192, v191 quad_perm:[0,0,0,0] row_mask:0xf bank_mask:0xf\n\t"
      "s_nop 0\n\t"
      "v_mov_b32_dpp v196, v190 quad_perm:[0,0,0,0] row_mask:0xf bank_mask:0xf\n\t"
      "v_mov_b32_dpp v193, v191 quad_perm:[1,1,1,1] row_mask:0xf bank_mask:0xf\n\t"
      "v_mov_b32_dpp v197, v190 quad_perm:[1,1,1,1] row_mask:0xf bank_mask:0xf\n\t"
      "v_mov_b32_dpp v194, v191 quad_perm:[2,2,2,2] row_mask:0xf bank_mask:0xf\n\t"
      "v_mov_b32_dpp v198, v190 quad_perm:[2,2,2,2] row_mask:0xf bank_mask:0xf\n\t"
      "v_mov_b32_dpp v195, v191 quad_perm:[3,3,3,3] row_mask:0xf bank_mask:0xf\n\t"
      "v_mov_b32_dpp v199, v190 quad_perm:[3,3,3,3] row_mask:0xf bank_mask:0xf\n\t"
      "v_pk_fma_f32 v[160:161], v[160:161], v[192:193], v[196:197]\n\t"
      "v_pk_fma_f32 v[162:163], v[162:163], v[194:195], v[198:199]\n\t"
      "v_pk_fma_f32 v[164:165], v[164:165], v[192:193], v[196:197]\n\t"
      "v_pk_fma_f32 v[166:167], v[166:167], v[194:195], v[198:199]\n\t"
      "v_cvt_pk_bf16_f32 v212, v160, v161\n\t"
      "v_cvt_pk_bf16_f32 v213, v162, v163\n\t"
      "v_cvt_pk_bf16_f32 v214, v164, v165\n\t"
      "v_cvt_pk_bf16_f32 v215, v166, v167\n\t"
      "v_min_u32_e32 v212, -2, v212\n\t"
      "v_min_u32_e32 v213, -2, v213\n\t"
      "v_min_u32_e64 v214, -2, v214\n\t"
      "v_min_u32_e32 v215, -2, v215\n\t"
      "s_nop 0\n\t"
      "v_permlane16_swap_b32_e32 v212, v214\n\t"
      "v_permlane16_swap_b32_e32 v213, v215\n\t"
      "global_store_dwordx4 v231, v[212:215], s[6:7] sc1\n\t"
      "v_pk_fma_f32 v[168:169], v[168:169], v[192:193], v[196:197]\n\t"
      "v_pk_fma_f32 v[170:171], v[170:171], v[194:195], v[198:199]\n\t"
      "v_pk_fma_f32 v[172:173], v[172:173], v[192:193], v[196:197]\n\t"
      "v_pk_fma_f32 v[174:175], v[174:175], v[194:195], v[198:199]\n\t"
      "v_cvt_pk_bf16_f32 v216, v168, v169\n\t"
      "v_cvt_pk_bf16_f32 v217, v170, v171\n\t"
      "v_cvt_pk_bf16_f32 v218, v172, v173\n\t"
      "v_cvt_pk_bf16_f32 v219, v174, v175\n\t"
      "v_min_u32_e32 v216, -2, v216\n\t"
      "v_min_u32_e32 v217, -2, v217\n\t"
      "v_min_u32_e32 v218, -2, v218\n\t"
      "v_min_u32_e32 v219, -2, v219\n\t"
      "s_nop 0\n\t"
      "v_permlane16_swap_b32_e32 v216, v218\n\t"
      "v_permlane16_swap_b32_e32 v217, v219\n\t"
      "s_mov_b64 exec, s[42:43]\n\t"
      "global_store_dwordx4 v238, v[216:219], s[6:7] sc1\n\t"
      "s_mov_b64 exec, -1\n\t"
      "v_xor_b32_e32 v0, 64, v229\n\t"
      "v_add_u32_e32 v1, 63488, v229\n\t"
      "v_add_u32_e32 v2, 95232, v0\n\t"
      "v_add_u32_e32 v0, 31744, v0\n\t"
      "s_add_u32 m0, s15, 130560\n\t"
      "s_bitcmp1_b32 s16, 2\n\t"
      "s_cselect_b64 exec, -1, 1\n\t"
      "global_load_lds_dwordx4 v1, s[10:11] offset:2048\n\t"
      "s_mov_b64 exec, -1\n\t"
      "s_bitcmp1_b32 s16, 2\n\t"
      "s_cselect_b64 exec, -1, 1\n\t"
      "global_load_lds_dwordx4 v2, s[10:11] offset:3072\n\t"
      "s_mov_b64 exec, -1\n\t"
      "s_nop 1\n\t"
      "v_mov_b32_e32 v240, v188\n\t"
      "v_mul_f32_e32 v241, v189, v191\n\t"
      "v_add_u32_e32 v246, 65536, v228\n\t"
      "v_xor_b32_e32 v242, s8, v188\n\t"
      "v_xor_b32_e32 v243, s9, v241\n\t"
      "s_nop 1\n\t"
      "s_branch BFX_%=\n\t"
      "PWR_%=:\n\t"
      "s_mov_b32 s51, 0\n\t"
      "PWL_%=:\n\t"
      "s_add_u32 s51, s51, 1\n\t"
      "s_cmp_lt_u32 s51, 262144\n\t"
      "s_cbranch_scc0 PWX_%=\n\t"
      "s_sleep 1\n\t"
      "v_min_u32_e32 v184, 32, v230\n\t"
      "v_add_u32_e32 v184, s28, v184\n\t"
      "global_load_dwordx4 v[180:183], v184, s[6:7] sc1\n\t"
      "s_waitcnt vmcnt(0)\n\t"
      "v_xor_b32_e32 v182, v182, v180\n\t"
      "v_xor_b32_e32 v183, v183, v181\n\t"
      "v_cmp_ne_u64_e32 vcc, s[8:9], v[182:183]\n\t"
      "s_cbranch_vccnz PWL_%=\n\t"
      "PWX_%=:\n\t"
      "v_and_b32_e32 v180, 0x2000, v180\n\t"
      "s_nop 1\n\t"
      "s_branch PWB_%=\n\t"
      "BFX_%=:\n\t"
      ".LhopB3:\n\t"
      "s_cbranch_execz .LbackB\n\t"
      : "+{s[4:11]}"(s1), "+{s[12:15]}"(s3), "+{s16}"(bits), "+{s[22:23]}"(po), "+{s[28:29]}"(par), "+{v[160:175]}"(acc), "+{v[224:231]}"(v1), "+{v[232:239]}"(v), "+{v[240:243]}"(vo), "+{v[244:247]}"(v3)
      :
      : "memory", "vcc", "scc",
        "v0", "v1", "v2", "v3", "v4", "v5", "v6", "v7", "v8", "v9", "v10", "v11", "v12", "v13", "v14", "v15", "v16",
        "v17", "v18", "v19", "v20", "v21", "v22", "v23", "v24", "v25", "v26", "v27", "v28", "v29", "v30", "v31", "v32",
        "v33", "v34", "v35", "v36", "v37", "v38", "v39", "v40", "v41", "v42", "v43", "v44", "v45", "v46", "v47", "v48",
        "v49", "v50", "v51", "v52", "v53", "v54", "v55", "v56", "v57", "v58", "v59", "v60", "v61", "v62", "v63", "v64",
        "v65", "v66", "v67", "v68", "v69", "v70", "v71", "v72", "v73", "v74", "v75", "v76", "v77", "v78", "v79", "v80",
        "v81", "v82", "v83", "v84", "v85", "v86", "v87", "v88", "v89", "v90", "v91", "v92", "v93", "v94", "v95", "v96",
        "v97", "v98", "v99", "v100", "v101", "v102", "v103", "v104", "v105", "v106", "v107", "v108", "v109", "v110",
        "v111", "v112", "v113", "v114", "v115", "v116", "v117", "v118", "v119", "v120", "v121", "v122", "v123", "v124",
        "v125", "v126", "v127", "v128", "v129", "v130", "v131", "v132", "v133", "v134", "v135", "v136", "v137", "v138",
        "v139", "v140", "v141", "v142", "v143", "v144", "v145", "v146", "v147", "v148", "v149", "v150", "v151", "v152",
        "v153", "v154", "v155", "v156", "v157", "v158", "v159", "v176", "v177", "v178", "v179", "v180", "v181", "v182",
        "v183", "v184", "v185", "v186", "v187", "v188", "v189", "v190", "v191", "v192", "v193", "v194", "v195", "v196",
        "v197", "v198", "v199", "v200", "v201", "v202", "v203", "v204", "v205", "v206", "v207", "v208", "v209", "v210",
        "v211", "v212", "v213", "v214", "v215", "v216", "v217", "v218", "v219", "v220", "v221", "v222", "v223", "v248",
        "v249", "v250", "v251", "v252", "v253", "s40", "s41", "s42", "s43", "s44", "s45", "s46", "s47", "s48", "s49",
        "s50", "s51"
    );
    }
    unsigned vq = v3[2];
    PIN();
    LANE_BITS(__builtin_amdgcn_mbcnt_hi(~0u, __builtin_amdgcn_mbcnt_lo(~0u, 0u)));
    u32x8 v;
    u32x4 s2;
    u32x16 s;
    v[0] = lane * 16;
    u64 kr = (u64)(6 - rn) | ((u64)rot << 32);
    v[1] = lr * 4096 + ((lc ^ swz_tr(lr)) << 4);
    v[2] = wave * 4096 + g * 1024 + q4 * 128 + (((2 * (wave & 3) + (p4 >> 1)) ^ swz_tr(8 * g + q4)) << 4) + (p4 & 1) * 8;
    v[3] = wave * 4096 + (wave & 3) * 1024 + ((lane * 16) ^ (g << 6));
    v[4] = (wave & 3) * 1024 + (((p4 * 16 + (lane >> 2)) * 16) ^ (p4 << 6));
    v[5] = (lane >> 2) * 896 + p4 * 8;
    v[6] = c * 2;
    const unsigned mh = (unsigned)par, mg = (unsigned)(par >> 32);
    v[7] = ST_H4 + xp * 16384 + ((side * 8192) ^ mg) + ((ib & 1) * 28 + ((7 * wave) >> 1) + p4) * 128;
    put(s, 0, (u64)(w2 + st * 128 + (wave * 224 + rn * 32) * 4096));
    put(s, 2, (u64)(ws + WS_G + (unit(blk0, wave, 0) ^ mg)));
    put(s, 4, (u64)(out + (64 * st + 16 * (wave & 3)) * 896 + dg * 128));
    unsigned od = (16 - dg) * 32;
    put(s, 6, (u64)(b2 + (64 * st + 16 * (wave & 3)) * 2));
    put(s, 8, (u64)(ws + WS_ST));
    s[12] = bits;
    s[13] = wave * G_WV;
    put(s2, 0, (u64)(ws + WS_HN + (unit(blk0, st >> 2, 0) ^ mh) + (2 * (st & 3) + ((wave >> 1) & 1)) * 1024));
    s2[2] = ST_FL + (64 * st + 16 * (wave & 3)) * 16;
    s2[3] = UNI(wave & 1 ? ~0u : 0u);
    asm volatile(
      "s_waitcnt lgkmcnt(0)\n\ts_nop 4\n\t"
      ".p2align 3\n\t"
      "s_lshr_b32 s31, s17, 2\n\t"
      "v_mov_b32_e32 v112, 163600\n\t"
      "ds_read_b128 v[112:115], v112\n\t"
      "v_xor_b32_e32 v248, 64, v241\n\t"
      "v_add_u32_e32 v249, 63488, v241\n\t"
      "v_add_u32_e32 v250, 95232, v248\n\t"
      "v_add_u32_e32 v248, 31744, v248\n\t"
      "v_xor_b32_e64 v251, 32, v242\n\t"
      "v_xor_b32_e32 v252, 64, v242\n\t"
      "v_xor_b32_e32 v253, 96, v242\n\t"
      "s_bitcmp1_b32 s16, 4\n\t"
      "s_cbranch_scc0 GW2_%=\n\t"
      "s_mov_b32 s48, 0xf000f\n\t"
      "s_mov_b32 s49, 0xf000f\n\t"
      "s_mov_b64 exec, s[48:49]\n\t"
      "global_store_dwordx4 v140, v[136:139], s[12:13] sc1\n\t"
      "s_mov_b64 exec, -1\n\t"
      "GW2_%=:\n\t"
      "s_add_u32 m0, s31, 66048\n\t"
      "s_nop 0\n\t"
      "global_load_lds_dwordx4 v241, s[4:5]\n\t"
      "global_load_lds_dwordx4 v248, s[4:5] offset:1024\n\t"
      "global_load_lds_dwordx4 v249, s[4:5] offset:2048\n\t"
      "global_load_lds_dwordx4 v250, s[4:5] offset:3072\n\t"
      "s_sub_u32 s28, s28, 1\n\t"
      "s_cselect_b32 s48, -6, 1\n\t"
      "s_cselect_b32 s49, -1, 0\n\t"
      "s_mul_i32 s48, s48, 131072\n\t"
      "s_add_u32 s4, s4, s48\n\t"
      "s_addc_u32 s5, s5, s49\n\t"
      "global_load_ushort v236, v246, s[10:11]\n\t"
      "s_waitcnt lgkmcnt(0)\n\t"
      "v_readfirstlane_b32 s14, v112\n\t"
      "v_readfirstlane_b32 s15, v113\n\t"
      "v_readfirstlane_b32 s26, v114\n\t"
      "v_readfirstlane_b32 s27, v115\n\t"
      "s_add_u32 s52, s6, 0\n\t"
      "s_addc_u32 s53, s7, 0\n\t"
      "s_add_u32 s54, s6, 131072\n\t"
      "s_addc_u32 s55, s7, 0\n\t"
      "s_add_u32 s56, s6, 262144\n\t"
      "s_addc_u32 s57, s7, 0\n\t"
      "s_add_u32 s58, s6, 393216\n\t"
      "s_addc_u32 s59, s7, 0\n\t"
      "s_add_u32 s60, s6, 524288\n\t"
      "s_addc_u32 s61, s7, 0\n\t"
      "s_add_u32 s62, s6, 655360\n\t"
      "s_addc_u32 s63, s7, 0\n\t"
      "s_add_u32 s64, s6, 786432\n\t"
      "s_addc_u32 s65, s7, 0\n\t"
      "s_add_u32 s48, s29, 0\n\t"
      "s_cmp_ge_u32 s48, 7\n\t"
      "s_cselect_b32 s49, 7, 0\n\t"
      "s_sub_u32 s48, s48, s49\n\t"
      "v_lshl_add_u32 v224, s48, 10, v240\n\t"
      "s_add_u32 s48, s29, 1\n\t"
      "s_cmp_ge_u32 s48, 7\n\t"
      "s_cselect_b32 s49, 7, 0\n\t"
      "s_sub_u32 s48, s48, s49\n\t"
      "v_lshl_add_u32 v225, s48, 10, v240\n\t"
      "s_add_u32 s48, s29, 2\n\t"
      "s_cmp_ge_u32 s48, 7\n\t"
      "s_cselect_b32 s49, 7, 0\n\t"
      "s_sub_u32 s48, s48, s49\n\t"
      "v_lshl_add_u32 v226, s48, 10, v240\n\t"
      "s_add_u32 s48, s29, 3\n\t"
      "s_cmp_ge_u32 s48, 7\n\t"
      "s_cselect_b32 s49, 7, 0\n\t"
      "s_sub_u32 s48, s48, s49\n\t"
      "v_lshl_add_u32 v227, s48, 10, v240\n\t"
      "s_add_u32 s48, s29, 4\n\t"
      "s_cmp_ge_u32 s48, 7\n\t"
      "s_cselect_b32 s49, 7, 0\n\t"
      "s_sub_u32 s48, s48, s49\n\t"
      "v_lshl_add_u32 v228, s48, 10, v240\n\t"
      "s_add_u32 s48, s29, 5\n\t"
      "s_cmp_ge_u32 s48, 7\n\t"
      "s_cselect_b32 s49, 7, 0\n\t"
      "s_sub_u32 s48, s48, s49\n\t"
      "v_lshl_add_u32 v229, s48, 10, v240\n\t"
      "s_add_u32 s48, s29, 6\n\t"
      "s_cmp_ge_u32 s48, 7\n\t"
      "s_cselect_b32 s49, 7, 0\n\t"
      "s_sub_u32 s48, s48, s49\n\t"
      "v_lshl_add_u32 v230, s48, 10, v240\n\t"
      "v_xor_b32_e32 v220, 1024, v243\n\t"
      "v_xor_b32_e32 v221, 2048, v243\n\t"
      "v_xor_b32_e32 v222, 3072, v243\n\t"
      "s_add_u32 s66, s8, s30\n\t"
      "s_addc_u32 s67, s9, 0\n\t"
      "v_lshrrev_b32_e32 v112, 4, v240\n\t"
      "v_and_b32_e32 v113, 15, v112\n\t"
      "v_lshrrev_b32_e32 v112, 4, v112\n\t"
      "s_lshr_b32 s48, s17, 10\n\t"
      "s_and_b32 s48, s48, 16\n\t"
      "v_lshlrev_b32_e32 v112, 3, v112\n\t"
      "v_sub_u32_e32 v112, v113, v112\n\t"
      "v_add_u32_e32 v112, s48, v112\n\t"
      "v_mov_b32_e32 v114, 0xffff\n\t"
      "v_mov_b32_e32 v115, 0xffff0000\n\t"
      "v_cmp_eq_u32_e32 vcc, 0, v112\n\t"
      "s_nop 1\n\t"
      "v_cndmask_b32_e32 v216, 0, v114, vcc\n\t"
      "v_cmp_eq_u32_e32 vcc, 1, v112\n\t"
      "s_nop 1\n\t"
      "v_cndmask_b32_e32 v216, v216, v115, vcc\n\t"
      "v_cmp_eq_u32_e32 vcc, 2, v112\n\t"
      "s_nop 1\n\t"
      "v_cndmask_b32_e32 v217, 0, v114, vcc\n\t"
      "v_cmp_eq_u32_e32 vcc, 3, v112\n\t"
      "s_nop 1\n\t"
      "v_cndmask_b32_e32 v217, v217, v115, vcc\n\t"
      "v_cmp_eq_u32_e32 vcc, 4, v112\n\t"
      "s_nop 1\n\t"
      "v_cndmask_b32_e32 v218, 0, v114, vcc\n\t"
      "v_cmp_eq_u32_e32 vcc, 5, v112\n\t"
      "s_nop 1\n\t"
      "v_cndmask_b32_e32 v218, v218, v115, vcc\n\t"
      "v_cmp_eq_u32_e32 vcc, 6, v112\n\t"
      "s_nop 1\n\t"
      "v_cndmask_b32_e32 v219, 0, v114, vcc\n\t"
      "v_cmp_eq_u32_e32 vcc, 7, v112\n\t"
      "s_nop 1\n\t"
      "v_cndmask_b32_e32 v219, v219, v115, vcc\n\t"
      "v_and_b32_e32 v116, 0xf0, v240\n\t"
      "v_add_u32_e32 v116, s22, v116\n\t"
      "v_mov_b64_e32 v[184:185], 0\n\t"
      "v_mov_b64_e32 v[186:187], 0\n\t"
      "v_mov_b64_e32 v[188:189], 0\n\t"
      "v_mov_b64_e32 v[190:191], 0\n\t"
      "v_mov_b64_e32 v[192:193], 0\n\t"
      "v_mov_b64_e32 v[194:195], 0\n\t"
      "v_mov_b64_e32 v[196:197], 0\n\t"
      "v_mov_b64_e32 v[198:199], 0\n\t"
      "v_mov_b64_e32 v[200:201], 0\n\t"
      "v_mov_b64_e32 v[202:203], 0\n\t"
      "v_mov_b64_e32 v[204:205], 0\n\t"
      "v_mov_b64_e32 v[206:207], 0\n\t"
      "v_mov_b64_e32 v[208:209], 0\n\t"
      "v_mov_b64_e32 v[210:211], 0\n\t"
      "s_waitcnt vmcnt(5)\n\t"
      "v_add_u32_e32 v242, 66048, v242\n\t"
      "v_add_u32_e32 v251, 66048, v251\n\t"
      "v_add_u32_e32 v252, 66048, v252\n\t"
      "v_add_u32_e32 v253, 66048, v253\n\t"
      "ds_read_b64_tr_b16 v[0:1], v242 offset:64512\n\t"
      "ds_read_b64_tr_b16 v[2:3], v242 offset:65024\n\t"
      "ds_read_b64_tr_b16 v[4:5], v251 offset:64512\n\t"
      "ds_read_b64_tr_b16 v[6:7], v251 offset:65024\n\t"
      "ds_read_b64_tr_b16 v[8:9], v252 offset:64512\n\t"
      "ds_read_b64_tr_b16 v[10:11], v252 offset:65024\n\t"
      "ds_read_b64_tr_b16 v[12:13], v253 offset:64512\n\t"
      "ds_read_b64_tr_b16 v[14:15], v253 offset:65024\n\t"
      "s_waitcnt vmcnt(1)\n\t"
      "s_add_u32 m0, s31, 0\n\t"
      "s_nop 0\n\t"
      "global_load_lds_dwordx4 v241, s[4:5]\n\t"
      "global_load_lds_dwordx4 v248, s[4:5] offset:1024\n\t"
      "global_load_lds_dwordx4 v249, s[4:5] offset:2048\n\t"
      "global_load_lds_dwordx4 v250, s[4:5] offset:3072\n\t"
      "s_sub_u32 s28, s28, 1\n\t"
      "s_cselect_b32 s48, -6, 1\n\t"
      "s_cselect_b32 s49, -1, 0\n\t"
      "s_mul_i32 s48, s48, 131072\n\t"
      "s_add_u32 s4, s4, s48\n\t"
      "s_addc_u32 s5, s5, s49\n\t"
      "ds_read_b64_tr_b16 v[16:17], v242\n\t"
      "ds_read_b64_tr_b16 v[18:19], v242 offset:512\n\t"
      "ds_read_b64_tr_b16 v[20:21], v251\n\t"
      "ds_read_b64_tr_b16 v[22:23], v251 offset:512\n\t"
      "ds_read_b64_tr_b16 v[24:25], v252\n\t"
      "ds_read_b64_tr_b16 v[26:27], v252 offset:512\n\t"
      "ds_read_b64_tr_b16 v[28:29], v253\n\t"
      "ds_read_b64_tr_b16 v[30:31], v253 offset:512\n\t"
      "s_mov_b64 exec, 0\n\t"
      "s_cmpk_eq_u32 s31, 0x0\n\t"
      "s_cbranch_scc1 .LhopC0\n\t"
      "s_cmpk_eq_u32 s31, 0x1000\n\t"
      "s_cbranch_scc1 .LhopC1\n\t"
      "s_cmpk_eq_u32 s31, 0x2000\n\t"
      "s_cbranch_scc1 .LhopC2\n\t"
      "s_cmpk_eq_u32 s31, 0x3000\n\t"
      "s_cbranch_scc1 .LhopC3\n\t"
      "s_cmpk_eq_u32 s31, 0x4000\n\t"
      "s_cbranch_scc1 .LhopC4\n\t"
      "s_cmpk_eq_u32 s31, 0x5000\n\t"
      "s_cbranch_scc1 .LbackC\n\t"
      "s_cmpk_eq_u32 s31, 0x6000\n\t"
      "s_cbranch_scc1 .LbackC\n\t"
      "s_cmpk_eq_u32 s31, 0x7000\n\t"
      "s_cbranch_scc1 .LbackC\n\t"
      ".LbackC:\n\t"
      "s_mov_b64 exec, -1\n\t"
      "s_waitcnt vmcnt(0)\n\t"
      "s_add_u32 m0, s31, 32768\n\t"
      "s_nop 0\n\t"
      "global_load_lds_dwordx4 v241, s[4:5]\n\t"
      "global_load_lds_dwordx4 v248, s[4:5] offset:1024\n\t"
      "global_load_lds_dwordx4 v249, s[4:5] offset:2048\n\t"
      "global_load_lds_dwordx4 v250, s[4:5] offset:3072\n\t"
      "s_sub_u32 s28, s28, 1\n\t"
      "s_cselect_b32 s48, -6, 1\n\t"
      "s_cselect_b32 s49, -1, 0\n\t"
      "s_mul_i32 s48, s48, 131072\n\t"
      "s_add_u32 s4, s4, s48\n\t"
      "s_addc_u32 s5, s5, s49\n\t"
      "s_nop 0\n\t"
      "v_add_u32_e32 v242, 4294901248, v242\n\t"
      "v_add_u32_e32 v251, 4294901248, v251\n\t"
      "v_add_u32_e32 v252, 4294901248, v252\n\t"
      "v_add_u32_e32 v253, 4294901248, v253\n\t"
      "ds_read_b64_tr_b16 v[32:33], v242\n\t"
      "ds_read_b64_tr_b16 v[34:35], v242 offset:512\n\t"
      "ds_read_b64_tr_b16 v[36:37], v251\n\t"
      "ds_read_b64_tr_b16 v[38:39], v251 offset:512\n\t"
      "ds_read_b64_tr_b16 v[40:41], v252\n\t"
      "ds_read_b64_tr_b16 v[42:43], v252 offset:512\n\t"
      "ds_read_b64_tr_b16 v[44:45], v253\n\t"
      "ds_read_b64_tr_b16 v[46:47], v253 offset:512\n\t"
      "s_waitcnt vmcnt(0)\n\t"
      "s_waitcnt lgkmcnt(14)\n\t"
      "s_add_u32 m0, s31, 130560\n\t"
      "s_nop 0\n\t"
      "global_load_lds_dwordx4 v241, s[4:5]\n\t"
      "global_load_lds_dwordx4 v248, s[4:5] offset:1024\n\t"
      "global_load_lds_dwordx4 v249, s[4:5] offset:2048\n\t"
      "global_load_lds_dwordx4 v250, s[4:5] offset:3072\n\t"
      "s_sub_u32 s28, s28, 1\n\t"
      "s_cselect_b32 s48, -6, 1\n\t"
      "s_cselect_b32 s49, -1, 0\n\t"
      "s_mul_i32 s48, s48, 131072\n\t"
      "s_add_u32 s4, s4, s48\n\t"
      "s_addc_u32 s5, s5, s49\n\t"
      "ds_read_b64_tr_b16 v[48:49], v242 offset:32768\n\t"
      "ds_read_b64_tr_b16 v[50:51], v242 offset:33280\n\t"
      "ds_read_b64_tr_b16 v[52:53], v251 offset:32768\n\t"
      "ds_read_b64_tr_b16 v[54:55], v251 offset:33280\n\t"
      "ds_read_b64_tr_b16 v[56:57], v252 offset:32768\n\t"
      "ds_read_b64_tr_b16 v[58:59], v252 offset:33280\n\t"
      "ds_read_b64_tr_b16 v[60:61], v253 offset:32768\n\t"
      "ds_read_b64_tr_b16 v[62:63], v253 offset:33280\n\t"
      "s_bitcmp1_b32 s16, 1\n\t"
      "s_cbranch_scc0 HWQ_%=\n\t"
      "global_load_dwordx4 v[112:115], v116, s[12:13] sc1\n\t"
      "HWQ_%=:\n\t"
      "s_waitcnt vmcnt(0)\n\t"
      "s_waitcnt lgkmcnt(14)\n\t"
      "s_add_u32 m0, s31, 66048\n\t"
      "s_nop 0\n\t"
      "global_load_lds_dwordx4 v241, s[4:5]\n\t"
      "global_load_lds_dwordx4 v248, s[4:5] offset:1024\n\t"
      "global_load_lds_dwordx4 v249, s[4:5] offset:2048\n\t"
      "global_load_lds_dwordx4 v250, s[4:5] offset:3072\n\t"
      "s_sub_u32 s28, s28, 1\n\t"
      "s_cselect_b32 s48, -6, 1\n\t"
      "s_cselect_b32 s49, -1, 0\n\t"
      "s_mul_i32 s48, s48, 131072\n\t"
      "s_add_u32 s4, s4, s48\n\t"
      "s_addc_u32 s5, s5, s49\n\t"
      "v_add_u32_e32 v242, 66048, v242\n\t"
      "v_add_u32_e32 v251, 66048, v251\n\t"
      "v_add_u32_e32 v252, 66048, v252\n\t"
      "v_add_u32_e32 v253, 66048, v253\n\t"
      "ds_read_b64_tr_b16 v[64:65], v242 offset:64512\n\t"
      "ds_read_b64_tr_b16 v[66:67], v242 offset:65024\n\t"
      "ds_read_b64_tr_b16 v[68:69], v251 offset:64512\n\t"
      "ds_read_b64_tr_b16 v[70:71], v251 offset:65024\n\t"
      "ds_read_b64_tr_b16 v[72:73], v252 offset:64512\n\t"
      "ds_read_b64_tr_b16 v[74:75], v252 offset:65024\n\t"
      "ds_read_b64_tr_b16 v[76:77], v253 offset:64512\n\t"
      "ds_read_b64_tr_b16 v[78:79], v253 offset:65024\n\t"
      "s_waitcnt vmcnt(0)\n\t"
      "s_waitcnt lgkmcnt(14)\n\t"
      "s_add_u32 m0, s31, 0\n\t"
      "s_nop 0\n\t"
      "global_load_lds_dwordx4 v241, s[4:5]\n\t"
      "global_load_lds_dwordx4 v248, s[4:5] offset:1024\n\t"
      "global_load_lds_dwordx4 v249, s[4:5] offset:2048\n\t"
      "global_load_lds_dwordx4 v250, s[4:5] offset:3072\n\t"
      "ds_read_b64_tr_b16 v[80:81], v242\n\t"
      "ds_read_b64_tr_b16 v[82:83], v242 offset:512\n\t"
      "ds_read_b64_tr_b16 v[84:85], v251\n\t"
      "ds_read_b64_tr_b16 v[86:87], v251 offset:512\n\t"
      "ds_read_b64_tr_b16 v[88:89], v252\n\t"
      "ds_read_b64_tr_b16 v[90:91], v252 offset:512\n\t"
      "ds_read_b64_tr_b16 v[92:93], v253\n\t"
      "ds_read_b64_tr_b16 v[94:95], v253 offset:512\n\t"
      "s_bitcmp1_b32 s16, 1\n\t"
      "s_cbranch_scc0 HWH_%=\n\t"
      "v_xor_b32_e32 v114, v114, v112\n\t"
      "v_xor_b32_e32 v115, v115, v113\n\t"
      "v_cmp_ne_u64_e32 vcc, s[14:15], v[114:115]\n\t"
      "s_cbranch_vccnz HWR_%=\n\t"
      "HWB_%=:\n\t"
      "s_mov_b32 exec_hi, s23\n\t"
      "s_not_b32 exec_lo, s23\n\t"
      "s_add_u32 s68, s20, 0\n\t"
      "s_addc_u32 s69, s21, 0\n\t"
      "global_load_dwordx4 v[184:187], v240, s[68:69] sc1\n\t"
      "s_add_u32 s68, s20, 131072\n\t"
      "s_addc_u32 s69, s21, 0\n\t"
      "global_load_dwordx4 v[188:191], v240, s[68:69] sc1\n\t"
      "s_add_u32 s68, s20, 262144\n\t"
      "s_addc_u32 s69, s21, 0\n\t"
      "global_load_dwordx4 v[192:195], v240, s[68:69] sc1\n\t"
      "s_add_u32 s68, s20, 393216\n\t"
      "s_addc_u32 s69, s21, 0\n\t"
      "global_load_dwordx4 v[196:199], v240, s[68:69] sc1\n\t"
      "s_add_u32 s68, s20, 524288\n\t"
      "s_addc_u32 s69, s21, 0\n\t"
      "global_load_dwordx4 v[200:203], v240, s[68:69] sc1\n\t"
      "s_add_u32 s68, s20, 655360\n\t"
      "s_addc_u32 s69, s21, 0\n\t"
      "global_load_dwordx4 v[204:207], v240, s[68:69] sc1\n\t"
      "s_add_u32 s68, s20, 786432\n\t"
      "s_addc_u32 s69, s21, 0\n\t"
      "global_load_dwordx4 v[208:211], v240, s[68:69] sc1\n\t"
      "s_mov_b64 exec, -1\n\t"
      "v_lshlrev_b32_e32 v236, 16, v236\n\t"
      "v_add_f32_e32 v236, v236, v112\n\t"
      "v_cvt_pk_bf16_f32 v114, v113, v113\n\t"
      "v_lshlrev_b32_e32 v115, 16, v114\n\t"
      "v_sub_f32_e32 v115, v113, v115\n\t"
      "v_cvt_pk_bf16_f32 v115, v115, v115\n\t"
      "v_mov_b32_e32 v237, v236\n\t"
      "v_mov_b32_e32 v238, v236\n\t"
      "v_mov_b32_e32 v239, v236\n\t"
      "v_and_b32_e32 v120, v216, v115\n\t"
      "v_and_b32_e32 v216, v216, v114\n\t"
      "v_and_b32_e32 v121, v217, v115\n\t"
      "v_and_b32_e32 v217, v217, v114\n\t"
      "v_and_b32_e32 v122, v218, v115\n\t"
      "v_and_b32_e32 v218, v218, v114\n\t"
      "v_and_b32_e32 v123, v219, v115\n\t"
      "v_and_b32_e32 v219, v219, v114\n\t"
      "HWH_%=:\n\t"
      "s_waitcnt vmcnt(0)\n\t"
      "v_add_u32_e32 v242, 4294901248, v242\n\t"
      "v_add_u32_e32 v251, 4294901248, v251\n\t"
      "v_add_u32_e32 v252, 4294901248, v252\n\t"
      "v_add_u32_e32 v253, 4294901248, v253\n\t"
      "ds_read_b64_tr_b16 v[96:97], v242\n\t"
      "ds_read_b64_tr_b16 v[98:99], v242 offset:512\n\t"
      "ds_read_b64_tr_b16 v[100:101], v251\n\t"
      "ds_read_b64_tr_b16 v[102:103], v251 offset:512\n\t"
      "ds_read_b64_tr_b16 v[104:105], v252\n\t"
      "ds_read_b64_tr_b16 v[106:107], v252 offset:512\n\t"
      "ds_read_b64_tr_b16 v[108:109], v253\n\t"
      "ds_read_b64_tr_b16 v[110:111], v253 offset:512\n\t"
      "s_mov_b64 exec, 15\n\t"
      "v_mov_b64_e32 v[232:233], 0\n\t"
      "global_atomic_or_x2 v[232:233], v247, v[232:233], s[12:13] sc0 sc1\n\t"
      "s_mov_b64 exec, -1\n\t"
      "s_waitcnt lgkmcnt(0)\n\t"
      "s_barrier\n\t"
      "s_mov_b64 exec, 15\n\t"
      "s_waitcnt vmcnt(0)\n\t"
      "v_subrev_u32_e32 v232, s14, v232\n\t"
      "v_cmp_ne_u32_e64 s[48:49], s15, v233\n\t"
      "v_subrev_u32_e32 v232, 0, v232\n\t"
      "v_cmp_lt_u32_e32 vcc, 7, v232\n\t"
      "s_mov_b64 exec, -1\n\t"
      "s_or_b64 vcc, vcc, s[48:49]\n\t"
      "s_cbranch_vccz PRY_%=\n\t"
      "s_mov_b32 s51, 0\n\t"
      "s_branch PRE_%=\n\t"
      "PRL_%=:\n\t"
      "s_sleep 4\n\t"
      "s_add_u32 s51, s51, 1\n\t"
      "s_cmp_lt_u32 s51, 262144\n\t"
      "s_cbranch_scc0 PRX_%=\n\t"
      "PRE_%=:\n\t"
      "s_mov_b64 exec, 15\n\t"
      "v_mov_b64_e32 v[232:233], 0\n\t"
      "global_atomic_or_x2 v[232:233], v247, v[232:233], s[12:13] sc0 sc1\n\t"
      "s_waitcnt vmcnt(0)\n\t"
      "v_subrev_u32_e32 v232, s14, v232\n\t"
      "v_cmp_ne_u32_e64 s[48:49], s15, v233\n\t"
      "v_subrev_u32_e32 v232, 0, v232\n\t"
      "v_cmp_lt_u32_e32 vcc, 7, v232\n\t"
      "s_mov_b64 exec, -1\n\t"
      "s_or_b64 vcc, vcc, s[48:49]\n\t"
      "s_cbranch_vccnz PRL_%=\n\t"
      "PRX_%=:\n\t"
      "PRY_%=:\n\t"
      "global_load_dwordx4 v[128:131], v224, s[52:53] sc1\n\t"
      "global_load_dwordx4 v[132:135], v225, s[52:53] sc1\n\t"
      "global_load_dwordx4 v[136:139], v226, s[52:53] sc1\n\t"
      "global_load_dwordx4 v[140:143], v227, s[52:53] sc1\n\t"
      "global_load_dwordx4 v[144:147], v228, s[52:53] sc1\n\t"
      "global_load_dwordx4 v[148:151], v229, s[52:53] sc1\n\t"
      "global_load_dwordx4 v[152:155], v230, s[52:53] sc1\n\t"
      "s_bitcmp1_b32 s16, 1\n\t"
      "s_cbranch_scc0 HWE_%=\n\t"
      "v_max3_u32 v234, v184, v185, v186\n\t"
      "v_max3_u32 v234, v234, v187, v188\n\t"
      "v_max3_u32 v234, v234, v189, v190\n\t"
      "v_max3_u32 v234, v234, v191, v192\n\t"
      "v_max3_u32 v234, v234, v193, v194\n\t"
      "v_max3_u32 v234, v234, v195, v196\n\t"
      "v_max3_u32 v234, v234, v197, v198\n\t"
      "v_max3_u32 v234, v234, v199, v200\n\t"
      "v_max3_u32 v234, v234, v201, v202\n\t"
      "v_max3_u32 v234, v234, v203, v204\n\t"
      "v_max3_u32 v234, v234, v205, v206\n\t"
      "v_max3_u32 v234, v234, v207, v208\n\t"
      "v_max3_u32 v234, v234, v209, v210\n\t"
      "v_max_u32_e32 v234, v234, v211\n\t"
      "v_cmp_eq_u32_e32 vcc, -1, v234\n\t"
      "s_cbranch_vccnz HXR_%=\n\t"
      "HXB_%=:\n\t"
      "v_mfma_f32_16x16x32_bf16 v[156:159], v[184:187], v[120:123], v[236:239]\n\t"
      "v_mfma_f32_16x16x32_bf16 v[160:163], v[188:191], v[120:123], v[236:239]\n\t"
      "v_mfma_f32_16x16x32_bf16 v[164:167], v[192:195], v[120:123], v[236:239]\n\t"
      "v_mfma_f32_16x16x32_bf16 v[168:171], v[196:199], v[120:123], v[236:239]\n\t"
      "v_mfma_f32_16x16x32_bf16 v[172:175], v[200:203], v[120:123], v[236:239]\n\t"
      "v_mfma_f32_16x16x32_bf16 v[176:179], v[204:207], v[120:123], v[236:239]\n\t"
      "v_mfma_f32_16x16x32_bf16 v[180:183], v[208:211], v[120:123], v[236:239]\n\t"
      "v_mfma_f32_16x16x32_bf16 v[156:159], v[184:187], v[216:219], v[156:159]\n\t"
      "v_mfma_f32_16x16x32_bf16 v[160:163], v[188:191], v[216:219], v[160:163]\n\t"
      "v_mfma_f32_16x16x32_bf16 v[164:167], v[192:195], v[216:219], v[164:167]\n\t"
      "v_mfma_f32_16x16x32_bf16 v[168:171], v[196:199], v[216:219], v[168:171]\n\t"
      "v_mfma_f32_16x16x32_bf16 v[172:175], v[200:203], v[216:219], v[172:175]\n\t"
      "v_mfma_f32_16x16x32_bf16 v[176:179], v[204:207], v[216:219], v[176:179]\n\t"
      "v_mfma_f32_16x16x32_bf16 v[180:183], v[208:211], v[216:219], v[180:183]\n\t"
      "HWE_%=:\n\t"
      "s_nop 7\n\t"
      "s_cmp_ge_u32 s17, 65536\n\t"
      "s_cbranch_scc1 GQ_%=\n\t"
      ".p2align 6\n\t"
      "s_setprio 1\n\t"
      "s_waitcnt vmcnt(6)\n\t"
      "v_mfma_f32_16x16x32_bf16 v[184:187], v[128:131], v[0:3], v[156:159]\n\t"
      "v_mfma_f32_16x16x32_bf16 v[188:191], v[128:131], v[4:7], 0\n\t"
      "v_mfma_f32_16x16x32_bf16 v[192:195], v[128:131], v[8:11], 0\n\t"
      "v_mfma_f32_16x16x32_bf16 v[196:199], v[128:131], v[12:15], 0\n\t"
      "global_load_dwordx4 v[128:131], v224, s[54:55] sc1\n\t"
      "s_waitcnt vmcnt(6)\n\t"
      "v_mfma_f32_16x16x32_bf16 v[184:187], v[132:135], v[16:19], v[184:187]\n\t"
      "s_waitcnt vmcnt(5)\n\t"
      "v_mfma_f32_16x16x32_bf16 v[184:187], v[136:139], v[32:35], v[184:187]\n\t"
      "v_mfma_f32_16x16x32_bf16 v[188:191], v[132:135], v[20:23], v[188:191]\n\t"
      "v_mfma_f32_16x16x32_bf16 v[188:191], v[136:139], v[36:39], v[188:191]\n\t"
      "v_mfma_f32_16x16x32_bf16 v[192:195], v[132:135], v[24:27], v[192:195]\n\t"
      "v_mfma_f32_16x16x32_bf16 v[192:195], v[136:139], v[40:43], v[192:195]\n\t"
      "v_mfma_f32_16x16x32_bf16 v[196:199], v[132:135], v[28:31], v[196:199]\n\t"
      "global_load_dwordx4 v[132:135], v225, s[54:55] sc1\n\t"
      "v_mfma_f32_16x16x32_bf16 v[196:199], v[136:139], v[44:47], v[196:199]\n\t"
      "global_load_dwordx4 v[136:139], v226, s[54:55] sc1\n\t"
      "s_waitcnt vmcnt(6)\n\t"
      "v_mfma_f32_16x16x32_bf16 v[184:187], v[140:143], v[48:51], v[184:187]\n\t"
      "s_waitcnt vmcnt(5)\n\t"
      "v_mfma_f32_16x16x32_bf16 v[184:187], v[144:147], v[64:67], v[184:187]\n\t"
      "v_mfma_f32_16x16x32_bf16 v[188:191], v[140:143], v[52:55], v[188:191]\n\t"
      "v_mfma_f32_16x16x32_bf16 v[188:191], v[144:147], v[68:71], v[188:191]\n\t"
      "v_mfma_f32_16x16x32_bf16 v[192:195], v[140:143], v[56:59], v[192:195]\n\t"
      "v_mfma_f32_16x16x32_bf16 v[192:195], v[144:147], v[72:75], v[192:195]\n\t"
      "v_mfma_f32_16x16x32_bf16 v[196:199], v[140:143], v[60:63], v[196:199]\n\t"
      "global_load_dwordx4 v[140:143], v227, s[54:55] sc1\n\t"
      "v_mfma_f32_16x16x32_bf16 v[196:199], v[144:147], v[76:79], v[196:199]\n\t"
      "global_load_dwordx4 v[144:147], v228, s[54:55] sc1\n\t"
      "s_waitcnt vmcnt(6)\n\t"
      "v_mfma_f32_16x16x32_bf16 v[184:187], v[148:151], v[80:83], v[184:187]\n\t"
      "s_waitcnt vmcnt(5)\n\t"
      "v_mfma_f32_16x16x32_bf16 v[184:187], v[152:155], v[96:99], v[184:187]\n\t"
      "v_mfma_f32_16x16x32_bf16 v[188:191], v[148:151], v[84:87], v[188:191]\n\t"
      "v_mfma_f32_16x16x32_bf16 v[188:191], v[152:155], v[100:103], v[188:191]\n\t"
      "s_nop 5\n\t"
      "v_cmp_u_f32_e32 vcc, v184, v185\n\t"
      "v_cmp_u_f32_e64 s[48:49], v186, v187\n\t"
      "v_mfma_f32_16x16x32_bf16 v[192:195], v[148:151], v[88:91], v[192:195]\n\t"
      "v_mfma_f32_16x16x32_bf16 v[192:195], v[152:155], v[104:107], v[192:195]\n\t"
      "s_or_b64 vcc, vcc, s[48:49]\n\t"
      "s_cbranch_scc1 NP0R_%=\n\t"
      "NP0B_%=:\n\t"
      "ds_write_b128 v243, v[184:187]\n\t"
      "v_mfma_f32_16x16x32_bf16 v[196:199], v[148:151], v[92:95], v[196:199]\n\t"
      "global_load_dwordx4 v[148:151], v229, s[54:55] sc1\n\t"
      "v_mfma_f32_16x16x32_bf16 v[196:199], v[152:155], v[108:111], v[196:199]\n\t"
      "global_load_dwordx4 v[152:155], v230, s[54:55] sc1\n\t"
      "ds_write_b128 v220, v[188:191]\n\t"
      "ds_write_b128 v221, v[192:195]\n\t"
      "s_nop 4\n\t"
      "ds_write_b128 v222, v[196:199]\n\t"
      "s_waitcnt lgkmcnt(0)\n\t"
      "s_barrier\n\t"
      "ds_read_b128 v[112:115], v244\n\t"
      "ds_read_b128 v[116:119], v244 offset:4096\n\t"
      "ds_read_b128 v[120:123], v244 offset:8192\n\t"
      "ds_read_b128 v[124:127], v244 offset:12288\n\t"
      "ds_read_b128 v[184:187], v244 offset:16384\n\t"
      "ds_read_b128 v[188:191], v244 offset:20480\n\t"
      "ds_read_b128 v[192:195], v244 offset:24576\n\t"
      "ds_read_b128 v[196:199], v244 offset:28672\n\t"
      "s_waitcnt vmcnt(6)\n\t"
      "v_mfma_f32_16x16x32_bf16 v[200:203], v[128:131], v[0:3], v[160:163]\n\t"
      "v_mfma_f32_16x16x32_bf16 v[204:207], v[128:131], v[4:7], 0\n\t"
      "s_waitcnt lgkmcnt(6)\n\t"
      "v_add_f32_e32 v112, v112, v116\n\t"
      "v_add_f32_e32 v113, v113, v117\n\t"
      "v_mfma_f32_16x16x32_bf16 v[208:211], v[128:131], v[8:11], 0\n\t"
      "v_add_f32_e32 v114, v114, v118\n\t"
      "v_add_f32_e32 v115, v115, v119\n\t"
      "v_mfma_f32_16x16x32_bf16 v[212:215], v[128:131], v[12:15], 0\n\t"
      "global_load_dwordx4 v[128:131], v224, s[56:57] sc1\n\t"
      "s_waitcnt vmcnt(6)\n\t"
      "v_mfma_f32_16x16x32_bf16 v[200:203], v[132:135], v[16:19], v[200:203]\n\t"
      "s_waitcnt lgkmcnt(4)\n\t"
      "v_add_f32_e32 v120, v120, v124\n\t"
      "s_waitcnt vmcnt(5)\n\t"
      "v_mfma_f32_16x16x32_bf16 v[200:203], v[136:139], v[32:35], v[200:203]\n\t"
      "v_add_f32_e32 v121, v121, v125\n\t"
      "v_add_f32_e32 v122, v122, v126\n\t"
      "v_mfma_f32_16x16x32_bf16 v[204:207], v[132:135], v[20:23], v[204:207]\n\t"
      "v_add_f32_e32 v123, v123, v127\n\t"
      "s_waitcnt lgkmcnt(2)\n\t"
      "v_add_f32_e32 v184, v184, v188\n\t"
      "v_mfma_f32_16x16x32_bf16 v[204:207], v[136:139], v[36:39], v[204:207]\n\t"
      "v_add_f32_e32 v185, v185, v189\n\t"
      "v_add_f32_e32 v186, v186, v190\n\t"
      "v_mfma_f32_16x16x32_bf16 v[208:211], v[132:135], v[24:27], v[208:211]\n\t"
      "v_add_f32_e32 v187, v187, v191\n\t"
      "v_add_f32_e32 v112, v112, v120\n\t"
      "v_mfma_f32_16x16x32_bf16 v[208:211], v[136:139], v[40:43], v[208:211]\n\t"
      "v_add_f32_e32 v113, v113, v121\n\t"
      "v_add_f32_e32 v114, v114, v122\n\t"
      "v_mfma_f32_16x16x32_bf16 v[212:215], v[132:135], v[28:31], v[212:215]\n\t"
      "global_load_dwordx4 v[132:135], v225, s[56:57] sc1\n\t"
      "v_add_f32_e32 v115, v115, v123\n\t"
      "v_mfma_f32_16x16x32_bf16 v[212:215], v[136:139], v[44:47], v[212:215]\n\t"
      "global_load_dwordx4 v[136:139], v226, s[56:57] sc1\n\t"
      "s_waitcnt vmcnt(6)\n\t"
      "v_mfma_f32_16x16x32_bf16 v[200:203], v[140:143], v[48:51], v[200:203]\n\t"
      "s_waitcnt lgkmcnt(0)\n\t"
      "v_add_f32_e32 v192, v192, v196\n\t"
      "s_waitcnt vmcnt(5)\n\t"
      "v_mfma_f32_16x16x32_bf16 v[200:203], v[144:147], v[64:67], v[200:203]\n\t"
      "v_add_f32_e32 v193, v193, v197\n\t"
      "v_add_f32_e32 v194, v194, v198\n\t"
      "v_mfma_f32_16x16x32_bf16 v[204:207], v[140:143], v[52:55], v[204:207]\n\t"
      "v_add_f32_e32 v195, v195, v199\n\t"
      "v_add_f32_e32 v184, v184, v192\n\t"
      "v_mfma_f32_16x16x32_bf16 v[204:207], v[144:147], v[68:71], v[204:207]\n\t"
      "v_add_f32_e32 v185, v185, v193\n\t"
      "v_add_f32_e32 v186, v186, v194\n\t"
      "v_mfma_f32_16x16x32_bf16 v[208:211], v[140:143], v[56:59], v[208:211]\n\t"
      "v_add_f32_e32 v187, v187, v195\n\t"
      "v_add_f32_e32 v112, v112, v184\n\t"
      "v_mfma_f32_16x16x32_bf16 v[208:211], v[144:147], v[72:75], v[208:211]\n\t"
      "v_add_f32_e32 v113, v113, v185\n\t"
      "v_add_f32_e32 v114, v114, v186\n\t"
      "v_mfma_f32_16x16x32_bf16 v[212:215], v[140:143], v[60:63], v[212:215]\n\t"
      "global_load_dwordx4 v[140:143], v227, s[56:57] sc1\n\t"
      "v_add_f32_e32 v115, v115, v187\n\t"
      "v_mfma_f32_16x16x32_bf16 v[212:215], v[144:147], v[76:79], v[212:215]\n\t"
      "global_load_dwordx4 v[144:147], v228, s[56:57] sc1\n\t"
      "s_waitcnt vmcnt(6)\n\t"
      "v_mfma_f32_16x16x32_bf16 v[200:203], v[148:151], v[80:83], v[200:203]\n\t"
      "v_cvt_pk_bf16_f32 v116, v112, v113\n\t"
      "s_waitcnt vmcnt(5)\n\t"
      "v_mfma_f32_16x16x32_bf16 v[200:203], v[152:155], v[96:99], v[200:203]\n\t"
      "v_cvt_pk_bf16_f32 v117, v114, v115\n\t"
      "global_store_dwordx2 v245, v[116:117], s[8:9]\n\t"
      "v_mfma_f32_16x16x32_bf16 v[204:207], v[148:151], v[84:87], v[204:207]\n\t"
      "v_mfma_f32_16x16x32_bf16 v[204:207], v[152:155], v[100:103], v[204:207]\n\t"
      "s_nop 3\n\t"
      "v_cmp_u_f32_e32 vcc, v200, v201\n\t"
      "v_cmp_u_f32_e64 s[48:49], v202, v203\n\t"
      "v_mfma_f32_16x16x32_bf16 v[208:211], v[148:151], v[88:91], v[208:211]\n\t"
      "v_mfma_f32_16x16x32_bf16 v[208:211], v[152:155], v[104:107], v[208:211]\n\t"
      "s_or_b64 vcc, vcc, s[48:49]\n\t"
      "s_cbranch_scc1 NP1R_%=\n\t"
      "NP1B_%=:\n\t"
      ".LhopC0:\n\t"
      "s_cbranch_execz .LbackC\n\t"
      "ds_write_b128 v243, v[200:203] offset:32768\n\t"
      "v_mfma_f32_16x16x32_bf16 v[212:215], v[148:151], v[92:95], v[212:215]\n\t"
      "global_load_dwordx4 v[148:151], v229, s[56:57] sc1\n\t"
      "v_mfma_f32_16x16x32_bf16 v[212:215], v[152:155], v[108:111], v[212:215]\n\t"
      "global_load_dwordx4 v[152:155], v230, s[56:57] sc1\n\t"
      "ds_write_b128 v220, v[204:207] offset:32768\n\t"
      "ds_write_b128 v221, v[208:211] offset:32768\n\t"
      "s_nop 4\n\t"
      ".LhopC1:\n\t"
      "s_cbranch_execz .LbackC\n\t"
      "ds_write_b128 v222, v[212:215] offset:32768\n\t"
      "s_waitcnt lgkmcnt(0)\n\t"
      "s_barrier\n\t"
      "ds_read_b128 v[112:115], v244 offset:32768\n\t"
      "ds_read_b128 v[116:119], v244 offset:36864\n\t"
      "ds_read_b128 v[120:123], v244 offset:40960\n\t"
      "ds_read_b128 v[124:127], v244 offset:45056\n\t"
      "ds_read_b128 v[200:203], v244 offset:49152\n\t"
      "ds_read_b128 v[204:207], v244 offset:53248\n\t"
      "ds_read_b128 v[208:211], v244 offset:57344\n\t"
      "ds_read_b128 v[212:215], v244 offset:61440\n\t"
      "s_waitcnt vmcnt(7)\n\t"
      "v_mfma_f32_16x16x32_bf16 v[184:187], v[128:131], v[0:3], v[164:167]\n\t"
      "v_mfma_f32_16x16x32_bf16 v[188:191], v[128:131], v[4:7], 0\n\t"
      "s_waitcnt lgkmcnt(6)\n\t"
      "v_add_f32_e32 v112, v112, v116\n\t"
      "v_add_f32_e32 v113, v113, v117\n\t"
      "v_mfma_f32_16x16x32_bf16 v[192:195], v[128:131], v[8:11], 0\n\t"
      "v_add_f32_e32 v114, v114, v118\n\t"
      "v_add_f32_e32 v115, v115, v119\n\t"
      "v_mfma_f32_16x16x32_bf16 v[196:199], v[128:131], v[12:15], 0\n\t"
      "global_load_dwordx4 v[128:131], v224, s[58:59] sc1\n\t"
      "s_waitcnt vmcnt(7)\n\t"
      "v_mfma_f32_16x16x32_bf16 v[184:187], v[132:135], v[16:19], v[184:187]\n\t"
      "s_waitcnt lgkmcnt(4)\n\t"
      "v_add_f32_e32 v120, v120, v124\n\t"
      "s_waitcnt vmcnt(6)\n\t"
      "v_mfma_f32_16x16x32_bf16 v[184:187], v[136:139], v[32:35], v[184:187]\n\t"
      "v_add_f32_e32 v121, v121, v125\n\t"
      "v_add_f32_e32 v122, v122, v126\n\t"
      "v_mfma_f32_16x16x32_bf16 v[188:191], v[132:135], v[20:23], v[188:191]\n\t"
      "v_add_f32_e32 v123, v123, v127\n\t"
      "s_waitcnt lgkmcnt(2)\n\t"
      "v_add_f32_e32 v200, v200, v204\n\t"
      "v_mfma_f32_16x16x32_bf16 v[188:191], v[136:139], v[36:39], v[188:191]\n\t"
      "v_add_f32_e32 v201, v201, v205\n\t"
      "v_add_f32_e32 v202, v202, v206\n\t"
      "v_mfma_f32_16x16x32_bf16 v[192:195], v[132:135], v[24:27], v[192:195]\n\t"
      "v_add_f32_e32 v203, v203, v207\n\t"
      "v_add_f32_e32 v112, v112, v120\n\t"
      "v_mfma_f32_16x16x32_bf16 v[192:195], v[136:139], v[40:43], v[192:195]\n\t"
      "v_add_f32_e32 v113, v113, v121\n\t"
      "v_add_f32_e32 v114, v114, v122\n\t"
      "v_mfma_f32_16x16x32_bf16 v[196:199], v[132:135], v[28:31], v[196:199]\n\t"
      "global_load_dwordx4 v[132:135], v225, s[58:59] sc1\n\t"
      "v_add_f32_e32 v115, v115, v123\n\t"
      "v_mfma_f32_16x16x32_bf16 v[196:199], v[136:139], v[44:47], v[196:199]\n\t"
      "global_load_dwordx4 v[136:139], v226, s[58:59] sc1\n\t"
      "s_waitcnt vmcnt(7)\n\t"
      "v_mfma_f32_16x16x32_bf16 v[184:187], v[140:143], v[48:51], v[184:187]\n\t"
      "s_waitcnt lgkmcnt(0)\n\t"
      "v_add_f32_e32 v208, v208, v212\n\t"
      "s_waitcnt vmcnt(6)\n\t"
      "v_mfma_f32_16x16x32_bf16 v[184:187], v[144:147], v[64:67], v[184:187]\n\t"
      "v_add_f32_e32 v209, v209, v213\n\t"
      "v_add_f32_e32 v210, v210, v214\n\t"
      "v_mfma_f32_16x16x32_bf16 v[188:191], v[140:143], v[52:55], v[188:191]\n\t"
      "v_add_f32_e32 v211, v211, v215\n\t"
      "v_add_f32_e32 v200, v200, v208\n\t"
      "v_mfma_f32_16x16x32_bf16 v[188:191], v[144:147], v[68:71], v[188:191]\n\t"
      "v_add_f32_e32 v201, v201, v209\n\t"
      "v_add_f32_e32 v202, v202, v210\n\t"
      "v_mfma_f32_16x16x32_bf16 v[192:195], v[140:143], v[56:59], v[192:195]\n\t"
      "v_add_f32_e32 v203, v203, v211\n\t"
      "v_add_f32_e32 v112, v112, v200\n\t"
      "v_mfma_f32_16x16x32_bf16 v[192:195], v[144:147], v[72:75], v[192:195]\n\t"
      "v_add_f32_e32 v113, v113, v201\n\t"
      "v_add_f32_e32 v114, v114, v202\n\t"
      "v_mfma_f32_16x16x32_bf16 v[196:199], v[140:143], v[60:63], v[196:199]\n\t"
      "global_load_dwordx4 v[140:143], v227, s[58:59] sc1\n\t"
      "v_add_f32_e32 v115, v115, v203\n\t"
      "v_mfma_f32_16x16x32_bf16 v[196:199], v[144:147], v[76:79], v[196:199]\n\t"
      "global_load_dwordx4 v[144:147], v228, s[58:59] sc1\n\t"
      "s_waitcnt vmcnt(6)\n\t"
      "v_mfma_f32_16x16x32_bf16 v[184:187], v[148:151], v[80:83], v[184:187]\n\t"
      "v_cvt_pk_bf16_f32 v116, v112, v113\n\t"
      "s_waitcnt vmcnt(5)\n\t"
      "v_mfma_f32_16x16x32_bf16 v[184:187], v[152:155], v[96:99], v[184:187]\n\t"
      "v_cvt_pk_bf16_f32 v117, v114, v115\n\t"
      "global_store_dwordx2 v245, v[116:117], s[8:9] offset:32\n\t"
      "v_mfma_f32_16x16x32_bf16 v[188:191], v[148:151], v[84:87], v[188:191]\n\t"
      "v_mfma_f32_16x16x32_bf16 v[188:191], v[152:155], v[100:103], v[188:191]\n\t"
      "s_nop 3\n\t"
      "v_cmp_u_f32_e32 vcc, v184, v185\n\t"
      "v_cmp_u_f32_e64 s[48:49], v186, v187\n\t"
      "v_mfma_f32_16x16x32_bf16 v[192:195], v[148:151], v[88:91], v[192:195]\n\t"
      "v_mfma_f32_16x16x32_bf16 v[192:195], v[152:155], v[104:107], v[192:195]\n\t"
      "s_or_b64 vcc, vcc, s[48:49]\n\t"
      "s_cbranch_scc1 NP2R_%=\n\t"
      "NP2B_%=:\n\t"
      "ds_write_b128 v243, v[184:187]\n\t"
      "v_mfma_f32_16x16x32_bf16 v[196:199], v[148:151], v[92:95], v[196:199]\n\t"
      "global_load_dwordx4 v[148:151], v229, s[58:59] sc1\n\t"
      "v_mfma_f32_16x16x32_bf16 v[196:199], v[152:155], v[108:111], v[196:199]\n\t"
      "global_load_dwordx4 v[152:155], v230, s[58:59] sc1\n\t"
      "ds_write_b128 v220, v[188:191]\n\t"
      "ds_write_b128 v221, v[192:195]\n\t"
      "s_nop 4\n\t"
      "ds_write_b128 v222, v[196:199]\n\t"
      "s_waitcnt lgkmcnt(0)\n\t"
      "s_barrier\n\t"
      "ds_read_b128 v[112:115], v244\n\t"
      "ds_read_b128 v[116:119], v244 offset:4096\n\t"
      "ds_read_b128 v[120:123], v244 offset:8192\n\t"
      "ds_read_b128 v[124:127], v244 offset:12288\n\t"
      "ds_read_b128 v[184:187], v244 offset:16384\n\t"
      "ds_read_b128 v[188:191], v244 offset:20480\n\t"
      "ds_read_b128 v[192:195], v244 offset:24576\n\t"
      "ds_read_b128 v[196:199], v244 offset:28672\n\t"
      "s_waitcnt vmcnt(7)\n\t"
      "v_mfma_f32_16x16x32_bf16 v[200:203], v[128:131], v[0:3], v[168:171]\n\t"
      "v_mfma_f32_16x16x32_bf16 v[204:207], v[128:131], v[4:7], 0\n\t"
      "s_waitcnt lgkmcnt(6)\n\t"
      "v_add_f32_e32 v112, v112, v116\n\t"
      "v_add_f32_e32 v113, v113, v117\n\t"
      "v_mfma_f32_16x16x32_bf16 v[208:211], v[128:131], v[8:11], 0\n\t"
      "v_add_f32_e32 v114, v114, v118\n\t"
      "v_add_f32_e32 v115, v115, v119\n\t"
      "v_mfma_f32_16x16x32_bf16 v[212:215], v[128:131], v[12:15], 0\n\t"
      "global_load_dwordx4 v[128:131], v224, s[60:61] sc1\n\t"
      "s_waitcnt vmcnt(7)\n\t"
      "v_mfma_f32_16x16x32_bf16 v[200:203], v[132:135], v[16:19], v[200:203]\n\t"
      "s_waitcnt lgkmcnt(4)\n\t"
      "v_add_f32_e32 v120, v120, v124\n\t"
      "s_waitcnt vmcnt(6)\n\t"
      "v_mfma_f32_16x16x32_bf16 v[200:203], v[136:139], v[32:35], v[200:203]\n\t"
      "v_add_f32_e32 v121, v121, v125\n\t"
      "v_add_f32_e32 v122, v122, v126\n\t"
      "v_mfma_f32_16x16x32_bf16 v[204:207], v[132:135], v[20:23], v[204:207]\n\t"
      "v_add_f32_e32 v123, v123, v127\n\t"
      "s_waitcnt lgkmcnt(2)\n\t"
      "v_add_f32_e32 v184, v184, v188\n\t"
      "v_mfma_f32_16x16x32_bf16 v[204:207], v[136:139], v[36:39], v[204:207]\n\t"
      "v_add_f32_e32 v185, v185, v189\n\t"
      "v_add_f32_e32 v186, v186, v190\n\t"
      "v_mfma_f32_16x16x32_bf16 v[208:211], v[132:135], v[24:27], v[208:211]\n\t"
      "v_add_f32_e32 v187, v187, v191\n\t"
      "v_add_f32_e32 v112, v112, v120\n\t"
      "v_mfma_f32_16x16x32_bf16 v[208:211], v[136:139], v[40:43], v[208:211]\n\t"
      "v_add_f32_e32 v113, v113, v121\n\t"
      "v_add_f32_e32 v114, v114, v122\n\t"
      "v_mfma_f32_16x16x32_bf16 v[212:215], v[132:135], v[28:31], v[212:215]\n\t"
      "global_load_dwordx4 v[132:135], v225, s[60:61] sc1\n\t"
      "v_add_f32_e32 v115, v115, v123\n\t"
      "v_mfma_f32_16x16x32_bf16 v[212:215], v[136:139], v[44:47], v[212:215]\n\t"
      "global_load_dwordx4 v[136:139], v226, s[60:61] sc1\n\t"
      "s_waitcnt vmcnt(7)\n\t"
      "v_mfma_f32_16x16x32_bf16 v[200:203], v[140:143], v[48:51], v[200:203]\n\t"
      "s_waitcnt lgkmcnt(0)\n\t"
      "v_add_f32_e32 v192, v192, v196\n\t"
      "s_waitcnt vmcnt(6)\n\t"
      "v_mfma_f32_16x16x32_bf16 v[200:203], v[144:147], v[64:67], v[200:203]\n\t"
      "v_add_f32_e32 v193, v193, v197\n\t"
      "v_add_f32_e32 v194, v194, v198\n\t"
      "v_mfma_f32_16x16x32_bf16 v[204:207], v[140:143], v[52:55], v[204:207]\n\t"
      "v_add_f32_e32 v195, v195, v199\n\t"
      "v_add_f32_e32 v184, v184, v192\n\t"
      "v_mfma_f32_16x16x32_bf16 v[204:207], v[144:147], v[68:71], v[204:207]\n\t"
      "v_add_f32_e32 v185, v185, v193\n\t"
      "v_add_f32_e32 v186, v186, v194\n\t"
      "v_mfma_f32_16x16x32_bf16 v[208:211], v[140:143], v[56:59], v[208:211]\n\t"
      "v_add_f32_e32 v187, v187, v195\n\t"
      "v_add_f32_e32 v112, v112, v184\n\t"
      "v_mfma_f32_16x16x32_bf16 v[208:211], v[144:147], v[72:75], v[208:211]\n\t"
      "v_add_f32_e32 v113, v113, v185\n\t"
      "v_add_f32_e32 v114, v114, v186\n\t"
      "v_mfma_f32_16x16x32_bf16 v[212:215], v[140:143], v[60:63], v[212:215]\n\t"
      "global_load_dwordx4 v[140:143], v227, s[60:61] sc1\n\t"
      "v_add_f32_e32 v115, v115, v187\n\t"
      "v_mfma_f32_16x16x32_bf16 v[212:215], v[144:147], v[76:79], v[212:215]\n\t"
      "global_load_dwordx4 v[144:147], v228, s[60:61] sc1\n\t"
      "s_waitcnt vmcnt(6)\n\t"
      "v_mfma_f32_16x16x32_bf16 v[200:203], v[148:151], v[80:83], v[200:203]\n\t"
      "v_cvt_pk_bf16_f32 v116, v112, v113\n\t"
      "s_waitcnt vmcnt(5)\n\t"
      "v_mfma_f32_16x16x32_bf16 v[200:203], v[152:155], v[96:99], v[200:203]\n\t"
      "v_cvt_pk_bf16_f32 v117, v114, v115\n\t"
      "global_store_dwordx2 v245, v[116:117], s[66:67]\n\t"
      "v_mfma_f32_16x16x32_bf16 v[204:207], v[148:151], v[84:87], v[204:207]\n\t"
      "v_mfma_f32_16x16x32_bf16 v[204:207], v[152:155], v[100:103], v[204:207]\n\t"
      "s_nop 3\n\t"
      "v_cmp_u_f32_e32 vcc, v200, v201\n\t"
      "v_cmp_u_f32_e64 s[48:49], v202, v203\n\t"
      "v_mfma_f32_16x16x32_bf16 v[208:211], v[148:151], v[88:91], v[208:211]\n\t"
      "v_mfma_f32_16x16x32_bf16 v[208:211], v[152:155], v[104:107], v[208:211]\n\t"
      "s_or_b64 vcc, vcc, s[48:49]\n\t"
      "s_cbranch_scc1 NP3R_%=\n\t"
      "NP3B_%=:\n\t"
      "ds_write_b128 v243, v[200:203] offset:32768\n\t"
      "v_mfma_f32_16x16x32_bf16 v[212:215], v[148:151], v[92:95], v[212:215]\n\t"
      "global_load_dwordx4 v[148:151], v229, s[60:61] sc1\n\t"
      "v_mfma_f32_16x16x32_bf16 v[212:215], v[152:155], v[108:111], v[212:215]\n\t"
      "global_load_dwordx4 v[152:155], v230, s[60:61] sc1\n\t"
      "ds_write_b128 v220, v[204:207] offset:32768\n\t"
      "ds_write_b128 v221, v[208:211] offset:32768\n\t"
      "s_nop 4\n\t"
      "ds_write_b128 v222, v[212:215] offset:32768\n\t"
      "s_waitcnt lgkmcnt(0)\n\t"
      "s_barrier\n\t"
      "ds_read_b128 v[112:115], v244 offset:32768\n\t"
      "ds_read_b128 v[116:119], v244 offset:36864\n\t"
      "ds_read_b128 v[120:123], v244 offset:40960\n\t"
      "ds_read_b128 v[124:127], v244 offset:45056\n\t"
      "ds_read_b128 v[200:203], v244 offset:49152\n\t"
      "ds_read_b128 v[204:207], v244 offset:53248\n\t"
      "ds_read_b128 v[208:211], v244 offset:57344\n\t"
      "ds_read_b128 v[212:215], v244 offset:61440\n\t"
      "s_waitcnt vmcnt(7)\n\t"
      "v_mfma_f32_16x16x32_bf16 v[184:187], v[128:131], v[0:3], v[172:175]\n\t"
      "v_mfma_f32_16x16x32_bf16 v[188:191], v[128:131], v[4:7], 0\n\t"
      "s_waitcnt lgkmcnt(6)\n\t"
      "v_add_f32_e32 v112, v112, v116\n\t"
      "v_add_f32_e32 v113, v113, v117\n\t"
      "v_mfma_f32_16x16x32_bf16 v[192:195], v[128:131], v[8:11], 0\n\t"
      "v_add_f32_e32 v114, v114, v118\n\t"
      "v_add_f32_e32 v115, v115, v119\n\t"
      "v_mfma_f32_16x16x32_bf16 v[196:199], v[128:131], v[12:15], 0\n\t"
      "global_load_dwordx4 v[128:131], v224, s[62:63] sc1\n\t"
      "s_waitcnt vmcnt(7)\n\t"
      "v_mfma_f32_16x16x32_bf16 v[184:187], v[132:135], v[16:19], v[184:187]\n\t"
      "s_waitcnt lgkmcnt(4)\n\t"
      "v_add_f32_e32 v120, v120, v124\n\t"
      "s_waitcnt vmcnt(6)\n\t"
      "v_mfma_f32_16x16x32_bf16 v[184:187], v[136:139], v[32:35], v[184:187]\n\t"
      "v_add_f32_e32 v121, v121, v125\n\t"
      "v_add_f32_e32 v122, v122, v126\n\t"
      "v_mfma_f32_16x16x32_bf16 v[188:191], v[132:135], v[20:23], v[188:191]\n\t"
      "v_add_f32_e32 v123, v123, v127\n\t"
      "s_waitcnt lgkmcnt(2)\n\t"
      "v_add_f32_e32 v200, v200, v204\n\t"
      "v_mfma_f32_16x16x32_bf16 v[188:191], v[136:139], v[36:39], v[188:191]\n\t"
      "v_add_f32_e32 v201, v201, v205\n\t"
      "v_add_f32_e32 v202, v202, v206\n\t"
      "v_mfma_f32_16x16x32_bf16 v[192:195], v[132:135], v[24:27], v[192:195]\n\t"
      "v_add_f32_e32 v203, v203, v207\n\t"
      "v_add_f32_e32 v112, v112, v120\n\t"
      "v_mfma_f32_16x16x32_bf16 v[192:195], v[136:139], v[40:43], v[192:195]\n\t"
      "v_add_f32_e32 v113, v113, v121\n\t"
      "v_add_f32_e32 v114, v114, v122\n\t"
      "v_mfma_f32_16x16x32_bf16 v[196:199], v[132:135], v[28:31], v[196:199]\n\t"
      "global_load_dwordx4 v[132:135], v225, s[62:63] sc1\n\t"
      "v_add_f32_e32 v115, v115, v123\n\t"
      "v_mfma_f32_16x16x32_bf16 v[196:199], v[136:139], v[44:47], v[196:199]\n\t"
      "global_load_dwordx4 v[136:139], v226, s[62:63] sc1\n\t"
      "s_waitcnt vmcnt(7)\n\t"
      "v_mfma_f32_16x16x32_bf16 v[184:187], v[140:143], v[48:51], v[184:187]\n\t"
      "s_waitcnt lgkmcnt(0)\n\t"
      "v_add_f32_e32 v208, v208, v212\n\t"
      "s_waitcnt vmcnt(6)\n\t"
      "v_mfma_f32_16x16x32_bf16 v[184:187], v[144:147], v[64:67], v[184:187]\n\t"
      "v_add_f32_e32 v209, v209, v213\n\t"
      "v_add_f32_e32 v210, v210, v214\n\t"
      "v_mfma_f32_16x16x32_bf16 v[188:191], v[140:143], v[52:55], v[188:191]\n\t"
      "v_add_f32_e32 v211, v211, v215\n\t"
      "v_add_f32_e32 v200, v200, v208\n\t"
      "v_mfma_f32_16x16x32_bf16 v[188:191], v[144:147], v[68:71], v[188:191]\n\t"
      "v_add_f32_e32 v201, v201, v209\n\t"
      "v_add_f32_e32 v202, v202, v210\n\t"
      "v_mfma_f32_16x16x32_bf16 v[192:195], v[140:143], v[56:59], v[192:195]\n\t"
      "v_add_f32_e32 v203, v203, v211\n\t"
      "v_add_f32_e32 v112, v112, v200\n\t"
      "v_mfma_f32_16x16x32_bf16 v[192:195], v[144:147], v[72:75], v[192:195]\n\t"
      "v_add_f32_e32 v113, v113, v201\n\t"
      "v_add_f32_e32 v114, v114, v202\n\t"
      "v_mfma_f32_16x16x32_bf16 v[196:199], v[140:143], v[60:63], v[196:199]\n\t"
      "global_load_dwordx4 v[140:143], v227, s[62:63] sc1\n\t"
      "v_add_f32_e32 v115, v115, v203\n\t"
      "v_mfma_f32_16x16x32_bf16 v[196:199], v[144:147], v[76:79], v[196:199]\n\t"
      "global_load_dwordx4 v[144:147], v228, s[62:63] sc1\n\t"
      "s_waitcnt vmcnt(6)\n\t"
      "v_mfma_f32_16x16x32_bf16 v[184:187], v[148:151], v[80:83], v[184:187]\n\t"
      "v_cvt_pk_bf16_f32 v116, v112, v113\n\t"
      "s_waitcnt vmcnt(5)\n\t"
      "v_mfma_f32_16x16x32_bf16 v[184:187], v[152:155], v[96:99], v[184:187]\n\t"
      "v_cvt_pk_bf16_f32 v117, v114, v115\n\t"
      "global_store_dwordx2 v245, v[116:117], s[66:67] offset:32\n\t"
      "v_mfma_f32_16x16x32_bf16 v[188:191], v[148:151], v[84:87], v[188:191]\n\t"
      "v_mfma_f32_16x16x32_bf16 v[188:191], v[152:155], v[100:103], v[188:191]\n\t"
      "s_nop 3\n\t"
      "v_cmp_u_f32_e32 vcc, v184, v185\n\t"
      "v_cmp_u_f32_e64 s[48:49], v186, v187\n\t"
      "v_mfma_f32_16x16x32_bf16 v[192:195], v[148:151], v[88:91], v[192:195]\n\t"
      "v_mfma_f32_16x16x32_bf16 v[192:195], v[152:155], v[104:107], v[192:195]\n\t"
      "s_or_b64 vcc, vcc, s[48:49]\n\t"
      "s_cbranch_scc1 NP4R_%=\n\t"
      "NP4B_%=:\n\t"
      "ds_write_b128 v243, v[184:187]\n\t"
      "v_mfma_f32_16x16x32_bf16 v[196:199], v[148:151], v[92:95], v[196:199]\n\t"
      "global_load_dwordx4 v[148:151], v229, s[62:63] sc1\n\t"
      "v_mfma_f32_16x16x32_bf16 v[196:199], v[152:155], v[108:111], v[196:199]\n\t"
      "global_load_dwordx4 v[152:155], v230, s[62:63] sc1\n\t"
      "ds_write_b128 v220, v[188:191]\n\t"
      "ds_write_b128 v221, v[192:195]\n\t"
      "s_nop 4\n\t"
      "ds_write_b128 v222, v[196:199]\n\t"
      "s_waitcnt lgkmcnt(0)\n\t"
      "s_barrier\n\t"
      "ds_read_b128 v[112:115], v244\n\t"
      "ds_read_b128 v[116:119], v244 offset:4096\n\t"
      "ds_read_b128 v[120:123], v244 offset:8192\n\t"
      "ds_read_b128 v[124:127], v244 offset:12288\n\t"
      "ds_read_b128 v[184:187], v244 offset:16384\n\t"
      "ds_read_b128 v[188:191], v244 offset:20480\n\t"
      "ds_read_b128 v[192:195], v244 offset:24576\n\t"
      "ds_read_b128 v[196:199], v244 offset:28672\n\t"
      "s_waitcnt vmcnt(7)\n\t"
      "v_mfma_f32_16x16x32_bf16 v[200:203], v[128:131], v[0:3], v[176:179]\n\t"
      "v_mfma_f32_16x16x32_bf16 v[204:207], v[128:131], v[4:7], 0\n\t"
      "s_waitcnt lgkmcnt(6)\n\t"
      "v_add_f32_e32 v112, v112, v116\n\t"
      "v_add_f32_e32 v113, v113, v117\n\t"
      "v_mfma_f32_16x16x32_bf16 v[208:211], v[128:131], v[8:11], 0\n\t"
      "v_add_f32_e32 v114, v114, v118\n\t"
      "v_add_f32_e32 v115, v115, v119\n\t"
      "v_mfma_f32_16x16x32_bf16 v[212:215], v[128:131], v[12:15], 0\n\t"
      "global_load_dwordx4 v[128:131], v224, s[64:65] sc1\n\t"
      "s_waitcnt vmcnt(7)\n\t"
      "v_mfma_f32_16x16x32_bf16 v[200:203], v[132:135], v[16:19], v[200:203]\n\t"
      "s_waitcnt lgkmcnt(4)\n\t"
      "v_add_f32_e32 v120, v120, v124\n\t"
      "s_waitcnt vmcnt(6)\n\t"
      "v_mfma_f32_16x16x32_bf16 v[200:203], v[136:139], v[32:35], v[200:203]\n\t"
      "v_add_f32_e32 v121, v121, v125\n\t"
      "v_add_f32_e32 v122, v122, v126\n\t"
      "v_mfma_f32_16x16x32_bf16 v[204:207], v[132:135], v[20:23], v[204:207]\n\t"
      "v_add_f32_e32 v123, v123, v127\n\t"
      "s_waitcnt lgkmcnt(2)\n\t"
      "v_add_f32_e32 v184, v184, v188\n\t"
      "v_mfma_f32_16x16x32_bf16 v[204:207], v[136:139], v[36:39], v[204:207]\n\t"
      "v_add_f32_e32 v185, v185, v189\n\t"
      "v_add_f32_e32 v186, v186, v190\n\t"
      "v_mfma_f32_16x16x32_bf16 v[208:211], v[132:135], v[24:27], v[208:211]\n\t"
      "v_add_f32_e32 v187, v187, v191\n\t"
      "v_add_f32_e32 v112, v112, v120\n\t"
      "v_mfma_f32_16x16x32_bf16 v[208:211], v[136:139], v[40:43], v[208:211]\n\t"
      "v_add_f32_e32 v113, v113, v121\n\t"
      "v_add_f32_e32 v114, v114, v122\n\t"
      "v_mfma_f32_16x16x32_bf16 v[212:215], v[132:135], v[28:31], v[212:215]\n\t"
      "global_load_dwordx4 v[132:135], v225, s[64:65] sc1\n\t"
      "v_add_f32_e32 v115, v115, v123\n\t"
      "v_mfma_f32_16x16x32_bf16 v[212:215], v[136:139], v[44:47], v[212:215]\n\t"
      "global_load_dwordx4 v[136:139], v226, s[64:65] sc1\n\t"
      "s_waitcnt vmcnt(7)\n\t"
      "v_mfma_f32_16x16x32_bf16 v[200:203], v[140:143], v[48:51], v[200:203]\n\t"
      "s_waitcnt lgkmcnt(0)\n\t"
      "v_add_f32_e32 v192, v192, v196\n\t"
      "s_waitcnt vmcnt(6)\n\t"
      "v_mfma_f32_16x16x32_bf16 v[200:203], v[144:147], v[64:67], v[200:203]\n\t"
      "v_add_f32_e32 v193, v193, v197\n\t"
      "v_add_f32_e32 v194, v194, v198\n\t"
      "v_mfma_f32_16x16x32_bf16 v[204:207], v[140:143], v[52:55], v[204:207]\n\t"
      "v_add_f32_e32 v195, v195, v199\n\t"
      "v_add_f32_e32 v184, v184, v192\n\t"
      "v_mfma_f32_16x16x32_bf16 v[204:207], v[144:147], v[68:71], v[204:207]\n\t"
      "v_add_f32_e32 v185, v185, v193\n\t"
      "v_add_f32_e32 v186, v186, v194\n\t"
      "v_mfma_f32_16x16x32_bf16 v[208:211], v[140:143], v[56:59], v[208:211]\n\t"
      "v_add_f32_e32 v187, v187, v195\n\t"
      "v_add_f32_e32 v112, v112, v184\n\t"
      "v_mfma_f32_16x16x32_bf16 v[208:211], v[144:147], v[72:75], v[208:211]\n\t"
      "v_add_f32_e32 v113, v113, v185\n\t"
      "v_add_f32_e32 v114, v114, v186\n\t"
      "v_mfma_f32_16x16x32_bf16 v[212:215], v[140:143], v[60:63], v[212:215]\n\t"
      "global_load_dwordx4 v[140:143], v227, s[64:65] sc1\n\t"
      "v_add_f32_e32 v115, v115, v187\n\t"
      "v_mfma_f32_16x16x32_bf16 v[212:215], v[144:147], v[76:79], v[212:215]\n\t"
      "global_load_dwordx4 v[144:147], v228, s[64:65] sc1\n\t"
      "s_waitcnt vmcnt(6)\n\t"
      "v_mfma_f32_16x16x32_bf16 v[200:203], v[148:151], v[80:83], v[200:203]\n\t"
      "v_cvt_pk_bf16_f32 v116, v112, v113\n\t"
      "s_waitcnt vmcnt(5)\n\t"
      "v_mfma_f32_16x16x32_bf16 v[200:203], v[152:155], v[96:99], v[200:203]\n\t"
      "v_cvt_pk_bf16_f32 v117, v114, v115\n\t"
      "global_store_dwordx2 v245, v[116:117], s[8:9] offset:64\n\t"
      "v_mfma_f32_16x16x32_bf16 v[204:207], v[148:151], v[84:87], v[204:207]\n\t"
      "v_mfma_f32_16x16x32_bf16 v[204:207], v[152:155], v[100:103], v[204:207]\n\t"
      "s_nop 3\n\t"
      "v_cmp_u_f32_e32 vcc, v200, v201\n\t"
      "v_cmp_u_f32_e64 s[48:49], v202, v203\n\t"
      "v_mfma_f32_16x16x32_bf16 v[208:211], v[148:151], v[88:91], v[208:211]\n\t"
      "v_mfma_f32_16x16x32_bf16 v[208:211], v[152:155], v[104:107], v[208:211]\n\t"
      "s_or_b64 vcc, vcc, s[48:49]\n\t"
      "s_cbranch_scc1 NP5R_%=\n\t"
      "NP5B_%=:\n\t"
      "ds_write_b128 v243, v[200:203] offset:32768\n\t"
      "v_mfma_f32_16x16x32_bf16 v[212:215], v[148:151], v[92:95], v[212:215]\n\t"
      "global_load_dwordx4 v[148:151], v229, s[64:65] sc1\n\t"
      "v_mfma_f32_16x16x32_bf16 v[212:215], v[152:155], v[108:111], v[212:215]\n\t"
      "global_load_dwordx4 v[152:155], v230, s[64:65] sc1\n\t"
      "ds_write_b128 v220, v[204:207] offset:32768\n\t"
      "ds_write_b128 v221, v[208:211] offset:32768\n\t"
      "s_nop 4\n\t"
      "ds_write_b128 v222, v[212:215] offset:32768\n\t"
      "s_waitcnt lgkmcnt(0)\n\t"
      "s_barrier\n\t"
      "ds_read_b128 v[112:115], v244 offset:32768\n\t"
      "ds_read_b128 v[116:119], v244 offset:36864\n\t"
      "ds_read_b128 v[120:123], v244 offset:40960\n\t"
      "ds_read_b128 v[124:127], v244 offset:45056\n\t"
      "ds_read_b128 v[200:203], v244 offset:49152\n\t"
      "ds_read_b128 v[204:207], v244 offset:53248\n\t"
      "ds_read_b128 v[208:211], v244 offset:57344\n\t"
      "ds_read_b128 v[212:215], v244 offset:61440\n\t"
      "s_waitcnt vmcnt(7)\n\t"
      "v_mfma_f32_16x16x32_bf16 v[184:187], v[128:131], v[0:3], v[180:183]\n\t"
      "v_mfma_f32_16x16x32_bf16 v[188:191], v[128:131], v[4:7], 0\n\t"
      "v_mfma_f32_16x16x32_bf16 v[192:195], v[128:131], v[8:11], 0\n\t"
      "v_mfma_f32_16x16x32_bf16 v[196:199], v[128:131], v[12:15], 0\n\t"
      "s_waitcnt vmcnt(6)\n\t"
      "v_mfma_f32_16x16x32_bf16 v[184:187], v[132:135], v[16:19], v[184:187]\n\t"
      "s_waitcnt lgkmcnt(6)\n\t"
      "v_add_f32_e32 v112, v112, v116\n\t"
      "s_waitcnt vmcnt(5)\n\t"
      "v_mfma_f32_16x16x32_bf16 v[184:187], v[136:139], v[32:35], v[184:187]\n\t"
      "v_add_f32_e32 v113, v113, v117\n\t"
      "v_add_f32_e32 v114, v114, v118\n\t"
      "v_mfma_f32_16x16x32_bf16 v[188:191], v[132:135], v[20:23], v[188:191]\n\t"
      "v_add_f32_e32 v115, v115, v119\n\t"
      "s_waitcnt lgkmcnt(4)\n\t"
      "v_add_f32_e32 v120, v120, v124\n\t"
      "v_mfma_f32_16x16x32_bf16 v[188:191], v[136:139], v[36:39], v[188:191]\n\t"
      "v_add_f32_e32 v121, v121, v125\n\t"
      "v_add_f32_e32 v122, v122, v126\n\t"
      "v_mfma_f32_16x16x32_bf16 v[192:195], v[132:135], v[24:27], v[192:195]\n\t"
      "v_add_f32_e32 v123, v123, v127\n\t"
      "s_waitcnt lgkmcnt(2)\n\t"
      "v_add_f32_e32 v200, v200, v204\n\t"
      "v_mfma_f32_16x16x32_bf16 v[192:195], v[136:139], v[40:43], v[192:195]\n\t"
      "v_add_f32_e32 v201, v201, v205\n\t"
      "v_add_f32_e32 v202, v202, v206\n\t"
      "v_mfma_f32_16x16x32_bf16 v[196:199], v[132:135], v[28:31], v[196:199]\n\t"
      "v_add_f32_e32 v203, v203, v207\n\t"
      "v_add_f32_e32 v112, v112, v120\n\t"
      "v_mfma_f32_16x16x32_bf16 v[196:199], v[136:139], v[44:47], v[196:199]\n\t"
      "v_add_f32_e32 v113, v113, v121\n\t"
      "s_waitcnt vmcnt(4)\n\t"
      "v_mfma_f32_16x16x32_bf16 v[184:187], v[140:143], v[48:51], v[184:187]\n\t"
      "v_add_f32_e32 v114, v114, v122\n\t"
      "s_waitcnt vmcnt(3)\n\t"
      "v_mfma_f32_16x16x32_bf16 v[184:187], v[144:147], v[64:67], v[184:187]\n\t"
      "v_add_f32_e32 v115, v115, v123\n\t"
      "s_waitcnt lgkmcnt(0)\n\t"
      "v_add_f32_e32 v208, v208, v212\n\t"
      "v_mfma_f32_16x16x32_bf16 v[188:191], v[140:143], v[52:55], v[188:191]\n\t"
      "v_add_f32_e32 v209, v209, v213\n\t"
      "v_add_f32_e32 v210, v210, v214\n\t"
      "v_mfma_f32_16x16x32_bf16 v[188:191], v[144:147], v[68:71], v[188:191]\n\t"
      "v_add_f32_e32 v211, v211, v215\n\t"
      "v_add_f32_e32 v200, v200, v208\n\t"
      "v_mfma_f32_16x16x32_bf16 v[192:195], v[140:143], v[56:59], v[192:195]\n\t"
      "v_add_f32_e32 v201, v201, v209\n\t"
      "v_add_f32_e32 v202, v202, v210\n\t"
      "v_mfma_f32_16x16x32_bf16 v[192:195], v[144:147], v[72:75], v[192:195]\n\t"
      "v_add_f32_e32 v203, v203, v211\n\t"
      "v_add_f32_e32 v112, v112, v200\n\t"
      "v_mfma_f32_16x16x32_bf16 v[196:199], v[140:143], v[60:63], v[196:199]\n\t"
      "v_add_f32_e32 v113, v113, v201\n\t"
      "v_add_f32_e32 v114, v114, v202\n\t"
      "v_mfma_f32_16x16x32_bf16 v[196:199], v[144:147], v[76:79], v[196:199]\n\t"
      "v_add_f32_e32 v115, v115, v203\n\t"
      "s_waitcnt vmcnt(1)\n\t"
      "v_mfma_f32_16x16x32_bf16 v[184:187], v[148:151], v[80:83], v[184:187]\n\t"
      "v_cvt_pk_bf16_f32 v116, v112, v113\n\t"
      "s_waitcnt vmcnt(0)\n\t"
      "v_mfma_f32_16x16x32_bf16 v[184:187], v[152:155], v[96:99], v[184:187]\n\t"
      "v_cvt_pk_bf16_f32 v117, v114, v115\n\t"
      "global_store_dwordx2 v245, v[116:117], s[8:9] offset:96\n\t"
      "v_mfma_f32_16x16x32_bf16 v[188:191], v[148:151], v[84:87], v[188:191]\n\t"
      "v_mfma_f32_16x16x32_bf16 v[188:191], v[152:155], v[100:103], v[188:191]\n\t"
      "s_nop 3\n\t"
      "v_cmp_u_f32_e32 vcc, v184, v185\n\t"
      "v_cmp_u_f32_e64 s[48:49], v186, v187\n\t"
      "v_mfma_f32_16x16x32_bf16 v[192:195], v[148:151], v[88:91], v[192:195]\n\t"
      "v_mfma_f32_16x16x32_bf16 v[192:195], v[152:155], v[104:107], v[192:195]\n\t"
      "s_or_b64 vcc, vcc, s[48:49]\n\t"
      "s_cbranch_scc1 NP6R_%=\n\t"
      "NP6B_%=:\n\t"
      "ds_write_b128 v243, v[184:187]\n\t"
      "v_mfma_f32_16x16x32_bf16 v[196:199], v[148:151], v[92:95], v[196:199]\n\t"
      "v_mfma_f32_16x16x32_bf16 v[196:199], v[152:155], v[108:111], v[196:199]\n\t"
      "ds_write_b128 v220, v[188:191]\n\t"
      "s_nop 1\n\t"
      "ds_write_b128 v221, v[192:195]\n\t"
      "s_nop 3\n\t"
      "ds_write_b128 v222, v[196:199]\n\t"
      "s_waitcnt lgkmcnt(0)\n\t"
      "s_barrier\n\t"
      "ds_read_b128 v[112:115], v244\n\t"
      "ds_read_b128 v[116:119], v244 offset:4096\n\t"
      "ds_read_b128 v[120:123], v244 offset:8192\n\t"
      "ds_read_b128 v[124:127], v244 offset:12288\n\t"
      "ds_read_b128 v[184:187], v244 offset:16384\n\t"
      "ds_read_b128 v[188:191], v244 offset:20480\n\t"
      "ds_read_b128 v[192:195], v244 offset:24576\n\t"
      "ds_read_b128 v[196:199], v244 offset:28672\n\t"
      "s_waitcnt lgkmcnt(6)\n\t"
      "v_add_f32_e32 v112, v112, v116\n\t"
      "v_add_f32_e32 v113, v113, v117\n\t"
      "v_add_f32_e32 v114, v114, v118\n\t"
      "v_add_f32_e32 v115, v115, v119\n\t"
      "s_waitcnt lgkmcnt(4)\n\t"
      "v_add_f32_e32 v120, v120, v124\n\t"
      "v_add_f32_e32 v121, v121, v125\n\t"
      "v_add_f32_e32 v122, v122, v126\n\t"
      "v_add_f32_e32 v123, v123, v127\n\t"
      "s_waitcnt lgkmcnt(2)\n\t"
      "v_add_f32_e32 v184, v184, v188\n\t"
      "v_add_f32_e32 v185, v185, v189\n\t"
      "v_add_f32_e32 v186, v186, v190\n\t"
      "v_add_f32_e32 v187, v187, v191\n\t"
      "v_add_f32_e32 v112, v112, v120\n\t"
      "v_add_f32_e32 v113, v113, v121\n\t"
      "v_add_f32_e32 v114, v114, v122\n\t"
      "v_add_f32_e32 v115, v115, v123\n\t"
      "s_waitcnt lgkmcnt(0)\n\t"
      "v_add_f32_e32 v192, v192, v196\n\t"
      "v_add_f32_e32 v193, v193, v197\n\t"
      "v_add_f32_e32 v194, v194, v198\n\t"
      "v_add_f32_e32 v195, v195, v199\n\t"
      "v_add_f32_e32 v184, v184, v192\n\t"
      "v_add_f32_e32 v185, v185, v193\n\t"
      "v_add_f32_e32 v186, v186, v194\n\t"
      "v_add_f32_e32 v187, v187, v195\n\t"
      "v_add_f32_e32 v112, v112, v184\n\t"
      "v_add_f32_e32 v113, v113, v185\n\t"
      "v_add_f32_e32 v114, v114, v186\n\t"
      "v_add_f32_e32 v115, v115, v187\n\t"
      "v_cvt_pk_bf16_f32 v116, v112, v113\n\t"
      "v_cvt_pk_bf16_f32 v117, v114, v115\n\t"
      "global_store_dwordx2 v245, v[116:117], s[66:67] offset:64\n\t"
      "GX_%=:\n\t"
      "s_endpgm\n\t"
      ".p2align 6\n\t"
      "GQ_%=:\n\t"
      ".LhopC2:\n\t"
      "s_cbranch_execz .LbackC\n\t"
      "s_waitcnt vmcnt(6)\n\t"
      "v_mfma_f32_16x16x32_bf16 v[184:187], v[128:131], v[0:3], 0\n\t"
      "s_waitcnt vmcnt(5)\n\t"
      "v_cmp_eq_u32_e32 vcc, -1, v132\n\t"
      "v_mfma_f32_16x16x32_bf16 v[188:191], v[128:131], v[4:7], 0\n\t"
      "v_mfma_f32_16x16x32_bf16 v[192:195], v[128:131], v[8:11], 0\n\t"
      "s_cbranch_vccnz FQ1R_%=\n\t"
      "FQ1B_%=:\n\t"
      "v_mfma_f32_16x16x32_bf16 v[196:199], v[128:131], v[12:15], 0\n\t"
      "global_load_dwordx4 v[128:131], v224, s[54:55] sc1\n\t"
      "v_mfma_f32_16x16x32_bf16 v[184:187], v[132:135], v[16:19], v[184:187]\n\t"
      "s_waitcnt vmcnt(5)\n\t"
      "v_cmp_eq_u32_e32 vcc, -1, v136\n\t"
      "v_mfma_f32_16x16x32_bf16 v[188:191], v[132:135], v[20:23], v[188:191]\n\t"
      "v_mfma_f32_16x16x32_bf16 v[192:195], v[132:135], v[24:27], v[192:195]\n\t"
      "s_cbranch_vccnz FQ2R_%=\n\t"
      "FQ2B_%=:\n\t"
      "v_mfma_f32_16x16x32_bf16 v[196:199], v[132:135], v[28:31], v[196:199]\n\t"
      "global_load_dwordx4 v[132:135], v225, s[54:55] sc1\n\t"
      "v_mfma_f32_16x16x32_bf16 v[184:187], v[136:139], v[32:35], v[184:187]\n\t"
      "s_waitcnt vmcnt(5)\n\t"
      "v_cmp_eq_u32_e32 vcc, -1, v140\n\t"
      "v_mfma_f32_16x16x32_bf16 v[188:191], v[136:139], v[36:39], v[188:191]\n\t"
      "v_mfma_f32_16x16x32_bf16 v[192:195], v[136:139], v[40:43], v[192:195]\n\t"
      "s_cbranch_vccnz FQ3R_%=\n\t"
      "FQ3B_%=:\n\t"
      "v_mfma_f32_16x16x32_bf16 v[196:199], v[136:139], v[44:47], v[196:199]\n\t"
      "global_load_dwordx4 v[136:139], v226, s[54:55] sc1\n\t"
      "v_mfma_f32_16x16x32_bf16 v[184:187], v[140:143], v[48:51], v[184:187]\n\t"
      "s_waitcnt vmcnt(5)\n\t"
      "v_cmp_eq_u32_e32 vcc, -1, v144\n\t"
      "v_mfma_f32_16x16x32_bf16 v[188:191], v[140:143], v[52:55], v[188:191]\n\t"
      "v_mfma_f32_16x16x32_bf16 v[192:195], v[140:143], v[56:59], v[192:195]\n\t"
      "s_cbranch_vccnz FQ4R_%=\n\t"
      "FQ4B_%=:\n\t"
      "v_mfma_f32_16x16x32_bf16 v[196:199], v[140:143], v[60:63], v[196:199]\n\t"
      "global_load_dwordx4 v[140:143], v227, s[54:55] sc1\n\t"
      "v_mfma_f32_16x16x32_bf16 v[184:187], v[144:147], v[64:67], v[184:187]\n\t"
      "s_waitcnt vmcnt(5)\n\t"
      "v_cmp_eq_u32_e32 vcc, -1, v148\n\t"
      "v_mfma_f32_16x16x32_bf16 v[188:191], v[144:147], v[68:71], v[188:191]\n\t"
      "v_mfma_f32_16x16x32_bf16 v[192:195], v[144:147], v[72:75], v[192:195]\n\t"
      "s_cbranch_vccnz FQ5R_%=\n\t"
      "FQ5B_%=:\n\t"
      "v_mfma_f32_16x16x32_bf16 v[196:199], v[144:147], v[76:79], v[196:199]\n\t"
      "global_load_dwordx4 v[144:147], v228, s[54:55] sc1\n\t"
      "v_mfma_f32_16x16x32_bf16 v[184:187], v[148:151], v[80:83], v[184:187]\n\t"
      "s_waitcnt vmcnt(5)\n\t"
      "v_cmp_eq_u32_e32 vcc, -1, v152\n\t"
      "v_mfma_f32_16x16x32_bf16 v[188:191], v[148:151], v[84:87], v[188:191]\n\t"
      "v_mfma_f32_16x16x32_bf16 v[192:195], v[148:151], v[88:91], v[192:195]\n\t"
      "s_cbranch_vccnz FQ6R_%=\n\t"
      "FQ6B_%=:\n\t"
      "v_mfma_f32_16x16x32_bf16 v[196:199], v[148:151], v[92:95], v[196:199]\n\t"
      "global_load_dwordx4 v[148:151], v229, s[54:55] sc1\n\t"
      "v_mfma_f32_16x16x32_bf16 v[184:187], v[152:155], v[96:99], v[184:187]\n\t"
      "s_waitcnt vmcnt(5)\n\t"
      "v_cmp_eq_u32_e32 vcc, -1, v128\n\t"
      "v_mfma_f32_16x16x32_bf16 v[188:191], v[152:155], v[100:103], v[188:191]\n\t"
      "v_mfma_f32_16x16x32_bf16 v[192:195], v[152:155], v[104:107], v[192:195]\n\t"
      "s_cbranch_vccnz FQ7R_%=\n\t"
      "FQ7B_%=:\n\t"
      "v_mfma_f32_16x16x32_bf16 v[196:199], v[152:155], v[108:111], v[196:199]\n\t"
      "global_load_dwordx4 v[152:155], v230, s[54:55] sc1\n\t"
      "s_nop 0\n\t"
      "v_cmp_u_f32_e32 vcc, v184, v185\n\t"
      "v_cmp_u_f32_e64 s[48:49], v186, v187\n\t"
      "s_nop 0\n\t"
      "s_or_b64 vcc, vcc, s[48:49]\n\t"
      "s_cbranch_scc1 NQ0R_%=\n\t"
      "NQ0B_%=:\n\t"
      "ds_write_b128 v243, v[184:187]\n\t"
      "ds_write_b128 v220, v[188:191]\n\t"
      "ds_write_b128 v221, v[192:195]\n\t"
      "ds_write_b128 v222, v[196:199]\n\t"
      "v_mfma_f32_16x16x32_bf16 v[200:203], v[128:131], v[0:3], 0\n\t"
      "s_waitcnt vmcnt(5)\n\t"
      "v_cmp_eq_u32_e32 vcc, -1, v132\n\t"
      "v_mfma_f32_16x16x32_bf16 v[204:207], v[128:131], v[4:7], 0\n\t"
      "v_mfma_f32_16x16x32_bf16 v[208:211], v[128:131], v[8:11], 0\n\t"
      "s_cbranch_vccnz FQ8R_%=\n\t"
      "FQ8B_%=:\n\t"
      "v_mfma_f32_16x16x32_bf16 v[212:215], v[128:131], v[12:15], 0\n\t"
      "global_load_dwordx4 v[128:131], v224, s[56:57] sc1\n\t"
      "v_mfma_f32_16x16x32_bf16 v[200:203], v[132:135], v[16:19], v[200:203]\n\t"
      "s_waitcnt vmcnt(5)\n\t"
      "v_cmp_eq_u32_e32 vcc, -1, v136\n\t"
      "v_mfma_f32_16x16x32_bf16 v[204:207], v[132:135], v[20:23], v[204:207]\n\t"
      "v_mfma_f32_16x16x32_bf16 v[208:211], v[132:135], v[24:27], v[208:211]\n\t"
      "s_cbranch_vccnz FQ9R_%=\n\t"
      "FQ9B_%=:\n\t"
      "v_mfma_f32_16x16x32_bf16 v[212:215], v[132:135], v[28:31], v[212:215]\n\t"
      "global_load_dwordx4 v[132:135], v225, s[56:57] sc1\n\t"
      "v_mfma_f32_16x16x32_bf16 v[200:203], v[136:139], v[32:35], v[200:203]\n\t"
      "s_waitcnt vmcnt(5)\n\t"
      "v_cmp_eq_u32_e32 vcc, -1, v140\n\t"
      "v_mfma_f32_16x16x32_bf16 v[204:207], v[136:139], v[36:39], v[204:207]\n\t"
      "v_mfma_f32_16x16x32_bf16 v[208:211], v[136:139], v[40:43], v[208:211]\n\t"
      "s_cbranch_vccnz FQ10R_%=\n\t"
      "FQ10B_%=:\n\t"
      "v_mfma_f32_16x16x32_bf16 v[212:215], v[136:139], v[44:47], v[212:215]\n\t"
      "global_load_dwordx4 v[136:139], v226, s[56:57] sc1\n\t"
      "v_mfma_f32_16x16x32_bf16 v[200:203], v[140:143], v[48:51], v[200:203]\n\t"
      "s_waitcnt vmcnt(5)\n\t"
      "v_cmp_eq_u32_e32 vcc, -1, v144\n\t"
      "v_mfma_f32_16x16x32_bf16 v[204:207], v[140:143], v[52:55], v[204:207]\n\t"
      "v_mfma_f32_16x16x32_bf16 v[208:211], v[140:143], v[56:59], v[208:211]\n\t"
      "s_cbranch_vccnz FQ11R_%=\n\t"
      "FQ11B_%=:\n\t"
      "v_mfma_f32_16x16x32_bf16 v[212:215], v[140:143], v[60:63], v[212:215]\n\t"
      "global_load_dwordx4 v[140:143], v227, s[56:57] sc1\n\t"
      "v_mfma_f32_16x16x32_bf16 v[200:203], v[144:147], v[64:67], v[200:203]\n\t"
      "s_waitcnt vmcnt(5)\n\t"
      "v_cmp_eq_u32_e32 vcc, -1, v148\n\t"
      "v_mfma_f32_16x16x32_bf16 v[204:207], v[144:147], v[68:71], v[204:207]\n\t"
      "v_mfma_f32_16x16x32_bf16 v[208:211], v[144:147], v[72:75], v[208:211]\n\t"
      "s_cbranch_vccnz FQ12R_%=\n\t"
      "FQ12B_%=:\n\t"
      "v_mfma_f32_16x16x32_bf16 v[212:215], v[144:147], v[76:79], v[212:215]\n\t"
      "global_load_dwordx4 v[144:147], v228, s[56:57] sc1\n\t"
      "s_waitcnt lgkmcnt(0)\n\t"
      "s_barrier\n\t"
      "s_setprio 2\n\t"
      "v_mfma_f32_16x16x32_bf16 v[200:203], v[148:151], v[80:83], v[200:203]\n\t"
      "s_waitcnt vmcnt(5)\n\t"
      "v_cmp_eq_u32_e32 vcc, -1, v152\n\t"
      "v_mfma_f32_16x16x32_bf16 v[204:207], v[148:151], v[84:87], v[204:207]\n\t"
      "v_mfma_f32_16x16x32_bf16 v[208:211], v[148:151], v[88:91], v[208:211]\n\t"
      "s_cbranch_vccnz FQ13R_%=\n\t"
      "FQ13B_%=:\n\t"
      "v_mfma_f32_16x16x32_bf16 v[212:215], v[148:151], v[92:95], v[212:215]\n\t"
      "global_load_dwordx4 v[148:151], v229, s[56:57] sc1\n\t"
      "v_mfma_f32_16x16x32_bf16 v[200:203], v[152:155], v[96:99], v[200:203]\n\t"
      "s_waitcnt vmcnt(5)\n\t"
      "v_cmp_eq_u32_e32 vcc, -1, v128\n\t"
      "v_mfma_f32_16x16x32_bf16 v[204:207], v[152:155], v[100:103], v[204:207]\n\t"
      "v_mfma_f32_16x16x32_bf16 v[208:211], v[152:155], v[104:107], v[208:211]\n\t"
      "s_cbranch_vccnz FQ14R_%=\n\t"
      "FQ14B_%=:\n\t"
      "v_mfma_f32_16x16x32_bf16 v[212:215], v[152:155], v[108:111], v[212:215]\n\t"
      "global_load_dwordx4 v[152:155], v230, s[56:57] sc1\n\t"
      "s_setprio 0\n\t"
      "v_cmp_u_f32_e32 vcc, v200, v201\n\t"
      "v_cmp_u_f32_e64 s[48:49], v202, v203\n\t"
      "s_nop 0\n\t"
      "s_or_b64 vcc, vcc, s[48:49]\n\t"
      ".LhopC3:\n\t"
      "s_cbranch_execz .LbackC\n\t"
      "s_cbranch_scc1 NQ1R_%=\n\t"
      "NQ1B_%=:\n\t"
      "ds_write_b128 v243, v[200:203] offset:32768\n\t"
      "ds_write_b128 v220, v[204:207] offset:32768\n\t"
      "ds_write_b128 v221, v[208:211] offset:32768\n\t"
      "ds_write_b128 v222, v[212:215] offset:32768\n\t"
      "v_mfma_f32_16x16x32_bf16 v[184:187], v[128:131], v[0:3], 0\n\t"
      "s_waitcnt vmcnt(5)\n\t"
      "v_cmp_eq_u32_e32 vcc, -1, v132\n\t"
      "v_mfma_f32_16x16x32_bf16 v[188:191], v[128:131], v[4:7], 0\n\t"
      ".LhopC4:\n\t"
      "s_cbranch_execz .LbackC\n\t"
      "v_mfma_f32_16x16x32_bf16 v[192:195], v[128:131], v[8:11], 0\n\t"
      "s_cbranch_vccnz FQ15R_%=\n\t"
      "FQ15B_%=:\n\t"
      "v_mfma_f32_16x16x32_bf16 v[196:199], v[128:131], v[12:15], 0\n\t"
      "global_load_dwordx4 v[128:131], v224, s[58:59] sc1\n\t"
      "v_mfma_f32_16x16x32_bf16 v[184:187], v[132:135], v[16:19], v[184:187]\n\t"
      "s_waitcnt vmcnt(5)\n\t"
      "v_cmp_eq_u32_e32 vcc, -1, v136\n\t"
      "v_mfma_f32_16x16x32_bf16 v[188:191], v[132:135], v[20:23], v[188:191]\n\t"
      "v_mfma_f32_16x16x32_bf16 v[192:195], v[132:135], v[24:27], v[192:195]\n\t"
      "s_cbranch_vccnz FQ16R_%=\n\t"
      "FQ16B_%=:\n\t"
      "v_mfma_f32_16x16x32_bf16 v[196:199], v[132:135], v[28:31], v[196:199]\n\t"
      "global_load_dwordx4 v[132:135], v225, s[58:59] sc1\n\t"
      "v_mfma_f32_16x16x32_bf16 v[184:187], v[136:139], v[32:35], v[184:187]\n\t"
      "s_waitcnt vmcnt(5)\n\t"
      "v_cmp_eq_u32_e32 vcc, -1, v140\n\t"
      "v_mfma_f32_16x16x32_bf16 v[188:191], v[136:139], v[36:39], v[188:191]\n\t"
      "v_mfma_f32_16x16x32_bf16 v[192:195], v[136:139], v[40:43], v[192:195]\n\t"
      "s_cbranch_vccnz FQ17R_%=\n\t"
      "FQ17B_%=:\n\t"
      "v_mfma_f32_16x16x32_bf16 v[196:199], v[136:139], v[44:47], v[196:199]\n\t"
      "global_load_dwordx4 v[136:139], v226, s[58:59] sc1\n\t"
      "v_mfma_f32_16x16x32_bf16 v[184:187], v[140:143], v[48:51], v[184:187]\n\t"
      "s_waitcnt vmcnt(5)\n\t"
      "v_cmp_eq_u32_e32 vcc, -1, v144\n\t"
      "v_mfma_f32_16x16x32_bf16 v[188:191], v[140:143], v[52:55], v[188:191]\n\t"
      "v_mfma_f32_16x16x32_bf16 v[192:195], v[140:143], v[56:59], v[192:195]\n\t"
      "s_cbranch_vccnz FQ18R_%=\n\t"
      "FQ18B_%=:\n\t"
      "v_mfma_f32_16x16x32_bf16 v[196:199], v[140:143], v[60:63], v[196:199]\n\t"
      "global_load_dwordx4 v[140:143], v227, s[58:59] sc1\n\t"
      "v_mfma_f32_16x16x32_bf16 v[184:187], v[144:147], v[64:67], v[184:187]\n\t"
      "s_waitcnt vmcnt(5)\n\t"
      "v_cmp_eq_u32_e32 vcc, -1, v148\n\t"
      "v_mfma_f32_16x16x32_bf16 v[188:191], v[144:147], v[68:71], v[188:191]\n\t"
      "v_mfma_f32_16x16x32_bf16 v[192:195], v[144:147], v[72:75], v[192:195]\n\t"
      "s_cbranch_vccnz FQ19R_%=\n\t"
      "FQ19B_%=:\n\t"
      "v_mfma_f32_16x16x32_bf16 v[196:199], v[144:147], v[76:79], v[196:199]\n\t"
      "global_load_dwordx4 v[144:147], v228, s[58:59] sc1\n\t"
      "s_waitcnt lgkmcnt(0)\n\t"
      "s_barrier\n\t"
      "s_setprio 2\n\t"
      "v_mfma_f32_16x16x32_bf16 v[184:187], v[148:151], v[80:83], v[184:187]\n\t"
      "s_waitcnt vmcnt(5)\n\t"
      "v_cmp_eq_u32_e32 vcc, -1, v152\n\t"
      "v_mfma_f32_16x16x32_bf16 v[188:191], v[148:151], v[84:87], v[188:191]\n\t"
      "v_mfma_f32_16x16x32_bf16 v[192:195], v[148:151], v[88:91], v[192:195]\n\t"
      "s_cbranch_vccnz FQ20R_%=\n\t"
      "FQ20B_%=:\n\t"
      "v_mfma_f32_16x16x32_bf16 v[196:199], v[148:151], v[92:95], v[196:199]\n\t"
      "global_load_dwordx4 v[148:151], v229, s[58:59] sc1\n\t"
      "v_mfma_f32_16x16x32_bf16 v[184:187], v[152:155], v[96:99], v[184:187]\n\t"
      "s_waitcnt vmcnt(5)\n\t"
      "v_cmp_eq_u32_e32 vcc, -1, v128\n\t"
      "v_mfma_f32_16x16x32_bf16 v[188:191], v[152:155], v[100:103], v[188:191]\n\t"
      "v_mfma_f32_16x16x32_bf16 v[192:195], v[152:155], v[104:107], v[192:195]\n\t"
      "s_cbranch_vccnz FQ21R_%=\n\t"
      "FQ21B_%=:\n\t"
      "v_mfma_f32_16x16x32_bf16 v[196:199], v[152:155], v[108:111], v[196:199]\n\t"
      "global_load_dwordx4 v[152:155], v230, s[58:59] sc1\n\t"
      "s_setprio 0\n\t"
      "v_cmp_u_f32_e32 vcc, v184, v185\n\t"
      "v_cmp_u_f32_e64 s[48:49], v186, v187\n\t"
      "s_nop 0\n\t"
      "s_or_b64 vcc, vcc, s[48:49]\n\t"
      "s_cbranch_scc1 NQ2R_%=\n\t"
      "NQ2B_%=:\n\t"
      "ds_write_b128 v243, v[184:187]\n\t"
      "ds_write_b128 v220, v[188:191]\n\t"
      "ds_write_b128 v221, v[192:195]\n\t"
      "ds_write_b128 v222, v[196:199]\n\t"
      "v_mfma_f32_16x16x32_bf16 v[200:203], v[128:131], v[0:3], 0\n\t"
      "s_waitcnt vmcnt(5)\n\t"
      "v_cmp_eq_u32_e32 vcc, -1, v132\n\t"
      "v_mfma_f32_16x16x32_bf16 v[204:207], v[128:131], v[4:7], 0\n\t"
      "v_mfma_f32_16x16x32_bf16 v[208:211], v[128:131], v[8:11], 0\n\t"
      "s_cbranch_vccnz FQ22R_%=\n\t"
      "FQ22B_%=:\n\t"
      "v_mfma_f32_16x16x32_bf16 v[212:215], v[128:131], v[12:15], 0\n\t"
      "global_load_dwordx4 v[128:131], v224, s[60:61] sc1\n\t"
      "v_mfma_f32_16x16x32_bf16 v[200:203], v[132:135], v[16:19], v[200:203]\n\t"
      "s_waitcnt vmcnt(5)\n\t"
      "v_cmp_eq_u32_e32 vcc, -1, v136\n\t"
      "v_mfma_f32_16x16x32_bf16 v[204:207], v[132:135], v[20:23], v[204:207]\n\t"
      "v_mfma_f32_16x16x32_bf16 v[208:211], v[132:135], v[24:27], v[208:211]\n\t"
      "s_cbranch_vccnz FQ23R_%=\n\t"
      "FQ23B_%=:\n\t"
      "v_mfma_f32_16x16x32_bf16 v[212:215], v[132:135], v[28:31], v[212:215]\n\t"
      "global_load_dwordx4 v[132:135], v225, s[60:61] sc1\n\t"
      "v_mfma_f32_16x16x32_bf16 v[200:203], v[136:139], v[32:35], v[200:203]\n\t"
      "s_waitcnt vmcnt(5)\n\t"
      "v_cmp_eq_u32_e32 vcc, -1, v140\n\t"
      "v_mfma_f32_16x16x32_bf16 v[204:207], v[136:139], v[36:39], v[204:207]\n\t"
      "v_mfma_f32_16x16x32_bf16 v[208:211], v[136:139], v[40:43], v[208:211]\n\t"
      "s_cbranch_vccnz FQ24R_%=\n\t"
      "FQ24B_%=:\n\t"
      "v_mfma_f32_16x16x32_bf16 v[212:215], v[136:139], v[44:47], v[212:215]\n\t"
      "global_load_dwordx4 v[136:139], v226, s[60:61] sc1\n\t"
      "v_mfma_f32_16x16x32_bf16 v[200:203], v[140:143], v[48:51], v[200:203]\n\t"
      "s_waitcnt vmcnt(5)\n\t"
      "v_cmp_eq_u32_e32 vcc, -1, v144\n\t"
      "v_mfma_f32_16x16x32_bf16 v[204:207], v[140:143], v[52:55], v[204:207]\n\t"
      "v_mfma_f32_16x16x32_bf16 v[208:211], v[140:143], v[56:59], v[208:211]\n\t"
      "s_cbranch_vccnz FQ25R_%=\n\t"
      "FQ25B_%=:\n\t"
      "v_mfma_f32_16x16x32_bf16 v[212:215], v[140:143], v[60:63], v[212:215]\n\t"
      "global_load_dwordx4 v[140:143], v227, s[60:61] sc1\n\t"
      "v_mfma_f32_16x16x32_bf16 v[200:203], v[144:147], v[64:67], v[200:203]\n\t"
      "s_waitcnt vmcnt(5)\n\t"
      "v_cmp_eq_u32_e32 vcc, -1, v148\n\t"
      "v_mfma_f32_16x16x32_bf16 v[204:207], v[144:147], v[68:71], v[204:207]\n\t"
      "v_mfma_f32_16x16x32_bf16 v[208:211], v[144:147], v[72:75], v[208:211]\n\t"
      "s_cbranch_vccnz FQ26R_%=\n\t"
      "FQ26B_%=:\n\t"
      "v_mfma_f32_16x16x32_bf16 v[212:215], v[144:147], v[76:79], v[212:215]\n\t"
      "global_load_dwordx4 v[144:147], v228, s[60:61] sc1\n\t"
      "s_waitcnt lgkmcnt(0)\n\t"
      "s_barrier\n\t"
      "s_setprio 2\n\t"
      "v_mfma_f32_16x16x32_bf16 v[200:203], v[148:151], v[80:83], v[200:203]\n\t"
      "s_waitcnt vmcnt(5)\n\t"
      "v_cmp_eq_u32_e32 vcc, -1, v152\n\t"
      "v_mfma_f32_16x16x32_bf16 v[204:207], v[148:151], v[84:87], v[204:207]\n\t"
      "v_mfma_f32_16x16x32_bf16 v[208:211], v[148:151], v[88:91], v[208:211]\n\t"
      "s_cbranch_vccnz FQ27R_%=\n\t"
      "FQ27B_%=:\n\t"
      "v_mfma_f32_16x16x32_bf16 v[212:215], v[148:151], v[92:95], v[212:215]\n\t"
      "global_load_dwordx4 v[148:151], v229, s[60:61] sc1\n\t"
      "v_mfma_f32_16x16x32_bf16 v[200:203], v[152:155], v[96:99], v[200:203]\n\t"
      "s_waitcnt vmcnt(5)\n\t"
      "v_cmp_eq_u32_e32 vcc, -1, v128\n\t"
      "v_mfma_f32_16x16x32_bf16 v[204:207], v[152:155], v[100:103], v[204:207]\n\t"
      "v_mfma_f32_16x16x32_bf16 v[208:211], v[152:155], v[104:107], v[208:211]\n\t"
      "s_cbranch_vccnz FQ28R_%=\n\t"
      "FQ28B_%=:\n\t"
      "v_mfma_f32_16x16x32_bf16 v[212:215], v[152:155], v[108:111], v[212:215]\n\t"
      "global_load_dwordx4 v[152:155], v230, s[60:61] sc1\n\t"
      "s_setprio 0\n\t"
      "v_cmp_u_f32_e32 vcc, v200, v201\n\t"
      "v_cmp_u_f32_e64 s[48:49], v202, v203\n\t"
      "s_nop 0\n\t"
      "s_or_b64 vcc, vcc, s[48:49]\n\t"
      "s_cbranch_scc1 NQ3R_%=\n\t"
      "NQ3B_%=:\n\t"
      "ds_write_b128 v243, v[200:203] offset:32768\n\t"
      "ds_write_b128 v220, v[204:207] offset:32768\n\t"
      "ds_write_b128 v221, v[208:211] offset:32768\n\t"
      "ds_write_b128 v222, v[212:215] offset:32768\n\t"
      "v_mfma_f32_16x16x32_bf16 v[184:187], v[128:131], v[0:3], 0\n\t"
      "s_waitcnt vmcnt(5)\n\t"
      "v_cmp_eq_u32_e32 vcc, -1, v132\n\t"
      "v_mfma_f32_16x16x32_bf16 v[188:191], v[128:131], v[4:7], 0\n\t"
      "v_mfma_f32_16x16x32_bf16 v[192:195], v[128:131], v[8:11], 0\n\t"
      "s_cbranch_vccnz FQ29R_%=\n\t"
      "FQ29B_%=:\n\t"
      "v_mfma_f32_16x16x32_bf16 v[196:199], v[128:131], v[12:15], 0\n\t"
      "global_load_dwordx4 v[128:131], v224, s[62:63] sc1\n\t"
      "v_mfma_f32_16x16x32_bf16 v[184:187], v[132:135], v[16:19], v[184:187]\n\t"
      "s_waitcnt vmcnt(5)\n\t"
      "v_cmp_eq_u32_e32 vcc, -1, v136\n\t"
      "v_mfma_f32_16x16x32_bf16 v[188:191], v[132:135], v[20:23], v[188:191]\n\t"
      "v_mfma_f32_16x16x32_bf16 v[192:195], v[132:135], v[24:27], v[192:195]\n\t"
      "s_cbranch_vccnz FQ30R_%=\n\t"
      "FQ30B_%=:\n\t"
      "v_mfma_f32_16x16x32_bf16 v[196:199], v[132:135], v[28:31], v[196:199]\n\t"
      "global_load_dwordx4 v[132:135], v225, s[62:63] sc1\n\t"
      "v_mfma_f32_16x16x32_bf16 v[184:187], v[136:139], v[32:35], v[184:187]\n\t"
      "s_waitcnt vmcnt(5)\n\t"
      "v_cmp_eq_u32_e32 vcc, -1, v140\n\t"
      "v_mfma_f32_16x16x32_bf16 v[188:191], v[136:139], v[36:39], v[188:191]\n\t"
      "v_mfma_f32_16x16x32_bf16 v[192:195], v[136:139], v[40:43], v[192:195]\n\t"
      "s_cbranch_vccnz FQ31R_%=\n\t"
      "FQ31B_%=:\n\t"
      "v_mfma_f32_16x16x32_bf16 v[196:199], v[136:139], v[44:47], v[196:199]\n\t"
      "global_load_dwordx4 v[136:139], v226, s[62:63] sc1\n\t"
      "v_mfma_f32_16x16x32_bf16 v[184:187], v[140:143], v[48:51], v[184:187]\n\t"
      "s_waitcnt vmcnt(5)\n\t"
      "v_cmp_eq_u32_e32 vcc, -1, v144\n\t"
      "v_mfma_f32_16x16x32_bf16 v[188:191], v[140:143], v[52:55], v[188:191]\n\t"
      "v_mfma_f32_16x16x32_bf16 v[192:195], v[140:143], v[56:59], v[192:195]\n\t"
      "s_cbranch_vccnz FQ32R_%=\n\t"
      "FQ32B_%=:\n\t"
      "v_mfma_f32_16x16x32_bf16 v[196:199], v[140:143], v[60:63], v[196:199]\n\t"
      "global_load_dwordx4 v[140:143], v227, s[62:63] sc1\n\t"
      "v_mfma_f32_16x16x32_bf16 v[184:187], v[144:147], v[64:67], v[184:187]\n\t"
      "s_waitcnt vmcnt(5)\n\t"
      "v_cmp_eq_u32_e32 vcc, -1, v148\n\t"
      "v_mfma_f32_16x16x32_bf16 v[188:191], v[144:147], v[68:71], v[188:191]\n\t"
      "v_mfma_f32_16x16x32_bf16 v[192:195], v[144:147], v[72:75], v[192:195]\n\t"
      "s_cbranch_vccnz FQ33R_%=\n\t"
      "FQ33B_%=:\n\t"
      "v_mfma_f32_16x16x32_bf16 v[196:199], v[144:147], v[76:79], v[196:199]\n\t"
      "global_load_dwordx4 v[144:147], v228, s[62:63] sc1\n\t"
      "s_waitcnt lgkmcnt(0)\n\t"
      "s_barrier\n\t"
      "s_setprio 2\n\t"
      "v_mfma_f32_16x16x32_bf16 v[184:187], v[148:151], v[80:83], v[184:187]\n\t"
      "s_waitcnt vmcnt(5)\n\t"
      "v_cmp_eq_u32_e32 vcc, -1, v152\n\t"
      "v_mfma_f32_16x16x32_bf16 v[188:191], v[148:151], v[84:87], v[188:191]\n\t"
      "v_mfma_f32_16x16x32_bf16 v[192:195], v[148:151], v[88:91], v[192:195]\n\t"
      "s_cbranch_vccnz FQ34R_%=\n\t"
      "FQ34B_%=:\n\t"
      "v_mfma_f32_16x16x32_bf16 v[196:199], v[148:151], v[92:95], v[196:199]\n\t"
      "global_load_dwordx4 v[148:151], v229, s[62:63] sc1\n\t"
      "v_mfma_f32_16x16x32_bf16 v[184:187], v[152:155], v[96:99], v[184:187]\n\t"
      "s_waitcnt vmcnt(5)\n\t"
      "v_cmp_eq_u32_e32 vcc, -1, v128\n\t"
      "v_mfma_f32_16x16x32_bf16 v[188:191], v[152:155], v[100:103], v[188:191]\n\t"
      "v_mfma_f32_16x16x32_bf16 v[192:195], v[152:155], v[104:107], v[192:195]\n\t"
      "s_cbranch_vccnz FQ35R_%=\n\t"
      "FQ35B_%=:\n\t"
      "v_mfma_f32_16x16x32_bf16 v[196:199], v[152:155], v[108:111], v[196:199]\n\t"
      "global_load_dwordx4 v[152:155], v230, s[62:63] sc1\n\t"
      "s_setprio 0\n\t"
      "v_cmp_u_f32_e32 vcc, v184, v185\n\t"
      "v_cmp_u_f32_e64 s[48:49], v186, v187\n\t"
      "s_nop 0\n\t"
      "s_or_b64 vcc, vcc, s[48:49]\n\t"
      "s_cbranch_scc1 NQ4R_%=\n\t"
      "NQ4B_%=:\n\t"
      "ds_write_b128 v243, v[184:187]\n\t"
      "ds_write_b128 v220, v[188:191]\n\t"
      "ds_write_b128 v221, v[192:195]\n\t"
      "ds_write_b128 v222, v[196:199]\n\t"
      "v_mfma_f32_16x16x32_bf16 v[200:203], v[128:131], v[0:3], 0\n\t"
      "s_waitcnt vmcnt(5)\n\t"
      "v_cmp_eq_u32_e32 vcc, -1, v132\n\t"
      "v_mfma_f32_16x16x32_bf16 v[204:207], v[128:131], v[4:7], 0\n\t"
      "v_mfma_f32_16x16x32_bf16 v[208:211], v[128:131], v[8:11], 0\n\t"
      "s_cbranch_vccnz FQ36R_%=\n\t"
      "FQ36B_%=:\n\t"
      "v_mfma_f32_16x16x32_bf16 v[212:215], v[128:131], v[12:15], 0\n\t"
      "global_load_dwordx4 v[128:131], v224, s[64:65] sc1\n\t"
      "v_mfma_f32_16x16x32_bf16 v[200:203], v[132:135], v[16:19], v[200:203]\n\t"
      "s_waitcnt vmcnt(5)\n\t"
      "v_cmp_eq_u32_e32 vcc, -1, v136\n\t"
      "v_mfma_f32_16x16x32_bf16 v[204:207], v[132:135], v[20:23], v[204:207]\n\t"
      "v_mfma_f32_16x16x32_bf16 v[208:211], v[132:135], v[24:27], v[208:211]\n\t"
      "s_cbranch_vccnz FQ37R_%=\n\t"
      "FQ37B_%=:\n\t"
      "v_mfma_f32_16x16x32_bf16 v[212:215], v[132:135], v[28:31], v[212:215]\n\t"
      "global_load_dwordx4 v[132:135], v225, s[64:65] sc1\n\t"
      "v_mfma_f32_16x16x32_bf16 v[200:203], v[136:139], v[32:35], v[200:203]\n\t"
      "s_waitcnt vmcnt(5)\n\t"
      "v_cmp_eq_u32_e32 vcc, -1, v140\n\t"
      "v_mfma_f32_16x16x32_bf16 v[204:207], v[136:139], v[36:39], v[204:207]\n\t"
      "v_mfma_f32_16x16x32_bf16 v[208:211], v[136:139], v[40:43], v[208:211]\n\t"
      "s_cbranch_vccnz FQ38R_%=\n\t"
      "FQ38B_%=:\n\t"
      "v_mfma_f32_16x16x32_bf16 v[212:215], v[136:139], v[44:47], v[212:215]\n\t"
      "global_load_dwordx4 v[136:139], v226, s[64:65] sc1\n\t"
      "v_mfma_f32_16x16x32_bf16 v[200:203], v[140:143], v[48:51], v[200:203]\n\t"
      "s_waitcnt vmcnt(5)\n\t"
      "v_cmp_eq_u32_e32 vcc, -1, v144\n\t"
      "v_mfma_f32_16x16x32_bf16 v[204:207], v[140:143], v[52:55], v[204:207]\n\t"
      "v_mfma_f32_16x16x32_bf16 v[208:211], v[140:143], v[56:59], v[208:211]\n\t"
      "s_cbranch_vccnz FQ39R_%=\n\t"
      "FQ39B_%=:\n\t"
      "v_mfma_f32_16x16x32_bf16 v[212:215], v[140:143], v[60:63], v[212:215]\n\t"
      "global_load_dwordx4 v[140:143], v227, s[64:65] sc1\n\t"
      "v_mfma_f32_16x16x32_bf16 v[200:203], v[144:147], v[64:67], v[200:203]\n\t"
      "s_waitcnt vmcnt(5)\n\t"
      "v_cmp_eq_u32_e32 vcc, -1, v148\n\t"
      "v_mfma_f32_16x16x32_bf16 v[204:207], v[144:147], v[68:71], v[204:207]\n\t"
      "v_mfma_f32_16x16x32_bf16 v[208:211], v[144:147], v[72:75], v[208:211]\n\t"
      "s_cbranch_vccnz FQ40R_%=\n\t"
      "FQ40B_%=:\n\t"
      "v_mfma_f32_16x16x32_bf16 v[212:215], v[144:147], v[76:79], v[212:215]\n\t"
      "global_load_dwordx4 v[144:147], v228, s[64:65] sc1\n\t"
      "s_waitcnt lgkmcnt(0)\n\t"
      "s_barrier\n\t"
      "s_setprio 2\n\t"
      "v_mfma_f32_16x16x32_bf16 v[200:203], v[148:151], v[80:83], v[200:203]\n\t"
      "s_waitcnt vmcnt(5)\n\t"
      "v_cmp_eq_u32_e32 vcc, -1, v152\n\t"
      "v_mfma_f32_16x16x32_bf16 v[204:207], v[148:151], v[84:87], v[204:207]\n\t"
      "v_mfma_f32_16x16x32_bf16 v[208:211], v[148:151], v[88:91], v[208:211]\n\t"
      "s_cbranch_vccnz FQ41R_%=\n\t"
      "FQ41B_%=:\n\t"
      "v_mfma_f32_16x16x32_bf16 v[212:215], v[148:151], v[92:95], v[212:215]\n\t"
      "global_load_dwordx4 v[148:151], v229, s[64:65] sc1\n\t"
      "v_mfma_f32_16x16x32_bf16 v[200:203], v[152:155], v[96:99], v[200:203]\n\t"
      "s_waitcnt vmcnt(5)\n\t"
      "v_cmp_eq_u32_e32 vcc, -1, v128\n\t"
      "v_mfma_f32_16x16x32_bf16 v[204:207], v[152:155], v[100:103], v[204:207]\n\t"
      "v_mfma_f32_16x16x32_bf16 v[208:211], v[152:155], v[104:107], v[208:211]\n\t"
      "s_cbranch_vccnz FQ42R_%=\n\t"
      "FQ42B_%=:\n\t"
      "v_mfma_f32_16x16x32_bf16 v[212:215], v[152:155], v[108:111], v[212:215]\n\t"
      "global_load_dwordx4 v[152:155], v230, s[64:65] sc1\n\t"
      "s_setprio 0\n\t"
      "v_cmp_u_f32_e32 vcc, v200, v201\n\t"
      "v_cmp_u_f32_e64 s[48:49], v202, v203\n\t"
      "s_nop 0\n\t"
      "s_or_b64 vcc, vcc, s[48:49]\n\t"
      "s_cbranch_scc1 NQ5R_%=\n\t"
      "NQ5B_%=:\n\t"
      "ds_write_b128 v243, v[200:203] offset:32768\n\t"
      "ds_write_b128 v220, v[204:207] offset:32768\n\t"
      "ds_write_b128 v221, v[208:211] offset:32768\n\t"
      "ds_write_b128 v222, v[212:215] offset:32768\n\t"
      "v_mfma_f32_16x16x32_bf16 v[184:187], v[128:131], v[0:3], 0\n\t"
      "s_waitcnt vmcnt(5)\n\t"
      "v_cmp_eq_u32_e32 vcc, -1, v132\n\t"
      "v_mfma_f32_16x16x32_bf16 v[188:191], v[128:131], v[4:7], 0\n\t"
      "v_mfma_f32_16x16x32_bf16 v[192:195], v[128:131], v[8:11], 0\n\t"
      "s_cbranch_vccnz FQ43R_%=\n\t"
      "FQ43B_%=:\n\t"
      "v_mfma_f32_16x16x32_bf16 v[196:199], v[128:131], v[12:15], 0\n\t"
      "v_mfma_f32_16x16x32_bf16 v[184:187], v[132:135], v[16:19], v[184:187]\n\t"
      "s_waitcnt vmcnt(4)\n\t"
      "v_cmp_eq_u32_e32 vcc, -1, v136\n\t"
      "v_mfma_f32_16x16x32_bf16 v[188:191], v[132:135], v[20:23], v[188:191]\n\t"
      "v_mfma_f32_16x16x32_bf16 v[192:195], v[132:135], v[24:27], v[192:195]\n\t"
      "s_cbranch_vccnz FQ44R_%=\n\t"
      "FQ44B_%=:\n\t"
      "v_mfma_f32_16x16x32_bf16 v[196:199], v[132:135], v[28:31], v[196:199]\n\t"
      "v_mfma_f32_16x16x32_bf16 v[184:187], v[136:139], v[32:35], v[184:187]\n\t"
      "s_waitcnt vmcnt(3)\n\t"
      "v_cmp_eq_u32_e32 vcc, -1, v140\n\t"
      "v_mfma_f32_16x16x32_bf16 v[188:191], v[136:139], v[36:39], v[188:191]\n\t"
      "v_mfma_f32_16x16x32_bf16 v[192:195], v[136:139], v[40:43], v[192:195]\n\t"
      "s_cbranch_vccnz FQ45R_%=\n\t"
      "FQ45B_%=:\n\t"
      "v_mfma_f32_16x16x32_bf16 v[196:199], v[136:139], v[44:47], v[196:199]\n\t"
      "v_mfma_f32_16x16x32_bf16 v[184:187], v[140:143], v[48:51], v[184:187]\n\t"
      "s_waitcnt vmcnt(2)\n\t"
      "v_cmp_eq_u32_e32 vcc, -1, v144\n\t"
      "v_mfma_f32_16x16x32_bf16 v[188:191], v[140:143], v[52:55], v[188:191]\n\t"
      "v_mfma_f32_16x16x32_bf16 v[192:195], v[140:143], v[56:59], v[192:195]\n\t"
      "s_cbranch_vccnz FQ46R_%=\n\t"
      "FQ46B_%=:\n\t"
      "v_mfma_f32_16x16x32_bf16 v[196:199], v[140:143], v[60:63], v[196:199]\n\t"
      "v_mfma_f32_16x16x32_bf16 v[184:187], v[144:147], v[64:67], v[184:187]\n\t"
      "s_waitcnt vmcnt(1)\n\t"
      "v_cmp_eq_u32_e32 vcc, -1, v148\n\t"
      "v_mfma_f32_16x16x32_bf16 v[188:191], v[144:147], v[68:71], v[188:191]\n\t"
      "v_mfma_f32_16x16x32_bf16 v[192:195], v[144:147], v[72:75], v[192:195]\n\t"
      "s_cbranch_vccnz FQ47R_%=\n\t"
      "FQ47B_%=:\n\t"
      "v_mfma_f32_16x16x32_bf16 v[196:199], v[144:147], v[76:79], v[196:199]\n\t"
      "s_waitcnt lgkmcnt(0)\n\t"
      "s_barrier\n\t"
      "s_setprio 2\n\t"
      "v_mfma_f32_16x16x32_bf16 v[184:187], v[148:151], v[80:83], v[184:187]\n\t"
      "s_waitcnt vmcnt(0)\n\t"
      "v_cmp_eq_u32_e32 vcc, -1, v152\n\t"
      "v_mfma_f32_16x16x32_bf16 v[188:191], v[148:151], v[84:87], v[188:191]\n\t"
      "v_mfma_f32_16x16x32_bf16 v[192:195], v[148:151], v[88:91], v[192:195]\n\t"
      "s_cbranch_vccnz FQ48R_%=\n\t"
      "FQ48B_%=:\n\t"
      "v_mfma_f32_16x16x32_bf16 v[196:199], v[148:151], v[92:95], v[196:199]\n\t"
      "v_mfma_f32_16x16x32_bf16 v[184:187], v[152:155], v[96:99], v[184:187]\n\t"
      "v_mfma_f32_16x16x32_bf16 v[188:191], v[152:155], v[100:103], v[188:191]\n\t"
      "v_mfma_f32_16x16x32_bf16 v[192:195], v[152:155], v[104:107], v[192:195]\n\t"
      "v_mfma_f32_16x16x32_bf16 v[196:199], v[152:155], v[108:111], v[196:199]\n\t"
      "s_nop 4\n\t"
      "v_cmp_u_f32_e32 vcc, v184, v185\n\t"
      "v_cmp_u_f32_e64 s[48:49], v186, v187\n\t"
      "s_nop 0\n\t"
      "s_or_b64 vcc, vcc, s[48:49]\n\t"
      "s_cbranch_scc1 NQ6R_%=\n\t"
      "NQ6B_%=:\n\t"
      "ds_write_b128 v243, v[184:187]\n\t"
      "ds_write_b128 v220, v[188:191]\n\t"
      "ds_write_b128 v221, v[192:195]\n\t"
      "ds_write_b128 v222, v[196:199]\n\t"
      "s_waitcnt lgkmcnt(0)\n\t"
      "s_barrier\n\t"
      "s_branch GX_%=\n\t"
      "HWR_%=:\n\t"
      "s_mov_b32 s51, 0\n\t"
      "HWL_%=:\n\t"
      "s_add_u32 s51, s51, 1\n\t"
      "s_cmp_lt_u32 s51, 262144\n\t"
      "s_cbranch_scc0 HWX_%=\n\t"
      "s_sleep 2\n\t"
      "global_load_dwordx4 v[112:115], v116, s[12:13] sc1\n\t"
      "s_waitcnt vmcnt(0)\n\t"
      "v_xor_b32_e64 v114, v114, v112\n\t"
      "v_xor_b32_e32 v115, v115, v113\n\t"
      "v_cmp_ne_u64_e32 vcc, s[14:15], v[114:115]\n\t"
      "s_cbranch_vccnz HWL_%=\n\t"
      "HWX_%=:\n\t"
      "s_branch HWB_%=\n\t"
      "HXR_%=:\n\t"
      "s_mov_b32 s51, 0\n\t"
      "HXL_%=:\n\t"
      "s_add_u32 s51, s51, 1\n\t"
      "s_cmp_lt_u32 s51, 262144\n\t"
      "s_cbranch_scc0 HXX_%=\n\t"
      "s_sleep 2\n\t"
      "s_mov_b32 exec_hi, s23\n\t"
      "s_not_b32 exec_lo, s23\n\t"
      "s_add_u32 s68, s20, 0\n\t"
      "s_addc_u32 s69, s21, 0\n\t"
      "global_load_dwordx4 v[184:187], v240, s[68:69] sc1\n\t"
      "s_add_u32 s68, s20, 131072\n\t"
      "s_addc_u32 s69, s21, 0\n\t"
      "global_load_dwordx4 v[188:191], v240, s[68:69] sc1\n\t"
      "s_add_u32 s68, s20, 262144\n\t"
      "s_addc_u32 s69, s21, 0\n\t"
      "global_load_dwordx4 v[192:195], v240, s[68:69] sc1\n\t"
      "s_add_u32 s68, s20, 393216\n\t"
      "s_addc_u32 s69, s21, 0\n\t"
      "global_load_dwordx4 v[196:199], v240, s[68:69] sc1\n\t"
      "s_add_u32 s68, s20, 524288\n\t"
      "s_addc_u32 s69, s21, 0\n\t"
      "global_load_dwordx4 v[200:203], v240, s[68:69] sc1\n\t"
      "s_add_u32 s68, s20, 655360\n\t"
      "s_addc_u32 s69, s21, 0\n\t"
      "global_load_dwordx4 v[204:207], v240, s[68:69] sc1\n\t"
      "s_add_u32 s68, s20, 786432\n\t"
      "s_addc_u32 s69, s21, 0\n\t"
      "global_load_dwordx4 v[208:211], v240, s[68:69] sc1\n\t"
      "s_mov_b64 exec, -1\n\t"
      "s_waitcnt vmcnt(0)\n\t"
      "v_max3_u32 v234, v184, v185, v186\n\t"
      "v_max3_u32 v234, v234, v187, v188\n\t"
      "v_max3_u32 v234, v234, v189, v190\n\t"
      "v_max3_u32 v234, v234, v191, v192\n\t"
      "v_max3_u32 v234, v234, v193, v194\n\t"
      "v_max3_u32 v234, v234, v195, v196\n\t"
      "v_max3_u32 v234, v234, v197, v198\n\t"
      "v_max3_u32 v234, v234, v199, v200\n\t"
      "v_max3_u32 v234, v234, v201, v202\n\t"
      "v_max3_u32 v234, v234, v203, v204\n\t"
      "v_max3_u32 v234, v234, v205, v206\n\t"
      "v_max3_u32 v234, v234, v207, v208\n\t"
      "v_max3_u32 v234, v234, v209, v210\n\t"
      "v_max_u32_e32 v234, v234, v211\n\t"
      "v_cmp_eq_u32_e32 vcc, -1, v234\n\t"
      "s_cbranch_vccnz HXL_%=\n\t"
      "HXX_%=:\n\t"
      "s_nop 1\n\t"
      "s_branch HXB_%=\n\t"
      "FQ1R_%=:\n\t"
      "s_mov_b32 s51, 0\n\t"
      "FQ1L_%=:\n\t"
      "s_add_u32 s51, s51, 1\n\t"
      "s_cmp_lt_u32 s51, 262144\n\t"
      "s_cbranch_scc0 FQ1X_%=\n\t"
      "s_waitcnt vmcnt(0)\n\t"
      "s_sleep 1\n\t"
      "global_load_dwordx4 v[132:135], v225, s[52:53] sc1\n\t"
      "global_load_dwordx4 v[136:139], v226, s[52:53] sc1\n\t"
      "global_load_dwordx4 v[140:143], v227, s[52:53] sc1\n\t"
      "global_load_dwordx4 v[144:147], v228, s[52:53] sc1\n\t"
      "global_load_dwordx4 v[148:151], v229, s[52:53] sc1\n\t"
      "global_load_dwordx4 v[152:155], v230, s[52:53] sc1\n\t"
      "s_waitcnt vmcnt(5)\n\t"
      "v_max3_u32 v234, v132, v133, v134\n\t"
      "v_max_u32_e32 v234, v234, v135\n\t"
      "v_cmp_eq_u32_e32 vcc, -1, v234\n\t"
      "s_cbranch_vccnz FQ1L_%=\n\t"
      "FQ1X_%=:\n\t"
      "s_branch FQ1B_%=\n\t"
      "FQ2R_%=:\n\t"
      "s_mov_b32 s51, 0\n\t"
      "FQ2L_%=:\n\t"
      "s_add_u32 s51, s51, 1\n\t"
      "s_cmp_lt_u32 s51, 262144\n\t"
      "s_cbranch_scc0 FQ2X_%=\n\t"
      "s_waitcnt vmcnt(0)\n\t"
      "s_sleep 1\n\t"
      "global_load_dwordx4 v[136:139], v226, s[52:53] sc1\n\t"
      "global_load_dwordx4 v[140:143], v227, s[52:53] sc1\n\t"
      "global_load_dwordx4 v[144:147], v228, s[52:53] sc1\n\t"
      "global_load_dwordx4 v[148:151], v229, s[52:53] sc1\n\t"
      "global_load_dwordx4 v[152:155], v230, s[52:53] sc1\n\t"
      "global_load_dwordx4 v[128:131], v224, s[54:55] sc1\n\t"
      "s_waitcnt vmcnt(5)\n\t"
      "v_max3_u32 v234, v136, v137, v138\n\t"
      "v_max_u32_e32 v234, v234, v139\n\t"
      "v_cmp_eq_u32_e32 vcc, -1, v234\n\t"
      "s_cbranch_vccnz FQ2L_%=\n\t"
      "FQ2X_%=:\n\t"
      "s_branch FQ2B_%=\n\t"
      "FQ3R_%=:\n\t"
      "s_mov_b32 s51, 0\n\t"
      "FQ3L_%=:\n\t"
      "s_add_u32 s51, s51, 1\n\t"
      "s_cmp_lt_u32 s51, 262144\n\t"
      "s_cbranch_scc0 FQ3X_%=\n\t"
      "s_waitcnt vmcnt(0)\n\t"
      "s_sleep 1\n\t"
      "global_load_dwordx4 v[140:143], v227, s[52:53] sc1\n\t"
      "global_load_dwordx4 v[144:147], v228, s[52:53] sc1\n\t"
      "global_load_dwordx4 v[148:151], v229, s[52:53] sc1\n\t"
      "global_load_dwordx4 v[152:155], v230, s[52:53] sc1\n\t"
      "global_load_dwordx4 v[128:131], v224, s[54:55] sc1\n\t"
      "global_load_dwordx4 v[132:135], v225, s[54:55] sc1\n\t"
      "s_waitcnt vmcnt(5)\n\t"
      "v_max3_u32 v234, v140, v141, v142\n\t"
      "v_max_u32_e32 v234, v234, v143\n\t"
      "v_cmp_eq_u32_e32 vcc, -1, v234\n\t"
      "s_cbranch_vccnz FQ3L_%=\n\t"
      "FQ3X_%=:\n\t"
      "s_branch FQ3B_%=\n\t"
      "FQ4R_%=:\n\t"
      "s_mov_b32 s51, 0\n\t"
      "FQ4L_%=:\n\t"
      "s_add_u32 s51, s51, 1\n\t"
      "s_cmp_lt_u32 s51, 262144\n\t"
      "s_cbranch_scc0 FQ4X_%=\n\t"
      "s_waitcnt vmcnt(0)\n\t"
      "s_sleep 1\n\t"
      "global_load_dwordx4 v[144:147], v228, s[52:53] sc1\n\t"
      "global_load_dwordx4 v[148:151], v229, s[52:53] sc1\n\t"
      "global_load_dwordx4 v[152:155], v230, s[52:53] sc1\n\t"
      "global_load_dwordx4 v[128:131], v224, s[54:55] sc1\n\t"
      "global_load_dwordx4 v[132:135], v225, s[54:55] sc1\n\t"
      "global_load_dwordx4 v[136:139], v226, s[54:55] sc1\n\t"
      "s_waitcnt vmcnt(5)\n\t"
      "v_max3_u32 v234, v144, v145, v146\n\t"
      "v_max_u32_e32 v234, v234, v147\n\t"
      "v_cmp_eq_u32_e32 vcc, -1, v234\n\t"
      "s_cbranch_vccnz FQ4L_%=\n\t"
      "FQ4X_%=:\n\t"
      "s_branch FQ4B_%=\n\t"
      "FQ5R_%=:\n\t"
      "s_mov_b32 s51, 0\n\t"
      "FQ5L_%=:\n\t"
      "s_add_u32 s51, s51, 1\n\t"
      "s_cmp_lt_u32 s51, 262144\n\t"
      "s_cbranch_scc0 FQ5X_%=\n\t"
      "s_waitcnt vmcnt(0)\n\t"
      "s_sleep 1\n\t"
      "global_load_dwordx4 v[148:151], v229, s[52:53] sc1\n\t"
      "global_load_dwordx4 v[152:155], v230, s[52:53] sc1\n\t"
      "global_load_dwordx4 v[128:131], v224, s[54:55] sc1\n\t"
      "global_load_dwordx4 v[132:135], v225, s[54:55] sc1\n\t"
      "global_load_dwordx4 v[136:139], v226, s[54:55] sc1\n\t"
      "global_load_dwordx4 v[140:143], v227, s[54:55] sc1\n\t"
      "s_waitcnt vmcnt(5)\n\t"
      "v_max3_u32 v234, v148, v149, v150\n\t"
      "v_max_u32_e32 v234, v234, v151\n\t"
      "v_cmp_eq_u32_e32 vcc, -1, v234\n\t"
      "s_cbranch_vccnz FQ5L_%=\n\t"
      "FQ5X_%=:\n\t"
      "s_branch FQ5B_%=\n\t"
      "FQ6R_%=:\n\t"
      "s_mov_b32 s51, 0\n\t"
      "FQ6L_%=:\n\t"
      "s_add_u32 s51, s51, 1\n\t"
      "s_cmp_lt_u32 s51, 262144\n\t"
      "s_cbranch_scc0 FQ6X_%=\n\t"
      "s_waitcnt vmcnt(0)\n\t"
      "s_sleep 1\n\t"
      "global_load_dwordx4 v[152:155], v230, s[52:53] sc1\n\t"
      "global_load_dwordx4 v[128:131], v224, s[54:55] sc1\n\t"
      "global_load_dwordx4 v[132:135], v225, s[54:55] sc1\n\t"
      "global_load_dwordx4 v[136:139], v226, s[54:55] sc1\n\t"
      "global_load_dwordx4 v[140:143], v227, s[54:55] sc1\n\t"
      "global_load_dwordx4 v[144:147], v228, s[54:55] sc1\n\t"
      "s_waitcnt vmcnt(5)\n\t"
      "v_max3_u32 v234, v152, v153, v154\n\t"
      "v_max_u32_e32 v234, v234, v155\n\t"
      "v_cmp_eq_u32_e32 vcc, -1, v234\n\t"
      "s_cbranch_vccnz FQ6L_%=\n\t"
      "FQ6X_%=:\n\t"
      "s_branch FQ6B_%=\n\t"
      "FQ7R_%=:\n\t"
      "s_mov_b32 s51, 0\n\t"
      "FQ7L_%=:\n\t"
      "s_add_u32 s51, s51, 1\n\t"
      "s_cmp_lt_u32 s51, 262144\n\t"
      "s_cbranch_scc0 FQ7X_%=\n\t"
      "s_waitcnt vmcnt(0)\n\t"
      "s_sleep 4\n\t"
      "global_load_dwordx4 v[128:131], v224, s[54:55] sc1\n\t"
      "global_load_dwordx4 v[132:135], v225, s[54:55] sc1\n\t"
      "global_load_dwordx4 v[136:139], v226, s[54:55] sc1\n\t"
      "global_load_dwordx4 v[140:143], v227, s[54:55] sc1\n\t"
      "global_load_dwordx4 v[144:147], v228, s[54:55] sc1\n\t"
      "global_load_dwordx4 v[148:151], v229, s[54:55] sc1\n\t"
      "s_waitcnt vmcnt(5)\n\t"
      "v_max3_u32 v234, v128, v129, v130\n\t"
      "v_max_u32_e32 v234, v234, v131\n\t"
      "v_cmp_eq_u32_e32 vcc, -1, v234\n\t"
      "s_cbranch_vccnz FQ7L_%=\n\t"
      "FQ7X_%=:\n\t"
      "s_branch FQ7B_%=\n\t"
      "NQ0R_%=:\n\t"
      "s_mov_b32 s51, 0\n\t"
      "NQ0L_%=:\n\t"
      "s_add_u32 s51, s51, 1\n\t"
      "s_cmp_lt_u32 s51, 262144\n\t"
      "s_cbranch_scc0 NQ0X_%=\n\t"
      "s_waitcnt vmcnt(0)\n\t"
      "s_sleep 1\n\t"
      "global_load_dwordx4 v[128:131], v224, s[52:53] sc1\n\t"
      "global_load_dwordx4 v[132:135], v225, s[52:53] sc1\n\t"
      "global_load_dwordx4 v[136:139], v226, s[52:53] sc1\n\t"
      "global_load_dwordx4 v[140:143], v227, s[52:53] sc1\n\t"
      "global_load_dwordx4 v[144:147], v228, s[52:53] sc1\n\t"
      "global_load_dwordx4 v[148:151], v229, s[52:53] sc1\n\t"
      "global_load_dwordx4 v[152:155], v230, s[52:53] sc1\n\t"
      "s_waitcnt vmcnt(0)\n\t"
      "s_nop 0\n\t"
      "v_max3_u32 v234, v128, v129, v130\n\t"
      "v_max3_u32 v234, v234, v131, v132\n\t"
      "v_max3_u32 v234, v234, v133, v134\n\t"
      "v_max3_u32 v234, v234, v135, v136\n\t"
      "v_max3_u32 v234, v234, v137, v138\n\t"
      "v_max3_u32 v234, v234, v139, v140\n\t"
      "v_max3_u32 v234, v234, v141, v142\n\t"
      "v_max3_u32 v234, v234, v143, v144\n\t"
      "v_max3_u32 v234, v234, v145, v146\n\t"
      "v_max3_u32 v234, v234, v147, v148\n\t"
      "v_max3_u32 v234, v234, v149, v150\n\t"
      "v_max3_u32 v234, v234, v151, v152\n\t"
      "v_max3_u32 v234, v234, v153, v154\n\t"
      "v_max_u32_e32 v234, v234, v155\n\t"
      "v_cmp_eq_u32_e32 vcc, -1, v234\n\t"
      "s_cbranch_vccnz NQ0L_%=\n\t"
      "NQ0X_%=:\n\t"
      "v_mfma_f32_16x16x32_bf16 v[184:187], v[128:131], v[0:3], 0\n\t"
      "v_mfma_f32_16x16x32_bf16 v[188:191], v[128:131], v[4:7], 0\n\t"
      "v_mfma_f32_16x16x32_bf16 v[192:195], v[128:131], v[8:11], 0\n\t"
      "v_mfma_f32_16x16x32_bf16 v[196:199], v[128:131], v[12:15], 0\n\t"
      "v_mfma_f32_16x16x32_bf16 v[184:187], v[132:135], v[16:19], v[184:187]\n\t"
      "v_mfma_f32_16x16x32_bf16 v[188:191], v[132:135], v[20:23], v[188:191]\n\t"
      "v_mfma_f32_16x16x32_bf16 v[192:195], v[132:135], v[24:27], v[192:195]\n\t"
      "v_mfma_f32_16x16x32_bf16 v[196:199], v[132:135], v[28:31], v[196:199]\n\t"
      "v_mfma_f32_16x16x32_bf16 v[184:187], v[136:139], v[32:35], v[184:187]\n\t"
      "v_mfma_f32_16x16x32_bf16 v[188:191], v[136:139], v[36:39], v[188:191]\n\t"
      "v_mfma_f32_16x16x32_bf16 v[192:195], v[136:139], v[40:43], v[192:195]\n\t"
      "v_mfma_f32_16x16x32_bf16 v[196:199], v[136:139], v[44:47], v[196:199]\n\t"
      "v_mfma_f32_16x16x32_bf16 v[184:187], v[140:143], v[48:51], v[184:187]\n\t"
      "v_mfma_f32_16x16x32_bf16 v[188:191], v[140:143], v[52:55], v[188:191]\n\t"
      "v_mfma_f32_16x16x32_bf16 v[192:195], v[140:143], v[56:59], v[192:195]\n\t"
      "v_mfma_f32_16x16x32_bf16 v[196:199], v[140:143], v[60:63], v[196:199]\n\t"
      "v_mfma_f32_16x16x32_bf16 v[184:187], v[144:147], v[64:67], v[184:187]\n\t"
      "v_mfma_f32_16x16x32_bf16 v[188:191], v[144:147], v[68:71], v[188:191]\n\t"
      "v_mfma_f32_16x16x32_bf16 v[192:195], v[144:147], v[72:75], v[192:195]\n\t"
      "v_mfma_f32_16x16x32_bf16 v[196:199], v[144:147], v[76:79], v[196:199]\n\t"
      "v_mfma_f32_16x16x32_bf16 v[184:187], v[148:151], v[80:83], v[184:187]\n\t"
      "v_mfma_f32_16x16x32_bf16 v[188:191], v[148:151], v[84:87], v[188:191]\n\t"
      "v_mfma_f32_16x16x32_bf16 v[192:195], v[148:151], v[88:91], v[192:195]\n\t"
      "v_mfma_f32_16x16x32_bf16 v[196:199], v[148:151], v[92:95], v[196:199]\n\t"
      "v_mfma_f32_16x16x32_bf16 v[184:187], v[152:155], v[96:99], v[184:187]\n\t"
      "v_mfma_f32_16x16x32_bf16 v[188:191], v[152:155], v[100:103], v[188:191]\n\t"
      "v_mfma_f32_16x16x32_bf16 v[192:195], v[152:155], v[104:107], v[192:195]\n\t"
      "v_mfma_f32_16x16x32_bf16 v[196:199], v[152:155], v[108:111], v[196:199]\n\t"
      "global_load_dwordx4 v[128:131], v224, s[54:55] sc1\n\t"
      "global_load_dwordx4 v[132:135], v225, s[54:55] sc1\n\t"
      "global_load_dwordx4 v[136:139], v226, s[54:55] sc1\n\t"
      "global_load_dwordx4 v[140:143], v227, s[54:55] sc1\n\t"
      "global_load_dwordx4 v[144:147], v228, s[54:55] sc1\n\t"
      "global_load_dwordx4 v[148:151], v229, s[54:55] sc1\n\t"
      "global_load_dwordx4 v[152:155], v230, s[54:55] sc1\n\t"
      "s_waitcnt vmcnt(0)\n\t"
      "s_nop 7\n\t"
      "s_branch NQ0B_%=\n\t"
      "FQ8R_%=:\n\t"
      "s_mov_b32 s51, 0\n\t"
      "FQ8L_%=:\n\t"
      "s_add_u32 s51, s51, 1\n\t"
      "s_cmp_lt_u32 s51, 262144\n\t"
      "s_cbranch_scc0 FQ8X_%=\n\t"
      "s_waitcnt vmcnt(0)\n\t"
      "s_sleep 4\n\t"
      "global_load_dwordx4 v[132:135], v225, s[54:55] sc1\n\t"
      "global_load_dwordx4 v[136:139], v226, s[54:55] sc1\n\t"
      "global_load_dwordx4 v[140:143], v227, s[54:55] sc1\n\t"
      "global_load_dwordx4 v[144:147], v228, s[54:55] sc1\n\t"
      "global_load_dwordx4 v[148:151], v229, s[54:55] sc1\n\t"
      "global_load_dwordx4 v[152:155], v230, s[54:55] sc1\n\t"
      "s_waitcnt vmcnt(5)\n\t"
      "v_max3_u32 v234, v132, v133, v134\n\t"
      "v_max_u32_e32 v234, v234, v135\n\t"
      "v_cmp_eq_u32_e32 vcc, -1, v234\n\t"
      "s_cbranch_vccnz FQ8L_%=\n\t"
      "FQ8X_%=:\n\t"
      "s_branch FQ8B_%=\n\t"
      "FQ9R_%=:\n\t"
      "s_mov_b32 s51, 0\n\t"
      "FQ9L_%=:\n\t"
      "s_add_u32 s51, s51, 1\n\t"
      "s_cmp_lt_u32 s51, 262144\n\t"
      "s_cbranch_scc0 FQ9X_%=\n\t"
      "s_waitcnt vmcnt(0)\n\t"
      "s_sleep 4\n\t"
      "global_load_dwordx4 v[136:139], v226, s[54:55] sc1\n\t"
      "global_load_dwordx4 v[140:143], v227, s[54:55] sc1\n\t"
      "global_load_dwordx4 v[144:147], v228, s[54:55] sc1\n\t"
      "global_load_dwordx4 v[148:151], v229, s[54:55] sc1\n\t"
      "global_load_dwordx4 v[152:155], v230, s[54:55] sc1\n\t"
      "global_load_dwordx4 v[128:131], v224, s[56:57] sc1\n\t"
      "s_waitcnt vmcnt(5)\n\t"
      "v_max3_u32 v234, v136, v137, v138\n\t"
      "v_max_u32_e32 v234, v234, v139\n\t"
      "v_cmp_eq_u32_e32 vcc, -1, v234\n\t"
      "s_cbranch_vccnz FQ9L_%=\n\t"
      "FQ9X_%=:\n\t"
      "s_branch FQ9B_%=\n\t"
      "FQ10R_%=:\n\t"
      "s_mov_b32 s51, 0\n\t"
      "FQ10L_%=:\n\t"
      "s_add_u32 s51, s51, 1\n\t"
      "s_cmp_lt_u32 s51, 262144\n\t"
      "s_cbranch_scc0 FQ10X_%=\n\t"
      "s_waitcnt vmcnt(0)\n\t"
      "s_sleep 4\n\t"
      "global_load_dwordx4 v[140:143], v227, s[54:55] sc1\n\t"
      "global_load_dwordx4 v[144:147], v228, s[54:55] sc1\n\t"
      "global_load_dwordx4 v[148:151], v229, s[54:55] sc1\n\t"
      "global_load_dwordx4 v[152:155], v230, s[54:55] sc1\n\t"
      "global_load_dwordx4 v[128:131], v224, s[56:57] sc1\n\t"
      "global_load_dwordx4 v[132:135], v225, s[56:57] sc1\n\t"
      "s_waitcnt vmcnt(5)\n\t"
      "v_max3_u32 v234, v140, v141, v142\n\t"
      "v_max_u32_e32 v234, v234, v143\n\t"
      "v_cmp_eq_u32_e32 vcc, -1, v234\n\t"
      "s_cbranch_vccnz FQ10L_%=\n\t"
      "FQ10X_%=:\n\t"
      "s_branch FQ10B_%=\n\t"
      "FQ11R_%=:\n\t"
      "s_mov_b32 s51, 0\n\t"
      "FQ11L_%=:\n\t"
      "s_add_u32 s51, s51, 1\n\t"
      "s_cmp_lt_u32 s51, 262144\n\t"
      "s_cbranch_scc0 FQ11X_%=\n\t"
      "s_waitcnt vmcnt(0)\n\t"
      "s_sleep 4\n\t"
      "global_load_dwordx4 v[144:147], v228, s[54:55] sc1\n\t"
      "global_load_dwordx4 v[148:151], v229, s[54:55] sc1\n\t"
      "global_load_dwordx4 v[152:155], v230, s[54:55] sc1\n\t"
      "global_load_dwordx4 v[128:131], v224, s[56:57] sc1\n\t"
      "global_load_dwordx4 v[132:135], v225, s[56:57] sc1\n\t"
      "global_load_dwordx4 v[136:139], v226, s[56:57] sc1\n\t"
      "s_waitcnt vmcnt(5)\n\t"
      "v_max3_u32 v234, v144, v145, v146\n\t"
      "v_max_u32_e32 v234, v234, v147\n\t"
      "v_cmp_eq_u32_e32 vcc, -1, v234\n\t"
      "s_cbranch_vccnz FQ11L_%=\n\t"
      "FQ11X_%=:\n\t"
      "s_branch FQ11B_%=\n\t"
      "FQ12R_%=:\n\t"
      "s_mov_b32 s51, 0\n\t"
      "FQ12L_%=:\n\t"
      "s_add_u32 s51, s51, 1\n\t"
      "s_cmp_lt_u32 s51, 262144\n\t"
      "s_cbranch_scc0 FQ12X_%=\n\t"
      "s_waitcnt vmcnt(0)\n\t"
      "s_sleep 4\n\t"
      "global_load_dwordx4 v[148:151], v229, s[54:55] sc1\n\t"
      "global_load_dwordx4 v[152:155], v230, s[54:55] sc1\n\t"
      "global_load_dwordx4 v[128:131], v224, s[56:57] sc1\n\t"
      "global_load_dwordx4 v[132:135], v225, s[56:57] sc1\n\t"
      "global_load_dwordx4 v[136:139], v226, s[56:57] sc1\n\t"
      "global_load_dwordx4 v[140:143], v227, s[56:57] sc1\n\t"
      "s_waitcnt vmcnt(5)\n\t"
      "v_max3_u32 v234, v148, v149, v150\n\t"
      "v_max_u32_e32 v234, v234, v151\n\t"
      "v_cmp_eq_u32_e32 vcc, -1, v234\n\t"
      "s_cbranch_vccnz FQ12L_%=\n\t"
      "FQ12X_%=:\n\t"
      "s_branch FQ12B_%=\n\t"
      "FQ13R_%=:\n\t"
      "s_mov_b32 s51, 0\n\t"
      "FQ13L_%=:\n\t"
      "s_add_u32 s51, s51, 1\n\t"
      "s_cmp_lt_u32 s51, 262144\n\t"
      "s_cbranch_scc0 FQ13X_%=\n\t"
      "s_waitcnt vmcnt(0)\n\t"
      "s_sleep 4\n\t"
      "global_load_dwordx4 v[152:155], v230, s[54:55] sc1\n\t"
      "global_load_dwordx4 v[128:131], v224, s[56:57] sc1\n\t"
      "global_load_dwordx4 v[132:135], v225, s[56:57] sc1\n\t"
      "global_load_dwordx4 v[136:139], v226, s[56:57] sc1\n\t"
      "global_load_dwordx4 v[140:143], v227, s[56:57] sc1\n\t"
      "global_load_dwordx4 v[144:147], v228, s[56:57] sc1\n\t"
      "s_waitcnt vmcnt(5)\n\t"
      "v_max3_u32 v234, v152, v153, v154\n\t"
      "v_max_u32_e32 v234, v234, v155\n\t"
      "v_cmp_eq_u32_e32 vcc, -1, v234\n\t"
      "s_cbranch_vccnz FQ13L_%=\n\t"
      "FQ13X_%=:\n\t"
      "s_branch FQ13B_%=\n\t"
      "FQ14R_%=:\n\t"
      "s_mov_b32 s51, 0\n\t"
      "FQ14L_%=:\n\t"
      "s_add_u32 s51, s51, 1\n\t"
      "s_cmp_lt_u32 s51, 262144\n\t"
      "s_cbranch_scc0 FQ14X_%=\n\t"
      "s_waitcnt vmcnt(0)\n\t"
      "s_sleep 4\n\t"
      "global_load_dwordx4 v[128:131], v224, s[56:57] sc1\n\t"
      "global_load_dwordx4 v[132:135], v225, s[56:57] sc1\n\t"
      "global_load_dwordx4 v[136:139], v226, s[56:57] sc1\n\t"
      "global_load_dwordx4 v[140:143], v227, s[56:57] sc1\n\t"
      "global_load_dwordx4 v[144:147], v228, s[56:57] sc1\n\t"
      "global_load_dwordx4 v[148:151], v229, s[56:57] sc1\n\t"
      "s_waitcnt vmcnt(5)\n\t"
      "v_max3_u32 v234, v128, v129, v130\n\t"
      "v_max_u32_e32 v234, v234, v131\n\t"
      "v_cmp_eq_u32_e32 vcc, -1, v234\n\t"
      "s_cbranch_vccnz FQ14L_%=\n\t"
      "FQ14X_%=:\n\t"
      "s_branch FQ14B_%=\n\t"
      "NQ1R_%=:\n\t"
      "s_mov_b32 s51, 0\n\t"
      "NQ1L_%=:\n\t"
      "s_add_u32 s51, s51, 1\n\t"
      "s_cmp_lt_u32 s51, 262144\n\t"
      "s_cbranch_scc0 NQ1X_%=\n\t"
      "s_waitcnt vmcnt(0)\n\t"
      "s_sleep 1\n\t"
      "global_load_dwordx4 v[128:131], v224, s[54:55] sc1\n\t"
      "global_load_dwordx4 v[132:135], v225, s[54:55] sc1\n\t"
      "global_load_dwordx4 v[136:139], v226, s[54:55] sc1\n\t"
      "global_load_dwordx4 v[140:143], v227, s[54:55] sc1\n\t"
      "global_load_dwordx4 v[144:147], v228, s[54:55] sc1\n\t"
      "global_load_dwordx4 v[148:151], v229, s[54:55] sc1\n\t"
      "global_load_dwordx4 v[152:155], v230, s[54:55] sc1\n\t"
      "s_waitcnt vmcnt(0)\n\t"
      "v_max3_u32 v234, v128, v129, v130\n\t"
      "v_max3_u32 v234, v234, v131, v132\n\t"
      "v_max3_u32 v234, v234, v133, v134\n\t"
      "v_max3_u32 v234, v234, v135, v136\n\t"
      "v_max3_u32 v234, v234, v137, v138\n\t"
      "v_max3_u32 v234, v234, v139, v140\n\t"
      "v_max3_u32 v234, v234, v141, v142\n\t"
      "v_max3_u32 v234, v234, v143, v144\n\t"
      "v_max3_u32 v234, v234, v145, v146\n\t"
      "v_max3_u32 v234, v234, v147, v148\n\t"
      "v_max3_u32 v234, v234, v149, v150\n\t"
      "v_max3_u32 v234, v234, v151, v152\n\t"
      "v_max3_u32 v234, v234, v153, v154\n\t"
      "v_max_u32_e32 v234, v234, v155\n\t"
      "v_cmp_eq_u32_e32 vcc, -1, v234\n\t"
      "s_cbranch_vccnz NQ1L_%=\n\t"
      "NQ1X_%=:\n\t"
      "v_mfma_f32_16x16x32_bf16 v[200:203], v[128:131], v[0:3], 0\n\t"
      "v_mfma_f32_16x16x32_bf16 v[204:207], v[128:131], v[4:7], 0\n\t"
      "v_mfma_f32_16x16x32_bf16 v[208:211], v[128:131], v[8:11], 0\n\t"
      "v_mfma_f32_16x16x32_bf16 v[212:215], v[128:131], v[12:15], 0\n\t"
      "v_mfma_f32_16x16x32_bf16 v[200:203], v[132:135], v[16:19], v[200:203]\n\t"
      "v_mfma_f32_16x16x32_bf16 v[204:207], v[132:135], v[20:23], v[204:207]\n\t"
      "v_mfma_f32_16x16x32_bf16 v[208:211], v[132:135], v[24:27], v[208:211]\n\t"
      "v_mfma_f32_16x16x32_bf16 v[212:215], v[132:135], v[28:31], v[212:215]\n\t"
      "v_mfma_f32_16x16x32_bf16 v[200:203], v[136:139], v[32:35], v[200:203]\n\t"
      "v_mfma_f32_16x16x32_bf16 v[204:207], v[136:139], v[36:39], v[204:207]\n\t"
      "v_mfma_f32_16x16x32_bf16 v[208:211], v[136:139], v[40:43], v[208:211]\n\t"
      "v_mfma_f32_16x16x32_bf16 v[212:215], v[136:139], v[44:47], v[212:215]\n\t"
      "v_mfma_f32_16x16x32_bf16 v[200:203], v[140:143], v[48:51], v[200:203]\n\t"
      "v_mfma_f32_16x16x32_bf16 v[204:207], v[140:143], v[52:55], v[204:207]\n\t"
      "v_mfma_f32_16x16x32_bf16 v[208:211], v[140:143], v[56:59], v[208:211]\n\t"
      "v_mfma_f32_16x16x32_bf16 v[212:215], v[140:143], v[60:63], v[212:215]\n\t"
      "v_mfma_f32_16x16x32_bf16 v[200:203], v[144:147], v[64:67], v[200:203]\n\t"
      "v_mfma_f32_16x16x32_bf16 v[204:207], v[144:147], v[68:71], v[204:207]\n\t"
      "v_mfma_f32_16x16x32_bf16 v[208:211], v[144:147], v[72:75], v[208:211]\n\t"
      "v_mfma_f32_16x16x32_bf16 v[212:215], v[144:147], v[76:79], v[212:215]\n\t"
      "v_mfma_f32_16x16x32_bf16 v[200:203], v[148:151], v[80:83], v[200:203]\n\t"
      "v_mfma_f32_16x16x32_bf16 v[204:207], v[148:151], v[84:87], v[204:207]\n\t"
      "v_mfma_f32_16x16x32_bf16 v[208:211], v[148:151], v[88:91], v[208:211]\n\t"
      "v_mfma_f32_16x16x32_bf16 v[212:215], v[148:151], v[92:95], v[212:215]\n\t"
      "v_mfma_f32_16x16x32_bf16 v[200:203], v[152:155], v[96:99], v[200:203]\n\t"
      "v_mfma_f32_16x16x32_bf16 v[204:207], v[152:155], v[100:103], v[204:207]\n\t"
      "v_mfma_f32_16x16x32_bf16 v[208:211], v[152:155], v[104:107], v[208:211]\n\t"
      "v_mfma_f32_16x16x32_bf16 v[212:215], v[152:155], v[108:111], v[212:215]\n\t"
      "global_load_dwordx4 v[128:131], v224, s[56:57] sc1\n\t"
      "global_load_dwordx4 v[132:135], v225, s[56:57] sc1\n\t"
      "global_load_dwordx4 v[136:139], v226, s[56:57] sc1\n\t"
      "global_load_dwordx4 v[140:143], v227, s[56:57] sc1\n\t"
      "global_load_dwordx4 v[144:147], v228, s[56:57] sc1\n\t"
      "global_load_dwordx4 v[148:151], v229, s[56:57] sc1\n\t"
      "global_load_dwordx4 v[152:155], v230, s[56:57] sc1\n\t"
      "s_waitcnt vmcnt(0)\n\t"
      "s_nop 7\n\t"
      "s_branch NQ1B_%=\n\t"
      "FQ15R_%=:\n\t"
      "s_mov_b32 s51, 0\n\t"
      "FQ15L_%=:\n\t"
      "s_add_u32 s51, s51, 1\n\t"
      "s_cmp_lt_u32 s51, 262144\n\t"
      "s_cbranch_scc0 FQ15X_%=\n\t"
      "s_waitcnt vmcnt(0)\n\t"
      "s_sleep 4\n\t"
      "global_load_dwordx4 v[132:135], v225, s[56:57] sc1\n\t"
      "global_load_dwordx4 v[136:139], v226, s[56:57] sc1\n\t"
      "global_load_dwordx4 v[140:143], v227, s[56:57] sc1\n\t"
      "global_load_dwordx4 v[144:147], v228, s[56:57] sc1\n\t"
      "global_load_dwordx4 v[148:151], v229, s[56:57] sc1\n\t"
      "global_load_dwordx4 v[152:155], v230, s[56:57] sc1\n\t"
      "s_waitcnt vmcnt(5)\n\t"
      "v_max3_u32 v234, v132, v133, v134\n\t"
      "v_max_u32_e32 v234, v234, v135\n\t"
      "v_cmp_eq_u32_e32 vcc, -1, v234\n\t"
      "s_cbranch_vccnz FQ15L_%=\n\t"
      "FQ15X_%=:\n\t"
      "s_branch FQ15B_%=\n\t"
      "FQ16R_%=:\n\t"
      "s_mov_b32 s51, 0\n\t"
      "FQ16L_%=:\n\t"
      "s_add_u32 s51, s51, 1\n\t"
      "s_cmp_lt_u32 s51, 262144\n\t"
      "s_cbranch_scc0 FQ16X_%=\n\t"
      "s_waitcnt vmcnt(0)\n\t"
      "s_sleep 4\n\t"
      "global_load_dwordx4 v[136:139], v226, s[56:57] sc1\n\t"
      "global_load_dwordx4 v[140:143], v227, s[56:57] sc1\n\t"
      "global_load_dwordx4 v[144:147], v228, s[56:57] sc1\n\t"
      "global_load_dwordx4 v[148:151], v229, s[56:57] sc1\n\t"
      "global_load_dwordx4 v[152:155], v230, s[56:57] sc1\n\t"
      "global_load_dwordx4 v[128:131], v224, s[58:59] sc1\n\t"
      "s_waitcnt vmcnt(5)\n\t"
      "v_max3_u32 v234, v136, v137, v138\n\t"
      "v_max_u32_e32 v234, v234, v139\n\t"
      "v_cmp_eq_u32_e32 vcc, -1, v234\n\t"
      "s_cbranch_vccnz FQ16L_%=\n\t"
      "FQ16X_%=:\n\t"
      "s_branch FQ16B_%=\n\t"
      "FQ17R_%=:\n\t"
      "s_mov_b32 s51, 0\n\t"
      "FQ17L_%=:\n\t"
      "s_add_u32 s51, s51, 1\n\t"
      "s_cmp_lt_u32 s51, 262144\n\t"
      "s_cbranch_scc0 FQ17X_%=\n\t"
      "s_waitcnt vmcnt(0)\n\t"
      "s_sleep 4\n\t"
      "global_load_dwordx4 v[140:143], v227, s[56:57] sc1\n\t"
      "global_load_dwordx4 v[144:147], v228, s[56:57] sc1\n\t"
      "global_load_dwordx4 v[148:151], v229, s[56:57] sc1\n\t"
      "global_load_dwordx4 v[152:155], v230, s[56:57] sc1\n\t"
      "global_load_dwordx4 v[128:131], v224, s[58:59] sc1\n\t"
      "global_load_dwordx4 v[132:135], v225, s[58:59] sc1\n\t"
      "s_waitcnt vmcnt(5)\n\t"
      "v_max3_u32 v234, v140, v141, v142\n\t"
      "v_max_u32_e32 v234, v234, v143\n\t"
      "v_cmp_eq_u32_e32 vcc, -1, v234\n\t"
      "s_cbranch_vccnz FQ17L_%=\n\t"
      "FQ17X_%=:\n\t"
      "s_branch FQ17B_%=\n\t"
      "FQ18R_%=:\n\t"
      "s_mov_b32 s51, 0\n\t"
      "FQ18L_%=:\n\t"
      "s_add_u32 s51, s51, 1\n\t"
      "s_cmp_lt_u32 s51, 262144\n\t"
      "s_cbranch_scc0 FQ18X_%=\n\t"
      "s_waitcnt vmcnt(0)\n\t"
      "s_sleep 4\n\t"
      "global_load_dwordx4 v[144:147], v228, s[56:57] sc1\n\t"
      "global_load_dwordx4 v[148:151], v229, s[56:57] sc1\n\t"
      "global_load_dwordx4 v[152:155], v230, s[56:57] sc1\n\t"
      "global_load_dwordx4 v[128:131], v224, s[58:59] sc1\n\t"
      "global_load_dwordx4 v[132:135], v225, s[58:59] sc1\n\t"
      "global_load_dwordx4 v[136:139], v226, s[58:59] sc1\n\t"
      "s_waitcnt vmcnt(5)\n\t"
      "v_max3_u32 v234, v144, v145, v146\n\t"
      "v_max_u32_e32 v234, v234, v147\n\t"
      "v_cmp_eq_u32_e32 vcc, -1, v234\n\t"
      "s_cbranch_vccnz FQ18L_%=\n\t"
      "FQ18X_%=:\n\t"
      "s_branch FQ18B_%=\n\t"
      "FQ19R_%=:\n\t"
      "s_mov_b32 s51, 0\n\t"
      "FQ19L_%=:\n\t"
      "s_add_u32 s51, s51, 1\n\t"
      "s_cmp_lt_u32 s51, 262144\n\t"
      "s_cbranch_scc0 FQ19X_%=\n\t"
      "s_waitcnt vmcnt(0)\n\t"
      "s_sleep 4\n\t"
      "global_load_dwordx4 v[148:151], v229, s[56:57] sc1\n\t"
      "global_load_dwordx4 v[152:155], v230, s[56:57] sc1\n\t"
      "global_load_dwordx4 v[128:131], v224, s[58:59] sc1\n\t"
      "global_load_dwordx4 v[132:135], v225, s[58:59] sc1\n\t"
      "global_load_dwordx4 v[136:139], v226, s[58:59] sc1\n\t"
      "global_load_dwordx4 v[140:143], v227, s[58:59] sc1\n\t"
      "s_waitcnt vmcnt(5)\n\t"
      "v_max3_u32 v234, v148, v149, v150\n\t"
      "v_max_u32_e32 v234, v234, v151\n\t"
      "v_cmp_eq_u32_e32 vcc, -1, v234\n\t"
      "s_cbranch_vccnz FQ19L_%=\n\t"
      "FQ19X_%=:\n\t"
      "s_branch FQ19B_%=\n\t"
      "FQ20R_%=:\n\t"
      "s_mov_b32 s51, 0\n\t"
      "FQ20L_%=:\n\t"
      "s_add_u32 s51, s51, 1\n\t"
      "s_cmp_lt_u32 s51, 262144\n\t"
      "s_cbranch_scc0 FQ20X_%=\n\t"
      "s_waitcnt vmcnt(0)\n\t"
      "s_sleep 4\n\t"
      "global_load_dwordx4 v[152:155], v230, s[56:57] sc1\n\t"
      "global_load_dwordx4 v[128:131], v224, s[58:59] sc1\n\t"
      "global_load_dwordx4 v[132:135], v225, s[58:59] sc1\n\t"
      "global_load_dwordx4 v[136:139], v226, s[58:59] sc1\n\t"
      "global_load_dwordx4 v[140:143], v227, s[58:59] sc1\n\t"
      "global_load_dwordx4 v[144:147], v228, s[58:59] sc1\n\t"
      "s_waitcnt vmcnt(5)\n\t"
      "v_max3_u32 v234, v152, v153, v154\n\t"
      "v_max_u32_e32 v234, v234, v155\n\t"
      "v_cmp_eq_u32_e32 vcc, -1, v234\n\t"
      "s_cbranch_vccnz FQ20L_%=\n\t"
      "FQ20X_%=:\n\t"
      "s_branch FQ20B_%=\n\t"
      "FQ21R_%=:\n\t"
      "s_mov_b32 s51, 0\n\t"
      "FQ21L_%=:\n\t"
      "s_add_u32 s51, s51, 1\n\t"
      "s_cmp_lt_u32 s51, 262144\n\t"
      "s_cbranch_scc0 FQ21X_%=\n\t"
      "s_waitcnt vmcnt(0)\n\t"
      "s_sleep 4\n\t"
      "global_load_dwordx4 v[128:131], v224, s[58:59] sc1\n\t"
      "global_load_dwordx4 v[132:135], v225, s[58:59] sc1\n\t"
      "global_load_dwordx4 v[136:139], v226, s[58:59] sc1\n\t"
      "global_load_dwordx4 v[140:143], v227, s[58:59] sc1\n\t"
      "global_load_dwordx4 v[144:147], v228, s[58:59] sc1\n\t"
      "global_load_dwordx4 v[148:151], v229, s[58:59] sc1\n\t"
      "s_waitcnt vmcnt(5)\n\t"
      "v_max3_u32 v234, v128, v129, v130\n\t"
      "v_max_u32_e32 v234, v234, v131\n\t"
      "v_cmp_eq_u32_e32 vcc, -1, v234\n\t"
      "s_cbranch_vccnz FQ21L_%=\n\t"
      "FQ21X_%=:\n\t"
      "s_branch FQ21B_%=\n\t"
      "NQ2R_%=:\n\t"
      "s_mov_b32 s51, 0\n\t"
      "NQ2L_%=:\n\t"
      "s_add_u32 s51, s51, 1\n\t"
      "s_cmp_lt_u32 s51, 262144\n\t"
      "s_cbranch_scc0 NQ2X_%=\n\t"
      "s_waitcnt vmcnt(0)\n\t"
      "s_sleep 1\n\t"
      "global_load_dwordx4 v[128:131], v224, s[56:57] sc1\n\t"
      "global_load_dwordx4 v[132:135], v225, s[56:57] sc1\n\t"
      "global_load_dwordx4 v[136:139], v226, s[56:57] sc1\n\t"
      "global_load_dwordx4 v[140:143], v227, s[56:57] sc1\n\t"
      "global_load_dwordx4 v[144:147], v228, s[56:57] sc1\n\t"
      "global_load_dwordx4 v[148:151], v229, s[56:57] sc1\n\t"
      "global_load_dwordx4 v[152:155], v230, s[56:57] sc1\n\t"
      "s_waitcnt vmcnt(0)\n\t"
      "v_max3_u32 v234, v128, v129, v130\n\t"
      "v_max3_u32 v234, v234, v131, v132\n\t"
      "v_max3_u32 v234, v234, v133, v134\n\t"
      "v_max3_u32 v234, v234, v135, v136\n\t"
      "v_max3_u32 v234, v234, v137, v138\n\t"
      "v_max3_u32 v234, v234, v139, v140\n\t"
      "v_max3_u32 v234, v234, v141, v142\n\t"
      "v_max3_u32 v234, v234, v143, v144\n\t"
      "v_max3_u32 v234, v234, v145, v146\n\t"
      "v_max3_u32 v234, v234, v147, v148\n\t"
      "v_max3_u32 v234, v234, v149, v150\n\t"
      "v_max3_u32 v234, v234, v151, v152\n\t"
      "v_max3_u32 v234, v234, v153, v154\n\t"
      "v_max_u32_e32 v234, v234, v155\n\t"
      "v_cmp_eq_u32_e32 vcc, -1, v234\n\t"
      "s_cbranch_vccnz NQ2L_%=\n\t"
      "NQ2X_%=:\n\t"
      "v_mfma_f32_16x16x32_bf16 v[184:187], v[128:131], v[0:3], 0\n\t"
      "v_mfma_f32_16x16x32_bf16 v[188:191], v[128:131], v[4:7], 0\n\t"
      "v_mfma_f32_16x16x32_bf16 v[192:195], v[128:131], v[8:11], 0\n\t"
      "v_mfma_f32_16x16x32_bf16 v[196:199], v[128:131], v[12:15], 0\n\t"
      "v_mfma_f32_16x16x32_bf16 v[184:187], v[132:135], v[16:19], v[184:187]\n\t"
      "v_mfma_f32_16x16x32_bf16 v[188:191], v[132:135], v[20:23], v[188:191]\n\t"
      "v_mfma_f32_16x16x32_bf16 v[192:195], v[132:135], v[24:27], v[192:195]\n\t"
      "v_mfma_f32_16x16x32_bf16 v[196:199], v[132:135], v[28:31], v[196:199]\n\t"
      "v_mfma_f32_16x16x32_bf16 v[184:187], v[136:139], v[32:35], v[184:187]\n\t"
      "v_mfma_f32_16x16x32_bf16 v[188:191], v[136:139], v[36:39], v[188:191]\n\t"
      "v_mfma_f32_16x16x32_bf16 v[192:195], v[136:139], v[40:43], v[192:195]\n\t"
      "v_mfma_f32_16x16x32_bf16 v[196:199], v[136:139], v[44:47], v[196:199]\n\t"
      "v_mfma_f32_16x16x32_bf16 v[184:187], v[140:143], v[48:51], v[184:187]\n\t"
      "v_mfma_f32_16x16x32_bf16 v[188:191], v[140:143], v[52:55], v[188:191]\n\t"
      "v_mfma_f32_16x16x32_bf16 v[192:195], v[140:143], v[56:59], v[192:195]\n\t"
      "v_mfma_f32_16x16x32_bf16 v[196:199], v[140:143], v[60:63], v[196:199]\n\t"
      "v_mfma_f32_16x16x32_bf16 v[184:187], v[144:147], v[64:67], v[184:187]\n\t"
      "v_mfma_f32_16x16x32_bf16 v[188:191], v[144:147], v[68:71], v[188:191]\n\t"
      "v_mfma_f32_16x16x32_bf16 v[192:195], v[144:147], v[72:75], v[192:195]\n\t"
      "v_mfma_f32_16x16x32_bf16 v[196:199], v[144:147], v[76:79], v[196:199]\n\t"
      "v_mfma_f32_16x16x32_bf16 v[184:187], v[148:151], v[80:83], v[184:187]\n\t"
      "v_mfma_f32_16x16x32_bf16 v[188:191], v[148:151], v[84:87], v[188:191]\n\t"
      "v_mfma_f32_16x16x32_bf16 v[192:195], v[148:151], v[88:91], v[192:195]\n\t"
      "v_mfma_f32_16x16x32_bf16 v[196:199], v[148:151], v[92:95], v[196:199]\n\t"
      "v_mfma_f32_16x16x32_bf16 v[184:187], v[152:155], v[96:99], v[184:187]\n\t"
      "v_mfma_f32_16x16x32_bf16 v[188:191], v[152:155], v[100:103], v[188:191]\n\t"
      "v_mfma_f32_16x16x32_bf16 v[192:195], v[152:155], v[104:107], v[192:195]\n\t"
      "v_mfma_f32_16x16x32_bf16 v[196:199], v[152:155], v[108:111], v[196:199]\n\t"
      "global_load_dwordx4 v[128:131], v224, s[58:59] sc1\n\t"
      "global_load_dwordx4 v[132:135], v225, s[58:59] sc1\n\t"
      "global_load_dwordx4 v[136:139], v226, s[58:59] sc1\n\t"
      "global_load_dwordx4 v[140:143], v227, s[58:59] sc1\n\t"
      "global_load_dwordx4 v[144:147], v228, s[58:59] sc1\n\t"
      "global_load_dwordx4 v[148:151], v229, s[58:59] sc1\n\t"
      "global_load_dwordx4 v[152:155], v230, s[58:59] sc1\n\t"
      "s_waitcnt vmcnt(0)\n\t"
      "s_nop 7\n\t"
      "s_branch NQ2B_%=\n\t"
      "FQ22R_%=:\n\t"
      "s_mov_b32 s51, 0\n\t"
      "FQ22L_%=:\n\t"
      "s_add_u32 s51, s51, 1\n\t"
      "s_cmp_lt_u32 s51, 262144\n\t"
      "s_cbranch_scc0 FQ22X_%=\n\t"
      "s_waitcnt vmcnt(0)\n\t"
      "s_sleep 4\n\t"
      "global_load_dwordx4 v[132:135], v225, s[58:59] sc1\n\t"
      "global_load_dwordx4 v[136:139], v226, s[58:59] sc1\n\t"
      "global_load_dwordx4 v[140:143], v227, s[58:59] sc1\n\t"
      "global_load_dwordx4 v[144:147], v228, s[58:59] sc1\n\t"
      "global_load_dwordx4 v[148:151], v229, s[58:59] sc1\n\t"
      "global_load_dwordx4 v[152:155], v230, s[58:59] sc1\n\t"
      "s_waitcnt vmcnt(5)\n\t"
      "v_max3_u32 v234, v132, v133, v134\n\t"
      "v_max_u32_e32 v234, v234, v135\n\t"
      "v_cmp_eq_u32_e32 vcc, -1, v234\n\t"
      "s_cbranch_vccnz FQ22L_%=\n\t"
      "FQ22X_%=:\n\t"
      "s_branch FQ22B_%=\n\t"
      "FQ23R_%=:\n\t"
      "s_mov_b32 s51, 0\n\t"
      "FQ23L_%=:\n\t"
      "s_add_u32 s51, s51, 1\n\t"
      "s_cmp_lt_u32 s51, 262144\n\t"
      "s_cbranch_scc0 FQ23X_%=\n\t"
      "s_waitcnt vmcnt(0)\n\t"
      "s_sleep 4\n\t"
      "global_load_dwordx4 v[136:139], v226, s[58:59] sc1\n\t"
      "global_load_dwordx4 v[140:143], v227, s[58:59] sc1\n\t"
      "global_load_dwordx4 v[144:147], v228, s[58:59] sc1\n\t"
      "global_load_dwordx4 v[148:151], v229, s[58:59] sc1\n\t"
      "global_load_dwordx4 v[152:155], v230, s[58:59] sc1\n\t"
      "global_load_dwordx4 v[128:131], v224, s[60:61] sc1\n\t"
      "s_waitcnt vmcnt(5)\n\t"
      "v_max3_u32 v234, v136, v137, v138\n\t"
      "v_max_u32_e32 v234, v234, v139\n\t"
      "v_cmp_eq_u32_e32 vcc, -1, v234\n\t"
      "s_cbranch_vccnz FQ23L_%=\n\t"
      "FQ23X_%=:\n\t"
      "s_branch FQ23B_%=\n\t"
      "FQ24R_%=:\n\t"
      "s_mov_b32 s51, 0\n\t"
      "FQ24L_%=:\n\t"
      "s_add_u32 s51, s51, 1\n\t"
      "s_cmp_lt_u32 s51, 262144\n\t"
      "s_cbranch_scc0 FQ24X_%=\n\t"
      "s_waitcnt vmcnt(0)\n\t"
      "s_sleep 4\n\t"
      "global_load_dwordx4 v[140:143], v227, s[58:59] sc1\n\t"
      "global_load_dwordx4 v[144:147], v228, s[58:59] sc1\n\t"
      "global_load_dwordx4 v[148:151], v229, s[58:59] sc1\n\t"
      "global_load_dwordx4 v[152:155], v230, s[58:59] sc1\n\t"
      "global_load_dwordx4 v[128:131], v224, s[60:61] sc1\n\t"
      "global_load_dwordx4 v[132:135], v225, s[60:61] sc1\n\t"
      "s_waitcnt vmcnt(5)\n\t"
      "v_max3_u32 v234, v140, v141, v142\n\t"
      "v_max_u32_e32 v234, v234, v143\n\t"
      "v_cmp_eq_u32_e32 vcc, -1, v234\n\t"
      "s_cbranch_vccnz FQ24L_%=\n\t"
      "FQ24X_%=:\n\t"
      "s_branch FQ24B_%=\n\t"
      "FQ25R_%=:\n\t"
      "s_mov_b32 s51, 0\n\t"
      "FQ25L_%=:\n\t"
      "s_add_u32 s51, s51, 1\n\t"
      "s_cmp_lt_u32 s51, 262144\n\t"
      "s_cbranch_scc0 FQ25X_%=\n\t"
      "s_waitcnt vmcnt(0)\n\t"
      "s_sleep 4\n\t"
      "global_load_dwordx4 v[144:147], v228, s[58:59] sc1\n\t"
      "global_load_dwordx4 v[148:151], v229, s[58:59] sc1\n\t"
      "global_load_dwordx4 v[152:155], v230, s[58:59] sc1\n\t"
      "global_load_dwordx4 v[128:131], v224, s[60:61] sc1\n\t"
      "global_load_dwordx4 v[132:135], v225, s[60:61] sc1\n\t"
      "global_load_dwordx4 v[136:139], v226, s[60:61] sc1\n\t"
      "s_waitcnt vmcnt(5)\n\t"
      "v_max3_u32 v234, v144, v145, v146\n\t"
      "v_max_u32_e32 v234, v234, v147\n\t"
      "v_cmp_eq_u32_e32 vcc, -1, v234\n\t"
      "s_cbranch_vccnz FQ25L_%=\n\t"
      "FQ25X_%=:\n\t"
      "s_branch FQ25B_%=\n\t"
      "FQ26R_%=:\n\t"
      "s_mov_b32 s51, 0\n\t"
      "FQ26L_%=:\n\t"
      "s_add_u32 s51, s51, 1\n\t"
      "s_cmp_lt_u32 s51, 262144\n\t"
      "s_cbranch_scc0 FQ26X_%=\n\t"
      "s_waitcnt vmcnt(0)\n\t"
      "s_sleep 4\n\t"
      "global_load_dwordx4 v[148:151], v229, s[58:59] sc1\n\t"
      "global_load_dwordx4 v[152:155], v230, s[58:59] sc1\n\t"
      "global_load_dwordx4 v[128:131], v224, s[60:61] sc1\n\t"
      "global_load_dwordx4 v[132:135], v225, s[60:61] sc1\n\t"
      "global_load_dwordx4 v[136:139], v226, s[60:61] sc1\n\t"
      "global_load_dwordx4 v[140:143], v227, s[60:61] sc1\n\t"
      "s_waitcnt vmcnt(5)\n\t"
      "v_max3_u32 v234, v148, v149, v150\n\t"
      "v_max_u32_e32 v234, v234, v151\n\t"
      "v_cmp_eq_u32_e32 vcc, -1, v234\n\t"
      "s_cbranch_vccnz FQ26L_%=\n\t"
      "FQ26X_%=:\n\t"
      "s_branch FQ26B_%=\n\t"
      "FQ27R_%=:\n\t"
      "s_mov_b32 s51, 0\n\t"
      "FQ27L_%=:\n\t"
      "s_add_u32 s51, s51, 1\n\t"
      "s_cmp_lt_u32 s51, 262144\n\t"
      "s_cbranch_scc0 FQ27X_%=\n\t"
      "s_waitcnt vmcnt(0)\n\t"
      "s_sleep 4\n\t"
      "global_load_dwordx4 v[152:155], v230, s[58:59] sc1\n\t"
      "global_load_dwordx4 v[128:131], v224, s[60:61] sc1\n\t"
      "global_load_dwordx4 v[132:135], v225, s[60:61] sc1\n\t"
      "global_load_dwordx4 v[136:139], v226, s[60:61] sc1\n\t"
      "global_load_dwordx4 v[140:143], v227, s[60:61] sc1\n\t"
      "global_load_dwordx4 v[144:147], v228, s[60:61] sc1\n\t"
      "s_waitcnt vmcnt(5)\n\t"
      "v_max3_u32 v234, v152, v153, v154\n\t"
      "v_max_u32_e32 v234, v234, v155\n\t"
      "v_cmp_eq_u32_e32 vcc, -1, v234\n\t"
      "s_cbranch_vccnz FQ27L_%=\n\t"
      "FQ27X_%=:\n\t"
      "s_branch FQ27B_%=\n\t"
      "FQ28R_%=:\n\t"
      "s_mov_b32 s51, 0\n\t"
      "FQ28L_%=:\n\t"
      "s_add_u32 s51, s51, 1\n\t"
      "s_cmp_lt_u32 s51, 262144\n\t"
      "s_cbranch_scc0 FQ28X_%=\n\t"
      "s_waitcnt vmcnt(0)\n\t"
      "s_sleep 4\n\t"
      "global_load_dwordx4 v[128:131], v224, s[60:61] sc1\n\t"
      "global_load_dwordx4 v[132:135], v225, s[60:61] sc1\n\t"
      "global_load_dwordx4 v[136:139], v226, s[60:61] sc1\n\t"
      "global_load_dwordx4 v[140:143], v227, s[60:61] sc1\n\t"
      "global_load_dwordx4 v[144:147], v228, s[60:61] sc1\n\t"
      "global_load_dwordx4 v[148:151], v229, s[60:61] sc1\n\t"
      "s_waitcnt vmcnt(5)\n\t"
      "v_max3_u32 v234, v128, v129, v130\n\t"
      "v_max_u32_e32 v234, v234, v131\n\t"
      "v_cmp_eq_u32_e32 vcc, -1, v234\n\t"
      "s_cbranch_vccnz FQ28L_%=\n\t"
      "FQ28X_%=:\n\t"
      "s_branch FQ28B_%=\n\t"
      "NQ3R_%=:\n\t"
      "s_mov_b32 s51, 0\n\t"
      "NQ3L_%=:\n\t"
      "s_add_u32 s51, s51, 1\n\t"
      "s_cmp_lt_u32 s51, 262144\n\t"
      "s_cbranch_scc0 NQ3X_%=\n\t"
      "s_waitcnt vmcnt(0)\n\t"
      "s_sleep 1\n\t"
      "global_load_dwordx4 v[128:131], v224, s[58:59] sc1\n\t"
      "global_load_dwordx4 v[132:135], v225, s[58:59] sc1\n\t"
      "global_load_dwordx4 v[136:139], v226, s[58:59] sc1\n\t"
      "global_load_dwordx4 v[140:143], v227, s[58:59] sc1\n\t"
      "global_load_dwordx4 v[144:147], v228, s[58:59] sc1\n\t"
      "global_load_dwordx4 v[148:151], v229, s[58:59] sc1\n\t"
      "global_load_dwordx4 v[152:155], v230, s[58:59] sc1\n\t"
      "s_waitcnt vmcnt(0)\n\t"
      "v_max3_u32 v234, v128, v129, v130\n\t"
      "v_max3_u32 v234, v234, v131, v132\n\t"
      "v_max3_u32 v234, v234, v133, v134\n\t"
      "v_max3_u32 v234, v234, v135, v136\n\t"
      "v_max3_u32 v234, v234, v137, v138\n\t"
      "v_max3_u32 v234, v234, v139, v140\n\t"
      "v_max3_u32 v234, v234, v141, v142\n\t"
      "v_max3_u32 v234, v234, v143, v144\n\t"
      "v_max3_u32 v234, v234, v145, v146\n\t"
      "v_max3_u32 v234, v234, v147, v148\n\t"
      "v_max3_u32 v234, v234, v149, v150\n\t"
      "v_max3_u32 v234, v234, v151, v152\n\t"
      "v_max3_u32 v234, v234, v153, v154\n\t"
      "v_max_u32_e32 v234, v234, v155\n\t"
      "v_cmp_eq_u32_e32 vcc, -1, v234\n\t"
      "s_cbranch_vccnz NQ3L_%=\n\t"
      "NQ3X_%=:\n\t"
      "v_mfma_f32_16x16x32_bf16 v[200:203], v[128:131], v[0:3], 0\n\t"
      "v_mfma_f32_16x16x32_bf16 v[204:207], v[128:131], v[4:7], 0\n\t"
      "v_mfma_f32_16x16x32_bf16 v[208:211], v[128:131], v[8:11], 0\n\t"
      "v_mfma_f32_16x16x32_bf16 v[212:215], v[128:131], v[12:15], 0\n\t"
      "v_mfma_f32_16x16x32_bf16 v[200:203], v[132:135], v[16:19], v[200:203]\n\t"
      "v_mfma_f32_16x16x32_bf16 v[204:207], v[132:135], v[20:23], v[204:207]\n\t"
      "v_mfma_f32_16x16x32_bf16 v[208:211], v[132:135], v[24:27], v[208:211]\n\t"
      "v_mfma_f32_16x16x32_bf16 v[212:215], v[132:135], v[28:31], v[212:215]\n\t"
      "v_mfma_f32_16x16x32_bf16 v[200:203], v[136:139], v[32:35], v[200:203]\n\t"
      "v_mfma_f32_16x16x32_bf16 v[204:207], v[136:139], v[36:39], v[204:207]\n\t"
      "v_mfma_f32_16x16x32_bf16 v[208:211], v[136:139], v[40:43], v[208:211]\n\t"
      "v_mfma_f32_16x16x32_bf16 v[212:215], v[136:139], v[44:47], v[212:215]\n\t"
      "v_mfma_f32_16x16x32_bf16 v[200:203], v[140:143], v[48:51], v[200:203]\n\t"
      "v_mfma_f32_16x16x32_bf16 v[204:207], v[140:143], v[52:55], v[204:207]\n\t"
      "v_mfma_f32_16x16x32_bf16 v[208:211], v[140:143], v[56:59], v[208:211]\n\t"
      "v_mfma_f32_16x16x32_bf16 v[212:215], v[140:143], v[60:63], v[212:215]\n\t"
      "v_mfma_f32_16x16x32_bf16 v[200:203], v[144:147], v[64:67], v[200:203]\n\t"
      "v_mfma_f32_16x16x32_bf16 v[204:207], v[144:147], v[68:71], v[204:207]\n\t"
      "v_mfma_f32_16x16x32_bf16 v[208:211], v[144:147], v[72:75], v[208:211]\n\t"
      "v_mfma_f32_16x16x32_bf16 v[212:215], v[144:147], v[76:79], v[212:215]\n\t"
      "v_mfma_f32_16x16x32_bf16 v[200:203], v[148:151], v[80:83], v[200:203]\n\t"
      "v_mfma_f32_16x16x32_bf16 v[204:207], v[148:151], v[84:87], v[204:207]\n\t"
      "v_mfma_f32_16x16x32_bf16 v[208:211], v[148:151], v[88:91], v[208:211]\n\t"
      "v_mfma_f32_16x16x32_bf16 v[212:215], v[148:151], v[92:95], v[212:215]\n\t"
      "v_mfma_f32_16x16x32_bf16 v[200:203], v[152:155], v[96:99], v[200:203]\n\t"
      "v_mfma_f32_16x16x32_bf16 v[204:207], v[152:155], v[100:103], v[204:207]\n\t"
      "v_mfma_f32_16x16x32_bf16 v[208:211], v[152:155], v[104:107], v[208:211]\n\t"
      "v_mfma_f32_16x16x32_bf16 v[212:215], v[152:155], v[108:111], v[212:215]\n\t"
      "global_load_dwordx4 v[128:131], v224, s[60:61] sc1\n\t"
      "global_load_dwordx4 v[132:135], v225, s[60:61] sc1\n\t"
      "global_load_dwordx4 v[136:139], v226, s[60:61] sc1\n\t"
      "global_load_dwordx4 v[140:143], v227, s[60:61] sc1\n\t"
      "global_load_dwordx4 v[144:147], v228, s[60:61] sc1\n\t"
      "global_load_dwordx4 v[148:151], v229, s[60:61] sc1\n\t"
      "global_load_dwordx4 v[152:155], v230, s[60:61] sc1\n\t"
      "s_waitcnt vmcnt(0)\n\t"
      "s_nop 7\n\t"
      "s_branch NQ3B_%=\n\t"
      "FQ29R_%=:\n\t"
      "s_mov_b32 s51, 0\n\t"
      "FQ29L_%=:\n\t"
      "s_add_u32 s51, s51, 1\n\t"
      "s_cmp_lt_u32 s51, 262144\n\t"
      "s_cbranch_scc0 FQ29X_%=\n\t"
      "s_waitcnt vmcnt(0)\n\t"
      "s_sleep 4\n\t"
      "global_load_dwordx4 v[132:135], v225, s[60:61] sc1\n\t"
      "global_load_dwordx4 v[136:139], v226, s[60:61] sc1\n\t"
      "global_load_dwordx4 v[140:143], v227, s[60:61] sc1\n\t"
      "global_load_dwordx4 v[144:147], v228, s[60:61] sc1\n\t"
      "global_load_dwordx4 v[148:151], v229, s[60:61] sc1\n\t"
      "global_load_dwordx4 v[152:155], v230, s[60:61] sc1\n\t"
      "s_waitcnt vmcnt(5)\n\t"
      "v_max3_u32 v234, v132, v133, v134\n\t"
      "v_max_u32_e32 v234, v234, v135\n\t"
      "v_cmp_eq_u32_e32 vcc, -1, v234\n\t"
      "s_cbranch_vccnz FQ29L_%=\n\t"
      "FQ29X_%=:\n\t"
      "s_branch FQ29B_%=\n\t"
      "FQ30R_%=:\n\t"
      "s_mov_b32 s51, 0\n\t"
      "FQ30L_%=:\n\t"
      "s_add_u32 s51, s51, 1\n\t"
      "s_cmp_lt_u32 s51, 262144\n\t"
      "s_cbranch_scc0 FQ30X_%=\n\t"
      "s_waitcnt vmcnt(0)\n\t"
      "s_sleep 4\n\t"
      "global_load_dwordx4 v[136:139], v226, s[60:61] sc1\n\t"
      "global_load_dwordx4 v[140:143], v227, s[60:61] sc1\n\t"
      "global_load_dwordx4 v[144:147], v228, s[60:61] sc1\n\t"
      "global_load_dwordx4 v[148:151], v229, s[60:61] sc1\n\t"
      "global_load_dwordx4 v[152:155], v230, s[60:61] sc1\n\t"
      "global_load_dwordx4 v[128:131], v224, s[62:63] sc1\n\t"
      "s_waitcnt vmcnt(5)\n\t"
      "v_max3_u32 v234, v136, v137, v138\n\t"
      "v_max_u32_e32 v234, v234, v139\n\t"
      "v_cmp_eq_u32_e32 vcc, -1, v234\n\t"
      "s_cbranch_vccnz FQ30L_%=\n\t"
      "FQ30X_%=:\n\t"
      "s_branch FQ30B_%=\n\t"
      "FQ31R_%=:\n\t"
      "s_mov_b32 s51, 0\n\t"
      "FQ31L_%=:\n\t"
      "s_add_u32 s51, s51, 1\n\t"
      "s_cmp_lt_u32 s51, 262144\n\t"
      "s_cbranch_scc0 FQ31X_%=\n\t"
      "s_waitcnt vmcnt(0)\n\t"
      "s_sleep 4\n\t"
      "global_load_dwordx4 v[140:143], v227, s[60:61] sc1\n\t"
      "global_load_dwordx4 v[144:147], v228, s[60:61] sc1\n\t"
      "global_load_dwordx4 v[148:151], v229, s[60:61] sc1\n\t"
      "global_load_dwordx4 v[152:155], v230, s[60:61] sc1\n\t"
      "global_load_dwordx4 v[128:131], v224, s[62:63] sc1\n\t"
      "global_load_dwordx4 v[132:135], v225, s[62:63] sc1\n\t"
      "s_waitcnt vmcnt(5)\n\t"
      "v_max3_u32 v234, v140, v141, v142\n\t"
      "v_max_u32_e32 v234, v234, v143\n\t"
      "v_cmp_eq_u32_e32 vcc, -1, v234\n\t"
      "s_cbranch_vccnz FQ31L_%=\n\t"
      "FQ31X_%=:\n\t"
      "s_branch FQ31B_%=\n\t"
      "FQ32R_%=:\n\t"
      "s_mov_b32 s51, 0\n\t"
      "FQ32L_%=:\n\t"
      "s_add_u32 s51, s51, 1\n\t"
      "s_cmp_lt_u32 s51, 262144\n\t"
      "s_cbranch_scc0 FQ32X_%=\n\t"
      "s_waitcnt vmcnt(0)\n\t"
      "s_sleep 4\n\t"
      "global_load_dwordx4 v[144:147], v228, s[60:61] sc1\n\t"
      "global_load_dwordx4 v[148:151], v229, s[60:61] sc1\n\t"
      "global_load_dwordx4 v[152:155], v230, s[60:61] sc1\n\t"
      "global_load_dwordx4 v[128:131], v224, s[62:63] sc1\n\t"
      "global_load_dwordx4 v[132:135], v225, s[62:63] sc1\n\t"
      "global_load_dwordx4 v[136:139], v226, s[62:63] sc1\n\t"
      "s_waitcnt vmcnt(5)\n\t"
      "v_max3_u32 v234, v144, v145, v146\n\t"
      "v_max_u32_e32 v234, v234, v147\n\t"
      "v_cmp_eq_u32_e32 vcc, -1, v234\n\t"
      "s_cbranch_vccnz FQ32L_%=\n\t"
      "FQ32X_%=:\n\t"
      "s_branch FQ32B_%=\n\t"
      "FQ33R_%=:\n\t"
      "s_mov_b32 s51, 0\n\t"
      "FQ33L_%=:\n\t"
      "s_add_u32 s51, s51, 1\n\t"
      "s_cmp_lt_u32 s51, 262144\n\t"
      "s_cbranch_scc0 FQ33X_%=\n\t"
      "s_waitcnt vmcnt(0)\n\t"
      "s_sleep 4\n\t"
      "global_load_dwordx4 v[148:151], v229, s[60:61] sc1\n\t"
      "global_load_dwordx4 v[152:155], v230, s[60:61] sc1\n\t"
      "global_load_dwordx4 v[128:131], v224, s[62:63] sc1\n\t"
      "global_load_dwordx4 v[132:135], v225, s[62:63] sc1\n\t"
      "global_load_dwordx4 v[136:139], v226, s[62:63] sc1\n\t"
      "global_load_dwordx4 v[140:143], v227, s[62:63] sc1\n\t"
      "s_waitcnt vmcnt(5)\n\t"
      "v_max3_u32 v234, v148, v149, v150\n\t"
      "v_max_u32_e32 v234, v234, v151\n\t"
      "v_cmp_eq_u32_e32 vcc, -1, v234\n\t"
      "s_cbranch_vccnz FQ33L_%=\n\t"
      "FQ33X_%=:\n\t"
      "s_branch FQ33B_%=\n\t"
      "FQ34R_%=:\n\t"
      "s_mov_b32 s51, 0\n\t"
      "FQ34L_%=:\n\t"
      "s_add_u32 s51, s51, 1\n\t"
      "s_cmp_lt_u32 s51, 262144\n\t"
      "s_cbranch_scc0 FQ34X_%=\n\t"
      "s_waitcnt vmcnt(0)\n\t"
      "s_sleep 4\n\t"
      "global_load_dwordx4 v[152:155], v230, s[60:61] sc1\n\t"
      "global_load_dwordx4 v[128:131], v224, s[62:63] sc1\n\t"
      "global_load_dwordx4 v[132:135], v225, s[62:63] sc1\n\t"
      "global_load_dwordx4 v[136:139], v226, s[62:63] sc1\n\t"
      "global_load_dwordx4 v[140:143], v227, s[62:63] sc1\n\t"
      "global_load_dwordx4 v[144:147], v228, s[62:63] sc1\n\t"
      "s_waitcnt vmcnt(5)\n\t"
      "v_max3_u32 v234, v152, v153, v154\n\t"
      "v_max_u32_e32 v234, v234, v155\n\t"
      "v_cmp_eq_u32_e32 vcc, -1, v234\n\t"
      "s_cbranch_vccnz FQ34L_%=\n\t"
      "FQ34X_%=:\n\t"
      "s_branch FQ34B_%=\n\t"
      "FQ35R_%=:\n\t"
      "s_mov_b32 s51, 0\n\t"
      "FQ35L_%=:\n\t"
      "s_add_u32 s51, s51, 1\n\t"
      "s_cmp_lt_u32 s51, 262144\n\t"
      "s_cbranch_scc0 FQ35X_%=\n\t"
      "s_waitcnt vmcnt(0)\n\t"
      "s_sleep 4\n\t"
      "global_load_dwordx4 v[128:131], v224, s[62:63] sc1\n\t"
      "global_load_dwordx4 v[132:135], v225, s[62:63] sc1\n\t"
      "global_load_dwordx4 v[136:139], v226, s[62:63] sc1\n\t"
      "global_load_dwordx4 v[140:143], v227, s[62:63] sc1\n\t"
      "global_load_dwordx4 v[144:147], v228, s[62:63] sc1\n\t"
      "global_load_dwordx4 v[148:151], v229, s[62:63] sc1\n\t"
      "s_waitcnt vmcnt(5)\n\t"
      "v_max3_u32 v234, v128, v129, v130\n\t"
      "v_max_u32_e32 v234, v234, v131\n\t"
      "v_cmp_eq_u32_e32 vcc, -1, v234\n\t"
      "s_cbranch_vccnz FQ35L_%=\n\t"
      "FQ35X_%=:\n\t"
      "s_branch FQ35B_%=\n\t"
      "NQ4R_%=:\n\t"
      "s_mov_b32 s51, 0\n\t"
      "NQ4L_%=:\n\t"
      "s_add_u32 s51, s51, 1\n\t"
      "s_cmp_lt_u32 s51, 262144\n\t"
      "s_cbranch_scc0 NQ4X_%=\n\t"
      "s_waitcnt vmcnt(0)\n\t"
      "s_sleep 1\n\t"
      "global_load_dwordx4 v[128:131], v224, s[60:61] sc1\n\t"
      "global_load_dwordx4 v[132:135], v225, s[60:61] sc1\n\t"
      "global_load_dwordx4 v[136:139], v226, s[60:61] sc1\n\t"
      "global_load_dwordx4 v[140:143], v227, s[60:61] sc1\n\t"
      "global_load_dwordx4 v[144:147], v228, s[60:61] sc1\n\t"
      "global_load_dwordx4 v[148:151], v229, s[60:61] sc1\n\t"
      "global_load_dwordx4 v[152:155], v230, s[60:61] sc1\n\t"
      "s_waitcnt vmcnt(0)\n\t"
      "v_max3_u32 v234, v128, v129, v130\n\t"
      "v_max3_u32 v234, v234, v131, v132\n\t"
      "v_max3_u32 v234, v234, v133, v134\n\t"
      "v_max3_u32 v234, v234, v135, v136\n\t"
      "v_max3_u32 v234, v234, v137, v138\n\t"
      "v_max3_u32 v234, v234, v139, v140\n\t"
      "v_max3_u32 v234, v234, v141, v142\n\t"
      "v_max3_u32 v234, v234, v143, v144\n\t"
      "v_max3_u32 v234, v234, v145, v146\n\t"
      "v_max3_u32 v234, v234, v147, v148\n\t"
      "v_max3_u32 v234, v234, v149, v150\n\t"
      "v_max3_u32 v234, v234, v151, v152\n\t"
      "v_max3_u32 v234, v234, v153, v154\n\t"
      "v_max_u32_e32 v234, v234, v155\n\t"
      "v_cmp_eq_u32_e32 vcc, -1, v234\n\t"
      "s_cbranch_vccnz NQ4L_%=\n\t"
      "NQ4X_%=:\n\t"
      "v_mfma_f32_16x16x32_bf16 v[184:187], v[128:131], v[0:3], 0\n\t"
      "v_mfma_f32_16x16x32_bf16 v[188:191], v[128:131], v[4:7], 0\n\t"
      "v_mfma_f32_16x16x32_bf16 v[192:195], v[128:131], v[8:11], 0\n\t"
      "v_mfma_f32_16x16x32_bf16 v[196:199], v[128:131], v[12:15], 0\n\t"
      "v_mfma_f32_16x16x32_bf16 v[184:187], v[132:135], v[16:19], v[184:187]\n\t"
      "v_mfma_f32_16x16x32_bf16 v[188:191], v[132:135], v[20:23], v[188:191]\n\t"
      "v_mfma_f32_16x16x32_bf16 v[192:195], v[132:135], v[24:27], v[192:195]\n\t"
      "v_mfma_f32_16x16x32_bf16 v[196:199], v[132:135], v[28:31], v[196:199]\n\t"
      "v_mfma_f32_16x16x32_bf16 v[184:187], v[136:139], v[32:35], v[184:187]\n\t"
      "v_mfma_f32_16x16x32_bf16 v[188:191], v[136:139], v[36:39], v[188:191]\n\t"
      "v_mfma_f32_16x16x32_bf16 v[192:195], v[136:139], v[40:43], v[192:195]\n\t"
      "v_mfma_f32_16x16x32_bf16 v[196:199], v[136:139], v[44:47], v[196:199]\n\t"
      "v_mfma_f32_16x16x32_bf16 v[184:187], v[140:143], v[48:51], v[184:187]\n\t"
      "v_mfma_f32_16x16x32_bf16 v[188:191], v[140:143], v[52:55], v[188:191]\n\t"
      "v_mfma_f32_16x16x32_bf16 v[192:195], v[140:143], v[56:59], v[192:195]\n\t"
      "v_mfma_f32_16x16x32_bf16 v[196:199], v[140:143], v[60:63], v[196:199]\n\t"
      "v_mfma_f32_16x16x32_bf16 v[184:187], v[144:147], v[64:67], v[184:187]\n\t"
      "v_mfma_f32_16x16x32_bf16 v[188:191], v[144:147], v[68:71], v[188:191]\n\t"
      "v_mfma_f32_16x16x32_bf16 v[192:195], v[144:147], v[72:75], v[192:195]\n\t"
      "v_mfma_f32_16x16x32_bf16 v[196:199], v[144:147], v[76:79], v[196:199]\n\t"
      "v_mfma_f32_16x16x32_bf16 v[184:187], v[148:151], v[80:83], v[184:187]\n\t"
      "v_mfma_f32_16x16x32_bf16 v[188:191], v[148:151], v[84:87], v[188:191]\n\t"
      "v_mfma_f32_16x16x32_bf16 v[192:195], v[148:151], v[88:91], v[192:195]\n\t"
      "v_mfma_f32_16x16x32_bf16 v[196:199], v[148:151], v[92:95], v[196:199]\n\t"
      "v_mfma_f32_16x16x32_bf16 v[184:187], v[152:155], v[96:99], v[184:187]\n\t"
      "v_mfma_f32_16x16x32_bf16 v[188:191], v[152:155], v[100:103], v[188:191]\n\t"
      "v_mfma_f32_16x16x32_bf16 v[192:195], v[152:155], v[104:107], v[192:195]\n\t"
      "v_mfma_f32_16x16x32_bf16 v[196:199], v[152:155], v[108:111], v[196:199]\n\t"
      "global_load_dwordx4 v[128:131], v224, s[62:63] sc1\n\t"
      "global_load_dwordx4 v[132:135], v225, s[62:63] sc1\n\t"
      "global_load_dwordx4 v[136:139], v226, s[62:63] sc1\n\t"
      "global_load_dwordx4 v[140:143], v227, s[62:63] sc1\n\t"
      "global_load_dwordx4 v[144:147], v228, s[62:63] sc1\n\t"
      "global_load_dwordx4 v[148:151], v229, s[62:63] sc1\n\t"
      "global_load_dwordx4 v[152:155], v230, s[62:63] sc1\n\t"
      "s_waitcnt vmcnt(0)\n\t"
      "s_nop 7\n\t"
      "s_branch NQ4B_%=\n\t"
      "FQ36R_%=:\n\t"
      "s_mov_b32 s51, 0\n\t"
      "FQ36L_%=:\n\t"
      "s_add_u32 s51, s51, 1\n\t"
      "s_cmp_lt_u32 s51, 262144\n\t"
      "s_cbranch_scc0 FQ36X_%=\n\t"
      "s_waitcnt vmcnt(0)\n\t"
      "s_sleep 4\n\t"
      "global_load_dwordx4 v[132:135], v225, s[62:63] sc1\n\t"
      "global_load_dwordx4 v[136:139], v226, s[62:63] sc1\n\t"
      "global_load_dwordx4 v[140:143], v227, s[62:63] sc1\n\t"
      "global_load_dwordx4 v[144:147], v228, s[62:63] sc1\n\t"
      "global_load_dwordx4 v[148:151], v229, s[62:63] sc1\n\t"
      "global_load_dwordx4 v[152:155], v230, s[62:63] sc1\n\t"
      "s_waitcnt vmcnt(5)\n\t"
      "v_max3_u32 v234, v132, v133, v134\n\t"
      "v_max_u32_e32 v234, v234, v135\n\t"
      "v_cmp_eq_u32_e32 vcc, -1, v234\n\t"
      "s_cbranch_vccnz FQ36L_%=\n\t"
      "FQ36X_%=:\n\t"
      "s_branch FQ36B_%=\n\t"
      "FQ37R_%=:\n\t"
      "s_mov_b32 s51, 0\n\t"
      "FQ37L_%=:\n\t"
      "s_add_u32 s51, s51, 1\n\t"
      "s_cmp_lt_u32 s51, 262144\n\t"
      "s_cbranch_scc0 FQ37X_%=\n\t"
      "s_waitcnt vmcnt(0)\n\t"
      "s_sleep 4\n\t"
      "global_load_dwordx4 v[136:139], v226, s[62:63] sc1\n\t"
      "global_load_dwordx4 v[140:143], v227, s[62:63] sc1\n\t"
      "global_load_dwordx4 v[144:147], v228, s[62:63] sc1\n\t"
      "global_load_dwordx4 v[148:151], v229, s[62:63] sc1\n\t"
      "global_load_dwordx4 v[152:155], v230, s[62:63] sc1\n\t"
      "global_load_dwordx4 v[128:131], v224, s[64:65] sc1\n\t"
      "s_waitcnt vmcnt(5)\n\t"
      "v_max3_u32 v234, v136, v137, v138\n\t"
      "v_max_u32_e32 v234, v234, v139\n\t"
      "v_cmp_eq_u32_e32 vcc, -1, v234\n\t"
      "s_cbranch_vccnz FQ37L_%=\n\t"
      "FQ37X_%=:\n\t"
      "s_branch FQ37B_%=\n\t"
      "FQ38R_%=:\n\t"
      "s_mov_b32 s51, 0\n\t"
      "FQ38L_%=:\n\t"
      "s_add_u32 s51, s51, 1\n\t"
      "s_cmp_lt_u32 s51, 262144\n\t"
      "s_cbranch_scc0 FQ38X_%=\n\t"
      "s_waitcnt vmcnt(0)\n\t"
      "s_sleep 4\n\t"
      "global_load_dwordx4 v[140:143], v227, s[62:63] sc1\n\t"
      "global_load_dwordx4 v[144:147], v228, s[62:63] sc1\n\t"
      "global_load_dwordx4 v[148:151], v229, s[62:63] sc1\n\t"
      "global_load_dwordx4 v[152:155], v230, s[62:63] sc1\n\t"
      "global_load_dwordx4 v[128:131], v224, s[64:65] sc1\n\t"
      "global_load_dwordx4 v[132:135], v225, s[64:65] sc1\n\t"
      "s_waitcnt vmcnt(5)\n\t"
      "v_max3_u32 v234, v140, v141, v142\n\t"
      "v_max_u32_e32 v234, v234, v143\n\t"
      "v_cmp_eq_u32_e32 vcc, -1, v234\n\t"
      "s_cbranch_vccnz FQ38L_%=\n\t"
      "FQ38X_%=:\n\t"
      "s_branch FQ38B_%=\n\t"
      "FQ39R_%=:\n\t"
      "s_mov_b32 s51, 0\n\t"
      "FQ39L_%=:\n\t"
      "s_add_u32 s51, s51, 1\n\t"
      "s_cmp_lt_u32 s51, 262144\n\t"
      "s_cbranch_scc0 FQ39X_%=\n\t"
      "s_waitcnt vmcnt(0)\n\t"
      "s_sleep 4\n\t"
      "global_load_dwordx4 v[144:147], v228, s[62:63] sc1\n\t"
      "global_load_dwordx4 v[148:151], v229, s[62:63] sc1\n\t"
      "global_load_dwordx4 v[152:155], v230, s[62:63] sc1\n\t"
      "global_load_dwordx4 v[128:131], v224, s[64:65] sc1\n\t"
      "global_load_dwordx4 v[132:135], v225, s[64:65] sc1\n\t"
      "global_load_dwordx4 v[136:139], v226, s[64:65] sc1\n\t"
      "s_waitcnt vmcnt(5)\n\t"
      "v_max3_u32 v234, v144, v145, v146\n\t"
      "v_max_u32_e32 v234, v234, v147\n\t"
      "v_cmp_eq_u32_e32 vcc, -1, v234\n\t"
      "s_cbranch_vccnz FQ39L_%=\n\t"
      "FQ39X_%=:\n\t"
      "s_branch FQ39B_%=\n\t"
      "FQ40R_%=:\n\t"
      "s_mov_b32 s51, 0\n\t"
      "FQ40L_%=:\n\t"
      "s_add_u32 s51, s51, 1\n\t"
      "s_cmp_lt_u32 s51, 262144\n\t"
      "s_cbranch_scc0 FQ40X_%=\n\t"
      "s_waitcnt vmcnt(0)\n\t"
      "s_sleep 4\n\t"
      "global_load_dwordx4 v[148:151], v229, s[62:63] sc1\n\t"
      "global_load_dwordx4 v[152:155], v230, s[62:63] sc1\n\t"
      "global_load_dwordx4 v[128:131], v224, s[64:65] sc1\n\t"
      "global_load_dwordx4 v[132:135], v225, s[64:65] sc1\n\t"
      "global_load_dwordx4 v[136:139], v226, s[64:65] sc1\n\t"
      "global_load_dwordx4 v[140:143], v227, s[64:65] sc1\n\t"
      "s_waitcnt vmcnt(5)\n\t"
      "v_max3_u32 v234, v148, v149, v150\n\t"
      "v_max_u32_e32 v234, v234, v151\n\t"
      "v_cmp_eq_u32_e32 vcc, -1, v234\n\t"
      "s_cbranch_vccnz FQ40L_%=\n\t"
      "FQ40X_%=:\n\t"
      "s_branch FQ40B_%=\n\t"
      "FQ41R_%=:\n\t"
      "s_mov_b32 s51, 0\n\t"
      "FQ41L_%=:\n\t"
      "s_add_u32 s51, s51, 1\n\t"
      "s_cmp_lt_u32 s51, 262144\n\t"
      "s_cbranch_scc0 FQ41X_%=\n\t"
      "s_waitcnt vmcnt(0)\n\t"
      "s_sleep 4\n\t"
      "global_load_dwordx4 v[152:155], v230, s[62:63] sc1\n\t"
      "global_load_dwordx4 v[128:131], v224, s[64:65] sc1\n\t"
      "global_load_dwordx4 v[132:135], v225, s[64:65] sc1\n\t"
      "global_load_dwordx4 v[136:139], v226, s[64:65] sc1\n\t"
      "global_load_dwordx4 v[140:143], v227, s[64:65] sc1\n\t"
      "global_load_dwordx4 v[144:147], v228, s[64:65] sc1\n\t"
      "s_waitcnt vmcnt(5)\n\t"
      "v_max3_u32 v234, v152, v153, v154\n\t"
      "v_max_u32_e32 v234, v234, v155\n\t"
      "v_cmp_eq_u32_e32 vcc, -1, v234\n\t"
      "s_cbranch_vccnz FQ41L_%=\n\t"
      "FQ41X_%=:\n\t"
      "s_branch FQ41B_%=\n\t"
      "FQ42R_%=:\n\t"
      "s_mov_b32 s51, 0\n\t"
      "FQ42L_%=:\n\t"
      "s_add_u32 s51, s51, 1\n\t"
      "s_cmp_lt_u32 s51, 262144\n\t"
      "s_cbranch_scc0 FQ42X_%=\n\t"
      "s_sleep 1\n\t"
      "s_mov_b64 exec, 15\n\t"
      "v_mov_b64_e32 v[232:233], 0\n\t"
      "global_atomic_or_x2 v[232:233], v247, v[232:233], s[12:13] sc0 sc1\n\t"
      "s_waitcnt vmcnt(0)\n\t"
      "v_subrev_u32_e32 v232, s14, v232\n\t"
      "v_cmp_ne_u32_e64 s[48:49], s15, v233\n\t"
      "v_subrev_u32_e32 v232, 6, v232\n\t"
      "v_cmp_lt_u32_e32 vcc, 7, v232\n\t"
      "s_mov_b64 exec, -1\n\t"
      "s_or_b64 vcc, vcc, s[48:49]\n\t"
      "s_cbranch_vccnz FQ42L_%=\n\t"
      "FQ42X_%=:\n\t"
      "FQ42DR_%=:\n\t"
      "s_mov_b32 s51, 0\n\t"
      "FQ42DL_%=:\n\t"
      "s_add_u32 s51, s51, 1\n\t"
      "s_cmp_lt_u32 s51, 262144\n\t"
      "s_cbranch_scc0 FQ42DX_%=\n\t"
      "s_waitcnt vmcnt(0)\n\t"
      "s_sleep 1\n\t"
      "global_load_dwordx4 v[128:131], v224, s[64:65] sc1\n\t"
      "global_load_dwordx4 v[132:135], v225, s[64:65] sc1\n\t"
      "global_load_dwordx4 v[136:139], v226, s[64:65] sc1\n\t"
      "global_load_dwordx4 v[140:143], v227, s[64:65] sc1\n\t"
      "global_load_dwordx4 v[144:147], v228, s[64:65] sc1\n\t"
      "global_load_dwordx4 v[148:151], v229, s[64:65] sc1\n\t"
      "s_waitcnt vmcnt(5)\n\t"
      "v_max3_u32 v234, v128, v129, v130\n\t"
      "v_max_u32_e32 v234, v234, v131\n\t"
      "v_cmp_eq_u32_e32 vcc, -1, v234\n\t"
      "s_cbranch_vccnz FQ42DL_%=\n\t"
      "FQ42DX_%=:\n\t"
      "s_branch FQ42B_%=\n\t"
      "NQ5R_%=:\n\t"
      "s_mov_b32 s51, 0\n\t"
      "NQ5L_%=:\n\t"
      "s_add_u32 s51, s51, 1\n\t"
      "s_cmp_lt_u32 s51, 262144\n\t"
      "s_cbranch_scc0 NQ5X_%=\n\t"
      "s_waitcnt vmcnt(0)\n\t"
      "s_sleep 1\n\t"
      "global_load_dwordx4 v[128:131], v224, s[62:63] sc1\n\t"
      "global_load_dwordx4 v[132:135], v225, s[62:63] sc1\n\t"
      "global_load_dwordx4 v[136:139], v226, s[62:63] sc1\n\t"
      "global_load_dwordx4 v[140:143], v227, s[62:63] sc1\n\t"
      "global_load_dwordx4 v[144:147], v228, s[62:63] sc1\n\t"
      "global_load_dwordx4 v[148:151], v229, s[62:63] sc1\n\t"
      "global_load_dwordx4 v[152:155], v230, s[62:63] sc1\n\t"
      "s_waitcnt vmcnt(0)\n\t"
      "s_nop 0\n\t"
      "v_max3_u32 v234, v128, v129, v130\n\t"
      "v_max3_u32 v234, v234, v131, v132\n\t"
      "v_max3_u32 v234, v234, v133, v134\n\t"
      "v_max3_u32 v234, v234, v135, v136\n\t"
      "v_max3_u32 v234, v234, v137, v138\n\t"
      "v_max3_u32 v234, v234, v139, v140\n\t"
      "v_max3_u32 v234, v234, v141, v142\n\t"
      "v_max3_u32 v234, v234, v143, v144\n\t"
      "v_max3_u32 v234, v234, v145, v146\n\t"
      "v_max3_u32 v234, v234, v147, v148\n\t"
      "v_max3_u32 v234, v234, v149, v150\n\t"
      "v_max3_u32 v234, v234, v151, v152\n\t"
      "v_max3_u32 v234, v234, v153, v154\n\t"
      "v_max_u32_e32 v234, v234, v155\n\t"
      "v_cmp_eq_u32_e32 vcc, -1, v234\n\t"
      "s_cbranch_vccnz NQ5L_%=\n\t"
      "NQ5X_%=:\n\t"
      "v_mfma_f32_16x16x32_bf16 v[200:203], v[128:131], v[0:3], 0\n\t"
      "v_mfma_f32_16x16x32_bf16 v[204:207], v[128:131], v[4:7], 0\n\t"
      "v_mfma_f32_16x16x32_bf16 v[208:211], v[128:131], v[8:11], 0\n\t"
      "v_mfma_f32_16x16x32_bf16 v[212:215], v[128:131], v[12:15], 0\n\t"
      "v_mfma_f32_16x16x32_bf16 v[200:203], v[132:135], v[16:19], v[200:203]\n\t"
      "v_mfma_f32_16x16x32_bf16 v[204:207], v[132:135], v[20:23], v[204:207]\n\t"
      "v_mfma_f32_16x16x32_bf16 v[208:211], v[132:135], v[24:27], v[208:211]\n\t"
      "v_mfma_f32_16x16x32_bf16 v[212:215], v[132:135], v[28:31], v[212:215]\n\t"
      "v_mfma_f32_16x16x32_bf16 v[200:203], v[136:139], v[32:35], v[200:203]\n\t"
      "v_mfma_f32_16x16x32_bf16 v[204:207], v[136:139], v[36:39], v[204:207]\n\t"
      "v_mfma_f32_16x16x32_bf16 v[208:211], v[136:139], v[40:43], v[208:211]\n\t"
      "v_mfma_f32_16x16x32_bf16 v[212:215], v[136:139], v[44:47], v[212:215]\n\t"
      "v_mfma_f32_16x16x32_bf16 v[200:203], v[140:143], v[48:51], v[200:203]\n\t"
      "v_mfma_f32_16x16x32_bf16 v[204:207], v[140:143], v[52:55], v[204:207]\n\t"
      "v_mfma_f32_16x16x32_bf16 v[208:211], v[140:143], v[56:59], v[208:211]\n\t"
      "v_mfma_f32_16x16x32_bf16 v[212:215], v[140:143], v[60:63], v[212:215]\n\t"
      "v_mfma_f32_16x16x32_bf16 v[200:203], v[144:147], v[64:67], v[200:203]\n\t"
      "v_mfma_f32_16x16x32_bf16 v[204:207], v[144:147], v[68:71], v[204:207]\n\t"
      "v_mfma_f32_16x16x32_bf16 v[208:211], v[144:147], v[72:75], v[208:211]\n\t"
      "v_mfma_f32_16x16x32_bf16 v[212:215], v[144:147], v[76:79], v[212:215]\n\t"
      "v_mfma_f32_16x16x32_bf16 v[200:203], v[148:151], v[80:83], v[200:203]\n\t"
      "v_mfma_f32_16x16x32_bf16 v[204:207], v[148:151], v[84:87], v[204:207]\n\t"
      "v_mfma_f32_16x16x32_bf16 v[208:211], v[148:151], v[88:91], v[208:211]\n\t"
      "v_mfma_f32_16x16x32_bf16 v[212:215], v[148:151], v[92:95], v[212:215]\n\t"
      "v_mfma_f32_16x16x32_bf16 v[200:203], v[152:155], v[96:99], v[200:203]\n\t"
      "v_mfma_f32_16x16x32_bf16 v[204:207], v[152:155], v[100:103], v[204:207]\n\t"
      "v_mfma_f32_16x16x32_bf16 v[208:211], v[152:155], v[104:107], v[208:211]\n\t"
      "v_mfma_f32_16x16x32_bf16 v[212:215], v[152:155], v[108:111], v[212:215]\n\t"
      "global_load_dwordx4 v[128:131], v224, s[64:65] sc1\n\t"
      "global_load_dwordx4 v[132:135], v225, s[64:65] sc1\n\t"
      "global_load_dwordx4 v[136:139], v226, s[64:65] sc1\n\t"
      "global_load_dwordx4 v[140:143], v227, s[64:65] sc1\n\t"
      "global_load_dwordx4 v[144:147], v228, s[64:65] sc1\n\t"
      "global_load_dwordx4 v[148:151], v229, s[64:65] sc1\n\t"
      "global_load_dwordx4 v[152:155], v230, s[64:65] sc1\n\t"
      "s_waitcnt vmcnt(0)\n\t"
      "s_nop 7\n\t"
      "s_branch NQ5B_%=\n\t"
      "FQ43R_%=:\n\t"
      "s_mov_b32 s51, 0\n\t"
      "FQ43L_%=:\n\t"
      "s_add_u32 s51, s51, 1\n\t"
      "s_cmp_lt_u32 s51, 262144\n\t"
      "s_cbranch_scc0 FQ43X_%=\n\t"
      "s_sleep 1\n\t"
      "s_mov_b64 exec, 15\n\t"
      "v_mov_b64_e32 v[232:233], 0\n\t"
      "global_atomic_or_x2 v[232:233], v247, v[232:233], s[12:13] sc0 sc1\n\t"
      "s_waitcnt vmcnt(0)\n\t"
      "v_subrev_u32_e32 v232, s14, v232\n\t"
      "v_cmp_ne_u32_e64 s[48:49], s15, v233\n\t"
      "v_subrev_u32_e32 v232, 6, v232\n\t"
      "v_cmp_lt_u32_e32 vcc, 7, v232\n\t"
      "s_mov_b64 exec, -1\n\t"
      "s_or_b64 vcc, vcc, s[48:49]\n\t"
      "s_cbranch_vccnz FQ43L_%=\n\t"
      "FQ43X_%=:\n\t"
      "FQ43DR_%=:\n\t"
      "s_mov_b32 s51, 0\n\t"
      "FQ43DL_%=:\n\t"
      "s_add_u32 s51, s51, 1\n\t"
      "s_cmp_lt_u32 s51, 262144\n\t"
      "s_cbranch_scc0 FQ43DX_%=\n\t"
      "s_waitcnt vmcnt(0)\n\t"
      "s_sleep 1\n\t"
      "global_load_dwordx4 v[132:135], v225, s[64:65] sc1\n\t"
      "global_load_dwordx4 v[136:139], v226, s[64:65] sc1\n\t"
      "global_load_dwordx4 v[140:143], v227, s[64:65] sc1\n\t"
      "global_load_dwordx4 v[144:147], v228, s[64:65] sc1\n\t"
      "global_load_dwordx4 v[148:151], v229, s[64:65] sc1\n\t"
      "global_load_dwordx4 v[152:155], v230, s[64:65] sc1\n\t"
      "s_waitcnt vmcnt(5)\n\t"
      "v_max3_u32 v234, v132, v133, v134\n\t"
      "v_max_u32_e32 v234, v234, v135\n\t"
      "v_cmp_eq_u32_e32 vcc, -1, v234\n\t"
      "s_cbranch_vccnz FQ43DL_%=\n\t"
      "FQ43DX_%=:\n\t"
      "s_branch FQ43B_%=\n\t"
      "FQ44R_%=:\n\t"
      "s_mov_b32 s51, 0\n\t"
      "FQ44L_%=:\n\t"
      "s_add_u32 s51, s51, 1\n\t"
      "s_cmp_lt_u32 s51, 262144\n\t"
      "s_cbranch_scc0 FQ44X_%=\n\t"
      "s_sleep 1\n\t"
      "s_mov_b64 exec, 15\n\t"
      "v_mov_b64_e32 v[232:233], 0\n\t"
      "global_atomic_or_x2 v[232:233], v247, v[232:233], s[12:13] sc0 sc1\n\t"
      "s_waitcnt vmcnt(0)\n\t"
      "v_subrev_u32_e32 v232, s14, v232\n\t"
      "v_cmp_ne_u32_e64 s[48:49], s15, v233\n\t"
      "v_subrev_u32_e32 v232, 6, v232\n\t"
      "v_cmp_lt_u32_e32 vcc, 7, v232\n\t"
      "s_mov_b64 exec, -1\n\t"
      "s_or_b64 vcc, vcc, s[48:49]\n\t"
      "s_cbranch_vccnz FQ44L_%=\n\t"
      "FQ44X_%=:\n\t"
      "FQ44DR_%=:\n\t"
      "s_mov_b32 s51, 0\n\t"
      "FQ44DL_%=:\n\t"
      "s_add_u32 s51, s51, 1\n\t"
      "s_cmp_lt_u32 s51, 262144\n\t"
      "s_cbranch_scc0 FQ44DX_%=\n\t"
      "s_waitcnt vmcnt(0)\n\t"
      "s_sleep 1\n\t"
      "global_load_dwordx4 v[136:139], v226, s[64:65] sc1\n\t"
      "global_load_dwordx4 v[140:143], v227, s[64:65] sc1\n\t"
      "global_load_dwordx4 v[144:147], v228, s[64:65] sc1\n\t"
      "global_load_dwordx4 v[148:151], v229, s[64:65] sc1\n\t"
      "global_load_dwordx4 v[152:155], v230, s[64:65] sc1\n\t"
      "s_waitcnt vmcnt(4)\n\t"
      "v_max3_u32 v234, v136, v137, v138\n\t"
      "v_max_u32_e32 v234, v234, v139\n\t"
      "v_cmp_eq_u32_e32 vcc, -1, v234\n\t"
      "s_cbranch_vccnz FQ44DL_%=\n\t"
      "FQ44DX_%=:\n\t"
      "s_branch FQ44B_%=\n\t"
      "FQ45R_%=:\n\t"
      "s_mov_b32 s51, 0\n\t"
      "FQ45L_%=:\n\t"
      "s_add_u32 s51, s51, 1\n\t"
      "s_cmp_lt_u32 s51, 262144\n\t"
      "s_cbranch_scc0 FQ45X_%=\n\t"
      "s_sleep 1\n\t"
      "s_mov_b64 exec, 15\n\t"
      "v_mov_b64_e32 v[232:233], 0\n\t"
      "global_atomic_or_x2 v[232:233], v247, v[232:233], s[12:13] sc0 sc1\n\t"
      "s_waitcnt vmcnt(0)\n\t"
      "v_subrev_u32_e32 v232, s14, v232\n\t"
      "v_cmp_ne_u32_e64 s[48:49], s15, v233\n\t"
      "v_subrev_u32_e32 v232, 6, v232\n\t"
      "v_cmp_lt_u32_e32 vcc, 7, v232\n\t"
      "s_mov_b64 exec, -1\n\t"
      "s_or_b64 vcc, vcc, s[48:49]\n\t"
      "s_cbranch_vccnz FQ45L_%=\n\t"
      "FQ45X_%=:\n\t"
      "FQ45DR_%=:\n\t"
      "s_mov_b32 s51, 0\n\t"
      "FQ45DL_%=:\n\t"
      "s_add_u32 s51, s51, 1\n\t"
      "s_cmp_lt_u32 s51, 262144\n\t"
      "s_cbranch_scc0 FQ45DX_%=\n\t"
      "s_waitcnt vmcnt(0)\n\t"
      "s_sleep 1\n\t"
      "global_load_dwordx4 v[140:143], v227, s[64:65] sc1\n\t"
      "global_load_dwordx4 v[144:147], v228, s[64:65] sc1\n\t"
      "global_load_dwordx4 v[148:151], v229, s[64:65] sc1\n\t"
      "global_load_dwordx4 v[152:155], v230, s[64:65] sc1\n\t"
      "s_waitcnt vmcnt(3)\n\t"
      "v_max3_u32 v234, v140, v141, v142\n\t"
      "v_max_u32_e32 v234, v234, v143\n\t"
      "v_cmp_eq_u32_e32 vcc, -1, v234\n\t"
      "s_cbranch_vccnz FQ45DL_%=\n\t"
      "FQ45DX_%=:\n\t"
      "s_branch FQ45B_%=\n\t"
      "FQ46R_%=:\n\t"
      "s_mov_b32 s51, 0\n\t"
      "FQ46L_%=:\n\t"
      "s_add_u32 s51, s51, 1\n\t"
      "s_cmp_lt_u32 s51, 262144\n\t"
      "s_cbranch_scc0 FQ46X_%=\n\t"
      "s_sleep 1\n\t"
      "s_mov_b64 exec, 15\n\t"
      "v_mov_b64_e32 v[232:233], 0\n\t"
      "global_atomic_or_x2 v[232:233], v247, v[232:233], s[12:13] sc0 sc1\n\t"
      "s_waitcnt vmcnt(0)\n\t"
      "v_subrev_u32_e32 v232, s14, v232\n\t"
      "v_cmp_ne_u32_e64 s[48:49], s15, v233\n\t"
      "v_subrev_u32_e32 v232, 6, v232\n\t"
      "v_cmp_lt_u32_e32 vcc, 7, v232\n\t"
      "s_mov_b64 exec, -1\n\t"
      "s_or_b64 vcc, vcc, s[48:49]\n\t"
      "s_cbranch_vccnz FQ46L_%=\n\t"
      "FQ46X_%=:\n\t"
      "FQ46DR_%=:\n\t"
      "s_mov_b32 s51, 0\n\t"
      "FQ46DL_%=:\n\t"
      "s_add_u32 s51, s51, 1\n\t"
      "s_cmp_lt_u32 s51, 262144\n\t"
      "s_cbranch_scc0 FQ46DX_%=\n\t"
      "s_waitcnt vmcnt(0)\n\t"
      "s_sleep 1\n\t"
      "global_load_dwordx4 v[144:147], v228, s[64:65] sc1\n\t"
      "global_load_dwordx4 v[148:151], v229, s[64:65] sc1\n\t"
      "global_load_dwordx4 v[152:155], v230, s[64:65] sc1\n\t"
      "s_waitcnt vmcnt(2)\n\t"
      "v_max3_u32 v234, v144, v145, v146\n\t"
      "v_max_u32_e32 v234, v234, v147\n\t"
      "v_cmp_eq_u32_e32 vcc, -1, v234\n\t"
      "s_cbranch_vccnz FQ46DL_%=\n\t"
      "FQ46DX_%=:\n\t"
      "s_branch FQ46B_%=\n\t"
      "FQ47R_%=:\n\t"
      "s_mov_b32 s51, 0\n\t"
      "FQ47L_%=:\n\t"
      "s_add_u32 s51, s51, 1\n\t"
      "s_cmp_lt_u32 s51, 262144\n\t"
      "s_cbranch_scc0 FQ47X_%=\n\t"
      "s_sleep 1\n\t"
      "s_mov_b64 exec, 15\n\t"
      "v_mov_b64_e32 v[232:233], 0\n\t"
      "global_atomic_or_x2 v[232:233], v247, v[232:233], s[12:13] sc0 sc1\n\t"
      "s_waitcnt vmcnt(0)\n\t"
      "v_subrev_u32_e32 v232, s14, v232\n\t"
      "v_cmp_ne_u32_e64 s[48:49], s15, v233\n\t"
      "v_subrev_u32_e32 v232, 6, v232\n\t"
      "v_cmp_lt_u32_e32 vcc, 7, v232\n\t"
      "s_mov_b64 exec, -1\n\t"
      "s_or_b64 vcc, vcc, s[48:49]\n\t"
      "s_cbranch_vccnz FQ47L_%=\n\t"
      "FQ47X_%=:\n\t"
      "FQ47DR_%=:\n\t"
      "s_mov_b32 s51, 0\n\t"
      "FQ47DL_%=:\n\t"
      "s_add_u32 s51, s51, 1\n\t"
      "s_cmp_lt_u32 s51, 262144\n\t"
      "s_cbranch_scc0 FQ47DX_%=\n\t"
      "s_waitcnt vmcnt(0)\n\t"
      "s_sleep 1\n\t"
      "global_load_dwordx4 v[148:151], v229, s[64:65] sc1\n\t"
      "global_load_dwordx4 v[152:155], v230, s[64:65] sc1\n\t"
      "s_waitcnt vmcnt(1)\n\t"
      "v_max3_u32 v234, v148, v149, v150\n\t"
      "v_max_u32_e32 v234, v234, v151\n\t"
      "v_cmp_eq_u32_e32 vcc, -1, v234\n\t"
      "s_cbranch_vccnz FQ47DL_%=\n\t"
      "FQ47DX_%=:\n\t"
      "s_branch FQ47B_%=\n\t"
      "FQ48R_%=:\n\t"
      "s_mov_b32 s51, 0\n\t"
      "FQ48L_%=:\n\t"
      "s_add_u32 s51, s51, 1\n\t"
      "s_cmp_lt_u32 s51, 262144\n\t"
      "s_cbranch_scc0 FQ48X_%=\n\t"
      "s_sleep 1\n\t"
      "s_mov_b64 exec, 15\n\t"
      "v_mov_b64_e32 v[232:233], 0\n\t"
      "global_atomic_or_x2 v[232:233], v247, v[232:233], s[12:13] sc0 sc1\n\t"
      "s_waitcnt vmcnt(0)\n\t"
      "v_subrev_u32_e32 v232, s14, v232\n\t"
      "v_cmp_ne_u32_e64 s[48:49], s15, v233\n\t"
      "v_subrev_u32_e32 v232, 6, v232\n\t"
      "v_cmp_lt_u32_e32 vcc, 7, v232\n\t"
      "s_mov_b64 exec, -1\n\t"
      "s_or_b64 vcc, vcc, s[48:49]\n\t"
      "s_cbranch_vccnz FQ48L_%=\n\t"
      "FQ48X_%=:\n\t"
      "FQ48DR_%=:\n\t"
      "s_mov_b32 s51, 0\n\t"
      "FQ48DL_%=:\n\t"
      "s_add_u32 s51, s51, 1\n\t"
      "s_cmp_lt_u32 s51, 262144\n\t"
      "s_cbranch_scc0 FQ48DX_%=\n\t"
      "s_waitcnt vmcnt(0)\n\t"
      "s_sleep 1\n\t"
      "global_load_dwordx4 v[152:155], v230, s[64:65] sc1\n\t"
      "s_waitcnt vmcnt(0)\n\t"
      "v_max3_u32 v234, v152, v153, v154\n\t"
      "v_max_u32_e32 v234, v234, v155\n\t"
      "v_cmp_eq_u32_e32 vcc, -1, v234\n\t"
      "s_cbranch_vccnz FQ48DL_%=\n\t"
      "FQ48DX_%=:\n\t"
      "s_branch FQ48B_%=\n\t"
      "NQ6R_%=:\n\t"
      "s_mov_b32 s51, 0\n\t"
      "NQ6L_%=:\n\t"
      "s_add_u32 s51, s51, 1\n\t"
      "s_cmp_lt_u32 s51, 262144\n\t"
      "s_cbranch_scc0 NQ6X_%=\n\t"
      "s_waitcnt vmcnt(0)\n\t"
      "s_sleep 1\n\t"
      "global_load_dwordx4 v[128:131], v224, s[64:65] sc1\n\t"
      "global_load_dwordx4 v[132:135], v225, s[64:65] sc1\n\t"
      "global_load_dwordx4 v[136:139], v226, s[64:65] sc1\n\t"
      "global_load_dwordx4 v[140:143], v227, s[64:65] sc1\n\t"
      "global_load_dwordx4 v[144:147], v228, s[64:65] sc1\n\t"
      "global_load_dwordx4 v[148:151], v229, s[64:65] sc1\n\t"
      "global_load_dwordx4 v[152:155], v230, s[64:65] sc1\n\t"
      "s_waitcnt vmcnt(0)\n\t"
      "v_max3_u32 v234, v128, v129, v130\n\t"
      "v_max3_u32 v234, v234, v131, v132\n\t"
      "v_max3_u32 v234, v234, v133, v134\n\t"
      "v_max3_u32 v234, v234, v135, v136\n\t"
      "v_max3_u32 v234, v234, v137, v138\n\t"
      "v_max3_u32 v234, v234, v139, v140\n\t"
      "v_max3_u32 v234, v234, v141, v142\n\t"
      "v_max3_u32 v234, v234, v143, v144\n\t"
      "v_max3_u32 v234, v234, v145, v146\n\t"
      "v_max3_u32 v234, v234, v147, v148\n\t"
      "v_max3_u32 v234, v234, v149, v150\n\t"
      "v_max3_u32 v234, v234, v151, v152\n\t"
      "v_max3_u32 v234, v234, v153, v154\n\t"
      "v_max_u32_e32 v234, v234, v155\n\t"
      "v_cmp_eq_u32_e32 vcc, -1, v234\n\t"
      "s_cbranch_vccnz NQ6L_%=\n\t"
      "NQ6X_%=:\n\t"
      "v_mfma_f32_16x16x32_bf16 v[184:187], v[128:131], v[0:3], 0\n\t"
      "v_mfma_f32_16x16x32_bf16 v[188:191], v[128:131], v[4:7], 0\n\t"
      "v_mfma_f32_16x16x32_bf16 v[192:195], v[128:131], v[8:11], 0\n\t"
      "v_mfma_f32_16x16x32_bf16 v[196:199], v[128:131], v[12:15], 0\n\t"
      "v_mfma_f32_16x16x32_bf16 v[184:187], v[132:135], v[16:19], v[184:187]\n\t"
      "v_mfma_f32_16x16x32_bf16 v[188:191], v[132:135], v[20:23], v[188:191]\n\t"
      "v_mfma_f32_16x16x32_bf16 v[192:195], v[132:135], v[24:27], v[192:195]\n\t"
      "v_mfma_f32_16x16x32_bf16 v[196:199], v[132:135], v[28:31], v[196:199]\n\t"
      "v_mfma_f32_16x16x32_bf16 v[184:187], v[136:139], v[32:35], v[184:187]\n\t"
      "v_mfma_f32_16x16x32_bf16 v[188:191], v[136:139], v[36:39], v[188:191]\n\t"
      "v_mfma_f32_16x16x32_bf16 v[192:195], v[136:139], v[40:43], v[192:195]\n\t"
      "v_mfma_f32_16x16x32_bf16 v[196:199], v[136:139], v[44:47], v[196:199]\n\t"
      "v_mfma_f32_16x16x32_bf16 v[184:187], v[140:143], v[48:51], v[184:187]\n\t"
      "v_mfma_f32_16x16x32_bf16 v[188:191], v[140:143], v[52:55], v[188:191]\n\t"
      "v_mfma_f32_16x16x32_bf16 v[192:195], v[140:143], v[56:59], v[192:195]\n\t"
      "v_mfma_f32_16x16x32_bf16 v[196:199], v[140:143], v[60:63], v[196:199]\n\t"
      "v_mfma_f32_16x16x32_bf16 v[184:187], v[144:147], v[64:67], v[184:187]\n\t"
      "v_mfma_f32_16x16x32_bf16 v[188:191], v[144:147], v[68:71], v[188:191]\n\t"
      "v_mfma_f32_16x16x32_bf16 v[192:195], v[144:147], v[72:75], v[192:195]\n\t"
      "v_mfma_f32_16x16x32_bf16 v[196:199], v[144:147], v[76:79], v[196:199]\n\t"
      "v_mfma_f32_16x16x32_bf16 v[184:187], v[148:151], v[80:83], v[184:187]\n\t"
      "v_mfma_f32_16x16x32_bf16 v[188:191], v[148:151], v[84:87], v[188:191]\n\t"
      "v_mfma_f32_16x16x32_bf16 v[192:195], v[148:151], v[88:91], v[192:195]\n\t"
      "v_mfma_f32_16x16x32_bf16 v[196:199], v[148:151], v[92:95], v[196:199]\n\t"
      "v_mfma_f32_16x16x32_bf16 v[184:187], v[152:155], v[96:99], v[184:187]\n\t"
      "v_mfma_f32_16x16x32_bf16 v[188:191], v[152:155], v[100:103], v[188:191]\n\t"
      "v_mfma_f32_16x16x32_bf16 v[192:195], v[152:155], v[104:107], v[192:195]\n\t"
      "v_mfma_f32_16x16x32_bf16 v[196:199], v[152:155], v[108:111], v[196:199]\n\t"
      "s_waitcnt vmcnt(0)\n\t"
      "s_nop 7\n\t"
      "s_branch NQ6B_%=\n\t"
      "NP0R_%=:\n\t"
      "s_mov_b32 s51, 0\n\t"
      "NP0L_%=:\n\t"
      "s_add_u32 s51, s51, 1\n\t"
      "s_cmp_lt_u32 s51, 262144\n\t"
      "s_cbranch_scc0 NP0X_%=\n\t"
      "s_waitcnt vmcnt(0)\n\t"
      "s_sleep 1\n\t"
      "global_load_dwordx4 v[128:131], v224, s[52:53] sc1\n\t"
      "global_load_dwordx4 v[132:135], v225, s[52:53] sc1\n\t"
      "global_load_dwordx4 v[136:139], v226, s[52:53] sc1\n\t"
      "global_load_dwordx4 v[140:143], v227, s[52:53] sc1\n\t"
      "global_load_dwordx4 v[144:147], v228, s[52:53] sc1\n\t"
      "global_load_dwordx4 v[148:151], v229, s[52:53] sc1\n\t"
      "global_load_dwordx4 v[152:155], v230, s[52:53] sc1\n\t"
      "s_waitcnt vmcnt(0)\n\t"
      "v_max3_u32 v234, v128, v129, v130\n\t"
      "v_max3_u32 v234, v234, v131, v132\n\t"
      "v_max3_u32 v234, v234, v133, v134\n\t"
      "v_max3_u32 v234, v234, v135, v136\n\t"
      "v_max3_u32 v234, v234, v137, v138\n\t"
      "v_max3_u32 v234, v234, v139, v140\n\t"
      "v_max3_u32 v234, v234, v141, v142\n\t"
      "v_max3_u32 v234, v234, v143, v144\n\t"
      "v_max3_u32 v234, v234, v145, v146\n\t"
      "v_max3_u32 v234, v234, v147, v148\n\t"
      "v_max3_u32 v234, v234, v149, v150\n\t"
      "v_max3_u32 v234, v234, v151, v152\n\t"
      "v_max3_u32 v234, v234, v153, v154\n\t"
      "v_max_u32_e32 v234, v234, v155\n\t"
      "v_cmp_eq_u32_e32 vcc, -1, v234\n\t"
      "s_cbranch_vccnz NP0L_%=\n\t"
      "NP0X_%=:\n\t"
      "v_mfma_f32_16x16x32_bf16 v[184:187], v[128:131], v[0:3], v[156:159]\n\t"
      "v_mfma_f32_16x16x32_bf16 v[188:191], v[128:131], v[4:7], 0\n\t"
      "v_mfma_f32_16x16x32_bf16 v[192:195], v[128:131], v[8:11], 0\n\t"
      "v_mfma_f32_16x16x32_bf16 v[196:199], v[128:131], v[12:15], 0\n\t"
      "v_mfma_f32_16x16x32_bf16 v[184:187], v[132:135], v[16:19], v[184:187]\n\t"
      "v_mfma_f32_16x16x32_bf16 v[188:191], v[132:135], v[20:23], v[188:191]\n\t"
      "v_mfma_f32_16x16x32_bf16 v[192:195], v[132:135], v[24:27], v[192:195]\n\t"
      "v_mfma_f32_16x16x32_bf16 v[196:199], v[132:135], v[28:31], v[196:199]\n\t"
      "v_mfma_f32_16x16x32_bf16 v[184:187], v[136:139], v[32:35], v[184:187]\n\t"
      "v_mfma_f32_16x16x32_bf16 v[188:191], v[136:139], v[36:39], v[188:191]\n\t"
      "v_mfma_f32_16x16x32_bf16 v[192:195], v[136:139], v[40:43], v[192:195]\n\t"
      "v_mfma_f32_16x16x32_bf16 v[196:199], v[136:139], v[44:47], v[196:199]\n\t"
      "v_mfma_f32_16x16x32_bf16 v[184:187], v[140:143], v[48:51], v[184:187]\n\t"
      "v_mfma_f32_16x16x32_bf16 v[188:191], v[140:143], v[52:55], v[188:191]\n\t"
      "v_mfma_f32_16x16x32_bf16 v[192:195], v[140:143], v[56:59], v[192:195]\n\t"
      "v_mfma_f32_16x16x32_bf16 v[196:199], v[140:143], v[60:63], v[196:199]\n\t"
      "v_mfma_f32_16x16x32_bf16 v[184:187], v[144:147], v[64:67], v[184:187]\n\t"
      "v_mfma_f32_16x16x32_bf16 v[188:191], v[144:147], v[68:71], v[188:191]\n\t"
      "v_mfma_f32_16x16x32_bf16 v[192:195], v[144:147], v[72:75], v[192:195]\n\t"
      "v_mfma_f32_16x16x32_bf16 v[196:199], v[144:147], v[76:79], v[196:199]\n\t"
      "v_mfma_f32_16x16x32_bf16 v[184:187], v[148:151], v[80:83], v[184:187]\n\t"
      "v_mfma_f32_16x16x32_bf16 v[188:191], v[148:151], v[84:87], v[188:191]\n\t"
      "v_mfma_f32_16x16x32_bf16 v[192:195], v[148:151], v[88:91], v[192:195]\n\t"
      "v_mfma_f32_16x16x32_bf16 v[184:187], v[152:155], v[96:99], v[184:187]\n\t"
      "v_mfma_f32_16x16x32_bf16 v[188:191], v[152:155], v[100:103], v[188:191]\n\t"
      "v_mfma_f32_16x16x32_bf16 v[192:195], v[152:155], v[104:107], v[192:195]\n\t"
      "global_load_dwordx4 v[128:131], v224, s[54:55] sc1\n\t"
      "global_load_dwordx4 v[132:135], v225, s[54:55] sc1\n\t"
      "global_load_dwordx4 v[136:139], v226, s[54:55] sc1\n\t"
      "global_load_dwordx4 v[140:143], v227, s[54:55] sc1\n\t"
      "global_load_dwordx4 v[144:147], v228, s[54:55] sc1\n\t"
      "s_waitcnt vmcnt(0)\n\t"
      "s_nop 7\n\t"
      "s_branch NP0B_%=\n\t"
      "NP1R_%=:\n\t"
      "s_mov_b32 s51, 0\n\t"
      "NP1L_%=:\n\t"
      "s_add_u32 s51, s51, 1\n\t"
      "s_cmp_lt_u32 s51, 262144\n\t"
      "s_cbranch_scc0 NP1X_%=\n\t"
      "s_waitcnt vmcnt(0)\n\t"
      "s_sleep 1\n\t"
      "global_load_dwordx4 v[128:131], v224, s[54:55] sc1\n\t"
      "global_load_dwordx4 v[132:135], v225, s[54:55] sc1\n\t"
      "global_load_dwordx4 v[136:139], v226, s[54:55] sc1\n\t"
      "global_load_dwordx4 v[140:143], v227, s[54:55] sc1\n\t"
      "global_load_dwordx4 v[144:147], v228, s[54:55] sc1\n\t"
      "global_load_dwordx4 v[148:151], v229, s[54:55] sc1\n\t"
      "global_load_dwordx4 v[152:155], v230, s[54:55] sc1\n\t"
      "s_waitcnt vmcnt(0)\n\t"
      "v_max3_u32 v234, v128, v129, v130\n\t"
      "v_max3_u32 v234, v234, v131, v132\n\t"
      "v_max3_u32 v234, v234, v133, v134\n\t"
      "v_max3_u32 v234, v234, v135, v136\n\t"
      "v_max3_u32 v234, v234, v137, v138\n\t"
      "v_max3_u32 v234, v234, v139, v140\n\t"
      "v_max3_u32 v234, v234, v141, v142\n\t"
      "v_max3_u32 v234, v234, v143, v144\n\t"
      "v_max3_u32 v234, v234, v145, v146\n\t"
      "v_max3_u32 v234, v234, v147, v148\n\t"
      "v_max3_u32 v234, v234, v149, v150\n\t"
      "v_max3_u32 v234, v234, v151, v152\n\t"
      "v_max3_u32 v234, v234, v153, v154\n\t"
      "v_max_u32_e32 v234, v234, v155\n\t"
      "v_cmp_eq_u32_e32 vcc, -1, v234\n\t"
      "s_cbranch_vccnz NP1L_%=\n\t"
      "NP1X_%=:\n\t"
      "v_mfma_f32_16x16x32_bf16 v[200:203], v[128:131], v[0:3], v[160:163]\n\t"
      "v_mfma_f32_16x16x32_bf16 v[204:207], v[128:131], v[4:7], 0\n\t"
      "v_mfma_f32_16x16x32_bf16 v[208:211], v[128:131], v[8:11], 0\n\t"
      "v_mfma_f32_16x16x32_bf16 v[212:215], v[128:131], v[12:15], 0\n\t"
      "v_mfma_f32_16x16x32_bf16 v[200:203], v[132:135], v[16:19], v[200:203]\n\t"
      "v_mfma_f32_16x16x32_bf16 v[204:207], v[132:135], v[20:23], v[204:207]\n\t"
      "v_mfma_f32_16x16x32_bf16 v[208:211], v[132:135], v[24:27], v[208:211]\n\t"
      "v_mfma_f32_16x16x32_bf16 v[212:215], v[132:135], v[28:31], v[212:215]\n\t"
      "v_mfma_f32_16x16x32_bf16 v[200:203], v[136:139], v[32:35], v[200:203]\n\t"
      "v_mfma_f32_16x16x32_bf16 v[204:207], v[136:139], v[36:39], v[204:207]\n\t"
      "v_mfma_f32_16x16x32_bf16 v[208:211], v[136:139], v[40:43], v[208:211]\n\t"
      "v_mfma_f32_16x16x32_bf16 v[212:215], v[136:139], v[44:47], v[212:215]\n\t"
      "v_mfma_f32_16x16x32_bf16 v[200:203], v[140:143], v[48:51], v[200:203]\n\t"
      "v_mfma_f32_16x16x32_bf16 v[204:207], v[140:143], v[52:55], v[204:207]\n\t"
      "v_mfma_f32_16x16x32_bf16 v[208:211], v[140:143], v[56:59], v[208:211]\n\t"
      "v_mfma_f32_16x16x32_bf16 v[212:215], v[140:143], v[60:63], v[212:215]\n\t"
      "v_mfma_f32_16x16x32_bf16 v[200:203], v[144:147], v[64:67], v[200:203]\n\t"
      "v_mfma_f32_16x16x32_bf16 v[204:207], v[144:147], v[68:71], v[204:207]\n\t"
      "v_mfma_f32_16x16x32_bf16 v[208:211], v[144:147], v[72:75], v[208:211]\n\t"
      "v_mfma_f32_16x16x32_bf16 v[212:215], v[144:147], v[76:79], v[212:215]\n\t"
      "v_mfma_f32_16x16x32_bf16 v[200:203], v[148:151], v[80:83], v[200:203]\n\t"
      "v_mfma_f32_16x16x32_bf16 v[204:207], v[148:151], v[84:87], v[204:207]\n\t"
      "v_mfma_f32_16x16x32_bf16 v[208:211], v[148:151], v[88:91], v[208:211]\n\t"
      "v_mfma_f32_16x16x32_bf16 v[200:203], v[152:155], v[96:99], v[200:203]\n\t"
      "v_mfma_f32_16x16x32_bf16 v[204:207], v[152:155], v[100:103], v[204:207]\n\t"
      "v_mfma_f32_16x16x32_bf16 v[208:211], v[152:155], v[104:107], v[208:211]\n\t"
      "global_load_dwordx4 v[128:131], v224, s[56:57] sc1\n\t"
      "global_load_dwordx4 v[132:135], v225, s[56:57] sc1\n\t"
      "global_load_dwordx4 v[136:139], v226, s[56:57] sc1\n\t"
      "global_load_dwordx4 v[140:143], v227, s[56:57] sc1\n\t"
      "global_load_dwordx4 v[144:147], v228, s[56:57] sc1\n\t"
      "s_waitcnt vmcnt(0)\n\t"
      "s_nop 7\n\t"
      "s_branch NP1B_%=\n\t"
      "NP2R_%=:\n\t"
      "s_mov_b32 s51, 0\n\t"
      "NP2L_%=:\n\t"
      "s_add_u32 s51, s51, 1\n\t"
      "s_cmp_lt_u32 s51, 262144\n\t"
      "s_cbranch_scc0 NP2X_%=\n\t"
      "s_waitcnt vmcnt(0)\n\t"
      "s_sleep 1\n\t"
      "global_load_dwordx4 v[128:131], v224, s[56:57] sc1\n\t"
      "global_load_dwordx4 v[132:135], v225, s[56:57] sc1\n\t"
      "global_load_dwordx4 v[136:139], v226, s[56:57] sc1\n\t"
      "global_load_dwordx4 v[140:143], v227, s[56:57] sc1\n\t"
      "global_load_dwordx4 v[144:147], v228, s[56:57] sc1\n\t"
      "global_load_dwordx4 v[148:151], v229, s[56:57] sc1\n\t"
      "global_load_dwordx4 v[152:155], v230, s[56:57] sc1\n\t"
      "s_waitcnt vmcnt(0)\n\t"
      "v_max3_u32 v234, v128, v129, v130\n\t"
      "v_max3_u32 v234, v234, v131, v132\n\t"
      "v_max3_u32 v234, v234, v133, v134\n\t"
      "v_max3_u32 v234, v234, v135, v136\n\t"
      "v_max3_u32 v234, v234, v137, v138\n\t"
      "v_max3_u32 v234, v234, v139, v140\n\t"
      "v_max3_u32 v234, v234, v141, v142\n\t"
      "v_max3_u32 v234, v234, v143, v144\n\t"
      "v_max3_u32 v234, v234, v145, v146\n\t"
      "v_max3_u32 v234, v234, v147, v148\n\t"
      "v_max3_u32 v234, v234, v149, v150\n\t"
      "v_max3_u32 v234, v234, v151, v152\n\t"
      "v_max3_u32 v234, v234, v153, v154\n\t"
      "v_max_u32_e32 v234, v234, v155\n\t"
      "v_cmp_eq_u32_e32 vcc, -1, v234\n\t"
      "s_cbranch_vccnz NP2L_%=\n\t"
      "NP2X_%=:\n\t"
      "v_mfma_f32_16x16x32_bf16 v[184:187], v[128:131], v[0:3], v[164:167]\n\t"
      "v_mfma_f32_16x16x32_bf16 v[188:191], v[128:131], v[4:7], 0\n\t"
      "v_mfma_f32_16x16x32_bf16 v[192:195], v[128:131], v[8:11], 0\n\t"
      "v_mfma_f32_16x16x32_bf16 v[196:199], v[128:131], v[12:15], 0\n\t"
      "v_mfma_f32_16x16x32_bf16 v[184:187], v[132:135], v[16:19], v[184:187]\n\t"
      "v_mfma_f32_16x16x32_bf16 v[188:191], v[132:135], v[20:23], v[188:191]\n\t"
      "v_mfma_f32_16x16x32_bf16 v[192:195], v[132:135], v[24:27], v[192:195]\n\t"
      "v_mfma_f32_16x16x32_bf16 v[196:199], v[132:135], v[28:31], v[196:199]\n\t"
      "v_mfma_f32_16x16x32_bf16 v[184:187], v[136:139], v[32:35], v[184:187]\n\t"
      "v_mfma_f32_16x16x32_bf16 v[188:191], v[136:139], v[36:39], v[188:191]\n\t"
      "v_mfma_f32_16x16x32_bf16 v[192:195], v[136:139], v[40:43], v[192:195]\n\t"
      "v_mfma_f32_16x16x32_bf16 v[196:199], v[136:139], v[44:47], v[196:199]\n\t"
      "v_mfma_f32_16x16x32_bf16 v[184:187], v[140:143], v[48:51], v[184:187]\n\t"
      "v_mfma_f32_16x16x32_bf16 v[188:191], v[140:143], v[52:55], v[188:191]\n\t"
      "v_mfma_f32_16x16x32_bf16 v[192:195], v[140:143], v[56:59], v[192:195]\n\t"
      "v_mfma_f32_16x16x32_bf16 v[196:199], v[140:143], v[60:63], v[196:199]\n\t"
      "v_mfma_f32_16x16x32_bf16 v[184:187], v[144:147], v[64:67], v[184:187]\n\t"
      "v_mfma_f32_16x16x32_bf16 v[188:191], v[144:147], v[68:71], v[188:191]\n\t"
      "v_mfma_f32_16x16x32_bf16 v[192:195], v[144:147], v[72:75], v[192:195]\n\t"
      "v_mfma_f32_16x16x32_bf16 v[196:199], v[144:147], v[76:79], v[196:199]\n\t"
      "v_mfma_f32_16x16x32_bf16 v[184:187], v[148:151], v[80:83], v[184:187]\n\t"
      "v_mfma_f32_16x16x32_bf16 v[188:191], v[148:151], v[84:87], v[188:191]\n\t"
      "v_mfma_f32_16x16x32_bf16 v[192:195], v[148:151], v[88:91], v[192:195]\n\t"
      "v_mfma_f32_16x16x32_bf16 v[184:187], v[152:155], v[96:99], v[184:187]\n\t"
      "v_mfma_f32_16x16x32_bf16 v[188:191], v[152:155], v[100:103], v[188:191]\n\t"
      "v_mfma_f32_16x16x32_bf16 v[192:195], v[152:155], v[104:107], v[192:195]\n\t"
      "global_load_dwordx4 v[128:131], v224, s[58:59] sc1\n\t"
      "global_load_dwordx4 v[132:135], v225, s[58:59] sc1\n\t"
      "global_load_dwordx4 v[136:139], v226, s[58:59] sc1\n\t"
      "global_load_dwordx4 v[140:143], v227, s[58:59] sc1\n\t"
      "global_load_dwordx4 v[144:147], v228, s[58:59] sc1\n\t"
      "s_waitcnt vmcnt(0)\n\t"
      "s_nop 7\n\t"
      "s_branch NP2B_%=\n\t"
      "NP3R_%=:\n\t"
      "s_mov_b32 s51, 0\n\t"
      "NP3L_%=:\n\t"
      "s_add_u32 s51, s51, 1\n\t"
      "s_cmp_lt_u32 s51, 262144\n\t"
      "s_cbranch_scc0 NP3X_%=\n\t"
      "s_waitcnt vmcnt(0)\n\t"
      "s_sleep 1\n\t"
      "global_load_dwordx4 v[128:131], v224, s[58:59] sc1\n\t"
      "global_load_dwordx4 v[132:135], v225, s[58:59] sc1\n\t"
      "global_load_dwordx4 v[136:139], v226, s[58:59] sc1\n\t"
      "global_load_dwordx4 v[140:143], v227, s[58:59] sc1\n\t"
      "global_load_dwordx4 v[144:147], v228, s[58:59] sc1\n\t"
      "global_load_dwordx4 v[148:151], v229, s[58:59] sc1\n\t"
      "global_load_dwordx4 v[152:155], v230, s[58:59] sc1\n\t"
      "s_waitcnt vmcnt(0)\n\t"
      "v_max3_u32 v234, v128, v129, v130\n\t"
      "v_max3_u32 v234, v234, v131, v132\n\t"
      "v_max3_u32 v234, v234, v133, v134\n\t"
      "v_max3_u32 v234, v234, v135, v136\n\t"
      "v_max3_u32 v234, v234, v137, v138\n\t"
      "v_max3_u32 v234, v234, v139, v140\n\t"
      "v_max3_u32 v234, v234, v141, v142\n\t"
      "v_max3_u32 v234, v234, v143, v144\n\t"
      "v_max3_u32 v234, v234, v145, v146\n\t"
      "v_max3_u32 v234, v234, v147, v148\n\t"
      "v_max3_u32 v234, v234, v149, v150\n\t"
      "v_max3_u32 v234, v234, v151, v152\n\t"
      "v_max3_u32 v234, v234, v153, v154\n\t"
      "v_max_u32_e32 v234, v234, v155\n\t"
      "v_cmp_eq_u32_e32 vcc, -1, v234\n\t"
      "s_cbranch_vccnz NP3L_%=\n\t"
      "NP3X_%=:\n\t"
      "v_mfma_f32_16x16x32_bf16 v[200:203], v[128:131], v[0:3], v[168:171]\n\t"
      "v_mfma_f32_16x16x32_bf16 v[204:207], v[128:131], v[4:7], 0\n\t"
      "v_mfma_f32_16x16x32_bf16 v[208:211], v[128:131], v[8:11], 0\n\t"
      "v_mfma_f32_16x16x32_bf16 v[212:215], v[128:131], v[12:15], 0\n\t"
      "v_mfma_f32_16x16x32_bf16 v[200:203], v[132:135], v[16:19], v[200:203]\n\t"
      "v_mfma_f32_16x16x32_bf16 v[204:207], v[132:135], v[20:23], v[204:207]\n\t"
      "v_mfma_f32_16x16x32_bf16 v[208:211], v[132:135], v[24:27], v[208:211]\n\t"
      "v_mfma_f32_16x16x32_bf16 v[212:215], v[132:135], v[28:31], v[212:215]\n\t"
      "v_mfma_f32_16x16x32_bf16 v[200:203], v[136:139], v[32:35], v[200:203]\n\t"
      "v_mfma_f32_16x16x32_bf16 v[204:207], v[136:139], v[36:39], v[204:207]\n\t"
      "v_mfma_f32_16x16x32_bf16 v[208:211], v[136:139], v[40:43], v[208:211]\n\t"
      "v_mfma_f32_16x16x32_bf16 v[212:215], v[136:139], v[44:47], v[212:215]\n\t"
      "v_mfma_f32_16x16x32_bf16 v[200:203], v[140:143], v[48:51], v[200:203]\n\t"
      "v_mfma_f32_16x16x32_bf16 v[204:207], v[140:143], v[52:55], v[204:207]\n\t"
      "v_mfma_f32_16x16x32_bf16 v[208:211], v[140:143], v[56:59], v[208:211]\n\t"
      "v_mfma_f32_16x16x32_bf16 v[212:215], v[140:143], v[60:63], v[212:215]\n\t"
      "v_mfma_f32_16x16x32_bf16 v[200:203], v[144:147], v[64:67], v[200:203]\n\t"
      "v_mfma_f32_16x16x32_bf16 v[204:207], v[144:147], v[68:71], v[204:207]\n\t"
      "v_mfma_f32_16x16x32_bf16 v[208:211], v[144:147], v[72:75], v[208:211]\n\t"
      "v_mfma_f32_16x16x32_bf16 v[212:215], v[144:147], v[76:79], v[212:215]\n\t"
      "v_mfma_f32_16x16x32_bf16 v[200:203], v[148:151], v[80:83], v[200:203]\n\t"
      "v_mfma_f32_16x16x32_bf16 v[204:207], v[148:151], v[84:87], v[204:207]\n\t"
      "v_mfma_f32_16x16x32_bf16 v[208:211], v[148:151], v[88:91], v[208:211]\n\t"
      "v_mfma_f32_16x16x32_bf16 v[200:203], v[152:155], v[96:99], v[200:203]\n\t"
      "v_mfma_f32_16x16x32_bf16 v[204:207], v[152:155], v[100:103], v[204:207]\n\t"
      "v_mfma_f32_16x16x32_bf16 v[208:211], v[152:155], v[104:107], v[208:211]\n\t"
      "global_load_dwordx4 v[128:131], v224, s[60:61] sc1\n\t"
      "global_load_dwordx4 v[132:135], v225, s[60:61] sc1\n\t"
      "global_load_dwordx4 v[136:139], v226, s[60:61] sc1\n\t"
      "global_load_dwordx4 v[140:143], v227, s[60:61] sc1\n\t"
      "global_load_dwordx4 v[144:147], v228, s[60:61] sc1\n\t"
      "s_waitcnt vmcnt(0)\n\t"
      "s_nop 7\n\t"
      "s_branch NP3B_%=\n\t"
      "NP4R_%=:\n\t"
      "s_mov_b32 s51, 0\n\t"
      "NP4L_%=:\n\t"
      "s_add_u32 s51, s51, 1\n\t"
      "s_cmp_lt_u32 s51, 262144\n\t"
      "s_cbranch_scc0 NP4X_%=\n\t"
      "s_waitcnt vmcnt(0)\n\t"
      "s_sleep 1\n\t"
      "global_load_dwordx4 v[128:131], v224, s[60:61] sc1\n\t"
      "global_load_dwordx4 v[132:135], v225, s[60:61] sc1\n\t"
      "global_load_dwordx4 v[136:139], v226, s[60:61] sc1\n\t"
      "global_load_dwordx4 v[140:143], v227, s[60:61] sc1\n\t"
      "global_load_dwordx4 v[144:147], v228, s[60:61] sc1\n\t"
      "global_load_dwordx4 v[148:151], v229, s[60:61] sc1\n\t"
      "global_load_dwordx4 v[152:155], v230, s[60:61] sc1\n\t"
      "s_waitcnt vmcnt(0)\n\t"
      "v_max3_u32 v234, v128, v129, v130\n\t"
      "v_max3_u32 v234, v234, v131, v132\n\t"
      "v_max3_u32 v234, v234, v133, v134\n\t"
      "v_max3_u32 v234, v234, v135, v136\n\t"
      "v_max3_u32 v234, v234, v137, v138\n\t"
      "v_max3_u32 v234, v234, v139, v140\n\t"
      "v_max3_u32 v234, v234, v141, v142\n\t"
      "v_max3_u32 v234, v234, v143, v144\n\t"
      "v_max3_u32 v234, v234, v145, v146\n\t"
      "v_max3_u32 v234, v234, v147, v148\n\t"
      "v_max3_u32 v234, v234, v149, v150\n\t"
      "v_max3_u32 v234, v234, v151, v152\n\t"
      "v_max3_u32 v234, v234, v153, v154\n\t"
      "v_max_u32_e32 v234, v234, v155\n\t"
      "v_cmp_eq_u32_e32 vcc, -1, v234\n\t"
      "s_cbranch_vccnz NP4L_%=\n\t"
      "NP4X_%=:\n\t"
      "v_mfma_f32_16x16x32_bf16 v[184:187], v[128:131], v[0:3], v[172:175]\n\t"
      "v_mfma_f32_16x16x32_bf16 v[188:191], v[128:131], v[4:7], 0\n\t"
      "v_mfma_f32_16x16x32_bf16 v[192:195], v[128:131], v[8:11], 0\n\t"
      "v_mfma_f32_16x16x32_bf16 v[196:199], v[128:131], v[12:15], 0\n\t"
      "v_mfma_f32_16x16x32_bf16 v[184:187], v[132:135], v[16:19], v[184:187]\n\t"
      "v_mfma_f32_16x16x32_bf16 v[188:191], v[132:135], v[20:23], v[188:191]\n\t"
      "v_mfma_f32_16x16x32_bf16 v[192:195], v[132:135], v[24:27], v[192:195]\n\t"
      "v_mfma_f32_16x16x32_bf16 v[196:199], v[132:135], v[28:31], v[196:199]\n\t"
      "v_mfma_f32_16x16x32_bf16 v[184:187], v[136:139], v[32:35], v[184:187]\n\t"
      "v_mfma_f32_16x16x32_bf16 v[188:191], v[136:139], v[36:39], v[188:191]\n\t"
      "v_mfma_f32_16x16x32_bf16 v[192:195], v[136:139], v[40:43], v[192:195]\n\t"
      "v_mfma_f32_16x16x32_bf16 v[196:199], v[136:139], v[44:47], v[196:199]\n\t"
      "v_mfma_f32_16x16x32_bf16 v[184:187], v[140:143], v[48:51], v[184:187]\n\t"
      "v_mfma_f32_16x16x32_bf16 v[188:191], v[140:143], v[52:55], v[188:191]\n\t"
      "v_mfma_f32_16x16x32_bf16 v[192:195], v[140:143], v[56:59], v[192:195]\n\t"
      "v_mfma_f32_16x16x32_bf16 v[196:199], v[140:143], v[60:63], v[196:199]\n\t"
      "v_mfma_f32_16x16x32_bf16 v[184:187], v[144:147], v[64:67], v[184:187]\n\t"
      "v_mfma_f32_16x16x32_bf16 v[188:191], v[144:147], v[68:71], v[188:191]\n\t"
      "v_mfma_f32_16x16x32_bf16 v[192:195], v[144:147], v[72:75], v[192:195]\n\t"
      "v_mfma_f32_16x16x32_bf16 v[196:199], v[144:147], v[76:79], v[196:199]\n\t"
      "v_mfma_f32_16x16x32_bf16 v[184:187], v[148:151], v[80:83], v[184:187]\n\t"
      "v_mfma_f32_16x16x32_bf16 v[188:191], v[148:151], v[84:87], v[188:191]\n\t"
      "v_mfma_f32_16x16x32_bf16 v[192:195], v[148:151], v[88:91], v[192:195]\n\t"
      "v_mfma_f32_16x16x32_bf16 v[184:187], v[152:155], v[96:99], v[184:187]\n\t"
      "v_mfma_f32_16x16x32_bf16 v[188:191], v[152:155], v[100:103], v[188:191]\n\t"
      "v_mfma_f32_16x16x32_bf16 v[192:195], v[152:155], v[104:107], v[192:195]\n\t"
      "global_load_dwordx4 v[128:131], v224, s[62:63] sc1\n\t"
      "global_load_dwordx4 v[132:135], v225, s[62:63] sc1\n\t"
      "global_load_dwordx4 v[136:139], v226, s[62:63] sc1\n\t"
      "global_load_dwordx4 v[140:143], v227, s[62:63] sc1\n\t"
      "global_load_dwordx4 v[144:147], v228, s[62:63] sc1\n\t"
      "s_waitcnt vmcnt(0)\n\t"
      "s_nop 7\n\t"
      "s_branch NP4B_%=\n\t"
      "NP5R_%=:\n\t"
      "s_mov_b32 s51, 0\n\t"
      "NP5L_%=:\n\t"
      "s_add_u32 s51, s51, 1\n\t"
      "s_cmp_lt_u32 s51, 262144\n\t"
      "s_cbranch_scc0 NP5X_%=\n\t"
      "s_waitcnt vmcnt(0)\n\t"
      "s_sleep 1\n\t"
      "global_load_dwordx4 v[128:131], v224, s[62:63] sc1\n\t"
      "global_load_dwordx4 v[132:135], v225, s[62:63] sc1\n\t"
      "global_load_dwordx4 v[136:139], v226, s[62:63] sc1\n\t"
      "global_load_dwordx4 v[140:143], v227, s[62:63] sc1\n\t"
      "global_load_dwordx4 v[144:147], v228, s[62:63] sc1\n\t"
      "global_load_dwordx4 v[148:151], v229, s[62:63] sc1\n\t"
      "global_load_dwordx4 v[152:155], v230, s[62:63] sc1\n\t"
      "s_waitcnt vmcnt(0)\n\t"
      "v_max3_u32 v234, v128, v129, v130\n\t"
      "v_max3_u32 v234, v234, v131, v132\n\t"
      "v_max3_u32 v234, v234, v133, v134\n\t"
      "v_max3_u32 v234, v234, v135, v136\n\t"
      "v_max3_u32 v234, v234, v137, v138\n\t"
      "v_max3_u32 v234, v234, v139, v140\n\t"
      "v_max3_u32 v234, v234, v141, v142\n\t"
      "v_max3_u32 v234, v234, v143, v144\n\t"
      "v_max3_u32 v234, v234, v145, v146\n\t"
      "v_max3_u32 v234, v234, v147, v148\n\t"
      "v_max3_u32 v234, v234, v149, v150\n\t"
      "v_max3_u32 v234, v234, v151, v152\n\t"
      "v_max3_u32 v234, v234, v153, v154\n\t"
      "v_max_u32_e32 v234, v234, v155\n\t"
      "v_cmp_eq_u32_e32 vcc, -1, v234\n\t"
      "s_cbranch_vccnz NP5L_%=\n\t"
      "NP5X_%=:\n\t"
      "v_mfma_f32_16x16x32_bf16 v[200:203], v[128:131], v[0:3], v[176:179]\n\t"
      "v_mfma_f32_16x16x32_bf16 v[204:207], v[128:131], v[4:7], 0\n\t"
      "v_mfma_f32_16x16x32_bf16 v[208:211], v[128:131], v[8:11], 0\n\t"
      "v_mfma_f32_16x16x32_bf16 v[212:215], v[128:131], v[12:15], 0\n\t"
      "v_mfma_f32_16x16x32_bf16 v[200:203], v[132:135], v[16:19], v[200:203]\n\t"
      "v_mfma_f32_16x16x32_bf16 v[204:207], v[132:135], v[20:23], v[204:207]\n\t"
      "v_mfma_f32_16x16x32_bf16 v[208:211], v[132:135], v[24:27], v[208:211]\n\t"
      "v_mfma_f32_16x16x32_bf16 v[212:215], v[132:135], v[28:31], v[212:215]\n\t"
      "v_mfma_f32_16x16x32_bf16 v[200:203], v[136:139], v[32:35], v[200:203]\n\t"
      "v_mfma_f32_16x16x32_bf16 v[204:207], v[136:139], v[36:39], v[204:207]\n\t"
      "v_mfma_f32_16x16x32_bf16 v[208:211], v[136:139], v[40:43], v[208:211]\n\t"
      "v_mfma_f32_16x16x32_bf16 v[212:215], v[136:139], v[44:47], v[212:215]\n\t"
      "v_mfma_f32_16x16x32_bf16 v[200:203], v[140:143], v[48:51], v[200:203]\n\t"
      "v_mfma_f32_16x16x32_bf16 v[204:207], v[140:143], v[52:55], v[204:207]\n\t"
      "v_mfma_f32_16x16x32_bf16 v[208:211], v[140:143], v[56:59], v[208:211]\n\t"
      "v_mfma_f32_16x16x32_bf16 v[212:215], v[140:143], v[60:63], v[212:215]\n\t"
      "v_mfma_f32_16x16x32_bf16 v[200:203], v[144:147], v[64:67], v[200:203]\n\t"
      "v_mfma_f32_16x16x32_bf16 v[204:207], v[144:147], v[68:71], v[204:207]\n\t"
      "v_mfma_f32_16x16x32_bf16 v[208:211], v[144:147], v[72:75], v[208:211]\n\t"
      "v_mfma_f32_16x16x32_bf16 v[212:215], v[144:147], v[76:79], v[212:215]\n\t"
      "v_mfma_f32_16x16x32_bf16 v[200:203], v[148:151], v[80:83], v[200:203]\n\t"
      "v_mfma_f32_16x16x32_bf16 v[204:207], v[148:151], v[84:87], v[204:207]\n\t"
      "v_mfma_f32_16x16x32_bf16 v[208:211], v[148:151], v[88:91], v[208:211]\n\t"
      "v_mfma_f32_16x16x32_bf16 v[200:203], v[152:155], v[96:99], v[200:203]\n\t"
      "v_mfma_f32_16x16x32_bf16 v[204:207], v[152:155], v[100:103], v[204:207]\n\t"
      "v_mfma_f32_16x16x32_bf16 v[208:211], v[152:155], v[104:107], v[208:211]\n\t"
      "global_load_dwordx4 v[128:131], v224, s[64:65] sc1\n\t"
      "global_load_dwordx4 v[132:135], v225, s[64:65] sc1\n\t"
      "global_load_dwordx4 v[136:139], v226, s[64:65] sc1\n\t"
      "global_load_dwordx4 v[140:143], v227, s[64:65] sc1\n\t"
      "global_load_dwordx4 v[144:147], v228, s[64:65] sc1\n\t"
      "s_waitcnt vmcnt(0)\n\t"
      "s_nop 7\n\t"
      "s_branch NP5B_%=\n\t"
      "NP6R_%=:\n\t"
      "s_mov_b32 s51, 0\n\t"
      "NP6L_%=:\n\t"
      "s_add_u32 s51, s51, 1\n\t"
      "s_cmp_lt_u32 s51, 262144\n\t"
      "s_cbranch_scc0 NP6X_%=\n\t"
      "s_waitcnt vmcnt(0)\n\t"
      "s_sleep 1\n\t"
      "global_load_dwordx4 v[128:131], v224, s[64:65] sc1\n\t"
      "global_load_dwordx4 v[132:135], v225, s[64:65] sc1\n\t"
      "global_load_dwordx4 v[136:139], v226, s[64:65] sc1\n\t"
      "global_load_dwordx4 v[140:143], v227, s[64:65] sc1\n\t"
      "global_load_dwordx4 v[144:147], v228, s[64:65] sc1\n\t"
      "global_load_dwordx4 v[148:151], v229, s[64:65] sc1\n\t"
      "global_load_dwordx4 v[152:155], v230, s[64:65] sc1\n\t"
      "s_waitcnt vmcnt(0)\n\t"
      "v_max3_u32 v234, v128, v129, v130\n\t"
      "v_max3_u32 v234, v234, v131, v132\n\t"
      "v_max3_u32 v234, v234, v133, v134\n\t"
      "v_max3_u32 v234, v234, v135, v136\n\t"
      "v_max3_u32 v234, v234, v137, v138\n\t"
      "v_max3_u32 v234, v234, v139, v140\n\t"
      "v_max3_u32 v234, v234, v141, v142\n\t"
      "v_max3_u32 v234, v234, v143, v144\n\t"
      "v_max3_u32 v234, v234, v145, v146\n\t"
      "v_max3_u32 v234, v234, v147, v148\n\t"
      "v_max3_u32 v234, v234, v149, v150\n\t"
      "v_max3_u32 v234, v234, v151, v152\n\t"
      "v_max3_u32 v234, v234, v153, v154\n\t"
      "v_max_u32_e32 v234, v234, v155\n\t"
      "v_cmp_eq_u32_e32 vcc, -1, v234\n\t"
      "s_cbranch_vccnz NP6L_%=\n\t"
      "NP6X_%=:\n\t"
      "v_mfma_f32_16x16x32_bf16 v[184:187], v[128:131], v[0:3], v[180:183]\n\t"
      "v_mfma_f32_16x16x32_bf16 v[188:191], v[128:131], v[4:7], 0\n\t"
      "v_mfma_f32_16x16x32_bf16 v[192:195], v[128:131], v[8:11], 0\n\t"
      "v_mfma_f32_16x16x32_bf16 v[196:199], v[128:131], v[12:15], 0\n\t"
      "v_mfma_f32_16x16x32_bf16 v[184:187], v[132:135], v[16:19], v[184:187]\n\t"
      "v_mfma_f32_16x16x32_bf16 v[188:191], v[132:135], v[20:23], v[188:191]\n\t"
      "v_mfma_f32_16x16x32_bf16 v[192:195], v[132:135], v[24:27], v[192:195]\n\t"
      "v_mfma_f32_16x16x32_bf16 v[196:199], v[132:135], v[28:31], v[196:199]\n\t"
      "v_mfma_f32_16x16x32_bf16 v[184:187], v[136:139], v[32:35], v[184:187]\n\t"
      "v_mfma_f32_16x16x32_bf16 v[188:191], v[136:139], v[36:39], v[188:191]\n\t"
      "v_mfma_f32_16x16x32_bf16 v[192:195], v[136:139], v[40:43], v[192:195]\n\t"
      "v_mfma_f32_16x16x32_bf16 v[196:199], v[136:139], v[44:47], v[196:199]\n\t"
      "v_mfma_f32_16x16x32_bf16 v[184:187], v[140:143], v[48:51], v[184:187]\n\t"
      "v_mfma_f32_16x16x32_bf16 v[188:191], v[140:143], v[52:55], v[188:191]\n\t"
      "v_mfma_f32_16x16x32_bf16 v[192:195], v[140:143], v[56:59], v[192:195]\n\t"
      "v_mfma_f32_16x16x32_bf16 v[196:199], v[140:143], v[60:63], v[196:199]\n\t"
      "v_mfma_f32_16x16x32_bf16 v[184:187], v[144:147], v[64:67], v[184:187]\n\t"
      "v_mfma_f32_16x16x32_bf16 v[188:191], v[144:147], v[68:71], v[188:191]\n\t"
      "v_mfma_f32_16x16x32_bf16 v[192:195], v[144:147], v[72:75], v[192:195]\n\t"
      "v_mfma_f32_16x16x32_bf16 v[196:199], v[144:147], v[76:79], v[196:199]\n\t"
      "v_mfma_f32_16x16x32_bf16 v[184:187], v[148:151], v[80:83], v[184:187]\n\t"
      "v_mfma_f32_16x16x32_bf16 v[188:191], v[148:151], v[84:87], v[188:191]\n\t"
      "v_mfma_f32_16x16x32_bf16 v[192:195], v[148:151], v[88:91], v[192:195]\n\t"
      "v_mfma_f32_16x16x32_bf16 v[184:187], v[152:155], v[96:99], v[184:187]\n\t"
      "v_mfma_f32_16x16x32_bf16 v[188:191], v[152:155], v[100:103], v[188:191]\n\t"
      "v_mfma_f32_16x16x32_bf16 v[192:195], v[152:155], v[104:107], v[192:195]\n\t"
      "s_waitcnt vmcnt(0)\n\t"
      "s_nop 7\n\t"
      "s_branch NP6B_%=\n\t"
      : "+{s[4:19]}"(s), "+{s[20:23]}"(s2), "+{s[28:29]}"(kr), "+{s30}"(od), "+{v[240:247]}"(v), "+{v[136:139]}"(vo), "+{v140}"(vq)
      :
      : "memory", "vcc", "scc",
        "v0", "v1", "v2", "v3", "v4", "v5", "v6", "v7", "v8", "v9", "v10", "v11", "v12", "v13", "v14", "v15", "v16",
        "v17", "v18", "v19", "v20", "v21", "v22", "v23", "v24", "v25", "v26", "v27", "v28", "v29", "v30", "v31", "v32",
        "v33", "v34", "v35", "v36", "v37", "v38", "v39", "v40", "v41", "v42", "v43", "v44", "v45", "v46", "v47", "v48",
        "v49", "v50", "v51", "v52", "v53", "v54", "v55", "v56", "v57", "v58", "v59", "v60", "v61", "v62", "v63", "v64",
        "v65", "v66", "v67", "v68", "v69", "v70", "v71", "v72", "v73", "v74", "v75", "v76", "v77", "v78", "v79", "v80",
        "v81", "v82", "v83", "v84", "v85", "v86", "v87", "v88", "v89", "v90", "v91", "v92", "v93", "v94", "v95", "v96",
        "v97", "v98", "v99", "v100", "v101", "v102", "v103", "v104", "v105", "v106", "v107", "v108", "v109", "v110",
        "v111", "v112", "v113", "v114", "v115", "v116", "v117", "v118", "v119", "v120", "v121", "v122", "v123", "v124",
        "v125", "v126", "v127", "v128", "v129", "v130", "v131", "v132", "v133", "v134", "v135", "v141", "v142", "v143",
        "v144", "v145", "v146", "v147", "v148", "v149", "v150", "v151", "v152", "v153", "v154", "v155", "v156", "v157",
        "v158", "v159", "v160", "v161", "v162", "v163", "v164", "v165", "v166", "v167", "v168", "v169", "v170", "v171",
        "v172", "v173", "v174", "v175", "v176", "v177", "v178", "v179", "v180", "v181", "v182", "v183", "v184", "v185",
        "v186", "v187", "v188", "v189", "v190", "v191", "v192", "v193", "v194", "v195", "v196", "v197", "v198", "v199",
        "v200", "v201", "v202", "v203", "v204", "v205", "v206", "v207", "v208", "v209", "v210", "v211", "v212", "v213",
        "v214", "v215", "v216", "v217", "v218", "v219", "v220", "v221", "v222", "v223", "v224", "v225", "v226", "v227",
        "v228", "v229", "v230", "v231", "v232", "v233", "v234", "v235", "v236", "v237", "v238", "v239", "v248", "v249",
        "v250", "v251", "v252", "v253", "s26", "s27", "s31", "s44", "s45", "s46", "s47", "s48", "s49", "s50", "s51",
        "s52", "s53", "s54", "s55", "s56", "s57", "s58", "s59", "s60", "s61", "s62", "s63", "s64", "s65", "s66", "s67",
        "s68", "s69", "s70"
    );
    __builtin_amdgcn_endpgm();
    }
  }
}

extern "C" void kernel_launch(void* const* d_in, const int* in_sizes, int n_in, void* d_out, int out_size,
                              void* d_ws, size_t ws_size, hipStream_t stream) {
  (void)hipFuncSetAttribute((const void*)fused_block, hipFuncAttributeMaxDynamicSharedMemorySize, LDS_BYTES);
  fused_block<<<256, 512, LDS_BYTES, stream>>>((const char*)d_in[0], (const char*)d_in[1], (const char*)d_in[2],
                                               (const char*)d_in[4], (const char*)d_in[5], (const char*)d_in[6],
                                               (const char*)d_in[7], (char*)d_out, (char*)d_ws);
}
```
